```python
import torch

def _softmax(x, axis=-1):
    return torch.softmax(x, dim=axis)

def _gelu(x):
    return torch.nn.functional.gelu(x, approximate='tanh')

def _silu(x):
    return torch.nn.functional.silu(x)

def _sigmoid(x):
    return torch.sigmoid(x)

def _layernorm(x, axis=-1, eps=1e-05):
    m = x.mean(dim=axis, keepdim=True)
    v = x.var(dim=axis, keepdim=True, unbiased=False)
    return (x - m) / torch.sqrt(v + eps)

def _scatter_add(tgt, idx, src, axis=0):
    return tgt.clone().index_add_(axis, idx, src)

def _segment_sum(src, seg, n):
    out = torch.zeros((n,) + src.shape[1:], dtype=src.dtype)
    return out.index_add_(0, seg, src)

def _pad(x, pw):
    flat = tuple((v for pair in reversed(pw) for v in pair))
    return torch.nn.functional.pad(x, flat)
kernel_name = 'gated_mix__memory_aug_recurrent_mem_tr__ssm_conv_s4d__prompt'

def setup_inputs():

    _w = lambda a: (lambda t: t.to(torch.bfloat16) if t.is_floating_point() else t)(torch.as_tensor(a))

    def _A_setup():
        B, S, Dm = (1, 2048, 576)
        H, Dh = (32, 128)
        M, seg = (8, 32)
        seg = min(seg, S)
        S = S // seg * seg
        return {'x': _w((lambda *s: torch.randn(*s, dtype=torch.bfloat16) * 0.05)(B, S, Dm)), 'mem0': _w((lambda *s: torch.randn(*s, dtype=torch.bfloat16) * 0.4)(B, M, Dm)), 'Wq': _w((lambda *s: torch.randn(*s, dtype=torch.bfloat16) * 6.4)(Dm, H, Dh) * Dm ** (-0.5)), 'Wk': _w((lambda *s: torch.randn(*s, dtype=torch.bfloat16) * 6.4)(Dm, H, Dh) * Dm ** (-0.5)), 'Wv': _w((lambda *s: torch.randn(*s, dtype=torch.bfloat16) * 0.4)(Dm, H, Dh) * Dm ** (-0.5)), 'Wo': _w((lambda *s: torch.randn(*s, dtype=torch.bfloat16) * 0.4)(H, Dh, Dm) * (H * Dh) ** (-0.5)), 'seg_len': seg}
    _A_in = _A_setup()

    def _B_setup():
        Bsz, L, D, N = (1, 2048, 576, 128)
        return {'u': _w((lambda *s: torch.randn(*s, dtype=torch.bfloat16) * 0.05)(Bsz, L, D)), 'log_A_real': _w((lambda *s: torch.randn(*s, dtype=torch.bfloat16) * 6.4)(D, N) * 0.5), 'A_imag': _w((lambda *s: torch.randn(*s, dtype=torch.bfloat16) * 0.05)(D, N) * 3.14), 'C_real': _w((lambda *s: torch.randn(*s, dtype=torch.bfloat16) * 0.2)(D, N) / N ** 0.5), 'C_imag': _w((lambda *s: torch.randn(*s, dtype=torch.bfloat16) * 0.2)(D, N) / N ** 0.5), 'log_dt': _w((lambda *s: torch.randn(*s, dtype=torch.bfloat16) * 0.05)(D) * 0.5 - 4.0), 'D_skip': _w((lambda *s: torch.randn(*s, dtype=torch.bfloat16) * 0.05)(D))}
    _B_in = _B_setup()
    return {'A_x': _A_in['x'], 'A_mem0': _A_in['mem0'], 'A_Wq': _A_in['Wq'], 'A_Wk': _A_in['Wk'], 'A_Wv': _A_in['Wv'], 'A_Wo': _A_in['Wo'], 'A_seg_len': _A_in['seg_len'], 'B_u': _B_in['u'], 'B_log_A_real': _B_in['log_A_real'], 'B_A_imag': _B_in['A_imag'], 'B_C_real': _B_in['C_real'], 'B_C_imag': _B_in['C_imag'], 'B_log_dt': _B_in['log_dt'], 'B_D_skip': _B_in['D_skip'], 'gate_w': _w((lambda *s: torch.randn(*s, dtype=torch.bfloat16) * 0.05)(576) * 16.0)}

def reference(A_x, A_mem0, A_Wq, A_Wk, A_Wv, A_Wo, A_seg_len, B_u, B_log_A_real, B_A_imag, B_C_real, B_C_imag, B_log_dt, B_D_skip, gate_w):

    def _A_ref(x, mem0, Wq, Wk, Wv, Wo, seg_len):
        B, S, Dm = x.shape
        H, Dh = (Wq.shape[1], Wq.shape[2])
        M = mem0.shape[1]
        scale = Dh ** (-0.5)
        n_seg = S // seg_len
        L = M + seg_len
        idx = torch.arange(L)
        mask = (idx[:, None] < M) | (idx[:, None] >= idx[None, :])
        mem = mem0
        outs = []
        for s in range(n_seg):
            xs = x[:, s * seg_len:(s + 1) * seg_len, :].clone()
            h = torch.concatenate([mem, xs], axis=1)
            q = torch.einsum('btd,dhe->bhte', h, Wq)
            k = torch.einsum('btd,dhe->bhte', h, Wk)
            v = torch.einsum('btd,dhe->bhte', h, Wv)
            scores = torch.einsum('bhqe,bhke->bhqk', q, k) * scale
            scores = torch.where(mask[None, None], scores, -1000000000.0)
            p = _softmax(scores, axis=-1)
            o = torch.einsum('bhqk,bhke->bhqe', p, v)
            o = torch.einsum('bhte,hed->btd', o, Wo)
            mem = o[:, :M, :].clone()
            outs.append(o[:, M:, :])
        return torch.concatenate(outs, axis=1)

    def _B_ref(u, log_A_real, A_imag, C_real, C_imag, log_dt, D_skip):
        L = u.shape[1]
        dt = torch.exp(log_dt)
        A = -torch.exp(log_A_real) + 1j * A_imag
        C = (C_real + 1j * C_imag) * dt[:, None]
        pos = (lambda x, dt: x.to(dt))(torch.arange(L), log_dt.dtype)
        P = torch.exp((A * dt[:, None])[:, :, None] * pos[None, None, :])
        K = 2.0 * torch.einsum('dn,dnl->dl', C, P).real
        L2 = 2 * L
        u_t = (lambda x, *d: x.permute(*d))(u, 0, 2, 1)
        Kf = (lambda x, *a, **k: torch.fft.rfft(x.float(), *a, **k))(K, L2)
        Uf = (lambda x, *a, **k: torch.fft.rfft(x.float(), *a, **k))(u_t, L2)
        y = (lambda x, *a, **k: torch.fft.irfft(x, *a, **k).to(torch.bfloat16))(Uf * Kf[None, :, :], L2)[:, :, :L]
        y = (lambda x, *d: x.permute(*d))(y, 0, 2, 1)
        return y + u * D_skip[None, None, :]
    x = A_x
    g = _sigmoid((x * gate_w).sum(-1, keepdims=True))
    a = _A_ref(x, A_mem0, A_Wq, A_Wk, A_Wv, A_Wo, A_seg_len)
    b = _B_ref(x, B_log_A_real, B_A_imag, B_C_real, B_C_imag, B_log_dt, B_D_skip)
    return g * a + (1 - g) * b
```

```cpp
#include <hip/hip_runtime.h>
#include <hip/hip_bf16.h>
#include <stdint.h>
#include <type_traits>

typedef __attribute__((ext_vector_type(2))) __bf16 bf16x2;
typedef __attribute__((ext_vector_type(4))) __bf16 bf16x4;
typedef __attribute__((ext_vector_type(8))) __bf16 bf16x8;
typedef __attribute__((ext_vector_type(2))) float f32x2;
typedef __attribute__((ext_vector_type(4))) float f32x4;
typedef __attribute__((ext_vector_type(2))) unsigned u32x2;
typedef __attribute__((ext_vector_type(4))) unsigned u32x4;
typedef unsigned short u16;
typedef __amdgpu_buffer_rsrc_t rsrc_t;
#define LAS __attribute__((address_space(3)))
#define GAS __attribute__((address_space(1)))

constexpr int SL = 2048, DM = 576, NH = 32, DH = 128, HD = 4096, NSEG = 64, NST = 128;

constexpr size_t WS_CTL = 0;
constexpr int CTL_BYTES = 65536;
constexpr int F_GEN = 0, F_TMO = 1, F_GO = 2, F_XCC = 10, F_PRB = 14, F_INITW = 16, F_Y = 20;
constexpr int F_R = 300;
constexpr int F_M = 65, F_O = 129, F_A = 200, F_Q = 256;
constexpr int W_VO = 1, W_PX = 19, W_PM = 23, W_PC = 24, W_MEM = 25;
constexpr int N_DUR = 0, N_K = 32, N_U = 64, N_RD = 96;
constexpr size_t COMM_BYTES = (size_t)8 << 20;
constexpr unsigned FR_BYTES = SL * HD * 2;
constexpr unsigned B_K = 0;
constexpr unsigned B_V = FR_BYTES;
constexpr unsigned B_U = FR_BYTES;
constexpr unsigned B_O = 3 * FR_BYTES;
constexpr unsigned B_Q = 4 * FR_BYTES;
constexpr unsigned B_AP = 5 * FR_BYTES + NH * 8192;
constexpr unsigned B_YT = B_AP + NSEG * 24 * 16384;
constexpr unsigned B_G = B_YT + DM * SL * 2;
constexpr int SDIR = 60, NDIR = NSEG - SDIR;
constexpr unsigned X_AT = 8u << 20;
constexpr int Y_NEAR = 32, Y_FAR = 144, F_Z = 310;
constexpr int F_S = 26;
#define RG_U 3
#define UNEAR 8192u
#define NCOPY 4
#define RG_QKV 63
#define RG_M 63
#define RG_O 63
#define RG_AP 63
#define PB_MB 12u
constexpr unsigned CO = 4u << 20, PB = PB_MB << 20, BASE_BYTES = 244u << 20;
constexpr unsigned U_AT = (128u << 20) - PB;
constexpr size_t WS_END = (size_t)240 << 20;
constexpr int LDS_BYTES = 163840;
constexpr unsigned SPIN_CAP = 1u << 21;

struct Params {
  const u16 *x, *mem0, *Wq, *Wk, *Wv, *Wo, *lar, *aim, *cre, *cim, *ldt, *dsk, *gw;
  u16* out;
  char* ws;
#define TUNE_LIST(X) X(keep_at, "KD", 35) X(jhi, "JHI", 32) X(t_a, "TA", 12) X(t_first, "TY", 170) X(t_d, "TD", 13) X(rd0, "RD0", 68) X(phase, "SKIP", 3) X(gentle, "GENTLE", 0) X(spike, "SPIKE", 24) X(boost, "BOOST", 0) X(boost0, "BOOST0", 0) X(push, "PUSH", 165) X(slow, "SLOW", -4) X(dec0, "DEC0", 1) X(dec1, "DEC1", 3) X(dsw, "DSW", 26) X(look, "LOOK", 36) X(push2, "TY2", 25) X(bsa, "BSA", 8) X(push3, "TY3", 35) X(dur_dec, "DDEC", 2) X(dur_inc, "DINC", 8) X(dur0, "DP0", 74) X(calm, "CALM", 0) X(bmax, "BMAX", 16) X(op_sleep, "OSL", 2) X(stag, "STAG", 40) X(tail, "TAIL", 267) X(psl, "PSL", 0) X(xin, "XIN", 64) X(pushx, "PUSHX", 115) X(dret, "DRET", 76) X(dk, "DK", 115) X(dkv, "DKV", 115) X(rsl, "RSL", 1) X(cpf, "CPF", 150) X(yz, "YZ", 1) X(sa, "SA", 20) X(sb, "SB", 44) X(xtra, "XTRA", 0) X(s4q, "S4Q", 0) X(s4f, "S4F", 8) X(wl2, "WL2", 1) X(s0, "S0", 0) X(v0, "V0", 0) X(xl0, "XL0", 56) X(dsh, "DSH", 3) X(tlb, "TLB", 0) X(xprep, "XPREP", 25) X(xt0, "XT0", 125) X(xt1, "XT1", 88) X(gate, "GATE", 40) X(vlead, "VLEAD", 23) X(npush, "NPUSH", 8) X(near0, "NEAR0", 0) X(att0, "ATT0", 0) X(xpre, "XPRE", 7) X(pj0, "PJ0", 0) X(tail2, "TAIL2", 217) X(xlast, "XLAST", 10) X(defer, "DEFER", 4) X(apre, "APRE", 0) X(hdel, "HDEL", 0) X(hdl2, "HDL2", 0) X(gate0, "GATE0", 0) X(q0, "Q0", 0) X(s4skip, "S4SKIP", 0) X(late_r, "LR", 8) X(late_s, "LS", 8)
#define TUNE_DECL(n, e, v) static constexpr int n = v;
  TUNE_LIST(TUNE_DECL)
};

__device__ __forceinline__ int lane_id() {
  int z = 0;
  asm volatile("" : "+v"(z));
  return __builtin_amdgcn_mbcnt_hi(~0u, __builtin_amdgcn_mbcnt_lo(~0u, z));
}
__device__ __forceinline__ float bf2f(u16 x) { return __uint_as_float((unsigned)x << 16); }
__device__ __forceinline__ float rbf(float x) { return (float)(__bf16)x; }
__device__ __forceinline__ unsigned pk2(float a, float b) {
  f32x2 v = {a, b};
  return __builtin_bit_cast(unsigned, __builtin_convertvector(v, bf16x2));
}
__device__ __forceinline__ float lo16(unsigned p) { return __uint_as_float(p << 16); }
__device__ __forceinline__ float hi16(unsigned p) { return __uint_as_float(p & 0xFFFF0000u); }
__device__ __forceinline__ bf16x8 zext8(u32x2 v) {
  u32x4 z = {v[0], v[1], 0u, 0u};
  return __builtin_bit_cast(bf16x8, z);
}
__device__ __forceinline__ bf16x4 tr_read(const char* smem_base, unsigned off) {
  return __builtin_amdgcn_ds_read_tr16_b64_v4bf16((LAS bf16x4*)((LAS char*)smem_base + off));
}
__device__ __forceinline__ float red_max_g(float x) {
  auto r = __builtin_amdgcn_permlane16_swap(__float_as_uint(x), __float_as_uint(x), false, false);
  x = fmaxf(__uint_as_float(r[0]), __uint_as_float(r[1]));
  auto q = __builtin_amdgcn_permlane32_swap(__float_as_uint(x), __float_as_uint(x), false, false);
  return fmaxf(__uint_as_float(q[0]), __uint_as_float(q[1]));
}
__device__ __forceinline__ float red_sum_g(float x) {
  auto r = __builtin_amdgcn_permlane16_swap(__float_as_uint(x), __float_as_uint(x), false, false);
  x = __uint_as_float(r[0]) + __uint_as_float(r[1]);
  auto q = __builtin_amdgcn_permlane32_swap(__float_as_uint(x), __float_as_uint(x), false, false);
  return __uint_as_float(q[0]) + __uint_as_float(q[1]);
}
__device__ __forceinline__ rsrc_t mk_rsrc(const void* p, size_t bytes) {
  return __builtin_amdgcn_make_buffer_rsrc((void*)p, 0, (int)bytes, 0x00020000);
}

#define STR2(x) #x
#define STR(x) STR2(x)
#define CODE_PAD(N) asm volatile("s_branch .Lpad_%=\n\t.fill " STR(N) ", 4, 0xBF800000\n\t.Lpad_%=:" ::: "memory")
#define PAD_RED 0
#define PAD_ATT 0
#define PAD_OP 0
#define PAD_S4 0
#define PAD_CH 0
#define WAIT_VM(n) asm volatile("s_waitcnt vmcnt(" #n ")" ::: "memory")
#define WAIT_LGKM0() asm volatile("s_waitcnt lgkmcnt(0)" ::: "memory")
#define BARRIER() do { WAIT_LGKM0(); __builtin_amdgcn_s_barrier(); asm volatile("" ::: "memory"); } while (0)
#define SB() __builtin_amdgcn_sched_barrier(0)
#define MF(a, b, c) __builtin_amdgcn_mfma_f32_16x16x32_bf16(a, b, c, 0, 0, 0)
#define RLX_AGENT __ATOMIC_RELAXED, __HIP_MEMORY_SCOPE_AGENT
#define DEV_COUNT(i, cond) do {} while (0)
#define DEV_COUNT_DECL do {} while (0)
#define DEV_COUNT_FLUSH() do {} while (0)
__device__ __forceinline__ bf16x8 cat2(bf16x4 a, bf16x4 b) { return __builtin_shufflevector(a, b, 0, 1, 2, 3, 4, 5, 6, 7); }
__device__ __forceinline__ u32x4 pack8(const f32x4& a, const f32x4& b) {
  return (u32x4){pk2(a[0], a[1]), pk2(a[2], a[3]), pk2(b[0], b[1]), pk2(b[2], b[3])};
}
__device__ __forceinline__ void dma16(rsrc_t rs, unsigned voff, unsigned soff, unsigned lds_addr) {
  unsigned keep;
  asm volatile("s_mov_b32 %0, m0\n\ts_mov_b32 m0, %3\n\ts_nop 0\n\tbuffer_load_dwordx4 %1, %2, %4 offen lds\n\ts_mov_b32 m0, %0"
               : "=&s"(keep) : "v"(voff), "s"(rs), "s"(lds_addr), "s"(soff) : "memory");
}
__device__ __forceinline__ void st128(u32x4 v, rsrc_t rs, unsigned voff, unsigned soff) {
  __builtin_amdgcn_raw_buffer_store_b128(v, rs, voff, soff, 16);
  asm volatile("s_nop 1" ::"v"(v) : "memory");
}
__device__ __forceinline__ u32x4 ld128(rsrc_t rs, unsigned voff, unsigned soff) { return __builtin_amdgcn_raw_buffer_load_b128(rs, voff, soff, 16); }
__device__ __forceinline__ bf16x8 ldfrag(rsrc_t rs, unsigned voff, unsigned soff) { return __builtin_bit_cast(bf16x8, ld128(rs, voff, soff)); }
__device__ __forceinline__ void sleep_n(int n) { for (int i = 0; i < n; ++i) __builtin_amdgcn_s_sleep(1); }

__device__ __forceinline__ char* comm_base(char* ws) {
  return (char*)(((uintptr_t)ws + CTL_BYTES + ((2u << 20) - 1)) & ~(uintptr_t)((2u << 20) - 1));
}
__device__ __forceinline__ unsigned rt_now() {
  unsigned long long t;
  asm volatile("s_memrealtime %0\n\ts_waitcnt lgkmcnt(0)" : "=s"(t) :: "memory");
  return (unsigned)t;
}
__device__ __forceinline__ int wait_rt(unsigned t) {
  int d = (int)(t - rt_now());
  if (d <= 0 || d > 2000) return -d;
  for (d -= 14; d > 0; d -= 3) __builtin_amdgcn_s_sleep(1);
  do { d = (int)(t - rt_now()); } while (d > 0);
  return -d;
}
__device__ __forceinline__ unsigned xcc_id() { return (unsigned)__builtin_amdgcn_s_getreg(20 | (3 << 11)) & 7u; }
typedef unsigned long long u64;
struct Ctx { char* ws; unsigned tag; };
__device__ __forceinline__ GAS u64* flag_ptr(char* ws, int idx) { return (GAS u64*)(ws + WS_CTL + idx * 128); }
__device__ __forceinline__ unsigned gen_load(char* ws) { return __hip_atomic_load((GAS unsigned*)flag_ptr(ws, F_GEN), RLX_AGENT); }
__device__ __forceinline__ unsigned flag_get(const Ctx& cx, GAS u64* p) {
  const u64 v = __hip_atomic_load(p, RLX_AGENT);
  return (unsigned)(v >> 32) == cx.tag ? (unsigned)v : 0u;
}
__device__ __forceinline__ void flag_put(const Ctx& cx, GAS u64* p, unsigned v) { __hip_atomic_store(p, ((u64)cx.tag << 32) | v, RLX_AGENT); }
__device__ __forceinline__ bool spin_fail(unsigned& spins, const Ctx& cx) {
  ++spins;
  if (spins > SPIN_CAP) { flag_put(cx, flag_ptr(cx.ws, F_TMO), 1u); return true; }
  return (spins & 1023u) == 0 && __builtin_amdgcn_readfirstlane(flag_get(cx, flag_ptr(cx.ws, F_TMO)));
}
template <int SLEEP = 1>
__device__ __forceinline__ void wait_ge_n(const Ctx& cx, int idx, int stride, int cnt, int target, int& seen) {
  unsigned spins = 0;
  const int k = lane_id() & 3;
  while (seen < target) {
    int v = (int)flag_get(cx, flag_ptr(cx.ws, idx + (k < cnt ? k : 0) * stride));
    seen = min(min(__builtin_amdgcn_readlane(v, 0), __builtin_amdgcn_readlane(v, 1)), min(__builtin_amdgcn_readlane(v, 2), __builtin_amdgcn_readlane(v, 3)));
    if (seen < target) {
      __builtin_amdgcn_s_sleep(SLEEP);
      if (spin_fail(spins, cx)) break;
    }
  }
  __builtin_amdgcn_fence(__ATOMIC_ACQUIRE, "wavefront");
}
template <int SLEEP>
__device__ __forceinline__ void wait_all(const Ctx& cx, int idx, int cnt) {
  unsigned spins = 0;
  const int k = lane_id();
  while (__any(flag_get(cx, flag_ptr(cx.ws, idx) + (k < cnt ? k : 0)) == 0u)) {
    __builtin_amdgcn_s_sleep(SLEEP);
    if (spin_fail(spins, cx)) break;
  }
  __builtin_amdgcn_fence(__ATOMIC_ACQUIRE, "wavefront");
}
__device__ __forceinline__ void publish(const Ctx& cx, int w, int idx, int value) {
  if (w == 0 && lane_id() == 0) flag_put(cx, flag_ptr(cx.ws, idx), (unsigned)value);
}

constexpr unsigned X1 = 0x101000u, TW_OFF = 27 * 32768 + 1024;
__device__ __forceinline__ unsigned win_static(int win) { return win * 32768 + (win >> 4) * 16384; }
__device__ __forceinline__ unsigned near_static(int i) { return win_static(28 + (i >> 5)) + (i & 31) * 128; }
struct Place {
  unsigned go;
  __device__ __forceinline__ void load(const Ctx& cx, int grp, unsigned need = 0xC0000000u) {
    unsigned spins = 0;
    while ((go = __builtin_amdgcn_readfirstlane(flag_get(cx, flag_ptr(cx.ws, F_GO + grp)))) < need && !spin_fail(spins, cx)) __builtin_amdgcn_s_sleep(8);
    __builtin_amdgcn_fence(__ATOMIC_ACQUIRE, "wavefront");
  }
  __device__ __forceinline__ unsigned dyn(int x) const { return ((go >> (4 * x)) & 7u) << 12; }
  __device__ __forceinline__ unsigned tog() const { return (((go >> 16) & 7u) << 12) | (2u << 20); }
  __device__ __forceinline__ unsigned slot(int s) const { return (((s ^ (s >> 1)) & 1) ? tog() : 0u) ^ ((s & 2) ? X1 : 0u); }
  __device__ __forceinline__ unsigned near(int i) const { return near_static(i) ^ dyn(0); }
};
#define BULK_FLIP 0u
#define OUT_AUX 0
#define PROBE_N 10
struct Bulk {
  rsrc_t rs;
  unsigned w13;
  Place pl;
  __device__ __forceinline__ GAS u64* proj_flag(const Ctx& cx, int m, int h) const {
    return m == 0 ? flag_ptr(cx.ws, F_Q + h) : (GAS u64*)(comm_base(cx.ws) + pl.near(N_K + h) + (m - 1) * 8);
  }
  __device__ __forceinline__ void init(const Ctx& cx, int grp, u64 early = 0) {
    pl.go = __builtin_amdgcn_readfirstlane((unsigned)(early >> 32) == cx.tag ? (unsigned)early : 0u);
    if (pl.go < 0x80000000u) pl.load(cx, grp, 0x80000000u);
    rs = mk_rsrc(comm_base(cx.ws) - CO, BASE_BYTES);
    w13 = PB + ((~pl.go ^ BULK_FLIP) & 2u) * 4096;
  }
  __device__ __forceinline__ unsigned at(unsigned o) const {
    const unsigned q = w13 + ((o >> 13) << 14) + (o & 8191u);
    return q ^ ((q >> 8) & 8192u);
  }
};
__device__ __forceinline__ unsigned near_head(unsigned b, int h) { return PB + 2 * b + h * (1u << 20) + ((h >> 1) & 1) * 8192 + ((2 * h) & 63) * 16384; }
__device__ __forceinline__ unsigned u_tile(int T, int hb, int h7) {
  const int wv = T < 32 ? (T >> 1) & 3 : T & 3;
  return PB + 2 * B_U + T * (1u << 20) + ((T >> 1) & 1) * 8192 + (((T >> 3) & 3) * 4 + wv) * 65536 + (hb ^ ((T >> 3) & 3)) * 16384 + (h7 ^ (2 * wv + (T < 32 ? T & 1 : 0))) * 1024;
}
__device__ __forceinline__ unsigned xcc_page(unsigned xc) { return ((xc >> 1) & 1u) | ((xc >> 2) << 1) | ((xc & 1u) << 2); }

__device__ __forceinline__ void comm_init(Ctx& cx, int rid, int w, char* smem) {
  const int l = lane_id(), tid = w * 64 + l;
  const rsrc_t rs = mk_rsrc(comm_base(cx.ws), COMM_BYTES);
  const unsigned gen = gen_load(cx.ws);
  if (rid >= 2) cx.tag = __builtin_amdgcn_readfirstlane(gen) + 1u;
  if (rid % 9 == 0 && rid && tid == 0) flag_put(cx, flag_ptr(cx.ws, F_XCC + rid / 9), xcc_id() + 1u);
  if (rid < 2) {
    LAS unsigned* const best = (LAS unsigned*)smem + 16;
    const unsigned base = rid * (2u << 20) + w * 1024;
#pragma unroll
    for (int k = 0; k < 8; ++k) __builtin_amdgcn_raw_buffer_store_b128((u32x4){0u, 0u, 0u, 0u}, rs, base + k * 4096 + l * 16, 0, 16);
    WAIT_VM(0);
    unsigned mine = ~0u;
    auto sample = [&](int pg, int k) {
      unsigned long long t0, t1;
      asm volatile("s_memtime %0\n\ts_waitcnt lgkmcnt(0)" : "=s"(t0) :: "memory");
      const unsigned v = __builtin_amdgcn_raw_buffer_load_b32(rs, base + pg * 4096 + k * 128, 0, 16);
      asm volatile("s_waitcnt vmcnt(0)\n\ts_memtime %0\n\ts_waitcnt lgkmcnt(0)" : "=s"(t1) : "v"(v) : "memory");
      if (l == pg) mine = min(mine, (unsigned)(t1 - t0));
    };
#pragma unroll
    for (int k = 0; k < PROBE_N; ++k) sample((3 * k + 2 * w) & 7, k >> 3);
    cx.tag = __builtin_amdgcn_readfirstlane(gen) + 1u;
    if (rid == 0 && tid == 0) flag_put(cx, flag_ptr(cx.ws, F_XCC), xcc_id() + 1u);
    if (l < 8) best[w * 8 + l] = mine;
    __syncthreads();
    const int k8 = l & 7;
    unsigned b = (min(min(best[k8], best[8 + k8]), min(best[16 + k8], best[24 + k8])) << 3) | k8;
#pragma unroll
    for (int m = 1; m < 8; m <<= 1) b = min(b, (unsigned)__shfl_xor((int)b, m));
    if (tid == 0) flag_put(cx, flag_ptr(cx.ws, F_PRB + rid), 8u | ((b & 7u) ^ xcc_page(xcc_id())));
  }
  unsigned go = 0;
  if (rid == 0 && w == 3) {
    unsigned v, spins = 0;
    while (__any((v = flag_get(cx, flag_ptr(cx.ws, F_XCC + min(l, 5)))) == 0u) && !spin_fail(spins, cx)) __builtin_amdgcn_s_sleep(2);
    const unsigned p0 = __builtin_amdgcn_readlane(v, 4), p1 = __builtin_amdgcn_readlane(v, 5);
    unsigned seen = 0;
    go = 0x80000000u | (((p0 ^ p1) & 7u) << 16);
#pragma unroll
    for (int x = 0; x < 4; ++x) seen |= 1u << ((__builtin_amdgcn_readlane(v, x) - 1u) & 3u);
#pragma unroll
    for (int x = 0; x < 4; ++x) {
      const unsigned xc = __builtin_amdgcn_readlane(v, x) - 1u;
      go |= ((xcc_page(seen == 15u ? xc : (xc & 4u) | x) ^ p0) & 7u) << (4 * x);
    }
    if (l < 8) flag_put(cx, flag_ptr(cx.ws, F_GO + l), go);
  }
  for (int j = rid < 2 ? 34 * 2048 : (rid - 2) * 256 + tid; j < 34 * 2048; j += 34 * 256) {
    const int wi = j >> 11, ch = j & 2047;
    const int win = wi < 24 ? wi + 1 : wi < 27 ? wi + 65 : wi < 32 ? wi : wi == 32 ? 59 : 123;
    const unsigned f = wi < 28 || wi >= 32 ? 0xFFFFFFFFu : 0u;
    __builtin_amdgcn_raw_buffer_store_b128((u32x4){f, f, f, f}, rs, win * 32768 + ch * 16, 0, 16);
  }
  WAIT_VM(0);
  BARRIER();
  if (tid == 0) flag_put(cx, flag_ptr(cx.ws, F_INITW) + rid, 1u);
  if (rid == 0 && w == 3) {
    wait_all<4>(cx, F_INITW, 37);
    if (l < 8) flag_put(cx, flag_ptr(cx.ws, F_GO + l), go | 0x40000000u);
  }
}

__device__ __forceinline__ void keep_role(const Params& p) {
  char* const ws = p.ws;
  const int l = lane_id();
  const Ctx cx = {ws, __builtin_amdgcn_readfirstlane(gen_load(ws)) + 1u};
  if (l == 0) flag_put(cx, flag_ptr(ws, F_INITW) + 36, 1u);
  const rsrc_t crs = mk_rsrc(comm_base(ws), COMM_BYTES);
  Place pl;
  pl.load(cx, 0);
  const unsigned to = l < 16 && !(l & 2) ? (16384u ^ pl.dyn(l >> 2)) + (l & 3) * 128 : 0x7F000000u;
  int dp[2] = {4 * p.dur0, 4 * p.rd0};
  unsigned t = rt_now();
  auto plan = [&](int s, int extra) {
    const unsigned t1 = t + ((dp[0] + 2) >> 2) + p.t_a + extra;
    t = t1 + ((dp[1] + 2) >> 2) + p.t_d;
    st128((u32x4){(unsigned)s, t1, t, 0u}, crs, to ^ pl.slot(s - 1), TW_OFF);
  };
  plan(1, 0);
  plan(2, 0);
  for (int s = 1; s < NSEG - 2; ++s) {
    unsigned spins = 0;
    while (__builtin_amdgcn_readfirstlane(flag_get(cx, (GAS u64*)(comm_base(ws) + TW_OFF + (16384u ^ pl.dyn(0)) + 512))) < (unsigned)s && !spin_fail(spins, cx)) __builtin_amdgcn_s_sleep(8);
    const u32x2 dw = __builtin_amdgcn_raw_buffer_load_b64(crs, pl.near((l < 32 ? N_DUR : N_RD - 32) + l), 0, 16);
    unsigned dur = dw[1] == cx.tag && (dw[0] & 1023u) ? (unsigned)((float)(dw[0] >> 10) * 64.f * __builtin_amdgcn_rcpf((float)(dw[0] & 1023u))) : 0u;
    const unsigned long long none = __ballot(dur == 0u);
#pragma unroll
    for (int m = 1; m < 32; m <<= 1) dur = max(dur, (unsigned)__shfl_xor((int)dur, m));
#pragma unroll
    for (int k = 0; k < 2; ++k) {
      const int dm = __builtin_amdgcn_readlane(dur, 32 * k), d = dp[k];
      if ((unsigned)(none >> (32 * k)) == 0u) dp[k] = dm > d + p.spike ? d + 2 : max(min(dm, d + p.dur_inc), d - max(p.dur_dec, (d - dm) >> p.dsh));
    }
    plan(s + 2, s > p.xl0 ? p.xlast : 0);
  }
}

#define X_ASM_TEXT \
  "s_cmp_gt_u32 %28, 1\n" \
  "s_cbranch_scc1 .LX_other_%=\n" \
  "s_mov_b64 s[40:41], %24\n" \
  "s_and_b32 s41, s41, 0xffff\n" \
  "s_mov_b32 s42, 255852544\n" \
  "s_mov_b32 s43, 0x00020000\n" \
  "s_mov_b32 s46, 4718592\n" \
  "s_mov_b32 s47, 0x00020000\n" \
  "s_lshl_b32 s72, %28, 11\n" \
  "buffer_load_dword v196, off, s[40:43], %40 sc1\n" \
  "s_mov_b64 s[44:45], %25\n" \
  "s_and_b32 s45, s45, 0xffff\n" \
  "s_add_u32 s60, %29, 0\n" \
  "s_lshl_b32 s61, s72, 1\n" \
  "s_add_u32 m0, s61, 16384\n" \
  "s_nop 0\n" \
  "buffer_load_dwordx4 v200, s[44:47], s60 offen lds\n" \
  "buffer_load_dwordx4 v201, s[44:47], s60 offen offset:1024 lds\n" \
  "buffer_load_dwordx4 v202, s[44:47], s60 offen offset:2048 lds\n" \
  "buffer_load_dwordx4 v203, s[44:47], s60 offen offset:3072 lds\n" \
  "s_add_u32 s60, %29, 524288\n" \
  "s_lshl_b32 s61, s72, 1\n" \
  "s_add_u32 m0, s61, 32768\n" \
  "s_nop 0\n" \
  "buffer_load_dwordx4 v200, s[44:47], s60 offen lds\n" \
  "buffer_load_dwordx4 v201, s[44:47], s60 offen offset:1024 lds\n" \
  "buffer_load_dwordx4 v202, s[44:47], s60 offen offset:2048 lds\n" \
  "buffer_load_dwordx4 v203, s[44:47], s60 offen offset:3072 lds\n" \
  "s_add_u32 s60, %29, 1048576\n" \
  "s_lshl_b32 s61, s72, 1\n" \
  "s_add_u32 m0, s61, 49152\n" \
  "s_nop 0\n" \
  "buffer_load_dwordx4 v200, s[44:47], s60 offen lds\n" \
  "buffer_load_dwordx4 v201, s[44:47], s60 offen offset:1024 lds\n" \
  "buffer_load_dwordx4 v202, s[44:47], s60 offen offset:2048 lds\n" \
  "buffer_load_dwordx4 v203, s[44:47], s60 offen offset:3072 lds\n" \
  "s_add_u32 s60, %29, 1572864\n" \
  "s_lshl_b32 s61, s72, 1\n" \
  "s_add_u32 m0, s61, 65536\n" \
  "s_nop 0\n" \
  "buffer_load_dwordx4 v200, s[44:47], s60 offen lds\n" \
  "buffer_load_dwordx4 v201, s[44:47], s60 offen offset:1024 lds\n" \
  "buffer_load_dwordx4 v202, s[44:47], s60 offen offset:2048 lds\n" \
  "buffer_load_dwordx4 v203, s[44:47], s60 offen offset:3072 lds\n" \
  "s_add_u32 s60, %29, 2097152\n" \
  "s_lshl_b32 s61, s72, 1\n" \
  "s_add_u32 m0, s61, 81920\n" \
  "s_nop 0\n" \
  "buffer_load_dwordx4 v200, s[44:47], s60 offen lds\n" \
  "buffer_load_dwordx4 v201, s[44:47], s60 offen offset:1024 lds\n" \
  "buffer_load_dwordx4 v202, s[44:47], s60 offen offset:2048 lds\n" \
  "buffer_load_dwordx4 v203, s[44:47], s60 offen offset:3072 lds\n" \
  "s_add_u32 s60, %29, 2621440\n" \
  "s_lshl_b32 s61, s72, 1\n" \
  "s_add_u32 m0, s61, 98304\n" \
  "s_nop 0\n" \
  "buffer_load_dwordx4 v200, s[44:47], s60 offen lds\n" \
  "buffer_load_dwordx4 v201, s[44:47], s60 offen offset:1024 lds\n" \
  "buffer_load_dwordx4 v202, s[44:47], s60 offen offset:2048 lds\n" \
  "buffer_load_dwordx4 v203, s[44:47], s60 offen offset:3072 lds\n" \
  "s_add_u32 s60, %29, 3145728\n" \
  "s_lshl_b32 s61, s72, 1\n" \
  "s_add_u32 m0, s61, 114688\n" \
  "s_nop 0\n" \
  "buffer_load_dwordx4 v200, s[44:47], s60 offen lds\n" \
  "buffer_load_dwordx4 v201, s[44:47], s60 offen offset:1024 lds\n" \
  "buffer_load_dwordx4 v202, s[44:47], s60 offen offset:2048 lds\n" \
  "buffer_load_dwordx4 v203, s[44:47], s60 offen offset:3072 lds\n" \
  "s_add_u32 s60, %29, 3670016\n" \
  "s_lshl_b32 s61, s72, 1\n" \
  "s_add_u32 m0, s61, 131072\n" \
  "s_nop 0\n" \
  "buffer_load_dwordx4 v200, s[44:47], s60 offen lds\n" \
  "buffer_load_dwordx4 v201, s[44:47], s60 offen offset:1024 lds\n" \
  "buffer_load_dwordx4 v202, s[44:47], s60 offen offset:2048 lds\n" \
  "buffer_load_dwordx4 v203, s[44:47], s60 offen offset:3072 lds\n" \
  "s_add_u32 s60, %29, 4194304\n" \
  "s_lshl_b32 s61, s72, 1\n" \
  "s_add_u32 m0, s61, 147456\n" \
  "s_nop 0\n" \
  "buffer_load_dwordx4 v200, s[44:47], s60 offen lds\n" \
  "buffer_load_dwordx4 v201, s[44:47], s60 offen offset:1024 lds\n" \
  "buffer_load_dwordx4 v202, s[44:47], s60 offen offset:2048 lds\n" \
  "buffer_load_dwordx4 v203, s[44:47], s60 offen offset:3072 lds\n" \
  "s_mov_b64 s[44:45], %27\n" \
  "s_and_b32 s45, s45, 0xffff\n" \
  "s_lshr_b32 s60, s72, 3\n" \
  "s_mov_b32 m0, s72\n" \
  "s_nop 0\n" \
  "buffer_load_dwordx4 v206, s[44:47], s60 offen lds\n" \
  "s_add_u32 s60, s60, 128\n" \
  "s_add_u32 m0, s72, 1024\n" \
  "s_nop 0\n" \
  "buffer_load_dwordx4 v206, s[44:47], s60 offen lds\n" \
  "s_movk_i32 s60, 1024\n" \
  "s_movk_i32 m0, 8192\n" \
  "s_nop 0\n" \
  "buffer_load_dwordx4 v206, s[44:47], s60 offen lds\n" \
  "s_waitcnt vmcnt(27)\n" \
  "s_barrier\n" \
  "v_add_u32 v194, 16384, v204\n" \
  "ds_read_b64_tr_b16 a[0:1], v194\n" \
  "ds_read_b64_tr_b16 a[2:3], v194 offset:512\n" \
  "ds_read_b64_tr_b16 a[4:5], v194 offset:8192\n" \
  "ds_read_b64_tr_b16 a[6:7], v194 offset:8704\n" \
  "v_add_u32 v195, 16384, v205\n" \
  "ds_read_b64_tr_b16 a[72:73], v195\n" \
  "ds_read_b64_tr_b16 a[74:75], v195 offset:512\n" \
  "ds_read_b64_tr_b16 a[76:77], v195 offset:8192\n" \
  "ds_read_b64_tr_b16 a[78:79], v195 offset:8704\n" \
  "v_add_u32 v194, 32768, v204\n" \
  "ds_read_b64_tr_b16 a[8:9], v194\n" \
  "ds_read_b64_tr_b16 a[10:11], v194 offset:512\n" \
  "ds_read_b64_tr_b16 a[12:13], v194 offset:8192\n" \
  "ds_read_b64_tr_b16 a[14:15], v194 offset:8704\n" \
  "v_add_u32 v195, 32768, v205\n" \
  "ds_read_b64_tr_b16 a[80:81], v195\n" \
  "ds_read_b64_tr_b16 a[82:83], v195 offset:512\n" \
  "ds_read_b64_tr_b16 a[84:85], v195 offset:8192\n" \
  "ds_read_b64_tr_b16 a[86:87], v195 offset:8704\n" \
  "v_add_u32 v194, 49152, v204\n" \
  "ds_read_b64_tr_b16 a[16:17], v194\n" \
  "ds_read_b64_tr_b16 a[18:19], v194 offset:512\n" \
  "ds_read_b64_tr_b16 a[20:21], v194 offset:8192\n" \
  "ds_read_b64_tr_b16 a[22:23], v194 offset:8704\n" \
  "v_add_u32 v195, 49152, v205\n" \
  "ds_read_b64_tr_b16 a[88:89], v195\n" \
  "ds_read_b64_tr_b16 a[90:91], v195 offset:512\n" \
  "ds_read_b64_tr_b16 a[92:93], v195 offset:8192\n" \
  "ds_read_b64_tr_b16 a[94:95], v195 offset:8704\n" \
  "s_waitcnt lgkmcnt(0)\n" \
  "s_barrier\n" \
  "s_mov_b64 s[44:45], %26\n" \
  "s_and_b32 s45, s45, 0xffff\n" \
  "s_add_u32 s60, %29, 0\n" \
  "s_lshl_b32 s61, s72, 1\n" \
  "s_add_u32 m0, s61, 16384\n" \
  "s_nop 0\n" \
  "buffer_load_dwordx4 v200, s[44:47], s60 offen lds\n" \
  "buffer_load_dwordx4 v201, s[44:47], s60 offen offset:1024 lds\n" \
  "buffer_load_dwordx4 v202, s[44:47], s60 offen offset:2048 lds\n" \
  "buffer_load_dwordx4 v203, s[44:47], s60 offen offset:3072 lds\n" \
  "s_add_u32 s60, %29, 524288\n" \
  "s_lshl_b32 s61, s72, 1\n" \
  "s_add_u32 m0, s61, 32768\n" \
  "s_nop 0\n" \
  "buffer_load_dwordx4 v200, s[44:47], s60 offen lds\n" \
  "buffer_load_dwordx4 v201, s[44:47], s60 offen offset:1024 lds\n" \
  "buffer_load_dwordx4 v202, s[44:47], s60 offen offset:2048 lds\n" \
  "buffer_load_dwordx4 v203, s[44:47], s60 offen offset:3072 lds\n" \
  "s_add_u32 s60, %29, 1048576\n" \
  "s_lshl_b32 s61, s72, 1\n" \
  "s_add_u32 m0, s61, 49152\n" \
  "s_nop 0\n" \
  "buffer_load_dwordx4 v200, s[44:47], s60 offen lds\n" \
  "buffer_load_dwordx4 v201, s[44:47], s60 offen offset:1024 lds\n" \
  "buffer_load_dwordx4 v202, s[44:47], s60 offen offset:2048 lds\n" \
  "buffer_load_dwordx4 v203, s[44:47], s60 offen offset:3072 lds\n" \
  "v_readfirstlane_b32 s55, v196\n" \
  "s_add_u32 s55, s55, 1\n" \
  "s_waitcnt vmcnt(27)\n" \
  "s_barrier\n" \
  "v_add_u32 v194, 65536, v204\n" \
  "ds_read_b64_tr_b16 a[24:25], v194\n" \
  "ds_read_b64_tr_b16 a[26:27], v194 offset:512\n" \
  "ds_read_b64_tr_b16 a[28:29], v194 offset:8192\n" \
  "ds_read_b64_tr_b16 a[30:31], v194 offset:8704\n" \
  "v_add_u32 v195, 65536, v205\n" \
  "ds_read_b64_tr_b16 a[96:97], v195\n" \
  "ds_read_b64_tr_b16 a[98:99], v195 offset:512\n" \
  "ds_read_b64_tr_b16 a[100:101], v195 offset:8192\n" \
  "ds_read_b64_tr_b16 a[102:103], v195 offset:8704\n" \
  "v_add_u32 v194, 81920, v204\n" \
  "ds_read_b64_tr_b16 a[32:33], v194\n" \
  "ds_read_b64_tr_b16 a[34:35], v194 offset:512\n" \
  "ds_read_b64_tr_b16 a[36:37], v194 offset:8192\n" \
  "ds_read_b64_tr_b16 a[38:39], v194 offset:8704\n" \
  "v_add_u32 v195, 81920, v205\n" \
  "ds_read_b64_tr_b16 a[104:105], v195\n" \
  "ds_read_b64_tr_b16 a[106:107], v195 offset:512\n" \
  "ds_read_b64_tr_b16 a[108:109], v195 offset:8192\n" \
  "ds_read_b64_tr_b16 a[110:111], v195 offset:8704\n" \
  "v_add_u32 v194, 98304, v204\n" \
  "ds_read_b64_tr_b16 a[40:41], v194\n" \
  "ds_read_b64_tr_b16 a[42:43], v194 offset:512\n" \
  "ds_read_b64_tr_b16 a[44:45], v194 offset:8192\n" \
  "ds_read_b64_tr_b16 a[46:47], v194 offset:8704\n" \
  "v_add_u32 v195, 98304, v205\n" \
  "ds_read_b64_tr_b16 a[112:113], v195\n" \
  "ds_read_b64_tr_b16 a[114:115], v195 offset:512\n" \
  "ds_read_b64_tr_b16 a[116:117], v195 offset:8192\n" \
  "ds_read_b64_tr_b16 a[118:119], v195 offset:8704\n" \
  "s_waitcnt lgkmcnt(0)\n" \
  "s_barrier\n" \
  "s_add_u32 s60, %29, 1572864\n" \
  "s_lshl_b32 s61, s72, 1\n" \
  "s_add_u32 m0, s61, 65536\n" \
  "s_nop 0\n" \
  "buffer_load_dwordx4 v200, s[44:47], s60 offen lds\n" \
  "buffer_load_dwordx4 v201, s[44:47], s60 offen offset:1024 lds\n" \
  "buffer_load_dwordx4 v202, s[44:47], s60 offen offset:2048 lds\n" \
  "buffer_load_dwordx4 v203, s[44:47], s60 offen offset:3072 lds\n" \
  "s_add_u32 s60, %29, 2097152\n" \
  "s_lshl_b32 s61, s72, 1\n" \
  "s_add_u32 m0, s61, 81920\n" \
  "s_nop 0\n" \
  "buffer_load_dwordx4 v200, s[44:47], s60 offen lds\n" \
  "buffer_load_dwordx4 v201, s[44:47], s60 offen offset:1024 lds\n" \
  "buffer_load_dwordx4 v202, s[44:47], s60 offen offset:2048 lds\n" \
  "buffer_load_dwordx4 v203, s[44:47], s60 offen offset:3072 lds\n" \
  "s_add_u32 s60, %29, 2621440\n" \
  "s_lshl_b32 s61, s72, 1\n" \
  "s_add_u32 m0, s61, 98304\n" \
  "s_nop 0\n" \
  "buffer_load_dwordx4 v200, s[44:47], s60 offen lds\n" \
  "buffer_load_dwordx4 v201, s[44:47], s60 offen offset:1024 lds\n" \
  "buffer_load_dwordx4 v202, s[44:47], s60 offen offset:2048 lds\n" \
  "buffer_load_dwordx4 v203, s[44:47], s60 offen offset:3072 lds\n" \
  "s_waitcnt vmcnt(27)\n" \
  "s_barrier\n" \
  "v_add_u32 v194, 114688, v204\n" \
  "ds_read_b64_tr_b16 a[48:49], v194\n" \
  "ds_read_b64_tr_b16 a[50:51], v194 offset:512\n" \
  "ds_read_b64_tr_b16 a[52:53], v194 offset:8192\n" \
  "ds_read_b64_tr_b16 a[54:55], v194 offset:8704\n" \
  "v_add_u32 v195, 114688, v205\n" \
  "ds_read_b64_tr_b16 a[120:121], v195\n" \
  "ds_read_b64_tr_b16 a[122:123], v195 offset:512\n" \
  "ds_read_b64_tr_b16 a[124:125], v195 offset:8192\n" \
  "ds_read_b64_tr_b16 a[126:127], v195 offset:8704\n" \
  "v_add_u32 v194, 131072, v204\n" \
  "ds_read_b64_tr_b16 a[56:57], v194\n" \
  "ds_read_b64_tr_b16 a[58:59], v194 offset:512\n" \
  "ds_read_b64_tr_b16 a[60:61], v194 offset:8192\n" \
  "ds_read_b64_tr_b16 a[62:63], v194 offset:8704\n" \
  "v_add_u32 v195, 131072, v205\n" \
  "ds_read_b64_tr_b16 a[128:129], v195\n" \
  "ds_read_b64_tr_b16 a[130:131], v195 offset:512\n" \
  "ds_read_b64_tr_b16 a[132:133], v195 offset:8192\n" \
  "ds_read_b64_tr_b16 a[134:135], v195 offset:8704\n" \
  "v_add_u32 v194, 147456, v204\n" \
  "ds_read_b64_tr_b16 a[64:65], v194\n" \
  "ds_read_b64_tr_b16 a[66:67], v194 offset:512\n" \
  "ds_read_b64_tr_b16 a[68:69], v194 offset:8192\n" \
  "ds_read_b64_tr_b16 a[70:71], v194 offset:8704\n" \
  "v_add_u32 v195, 147456, v205\n" \
  "ds_read_b64_tr_b16 a[136:137], v195\n" \
  "ds_read_b64_tr_b16 a[138:139], v195 offset:512\n" \
  "ds_read_b64_tr_b16 a[140:141], v195 offset:8192\n" \
  "ds_read_b64_tr_b16 a[142:143], v195 offset:8704\n" \
  "s_waitcnt lgkmcnt(0)\n" \
  "s_barrier\n" \
  "s_add_u32 s60, %29, 3145728\n" \
  "s_lshl_b32 s61, s72, 1\n" \
  "s_add_u32 m0, s61, 114688\n" \
  "s_nop 0\n" \
  "buffer_load_dwordx4 v200, s[44:47], s60 offen lds\n" \
  "buffer_load_dwordx4 v201, s[44:47], s60 offen offset:1024 lds\n" \
  "buffer_load_dwordx4 v202, s[44:47], s60 offen offset:2048 lds\n" \
  "buffer_load_dwordx4 v203, s[44:47], s60 offen offset:3072 lds\n" \
  "s_add_u32 s60, %29, 3670016\n" \
  "s_lshl_b32 s61, s72, 1\n" \
  "s_add_u32 m0, s61, 131072\n" \
  "s_nop 0\n" \
  "buffer_load_dwordx4 v200, s[44:47], s60 offen lds\n" \
  "buffer_load_dwordx4 v201, s[44:47], s60 offen offset:1024 lds\n" \
  "buffer_load_dwordx4 v202, s[44:47], s60 offen offset:2048 lds\n" \
  "buffer_load_dwordx4 v203, s[44:47], s60 offen offset:3072 lds\n" \
  "s_add_u32 s60, %29, 4194304\n" \
  "s_lshl_b32 s61, s72, 1\n" \
  "s_add_u32 m0, s61, 147456\n" \
  "s_nop 0\n" \
  "buffer_load_dwordx4 v200, s[44:47], s60 offen lds\n" \
  "buffer_load_dwordx4 v201, s[44:47], s60 offen offset:1024 lds\n" \
  "buffer_load_dwordx4 v202, s[44:47], s60 offen offset:2048 lds\n" \
  "buffer_load_dwordx4 v203, s[44:47], s60 offen offset:3072 lds\n" \
  "buffer_load_dwordx2 v[198:199], off, s[40:43], %39 sc1\n" \
  "s_waitcnt vmcnt(25)\n" \
  "s_barrier\n" \
  "v_add_u32 v195, 16384, v205\n" \
  "ds_read_b64_tr_b16 a[216:217], v195\n" \
  "ds_read_b64_tr_b16 a[218:219], v195 offset:512\n" \
  "ds_read_b64_tr_b16 a[220:221], v195 offset:8192\n" \
  "ds_read_b64_tr_b16 a[222:223], v195 offset:8704\n" \
  "v_add_u32 v195, 32768, v205\n" \
  "ds_read_b64_tr_b16 a[224:225], v195\n" \
  "ds_read_b64_tr_b16 a[226:227], v195 offset:512\n" \
  "ds_read_b64_tr_b16 a[228:229], v195 offset:8192\n" \
  "ds_read_b64_tr_b16 a[230:231], v195 offset:8704\n" \
  "v_add_u32 v195, 49152, v205\n" \
  "ds_read_b64_tr_b16 a[232:233], v195\n" \
  "ds_read_b64_tr_b16 a[234:235], v195 offset:512\n" \
  "ds_read_b64_tr_b16 a[236:237], v195 offset:8192\n" \
  "ds_read_b64_tr_b16 a[238:239], v195 offset:8704\n" \
  "s_waitcnt lgkmcnt(0)\n" \
  "s_waitcnt vmcnt(13)\n" \
  "s_barrier\n" \
  "v_add_u32 v195, 65536, v205\n" \
  "ds_read_b64_tr_b16 a[240:241], v195\n" \
  "ds_read_b64_tr_b16 a[242:243], v195 offset:512\n" \
  "ds_read_b64_tr_b16 a[244:245], v195 offset:8192\n" \
  "ds_read_b64_tr_b16 a[246:247], v195 offset:8704\n" \
  "v_add_u32 v194, 81920, v204\n" \
  "ds_read_b64_tr_b16 a[176:177], v194\n" \
  "ds_read_b64_tr_b16 a[178:179], v194 offset:512\n" \
  "ds_read_b64_tr_b16 a[180:181], v194 offset:8192\n" \
  "ds_read_b64_tr_b16 a[182:183], v194 offset:8704\n" \
  "v_add_u32 v195, 81920, v205\n" \
  "ds_read_b64_tr_b16 a[248:249], v195\n" \
  "ds_read_b64_tr_b16 a[250:251], v195 offset:512\n" \
  "ds_read_b64_tr_b16 a[252:253], v195 offset:8192\n" \
  "ds_read_b64_tr_b16 a[254:255], v195 offset:8704\n" \
  "v_add_u32 v194, 98304, v204\n" \
  "ds_read_b64_tr_b16 a[184:185], v194\n" \
  "ds_read_b64_tr_b16 a[186:187], v194 offset:512\n" \
  "ds_read_b64_tr_b16 a[188:189], v194 offset:8192\n" \
  "ds_read_b64_tr_b16 a[190:191], v194 offset:8704\n" \
  "v_add_u32 v195, 98304, v205\n" \
  "ds_read_b64_tr_b16 v[0:1], v195\n" \
  "ds_read_b64_tr_b16 v[2:3], v195 offset:512\n" \
  "ds_read_b64_tr_b16 v[4:5], v195 offset:8192\n" \
  "ds_read_b64_tr_b16 v[6:7], v195 offset:8704\n" \
  "s_waitcnt lgkmcnt(0)\n" \
  "s_waitcnt vmcnt(1)\n" \
  "s_barrier\n" \
  "v_add_u32 v194, 114688, v204\n" \
  "ds_read_b64_tr_b16 a[192:193], v194\n" \
  "ds_read_b64_tr_b16 a[194:195], v194 offset:512\n" \
  "ds_read_b64_tr_b16 a[196:197], v194 offset:8192\n" \
  "ds_read_b64_tr_b16 a[198:199], v194 offset:8704\n" \
  "v_add_u32 v195, 114688, v205\n" \
  "ds_read_b64_tr_b16 v[8:9], v195\n" \
  "ds_read_b64_tr_b16 v[10:11], v195 offset:512\n" \
  "ds_read_b64_tr_b16 v[12:13], v195 offset:8192\n" \
  "ds_read_b64_tr_b16 v[14:15], v195 offset:8704\n" \
  "v_add_u32 v194, 131072, v204\n" \
  "ds_read_b64_tr_b16 a[200:201], v194\n" \
  "ds_read_b64_tr_b16 a[202:203], v194 offset:512\n" \
  "ds_read_b64_tr_b16 a[204:205], v194 offset:8192\n" \
  "ds_read_b64_tr_b16 a[206:207], v194 offset:8704\n" \
  "v_add_u32 v195, 131072, v205\n" \
  "ds_read_b64_tr_b16 v[16:17], v195\n" \
  "ds_read_b64_tr_b16 v[18:19], v195 offset:512\n" \
  "ds_read_b64_tr_b16 v[20:21], v195 offset:8192\n" \
  "ds_read_b64_tr_b16 v[22:23], v195 offset:8704\n" \
  "v_add_u32 v194, 147456, v204\n" \
  "ds_read_b64_tr_b16 a[208:209], v194\n" \
  "ds_read_b64_tr_b16 a[210:211], v194 offset:512\n" \
  "ds_read_b64_tr_b16 a[212:213], v194 offset:8192\n" \
  "ds_read_b64_tr_b16 a[214:215], v194 offset:8704\n" \
  "v_add_u32 v195, 147456, v205\n" \
  "ds_read_b64_tr_b16 v[24:25], v195\n" \
  "ds_read_b64_tr_b16 v[26:27], v195 offset:512\n" \
  "ds_read_b64_tr_b16 v[28:29], v195 offset:8192\n" \
  "ds_read_b64_tr_b16 v[30:31], v195 offset:8704\n" \
  "s_waitcnt lgkmcnt(0)\n" \
  "s_waitcnt vmcnt(0)\n" \
  "v_readfirstlane_b32 s96, v198\n" \
  "v_readfirstlane_b32 s63, v199\n" \
  "s_cmp_eq_u32 s63, s55\n" \
  "s_cselect_b32 s96, s96, 0\n" \
  "s_cmp_ge_u32 s96, 0xc0000000\n" \
  "s_cbranch_scc1 .LX_go_x_%=\n" \
  "s_mov_b32 s69, 0\n" \
  ".LX_go_%=:\n" \
  "buffer_load_dwordx2 v[196:197], off, s[40:43], %39 sc1\n" \
  "s_waitcnt vmcnt(0)\n" \
  "v_readfirstlane_b32 s96, v196\n" \
  "v_readfirstlane_b32 s63, v197\n" \
  "s_cmp_eq_u32 s63, s55\n" \
  "s_cselect_b32 s96, s96, 0\n" \
  "s_cmp_ge_u32 s96, 0xc0000000\n" \
  "s_cbranch_scc1 .LX_go_x_%=\n" \
  "s_sleep 8\n" \
  "s_add_u32 s69, s69, 1\n" \
  "s_and_b32 s63, s69, 1023\n" \
  "s_cbranch_scc1 .LX_go_%=\n" \
  "s_cmp_gt_u32 s69, 2097152\n" \
  "s_cbranch_scc1 .LX_go_set_%=\n" \
  "buffer_load_dwordx2 v[196:197], off, s[40:43], %40 offset:128 sc1\n" \
  "s_waitcnt vmcnt(0)\n" \
  "v_readfirstlane_b32 s63, v197\n" \
  "s_cmp_eq_u32 s63, s55\n" \
  "s_cbranch_scc0 .LX_go_%=\n" \
  "s_branch .LX_go_x_%=\n" \
  ".LX_go_set_%=:\n" \
  "v_mov_b32 v196, 1\n" \
  "v_mov_b32 v197, s55\n" \
  "buffer_store_dwordx2 v[196:197], off, s[40:43], %40 offset:128 sc1\n" \
  "s_waitcnt vmcnt(0)\n" \
  ".LX_go_x_%=:\n" \
  "s_lshr_b32 s60, s96, 0\n" \
  "s_and_b32 s60, s60, 7\n" \
  "s_lshl_b32 s60, s60, 12\n" \
  "s_xor_b32 s74, %35, s60\n" \
  "s_xor_b32 s75, %38, s60\n" \
  "s_bfe_u32 s56, s96, 0x30010\n" \
  "s_lshl_b32 s56, s56, 12\n" \
  "s_or_b32 s56, s56, 0x200000\n" \
  "s_mov_b32 s57, 1052672\n" \
  "s_lshl_b32 s61, %34, 2\n" \
  "s_lshr_b32 s51, s96, s61\n" \
  "s_and_b32 s51, s51, 7\n" \
  "s_lshl_b32 s51, s51, 12\n" \
  "s_xor_b32 s51, s51, 4210688\n" \
  "s_mov_b32 s71, 0\n" \
  "v_mov_b64 v[32:33], -1\n" \
  "v_mov_b64 v[34:35], -1\n" \
  "v_and_b32 v194, 3, v246\n" \
  "v_lshlrev_b32 v194, 2, v194\n" \
  "v_lshrrev_b32 v194, v194, s96\n" \
  "v_and_b32 v194, 7, v194\n" \
  "v_lshlrev_b32 v194, 12, v194\n" \
  "v_and_b32 v246, -4, v246\n" \
  "v_xor_b32 v246, v246, v194\n" \
  "v_and_b32 v194, 3, v247\n" \
  "v_lshlrev_b32 v194, 2, v194\n" \
  "v_lshrrev_b32 v194, v194, s96\n" \
  "v_and_b32 v194, 7, v194\n" \
  "v_lshlrev_b32 v194, 12, v194\n" \
  "v_and_b32 v247, -4, v247\n" \
  "v_xor_b32 v247, v247, v194\n" \
  "v_xor_b32 v190, s56, v246\n" \
  "buffer_store_dwordx2 v[32:33], v190, s[40:43], 0 offen sc1\n" \
  "v_xor_b32 v191, s56, v247\n" \
  "buffer_store_dwordx2 v[32:33], v191, s[40:43], 0 offen sc1\n" \
  "s_xor_b32 s62, s56, s57\n" \
  "s_nop 1\n" \
  "v_xor_b32 v190, s62, v246\n" \
  "buffer_store_dwordx2 v[32:33], v190, s[40:43], 0 offen sc1\n" \
  "v_xor_b32 v191, s62, v247\n" \
  "buffer_store_dwordx2 v[32:33], v191, s[40:43], 0 offen sc1\n" \
  "s_andn2_b32 s60, 2, s96\n" \
  "s_lshl_b32 s60, s60, 12\n" \
  "s_xor_b32 s60, s60, 8192\n" \
  "s_xor_b32 s58, %31, s60\n" \
  "s_mov_b32 s49, 0\n" \
  "s_add_u32 s47, %45, s60\n" \
  "s_add_u32 s97, s60, 8388608\n" \
  "s_xor_b32 s50, %32, s60\n" \
  "s_mov_b32 s48, 0\n" \
  "s_mov_b32 s54, 0\n" \
  "s_cmp_eq_u32 %28, 0\n" \
  "s_cselect_b64 s[88:89], -1, 0\n" \
  "s_mov_b32 s68, 0\n" \
  "s_mov_b32 s70, 1\n" \
  "s_mov_b32 s73, 0\n" \
  "s_mov_b32 s44, 0\n" \
  "s_mov_b32 s53, 0\n" \
  "s_memrealtime s[86:87]\n" \
  "s_memtime s[76:77]\n" \
  "s_waitcnt lgkmcnt(0)\n" \
  "s_mov_b32 s90, s76\n" \
  "s_mov_b32 s87, 340\n" \
  "s_mov_b32 s64, 0x3e0293ee\n" \
  "s_mov_b32 s65, 0xbe0293ee\n" \
  "v_mov_b64 v[40:41], 0\n" \
  "v_mov_b64 v[42:43], 0\n" \
  "v_mov_b64 v[44:45], 0\n" \
  "v_mov_b64 v[46:47], 0\n" \
  "v_mov_b64 v[48:49], 0\n" \
  "v_mov_b64 v[50:51], 0\n" \
  "v_mov_b64 v[52:53], 0\n" \
  "v_mov_b64 v[54:55], 0\n" \
  "v_mov_b32 v36, 0\n" \
  "v_mov_b32 v37, s55\n" \
  "v_mov_b64 v[38:39], v[36:37]\n" \
  "s_mov_b64 s[92:93], 0\n" \
  "s_mov_b32 s92, 0xffff0000\n" \
  "s_lshr_b32 s60, s72, 1\n" \
  "s_add_u32 s60, s60, 32768\n" \
  "v_add_u32 v179, s60, v240\n" \
  "v_xor_b32 v246, 1073741824, v246\n" \
  "v_xor_b32 v247, 1073741824, v247\n" \
  "s_lshr_b32 s60, s72, 9\n" \
  "s_add_u32 s60, s60, 17408\n" \
  "v_mov_b32 v184, s60\n" \
  "v_mov_b32 v182, 1065369472\n" \
  "v_mov_b32 v183, 1065369472\n" \
  "s_mov_b32 s52, 0\n" \
  "s_barrier\n" \
  "s_branch .LX_xr_%=\n" \
  ".LX_dry_b_%=:\n" \
  "s_waitcnt vmcnt(0)\n" \
  "s_barrier\n" \
  "ds_read_b128 v[60:63], v179\n" \
  "ds_read_b128 v[64:67], v179 offset:2048\n" \
  "ds_read_b128 v[68:71], v179 offset:4096\n" \
  "ds_read_b128 v[72:75], v179 offset:6144\n" \
  "v_xor_b32 v179, 8192, v179\n" \
  "s_mov_b32 s67, 2130706432\n" \
  "s_waitcnt vmcnt(0) lgkmcnt(0)\n" \
  "s_barrier\n" \
  "s_branch .LX_p_%=\n" \
  "s_nop 7\n" \
  ".p2align 6\n" \
  ".fill (((44 - (.LX_p_%= - .LX_rot_%=)) & 63) >> 2), 4, 0xBF800000\n" \
  ".LX_rot_%=:\n" \
  "s_xor_b32 s51, s51, s56\n" \
  "s_xor_b32 s71, s71, s56\n" \
  "v_xor_b32 v246, s56, v246\n" \
  "v_xor_b32 v247, s56, v247\n" \
  "s_mov_b32 s60, s56\n" \
  "s_mov_b32 s56, s57\n" \
  "s_mov_b32 s57, s60\n" \
  "s_add_u32 s59, s51, 819200\n" \
  "s_mov_b32 m0, 0\n" \
  "s_mov_b32 s70, 0\n" \
  "s_mov_b32 s68, 0\n" \
  "s_waitcnt lgkmcnt(0)\n" \
  "s_cmp_lg_u32 %28, 0\n" \
  "s_cbranch_scc1 .LX_g3_%=\n" \
  "s_mov_b32 s69, 0\n" \
  ".LX_tw_%=:\n" \
  "v_readfirstlane_b32 s63, v96\n" \
  "s_cmp_lg_u32 s63, s48\n" \
  "s_cbranch_scc1 .LX_tw_re_%=\n" \
  "v_readfirstlane_b32 s53, v98\n" \
  ".LX_rt_%=:\n" \
  "s_memrealtime s[60:61]\n" \
  "s_add_u32 s73, s53, s44\n" \
  "s_waitcnt lgkmcnt(0)\n" \
  "s_sub_u32 s62, s73, s60\n" \
  "s_cmp_ge_u32 s62, 2000\n" \
  "s_cbranch_scc1 .LX_lat_%=\n" \
  "s_sub_u32 s62, s62, 30\n" \
  ".LX_slp_%=:\n" \
  "s_cmp_le_i32 s62, 0\n" \
  "s_cbranch_scc1 .LX_clk_%=\n" \
  "s_sleep 1\n" \
  "s_sub_u32 s62, s62, 3\n" \
  "s_branch .LX_slp_%=\n" \
  ".LX_clk_%=:\n" \
  "s_memrealtime s[60:61]\n" \
  "s_waitcnt lgkmcnt(0)\n" \
  "s_sub_u32 s62, s73, s60\n" \
  "s_cmp_gt_i32 s62, 21\n" \
  "s_cbranch_scc1 .LX_clk_%=\n" \
  "s_cmp_le_i32 s62, 11\n" \
  "s_cbranch_scc1 .LX_clk2_%=\n" \
  "s_memrealtime s[76:77]\n" \
  "s_nop 4\n" \
  "s_memrealtime s[78:79]\n" \
  "s_nop 4\n" \
  "s_memrealtime s[80:81]\n" \
  "s_nop 4\n" \
  "s_memrealtime s[82:83]\n" \
  "s_waitcnt lgkmcnt(0)\n" \
  "s_mov_b32 s77, 1\n" \
  "s_cmp_eq_u32 s78, s76\n" \
  "s_addc_u32 s77, s77, 0\n" \
  "s_cmp_eq_u32 s80, s76\n" \
  "s_addc_u32 s77, s77, 0\n" \
  "s_cmp_eq_u32 s82, s76\n" \
  "s_addc_u32 s77, s77, 0\n" \
  "s_mul_i32 s77, s77, 24\n" \
  "s_lshr_b32 s78, s87, 2\n" \
  "s_sub_u32 s77, s78, s77\n" \
  "s_max_i32 s77, s77, 0\n" \
  "s_sub_u32 s76, s73, s76\n" \
  "s_mul_i32 s76, s76, s87\n" \
  "s_ashr_i32 s76, s76, 4\n" \
  "s_sub_u32 s76, s76, s77\n" \
  "s_sub_u32 s76, s76, 200\n" \
  "s_max_i32 s76, s76, 0\n" \
  "s_min_i32 s76, s76, 320\n" \
  "s_lshr_b32 s76, s76, 3\n" \
  "s_lshl_b32 s76, s76, 2\n" \
  "s_mov_b32 s68, 1\n" \
  "s_getpc_b64 s[78:79]\n" \
  "s_sub_u32 s76, 180, s76\n" \
  "s_add_u32 s78, s78, s76\n" \
  "s_addc_u32 s79, s79, 0\n" \
  "s_setpc_b64 s[78:79]\n" \
  "s_nop 1\n" \
  "s_nop 1\n" \
  "s_nop 1\n" \
  "s_nop 1\n" \
  "s_nop 1\n" \
  "s_nop 1\n" \
  "s_nop 1\n" \
  "s_nop 1\n" \
  "s_nop 1\n" \
  "s_nop 1\n" \
  "s_nop 1\n" \
  "s_nop 1\n" \
  "s_nop 1\n" \
  "s_nop 1\n" \
  "s_nop 1\n" \
  "s_nop 1\n" \
  "s_nop 1\n" \
  "s_nop 1\n" \
  "s_nop 1\n" \
  "s_nop 1\n" \
  "s_nop 1\n" \
  "s_nop 1\n" \
  "s_nop 1\n" \
  "s_nop 1\n" \
  "s_nop 1\n" \
  "s_nop 1\n" \
  "s_nop 1\n" \
  "s_nop 1\n" \
  "s_nop 1\n" \
  "s_nop 1\n" \
  "s_nop 1\n" \
  "s_nop 1\n" \
  "s_nop 1\n" \
  "s_nop 1\n" \
  "s_nop 1\n" \
  "s_nop 1\n" \
  "s_nop 1\n" \
  "s_nop 1\n" \
  "s_nop 1\n" \
  "s_nop 1\n" \
  ".LX_noclk_%=:\n" \
  "buffer_load_dwordx4 v240, s[40:43], s59 offen sc1 lds\n" \
  "s_memtime s[66:67]\n" \
  "buffer_load_dwordx4 v240, s[40:43], s59 offen offset:1024 sc1 lds\n" \
  "buffer_load_dwordx4 v240, s[40:43], s59 offen offset:2048 sc1 lds\n" \
  "buffer_load_dwordx4 v240, s[40:43], s59 offen offset:3072 sc1 lds\n" \
  "s_add_u32 s60, s59, 32768\n" \
  "s_movk_i32 m0, 4096\n" \
  "s_nop 0\n" \
  "buffer_load_dwordx4 v240, s[40:43], s60 offen sc1 lds\n" \
  "buffer_load_dwordx4 v240, s[40:43], s60 offen offset:1024 sc1 lds\n" \
  "buffer_load_dwordx4 v240, s[40:43], s60 offen offset:2048 sc1 lds\n" \
  "buffer_load_dwordx4 v240, s[40:43], s60 offen offset:3072 sc1 lds\n" \
  "s_add_u32 s60, s59, 65536\n" \
  "s_movk_i32 m0, 8192\n" \
  "s_nop 0\n" \
  "buffer_load_dwordx4 v240, s[40:43], s60 offen sc1 lds\n" \
  ".LX_g3_%=:\n" \
  "s_waitcnt vmcnt(6)\n" \
  "s_barrier\n" \
  ".LX_p_%=:\n" \
  "ds_read_b128 v[96:99], v240\n" \
  "ds_read_b128 v[100:103], v240 offset:1024\n" \
  "ds_read_b128 v[104:107], v240 offset:2048\n" \
  "s_waitcnt lgkmcnt(2)\n" \
  "v_smfmac_f32_16x16x64_bf16 v[40:43], v[96:99], a[0:7], v239\n" \
  "s_waitcnt lgkmcnt(1)\n" \
  "v_smfmac_f32_16x16x64_bf16 v[40:43], v[100:103], a[8:15], v239\n" \
  "s_waitcnt vmcnt(0)\n" \
  "s_barrier\n" \
  "v_smfmac_f32_16x16x64_bf16 v[44:47], v[96:99], a[72:79], v239\n" \
  "ds_read_b128 v[108:111], v240 offset:3072\n" \
  "ds_read_b128 v[112:115], v240 offset:4096\n" \
  "s_waitcnt lgkmcnt(2)\n" \
  "v_smfmac_f32_16x16x64_bf16 v[40:43], v[104:107], a[16:23], v239\n" \
  "ds_read_b128 v[116:119], v240 offset:6144\n" \
  "v_smfmac_f32_16x16x64_bf16 v[44:47], v[100:103], a[80:87], v239\n" \
  "ds_read_b128 v[120:123], v240 offset:7168\n" \
  "v_smfmac_f32_16x16x64_bf16 v[44:47], v[104:107], a[88:95], v239\n" \
  "ds_read_b128 v[124:127], v240 offset:5120\n" \
  "s_waitcnt lgkmcnt(4)\n" \
  "v_smfmac_f32_16x16x64_bf16 v[44:47], v[108:111], a[96:103], v239\n" \
  "s_waitcnt lgkmcnt(3)\n" \
  "v_smfmac_f32_16x16x64_bf16 v[44:47], v[112:115], a[104:111], v239\n" \
  "v_smfmac_f32_16x16x64_bf16 v[40:43], v[108:111], a[24:31], v239\n" \
  "ds_read_b128 v[128:131], v240 offset:8192\n" \
  "v_smfmac_f32_16x16x64_bf16 v[40:43], v[112:115], a[32:39], v239\n" \
  "s_waitcnt lgkmcnt(1)\n" \
  "v_smfmac_f32_16x16x64_bf16 v[40:43], v[124:127], a[40:47], v239\n" \
  "v_smfmac_f32_16x16x64_bf16 v[40:43], v[116:119], a[48:55], v239\n" \
  "v_smfmac_f32_16x16x64_bf16 v[40:43], v[120:123], a[56:63], v239\n" \
  "s_waitcnt lgkmcnt(0)\n" \
  "v_smfmac_f32_16x16x64_bf16 v[40:43], v[128:131], a[64:71], v239\n" \
  "s_nop 2\n" \
  "v_smfmac_f32_16x16x64_bf16 v[44:47], v[124:127], a[112:119], v239\n" \
  "s_nop 2\n" \
  "v_smfmac_f32_16x16x64_bf16 v[44:47], v[116:119], a[120:127], v239\n" \
  "v_add_f32 v41, v41, v43\n" \
  "v_smfmac_f32_16x16x64_bf16 v[44:47], v[120:123], a[128:135], v239\n" \
  "v_add_f32 v40, v40, v42\n" \
  "v_smfmac_f32_16x16x64_bf16 v[44:47], v[128:131], a[136:143], v239\n" \
  "s_nop 1\n" \
  "v_cmp_u_f32 vcc, v40, v41\n" \
  "v_smfmac_f32_16x16x64_bf16 v[48:51], v[112:115], a[176:183], v239\n" \
  "s_cbranch_vccnz .LX_slow_%=\n" \
  ".LX_back_%=:\n" \
  "s_cmp_eq_u32 s48, 63\n" \
  "v_mul_f32 v40, s64, v40\n" \
  "v_mul_f32 v41, s64, v41\n" \
  "v_add_f32 v44, v44, v46\n" \
  "v_add_f32 v45, v45, v47\n" \
  "v_cvt_pk_bf16_f32 v40, v40, v41\n" \
  "v_mul_f32 v44, s64, v44\n" \
  "v_smfmac_f32_16x16x64_bf16 v[52:55], v[96:99], a[216:223], v239\n" \
  "v_mul_f32 v45, s64, v45\n" \
  "v_smfmac_f32_16x16x64_bf16 v[48:51], v[124:127], a[184:191], v239\n" \
  "ds_write_b32 v241, v40\n" \
  "v_cvt_pk_bf16_f32 v44, v44, v45\n" \
  "v_smfmac_f32_16x16x64_bf16 v[48:51], v[116:119], a[192:199], v239\n" \
  "ds_write_b32 v241, v44 offset:256\n" \
  "v_smfmac_f32_16x16x64_bf16 v[48:51], v[120:123], a[200:207], v239\n" \
  "v_smfmac_f32_16x16x64_bf16 v[52:55], v[100:103], a[224:231], v239\n" \
  "v_smfmac_f32_16x16x64_bf16 v[52:55], v[104:107], a[232:239], v239\n" \
  "v_smfmac_f32_16x16x64_bf16 v[48:51], v[128:131], a[208:215], v239\n" \
  "s_waitcnt lgkmcnt(0)\n" \
  "s_barrier\n" \
  "v_smfmac_f32_16x16x64_bf16 v[52:55], v[108:111], a[240:247], v239\n" \
  "ds_read_b64_tr_b16 v[96:97], v243\n" \
  "v_smfmac_f32_16x16x64_bf16 v[52:55], v[112:115], a[248:255], v239\n" \
  "ds_read_b64_tr_b16 v[102:103], v243 offset:1280\n" \
  "ds_read_b64_tr_b16 v[106:107], v243 offset:1792\n" \
  "v_smfmac_f32_16x16x64_bf16 v[52:55], v[124:127], v[0:7], v239\n" \
  "ds_read_b64_tr_b16 v[108:109], v243 offset:512\n" \
  "ds_read_b64_tr_b16 v[98:99], v243 offset:256\n" \
  "v_smfmac_f32_16x16x64_bf16 v[52:55], v[116:119], v[8:15], v239\n" \
  "ds_read_b64_tr_b16 v[110:111], v243 offset:768\n" \
  "ds_read_b64_tr_b16 v[100:101], v243 offset:1024\n" \
  "v_smfmac_f32_16x16x64_bf16 v[52:55], v[120:123], v[16:23], v239\n" \
  "ds_read_b64_tr_b16 v[104:105], v243 offset:1536\n" \
  "v_add_f32 v48, v48, v50\n" \
  "v_smfmac_f32_16x16x64_bf16 v[52:55], v[128:131], v[24:31], v239\n" \
  "s_waitcnt lgkmcnt(3)\n" \
  "v_mfma_f32_16x16x32_bf16 v[112:115], v[60:63], v[96:99], 0\n" \
  "s_barrier\n" \
  "ds_read_b64 v[188:189], v255\n" \
  "s_waitcnt lgkmcnt(3)\n" \
  "v_mfma_f32_16x16x32_bf16 v[112:115], v[64:67], v[108:111], v[112:115]\n" \
  "s_waitcnt lgkmcnt(2)\n" \
  "v_add_f32 v49, v49, v51\n" \
  "v_add_f32 v52, v52, v54\n" \
  "v_mfma_f32_16x16x32_bf16 v[112:115], v[68:71], v[100:103], v[112:115]\n" \
  "s_waitcnt lgkmcnt(1)\n" \
  "v_add_f32 v53, v53, v55\n" \
  "v_cvt_pk_bf16_f32 v52, v52, v53\n" \
  "v_mfma_f32_16x16x32_bf16 v[112:115], v[72:75], v[104:107], v[112:115]\n" \
  "ds_write_b32 v242, v52 offset:256\n" \
  "s_waitcnt lgkmcnt(1)\n" \
  "v_add_f32 v49, v49, v189\n" \
  "v_add_f32 v48, v48, v188\n" \
  "v_cvt_pk_bf16_f32 v48, v48, v49\n" \
  "ds_write_b32 v242, v48\n" \
  "s_nop 1\n" \
  "v_exp_f32 v97, v113\n" \
  "v_exp_f32 v98, v114\n" \
  "v_exp_f32 v96, v112\n" \
  "v_exp_f32 v99, v115\n" \
  "v_cvt_pk_bf16_f32 v188, v96, v97\n" \
  "v_cvt_pk_bf16_f32 v189, v98, v99\n" \
  "s_nop 1\n" \
  "v_mfma_f32_16x16x16_bf16 v[96:99], v[182:183], v[188:189], 0\n" \
  "buffer_store_dwordx2 v[188:189], v246, s[40:43], 0 offen sc1\n" \
  "buffer_store_dwordx2 v[188:189], v247, s[40:43], 0 offen sc1\n" \
  "s_waitcnt lgkmcnt(0)\n" \
  "s_barrier\n" \
  "s_nop 3\n" \
  "ds_write_b32 v248, v96\n" \
  "s_waitcnt lgkmcnt(0)\n" \
  "s_barrier\n" \
  "s_cbranch_scc1 .LX_xr_%=\n" \
  "s_memrealtime s[98:99]\n" \
  "s_memtime s[84:85]\n" \
  ".LX_loop_%=:\n" \
  "s_cmp_ge_u32 s48, 58\n" \
  "s_cbranch_scc1 .LX_xl_%=\n" \
  ".LX_xr_b_%=:\n" \
  "s_add_u32 s95, s51, 885760\n" \
  "s_mov_b64 exec, s[88:89]\n" \
  "buffer_load_dwordx4 v[96:99], off, s[40:43], s95 sc1\n" \
  "s_mov_b64 exec, -1\n" \
  "v_and_b32 v246, 3221225471, v246\n" \
  "v_and_b32 v247, 3221225471, v247\n" \
  "v_xor_b32 v189, s57, v246\n" \
  "buffer_store_dwordx2 v[32:33], v189, s[40:43], 0 offen sc1\n" \
  "v_xor_b32 v189, s57, v247\n" \
  "buffer_store_dwordx2 v[32:33], v189, s[40:43], 0 offen sc1\n" \
  "s_waitcnt lgkmcnt(0)\n" \
  "s_sub_u32 s60, s84, s66\n" \
  "s_cmp_lt_u32 s60, 4000\n" \
  "s_cselect_b32 s61, s68, 0\n" \
  "s_lshl_b32 s60, s60, 10\n" \
  "s_or_b32 s60, s60, s87\n" \
  "v_mov_b32 v188, s60\n" \
  "v_mov_b32 v189, s55\n" \
  "s_cmp_lg_u32 s61, 0\n" \
  "s_cselect_b32 s61, s75, 2130706432\n" \
  "s_mov_b64 exec, 1\n" \
  "buffer_store_dwordx2 v[188:189], off, s[40:43], s61 sc1\n" \
  "s_mov_b64 exec, -1\n" \
  "s_sub_u32 s76, s84, s90\n" \
  "s_sub_u32 s77, s98, s86\n" \
  "v_cvt_f32_u32 v188, s76\n" \
  "v_cvt_f32_u32 v189, s77\n" \
  "v_rcp_f32 v189, v189\n" \
  "v_mul_f32 v188, 16.0, v188\n" \
  "v_fma_f32 v188, v188, v189, 0.5\n" \
  "v_cvt_u32_f32 v188, v188\n" \
  "s_nop 0\n" \
  "v_readfirstlane_b32 s76, v188\n" \
  "s_max_u32 s76, s76, 160\n" \
  "s_min_u32 s76, s76, 400\n" \
  "s_add_u32 s77, s48, 0\n" \
  "s_and_b32 s77, s77, 3\n" \
  "s_cselect_b32 s87, s87, s76\n" \
  "s_cselect_b32 s86, s86, s98\n" \
  "s_cselect_b32 s90, s90, s84\n" \
  "s_add_u32 s48, s48, 1\n" \
  "ds_read_b128 v[60:63], v179\n" \
  "ds_read_b128 v[64:67], v179 offset:2048\n" \
  "ds_read_b128 v[68:71], v179 offset:4096\n" \
  "ds_read_b128 v[72:75], v179 offset:6144\n" \
  "v_xor_b32 v179, 8192, v179\n" \
  "v_mov_b64 v[40:41], 0\n" \
  "v_mov_b64 v[42:43], 0\n" \
  "v_mov_b64 v[44:45], 0\n" \
  "v_mov_b64 v[46:47], 0\n" \
  "v_mov_b64 v[48:49], 0\n" \
  "v_mov_b64 v[50:51], 0\n" \
  "v_mov_b64 v[52:53], 0\n" \
  "v_mov_b64 v[54:55], 0\n" \
  "s_waitcnt vmcnt(3)\n" \
  "v_mov_b32 v189, s48\n" \
  "ds_write_b32 v184, v189\n" \
  "s_branch .LX_rot_%=\n" \
  "s_nop 7\n" \
  ".LX_slow_%=:\n" \
  "s_bitcmp1_b32 s70, 0\n" \
  "s_cbranch_scc1 .LX_back_%=\n" \
  "s_waitcnt lgkmcnt(0)\n" \
  "s_cmp_lt_u32 s70, 10\n" \
  "s_nop 3\n" \
  "s_cbranch_scc1 .LX_nol_%=\n" \
  "v_max_u32 v189, v96, v97\n" \
  "v_max3_u32 v189, v189, v98, v99\n" \
  "v_max3_u32 v189, v189, v100, v101\n" \
  "v_max3_u32 v189, v189, v102, v103\n" \
  "v_max3_u32 v189, v189, v104, v105\n" \
  "v_max3_u32 v189, v189, v106, v107\n" \
  "v_max3_u32 v189, v189, v108, v109\n" \
  "v_max3_u32 v189, v189, v110, v111\n" \
  "v_max3_u32 v189, v189, v112, v113\n" \
  "v_max3_u32 v189, v189, v114, v115\n" \
  "v_max3_u32 v189, v189, v124, v125\n" \
  "v_max3_u32 v189, v189, v126, v127\n" \
  "v_max3_u32 v189, v189, v116, v117\n" \
  "v_max3_u32 v189, v189, v118, v119\n" \
  "v_max3_u32 v189, v189, v120, v121\n" \
  "v_max3_u32 v189, v189, v122, v123\n" \
  "v_max3_u32 v189, v189, v128, v129\n" \
  "v_max3_u32 v189, v189, v130, v131\n" \
  "v_cmp_eq_u32 vcc, -1, v189\n" \
  "s_cbranch_vccz .LX_back_%=\n" \
  ".LX_nol_%=:\n" \
  "s_add_u32 s70, s70, 2\n" \
  "s_cmp_gt_u32 s70, 65536\n" \
  "s_cbranch_scc1 .LX_dset_%=\n" \
  "s_barrier\n" \
  "v_mov_b64 v[40:41], 0\n" \
  "v_mov_b64 v[42:43], 0\n" \
  "v_mov_b64 v[44:45], 0\n" \
  "v_mov_b64 v[46:47], 0\n" \
  "v_mov_b64 v[48:49], 0\n" \
  "v_mov_b64 v[50:51], 0\n" \
  "v_mov_b64 v[52:53], 0\n" \
  "v_mov_b64 v[54:55], 0\n" \
  "s_cmp_lg_u32 %28, 0\n" \
  "s_cbranch_scc1 .LX_g3_%=\n" \
  "s_and_b32 s62, %43, 0xffff\n" \
  "s_add_u32 s44, s44, s62\n" \
  "s_lshr_b32 s62, s70, 1\n" \
  "s_add_u32 s76, %34, s62\n" \
  "s_add_u32 s77, s76, 3\n" \
  "s_and_b32 s76, s76, 3\n" \
  "s_and_b32 s77, s77, 3\n" \
  "s_lshl_b32 s76, s76, 2\n" \
  "s_lshl_b32 s77, s77, 2\n" \
  "s_lshr_b32 s76, s96, s76\n" \
  "s_lshr_b32 s77, s96, s77\n" \
  "s_xor_b32 s76, s76, s77\n" \
  "s_and_b32 s76, s76, 7\n" \
  "s_lshl_b32 s76, s76, 12\n" \
  "s_xor_b32 s59, s59, 0\n" \
  "s_mov_b32 s68, 0\n" \
  "s_mov_b32 m0, 0\n" \
  "s_branch .LX_rt_%=\n" \
  ".LX_dset_%=:\n" \
  "v_mov_b32 v124, 1\n" \
  "v_mov_b32 v125, s55\n" \
  "buffer_store_dwordx2 v[124:125], off, s[40:43], %40 offset:128 sc1\n" \
  "s_mov_b32 s70, 1\n" \
  "v_mov_b64 v[40:41], 0\n" \
  "v_mov_b64 v[42:43], 0\n" \
  "v_mov_b64 v[44:45], 0\n" \
  "v_mov_b64 v[46:47], 0\n" \
  "v_mov_b64 v[48:49], 0\n" \
  "v_mov_b64 v[50:51], 0\n" \
  "v_mov_b64 v[52:53], 0\n" \
  "v_mov_b64 v[54:55], 0\n" \
  "s_waitcnt vmcnt(0) lgkmcnt(0)\n" \
  "s_barrier\n" \
  "s_branch .LX_p_%=\n" \
  ".LX_xr_%=:\n" \
  "s_waitcnt lgkmcnt(0)\n" \
  "s_nop 7\n" \
  "ds_read_b64_tr_b16 v[132:133], v244\n" \
  "ds_read_b64_tr_b16 v[134:135], v244 offset:256\n" \
  "ds_read_b64_tr_b16 v[136:137], v244 offset:512\n" \
  "ds_read_b64_tr_b16 v[138:139], v244 offset:768\n" \
  "ds_read_b64_tr_b16 v[140:141], v244 offset:1024\n" \
  "ds_read_b64_tr_b16 v[142:143], v244 offset:1280\n" \
  "ds_read_b64_tr_b16 v[144:145], v244 offset:1536\n" \
  "ds_read_b64_tr_b16 v[146:147], v244 offset:1792\n" \
  "s_waitcnt lgkmcnt(6)\n" \
  "v_mfma_f32_16x16x32_bf16 v[148:151], v[132:135], v[80:83], 0\n" \
  "s_waitcnt lgkmcnt(4)\n" \
  "v_mfma_f32_16x16x32_bf16 v[148:151], v[136:139], v[84:87], v[148:151]\n" \
  "s_waitcnt lgkmcnt(2)\n" \
  "v_mfma_f32_16x16x32_bf16 v[148:151], v[140:143], v[88:91], v[148:151]\n" \
  "s_waitcnt lgkmcnt(0)\n" \
  "v_mfma_f32_16x16x32_bf16 v[148:151], v[144:147], v[92:95], v[148:151]\n" \
  "s_sub_u32 s60, s48, 60\n" \
  "s_lshl_b32 s60, s60, 16\n" \
  "s_add_u32 s60, s60, s97\n" \
  "s_cmp_eq_u32 s48, 0\n" \
  "s_cselect_b32 s60, 2130706432, s60\n" \
  "s_nop 2\n" \
  "v_max_f32 v158, v148, v149\n" \
  "v_max3_f32 v158, v150, v151, v158\n" \
  "v_mov_b32 v159, v158\n" \
  "s_nop 1\n" \
  "v_permlane32_swap_b32 v158, v159\n" \
  "v_max_f32 v158, v158, v159\n" \
  "v_mul_f32 v152, s65, v158\n" \
  "v_min_f32 v152, v152, v180\n" \
  "v_fma_f32 v148, v148, s64, v152\n" \
  "v_fma_f32 v149, v149, s64, v152\n" \
  "v_fma_f32 v150, v150, s64, v152\n" \
  "v_fma_f32 v151, v151, s64, v152\n" \
  "v_sub_f32 v160, v152, v180\n" \
  "v_exp_f32 v148, v148\n" \
  "v_exp_f32 v149, v149\n" \
  "v_exp_f32 v150, v150\n" \
  "v_exp_f32 v151, v151\n" \
  "v_exp_f32 v160, v160\n" \
  "v_add_f32 v153, v148, v149\n" \
  "v_add_f32 v158, v150, v151\n" \
  "v_add_f32 v153, v153, v158\n" \
  "v_mov_b32 v159, v153\n" \
  "s_nop 1\n" \
  "v_permlane32_swap_b32 v153, v159\n" \
  "v_add_f32 v153, v153, v159\n" \
  "v_fmac_f32 v153, v160, v181\n" \
  "v_rcp_f32 v154, v153\n" \
  "s_nop 0\n" \
  "v_mul_f32 v148, v148, v154\n" \
  "v_mul_f32 v149, v149, v154\n" \
  "v_mul_f32 v150, v150, v154\n" \
  "v_mul_f32 v151, v151, v154\n" \
  "v_mul_f32 v160, v160, v154\n" \
  "v_cvt_pk_bf16_f32 v156, v148, v149\n" \
  "v_cvt_pk_bf16_f32 v157, v150, v151\n" \
  "v_cndmask_b32 v156, v156, v160, s[92:93]\n" \
  "buffer_store_dwordx2 v[156:157], v251, s[40:43], s60 offen sc1\n" \
  "s_cmp_eq_u32 s48, 63\n" \
  "s_cbranch_scc1 .LX_end_%=\n" \
  ".LX_xq_%=:\n" \
  "s_add_u32 s61, s48, 1\n" \
  "s_lshl_b32 s60, s61, 19\n" \
  "s_add_u32 s60, s60, s47\n" \
  "s_lshr_b32 s62, s60, 8\n" \
  "s_and_b32 s62, s62, 8192\n" \
  "s_xor_b32 s62, s62, s60\n" \
  "s_cmp_eq_u32 s48, 0\n" \
  "s_cselect_b32 s62, 2130706432, s62\n" \
  "s_add_u32 s63, s62, 4096\n" \
  "buffer_load_dwordx4 v[80:83], v249, s[40:43], s62 offen sc1\n" \
  "buffer_load_dwordx4 v[84:87], v249, s[40:43], s62 offen offset:2048 sc1\n" \
  "buffer_load_dwordx4 v[88:91], v249, s[40:43], s63 offen sc1\n" \
  "buffer_load_dwordx4 v[92:95], v249, s[40:43], s63 offen offset:2048 sc1\n" \
  "s_sub_u32 s60, s61, 60\n" \
  "s_lshl_b32 s60, s60, 16\n" \
  "s_add_u32 s60, s60, s97\n" \
  "s_cmp_eq_u32 s48, 0\n" \
  "s_cselect_b32 s60, 2130706432, s60\n" \
  "buffer_load_dwordx2 v[180:181], v250, s[40:43], s60 offen sc1\n" \
  "s_cbranch_scc1 .LX_dry_b_%=\n" \
  "s_branch .LX_xr_b_%=\n" \
  ".LX_xl_%=:\n" \
  "s_cmp_eq_u32 s48, 59\n" \
  "s_cbranch_scc1 .LX_xc_%=\n" \
  "s_cmp_lg_u32 s48, 58\n" \
  "s_cbranch_scc1 .LX_xr_%=\n" \
  "buffer_load_dwordx2 v[180:181], off, s[40:43], %46 sc1\n" \
  "s_branch .LX_xr_b_%=\n" \
  ".LX_xc_%=:\n" \
  "v_readfirstlane_b32 s63, v181\n" \
  "s_cmp_eq_u32 s63, s55\n" \
  "s_cbranch_scc1 .LX_xq_%=\n" \
  "s_mov_b32 s69, 0\n" \
  ".LX_xs_%=:\n" \
  "buffer_load_dwordx2 v[180:181], off, s[40:43], %46 sc1\n" \
  "s_waitcnt vmcnt(0)\n" \
  "v_readfirstlane_b32 s62, v180\n" \
  "v_readfirstlane_b32 s63, v181\n" \
  "s_cmp_eq_u32 s63, s55\n" \
  "s_cselect_b32 s62, s62, 0\n" \
  "s_cmp_lg_u32 s62, 0\n" \
  "s_cbranch_scc1 .LX_xs_x_%=\n" \
  "s_sleep 2\n" \
  "s_add_u32 s69, s69, 1\n" \
  "s_and_b32 s63, s69, 1023\n" \
  "s_cbranch_scc1 .LX_xs_%=\n" \
  "s_cmp_gt_u32 s69, 2097152\n" \
  "s_cbranch_scc1 .LX_xs_set_%=\n" \
  "buffer_load_dwordx2 v[180:181], off, s[40:43], %40 offset:128 sc1\n" \
  "s_waitcnt vmcnt(0)\n" \
  "v_readfirstlane_b32 s63, v181\n" \
  "s_cmp_eq_u32 s63, s55\n" \
  "s_cbranch_scc0 .LX_xs_%=\n" \
  "s_branch .LX_xs_x_%=\n" \
  ".LX_xs_set_%=:\n" \
  "v_mov_b32 v180, 1\n" \
  "v_mov_b32 v181, s55\n" \
  "buffer_store_dwordx2 v[180:181], off, s[40:43], %40 offset:128 sc1\n" \
  "s_waitcnt vmcnt(0)\n" \
  ".LX_xs_x_%=:\n" \
  "s_branch .LX_xq_%=\n" \
  ".LX_tw_re_%=:\n" \
  "s_add_u32 s69, s69, 1\n" \
  "s_cmp_gt_u32 s69, 64\n" \
  "s_cbranch_scc1 .LX_noclk_%=\n" \
  "buffer_load_dwordx4 v[96:99], off, s[40:43], s95 sc1\n" \
  "s_waitcnt vmcnt(0)\n" \
  "s_branch .LX_tw_%=\n" \
  ".LX_lat_%=:\n" \
  "s_lshr_b32 s63, %43, 16\n" \
  "s_add_u32 s62, s62, s63\n" \
  "s_cmp_lt_u32 s62, 4294937296\n" \
  "s_cbranch_scc1 .LX_noclk_%=\n" \
  "s_and_b32 s63, %43, 0xffff\n" \
  ".LX_lat1_%=:\n" \
  "s_add_u32 s44, s44, s63\n" \
  "s_add_u32 s62, s62, s63\n" \
  "s_cbranch_scc0 .LX_lat1_%=\n" \
  "s_branch .LX_rt_%=\n" \
  ".LX_clk2_%=:\n" \
  "s_cmp_le_i32 s62, 3\n" \
  "s_cbranch_scc1 .LX_clk_done_%=\n" \
  "s_memrealtime s[60:61]\n" \
  "s_waitcnt lgkmcnt(0)\n" \
  "s_sub_u32 s62, s73, s60\n" \
  "s_branch .LX_clk2_%=\n" \
  ".LX_clk_done_%=:\n" \
  "s_branch .LX_noclk_%=\n" \
  ".LX_end_%=:\n" \
  "s_waitcnt vmcnt(0)\n" \
  "s_waitcnt vmcnt(0)\n" \
  "s_nop 7\n" \
  "s_endpgm\n" \
  ".LX_other_%=:\n"
#define X_ASM_VOPS "+{v239}"(ci.sidx), "+{v240}"(ci.l16), "+{v241}"(ci.qw), "+{v242}"(ci.kw), "+{v243}"(ci.fq), "+{v244}"(ci.fk), "+{v245}"(ci.kxo), "+{v246}"(ci.pst), "+{v247}"(ci.ps2), "+{v248}"(ci.lx), "+{v249}"(ci.qxo), "+{v250}"(ci.sto), "+{v251}"(ci.plx), "+{v252}"(ci.mk), "+{v253}"(ci.ck0), "+{v254}"(ci.ck2), "+{v255}"(ci.hx), "+{v200}"(ci.vq0), "+{v201}"(ci.vq1), "+{v202}"(ci.vq2), "+{v203}"(ci.vq3), "+{v204}"(ci.tro0), "+{v205}"(ci.tro1), "+{v206}"(ci.m0o)
#define X_ASM_SOPS "s"(ci.ws), "s"(ci.Wa), "s"(ci.Wb), "s"(ci.mem0), "s"(ci.w), "s"(ci.o_a), "s"(ci.o_b), "s"(ci.o_nx), "s"(ci.o_exp), "s"(ci.o_u), "s"(ci.lab), "s"(ci.f_p), "s"(ci.f_m), "s"(ci.f_u), "s"(ci.f_dur), "s"(ci.f_go), "s"(ci.o_ctl), "s"(ci.o_dbg), "s"(ci.push), "s"(ci.dret), "s"(ci.u5), "s"(ci.o_qx), "s"(ci.f_s), "s"(ci.f_r)
#define X_ASM_CLOB "v0", "v1", "v2", "v3", "v4", "v5", "v6", "v7", "v8", "v9", "v10", "v11", "v12", "v13", "v14", "v15", "v16", "v17", "v18", "v19", "v20", "v21", "v22", "v23", "v24", "v25", "v26", "v27", "v28", "v29", "v30", "v31", "v32", "v33", "v34", "v35", "v36", "v37", "v38", "v39", "v40", "v41", "v42", "v43", "v44", "v45", "v46", "v47", "v48", "v49", "v50", "v51", "v52", "v53", "v54", "v55", "v56", "v57", "v58", "v59", "v60", "v61", "v62", "v63", "v64", "v65", "v66", "v67", "v68", "v69", "v70", "v71", "v72", "v73", "v74", "v75", "v76", "v77", "v78", "v79", "v80", "v81", "v82", "v83", "v84", "v85", "v86", "v87", "v88", "v89", "v90", "v91", "v92", "v93", "v94", "v95", "v96", "v97", "v98", "v99", "v100", "v101", "v102", "v103", "v104", "v105", "v106", "v107", "v108", "v109", "v110", "v111", "v112", "v113", "v114", "v115", "v116", "v117", "v118", "v119", "v120", "v121", "v122", "v123", "v124", "v125", "v126", "v127", "v128", "v129", "v130", "v131", "v132", "v133", "v134", "v135", "v136", "v137", "v138", "v139", "v140", "v141", "v142", "v143", "v144", "v145", "v146", "v147", "v148", "v149", "v150", "v151", "v152", "v153", "v154", "v155", "v156", "v157", "v158", "v159", "v160", "v161", "v162", "v163", "v164", "v165", "v166", "v167", "v168", "v169", "v170", "v171", "v172", "v173", "v174", "v175", "v176", "v177", "v178", "v179", "v180", "v181", "v182", "v183", "v184", "v185", "v186", "v187", "v188", "v189", "v190", "v191", "v192", "v193", "v194", "v195", "v196", "v197", "v198", "v199", "a0", "a1", "a2", "a3", "a4", "a5", "a6", "a7", "a8", "a9", "a10", "a11", "a12", "a13", "a14", "a15", "a16", "a17", "a18", "a19", "a20", "a21", "a22", "a23", "a24", "a25", "a26", "a27", "a28", "a29", "a30", "a31", "a32", "a33", "a34", "a35", "a36", "a37", "a38", "a39", "a40", "a41", "a42", "a43", "a44", "a45", "a46", "a47", "a48", "a49", "a50", "a51", "a52", "a53", "a54", "a55", "a56", "a57", "a58", "a59", "a60", "a61", "a62", "a63", "a64", "a65", "a66", "a67", "a68", "a69", "a70", "a71", "a72", "a73", "a74", "a75", "a76", "a77", "a78", "a79", "a80", "a81", "a82", "a83", "a84", "a85", "a86", "a87", "a88", "a89", "a90", "a91", "a92", "a93", "a94", "a95", "a96", "a97", "a98", "a99", "a100", "a101", "a102", "a103", "a104", "a105", "a106", "a107", "a108", "a109", "a110", "a111", "a112", "a113", "a114", "a115", "a116", "a117", "a118", "a119", "a120", "a121", "a122", "a123", "a124", "a125", "a126", "a127", "a128", "a129", "a130", "a131", "a132", "a133", "a134", "a135", "a136", "a137", "a138", "a139", "a140", "a141", "a142", "a143", "a144", "a145", "a146", "a147", "a148", "a149", "a150", "a151", "a152", "a153", "a154", "a155", "a156", "a157", "a158", "a159", "a160", "a161", "a162", "a163", "a164", "a165", "a166", "a167", "a168", "a169", "a170", "a171", "a172", "a173", "a174", "a175", "a176", "a177", "a178", "a179", "a180", "a181", "a182", "a183", "a184", "a185", "a186", "a187", "a188", "a189", "a190", "a191", "a192", "a193", "a194", "a195", "a196", "a197", "a198", "a199", "a200", "a201", "a202", "a203", "a204", "a205", "a206", "a207", "a208", "a209", "a210", "a211", "a212", "a213", "a214", "a215", "a216", "a217", "a218", "a219", "a220", "a221", "a222", "a223", "a224", "a225", "a226", "a227", "a228", "a229", "a230", "a231", "a232", "a233", "a234", "a235", "a236", "a237", "a238", "a239", "a240", "a241", "a242", "a243", "a244", "a245", "a246", "a247", "a248", "a249", "a250", "a251", "a252", "a253", "a254", "a255", "s40", "s41", "s42", "s43", "s44", "s45", "s46", "s47", "s48", "s49", "s50", "s51", "s52", "s53", "s54", "s55", "s56", "s57", "s58", "s59", "s60", "s61", "s62", "s63", "s64", "s65", "s66", "s67", "s68", "s69", "s70", "s71", "s72", "s73", "s74", "s75", "s76", "s77", "s78", "s79", "s80", "s81", "s82", "s83", "s84", "s85", "s86", "s87", "s88", "s89", "s90", "s91", "s92", "s93", "s94", "s95", "s96", "s97", "s98", "s99", "vcc", "scc", "memory"
#define M_ASM_TEXT \
  "s_mov_b64 s[40:41], %24\n" \
  "s_and_b32 s41, s41, 0xffff\n" \
  "s_mov_b32 s42, 255852544\n" \
  "s_mov_b32 s43, 0x00020000\n" \
  "s_mov_b32 s46, 4718592\n" \
  "s_mov_b32 s47, 0x00020000\n" \
  "s_lshl_b32 s72, %28, 11\n" \
  "buffer_load_dword v196, off, s[40:43], %40 sc1\n" \
  "s_mov_b64 s[44:45], %25\n" \
  "s_and_b32 s45, s45, 0xffff\n" \
  "s_add_u32 s60, %29, 0\n" \
  "s_lshl_b32 s61, s72, 1\n" \
  "s_add_u32 m0, s61, 16384\n" \
  "s_nop 0\n" \
  "buffer_load_dwordx4 v200, s[44:47], s60 offen lds\n" \
  "buffer_load_dwordx4 v201, s[44:47], s60 offen offset:1024 lds\n" \
  "buffer_load_dwordx4 v202, s[44:47], s60 offen offset:2048 lds\n" \
  "buffer_load_dwordx4 v203, s[44:47], s60 offen offset:3072 lds\n" \
  "s_add_u32 s60, %29, 524288\n" \
  "s_lshl_b32 s61, s72, 1\n" \
  "s_add_u32 m0, s61, 32768\n" \
  "s_nop 0\n" \
  "buffer_load_dwordx4 v200, s[44:47], s60 offen lds\n" \
  "buffer_load_dwordx4 v201, s[44:47], s60 offen offset:1024 lds\n" \
  "buffer_load_dwordx4 v202, s[44:47], s60 offen offset:2048 lds\n" \
  "buffer_load_dwordx4 v203, s[44:47], s60 offen offset:3072 lds\n" \
  "s_add_u32 s60, %29, 1048576\n" \
  "s_lshl_b32 s61, s72, 1\n" \
  "s_add_u32 m0, s61, 49152\n" \
  "s_nop 0\n" \
  "buffer_load_dwordx4 v200, s[44:47], s60 offen lds\n" \
  "buffer_load_dwordx4 v201, s[44:47], s60 offen offset:1024 lds\n" \
  "buffer_load_dwordx4 v202, s[44:47], s60 offen offset:2048 lds\n" \
  "buffer_load_dwordx4 v203, s[44:47], s60 offen offset:3072 lds\n" \
  "s_add_u32 s60, %29, 1572864\n" \
  "s_lshl_b32 s61, s72, 1\n" \
  "s_add_u32 m0, s61, 65536\n" \
  "s_nop 0\n" \
  "buffer_load_dwordx4 v200, s[44:47], s60 offen lds\n" \
  "buffer_load_dwordx4 v201, s[44:47], s60 offen offset:1024 lds\n" \
  "buffer_load_dwordx4 v202, s[44:47], s60 offen offset:2048 lds\n" \
  "buffer_load_dwordx4 v203, s[44:47], s60 offen offset:3072 lds\n" \
  "s_add_u32 s60, %29, 2097152\n" \
  "s_lshl_b32 s61, s72, 1\n" \
  "s_add_u32 m0, s61, 81920\n" \
  "s_nop 0\n" \
  "buffer_load_dwordx4 v200, s[44:47], s60 offen lds\n" \
  "buffer_load_dwordx4 v201, s[44:47], s60 offen offset:1024 lds\n" \
  "buffer_load_dwordx4 v202, s[44:47], s60 offen offset:2048 lds\n" \
  "buffer_load_dwordx4 v203, s[44:47], s60 offen offset:3072 lds\n" \
  "s_add_u32 s60, %29, 2621440\n" \
  "s_lshl_b32 s61, s72, 1\n" \
  "s_add_u32 m0, s61, 98304\n" \
  "s_nop 0\n" \
  "buffer_load_dwordx4 v200, s[44:47], s60 offen lds\n" \
  "buffer_load_dwordx4 v201, s[44:47], s60 offen offset:1024 lds\n" \
  "buffer_load_dwordx4 v202, s[44:47], s60 offen offset:2048 lds\n" \
  "buffer_load_dwordx4 v203, s[44:47], s60 offen offset:3072 lds\n" \
  "s_add_u32 s60, %29, 3145728\n" \
  "s_lshl_b32 s61, s72, 1\n" \
  "s_add_u32 m0, s61, 114688\n" \
  "s_nop 0\n" \
  "buffer_load_dwordx4 v200, s[44:47], s60 offen lds\n" \
  "buffer_load_dwordx4 v201, s[44:47], s60 offen offset:1024 lds\n" \
  "buffer_load_dwordx4 v202, s[44:47], s60 offen offset:2048 lds\n" \
  "buffer_load_dwordx4 v203, s[44:47], s60 offen offset:3072 lds\n" \
  "s_add_u32 s60, %29, 3670016\n" \
  "s_lshl_b32 s61, s72, 1\n" \
  "s_add_u32 m0, s61, 131072\n" \
  "s_nop 0\n" \
  "buffer_load_dwordx4 v200, s[44:47], s60 offen lds\n" \
  "buffer_load_dwordx4 v201, s[44:47], s60 offen offset:1024 lds\n" \
  "buffer_load_dwordx4 v202, s[44:47], s60 offen offset:2048 lds\n" \
  "buffer_load_dwordx4 v203, s[44:47], s60 offen offset:3072 lds\n" \
  "s_add_u32 s60, %29, 4194304\n" \
  "s_lshl_b32 s61, s72, 1\n" \
  "s_add_u32 m0, s61, 147456\n" \
  "s_nop 0\n" \
  "buffer_load_dwordx4 v200, s[44:47], s60 offen lds\n" \
  "buffer_load_dwordx4 v201, s[44:47], s60 offen offset:1024 lds\n" \
  "buffer_load_dwordx4 v202, s[44:47], s60 offen offset:2048 lds\n" \
  "buffer_load_dwordx4 v203, s[44:47], s60 offen offset:3072 lds\n" \
  "s_mov_b64 s[44:45], %27\n" \
  "s_and_b32 s45, s45, 0xffff\n" \
  "s_lshr_b32 s60, s72, 3\n" \
  "s_mov_b32 m0, s72\n" \
  "s_nop 0\n" \
  "buffer_load_dwordx4 v206, s[44:47], s60 offen lds\n" \
  "s_add_u32 s60, s60, 128\n" \
  "s_add_u32 m0, s72, 1024\n" \
  "s_nop 0\n" \
  "buffer_load_dwordx4 v206, s[44:47], s60 offen lds\n" \
  "s_movk_i32 s60, 1024\n" \
  "s_movk_i32 m0, 8192\n" \
  "s_nop 0\n" \
  "buffer_load_dwordx4 v206, s[44:47], s60 offen lds\n" \
  "s_waitcnt vmcnt(27)\n" \
  "s_barrier\n" \
  "v_add_u32 v194, 16384, v204\n" \
  "ds_read_b64_tr_b16 a[0:1], v194\n" \
  "ds_read_b64_tr_b16 a[2:3], v194 offset:512\n" \
  "ds_read_b64_tr_b16 a[4:5], v194 offset:8192\n" \
  "ds_read_b64_tr_b16 a[6:7], v194 offset:8704\n" \
  "v_add_u32 v195, 16384, v205\n" \
  "ds_read_b64_tr_b16 a[72:73], v195\n" \
  "ds_read_b64_tr_b16 a[74:75], v195 offset:512\n" \
  "ds_read_b64_tr_b16 a[76:77], v195 offset:8192\n" \
  "ds_read_b64_tr_b16 a[78:79], v195 offset:8704\n" \
  "v_add_u32 v194, 32768, v204\n" \
  "ds_read_b64_tr_b16 a[8:9], v194\n" \
  "ds_read_b64_tr_b16 a[10:11], v194 offset:512\n" \
  "ds_read_b64_tr_b16 a[12:13], v194 offset:8192\n" \
  "ds_read_b64_tr_b16 a[14:15], v194 offset:8704\n" \
  "v_add_u32 v195, 32768, v205\n" \
  "ds_read_b64_tr_b16 a[80:81], v195\n" \
  "ds_read_b64_tr_b16 a[82:83], v195 offset:512\n" \
  "ds_read_b64_tr_b16 a[84:85], v195 offset:8192\n" \
  "ds_read_b64_tr_b16 a[86:87], v195 offset:8704\n" \
  "v_add_u32 v194, 49152, v204\n" \
  "ds_read_b64_tr_b16 a[16:17], v194\n" \
  "ds_read_b64_tr_b16 a[18:19], v194 offset:512\n" \
  "ds_read_b64_tr_b16 a[20:21], v194 offset:8192\n" \
  "ds_read_b64_tr_b16 a[22:23], v194 offset:8704\n" \
  "v_add_u32 v195, 49152, v205\n" \
  "ds_read_b64_tr_b16 a[88:89], v195\n" \
  "ds_read_b64_tr_b16 a[90:91], v195 offset:512\n" \
  "ds_read_b64_tr_b16 a[92:93], v195 offset:8192\n" \
  "ds_read_b64_tr_b16 a[94:95], v195 offset:8704\n" \
  "s_waitcnt lgkmcnt(0)\n" \
  "s_barrier\n" \
  "s_mov_b64 s[44:45], %26\n" \
  "s_and_b32 s45, s45, 0xffff\n" \
  "s_add_u32 s60, %29, 0\n" \
  "s_lshl_b32 s61, s72, 1\n" \
  "s_add_u32 m0, s61, 16384\n" \
  "s_nop 0\n" \
  "buffer_load_dwordx4 v200, s[44:47], s60 offen lds\n" \
  "buffer_load_dwordx4 v201, s[44:47], s60 offen offset:1024 lds\n" \
  "buffer_load_dwordx4 v202, s[44:47], s60 offen offset:2048 lds\n" \
  "buffer_load_dwordx4 v203, s[44:47], s60 offen offset:3072 lds\n" \
  "s_add_u32 s60, %29, 524288\n" \
  "s_lshl_b32 s61, s72, 1\n" \
  "s_add_u32 m0, s61, 32768\n" \
  "s_nop 0\n" \
  "buffer_load_dwordx4 v200, s[44:47], s60 offen lds\n" \
  "buffer_load_dwordx4 v201, s[44:47], s60 offen offset:1024 lds\n" \
  "buffer_load_dwordx4 v202, s[44:47], s60 offen offset:2048 lds\n" \
  "buffer_load_dwordx4 v203, s[44:47], s60 offen offset:3072 lds\n" \
  "s_add_u32 s60, %29, 1048576\n" \
  "s_lshl_b32 s61, s72, 1\n" \
  "s_add_u32 m0, s61, 49152\n" \
  "s_nop 0\n" \
  "buffer_load_dwordx4 v200, s[44:47], s60 offen lds\n" \
  "buffer_load_dwordx4 v201, s[44:47], s60 offen offset:1024 lds\n" \
  "buffer_load_dwordx4 v202, s[44:47], s60 offen offset:2048 lds\n" \
  "buffer_load_dwordx4 v203, s[44:47], s60 offen offset:3072 lds\n" \
  "v_readfirstlane_b32 s55, v196\n" \
  "s_add_u32 s55, s55, 1\n" \
  "s_waitcnt vmcnt(27)\n" \
  "s_barrier\n" \
  "v_add_u32 v194, 65536, v204\n" \
  "ds_read_b64_tr_b16 a[24:25], v194\n" \
  "ds_read_b64_tr_b16 a[26:27], v194 offset:512\n" \
  "ds_read_b64_tr_b16 a[28:29], v194 offset:8192\n" \
  "ds_read_b64_tr_b16 a[30:31], v194 offset:8704\n" \
  "v_add_u32 v195, 65536, v205\n" \
  "ds_read_b64_tr_b16 a[96:97], v195\n" \
  "ds_read_b64_tr_b16 a[98:99], v195 offset:512\n" \
  "ds_read_b64_tr_b16 a[100:101], v195 offset:8192\n" \
  "ds_read_b64_tr_b16 a[102:103], v195 offset:8704\n" \
  "v_add_u32 v194, 81920, v204\n" \
  "ds_read_b64_tr_b16 a[32:33], v194\n" \
  "ds_read_b64_tr_b16 a[34:35], v194 offset:512\n" \
  "ds_read_b64_tr_b16 a[36:37], v194 offset:8192\n" \
  "ds_read_b64_tr_b16 a[38:39], v194 offset:8704\n" \
  "v_add_u32 v195, 81920, v205\n" \
  "ds_read_b64_tr_b16 a[104:105], v195\n" \
  "ds_read_b64_tr_b16 a[106:107], v195 offset:512\n" \
  "ds_read_b64_tr_b16 a[108:109], v195 offset:8192\n" \
  "ds_read_b64_tr_b16 a[110:111], v195 offset:8704\n" \
  "v_add_u32 v194, 98304, v204\n" \
  "ds_read_b64_tr_b16 a[40:41], v194\n" \
  "ds_read_b64_tr_b16 a[42:43], v194 offset:512\n" \
  "ds_read_b64_tr_b16 a[44:45], v194 offset:8192\n" \
  "ds_read_b64_tr_b16 a[46:47], v194 offset:8704\n" \
  "v_add_u32 v195, 98304, v205\n" \
  "ds_read_b64_tr_b16 a[112:113], v195\n" \
  "ds_read_b64_tr_b16 a[114:115], v195 offset:512\n" \
  "ds_read_b64_tr_b16 a[116:117], v195 offset:8192\n" \
  "ds_read_b64_tr_b16 a[118:119], v195 offset:8704\n" \
  "s_waitcnt lgkmcnt(0)\n" \
  "s_barrier\n" \
  "s_add_u32 s60, %29, 1572864\n" \
  "s_lshl_b32 s61, s72, 1\n" \
  "s_add_u32 m0, s61, 65536\n" \
  "s_nop 0\n" \
  "buffer_load_dwordx4 v200, s[44:47], s60 offen lds\n" \
  "buffer_load_dwordx4 v201, s[44:47], s60 offen offset:1024 lds\n" \
  "buffer_load_dwordx4 v202, s[44:47], s60 offen offset:2048 lds\n" \
  "buffer_load_dwordx4 v203, s[44:47], s60 offen offset:3072 lds\n" \
  "s_add_u32 s60, %29, 2097152\n" \
  "s_lshl_b32 s61, s72, 1\n" \
  "s_add_u32 m0, s61, 81920\n" \
  "s_nop 0\n" \
  "buffer_load_dwordx4 v200, s[44:47], s60 offen lds\n" \
  "buffer_load_dwordx4 v201, s[44:47], s60 offen offset:1024 lds\n" \
  "buffer_load_dwordx4 v202, s[44:47], s60 offen offset:2048 lds\n" \
  "buffer_load_dwordx4 v203, s[44:47], s60 offen offset:3072 lds\n" \
  "s_add_u32 s60, %29, 2621440\n" \
  "s_lshl_b32 s61, s72, 1\n" \
  "s_add_u32 m0, s61, 98304\n" \
  "s_nop 0\n" \
  "buffer_load_dwordx4 v200, s[44:47], s60 offen lds\n" \
  "buffer_load_dwordx4 v201, s[44:47], s60 offen offset:1024 lds\n" \
  "buffer_load_dwordx4 v202, s[44:47], s60 offen offset:2048 lds\n" \
  "buffer_load_dwordx4 v203, s[44:47], s60 offen offset:3072 lds\n" \
  "s_waitcnt vmcnt(27)\n" \
  "s_barrier\n" \
  "v_add_u32 v194, 114688, v204\n" \
  "ds_read_b64_tr_b16 a[48:49], v194\n" \
  "ds_read_b64_tr_b16 a[50:51], v194 offset:512\n" \
  "ds_read_b64_tr_b16 a[52:53], v194 offset:8192\n" \
  "ds_read_b64_tr_b16 a[54:55], v194 offset:8704\n" \
  "v_add_u32 v195, 114688, v205\n" \
  "ds_read_b64_tr_b16 a[120:121], v195\n" \
  "ds_read_b64_tr_b16 a[122:123], v195 offset:512\n" \
  "ds_read_b64_tr_b16 a[124:125], v195 offset:8192\n" \
  "ds_read_b64_tr_b16 a[126:127], v195 offset:8704\n" \
  "v_add_u32 v194, 131072, v204\n" \
  "ds_read_b64_tr_b16 a[56:57], v194\n" \
  "ds_read_b64_tr_b16 a[58:59], v194 offset:512\n" \
  "ds_read_b64_tr_b16 a[60:61], v194 offset:8192\n" \
  "ds_read_b64_tr_b16 a[62:63], v194 offset:8704\n" \
  "v_add_u32 v195, 131072, v205\n" \
  "ds_read_b64_tr_b16 a[128:129], v195\n" \
  "ds_read_b64_tr_b16 a[130:131], v195 offset:512\n" \
  "ds_read_b64_tr_b16 a[132:133], v195 offset:8192\n" \
  "ds_read_b64_tr_b16 a[134:135], v195 offset:8704\n" \
  "v_add_u32 v194, 147456, v204\n" \
  "ds_read_b64_tr_b16 a[64:65], v194\n" \
  "ds_read_b64_tr_b16 a[66:67], v194 offset:512\n" \
  "ds_read_b64_tr_b16 a[68:69], v194 offset:8192\n" \
  "ds_read_b64_tr_b16 a[70:71], v194 offset:8704\n" \
  "v_add_u32 v195, 147456, v205\n" \
  "ds_read_b64_tr_b16 a[136:137], v195\n" \
  "ds_read_b64_tr_b16 a[138:139], v195 offset:512\n" \
  "ds_read_b64_tr_b16 a[140:141], v195 offset:8192\n" \
  "ds_read_b64_tr_b16 a[142:143], v195 offset:8704\n" \
  "s_waitcnt lgkmcnt(0)\n" \
  "s_barrier\n" \
  "s_add_u32 s60, %29, 3145728\n" \
  "s_lshl_b32 s61, s72, 1\n" \
  "s_add_u32 m0, s61, 114688\n" \
  "s_nop 0\n" \
  "buffer_load_dwordx4 v200, s[44:47], s60 offen lds\n" \
  "buffer_load_dwordx4 v201, s[44:47], s60 offen offset:1024 lds\n" \
  "buffer_load_dwordx4 v202, s[44:47], s60 offen offset:2048 lds\n" \
  "buffer_load_dwordx4 v203, s[44:47], s60 offen offset:3072 lds\n" \
  "s_add_u32 s60, %29, 3670016\n" \
  "s_lshl_b32 s61, s72, 1\n" \
  "s_add_u32 m0, s61, 131072\n" \
  "s_nop 0\n" \
  "buffer_load_dwordx4 v200, s[44:47], s60 offen lds\n" \
  "buffer_load_dwordx4 v201, s[44:47], s60 offen offset:1024 lds\n" \
  "buffer_load_dwordx4 v202, s[44:47], s60 offen offset:2048 lds\n" \
  "buffer_load_dwordx4 v203, s[44:47], s60 offen offset:3072 lds\n" \
  "s_add_u32 s60, %29, 4194304\n" \
  "s_lshl_b32 s61, s72, 1\n" \
  "s_add_u32 m0, s61, 147456\n" \
  "s_nop 0\n" \
  "buffer_load_dwordx4 v200, s[44:47], s60 offen lds\n" \
  "buffer_load_dwordx4 v201, s[44:47], s60 offen offset:1024 lds\n" \
  "buffer_load_dwordx4 v202, s[44:47], s60 offen offset:2048 lds\n" \
  "buffer_load_dwordx4 v203, s[44:47], s60 offen offset:3072 lds\n" \
  "buffer_load_dwordx2 v[198:199], off, s[40:43], %39 sc1\n" \
  "s_waitcnt vmcnt(25)\n" \
  "s_barrier\n" \
  "v_add_u32 v194, 16384, v204\n" \
  "ds_read_b64_tr_b16 a[144:145], v194\n" \
  "ds_read_b64_tr_b16 a[146:147], v194 offset:512\n" \
  "ds_read_b64_tr_b16 a[148:149], v194 offset:8192\n" \
  "ds_read_b64_tr_b16 a[150:151], v194 offset:8704\n" \
  "v_add_u32 v195, 16384, v205\n" \
  "ds_read_b64_tr_b16 a[216:217], v195\n" \
  "ds_read_b64_tr_b16 a[218:219], v195 offset:512\n" \
  "ds_read_b64_tr_b16 a[220:221], v195 offset:8192\n" \
  "ds_read_b64_tr_b16 a[222:223], v195 offset:8704\n" \
  "v_xor_b32 v195, 128, v194\n" \
  "ds_read_b64_tr_b16 v[32:33], v195\n" \
  "ds_read_b64_tr_b16 v[34:35], v195 offset:512\n" \
  "ds_read_b64_tr_b16 v[36:37], v195 offset:8192\n" \
  "ds_read_b64_tr_b16 v[38:39], v195 offset:8704\n" \
  "v_add_u32 v194, 32768, v204\n" \
  "ds_read_b64_tr_b16 a[152:153], v194\n" \
  "ds_read_b64_tr_b16 a[154:155], v194 offset:512\n" \
  "ds_read_b64_tr_b16 a[156:157], v194 offset:8192\n" \
  "ds_read_b64_tr_b16 a[158:159], v194 offset:8704\n" \
  "v_add_u32 v195, 32768, v205\n" \
  "ds_read_b64_tr_b16 a[224:225], v195\n" \
  "ds_read_b64_tr_b16 a[226:227], v195 offset:512\n" \
  "ds_read_b64_tr_b16 a[228:229], v195 offset:8192\n" \
  "ds_read_b64_tr_b16 a[230:231], v195 offset:8704\n" \
  "v_xor_b32 v195, 128, v194\n" \
  "ds_read_b64_tr_b16 v[40:41], v195\n" \
  "ds_read_b64_tr_b16 v[42:43], v195 offset:512\n" \
  "ds_read_b64_tr_b16 v[44:45], v195 offset:8192\n" \
  "ds_read_b64_tr_b16 v[46:47], v195 offset:8704\n" \
  "v_add_u32 v194, 49152, v204\n" \
  "ds_read_b64_tr_b16 a[160:161], v194\n" \
  "ds_read_b64_tr_b16 a[162:163], v194 offset:512\n" \
  "ds_read_b64_tr_b16 a[164:165], v194 offset:8192\n" \
  "ds_read_b64_tr_b16 a[166:167], v194 offset:8704\n" \
  "v_add_u32 v195, 49152, v205\n" \
  "ds_read_b64_tr_b16 a[232:233], v195\n" \
  "ds_read_b64_tr_b16 a[234:235], v195 offset:512\n" \
  "ds_read_b64_tr_b16 a[236:237], v195 offset:8192\n" \
  "ds_read_b64_tr_b16 a[238:239], v195 offset:8704\n" \
  "v_xor_b32 v195, 128, v194\n" \
  "ds_read_b64_tr_b16 v[48:49], v195\n" \
  "ds_read_b64_tr_b16 v[50:51], v195 offset:512\n" \
  "ds_read_b64_tr_b16 v[52:53], v195 offset:8192\n" \
  "ds_read_b64_tr_b16 v[54:55], v195 offset:8704\n" \
  "s_waitcnt lgkmcnt(0)\n" \
  "s_waitcnt vmcnt(13)\n" \
  "s_barrier\n" \
  "v_add_u32 v194, 65536, v204\n" \
  "ds_read_b64_tr_b16 a[168:169], v194\n" \
  "ds_read_b64_tr_b16 a[170:171], v194 offset:512\n" \
  "ds_read_b64_tr_b16 a[172:173], v194 offset:8192\n" \
  "ds_read_b64_tr_b16 a[174:175], v194 offset:8704\n" \
  "v_add_u32 v195, 65536, v205\n" \
  "ds_read_b64_tr_b16 a[240:241], v195\n" \
  "ds_read_b64_tr_b16 a[242:243], v195 offset:512\n" \
  "ds_read_b64_tr_b16 a[244:245], v195 offset:8192\n" \
  "ds_read_b64_tr_b16 a[246:247], v195 offset:8704\n" \
  "v_xor_b32 v195, 128, v194\n" \
  "ds_read_b64_tr_b16 v[56:57], v195\n" \
  "ds_read_b64_tr_b16 v[58:59], v195 offset:512\n" \
  "ds_read_b64_tr_b16 v[60:61], v195 offset:8192\n" \
  "ds_read_b64_tr_b16 v[62:63], v195 offset:8704\n" \
  "v_add_u32 v194, 81920, v204\n" \
  "ds_read_b64_tr_b16 a[176:177], v194\n" \
  "ds_read_b64_tr_b16 a[178:179], v194 offset:512\n" \
  "ds_read_b64_tr_b16 a[180:181], v194 offset:8192\n" \
  "ds_read_b64_tr_b16 a[182:183], v194 offset:8704\n" \
  "v_add_u32 v195, 81920, v205\n" \
  "ds_read_b64_tr_b16 a[248:249], v195\n" \
  "ds_read_b64_tr_b16 a[250:251], v195 offset:512\n" \
  "ds_read_b64_tr_b16 a[252:253], v195 offset:8192\n" \
  "ds_read_b64_tr_b16 a[254:255], v195 offset:8704\n" \
  "v_add_u32 v194, 98304, v204\n" \
  "ds_read_b64_tr_b16 a[184:185], v194\n" \
  "ds_read_b64_tr_b16 a[186:187], v194 offset:512\n" \
  "ds_read_b64_tr_b16 a[188:189], v194 offset:8192\n" \
  "ds_read_b64_tr_b16 a[190:191], v194 offset:8704\n" \
  "v_add_u32 v195, 98304, v205\n" \
  "ds_read_b64_tr_b16 v[0:1], v195\n" \
  "ds_read_b64_tr_b16 v[2:3], v195 offset:512\n" \
  "ds_read_b64_tr_b16 v[4:5], v195 offset:8192\n" \
  "ds_read_b64_tr_b16 v[6:7], v195 offset:8704\n" \
  "s_waitcnt lgkmcnt(0)\n" \
  "s_waitcnt vmcnt(1)\n" \
  "s_barrier\n" \
  "v_add_u32 v194, 114688, v204\n" \
  "ds_read_b64_tr_b16 a[192:193], v194\n" \
  "ds_read_b64_tr_b16 a[194:195], v194 offset:512\n" \
  "ds_read_b64_tr_b16 a[196:197], v194 offset:8192\n" \
  "ds_read_b64_tr_b16 a[198:199], v194 offset:8704\n" \
  "v_add_u32 v195, 114688, v205\n" \
  "ds_read_b64_tr_b16 v[8:9], v195\n" \
  "ds_read_b64_tr_b16 v[10:11], v195 offset:512\n" \
  "ds_read_b64_tr_b16 v[12:13], v195 offset:8192\n" \
  "ds_read_b64_tr_b16 v[14:15], v195 offset:8704\n" \
  "v_add_u32 v194, 131072, v204\n" \
  "ds_read_b64_tr_b16 a[200:201], v194\n" \
  "ds_read_b64_tr_b16 a[202:203], v194 offset:512\n" \
  "ds_read_b64_tr_b16 a[204:205], v194 offset:8192\n" \
  "ds_read_b64_tr_b16 a[206:207], v194 offset:8704\n" \
  "v_add_u32 v195, 131072, v205\n" \
  "ds_read_b64_tr_b16 v[16:17], v195\n" \
  "ds_read_b64_tr_b16 v[18:19], v195 offset:512\n" \
  "ds_read_b64_tr_b16 v[20:21], v195 offset:8192\n" \
  "ds_read_b64_tr_b16 v[22:23], v195 offset:8704\n" \
  "v_add_u32 v194, 147456, v204\n" \
  "ds_read_b64_tr_b16 a[208:209], v194\n" \
  "ds_read_b64_tr_b16 a[210:211], v194 offset:512\n" \
  "ds_read_b64_tr_b16 a[212:213], v194 offset:8192\n" \
  "ds_read_b64_tr_b16 a[214:215], v194 offset:8704\n" \
  "v_add_u32 v195, 147456, v205\n" \
  "ds_read_b64_tr_b16 v[24:25], v195\n" \
  "ds_read_b64_tr_b16 v[26:27], v195 offset:512\n" \
  "ds_read_b64_tr_b16 v[28:29], v195 offset:8192\n" \
  "ds_read_b64_tr_b16 v[30:31], v195 offset:8704\n" \
  "s_waitcnt lgkmcnt(0)\n" \
  "s_waitcnt vmcnt(0)\n" \
  "v_readfirstlane_b32 s96, v198\n" \
  "v_readfirstlane_b32 s63, v199\n" \
  "s_cmp_eq_u32 s63, s55\n" \
  "s_cselect_b32 s96, s96, 0\n" \
  "s_cmp_ge_u32 s96, 0xc0000000\n" \
  "s_cbranch_scc1 .LM_go_x_%=\n" \
  "s_mov_b32 s69, 0\n" \
  ".LM_go_%=:\n" \
  "buffer_load_dwordx2 v[196:197], off, s[40:43], %39 sc1\n" \
  "s_waitcnt vmcnt(0)\n" \
  "v_readfirstlane_b32 s96, v196\n" \
  "v_readfirstlane_b32 s63, v197\n" \
  "s_cmp_eq_u32 s63, s55\n" \
  "s_cselect_b32 s96, s96, 0\n" \
  "s_cmp_ge_u32 s96, 0xc0000000\n" \
  "s_cbranch_scc1 .LM_go_x_%=\n" \
  "s_sleep 8\n" \
  "s_add_u32 s69, s69, 1\n" \
  "s_and_b32 s63, s69, 1023\n" \
  "s_cbranch_scc1 .LM_go_%=\n" \
  "s_cmp_gt_u32 s69, 2097152\n" \
  "s_cbranch_scc1 .LM_go_set_%=\n" \
  "buffer_load_dwordx2 v[196:197], off, s[40:43], %40 offset:128 sc1\n" \
  "s_waitcnt vmcnt(0)\n" \
  "v_readfirstlane_b32 s63, v197\n" \
  "s_cmp_eq_u32 s63, s55\n" \
  "s_cbranch_scc0 .LM_go_%=\n" \
  "s_branch .LM_go_x_%=\n" \
  ".LM_go_set_%=:\n" \
  "v_mov_b32 v196, 1\n" \
  "v_mov_b32 v197, s55\n" \
  "buffer_store_dwordx2 v[196:197], off, s[40:43], %40 offset:128 sc1\n" \
  "s_waitcnt vmcnt(0)\n" \
  ".LM_go_x_%=:\n" \
  "s_lshr_b32 s60, s96, 0\n" \
  "s_and_b32 s60, s60, 7\n" \
  "s_lshl_b32 s60, s60, 12\n" \
  "s_xor_b32 s74, %35, s60\n" \
  "s_xor_b32 s75, %38, s60\n" \
  "s_bfe_u32 s56, s96, 0x30010\n" \
  "s_lshl_b32 s56, s56, 12\n" \
  "s_or_b32 s56, s56, 0x200000\n" \
  "s_mov_b32 s57, 1052672\n" \
  "s_lshl_b32 s61, %34, 2\n" \
  "s_lshr_b32 s51, s96, s61\n" \
  "s_and_b32 s51, s51, 7\n" \
  "s_lshl_b32 s51, s51, 12\n" \
  "s_xor_b32 s51, s51, 4210688\n" \
  "s_mov_b32 s71, 0\n" \
  "v_mov_b64 v[64:65], -1\n" \
  "v_mov_b64 v[66:67], -1\n" \
  "v_and_b32 v194, 3, v246\n" \
  "v_lshlrev_b32 v194, 2, v194\n" \
  "v_lshrrev_b32 v194, v194, s96\n" \
  "v_and_b32 v194, 7, v194\n" \
  "v_lshlrev_b32 v194, 12, v194\n" \
  "v_and_b32 v246, -4, v246\n" \
  "v_xor_b32 v246, v246, v194\n" \
  "v_and_b32 v194, 3, v247\n" \
  "v_lshlrev_b32 v194, 2, v194\n" \
  "v_lshrrev_b32 v194, v194, s96\n" \
  "v_and_b32 v194, 7, v194\n" \
  "v_lshlrev_b32 v194, 12, v194\n" \
  "v_and_b32 v247, -4, v247\n" \
  "v_xor_b32 v247, v247, v194\n" \
  "v_xor_b32 v190, s56, v246\n" \
  "buffer_store_dwordx2 v[64:65], v190, s[40:43], 0 offen sc1\n" \
  "s_xor_b32 s62, s56, s57\n" \
  "s_nop 1\n" \
  "v_xor_b32 v190, s62, v246\n" \
  "buffer_store_dwordx2 v[64:65], v190, s[40:43], 0 offen sc1\n" \
  "s_andn2_b32 s60, 2, s96\n" \
  "s_lshl_b32 s60, s60, 12\n" \
  "s_xor_b32 s60, s60, 8192\n" \
  "s_xor_b32 s58, %31, s60\n" \
  "s_mov_b32 s49, 0\n" \
  "s_add_u32 s47, %45, s60\n" \
  "s_add_u32 s97, s60, 8388608\n" \
  "s_xor_b32 s50, %32, s60\n" \
  "s_mov_b32 s48, 0\n" \
  "s_mov_b32 s54, 0\n" \
  "s_cmp_eq_u32 %28, 0\n" \
  "s_cselect_b64 s[88:89], -1, 0\n" \
  "s_mov_b32 s68, 0\n" \
  "s_mov_b32 s70, 1\n" \
  "s_mov_b32 s73, 0\n" \
  "s_mov_b32 s44, 0\n" \
  "s_mov_b32 s53, 0\n" \
  "s_memrealtime s[86:87]\n" \
  "s_memtime s[76:77]\n" \
  "s_waitcnt lgkmcnt(0)\n" \
  "s_mov_b32 s90, s76\n" \
  "s_mov_b32 s87, 340\n" \
  "s_mov_b32 s64, 0x3e0293ee\n" \
  "s_mov_b32 s65, 0xbe0293ee\n" \
  "v_mov_b64 v[72:73], 0\n" \
  "v_mov_b64 v[74:75], 0\n" \
  "v_mov_b64 v[76:77], 0\n" \
  "v_mov_b64 v[78:79], 0\n" \
  "v_mov_b64 v[80:81], 0\n" \
  "v_mov_b64 v[82:83], 0\n" \
  "v_mov_b64 v[84:85], 0\n" \
  "v_mov_b64 v[86:87], 0\n" \
  "v_mov_b64 v[88:89], 0\n" \
  "v_mov_b64 v[90:91], 0\n" \
  "v_mov_b32 v68, 0\n" \
  "v_mov_b32 v69, s55\n" \
  "v_mov_b64 v[70:71], v[68:69]\n" \
  "s_mov_b64 s[92:93], 0\n" \
  "s_mov_b32 s92, 0xffff0000\n" \
  "s_mov_b32 s60, 8192\n" \
  "s_add_u32 s60, s60, 32768\n" \
  "v_add_u32 v177, s60, v240\n" \
  "v_mov_b32 v180, v252\n" \
  "v_mov_b64 v[92:93], 0\n" \
  "v_mov_b64 v[94:95], 0\n" \
  "v_mov_b32 v92, v253\n" \
  "v_mov_b32 v94, v254\n" \
  "s_mov_b32 s52, 0\n" \
  "s_barrier\n" \
  "s_branch .LM_xr_%=\n" \
  ".LM_dry_b_%=:\n" \
  "s_and_b32 s60, s72, 2048\n" \
  "s_lshl_b32 s91, s60, 1\n" \
  "s_add_u32 s91, s91, 40960\n" \
  "s_lshr_b32 s60, s60, 1\n" \
  "v_add_u32 v184, s60, v244\n" \
  "v_mov_b64 v[182:183], 0\n" \
  "s_mov_b32 s69, 0\n" \
  ".LM_k0_%=:\n" \
  "buffer_load_dwordx2 v[196:197], off, s[40:43], s74 sc1\n" \
  "s_waitcnt vmcnt(0)\n" \
  "v_readfirstlane_b32 s54, v196\n" \
  "v_readfirstlane_b32 s63, v197\n" \
  "s_cmp_eq_u32 s63, s55\n" \
  "s_cselect_b32 s54, s54, 0\n" \
  "s_cmp_gt_i32 s54, 1\n" \
  "s_cbranch_scc1 .LM_k0_x_%=\n" \
  "s_sleep 2\n" \
  "s_add_u32 s69, s69, 1\n" \
  "s_and_b32 s63, s69, 1023\n" \
  "s_cbranch_scc1 .LM_k0_%=\n" \
  "s_cmp_gt_u32 s69, 2097152\n" \
  "s_cbranch_scc1 .LM_k0_set_%=\n" \
  "buffer_load_dwordx2 v[196:197], off, s[40:43], %40 offset:128 sc1\n" \
  "s_waitcnt vmcnt(0)\n" \
  "v_readfirstlane_b32 s63, v197\n" \
  "s_cmp_eq_u32 s63, s55\n" \
  "s_cbranch_scc0 .LM_k0_%=\n" \
  "s_branch .LM_k0_x_%=\n" \
  ".LM_k0_set_%=:\n" \
  "v_mov_b32 v196, 1\n" \
  "v_mov_b32 v197, s55\n" \
  "buffer_store_dwordx2 v[196:197], off, s[40:43], %40 offset:128 sc1\n" \
  "s_waitcnt vmcnt(0)\n" \
  ".LM_k0_x_%=:\n" \
  "s_xor_b32 s60, s49, s58\n" \
  "s_add_u32 s49, s49, 16384\n" \
  "s_xor_b32 s91, s91, 8192\n" \
  "s_mov_b32 m0, s91\n" \
  "s_nop 0\n" \
  "buffer_load_dwordx4 v245, s[40:43], s60 offen sc1 lds\n" \
  "buffer_load_dwordx4 v245, s[40:43], s60 offen offset:1024 sc1 lds\n" \
  "buffer_load_dwordx4 v245, s[40:43], s60 offen offset:2048 sc1 lds\n" \
  "buffer_load_dwordx4 v245, s[40:43], s60 offen offset:3072 sc1 lds\n" \
  "s_xor_b32 s60, s49, s58\n" \
  "s_add_u32 s49, s49, 16384\n" \
  "s_xor_b32 s91, s91, 8192\n" \
  "s_mov_b32 m0, s91\n" \
  "s_nop 0\n" \
  "buffer_load_dwordx4 v245, s[40:43], s60 offen sc1 lds\n" \
  "buffer_load_dwordx4 v245, s[40:43], s60 offen offset:1024 sc1 lds\n" \
  "buffer_load_dwordx4 v245, s[40:43], s60 offen offset:2048 sc1 lds\n" \
  "buffer_load_dwordx4 v245, s[40:43], s60 offen offset:3072 sc1 lds\n" \
  "s_waitcnt vmcnt(0)\n" \
  "s_barrier\n" \
  "v_xor_b32 v177, 8192, v177\n" \
  "s_mov_b32 s67, 2130706432\n" \
  "s_waitcnt vmcnt(0) lgkmcnt(0)\n" \
  "s_barrier\n" \
  "s_branch .LM_p_%=\n" \
  "s_nop 7\n" \
  ".p2align 6\n" \
  ".fill (((52 - (.LM_p_%= - .LM_rot_%=)) & 63) >> 2), 4, 0xBF800000\n" \
  ".LM_rot_%=:\n" \
  "s_xor_b32 s51, s51, s56\n" \
  "s_xor_b32 s71, s71, s56\n" \
  "v_xor_b32 v246, s56, v246\n" \
  "v_xor_b32 v247, s56, v247\n" \
  "s_mov_b32 s60, s56\n" \
  "s_mov_b32 s56, s57\n" \
  "s_mov_b32 s57, s60\n" \
  "s_add_u32 s59, s51, 819200\n" \
  "s_mov_b32 m0, 0\n" \
  "s_mov_b32 s70, 0\n" \
  "s_waitcnt lgkmcnt(0)\n" \
  "s_waitcnt vmcnt(0)\n" \
  ".LM_rt_%=:\n" \
  "s_barrier\n" \
  ".LM_p_%=:\n" \
  "ds_read_b128 v[112:115], v240\n" \
  "ds_read_b128 v[116:119], v240 offset:1024\n" \
  "buffer_store_dwordx2 v[68:69], off, s[40:43], %36 sc1\n" \
  "ds_read_b128 v[120:123], v240 offset:2048\n" \
  "s_waitcnt lgkmcnt(2)\n" \
  "v_smfmac_f32_16x16x64_bf16 v[76:79], v[112:115], a[72:79], v239\n" \
  "s_waitcnt lgkmcnt(1)\n" \
  "v_smfmac_f32_16x16x64_bf16 v[76:79], v[116:119], a[80:87], v239\n" \
  "s_barrier\n" \
  "ds_read_b128 v[124:127], v240 offset:3072\n" \
  "v_smfmac_f32_16x16x64_bf16 v[72:75], v[112:115], a[0:7], v239\n" \
  "ds_read_b128 v[128:131], v240 offset:4096\n" \
  "s_waitcnt lgkmcnt(2)\n" \
  "v_smfmac_f32_16x16x64_bf16 v[76:79], v[120:123], a[88:95], v239\n" \
  "ds_read_b128 v[132:135], v240 offset:5120\n" \
  "v_smfmac_f32_16x16x64_bf16 v[72:75], v[116:119], a[8:15], v239\n" \
  "v_smfmac_f32_16x16x64_bf16 v[72:75], v[120:123], a[16:23], v239\n" \
  "s_waitcnt lgkmcnt(2)\n" \
  "v_smfmac_f32_16x16x64_bf16 v[72:75], v[124:127], a[24:31], v239\n" \
  "ds_read_b128 v[136:139], v240 offset:7168\n" \
  "ds_read_b128 v[140:143], v240 offset:6144\n" \
  "s_waitcnt lgkmcnt(3)\n" \
  "v_smfmac_f32_16x16x64_bf16 v[72:75], v[128:131], a[32:39], v239\n" \
  "v_smfmac_f32_16x16x64_bf16 v[76:79], v[124:127], a[96:103], v239\n" \
  "ds_read_b128 v[144:147], v240 offset:8192\n" \
  "s_waitcnt lgkmcnt(3)\n" \
  "v_smfmac_f32_16x16x64_bf16 v[72:75], v[132:135], a[40:47], v239\n" \
  "v_smfmac_f32_16x16x64_bf16 v[76:79], v[128:131], a[104:111], v239\n" \
  "v_smfmac_f32_16x16x64_bf16 v[76:79], v[132:135], a[112:119], v239\n" \
  "s_waitcnt lgkmcnt(1)\n" \
  "v_smfmac_f32_16x16x64_bf16 v[72:75], v[140:143], a[48:55], v239\n" \
  "v_smfmac_f32_16x16x64_bf16 v[76:79], v[140:143], a[120:127], v239\n" \
  "v_smfmac_f32_16x16x64_bf16 v[72:75], v[136:139], a[56:63], v239\n" \
  "s_waitcnt lgkmcnt(0)\n" \
  "v_smfmac_f32_16x16x64_bf16 v[72:75], v[144:147], a[64:71], v239\n" \
  "s_nop 2\n" \
  "v_smfmac_f32_16x16x64_bf16 v[76:79], v[136:139], a[128:135], v239\n" \
  "s_nop 2\n" \
  "v_smfmac_f32_16x16x64_bf16 v[76:79], v[144:147], a[136:143], v239\n" \
  "v_add_f32 v72, v72, v74\n" \
  "v_add_f32 v73, v73, v75\n" \
  "v_cmp_u_f32 vcc, v72, v73\n" \
  "v_mul_f32 v72, s64, v72\n" \
  "v_mul_f32 v73, s64, v73\n" \
  "v_cvt_pk_bf16_f32 v72, v72, v73\n" \
  "v_smfmac_f32_16x16x64_bf16 v[88:91], v[112:115], v[32:39], v239\n" \
  "s_cbranch_vccnz .LM_slow_%=\n" \
  ".LM_back_%=:\n" \
  "s_cmp_eq_u32 s48, 63\n" \
  "v_add_f32 v77, v77, v79\n" \
  "v_add_f32 v76, v76, v78\n" \
  "v_mul_f32 v76, s64, v76\n" \
  "v_mul_f32 v77, s64, v77\n" \
  "v_cvt_pk_bf16_f32 v76, v76, v77\n" \
  "v_smfmac_f32_16x16x64_bf16 v[88:91], v[116:119], v[40:47], v239\n" \
  "ds_write_b32 v241, v76 offset:256\n" \
  "v_smfmac_f32_16x16x64_bf16 v[88:91], v[120:123], v[48:55], v239\n" \
  "ds_write_b32 v241, v72\n" \
  "v_smfmac_f32_16x16x64_bf16 v[80:83], v[112:115], a[144:151], v239\n" \
  "v_smfmac_f32_16x16x64_bf16 v[88:91], v[124:127], v[56:63], v239\n" \
  "s_nop 2\n" \
  "v_smfmac_f32_16x16x64_bf16 v[80:83], v[116:119], a[152:159], v239\n" \
  "s_nop 0\n" \
  "v_smfmac_f32_16x16x64_bf16 v[80:83], v[120:123], a[160:167], v239\n" \
  "s_waitcnt lgkmcnt(0)\n" \
  "s_barrier\n" \
  "v_add_f32 v88, v88, v90\n" \
  "v_smfmac_f32_16x16x64_bf16 v[84:87], v[112:115], a[216:223], v239\n" \
  "ds_read_b64_tr_b16 v[114:115], v243 offset:1792\n" \
  "v_add_f32 v89, v89, v91\n" \
  "v_smfmac_f32_16x16x64_bf16 v[80:83], v[124:127], a[168:175], v239\n" \
  "ds_write_b64 v255, v[88:89]\n" \
  "ds_read_b64_tr_b16 v[148:149], v243 offset:256\n" \
  "v_smfmac_f32_16x16x64_bf16 v[80:83], v[128:131], a[176:183], v239\n" \
  "v_smfmac_f32_16x16x64_bf16 v[80:83], v[132:135], a[184:191], v239\n" \
  "ds_read_b64_tr_b16 v[112:113], v243 offset:1280\n" \
  "v_smfmac_f32_16x16x64_bf16 v[80:83], v[140:143], a[192:199], v239\n" \
  "v_smfmac_f32_16x16x64_bf16 v[80:83], v[136:139], a[200:207], v239\n" \
  "ds_read_b64_tr_b16 v[152:153], v243\n" \
  "s_waitcnt lgkmcnt(3)\n" \
  "s_barrier\n" \
  "v_smfmac_f32_16x16x64_bf16 v[80:83], v[144:147], a[208:215], v239\n" \
  "s_nop 2\n" \
  "v_smfmac_f32_16x16x64_bf16 v[84:87], v[116:119], a[224:231], v239\n" \
  "s_nop 0\n" \
  "v_smfmac_f32_16x16x64_bf16 v[84:87], v[120:123], a[232:239], v239\n" \
  "ds_read_b64_tr_b16 v[116:117], v243 offset:1024\n" \
  "v_smfmac_f32_16x16x64_bf16 v[84:87], v[124:127], a[240:247], v239\n" \
  "v_add_f32 v81, v81, v83\n" \
  "v_smfmac_f32_16x16x64_bf16 v[84:87], v[128:131], a[248:255], v239\n" \
  "v_add_f32 v80, v80, v82\n" \
  "v_smfmac_f32_16x16x64_bf16 v[84:87], v[132:135], v[0:7], v239\n" \
  "v_cvt_pk_bf16_f32 v80, v80, v81\n" \
  "v_smfmac_f32_16x16x64_bf16 v[84:87], v[140:143], v[8:15], v239\n" \
  "ds_write_b32 v242, v80\n" \
  "v_smfmac_f32_16x16x64_bf16 v[84:87], v[136:139], v[16:23], v239\n" \
  "ds_read_b64_tr_b16 v[118:119], v243 offset:1536\n" \
  "v_smfmac_f32_16x16x64_bf16 v[84:87], v[144:147], v[24:31], v239\n" \
  "s_nop 7\n" \
  "v_add_f32 v84, v84, v86\n" \
  "v_add_f32 v85, v85, v87\n" \
  "v_cvt_pk_bf16_f32 v84, v84, v85\n" \
  "s_waitcnt lgkmcnt(1)\n" \
  "s_barrier\n" \
  "ds_write_b32 v242, v84 offset:256\n" \
  "ds_read_b64_tr_b16 v[120:121], v244\n" \
  "ds_read_b64_tr_b16 v[150:151], v243 offset:768\n" \
  "ds_read_b64_tr_b16 v[154:155], v243 offset:512\n" \
  "ds_read_b64_tr_b16 v[126:127], v244 offset:768\n" \
  "ds_read_b64_tr_b16 v[122:123], v244 offset:512\n" \
  "ds_read_b64_tr_b16 v[128:129], v244 offset:1024\n" \
  "ds_read_b64_tr_b16 v[130:131], v244 offset:1536\n" \
  "ds_read_b64_tr_b16 v[124:125], v244 offset:256\n" \
  "s_waitcnt lgkmcnt(8)\n" \
  "s_barrier\n" \
  "s_cbranch_scc1 .LM_xr_%=\n" \
  "ds_read_b64_tr_b16 v[134:135], v244 offset:1792\n" \
  "ds_read_b64 v[188:189], v248\n" \
  "ds_read_b64_tr_b16 v[132:133], v244 offset:1280\n" \
  "s_waitcnt lgkmcnt(6)\n" \
  "v_mfma_f32_16x16x32_bf16 v[136:139], v[120:123], v[152:155], 0\n" \
  "s_waitcnt lgkmcnt(4)\n" \
  "v_mfma_f32_16x16x32_bf16 v[136:139], v[128:131], v[116:119], v[136:139]\n" \
  "s_waitcnt lgkmcnt(3)\n" \
  "v_mfma_f32_16x16x32_bf16 v[136:139], v[124:127], v[148:151], v[136:139]\n" \
  "s_waitcnt lgkmcnt(0)\n" \
  "v_mfma_f32_16x16x32_bf16 v[136:139], v[132:135], v[112:115], v[136:139]\n" \
  "s_nop 7\n" \
  "v_exp_f32 v112, v136\n" \
  "v_exp_f32 v114, v138\n" \
  "v_exp_f32 v115, v139\n" \
  "v_exp_f32 v113, v137\n" \
  "s_nop 0\n" \
  "v_pk_add_f32 v[186:187], v[112:113], v[114:115]\n" \
  "v_pk_fma_f32 v[114:115], v[114:115], v[180:181], v[94:95] op_sel_hi:[1,0,1]\n" \
  "v_pk_add_f32 v[174:175], v[186:187], v[186:187] op_sel:[0,1] op_sel_hi:[1,0]\n" \
  "v_add_f32 v188, v188, v189\n" \
  "s_nop 0\n" \
  "v_permlane32_swap_b32 v174, v175\n" \
  "v_add_f32 v174, v174, v175\n" \
  "v_add_f32 v174, v174, v188\n" \
  "v_rcp_f32 v188, v174\n" \
  "v_pk_fma_f32 v[112:113], v[112:113], v[180:181], v[92:93] op_sel_hi:[1,0,1]\n" \
  "v_add_u32 v187, 3766484992, v174\n" \
  "v_pk_mul_f32 v[112:113], v[112:113], v[188:189] op_sel_hi:[1,0]\n" \
  "v_cmp_le_u32 vcc, s52, v187\n" \
  "v_pk_mul_f32 v[114:115], v[114:115], v[188:189] op_sel_hi:[1,0]\n" \
  "v_cvt_pk_bf16_f32 v188, v112, v113\n" \
  "v_cvt_pk_bf16_f32 v189, v114, v115\n" \
  "s_cbranch_vccnz .LM_ex_%=\n" \
  ".LM_ex_b_%=:\n" \
  "buffer_store_dwordx2 v[188:189], v246, s[40:43], 0 offen sc1\n" \
  "s_memrealtime s[98:99]\n" \
  "s_memtime s[84:85]\n" \
  ".LM_loop_%=:\n" \
  "s_cmp_ge_u32 s48, 58\n" \
  "s_cbranch_scc1 .LM_xl_%=\n" \
  ".LM_xr_b_%=:\n" \
  "ds_read_b64_tr_b16 v[112:113], v184\n" \
  "ds_read_b64_tr_b16 v[114:115], v184 offset:256\n" \
  "ds_read_b64_tr_b16 v[116:117], v184 offset:512\n" \
  "ds_read_b64_tr_b16 v[118:119], v184 offset:768\n" \
  "v_readfirstlane_b32 s63, v183\n" \
  "v_readfirstlane_b32 s62, v182\n" \
  "s_cmp_eq_u32 s63, s55\n" \
  "s_cselect_b32 s54, s62, s54\n" \
  "s_add_u32 s48, s48, 1\n" \
  "s_add_u32 s62, s48, 1\n" \
  "s_min_u32 s62, s62, 63\n" \
  "s_cmp_le_i32 s54, s62\n" \
  "s_cbranch_scc1 .LM_pk_go_%=\n" \
  ".LM_pk_ok_%=:\n" \
  "s_xor_b32 s60, s49, s58\n" \
  "s_add_u32 s49, s49, 16384\n" \
  "s_xor_b32 s91, s91, 8192\n" \
  "s_mov_b32 m0, s91\n" \
  "s_nop 0\n" \
  "buffer_load_dwordx4 v245, s[40:43], s60 offen sc1 lds\n" \
  "buffer_load_dwordx4 v245, s[40:43], s60 offen offset:1024 sc1 lds\n" \
  "buffer_load_dwordx4 v245, s[40:43], s60 offen offset:2048 sc1 lds\n" \
  "buffer_load_dwordx4 v245, s[40:43], s60 offen offset:3072 sc1 lds\n" \
  ".LM_nopf_%=:\n" \
  "buffer_load_dwordx2 v[182:183], off, s[40:43], s74 sc1\n" \
  "v_xor_b32 v177, 8192, v177\n" \
  "v_mov_b64 v[72:73], 0\n" \
  "v_mov_b64 v[74:75], 0\n" \
  "v_mov_b64 v[76:77], 0\n" \
  "v_mov_b64 v[78:79], 0\n" \
  "v_mov_b64 v[80:81], 0\n" \
  "v_mov_b64 v[82:83], 0\n" \
  "v_mov_b64 v[84:85], 0\n" \
  "v_mov_b64 v[86:87], 0\n" \
  "v_mov_b64 v[88:89], 0\n" \
  "v_mov_b64 v[90:91], 0\n" \
  "s_cmp_gt_u32 s48, 59\n" \
  "s_cbranch_scc1 .LM_nsl_%=\n" \
  "s_sleep 4\n" \
  ".LM_nsl_%=:\n" \
  "v_xor_b32 v189, s57, v246\n" \
  "buffer_store_dwordx2 v[64:65], v189, s[40:43], 0 offen sc1\n" \
  "s_sub_u32 s60, s48, 2\n" \
  "s_cselect_b32 s60, 8192, s60\n" \
  "s_lshl_b32 s60, s60, 14\n" \
  "s_xor_b32 s60, s60, s50\n" \
  "s_waitcnt lgkmcnt(2)\n" \
  "buffer_store_dwordx4 v[112:115], v240, s[40:43], s60 offen sc1\n" \
  "s_waitcnt lgkmcnt(0)\n" \
  "buffer_store_dwordx4 v[116:119], v240, s[40:43], s60 offen offset:1024 sc1\n" \
  "v_mov_b32 v68, s48\n" \
  "s_sub_u32 s60, s48, 60\n" \
  "s_cmp_lt_u32 s60, 1\n" \
  "s_cbranch_scc1 .LM_ef_%=\n" \
  ".LM_ef_b_%=:\n" \
  "s_branch .LM_rot_%=\n" \
  "s_nop 7\n" \
  ".LM_slow_%=:\n" \
  "s_bitcmp1_b32 s70, 0\n" \
  "s_cbranch_scc1 .LM_back_%=\n" \
  "s_waitcnt lgkmcnt(0)\n" \
  "s_cmp_lt_u32 s70, 10\n" \
  "s_nop 3\n" \
  "s_cbranch_scc1 .LM_nol_%=\n" \
  "v_max_u32 v189, v112, v113\n" \
  "v_max3_u32 v189, v189, v114, v115\n" \
  "v_max3_u32 v189, v189, v116, v117\n" \
  "v_max3_u32 v189, v189, v118, v119\n" \
  "v_max3_u32 v189, v189, v120, v121\n" \
  "v_max3_u32 v189, v189, v122, v123\n" \
  "v_max3_u32 v189, v189, v124, v125\n" \
  "v_max3_u32 v189, v189, v126, v127\n" \
  "v_max3_u32 v189, v189, v128, v129\n" \
  "v_max3_u32 v189, v189, v130, v131\n" \
  "v_max3_u32 v189, v189, v132, v133\n" \
  "v_max3_u32 v189, v189, v134, v135\n" \
  "v_max3_u32 v189, v189, v140, v141\n" \
  "v_max3_u32 v189, v189, v142, v143\n" \
  "v_max3_u32 v189, v189, v136, v137\n" \
  "v_max3_u32 v189, v189, v138, v139\n" \
  "v_max3_u32 v189, v189, v144, v145\n" \
  "v_max3_u32 v189, v189, v146, v147\n" \
  "v_cmp_eq_u32 vcc, -1, v189\n" \
  "s_cbranch_vccz .LM_back_%=\n" \
  ".LM_nol_%=:\n" \
  "s_add_u32 s70, s70, 2\n" \
  "s_cmp_gt_u32 s70, 65536\n" \
  "s_cbranch_scc1 .LM_dset_%=\n" \
  "s_barrier\n" \
  "v_mov_b64 v[72:73], 0\n" \
  "v_mov_b64 v[74:75], 0\n" \
  "v_mov_b64 v[76:77], 0\n" \
  "v_mov_b64 v[78:79], 0\n" \
  "v_mov_b64 v[80:81], 0\n" \
  "v_mov_b64 v[82:83], 0\n" \
  "v_mov_b64 v[84:85], 0\n" \
  "v_mov_b64 v[86:87], 0\n" \
  "v_mov_b64 v[88:89], 0\n" \
  "v_mov_b64 v[90:91], 0\n" \
  "s_branch .LM_rt_%=\n" \
  ".LM_dset_%=:\n" \
  "v_mov_b32 v132, 1\n" \
  "v_mov_b32 v133, s55\n" \
  "buffer_store_dwordx2 v[132:133], off, s[40:43], %40 offset:128 sc1\n" \
  "s_mov_b32 s70, 1\n" \
  "v_mov_b64 v[72:73], 0\n" \
  "v_mov_b64 v[74:75], 0\n" \
  "v_mov_b64 v[76:77], 0\n" \
  "v_mov_b64 v[78:79], 0\n" \
  "v_mov_b64 v[80:81], 0\n" \
  "v_mov_b64 v[82:83], 0\n" \
  "v_mov_b64 v[84:85], 0\n" \
  "v_mov_b64 v[86:87], 0\n" \
  "v_mov_b64 v[88:89], 0\n" \
  "v_mov_b64 v[90:91], 0\n" \
  "s_waitcnt vmcnt(0) lgkmcnt(0)\n" \
  "s_barrier\n" \
  "s_branch .LM_p_%=\n" \
  ".LM_xr_%=:\n" \
  "s_waitcnt lgkmcnt(0)\n" \
  "s_nop 7\n" \
  "ds_read_b64_tr_b16 v[132:133], v244\n" \
  "ds_read_b64_tr_b16 v[134:135], v244 offset:256\n" \
  "ds_read_b64_tr_b16 v[136:137], v244 offset:512\n" \
  "ds_read_b64_tr_b16 v[138:139], v244 offset:768\n" \
  "ds_read_b64_tr_b16 v[140:141], v244 offset:1024\n" \
  "ds_read_b64_tr_b16 v[142:143], v244 offset:1280\n" \
  "ds_read_b64_tr_b16 v[144:145], v244 offset:1536\n" \
  "ds_read_b64_tr_b16 v[146:147], v244 offset:1792\n" \
  "s_waitcnt lgkmcnt(6)\n" \
  "v_mfma_f32_16x16x32_bf16 v[148:151], v[132:135], v[96:99], 0\n" \
  "s_waitcnt lgkmcnt(4)\n" \
  "v_mfma_f32_16x16x32_bf16 v[148:151], v[136:139], v[100:103], v[148:151]\n" \
  "s_waitcnt lgkmcnt(2)\n" \
  "v_mfma_f32_16x16x32_bf16 v[148:151], v[140:143], v[104:107], v[148:151]\n" \
  "s_waitcnt lgkmcnt(0)\n" \
  "v_mfma_f32_16x16x32_bf16 v[148:151], v[144:147], v[108:111], v[148:151]\n" \
  "s_sub_u32 s60, s48, 60\n" \
  "s_lshl_b32 s60, s60, 16\n" \
  "s_add_u32 s60, s60, s97\n" \
  "s_cmp_eq_u32 s48, 0\n" \
  "s_cselect_b32 s60, 2130706432, s60\n" \
  "s_nop 2\n" \
  "v_max_f32 v158, v148, v149\n" \
  "v_max3_f32 v158, v150, v151, v158\n" \
  "v_mov_b32 v159, v158\n" \
  "s_nop 1\n" \
  "v_permlane32_swap_b32 v158, v159\n" \
  "v_max_f32 v158, v158, v159\n" \
  "v_mul_f32 v152, s65, v158\n" \
  "v_min_f32 v152, v152, v178\n" \
  "v_fma_f32 v148, v148, s64, v152\n" \
  "v_fma_f32 v149, v149, s64, v152\n" \
  "v_fma_f32 v150, v150, s64, v152\n" \
  "v_fma_f32 v151, v151, s64, v152\n" \
  "v_sub_f32 v160, v152, v178\n" \
  "v_exp_f32 v148, v148\n" \
  "v_exp_f32 v149, v149\n" \
  "v_exp_f32 v150, v150\n" \
  "v_exp_f32 v151, v151\n" \
  "v_exp_f32 v160, v160\n" \
  "v_add_f32 v153, v148, v149\n" \
  "v_add_f32 v158, v150, v151\n" \
  "v_add_f32 v153, v153, v158\n" \
  "v_mov_b32 v159, v153\n" \
  "s_nop 1\n" \
  "v_permlane32_swap_b32 v153, v159\n" \
  "v_add_f32 v153, v153, v159\n" \
  "v_fmac_f32 v153, v160, v179\n" \
  "v_rcp_f32 v154, v153\n" \
  "s_nop 0\n" \
  "v_mul_f32 v148, v148, v154\n" \
  "v_mul_f32 v149, v149, v154\n" \
  "v_mul_f32 v150, v150, v154\n" \
  "v_mul_f32 v151, v151, v154\n" \
  "v_mul_f32 v160, v160, v154\n" \
  "v_cvt_pk_bf16_f32 v156, v148, v149\n" \
  "v_cvt_pk_bf16_f32 v157, v150, v151\n" \
  "v_cndmask_b32 v156, v156, v160, s[92:93]\n" \
  "buffer_store_dwordx2 v[156:157], v251, s[40:43], s60 offen sc1\n" \
  "s_cmp_eq_u32 s48, 63\n" \
  "s_cbranch_scc1 .LM_end_%=\n" \
  ".LM_xq_%=:\n" \
  "s_add_u32 s61, s48, 1\n" \
  "s_lshl_b32 s60, s61, 19\n" \
  "s_add_u32 s60, s60, s47\n" \
  "s_lshr_b32 s62, s60, 8\n" \
  "s_and_b32 s62, s62, 8192\n" \
  "s_xor_b32 s62, s62, s60\n" \
  "s_cmp_eq_u32 s48, 0\n" \
  "s_cselect_b32 s62, 2130706432, s62\n" \
  "s_add_u32 s63, s62, 4096\n" \
  "buffer_load_dwordx4 v[96:99], v249, s[40:43], s62 offen sc1\n" \
  "buffer_load_dwordx4 v[100:103], v249, s[40:43], s62 offen offset:2048 sc1\n" \
  "buffer_load_dwordx4 v[104:107], v249, s[40:43], s63 offen sc1\n" \
  "buffer_load_dwordx4 v[108:111], v249, s[40:43], s63 offen offset:2048 sc1\n" \
  "s_sub_u32 s60, s61, 60\n" \
  "s_lshl_b32 s60, s60, 16\n" \
  "s_add_u32 s60, s60, s97\n" \
  "s_cmp_eq_u32 s48, 0\n" \
  "s_cselect_b32 s60, 2130706432, s60\n" \
  "buffer_load_dwordx2 v[178:179], v250, s[40:43], s60 offen sc1\n" \
  "s_cbranch_scc1 .LM_dry_b_%=\n" \
  "s_branch .LM_xr_b_%=\n" \
  ".LM_xl_%=:\n" \
  "s_cmp_eq_u32 s48, 59\n" \
  "s_cbranch_scc1 .LM_xc_%=\n" \
  "s_cmp_lg_u32 s48, 58\n" \
  "s_cbranch_scc1 .LM_xr_%=\n" \
  "buffer_load_dwordx2 v[178:179], off, s[40:43], %46 sc1\n" \
  "s_branch .LM_xr_b_%=\n" \
  ".LM_xc_%=:\n" \
  "v_readfirstlane_b32 s63, v179\n" \
  "s_cmp_eq_u32 s63, s55\n" \
  "s_cbranch_scc1 .LM_xq_%=\n" \
  "s_mov_b32 s69, 0\n" \
  ".LM_xs_%=:\n" \
  "buffer_load_dwordx2 v[178:179], off, s[40:43], %46 sc1\n" \
  "s_waitcnt vmcnt(0)\n" \
  "v_readfirstlane_b32 s62, v178\n" \
  "v_readfirstlane_b32 s63, v179\n" \
  "s_cmp_eq_u32 s63, s55\n" \
  "s_cselect_b32 s62, s62, 0\n" \
  "s_cmp_lg_u32 s62, 0\n" \
  "s_cbranch_scc1 .LM_xs_x_%=\n" \
  "s_sleep 2\n" \
  "s_add_u32 s69, s69, 1\n" \
  "s_and_b32 s63, s69, 1023\n" \
  "s_cbranch_scc1 .LM_xs_%=\n" \
  "s_cmp_gt_u32 s69, 2097152\n" \
  "s_cbranch_scc1 .LM_xs_set_%=\n" \
  "buffer_load_dwordx2 v[178:179], off, s[40:43], %40 offset:128 sc1\n" \
  "s_waitcnt vmcnt(0)\n" \
  "v_readfirstlane_b32 s63, v179\n" \
  "s_cmp_eq_u32 s63, s55\n" \
  "s_cbranch_scc0 .LM_xs_%=\n" \
  "s_branch .LM_xs_x_%=\n" \
  ".LM_xs_set_%=:\n" \
  "v_mov_b32 v178, 1\n" \
  "v_mov_b32 v179, s55\n" \
  "buffer_store_dwordx2 v[178:179], off, s[40:43], %40 offset:128 sc1\n" \
  "s_waitcnt vmcnt(0)\n" \
  ".LM_xs_x_%=:\n" \
  "s_branch .LM_xq_%=\n" \
  ".LM_pk_go_%=:\n" \
  "s_mov_b32 s94, s62\n" \
  "s_mov_b32 s69, 0\n" \
  ".LM_pk_%=:\n" \
  "buffer_load_dwordx2 v[188:189], off, s[40:43], s74 sc1\n" \
  "s_waitcnt vmcnt(0)\n" \
  "v_readfirstlane_b32 s54, v188\n" \
  "v_readfirstlane_b32 s63, v189\n" \
  "s_cmp_eq_u32 s63, s55\n" \
  "s_cselect_b32 s54, s54, 0\n" \
  "s_cmp_gt_i32 s54, s94\n" \
  "s_cbranch_scc1 .LM_pk_x_%=\n" \
  "s_sleep 2\n" \
  "s_add_u32 s69, s69, 1\n" \
  "s_and_b32 s63, s69, 1023\n" \
  "s_cbranch_scc1 .LM_pk_%=\n" \
  "s_cmp_gt_u32 s69, 2097152\n" \
  "s_cbranch_scc1 .LM_pk_set_%=\n" \
  "buffer_load_dwordx2 v[188:189], off, s[40:43], %40 offset:128 sc1\n" \
  "s_waitcnt vmcnt(0)\n" \
  "v_readfirstlane_b32 s63, v189\n" \
  "s_cmp_eq_u32 s63, s55\n" \
  "s_cbranch_scc0 .LM_pk_%=\n" \
  "s_branch .LM_pk_x_%=\n" \
  ".LM_pk_set_%=:\n" \
  "v_mov_b32 v188, 1\n" \
  "v_mov_b32 v189, s55\n" \
  "buffer_store_dwordx2 v[188:189], off, s[40:43], %40 offset:128 sc1\n" \
  "s_waitcnt vmcnt(0)\n" \
  ".LM_pk_x_%=:\n" \
  "s_branch .LM_pk_ok_%=\n" \
  ".LM_ef_%=:\n" \
  "s_waitcnt vmcnt(0)\n" \
  "buffer_store_dwordx2 v[68:69], off, s[40:43], %36 sc1\n" \
  "s_branch .LM_ef_b_%=\n" \
  ".LM_ex_%=:\n" \
  "s_waitcnt vmcnt(0) lgkmcnt(0)\n" \
  "s_nop 7\n" \
  "ds_read_b64_tr_b16 v[116:117], v243\n" \
  "ds_read_b64_tr_b16 v[118:119], v243 offset:256\n" \
  "ds_read_b64_tr_b16 v[120:121], v243 offset:512\n" \
  "ds_read_b64_tr_b16 v[122:123], v243 offset:768\n" \
  "ds_read_b64_tr_b16 v[124:125], v243 offset:1024\n" \
  "ds_read_b64_tr_b16 v[126:127], v243 offset:1280\n" \
  "ds_read_b64_tr_b16 v[128:129], v243 offset:1536\n" \
  "ds_read_b64_tr_b16 v[130:131], v243 offset:1792\n" \
  "ds_read_b128 v[132:135], v177\n" \
  "ds_read_b128 v[140:143], v177 offset:2048\n" \
  "ds_read_b128 v[144:147], v177 offset:4096\n" \
  "ds_read_b128 v[148:151], v177 offset:6144\n" \
  "s_waitcnt lgkmcnt(3)\n" \
  "v_mfma_f32_16x16x32_bf16 v[152:155], v[132:135], v[116:119], 0\n" \
  "s_waitcnt lgkmcnt(2)\n" \
  "v_mfma_f32_16x16x32_bf16 v[152:155], v[140:143], v[120:123], v[152:155]\n" \
  "s_waitcnt lgkmcnt(1)\n" \
  "v_mfma_f32_16x16x32_bf16 v[152:155], v[144:147], v[124:127], v[152:155]\n" \
  "s_waitcnt lgkmcnt(0)\n" \
  "v_mfma_f32_16x16x32_bf16 v[152:155], v[148:151], v[128:131], v[152:155]\n" \
  "s_nop 7\n" \
  "ds_read_b128 v[132:135], v177 offset:1024\n" \
  "ds_read_b128 v[140:143], v177 offset:3072\n" \
  "ds_read_b128 v[144:147], v177 offset:5120\n" \
  "ds_read_b128 v[148:151], v177 offset:7168\n" \
  "s_waitcnt lgkmcnt(3)\n" \
  "v_mfma_f32_16x16x32_bf16 v[156:159], v[132:135], v[116:119], 0\n" \
  "s_waitcnt lgkmcnt(2)\n" \
  "v_mfma_f32_16x16x32_bf16 v[156:159], v[140:143], v[120:123], v[156:159]\n" \
  "s_waitcnt lgkmcnt(1)\n" \
  "v_mfma_f32_16x16x32_bf16 v[156:159], v[144:147], v[124:127], v[156:159]\n" \
  "s_waitcnt lgkmcnt(0)\n" \
  "v_mfma_f32_16x16x32_bf16 v[156:159], v[148:151], v[128:131], v[156:159]\n" \
  "s_nop 7\n" \
  "v_max_f32 v160, v136, v137\n" \
  "v_max3_f32 v160, v160, v138, v139\n" \
  "v_max3_f32 v160, v160, v152, v153\n" \
  "v_max3_f32 v160, v160, v154, v155\n" \
  "s_nop 2\n" \
  "v_max3_f32 v160, v160, v156, v157\n" \
  "v_max3_f32 v160, v160, v158, v159\n" \
  "v_mov_b32 v161, v160\n" \
  "s_nop 1\n" \
  "v_permlane32_swap_b32 v160, v161\n" \
  "v_max_f32 v160, v160, v161\n" \
  "v_mov_b32 v161, v160\n" \
  "s_nop 1\n" \
  "v_permlane16_swap_b32 v160, v161\n" \
  "v_max_f32 v160, v160, v161\n" \
  "v_sub_f32 v112, v136, v160\n" \
  "v_sub_f32 v113, v137, v160\n" \
  "v_sub_f32 v114, v138, v160\n" \
  "v_sub_f32 v115, v139, v160\n" \
  "v_sub_f32 v152, v152, v160\n" \
  "v_sub_f32 v153, v153, v160\n" \
  "v_sub_f32 v154, v154, v160\n" \
  "v_sub_f32 v155, v155, v160\n" \
  "v_sub_f32 v156, v156, v160\n" \
  "v_sub_f32 v157, v157, v160\n" \
  "v_sub_f32 v158, v158, v160\n" \
  "v_sub_f32 v159, v159, v160\n" \
  "v_exp_f32 v112, v112\n" \
  "v_exp_f32 v113, v113\n" \
  "v_exp_f32 v114, v114\n" \
  "v_exp_f32 v115, v115\n" \
  "v_exp_f32 v152, v152\n" \
  "v_exp_f32 v153, v153\n" \
  "v_exp_f32 v154, v154\n" \
  "v_exp_f32 v155, v155\n" \
  "v_exp_f32 v156, v156\n" \
  "v_exp_f32 v157, v157\n" \
  "v_exp_f32 v158, v158\n" \
  "v_exp_f32 v159, v159\n" \
  "v_add_f32 v160, v112, v113\n" \
  "v_add_f32 v160, v160, v114\n" \
  "v_add_f32 v160, v160, v115\n" \
  "v_mul_f32 v160, 0.5, v160\n" \
  "v_add_f32 v160, v160, v152\n" \
  "v_add_f32 v160, v160, v153\n" \
  "v_add_f32 v160, v160, v154\n" \
  "v_add_f32 v160, v160, v155\n" \
  "v_add_f32 v160, v160, v156\n" \
  "v_add_f32 v160, v160, v157\n" \
  "v_add_f32 v160, v160, v158\n" \
  "v_add_f32 v160, v160, v159\n" \
  "v_mov_b32 v161, v160\n" \
  "s_nop 1\n" \
  "v_permlane32_swap_b32 v160, v161\n" \
  "v_add_f32 v160, v160, v161\n" \
  "v_mov_b32 v161, v160\n" \
  "s_nop 1\n" \
  "v_permlane16_swap_b32 v160, v161\n" \
  "v_add_f32 v160, v160, v161\n" \
  "v_rcp_f32 v188, v160\n" \
  "v_cvt_pk_bf16_f32 v152, v152, v153\n" \
  "v_cvt_pk_bf16_f32 v153, v154, v155\n" \
  "v_cvt_pk_bf16_f32 v156, v156, v157\n" \
  "v_cvt_pk_bf16_f32 v157, v158, v159\n" \
  "v_mov_b32 v161, 17408\n" \
  "s_cmp_eq_u32 s48, 0\n" \
  "s_cbranch_scc1 .LM_ex2_%=\n" \
  ".LM_ex1_%=:\n" \
  "ds_read_b64 v[162:163], v161\n" \
  "s_waitcnt lgkmcnt(0)\n" \
  "v_min_u32 v162, v162, v163\n" \
  "v_cmp_ge_u32 vcc, s48, v162\n" \
  "s_cbranch_vccnz .LM_ex1_%=\n" \
  ".LM_ex2_%=:\n" \
  "buffer_store_dwordx2 v[152:153], v247, s[40:43], 0 offen sc1\n" \
  "buffer_store_dwordx2 v[156:157], v247, s[40:43], 0 offen offset:256 sc1\n" \
  "v_pk_fma_f32 v[112:113], v[112:113], v[180:181], v[92:93] op_sel_hi:[1,0,1]\n" \
  "v_pk_fma_f32 v[114:115], v[114:115], v[180:181], v[94:95] op_sel_hi:[1,0,1]\n" \
  "v_pk_mul_f32 v[112:113], v[112:113], v[188:189] op_sel_hi:[1,0]\n" \
  "v_pk_mul_f32 v[114:115], v[114:115], v[188:189] op_sel_hi:[1,0]\n" \
  "v_cvt_pk_bf16_f32 v188, v112, v113\n" \
  "v_cvt_pk_bf16_f32 v189, v114, v115\n" \
  "s_waitcnt vmcnt(0)\n" \
  "s_mov_b32 s52, 2.0\n" \
  "s_cmp_eq_u32 s48, 0\n" \
  "s_cbranch_scc1 .LM_ex_b_%=\n" \
  "v_lshrrev_b32 v161, 1, v240\n" \
  "v_min_u32 v161, 280, v161\n" \
  "buffer_load_dwordx2 v[162:163], v161, s[40:43], %47 offen sc1\n" \
  "s_waitcnt vmcnt(0)\n" \
  "v_cmp_eq_u32 vcc, s55, v163\n" \
  "s_nop 1\n" \
  "v_cndmask_b32 v162, 0, v162, vcc\n" \
  "v_add_u32 v160, 2, v162\n" \
  "s_mov_b32 s69, 0\n" \
  ".LM_ex3_%=:\n" \
  "s_sleep 8\n" \
  "buffer_load_dwordx2 v[162:163], v161, s[40:43], %47 offen sc1\n" \
  "s_waitcnt vmcnt(0)\n" \
  "v_cmp_eq_u32 vcc, s55, v163\n" \
  "s_nop 1\n" \
  "v_cndmask_b32 v162, 0, v162, vcc\n" \
  "v_cmp_gt_u32 vcc, v160, v162\n" \
  "s_cbranch_vccz .LM_ex_b_%=\n" \
  "s_add_u32 s69, s69, 1\n" \
  "s_and_b32 s63, s69, 1023\n" \
  "s_cbranch_scc1 .LM_ex3_%=\n" \
  "s_cmp_gt_u32 s69, 2097152\n" \
  "s_cbranch_scc1 .LM_ex3_set_%=\n" \
  "buffer_load_dwordx2 v[162:163], off, s[40:43], %40 offset:128 sc1\n" \
  "s_waitcnt vmcnt(0)\n" \
  "v_readfirstlane_b32 s63, v163\n" \
  "s_cmp_eq_u32 s63, s55\n" \
  "s_cbranch_scc0 .LM_ex3_%=\n" \
  "s_branch .LM_ex_b_%=\n" \
  ".LM_ex3_set_%=:\n" \
  "v_mov_b32 v162, 1\n" \
  "v_mov_b32 v163, s55\n" \
  "buffer_store_dwordx2 v[162:163], off, s[40:43], %40 offset:128 sc1\n" \
  "s_waitcnt vmcnt(0)\n" \
  "s_branch .LM_ex_b_%=\n" \
  ".LM_end_%=:\n" \
  "s_waitcnt vmcnt(0)\n" \
  "s_waitcnt vmcnt(0)\n" \
  "s_nop 7\n" \
  "s_endpgm\n"
#define M_ASM_VOPS "+{v239}"(ci.sidx), "+{v240}"(ci.l16), "+{v241}"(ci.qw), "+{v242}"(ci.kw), "+{v243}"(ci.fq), "+{v244}"(ci.fk), "+{v245}"(ci.kxo), "+{v246}"(ci.pst), "+{v247}"(ci.ps2), "+{v248}"(ci.lx), "+{v249}"(ci.qxo), "+{v250}"(ci.sto), "+{v251}"(ci.plx), "+{v252}"(ci.mk), "+{v253}"(ci.ck0), "+{v254}"(ci.ck2), "+{v255}"(ci.hx), "+{v200}"(ci.vq0), "+{v201}"(ci.vq1), "+{v202}"(ci.vq2), "+{v203}"(ci.vq3), "+{v204}"(ci.tro0), "+{v205}"(ci.tro1), "+{v206}"(ci.m0o)
#define M_ASM_SOPS "s"(ci.ws), "s"(ci.Wa), "s"(ci.Wb), "s"(ci.mem0), "s"(ci.w), "s"(ci.o_a), "s"(ci.o_b), "s"(ci.o_nx), "s"(ci.o_exp), "s"(ci.o_u), "s"(ci.lab), "s"(ci.f_p), "s"(ci.f_m), "s"(ci.f_u), "s"(ci.f_dur), "s"(ci.f_go), "s"(ci.o_ctl), "s"(ci.o_dbg), "s"(ci.push), "s"(ci.dret), "s"(ci.u5), "s"(ci.o_qx), "s"(ci.f_s), "s"(ci.f_r)
#define M_ASM_CLOB "v0", "v1", "v2", "v3", "v4", "v5", "v6", "v7", "v8", "v9", "v10", "v11", "v12", "v13", "v14", "v15", "v16", "v17", "v18", "v19", "v20", "v21", "v22", "v23", "v24", "v25", "v26", "v27", "v28", "v29", "v30", "v31", "v32", "v33", "v34", "v35", "v36", "v37", "v38", "v39", "v40", "v41", "v42", "v43", "v44", "v45", "v46", "v47", "v48", "v49", "v50", "v51", "v52", "v53", "v54", "v55", "v56", "v57", "v58", "v59", "v60", "v61", "v62", "v63", "v64", "v65", "v66", "v67", "v68", "v69", "v70", "v71", "v72", "v73", "v74", "v75", "v76", "v77", "v78", "v79", "v80", "v81", "v82", "v83", "v84", "v85", "v86", "v87", "v88", "v89", "v90", "v91", "v92", "v93", "v94", "v95", "v96", "v97", "v98", "v99", "v100", "v101", "v102", "v103", "v104", "v105", "v106", "v107", "v108", "v109", "v110", "v111", "v112", "v113", "v114", "v115", "v116", "v117", "v118", "v119", "v120", "v121", "v122", "v123", "v124", "v125", "v126", "v127", "v128", "v129", "v130", "v131", "v132", "v133", "v134", "v135", "v136", "v137", "v138", "v139", "v140", "v141", "v142", "v143", "v144", "v145", "v146", "v147", "v148", "v149", "v150", "v151", "v152", "v153", "v154", "v155", "v156", "v157", "v158", "v159", "v160", "v161", "v162", "v163", "v164", "v165", "v166", "v167", "v168", "v169", "v170", "v171", "v172", "v173", "v174", "v175", "v176", "v177", "v178", "v179", "v180", "v181", "v182", "v183", "v184", "v185", "v186", "v187", "v188", "v189", "v190", "v191", "v192", "v193", "v194", "v195", "v196", "v197", "v198", "v199", "a0", "a1", "a2", "a3", "a4", "a5", "a6", "a7", "a8", "a9", "a10", "a11", "a12", "a13", "a14", "a15", "a16", "a17", "a18", "a19", "a20", "a21", "a22", "a23", "a24", "a25", "a26", "a27", "a28", "a29", "a30", "a31", "a32", "a33", "a34", "a35", "a36", "a37", "a38", "a39", "a40", "a41", "a42", "a43", "a44", "a45", "a46", "a47", "a48", "a49", "a50", "a51", "a52", "a53", "a54", "a55", "a56", "a57", "a58", "a59", "a60", "a61", "a62", "a63", "a64", "a65", "a66", "a67", "a68", "a69", "a70", "a71", "a72", "a73", "a74", "a75", "a76", "a77", "a78", "a79", "a80", "a81", "a82", "a83", "a84", "a85", "a86", "a87", "a88", "a89", "a90", "a91", "a92", "a93", "a94", "a95", "a96", "a97", "a98", "a99", "a100", "a101", "a102", "a103", "a104", "a105", "a106", "a107", "a108", "a109", "a110", "a111", "a112", "a113", "a114", "a115", "a116", "a117", "a118", "a119", "a120", "a121", "a122", "a123", "a124", "a125", "a126", "a127", "a128", "a129", "a130", "a131", "a132", "a133", "a134", "a135", "a136", "a137", "a138", "a139", "a140", "a141", "a142", "a143", "a144", "a145", "a146", "a147", "a148", "a149", "a150", "a151", "a152", "a153", "a154", "a155", "a156", "a157", "a158", "a159", "a160", "a161", "a162", "a163", "a164", "a165", "a166", "a167", "a168", "a169", "a170", "a171", "a172", "a173", "a174", "a175", "a176", "a177", "a178", "a179", "a180", "a181", "a182", "a183", "a184", "a185", "a186", "a187", "a188", "a189", "a190", "a191", "a192", "a193", "a194", "a195", "a196", "a197", "a198", "a199", "a200", "a201", "a202", "a203", "a204", "a205", "a206", "a207", "a208", "a209", "a210", "a211", "a212", "a213", "a214", "a215", "a216", "a217", "a218", "a219", "a220", "a221", "a222", "a223", "a224", "a225", "a226", "a227", "a228", "a229", "a230", "a231", "a232", "a233", "a234", "a235", "a236", "a237", "a238", "a239", "a240", "a241", "a242", "a243", "a244", "a245", "a246", "a247", "a248", "a249", "a250", "a251", "a252", "a253", "a254", "a255", "s40", "s41", "s42", "s43", "s44", "s45", "s46", "s47", "s48", "s49", "s50", "s51", "s52", "s53", "s54", "s55", "s56", "s57", "s58", "s59", "s60", "s61", "s62", "s63", "s64", "s65", "s66", "s67", "s68", "s69", "s70", "s71", "s72", "s73", "s74", "s75", "s76", "s77", "s78", "s79", "s80", "s81", "s82", "s83", "s84", "s85", "s86", "s87", "s88", "s89", "s90", "s91", "s92", "s93", "s94", "s95", "s96", "s97", "s98", "s99", "vcc", "scc", "memory"
#define V_ASM_TEXT \
  "s_mov_b64 s[40:41], %32\n" \
  "s_and_b32 s41, s41, 0xffff\n" \
  "s_mov_b32 s42, 255852544\n" \
  "s_mov_b32 s43, 0x00020000\n" \
  "s_mov_b32 s46, 4718592\n" \
  "s_mov_b32 s47, 0x00020000\n" \
  "s_lshl_b32 s72, %36, 11\n" \
  "buffer_load_dword v196, off, s[40:43], %48 sc1\n" \
  "s_mov_b64 s[44:45], %34\n" \
  "s_and_b32 s45, s45, 0xffff\n" \
  "s_add_u32 s60, %38, 0\n" \
  "s_lshl_b32 s61, s72, 1\n" \
  "s_add_u32 m0, s61, 16384\n" \
  "s_nop 0\n" \
  "buffer_load_dwordx4 v204, s[44:47], s60 offen lds\n" \
  "buffer_load_dwordx4 v205, s[44:47], s60 offen offset:1024 lds\n" \
  "buffer_load_dwordx4 v206, s[44:47], s60 offen offset:2048 lds\n" \
  "buffer_load_dwordx4 v207, s[44:47], s60 offen offset:3072 lds\n" \
  "s_add_u32 s60, %38, 18432\n" \
  "s_lshl_b32 s61, s72, 1\n" \
  "s_add_u32 m0, s61, 32768\n" \
  "s_nop 0\n" \
  "buffer_load_dwordx4 v204, s[44:47], s60 offen lds\n" \
  "buffer_load_dwordx4 v205, s[44:47], s60 offen offset:1024 lds\n" \
  "buffer_load_dwordx4 v206, s[44:47], s60 offen offset:2048 lds\n" \
  "buffer_load_dwordx4 v207, s[44:47], s60 offen offset:3072 lds\n" \
  "s_add_u32 s60, %38, 256\n" \
  "s_lshl_b32 s61, s72, 1\n" \
  "s_add_u32 m0, s61, 49152\n" \
  "s_nop 0\n" \
  "buffer_load_dwordx4 v204, s[44:47], s60 offen lds\n" \
  "buffer_load_dwordx4 v205, s[44:47], s60 offen offset:1024 lds\n" \
  "buffer_load_dwordx4 v206, s[44:47], s60 offen offset:2048 lds\n" \
  "buffer_load_dwordx4 v207, s[44:47], s60 offen offset:3072 lds\n" \
  "s_add_u32 s60, %38, 18688\n" \
  "s_lshl_b32 s61, s72, 1\n" \
  "s_add_u32 m0, s61, 65536\n" \
  "s_nop 0\n" \
  "buffer_load_dwordx4 v204, s[44:47], s60 offen lds\n" \
  "buffer_load_dwordx4 v205, s[44:47], s60 offen offset:1024 lds\n" \
  "buffer_load_dwordx4 v206, s[44:47], s60 offen offset:2048 lds\n" \
  "buffer_load_dwordx4 v207, s[44:47], s60 offen offset:3072 lds\n" \
  "s_add_u32 s60, %38, 512\n" \
  "s_lshl_b32 s61, s72, 1\n" \
  "s_add_u32 m0, s61, 81920\n" \
  "s_nop 0\n" \
  "buffer_load_dwordx4 v204, s[44:47], s60 offen lds\n" \
  "buffer_load_dwordx4 v205, s[44:47], s60 offen offset:1024 lds\n" \
  "buffer_load_dwordx4 v206, s[44:47], s60 offen offset:2048 lds\n" \
  "buffer_load_dwordx4 v207, s[44:47], s60 offen offset:3072 lds\n" \
  "s_add_u32 s60, %38, 18944\n" \
  "s_lshl_b32 s61, s72, 1\n" \
  "s_add_u32 m0, s61, 98304\n" \
  "s_nop 0\n" \
  "buffer_load_dwordx4 v204, s[44:47], s60 offen lds\n" \
  "buffer_load_dwordx4 v205, s[44:47], s60 offen offset:1024 lds\n" \
  "buffer_load_dwordx4 v206, s[44:47], s60 offen offset:2048 lds\n" \
  "buffer_load_dwordx4 v207, s[44:47], s60 offen offset:3072 lds\n" \
  "s_add_u32 s60, %38, 768\n" \
  "s_lshl_b32 s61, s72, 1\n" \
  "s_add_u32 m0, s61, 114688\n" \
  "s_nop 0\n" \
  "buffer_load_dwordx4 v204, s[44:47], s60 offen lds\n" \
  "buffer_load_dwordx4 v205, s[44:47], s60 offen offset:1024 lds\n" \
  "buffer_load_dwordx4 v206, s[44:47], s60 offen offset:2048 lds\n" \
  "buffer_load_dwordx4 v207, s[44:47], s60 offen offset:3072 lds\n" \
  "s_add_u32 s60, %38, 19200\n" \
  "s_lshl_b32 s61, s72, 1\n" \
  "s_add_u32 m0, s61, 131072\n" \
  "s_nop 0\n" \
  "buffer_load_dwordx4 v204, s[44:47], s60 offen lds\n" \
  "buffer_load_dwordx4 v205, s[44:47], s60 offen offset:1024 lds\n" \
  "buffer_load_dwordx4 v206, s[44:47], s60 offen offset:2048 lds\n" \
  "buffer_load_dwordx4 v207, s[44:47], s60 offen offset:3072 lds\n" \
  "s_mov_b32 s60, %38\n" \
  "s_lshl_b32 s61, s72, 1\n" \
  "s_add_u32 m0, s61, 147456\n" \
  "s_nop 0\n" \
  "buffer_load_dwordx4 v208, s[44:47], s60 offen lds\n" \
  "buffer_load_dwordx4 v209, s[44:47], s60 offen offset:1024 lds\n" \
  "buffer_load_dwordx4 v210, s[44:47], s60 offen offset:2048 lds\n" \
  "buffer_load_dwordx4 v211, s[44:47], s60 offen offset:3072 lds\n" \
  "s_mov_b64 s[44:45], %35\n" \
  "s_and_b32 s45, s45, 0xffff\n" \
  "s_lshr_b32 s60, s72, 3\n" \
  "s_mov_b32 m0, s72\n" \
  "s_nop 0\n" \
  "buffer_load_dwordx4 v214, s[44:47], s60 offen lds\n" \
  "s_add_u32 s60, s60, 128\n" \
  "s_add_u32 m0, s72, 1024\n" \
  "s_nop 0\n" \
  "buffer_load_dwordx4 v214, s[44:47], s60 offen lds\n" \
  "s_movk_i32 s60, 1024\n" \
  "s_movk_i32 m0, 8192\n" \
  "s_nop 0\n" \
  "buffer_load_dwordx4 v214, s[44:47], s60 offen lds\n" \
  "s_waitcnt vmcnt(27)\n" \
  "s_barrier\n" \
  "v_add_u32 v194, 16384, v215\n" \
  "v_xor_b32 v195, 2176, v194\n" \
  "ds_read_b64_tr_b16 a[144:145], v194\n" \
  "ds_read_b64_tr_b16 a[146:147], v194 offset:512\n" \
  "ds_read_b64_tr_b16 a[148:149], v195\n" \
  "ds_read_b64_tr_b16 a[150:151], v195 offset:512\n" \
  "ds_read_b64_tr_b16 a[152:153], v194 offset:8192\n" \
  "ds_read_b64_tr_b16 a[154:155], v194 offset:8704\n" \
  "ds_read_b64_tr_b16 a[156:157], v195 offset:8192\n" \
  "ds_read_b64_tr_b16 a[158:159], v195 offset:8704\n" \
  "v_add_u32 v194, 32768, v215\n" \
  "v_xor_b32 v195, 2176, v194\n" \
  "ds_read_b64_tr_b16 a[160:161], v194\n" \
  "ds_read_b64_tr_b16 a[162:163], v194 offset:512\n" \
  "ds_read_b64_tr_b16 a[164:165], v195\n" \
  "ds_read_b64_tr_b16 a[166:167], v195 offset:512\n" \
  "ds_read_b64_tr_b16 a[168:169], v194 offset:8192\n" \
  "ds_read_b64_tr_b16 a[170:171], v194 offset:8704\n" \
  "ds_read_b64_tr_b16 a[172:173], v195 offset:8192\n" \
  "ds_read_b64_tr_b16 a[174:175], v195 offset:8704\n" \
  "v_add_u32 v194, 49152, v215\n" \
  "v_xor_b32 v195, 2176, v194\n" \
  "ds_read_b64_tr_b16 a[176:177], v194\n" \
  "ds_read_b64_tr_b16 a[178:179], v194 offset:512\n" \
  "ds_read_b64_tr_b16 a[180:181], v195\n" \
  "ds_read_b64_tr_b16 a[182:183], v195 offset:512\n" \
  "ds_read_b64_tr_b16 a[184:185], v194 offset:8192\n" \
  "ds_read_b64_tr_b16 a[186:187], v194 offset:8704\n" \
  "ds_read_b64_tr_b16 a[188:189], v195 offset:8192\n" \
  "ds_read_b64_tr_b16 a[190:191], v195 offset:8704\n" \
  "s_waitcnt lgkmcnt(0)\n" \
  "s_barrier\n" \
  "s_mov_b64 s[44:45], %33\n" \
  "s_and_b32 s45, s45, 0xffff\n" \
  "s_add_u32 s60, %37, 0\n" \
  "s_lshl_b32 s61, s72, 1\n" \
  "s_add_u32 m0, s61, 16384\n" \
  "s_nop 0\n" \
  "buffer_load_dwordx4 v200, s[44:47], s60 offen lds\n" \
  "buffer_load_dwordx4 v201, s[44:47], s60 offen offset:1024 lds\n" \
  "buffer_load_dwordx4 v202, s[44:47], s60 offen offset:2048 lds\n" \
  "buffer_load_dwordx4 v203, s[44:47], s60 offen offset:3072 lds\n" \
  "s_add_u32 s60, %37, 524288\n" \
  "s_lshl_b32 s61, s72, 1\n" \
  "s_add_u32 m0, s61, 32768\n" \
  "s_nop 0\n" \
  "buffer_load_dwordx4 v200, s[44:47], s60 offen lds\n" \
  "buffer_load_dwordx4 v201, s[44:47], s60 offen offset:1024 lds\n" \
  "buffer_load_dwordx4 v202, s[44:47], s60 offen offset:2048 lds\n" \
  "buffer_load_dwordx4 v203, s[44:47], s60 offen offset:3072 lds\n" \
  "s_add_u32 s60, %37, 1048576\n" \
  "s_lshl_b32 s61, s72, 1\n" \
  "s_add_u32 m0, s61, 49152\n" \
  "s_nop 0\n" \
  "buffer_load_dwordx4 v200, s[44:47], s60 offen lds\n" \
  "buffer_load_dwordx4 v201, s[44:47], s60 offen offset:1024 lds\n" \
  "buffer_load_dwordx4 v202, s[44:47], s60 offen offset:2048 lds\n" \
  "buffer_load_dwordx4 v203, s[44:47], s60 offen offset:3072 lds\n" \
  "v_readfirstlane_b32 s55, v196\n" \
  "s_add_u32 s55, s55, 1\n" \
  "s_waitcnt vmcnt(27)\n" \
  "s_barrier\n" \
  "v_add_u32 v194, 65536, v215\n" \
  "v_xor_b32 v195, 2176, v194\n" \
  "ds_read_b64_tr_b16 a[192:193], v194\n" \
  "ds_read_b64_tr_b16 a[194:195], v194 offset:512\n" \
  "ds_read_b64_tr_b16 a[196:197], v195\n" \
  "ds_read_b64_tr_b16 a[198:199], v195 offset:512\n" \
  "ds_read_b64_tr_b16 a[200:201], v194 offset:8192\n" \
  "ds_read_b64_tr_b16 a[202:203], v194 offset:8704\n" \
  "ds_read_b64_tr_b16 a[204:205], v195 offset:8192\n" \
  "ds_read_b64_tr_b16 a[206:207], v195 offset:8704\n" \
  "v_add_u32 v194, 81920, v215\n" \
  "v_xor_b32 v195, 2176, v194\n" \
  "ds_read_b64_tr_b16 a[208:209], v194\n" \
  "ds_read_b64_tr_b16 a[210:211], v194 offset:512\n" \
  "ds_read_b64_tr_b16 a[212:213], v195\n" \
  "ds_read_b64_tr_b16 a[214:215], v195 offset:512\n" \
  "ds_read_b64_tr_b16 a[216:217], v194 offset:8192\n" \
  "ds_read_b64_tr_b16 a[218:219], v194 offset:8704\n" \
  "ds_read_b64_tr_b16 a[220:221], v195 offset:8192\n" \
  "ds_read_b64_tr_b16 a[222:223], v195 offset:8704\n" \
  "v_add_u32 v194, 98304, v215\n" \
  "v_xor_b32 v195, 2176, v194\n" \
  "ds_read_b64_tr_b16 a[224:225], v194\n" \
  "ds_read_b64_tr_b16 a[226:227], v194 offset:512\n" \
  "ds_read_b64_tr_b16 a[228:229], v195\n" \
  "ds_read_b64_tr_b16 a[230:231], v195 offset:512\n" \
  "ds_read_b64_tr_b16 a[232:233], v194 offset:8192\n" \
  "ds_read_b64_tr_b16 a[234:235], v194 offset:8704\n" \
  "ds_read_b64_tr_b16 a[236:237], v195 offset:8192\n" \
  "ds_read_b64_tr_b16 a[238:239], v195 offset:8704\n" \
  "s_waitcnt lgkmcnt(0)\n" \
  "s_barrier\n" \
  "s_add_u32 s60, %37, 1572864\n" \
  "s_lshl_b32 s61, s72, 1\n" \
  "s_add_u32 m0, s61, 65536\n" \
  "s_nop 0\n" \
  "buffer_load_dwordx4 v200, s[44:47], s60 offen lds\n" \
  "buffer_load_dwordx4 v201, s[44:47], s60 offen offset:1024 lds\n" \
  "buffer_load_dwordx4 v202, s[44:47], s60 offen offset:2048 lds\n" \
  "buffer_load_dwordx4 v203, s[44:47], s60 offen offset:3072 lds\n" \
  "s_add_u32 s60, %37, 2097152\n" \
  "s_lshl_b32 s61, s72, 1\n" \
  "s_add_u32 m0, s61, 81920\n" \
  "s_nop 0\n" \
  "buffer_load_dwordx4 v200, s[44:47], s60 offen lds\n" \
  "buffer_load_dwordx4 v201, s[44:47], s60 offen offset:1024 lds\n" \
  "buffer_load_dwordx4 v202, s[44:47], s60 offen offset:2048 lds\n" \
  "buffer_load_dwordx4 v203, s[44:47], s60 offen offset:3072 lds\n" \
  "s_add_u32 s60, %37, 2621440\n" \
  "s_lshl_b32 s61, s72, 1\n" \
  "s_add_u32 m0, s61, 98304\n" \
  "s_nop 0\n" \
  "buffer_load_dwordx4 v200, s[44:47], s60 offen lds\n" \
  "buffer_load_dwordx4 v201, s[44:47], s60 offen offset:1024 lds\n" \
  "buffer_load_dwordx4 v202, s[44:47], s60 offen offset:2048 lds\n" \
  "buffer_load_dwordx4 v203, s[44:47], s60 offen offset:3072 lds\n" \
  "s_waitcnt vmcnt(27)\n" \
  "s_barrier\n" \
  "v_add_u32 v194, 114688, v215\n" \
  "v_xor_b32 v195, 2176, v194\n" \
  "ds_read_b64_tr_b16 a[240:241], v194\n" \
  "ds_read_b64_tr_b16 a[242:243], v194 offset:512\n" \
  "ds_read_b64_tr_b16 a[244:245], v195\n" \
  "ds_read_b64_tr_b16 a[246:247], v195 offset:512\n" \
  "ds_read_b64_tr_b16 a[248:249], v194 offset:8192\n" \
  "ds_read_b64_tr_b16 a[250:251], v194 offset:8704\n" \
  "ds_read_b64_tr_b16 a[252:253], v195 offset:8192\n" \
  "ds_read_b64_tr_b16 a[254:255], v195 offset:8704\n" \
  "v_add_u32 v194, 131072, v215\n" \
  "v_xor_b32 v195, 2176, v194\n" \
  "ds_read_b64_tr_b16 v[0:1], v194\n" \
  "ds_read_b64_tr_b16 v[2:3], v194 offset:512\n" \
  "ds_read_b64_tr_b16 v[4:5], v195\n" \
  "ds_read_b64_tr_b16 v[6:7], v195 offset:512\n" \
  "ds_read_b64_tr_b16 v[8:9], v194 offset:8192\n" \
  "ds_read_b64_tr_b16 v[10:11], v194 offset:8704\n" \
  "ds_read_b64_tr_b16 v[12:13], v195 offset:8192\n" \
  "ds_read_b64_tr_b16 v[14:15], v195 offset:8704\n" \
  "v_mov_b32 v194, v216\n" \
  "v_xor_b32 v195, 2176, v194\n" \
  "ds_read_b64_tr_b16 v[16:17], v194\n" \
  "ds_read_b64_tr_b16 v[18:19], v194 offset:512\n" \
  "ds_read_b64_tr_b16 v[20:21], v195\n" \
  "ds_read_b64_tr_b16 v[22:23], v195 offset:512\n" \
  "ds_read_b64_tr_b16 v[24:25], v194 offset:8192\n" \
  "ds_read_b64_tr_b16 v[26:27], v194 offset:8704\n" \
  "ds_read_b64_tr_b16 v[28:29], v195 offset:8192\n" \
  "ds_read_b64_tr_b16 v[30:31], v195 offset:8704\n" \
  "v_mov_b32 v194, v217\n" \
  "v_xor_b32 v195, 2176, v194\n" \
  "ds_read_b64_tr_b16 v[32:33], v194\n" \
  "ds_read_b64_tr_b16 v[34:35], v194 offset:512\n" \
  "ds_read_b64_tr_b16 v[36:37], v195\n" \
  "ds_read_b64_tr_b16 v[38:39], v195 offset:512\n" \
  "ds_read_b64_tr_b16 v[40:41], v194 offset:8192\n" \
  "ds_read_b64_tr_b16 v[42:43], v194 offset:8704\n" \
  "ds_read_b64_tr_b16 v[44:45], v195 offset:8192\n" \
  "ds_read_b64_tr_b16 v[46:47], v195 offset:8704\n" \
  "s_waitcnt lgkmcnt(0)\n" \
  "s_barrier\n" \
  "s_add_u32 s60, %37, 3145728\n" \
  "s_lshl_b32 s61, s72, 1\n" \
  "s_add_u32 m0, s61, 114688\n" \
  "s_nop 0\n" \
  "buffer_load_dwordx4 v200, s[44:47], s60 offen lds\n" \
  "buffer_load_dwordx4 v201, s[44:47], s60 offen offset:1024 lds\n" \
  "buffer_load_dwordx4 v202, s[44:47], s60 offen offset:2048 lds\n" \
  "buffer_load_dwordx4 v203, s[44:47], s60 offen offset:3072 lds\n" \
  "s_add_u32 s60, %37, 3670016\n" \
  "s_lshl_b32 s61, s72, 1\n" \
  "s_add_u32 m0, s61, 131072\n" \
  "s_nop 0\n" \
  "buffer_load_dwordx4 v200, s[44:47], s60 offen lds\n" \
  "buffer_load_dwordx4 v201, s[44:47], s60 offen offset:1024 lds\n" \
  "buffer_load_dwordx4 v202, s[44:47], s60 offen offset:2048 lds\n" \
  "buffer_load_dwordx4 v203, s[44:47], s60 offen offset:3072 lds\n" \
  "s_add_u32 s60, %37, 4194304\n" \
  "s_lshl_b32 s61, s72, 1\n" \
  "s_add_u32 m0, s61, 147456\n" \
  "s_nop 0\n" \
  "buffer_load_dwordx4 v200, s[44:47], s60 offen lds\n" \
  "buffer_load_dwordx4 v201, s[44:47], s60 offen offset:1024 lds\n" \
  "buffer_load_dwordx4 v202, s[44:47], s60 offen offset:2048 lds\n" \
  "buffer_load_dwordx4 v203, s[44:47], s60 offen offset:3072 lds\n" \
  "buffer_load_dwordx2 v[198:199], off, s[40:43], %47 sc1\n" \
  "s_waitcnt vmcnt(25)\n" \
  "s_barrier\n" \
  "v_add_u32 v194, 16384, v212\n" \
  "ds_read_b64_tr_b16 a[0:1], v194\n" \
  "ds_read_b64_tr_b16 a[2:3], v194 offset:512\n" \
  "ds_read_b64_tr_b16 a[4:5], v194 offset:8192\n" \
  "ds_read_b64_tr_b16 a[6:7], v194 offset:8704\n" \
  "v_add_u32 v195, 16384, v213\n" \
  "ds_read_b64_tr_b16 a[72:73], v195\n" \
  "ds_read_b64_tr_b16 a[74:75], v195 offset:512\n" \
  "ds_read_b64_tr_b16 a[76:77], v195 offset:8192\n" \
  "ds_read_b64_tr_b16 a[78:79], v195 offset:8704\n" \
  "v_add_u32 v194, 32768, v212\n" \
  "ds_read_b64_tr_b16 a[8:9], v194\n" \
  "ds_read_b64_tr_b16 a[10:11], v194 offset:512\n" \
  "ds_read_b64_tr_b16 a[12:13], v194 offset:8192\n" \
  "ds_read_b64_tr_b16 a[14:15], v194 offset:8704\n" \
  "v_add_u32 v195, 32768, v213\n" \
  "ds_read_b64_tr_b16 a[80:81], v195\n" \
  "ds_read_b64_tr_b16 a[82:83], v195 offset:512\n" \
  "ds_read_b64_tr_b16 a[84:85], v195 offset:8192\n" \
  "ds_read_b64_tr_b16 a[86:87], v195 offset:8704\n" \
  "v_add_u32 v194, 49152, v212\n" \
  "ds_read_b64_tr_b16 a[16:17], v194\n" \
  "ds_read_b64_tr_b16 a[18:19], v194 offset:512\n" \
  "ds_read_b64_tr_b16 a[20:21], v194 offset:8192\n" \
  "ds_read_b64_tr_b16 a[22:23], v194 offset:8704\n" \
  "v_add_u32 v195, 49152, v213\n" \
  "ds_read_b64_tr_b16 a[88:89], v195\n" \
  "ds_read_b64_tr_b16 a[90:91], v195 offset:512\n" \
  "ds_read_b64_tr_b16 a[92:93], v195 offset:8192\n" \
  "ds_read_b64_tr_b16 a[94:95], v195 offset:8704\n" \
  "s_waitcnt lgkmcnt(0)\n" \
  "s_waitcnt vmcnt(13)\n" \
  "s_barrier\n" \
  "v_add_u32 v194, 65536, v212\n" \
  "ds_read_b64_tr_b16 a[24:25], v194\n" \
  "ds_read_b64_tr_b16 a[26:27], v194 offset:512\n" \
  "ds_read_b64_tr_b16 a[28:29], v194 offset:8192\n" \
  "ds_read_b64_tr_b16 a[30:31], v194 offset:8704\n" \
  "v_add_u32 v195, 65536, v213\n" \
  "ds_read_b64_tr_b16 a[96:97], v195\n" \
  "ds_read_b64_tr_b16 a[98:99], v195 offset:512\n" \
  "ds_read_b64_tr_b16 a[100:101], v195 offset:8192\n" \
  "ds_read_b64_tr_b16 a[102:103], v195 offset:8704\n" \
  "v_add_u32 v194, 81920, v212\n" \
  "ds_read_b64_tr_b16 a[32:33], v194\n" \
  "ds_read_b64_tr_b16 a[34:35], v194 offset:512\n" \
  "ds_read_b64_tr_b16 a[36:37], v194 offset:8192\n" \
  "ds_read_b64_tr_b16 a[38:39], v194 offset:8704\n" \
  "v_add_u32 v195, 81920, v213\n" \
  "ds_read_b64_tr_b16 a[104:105], v195\n" \
  "ds_read_b64_tr_b16 a[106:107], v195 offset:512\n" \
  "ds_read_b64_tr_b16 a[108:109], v195 offset:8192\n" \
  "ds_read_b64_tr_b16 a[110:111], v195 offset:8704\n" \
  "v_add_u32 v194, 98304, v212\n" \
  "ds_read_b64_tr_b16 a[40:41], v194\n" \
  "ds_read_b64_tr_b16 a[42:43], v194 offset:512\n" \
  "ds_read_b64_tr_b16 a[44:45], v194 offset:8192\n" \
  "ds_read_b64_tr_b16 a[46:47], v194 offset:8704\n" \
  "v_add_u32 v195, 98304, v213\n" \
  "ds_read_b64_tr_b16 a[112:113], v195\n" \
  "ds_read_b64_tr_b16 a[114:115], v195 offset:512\n" \
  "ds_read_b64_tr_b16 a[116:117], v195 offset:8192\n" \
  "ds_read_b64_tr_b16 a[118:119], v195 offset:8704\n" \
  "s_waitcnt lgkmcnt(0)\n" \
  "s_waitcnt vmcnt(1)\n" \
  "s_barrier\n" \
  "v_add_u32 v194, 114688, v212\n" \
  "ds_read_b64_tr_b16 a[48:49], v194\n" \
  "ds_read_b64_tr_b16 a[50:51], v194 offset:512\n" \
  "ds_read_b64_tr_b16 a[52:53], v194 offset:8192\n" \
  "ds_read_b64_tr_b16 a[54:55], v194 offset:8704\n" \
  "v_add_u32 v195, 114688, v213\n" \
  "ds_read_b64_tr_b16 a[120:121], v195\n" \
  "ds_read_b64_tr_b16 a[122:123], v195 offset:512\n" \
  "ds_read_b64_tr_b16 a[124:125], v195 offset:8192\n" \
  "ds_read_b64_tr_b16 a[126:127], v195 offset:8704\n" \
  "v_add_u32 v194, 131072, v212\n" \
  "ds_read_b64_tr_b16 a[56:57], v194\n" \
  "ds_read_b64_tr_b16 a[58:59], v194 offset:512\n" \
  "ds_read_b64_tr_b16 a[60:61], v194 offset:8192\n" \
  "ds_read_b64_tr_b16 a[62:63], v194 offset:8704\n" \
  "v_add_u32 v195, 131072, v213\n" \
  "ds_read_b64_tr_b16 a[128:129], v195\n" \
  "ds_read_b64_tr_b16 a[130:131], v195 offset:512\n" \
  "ds_read_b64_tr_b16 a[132:133], v195 offset:8192\n" \
  "ds_read_b64_tr_b16 a[134:135], v195 offset:8704\n" \
  "v_add_u32 v194, 147456, v212\n" \
  "ds_read_b64_tr_b16 a[64:65], v194\n" \
  "ds_read_b64_tr_b16 a[66:67], v194 offset:512\n" \
  "ds_read_b64_tr_b16 a[68:69], v194 offset:8192\n" \
  "ds_read_b64_tr_b16 a[70:71], v194 offset:8704\n" \
  "v_add_u32 v195, 147456, v213\n" \
  "ds_read_b64_tr_b16 a[136:137], v195\n" \
  "ds_read_b64_tr_b16 a[138:139], v195 offset:512\n" \
  "ds_read_b64_tr_b16 a[140:141], v195 offset:8192\n" \
  "ds_read_b64_tr_b16 a[142:143], v195 offset:8704\n" \
  "s_waitcnt lgkmcnt(0)\n" \
  "s_waitcnt vmcnt(0)\n" \
  "v_readfirstlane_b32 s96, v198\n" \
  "v_readfirstlane_b32 s63, v199\n" \
  "s_cmp_eq_u32 s63, s55\n" \
  "s_cselect_b32 s96, s96, 0\n" \
  "s_cmp_ge_u32 s96, 0xc0000000\n" \
  "s_cbranch_scc1 .LV_go_x_%=\n" \
  "s_mov_b32 s69, 0\n" \
  ".LV_go_%=:\n" \
  "buffer_load_dwordx2 v[196:197], off, s[40:43], %47 sc1\n" \
  "s_waitcnt vmcnt(0)\n" \
  "v_readfirstlane_b32 s96, v196\n" \
  "v_readfirstlane_b32 s63, v197\n" \
  "s_cmp_eq_u32 s63, s55\n" \
  "s_cselect_b32 s96, s96, 0\n" \
  "s_cmp_ge_u32 s96, 0xc0000000\n" \
  "s_cbranch_scc1 .LV_go_x_%=\n" \
  "s_sleep 8\n" \
  "s_add_u32 s69, s69, 1\n" \
  "s_and_b32 s63, s69, 1023\n" \
  "s_cbranch_scc1 .LV_go_%=\n" \
  "s_cmp_gt_u32 s69, 2097152\n" \
  "s_cbranch_scc1 .LV_go_set_%=\n" \
  "buffer_load_dwordx2 v[196:197], off, s[40:43], %48 offset:128 sc1\n" \
  "s_waitcnt vmcnt(0)\n" \
  "v_readfirstlane_b32 s63, v197\n" \
  "s_cmp_eq_u32 s63, s55\n" \
  "s_cbranch_scc0 .LV_go_%=\n" \
  "s_branch .LV_go_x_%=\n" \
  ".LV_go_set_%=:\n" \
  "v_mov_b32 v196, 1\n" \
  "v_mov_b32 v197, s55\n" \
  "buffer_store_dwordx2 v[196:197], off, s[40:43], %48 offset:128 sc1\n" \
  "s_waitcnt vmcnt(0)\n" \
  ".LV_go_x_%=:\n" \
  "s_lshr_b32 s60, s96, 0\n" \
  "s_and_b32 s60, s60, 7\n" \
  "s_lshl_b32 s60, s60, 12\n" \
  "s_xor_b32 s74, %43, s60\n" \
  "s_xor_b32 s75, %46, s60\n" \
  "s_bfe_u32 s56, s96, 0x30010\n" \
  "s_lshl_b32 s56, s56, 12\n" \
  "s_or_b32 s56, s56, 0x200000\n" \
  "s_mov_b32 s57, 1052672\n" \
  "s_lshl_b32 s61, %42, 2\n" \
  "s_lshr_b32 s51, s96, s61\n" \
  "s_and_b32 s51, s51, 7\n" \
  "s_lshl_b32 s51, s51, 12\n" \
  "s_xor_b32 s51, s51, 4210688\n" \
  "s_mov_b32 s71, 0\n" \
  "v_mov_b64 v[48:49], -1\n" \
  "v_mov_b64 v[50:51], -1\n" \
  "v_and_b32 v194, 3, v251\n" \
  "v_lshlrev_b32 v194, 2, v194\n" \
  "v_lshrrev_b32 v194, v194, s96\n" \
  "v_and_b32 v194, 7, v194\n" \
  "v_lshlrev_b32 v194, 12, v194\n" \
  "v_and_b32 v251, -4, v251\n" \
  "v_xor_b32 v251, v251, v194\n" \
  "v_and_b32 v194, 3, v252\n" \
  "v_lshlrev_b32 v194, 2, v194\n" \
  "v_lshrrev_b32 v194, v194, s96\n" \
  "v_and_b32 v194, 7, v194\n" \
  "v_lshlrev_b32 v194, 12, v194\n" \
  "v_and_b32 v252, -4, v252\n" \
  "v_xor_b32 v252, v252, v194\n" \
  "v_and_b32 v194, 3, v253\n" \
  "v_lshlrev_b32 v194, 2, v194\n" \
  "v_lshrrev_b32 v194, v194, s96\n" \
  "v_and_b32 v194, 7, v194\n" \
  "v_lshlrev_b32 v194, 12, v194\n" \
  "v_and_b32 v253, -4, v253\n" \
  "v_xor_b32 v253, v253, v194\n" \
  "v_and_b32 v194, 3, v254\n" \
  "v_lshlrev_b32 v194, 2, v194\n" \
  "v_lshrrev_b32 v194, v194, s96\n" \
  "v_and_b32 v194, 7, v194\n" \
  "v_lshlrev_b32 v194, 12, v194\n" \
  "v_and_b32 v254, -4, v254\n" \
  "v_xor_b32 v254, v254, v194\n" \
  "v_and_b32 v194, 3, v255\n" \
  "v_lshlrev_b32 v194, 2, v194\n" \
  "v_lshrrev_b32 v194, v194, s96\n" \
  "v_and_b32 v194, 7, v194\n" \
  "v_lshlrev_b32 v194, 12, v194\n" \
  "v_and_b32 v255, -4, v255\n" \
  "v_xor_b32 v255, v255, v194\n" \
  "v_xor_b32 v185, s56, v251\n" \
  "buffer_store_dwordx2 v[48:49], v185, s[40:43], 0 offen sc1\n" \
  "v_xor_b32 v186, s56, v252\n" \
  "buffer_store_dwordx2 v[48:49], v186, s[40:43], 0 offen sc1\n" \
  "v_xor_b32 v187, s56, v253\n" \
  "buffer_store_dwordx2 v[48:49], v187, s[40:43], 0 offen sc1\n" \
  "v_xor_b32 v188, s56, v254\n" \
  "buffer_store_dwordx2 v[48:49], v188, s[40:43], 0 offen sc1\n" \
  "v_xor_b32 v189, s56, v255\n" \
  "buffer_store_dwordx2 v[48:49], v189, s[40:43], 0 offen sc1\n" \
  "s_xor_b32 s62, s56, s57\n" \
  "s_nop 1\n" \
  "v_xor_b32 v185, s62, v251\n" \
  "buffer_store_dwordx2 v[48:49], v185, s[40:43], 0 offen sc1\n" \
  "v_xor_b32 v186, s62, v252\n" \
  "buffer_store_dwordx2 v[48:49], v186, s[40:43], 0 offen sc1\n" \
  "v_xor_b32 v187, s62, v253\n" \
  "buffer_store_dwordx2 v[48:49], v187, s[40:43], 0 offen sc1\n" \
  "v_xor_b32 v188, s62, v254\n" \
  "buffer_store_dwordx2 v[48:49], v188, s[40:43], 0 offen sc1\n" \
  "v_xor_b32 v189, s62, v255\n" \
  "buffer_store_dwordx2 v[48:49], v189, s[40:43], 0 offen sc1\n" \
  "s_andn2_b32 s60, 2, s96\n" \
  "s_lshl_b32 s60, s60, 12\n" \
  "s_xor_b32 s60, s60, 8192\n" \
  "s_xor_b32 s58, %39, s60\n" \
  "s_mov_b32 s49, 0\n" \
  "s_add_u32 s47, %53, s60\n" \
  "s_add_u32 s97, s60, 8388608\n" \
  "s_xor_b32 s50, %40, s60\n" \
  "s_mov_b32 s48, 0\n" \
  "s_mov_b32 s54, 0\n" \
  "s_cmp_eq_u32 %36, 0\n" \
  "s_cselect_b64 s[88:89], -1, 0\n" \
  "s_mov_b32 s68, 0\n" \
  "s_mov_b32 s70, 1\n" \
  "s_mov_b32 s73, 0\n" \
  "s_mov_b32 s44, 0\n" \
  "s_mov_b32 s53, 0\n" \
  "s_memrealtime s[86:87]\n" \
  "s_memtime s[76:77]\n" \
  "s_waitcnt lgkmcnt(0)\n" \
  "s_mov_b32 s90, s76\n" \
  "s_mov_b32 s87, 340\n" \
  "s_mov_b32 s64, 0x3e0293ee\n" \
  "s_mov_b32 s65, 0xbe0293ee\n" \
  "v_mov_b64 v[56:57], 0\n" \
  "v_mov_b64 v[58:59], 0\n" \
  "v_mov_b64 v[60:61], 0\n" \
  "v_mov_b64 v[62:63], 0\n" \
  "v_mov_b64 v[64:65], 0\n" \
  "v_mov_b64 v[66:67], 0\n" \
  "v_mov_b64 v[68:69], 0\n" \
  "v_mov_b64 v[70:71], 0\n" \
  "v_mov_b64 v[72:73], 0\n" \
  "v_mov_b64 v[74:75], 0\n" \
  "v_mov_b64 v[76:77], 0\n" \
  "v_mov_b64 v[78:79], 0\n" \
  "v_mov_b64 v[80:81], 0\n" \
  "v_mov_b64 v[82:83], 0\n" \
  "v_mov_b32 v52, 0\n" \
  "v_mov_b32 v53, s55\n" \
  "v_mov_b64 v[54:55], v[52:53]\n" \
  "v_mov_b64 v[86:87], 0\n" \
  "s_mov_b32 s67, 2130706432\n" \
  "s_waitcnt vmcnt(0) lgkmcnt(0)\n" \
  "s_barrier\n" \
  "s_branch .LV_p_%=\n" \
  "s_nop 7\n" \
  ".p2align 6\n" \
  ".fill (((16 - (.LV_p_%= - .LV_rot_%=)) & 63) >> 2), 4, 0xBF800000\n" \
  ".LV_rot_%=:\n" \
  "s_xor_b32 s51, s51, s56\n" \
  "s_xor_b32 s71, s71, s56\n" \
  "v_xor_b32 v251, s56, v251\n" \
  "v_xor_b32 v252, s56, v252\n" \
  "v_xor_b32 v253, s56, v253\n" \
  "v_xor_b32 v254, s56, v254\n" \
  "v_xor_b32 v255, s56, v255\n" \
  "s_mov_b32 s60, s56\n" \
  "s_mov_b32 s56, s57\n" \
  "s_mov_b32 s57, s60\n" \
  "s_add_u32 s59, s51, 819200\n" \
  "s_mov_b32 m0, 0\n" \
  "s_mov_b32 s70, 0\n" \
  "s_waitcnt lgkmcnt(0)\n" \
  "s_waitcnt vmcnt(0)\n" \
  ".LV_rt_%=:\n" \
  "s_barrier\n" \
  ".LV_p_%=:\n" \
  "ds_read_b128 v[88:91], v243\n" \
  "ds_read_b128 v[92:95], v243 offset:1024\n" \
  "ds_read_b128 v[96:99], v243 offset:2048\n" \
  "ds_read_b128 v[100:103], v243 offset:3072\n" \
  "ds_read_b128 v[104:107], v243 offset:4096\n" \
  "s_waitcnt lgkmcnt(4)\n" \
  "v_smfmac_f32_16x16x64_bf16 v[56:59], v[88:91], a[0:7], v242\n" \
  "s_barrier\n" \
  "ds_read_b128 v[108:111], v243 offset:5120\n" \
  "ds_read_b128 v[112:115], v243 offset:6144\n" \
  "s_waitcnt lgkmcnt(5)\n" \
  "v_smfmac_f32_16x16x64_bf16 v[56:59], v[92:95], a[8:15], v242\n" \
  "ds_read_b128 v[116:119], v243 offset:7168\n" \
  "s_waitcnt lgkmcnt(5)\n" \
  "v_smfmac_f32_16x16x64_bf16 v[56:59], v[96:99], a[16:23], v242\n" \
  "ds_read_b128 v[120:123], v243 offset:8192\n" \
  "s_waitcnt lgkmcnt(5)\n" \
  "v_smfmac_f32_16x16x64_bf16 v[56:59], v[100:103], a[24:31], v242\n" \
  "s_waitcnt lgkmcnt(4)\n" \
  "v_smfmac_f32_16x16x64_bf16 v[56:59], v[104:107], a[32:39], v242\n" \
  "s_waitcnt lgkmcnt(3)\n" \
  "v_smfmac_f32_16x16x64_bf16 v[56:59], v[108:111], a[40:47], v242\n" \
  "s_waitcnt lgkmcnt(2)\n" \
  "v_smfmac_f32_16x16x64_bf16 v[56:59], v[112:115], a[48:55], v242\n" \
  "s_waitcnt lgkmcnt(1)\n" \
  "v_smfmac_f32_16x16x64_bf16 v[56:59], v[116:119], a[56:63], v242\n" \
  "s_waitcnt lgkmcnt(0)\n" \
  "v_smfmac_f32_16x16x64_bf16 v[56:59], v[120:123], a[64:71], v242\n" \
  "s_nop 2\n" \
  "v_smfmac_f32_16x16x64_bf16 v[60:63], v[88:91], a[72:79], v242\n" \
  "s_nop 2\n" \
  "v_smfmac_f32_16x16x64_bf16 v[60:63], v[92:95], a[80:87], v242\n" \
  "v_add_f32 v57, v57, v59\n" \
  "v_add_f32 v56, v56, v58\n" \
  "v_cmp_u_f32 vcc, v56, v57\n" \
  "v_cvt_pk_bf16_f32 v84, v56, v57\n" \
  "v_smfmac_f32_16x16x64_bf16 v[60:63], v[96:99], a[88:95], v242\n" \
  "s_cbranch_vccnz .LV_slow_%=\n" \
  ".LV_back_%=:\n" \
  "ds_write_b32 v244, v84\n" \
  "v_smfmac_f32_16x16x64_bf16 v[60:63], v[100:103], a[96:103], v242\n" \
  "buffer_store_dwordx2 v[52:53], off, s[40:43], %44 sc1\n" \
  "v_smfmac_f32_16x16x64_bf16 v[60:63], v[104:107], a[104:111], v242\n" \
  "v_smfmac_f32_16x16x64_bf16 v[60:63], v[108:111], a[112:119], v242\n" \
  "v_smfmac_f32_16x16x64_bf16 v[60:63], v[112:115], a[120:127], v242\n" \
  "s_waitcnt lgkmcnt(0)\n" \
  "s_barrier\n" \
  "v_smfmac_f32_16x16x64_bf16 v[60:63], v[116:119], a[128:135], v242\n" \
  "ds_read_b64_tr_b16 v[88:89], v245\n" \
  "ds_read_b64_tr_b16 v[90:91], v245 offset:512\n" \
  "v_smfmac_f32_16x16x64_bf16 v[60:63], v[120:123], a[136:143], v242\n" \
  "ds_read_b64_tr_b16 v[94:95], v245 offset:1536\n" \
  "ds_read_b64_tr_b16 v[92:93], v245 offset:1024\n" \
  "s_nop 5\n" \
  "v_add_f32 v60, v60, v62\n" \
  "v_add_f32 v61, v61, v63\n" \
  "v_cvt_pk_bf16_f32 v85, v60, v61\n" \
  "s_waitcnt lgkmcnt(2)\n" \
  "v_smfmac_f32_16x16x64_bf16 v[64:67], v[88:91], a[144:151], v248\n" \
  "ds_write_b32 v244, v85 offset:256\n" \
  "v_smfmac_f32_16x16x64_bf16 v[72:75], v[88:91], a[208:215], v248\n" \
  "v_smfmac_f32_16x16x64_bf16 v[68:71], v[88:91], a[176:183], v248\n" \
  "s_waitcnt lgkmcnt(1)\n" \
  "v_smfmac_f32_16x16x64_bf16 v[64:67], v[92:95], a[152:159], v248\n" \
  "v_smfmac_f32_16x16x64_bf16 v[72:75], v[92:95], a[216:223], v248\n" \
  "s_waitcnt lgkmcnt(0)\n" \
  "s_barrier\n" \
  "v_smfmac_f32_16x16x64_bf16 v[68:71], v[92:95], a[184:191], v248\n" \
  "ds_read_b64_tr_b16 v[98:99], v245 offset:768\n" \
  "v_smfmac_f32_16x16x64_bf16 v[76:79], v[88:91], a[240:247], v248\n" \
  "ds_read_b64_tr_b16 v[96:97], v245 offset:256\n" \
  "ds_read_b64_tr_b16 v[102:103], v245 offset:1792\n" \
  "v_smfmac_f32_16x16x64_bf16 v[76:79], v[92:95], a[248:255], v248\n" \
  "ds_read_b64_tr_b16 v[100:101], v245 offset:1280\n" \
  "v_smfmac_f32_16x16x64_bf16 v[80:83], v[88:91], v[16:23], v248\n" \
  "v_smfmac_f32_16x16x64_bf16 v[80:83], v[92:95], v[24:31], v248\n" \
  "s_waitcnt lgkmcnt(2)\n" \
  "v_smfmac_f32_16x16x64_bf16 v[64:67], v[96:99], a[160:167], v248\n" \
  "s_waitcnt lgkmcnt(0)\n" \
  "v_smfmac_f32_16x16x64_bf16 v[64:67], v[100:103], a[168:175], v248\n" \
  "s_nop 2\n" \
  "v_smfmac_f32_16x16x64_bf16 v[68:71], v[96:99], a[192:199], v248\n" \
  "s_nop 2\n" \
  "v_smfmac_f32_16x16x64_bf16 v[68:71], v[100:103], a[200:207], v248\n" \
  "s_nop 1\n" \
  "v_cvt_pk_bf16_f32 v64, v64, v65\n" \
  "v_smfmac_f32_16x16x64_bf16 v[72:75], v[96:99], a[224:231], v248\n" \
  "s_nop 1\n" \
  "v_cvt_pk_bf16_f32 v65, v66, v67\n" \
  "v_smfmac_f32_16x16x64_bf16 v[72:75], v[100:103], a[232:239], v248\n" \
  "s_nop 0\n" \
  "v_cvt_pk_bf16_f32 v68, v68, v69\n" \
  "v_smfmac_f32_16x16x64_bf16 v[80:83], v[96:99], v[32:39], v248\n" \
  "v_cvt_pk_bf16_f32 v69, v70, v71\n" \
  "v_smfmac_f32_16x16x64_bf16 v[76:79], v[96:99], v[0:7], v248\n" \
  "buffer_store_dwordx2 v[64:65], v251, s[40:43], 0 offen sc1\n" \
  "v_smfmac_f32_16x16x64_bf16 v[76:79], v[100:103], v[8:15], v248\n" \
  "buffer_store_dwordx2 v[68:69], v252, s[40:43], 0 offen sc1\n" \
  "v_cvt_pk_bf16_f32 v72, v72, v73\n" \
  "v_cvt_pk_bf16_f32 v73, v74, v75\n" \
  "v_smfmac_f32_16x16x64_bf16 v[80:83], v[100:103], v[40:47], v248\n" \
  "buffer_store_dwordx2 v[72:73], v253, s[40:43], 0 offen sc1\n" \
  "s_nop 2\n" \
  "v_cvt_pk_bf16_f32 v76, v76, v77\n" \
  "v_cvt_pk_bf16_f32 v77, v78, v79\n" \
  "buffer_store_dwordx2 v[76:77], v254, s[40:43], 0 offen sc1\n" \
  "s_nop 0\n" \
  "v_cvt_pk_bf16_f32 v80, v80, v81\n" \
  "v_cvt_pk_bf16_f32 v81, v82, v83\n" \
  "buffer_store_dwordx2 v[80:81], v255, s[40:43], 0 offen sc1\n" \
  ".LV_loop_%=:\n" \
  "v_mov_b64 v[56:57], 0\n" \
  "v_mov_b64 v[58:59], 0\n" \
  "v_mov_b64 v[60:61], 0\n" \
  "v_mov_b64 v[62:63], 0\n" \
  "v_mov_b64 v[64:65], 0\n" \
  "v_mov_b64 v[66:67], 0\n" \
  "v_mov_b64 v[68:69], 0\n" \
  "v_mov_b64 v[70:71], 0\n" \
  "v_mov_b64 v[72:73], 0\n" \
  "v_mov_b64 v[74:75], 0\n" \
  "v_mov_b64 v[76:77], 0\n" \
  "v_mov_b64 v[78:79], 0\n" \
  "v_mov_b64 v[80:81], 0\n" \
  "v_mov_b64 v[82:83], 0\n" \
  "ds_read_b32 v88, v246\n" \
  "ds_read_b32 v89, v246 offset:256\n" \
  "v_mov_b64 v[90:91], 0\n" \
  "s_sub_u32 s60, s48, 1\n" \
  "s_cselect_b32 s60, 8192, s60\n" \
  "s_lshl_b32 s60, s60, 14\n" \
  "s_xor_b32 s60, s60, s50\n" \
  "buffer_store_dwordx4 v[84:87], v247, s[40:43], s60 offen sc1\n" \
  "s_waitcnt lgkmcnt(0)\n" \
  "buffer_store_dwordx4 v[88:91], v249, s[40:43], s60 offen sc1\n" \
  "s_add_u32 s48, s48, 1\n" \
  "v_mov_b32 v52, s48\n" \
  "s_waitcnt lgkmcnt(0)\n" \
  "s_cmp_gt_u32 s48, 59\n" \
  "s_cbranch_scc1 .LV_ef_%=\n" \
  ".LV_ef_b_%=:\n" \
  "s_branch .LV_rot_%=\n" \
  "s_nop 7\n" \
  ".LV_slow_%=:\n" \
  "s_bitcmp1_b32 s70, 0\n" \
  "s_cbranch_scc1 .LV_back_%=\n" \
  "s_waitcnt lgkmcnt(0)\n" \
  "s_cmp_lt_u32 s70, 10\n" \
  "s_nop 3\n" \
  "s_cbranch_scc1 .LV_nol_%=\n" \
  "v_max_u32 v217, v88, v89\n" \
  "v_max3_u32 v217, v217, v90, v91\n" \
  "v_max3_u32 v217, v217, v92, v93\n" \
  "v_max3_u32 v217, v217, v94, v95\n" \
  "v_max3_u32 v217, v217, v96, v97\n" \
  "v_max3_u32 v217, v217, v98, v99\n" \
  "v_max3_u32 v217, v217, v100, v101\n" \
  "v_max3_u32 v217, v217, v102, v103\n" \
  "v_max3_u32 v217, v217, v104, v105\n" \
  "v_max3_u32 v217, v217, v106, v107\n" \
  "v_max3_u32 v217, v217, v108, v109\n" \
  "v_max3_u32 v217, v217, v110, v111\n" \
  "v_max3_u32 v217, v217, v112, v113\n" \
  "v_max3_u32 v217, v217, v114, v115\n" \
  "v_max3_u32 v217, v217, v116, v117\n" \
  "v_max3_u32 v217, v217, v118, v119\n" \
  "v_max3_u32 v217, v217, v120, v121\n" \
  "v_max3_u32 v217, v217, v122, v123\n" \
  "v_cmp_eq_u32 vcc, -1, v217\n" \
  "s_cbranch_vccz .LV_back_%=\n" \
  ".LV_nol_%=:\n" \
  "s_add_u32 s70, s70, 2\n" \
  "s_cmp_gt_u32 s70, 65536\n" \
  "s_cbranch_scc1 .LV_dset_%=\n" \
  "s_barrier\n" \
  "v_mov_b64 v[56:57], 0\n" \
  "v_mov_b64 v[58:59], 0\n" \
  "v_mov_b64 v[60:61], 0\n" \
  "v_mov_b64 v[62:63], 0\n" \
  "v_mov_b64 v[64:65], 0\n" \
  "v_mov_b64 v[66:67], 0\n" \
  "v_mov_b64 v[68:69], 0\n" \
  "v_mov_b64 v[70:71], 0\n" \
  "v_mov_b64 v[72:73], 0\n" \
  "v_mov_b64 v[74:75], 0\n" \
  "v_mov_b64 v[76:77], 0\n" \
  "v_mov_b64 v[78:79], 0\n" \
  "v_mov_b64 v[80:81], 0\n" \
  "v_mov_b64 v[82:83], 0\n" \
  "s_branch .LV_rt_%=\n" \
  ".LV_dset_%=:\n" \
  "v_mov_b32 v108, 1\n" \
  "v_mov_b32 v109, s55\n" \
  "buffer_store_dwordx2 v[108:109], off, s[40:43], %48 offset:128 sc1\n" \
  "s_mov_b32 s70, 1\n" \
  "v_mov_b64 v[56:57], 0\n" \
  "v_mov_b64 v[58:59], 0\n" \
  "v_mov_b64 v[60:61], 0\n" \
  "v_mov_b64 v[62:63], 0\n" \
  "v_mov_b64 v[64:65], 0\n" \
  "v_mov_b64 v[66:67], 0\n" \
  "v_mov_b64 v[68:69], 0\n" \
  "v_mov_b64 v[70:71], 0\n" \
  "v_mov_b64 v[72:73], 0\n" \
  "v_mov_b64 v[74:75], 0\n" \
  "v_mov_b64 v[76:77], 0\n" \
  "v_mov_b64 v[78:79], 0\n" \
  "v_mov_b64 v[80:81], 0\n" \
  "v_mov_b64 v[82:83], 0\n" \
  "s_waitcnt vmcnt(0) lgkmcnt(0)\n" \
  "s_barrier\n" \
  "s_branch .LV_p_%=\n" \
  ".LV_ef_%=:\n" \
  "s_cmp_eq_u32 s48, 64\n" \
  "s_cbranch_scc1 .LV_end_%=\n" \
  "s_waitcnt vmcnt(0)\n" \
  "s_barrier\n" \
  "buffer_store_dwordx2 v[52:53], off, s[40:43], %44 sc1\n" \
  "s_branch .LV_ef_b_%=\n" \
  ".LV_end_%=:\n" \
  "s_waitcnt vmcnt(0)\n" \
  "s_waitcnt vmcnt(0)\n" \
  "s_nop 7\n" \
  "s_endpgm\n"
#define V_ASM_VOPS "+{v242}"(ci.sidx), "+{v243}"(ci.l16), "+{v244}"(ci.viw), "+{v245}"(ci.fva), "+{v246}"(ci.pk3), "+{v247}"(ci.fvs), "+{v248}"(ci.sid2), "+{v249}"(ci.uo), "+{v250}"(ci.tlb), "+{v251}"(ci.po0), "+{v252}"(ci.po1), "+{v253}"(ci.po2), "+{v254}"(ci.po3), "+{v255}"(ci.po4), "+{v200}"(ci.vq0), "+{v201}"(ci.vq1), "+{v202}"(ci.vq2), "+{v203}"(ci.vq3), "+{v204}"(ci.vo0), "+{v205}"(ci.vo1), "+{v206}"(ci.vo2), "+{v207}"(ci.vo3), "+{v208}"(ci.v50), "+{v209}"(ci.v51), "+{v210}"(ci.v52), "+{v211}"(ci.v53), "+{v212}"(ci.tro0), "+{v213}"(ci.tro1), "+{v214}"(ci.m0o), "+{v215}"(ci.tro2), "+{v216}"(ci.tro3), "+{v217}"(ci.tro4)
#define V_ASM_SOPS "s"(ci.ws), "s"(ci.Wa), "s"(ci.Wb), "s"(ci.mem0), "s"(ci.w), "s"(ci.o_a), "s"(ci.o_b), "s"(ci.o_nx), "s"(ci.o_exp), "s"(ci.o_u), "s"(ci.lab), "s"(ci.f_p), "s"(ci.f_m), "s"(ci.f_u), "s"(ci.f_dur), "s"(ci.f_go), "s"(ci.o_ctl), "s"(ci.o_dbg), "s"(ci.push), "s"(ci.dret), "s"(ci.u5), "s"(ci.o_qx), "s"(ci.f_s), "s"(ci.f_r)
#define V_ASM_CLOB "v0", "v1", "v2", "v3", "v4", "v5", "v6", "v7", "v8", "v9", "v10", "v11", "v12", "v13", "v14", "v15", "v16", "v17", "v18", "v19", "v20", "v21", "v22", "v23", "v24", "v25", "v26", "v27", "v28", "v29", "v30", "v31", "v32", "v33", "v34", "v35", "v36", "v37", "v38", "v39", "v40", "v41", "v42", "v43", "v44", "v45", "v46", "v47", "v48", "v49", "v50", "v51", "v52", "v53", "v54", "v55", "v56", "v57", "v58", "v59", "v60", "v61", "v62", "v63", "v64", "v65", "v66", "v67", "v68", "v69", "v70", "v71", "v72", "v73", "v74", "v75", "v76", "v77", "v78", "v79", "v80", "v81", "v82", "v83", "v84", "v85", "v86", "v87", "v88", "v89", "v90", "v91", "v92", "v93", "v94", "v95", "v96", "v97", "v98", "v99", "v100", "v101", "v102", "v103", "v104", "v105", "v106", "v107", "v108", "v109", "v110", "v111", "v112", "v113", "v114", "v115", "v116", "v117", "v118", "v119", "v120", "v121", "v122", "v123", "v124", "v125", "v126", "v127", "v128", "v129", "v130", "v131", "v132", "v133", "v134", "v135", "v136", "v137", "v138", "v139", "v140", "v141", "v142", "v143", "v144", "v145", "v146", "v147", "v148", "v149", "v150", "v151", "v152", "v153", "v154", "v155", "v156", "v157", "v158", "v159", "v160", "v161", "v162", "v163", "v164", "v165", "v166", "v167", "v168", "v169", "v170", "v171", "v172", "v173", "v174", "v175", "v176", "v177", "v178", "v179", "v180", "v181", "v182", "v183", "v184", "v185", "v186", "v187", "v188", "v189", "v190", "v191", "v192", "v193", "v194", "v195", "v196", "v197", "v198", "v199", "a0", "a1", "a2", "a3", "a4", "a5", "a6", "a7", "a8", "a9", "a10", "a11", "a12", "a13", "a14", "a15", "a16", "a17", "a18", "a19", "a20", "a21", "a22", "a23", "a24", "a25", "a26", "a27", "a28", "a29", "a30", "a31", "a32", "a33", "a34", "a35", "a36", "a37", "a38", "a39", "a40", "a41", "a42", "a43", "a44", "a45", "a46", "a47", "a48", "a49", "a50", "a51", "a52", "a53", "a54", "a55", "a56", "a57", "a58", "a59", "a60", "a61", "a62", "a63", "a64", "a65", "a66", "a67", "a68", "a69", "a70", "a71", "a72", "a73", "a74", "a75", "a76", "a77", "a78", "a79", "a80", "a81", "a82", "a83", "a84", "a85", "a86", "a87", "a88", "a89", "a90", "a91", "a92", "a93", "a94", "a95", "a96", "a97", "a98", "a99", "a100", "a101", "a102", "a103", "a104", "a105", "a106", "a107", "a108", "a109", "a110", "a111", "a112", "a113", "a114", "a115", "a116", "a117", "a118", "a119", "a120", "a121", "a122", "a123", "a124", "a125", "a126", "a127", "a128", "a129", "a130", "a131", "a132", "a133", "a134", "a135", "a136", "a137", "a138", "a139", "a140", "a141", "a142", "a143", "a144", "a145", "a146", "a147", "a148", "a149", "a150", "a151", "a152", "a153", "a154", "a155", "a156", "a157", "a158", "a159", "a160", "a161", "a162", "a163", "a164", "a165", "a166", "a167", "a168", "a169", "a170", "a171", "a172", "a173", "a174", "a175", "a176", "a177", "a178", "a179", "a180", "a181", "a182", "a183", "a184", "a185", "a186", "a187", "a188", "a189", "a190", "a191", "a192", "a193", "a194", "a195", "a196", "a197", "a198", "a199", "a200", "a201", "a202", "a203", "a204", "a205", "a206", "a207", "a208", "a209", "a210", "a211", "a212", "a213", "a214", "a215", "a216", "a217", "a218", "a219", "a220", "a221", "a222", "a223", "a224", "a225", "a226", "a227", "a228", "a229", "a230", "a231", "a232", "a233", "a234", "a235", "a236", "a237", "a238", "a239", "a240", "a241", "a242", "a243", "a244", "a245", "a246", "a247", "a248", "a249", "a250", "a251", "a252", "a253", "a254", "a255", "s40", "s41", "s42", "s43", "s44", "s45", "s46", "s47", "s48", "s49", "s50", "s51", "s52", "s53", "s54", "s55", "s56", "s57", "s58", "s59", "s60", "s61", "s62", "s63", "s64", "s65", "s66", "s67", "s68", "s69", "s70", "s71", "s72", "s73", "s74", "s75", "s76", "s77", "s78", "s79", "s80", "s81", "s82", "s83", "s84", "s85", "s86", "s87", "s88", "s89", "s90", "s91", "s92", "s93", "s94", "s95", "s96", "s97", "s98", "s99", "vcc", "scc", "memory"
#define W_ASM_TEXT \
  "s_cmp_gt_u32 %36, 0\n" \
  "s_cbranch_scc1 .LW_other_%=\n" \
  "s_mov_b64 s[40:41], %32\n" \
  "s_and_b32 s41, s41, 0xffff\n" \
  "s_mov_b32 s42, 255852544\n" \
  "s_mov_b32 s43, 0x00020000\n" \
  "s_mov_b32 s46, 4718592\n" \
  "s_mov_b32 s47, 0x00020000\n" \
  "s_lshl_b32 s72, %36, 11\n" \
  "buffer_load_dword v196, off, s[40:43], %48 sc1\n" \
  "s_mov_b64 s[44:45], %34\n" \
  "s_and_b32 s45, s45, 0xffff\n" \
  "s_add_u32 s60, %38, 0\n" \
  "s_lshl_b32 s61, s72, 1\n" \
  "s_add_u32 m0, s61, 16384\n" \
  "s_nop 0\n" \
  "buffer_load_dwordx4 v204, s[44:47], s60 offen lds\n" \
  "buffer_load_dwordx4 v205, s[44:47], s60 offen offset:1024 lds\n" \
  "buffer_load_dwordx4 v206, s[44:47], s60 offen offset:2048 lds\n" \
  "buffer_load_dwordx4 v207, s[44:47], s60 offen offset:3072 lds\n" \
  "s_add_u32 s60, %38, 18432\n" \
  "s_lshl_b32 s61, s72, 1\n" \
  "s_add_u32 m0, s61, 32768\n" \
  "s_nop 0\n" \
  "buffer_load_dwordx4 v204, s[44:47], s60 offen lds\n" \
  "buffer_load_dwordx4 v205, s[44:47], s60 offen offset:1024 lds\n" \
  "buffer_load_dwordx4 v206, s[44:47], s60 offen offset:2048 lds\n" \
  "buffer_load_dwordx4 v207, s[44:47], s60 offen offset:3072 lds\n" \
  "s_add_u32 s60, %38, 256\n" \
  "s_lshl_b32 s61, s72, 1\n" \
  "s_add_u32 m0, s61, 49152\n" \
  "s_nop 0\n" \
  "buffer_load_dwordx4 v204, s[44:47], s60 offen lds\n" \
  "buffer_load_dwordx4 v205, s[44:47], s60 offen offset:1024 lds\n" \
  "buffer_load_dwordx4 v206, s[44:47], s60 offen offset:2048 lds\n" \
  "buffer_load_dwordx4 v207, s[44:47], s60 offen offset:3072 lds\n" \
  "s_add_u32 s60, %38, 18688\n" \
  "s_lshl_b32 s61, s72, 1\n" \
  "s_add_u32 m0, s61, 65536\n" \
  "s_nop 0\n" \
  "buffer_load_dwordx4 v204, s[44:47], s60 offen lds\n" \
  "buffer_load_dwordx4 v205, s[44:47], s60 offen offset:1024 lds\n" \
  "buffer_load_dwordx4 v206, s[44:47], s60 offen offset:2048 lds\n" \
  "buffer_load_dwordx4 v207, s[44:47], s60 offen offset:3072 lds\n" \
  "s_add_u32 s60, %38, 512\n" \
  "s_lshl_b32 s61, s72, 1\n" \
  "s_add_u32 m0, s61, 81920\n" \
  "s_nop 0\n" \
  "buffer_load_dwordx4 v204, s[44:47], s60 offen lds\n" \
  "buffer_load_dwordx4 v205, s[44:47], s60 offen offset:1024 lds\n" \
  "buffer_load_dwordx4 v206, s[44:47], s60 offen offset:2048 lds\n" \
  "buffer_load_dwordx4 v207, s[44:47], s60 offen offset:3072 lds\n" \
  "s_add_u32 s60, %38, 18944\n" \
  "s_lshl_b32 s61, s72, 1\n" \
  "s_add_u32 m0, s61, 98304\n" \
  "s_nop 0\n" \
  "buffer_load_dwordx4 v204, s[44:47], s60 offen lds\n" \
  "buffer_load_dwordx4 v205, s[44:47], s60 offen offset:1024 lds\n" \
  "buffer_load_dwordx4 v206, s[44:47], s60 offen offset:2048 lds\n" \
  "buffer_load_dwordx4 v207, s[44:47], s60 offen offset:3072 lds\n" \
  "s_add_u32 s60, %38, 768\n" \
  "s_lshl_b32 s61, s72, 1\n" \
  "s_add_u32 m0, s61, 114688\n" \
  "s_nop 0\n" \
  "buffer_load_dwordx4 v204, s[44:47], s60 offen lds\n" \
  "buffer_load_dwordx4 v205, s[44:47], s60 offen offset:1024 lds\n" \
  "buffer_load_dwordx4 v206, s[44:47], s60 offen offset:2048 lds\n" \
  "buffer_load_dwordx4 v207, s[44:47], s60 offen offset:3072 lds\n" \
  "s_add_u32 s60, %38, 19200\n" \
  "s_lshl_b32 s61, s72, 1\n" \
  "s_add_u32 m0, s61, 131072\n" \
  "s_nop 0\n" \
  "buffer_load_dwordx4 v204, s[44:47], s60 offen lds\n" \
  "buffer_load_dwordx4 v205, s[44:47], s60 offen offset:1024 lds\n" \
  "buffer_load_dwordx4 v206, s[44:47], s60 offen offset:2048 lds\n" \
  "buffer_load_dwordx4 v207, s[44:47], s60 offen offset:3072 lds\n" \
  "s_mov_b32 s60, %38\n" \
  "s_lshl_b32 s61, s72, 1\n" \
  "s_add_u32 m0, s61, 147456\n" \
  "s_nop 0\n" \
  "buffer_load_dwordx4 v208, s[44:47], s60 offen lds\n" \
  "buffer_load_dwordx4 v209, s[44:47], s60 offen offset:1024 lds\n" \
  "buffer_load_dwordx4 v210, s[44:47], s60 offen offset:2048 lds\n" \
  "buffer_load_dwordx4 v211, s[44:47], s60 offen offset:3072 lds\n" \
  "s_mov_b64 s[44:45], %35\n" \
  "s_and_b32 s45, s45, 0xffff\n" \
  "s_lshr_b32 s60, s72, 3\n" \
  "s_mov_b32 m0, s72\n" \
  "s_nop 0\n" \
  "buffer_load_dwordx4 v214, s[44:47], s60 offen lds\n" \
  "s_add_u32 s60, s60, 128\n" \
  "s_add_u32 m0, s72, 1024\n" \
  "s_nop 0\n" \
  "buffer_load_dwordx4 v214, s[44:47], s60 offen lds\n" \
  "s_movk_i32 s60, 1024\n" \
  "s_movk_i32 m0, 8192\n" \
  "s_nop 0\n" \
  "buffer_load_dwordx4 v214, s[44:47], s60 offen lds\n" \
  "s_waitcnt vmcnt(27)\n" \
  "s_barrier\n" \
  "v_add_u32 v194, 16384, v215\n" \
  "v_xor_b32 v195, 2176, v194\n" \
  "ds_read_b64_tr_b16 a[144:145], v194\n" \
  "ds_read_b64_tr_b16 a[146:147], v194 offset:512\n" \
  "ds_read_b64_tr_b16 a[148:149], v195\n" \
  "ds_read_b64_tr_b16 a[150:151], v195 offset:512\n" \
  "ds_read_b64_tr_b16 a[152:153], v194 offset:8192\n" \
  "ds_read_b64_tr_b16 a[154:155], v194 offset:8704\n" \
  "ds_read_b64_tr_b16 a[156:157], v195 offset:8192\n" \
  "ds_read_b64_tr_b16 a[158:159], v195 offset:8704\n" \
  "v_add_u32 v194, 32768, v215\n" \
  "v_xor_b32 v195, 2176, v194\n" \
  "ds_read_b64_tr_b16 a[160:161], v194\n" \
  "ds_read_b64_tr_b16 a[162:163], v194 offset:512\n" \
  "ds_read_b64_tr_b16 a[164:165], v195\n" \
  "ds_read_b64_tr_b16 a[166:167], v195 offset:512\n" \
  "ds_read_b64_tr_b16 a[168:169], v194 offset:8192\n" \
  "ds_read_b64_tr_b16 a[170:171], v194 offset:8704\n" \
  "ds_read_b64_tr_b16 a[172:173], v195 offset:8192\n" \
  "ds_read_b64_tr_b16 a[174:175], v195 offset:8704\n" \
  "v_add_u32 v194, 49152, v215\n" \
  "v_xor_b32 v195, 2176, v194\n" \
  "ds_read_b64_tr_b16 a[176:177], v194\n" \
  "ds_read_b64_tr_b16 a[178:179], v194 offset:512\n" \
  "ds_read_b64_tr_b16 a[180:181], v195\n" \
  "ds_read_b64_tr_b16 a[182:183], v195 offset:512\n" \
  "ds_read_b64_tr_b16 a[184:185], v194 offset:8192\n" \
  "ds_read_b64_tr_b16 a[186:187], v194 offset:8704\n" \
  "ds_read_b64_tr_b16 a[188:189], v195 offset:8192\n" \
  "ds_read_b64_tr_b16 a[190:191], v195 offset:8704\n" \
  "s_waitcnt lgkmcnt(0)\n" \
  "s_barrier\n" \
  "s_mov_b64 s[44:45], %33\n" \
  "s_and_b32 s45, s45, 0xffff\n" \
  "s_add_u32 s60, %37, 0\n" \
  "s_lshl_b32 s61, s72, 1\n" \
  "s_add_u32 m0, s61, 16384\n" \
  "s_nop 0\n" \
  "buffer_load_dwordx4 v200, s[44:47], s60 offen lds\n" \
  "buffer_load_dwordx4 v201, s[44:47], s60 offen offset:1024 lds\n" \
  "buffer_load_dwordx4 v202, s[44:47], s60 offen offset:2048 lds\n" \
  "buffer_load_dwordx4 v203, s[44:47], s60 offen offset:3072 lds\n" \
  "s_add_u32 s60, %37, 524288\n" \
  "s_lshl_b32 s61, s72, 1\n" \
  "s_add_u32 m0, s61, 32768\n" \
  "s_nop 0\n" \
  "buffer_load_dwordx4 v200, s[44:47], s60 offen lds\n" \
  "buffer_load_dwordx4 v201, s[44:47], s60 offen offset:1024 lds\n" \
  "buffer_load_dwordx4 v202, s[44:47], s60 offen offset:2048 lds\n" \
  "buffer_load_dwordx4 v203, s[44:47], s60 offen offset:3072 lds\n" \
  "s_add_u32 s60, %37, 1048576\n" \
  "s_lshl_b32 s61, s72, 1\n" \
  "s_add_u32 m0, s61, 49152\n" \
  "s_nop 0\n" \
  "buffer_load_dwordx4 v200, s[44:47], s60 offen lds\n" \
  "buffer_load_dwordx4 v201, s[44:47], s60 offen offset:1024 lds\n" \
  "buffer_load_dwordx4 v202, s[44:47], s60 offen offset:2048 lds\n" \
  "buffer_load_dwordx4 v203, s[44:47], s60 offen offset:3072 lds\n" \
  "v_readfirstlane_b32 s55, v196\n" \
  "s_add_u32 s55, s55, 1\n" \
  "s_waitcnt vmcnt(27)\n" \
  "s_barrier\n" \
  "v_add_u32 v194, 65536, v215\n" \
  "v_xor_b32 v195, 2176, v194\n" \
  "ds_read_b64_tr_b16 a[192:193], v194\n" \
  "ds_read_b64_tr_b16 a[194:195], v194 offset:512\n" \
  "ds_read_b64_tr_b16 a[196:197], v195\n" \
  "ds_read_b64_tr_b16 a[198:199], v195 offset:512\n" \
  "ds_read_b64_tr_b16 a[200:201], v194 offset:8192\n" \
  "ds_read_b64_tr_b16 a[202:203], v194 offset:8704\n" \
  "ds_read_b64_tr_b16 a[204:205], v195 offset:8192\n" \
  "ds_read_b64_tr_b16 a[206:207], v195 offset:8704\n" \
  "v_add_u32 v194, 81920, v215\n" \
  "v_xor_b32 v195, 2176, v194\n" \
  "ds_read_b64_tr_b16 a[208:209], v194\n" \
  "ds_read_b64_tr_b16 a[210:211], v194 offset:512\n" \
  "ds_read_b64_tr_b16 a[212:213], v195\n" \
  "ds_read_b64_tr_b16 a[214:215], v195 offset:512\n" \
  "ds_read_b64_tr_b16 a[216:217], v194 offset:8192\n" \
  "ds_read_b64_tr_b16 a[218:219], v194 offset:8704\n" \
  "ds_read_b64_tr_b16 a[220:221], v195 offset:8192\n" \
  "ds_read_b64_tr_b16 a[222:223], v195 offset:8704\n" \
  "v_add_u32 v194, 98304, v215\n" \
  "v_xor_b32 v195, 2176, v194\n" \
  "ds_read_b64_tr_b16 a[224:225], v194\n" \
  "ds_read_b64_tr_b16 a[226:227], v194 offset:512\n" \
  "ds_read_b64_tr_b16 a[228:229], v195\n" \
  "ds_read_b64_tr_b16 a[230:231], v195 offset:512\n" \
  "ds_read_b64_tr_b16 a[232:233], v194 offset:8192\n" \
  "ds_read_b64_tr_b16 a[234:235], v194 offset:8704\n" \
  "ds_read_b64_tr_b16 a[236:237], v195 offset:8192\n" \
  "ds_read_b64_tr_b16 a[238:239], v195 offset:8704\n" \
  "s_waitcnt lgkmcnt(0)\n" \
  "s_barrier\n" \
  "s_add_u32 s60, %37, 1572864\n" \
  "s_lshl_b32 s61, s72, 1\n" \
  "s_add_u32 m0, s61, 65536\n" \
  "s_nop 0\n" \
  "buffer_load_dwordx4 v200, s[44:47], s60 offen lds\n" \
  "buffer_load_dwordx4 v201, s[44:47], s60 offen offset:1024 lds\n" \
  "buffer_load_dwordx4 v202, s[44:47], s60 offen offset:2048 lds\n" \
  "buffer_load_dwordx4 v203, s[44:47], s60 offen offset:3072 lds\n" \
  "s_add_u32 s60, %37, 2097152\n" \
  "s_lshl_b32 s61, s72, 1\n" \
  "s_add_u32 m0, s61, 81920\n" \
  "s_nop 0\n" \
  "buffer_load_dwordx4 v200, s[44:47], s60 offen lds\n" \
  "buffer_load_dwordx4 v201, s[44:47], s60 offen offset:1024 lds\n" \
  "buffer_load_dwordx4 v202, s[44:47], s60 offen offset:2048 lds\n" \
  "buffer_load_dwordx4 v203, s[44:47], s60 offen offset:3072 lds\n" \
  "s_add_u32 s60, %37, 2621440\n" \
  "s_lshl_b32 s61, s72, 1\n" \
  "s_add_u32 m0, s61, 98304\n" \
  "s_nop 0\n" \
  "buffer_load_dwordx4 v200, s[44:47], s60 offen lds\n" \
  "buffer_load_dwordx4 v201, s[44:47], s60 offen offset:1024 lds\n" \
  "buffer_load_dwordx4 v202, s[44:47], s60 offen offset:2048 lds\n" \
  "buffer_load_dwordx4 v203, s[44:47], s60 offen offset:3072 lds\n" \
  "s_waitcnt vmcnt(27)\n" \
  "s_barrier\n" \
  "s_waitcnt lgkmcnt(0)\n" \
  "s_barrier\n" \
  "s_add_u32 s60, %37, 3145728\n" \
  "s_lshl_b32 s61, s72, 1\n" \
  "s_add_u32 m0, s61, 114688\n" \
  "s_nop 0\n" \
  "buffer_load_dwordx4 v200, s[44:47], s60 offen lds\n" \
  "buffer_load_dwordx4 v201, s[44:47], s60 offen offset:1024 lds\n" \
  "buffer_load_dwordx4 v202, s[44:47], s60 offen offset:2048 lds\n" \
  "buffer_load_dwordx4 v203, s[44:47], s60 offen offset:3072 lds\n" \
  "s_add_u32 s60, %37, 3670016\n" \
  "s_lshl_b32 s61, s72, 1\n" \
  "s_add_u32 m0, s61, 131072\n" \
  "s_nop 0\n" \
  "buffer_load_dwordx4 v200, s[44:47], s60 offen lds\n" \
  "buffer_load_dwordx4 v201, s[44:47], s60 offen offset:1024 lds\n" \
  "buffer_load_dwordx4 v202, s[44:47], s60 offen offset:2048 lds\n" \
  "buffer_load_dwordx4 v203, s[44:47], s60 offen offset:3072 lds\n" \
  "s_add_u32 s60, %37, 4194304\n" \
  "s_lshl_b32 s61, s72, 1\n" \
  "s_add_u32 m0, s61, 147456\n" \
  "s_nop 0\n" \
  "buffer_load_dwordx4 v200, s[44:47], s60 offen lds\n" \
  "buffer_load_dwordx4 v201, s[44:47], s60 offen offset:1024 lds\n" \
  "buffer_load_dwordx4 v202, s[44:47], s60 offen offset:2048 lds\n" \
  "buffer_load_dwordx4 v203, s[44:47], s60 offen offset:3072 lds\n" \
  "buffer_load_dwordx2 v[198:199], off, s[40:43], %47 sc1\n" \
  "s_waitcnt vmcnt(25)\n" \
  "s_barrier\n" \
  "v_add_u32 v194, 16384, v212\n" \
  "ds_read_b64_tr_b16 a[0:1], v194\n" \
  "ds_read_b64_tr_b16 a[2:3], v194 offset:512\n" \
  "ds_read_b64_tr_b16 a[4:5], v194 offset:8192\n" \
  "ds_read_b64_tr_b16 a[6:7], v194 offset:8704\n" \
  "v_add_u32 v195, 16384, v213\n" \
  "ds_read_b64_tr_b16 a[72:73], v195\n" \
  "ds_read_b64_tr_b16 a[74:75], v195 offset:512\n" \
  "ds_read_b64_tr_b16 a[76:77], v195 offset:8192\n" \
  "ds_read_b64_tr_b16 a[78:79], v195 offset:8704\n" \
  "v_add_u32 v194, 32768, v212\n" \
  "ds_read_b64_tr_b16 a[8:9], v194\n" \
  "ds_read_b64_tr_b16 a[10:11], v194 offset:512\n" \
  "ds_read_b64_tr_b16 a[12:13], v194 offset:8192\n" \
  "ds_read_b64_tr_b16 a[14:15], v194 offset:8704\n" \
  "v_add_u32 v195, 32768, v213\n" \
  "ds_read_b64_tr_b16 a[80:81], v195\n" \
  "ds_read_b64_tr_b16 a[82:83], v195 offset:512\n" \
  "ds_read_b64_tr_b16 a[84:85], v195 offset:8192\n" \
  "ds_read_b64_tr_b16 a[86:87], v195 offset:8704\n" \
  "v_add_u32 v194, 49152, v212\n" \
  "ds_read_b64_tr_b16 a[16:17], v194\n" \
  "ds_read_b64_tr_b16 a[18:19], v194 offset:512\n" \
  "ds_read_b64_tr_b16 a[20:21], v194 offset:8192\n" \
  "ds_read_b64_tr_b16 a[22:23], v194 offset:8704\n" \
  "v_add_u32 v195, 49152, v213\n" \
  "ds_read_b64_tr_b16 a[88:89], v195\n" \
  "ds_read_b64_tr_b16 a[90:91], v195 offset:512\n" \
  "ds_read_b64_tr_b16 a[92:93], v195 offset:8192\n" \
  "ds_read_b64_tr_b16 a[94:95], v195 offset:8704\n" \
  "s_waitcnt lgkmcnt(0)\n" \
  "s_waitcnt vmcnt(13)\n" \
  "s_barrier\n" \
  "v_add_u32 v194, 65536, v212\n" \
  "ds_read_b64_tr_b16 a[24:25], v194\n" \
  "ds_read_b64_tr_b16 a[26:27], v194 offset:512\n" \
  "ds_read_b64_tr_b16 a[28:29], v194 offset:8192\n" \
  "ds_read_b64_tr_b16 a[30:31], v194 offset:8704\n" \
  "v_add_u32 v195, 65536, v213\n" \
  "ds_read_b64_tr_b16 a[96:97], v195\n" \
  "ds_read_b64_tr_b16 a[98:99], v195 offset:512\n" \
  "ds_read_b64_tr_b16 a[100:101], v195 offset:8192\n" \
  "ds_read_b64_tr_b16 a[102:103], v195 offset:8704\n" \
  "v_add_u32 v194, 81920, v212\n" \
  "ds_read_b64_tr_b16 a[32:33], v194\n" \
  "ds_read_b64_tr_b16 a[34:35], v194 offset:512\n" \
  "ds_read_b64_tr_b16 a[36:37], v194 offset:8192\n" \
  "ds_read_b64_tr_b16 a[38:39], v194 offset:8704\n" \
  "v_add_u32 v195, 81920, v213\n" \
  "ds_read_b64_tr_b16 a[104:105], v195\n" \
  "ds_read_b64_tr_b16 a[106:107], v195 offset:512\n" \
  "ds_read_b64_tr_b16 a[108:109], v195 offset:8192\n" \
  "ds_read_b64_tr_b16 a[110:111], v195 offset:8704\n" \
  "v_add_u32 v194, 98304, v212\n" \
  "ds_read_b64_tr_b16 a[40:41], v194\n" \
  "ds_read_b64_tr_b16 a[42:43], v194 offset:512\n" \
  "ds_read_b64_tr_b16 a[44:45], v194 offset:8192\n" \
  "ds_read_b64_tr_b16 a[46:47], v194 offset:8704\n" \
  "v_add_u32 v195, 98304, v213\n" \
  "ds_read_b64_tr_b16 a[112:113], v195\n" \
  "ds_read_b64_tr_b16 a[114:115], v195 offset:512\n" \
  "ds_read_b64_tr_b16 a[116:117], v195 offset:8192\n" \
  "ds_read_b64_tr_b16 a[118:119], v195 offset:8704\n" \
  "s_waitcnt lgkmcnt(0)\n" \
  "s_waitcnt vmcnt(1)\n" \
  "s_barrier\n" \
  "v_add_u32 v194, 114688, v212\n" \
  "ds_read_b64_tr_b16 a[48:49], v194\n" \
  "ds_read_b64_tr_b16 a[50:51], v194 offset:512\n" \
  "ds_read_b64_tr_b16 a[52:53], v194 offset:8192\n" \
  "ds_read_b64_tr_b16 a[54:55], v194 offset:8704\n" \
  "v_add_u32 v195, 114688, v213\n" \
  "ds_read_b64_tr_b16 a[120:121], v195\n" \
  "ds_read_b64_tr_b16 a[122:123], v195 offset:512\n" \
  "ds_read_b64_tr_b16 a[124:125], v195 offset:8192\n" \
  "ds_read_b64_tr_b16 a[126:127], v195 offset:8704\n" \
  "v_add_u32 v194, 131072, v212\n" \
  "ds_read_b64_tr_b16 a[56:57], v194\n" \
  "ds_read_b64_tr_b16 a[58:59], v194 offset:512\n" \
  "ds_read_b64_tr_b16 a[60:61], v194 offset:8192\n" \
  "ds_read_b64_tr_b16 a[62:63], v194 offset:8704\n" \
  "v_add_u32 v195, 131072, v213\n" \
  "ds_read_b64_tr_b16 a[128:129], v195\n" \
  "ds_read_b64_tr_b16 a[130:131], v195 offset:512\n" \
  "ds_read_b64_tr_b16 a[132:133], v195 offset:8192\n" \
  "ds_read_b64_tr_b16 a[134:135], v195 offset:8704\n" \
  "v_add_u32 v194, 147456, v212\n" \
  "ds_read_b64_tr_b16 a[64:65], v194\n" \
  "ds_read_b64_tr_b16 a[66:67], v194 offset:512\n" \
  "ds_read_b64_tr_b16 a[68:69], v194 offset:8192\n" \
  "ds_read_b64_tr_b16 a[70:71], v194 offset:8704\n" \
  "v_add_u32 v195, 147456, v213\n" \
  "ds_read_b64_tr_b16 a[136:137], v195\n" \
  "ds_read_b64_tr_b16 a[138:139], v195 offset:512\n" \
  "ds_read_b64_tr_b16 a[140:141], v195 offset:8192\n" \
  "ds_read_b64_tr_b16 a[142:143], v195 offset:8704\n" \
  "s_waitcnt lgkmcnt(0)\n" \
  "s_waitcnt vmcnt(0)\n" \
  "v_readfirstlane_b32 s96, v198\n" \
  "v_readfirstlane_b32 s63, v199\n" \
  "s_cmp_eq_u32 s63, s55\n" \
  "s_cselect_b32 s96, s96, 0\n" \
  "s_cmp_ge_u32 s96, 0xc0000000\n" \
  "s_cbranch_scc1 .LW_go_x_%=\n" \
  "s_mov_b32 s69, 0\n" \
  ".LW_go_%=:\n" \
  "buffer_load_dwordx2 v[196:197], off, s[40:43], %47 sc1\n" \
  "s_waitcnt vmcnt(0)\n" \
  "v_readfirstlane_b32 s96, v196\n" \
  "v_readfirstlane_b32 s63, v197\n" \
  "s_cmp_eq_u32 s63, s55\n" \
  "s_cselect_b32 s96, s96, 0\n" \
  "s_cmp_ge_u32 s96, 0xc0000000\n" \
  "s_cbranch_scc1 .LW_go_x_%=\n" \
  "s_sleep 8\n" \
  "s_add_u32 s69, s69, 1\n" \
  "s_and_b32 s63, s69, 1023\n" \
  "s_cbranch_scc1 .LW_go_%=\n" \
  "s_cmp_gt_u32 s69, 2097152\n" \
  "s_cbranch_scc1 .LW_go_set_%=\n" \
  "buffer_load_dwordx2 v[196:197], off, s[40:43], %48 offset:128 sc1\n" \
  "s_waitcnt vmcnt(0)\n" \
  "v_readfirstlane_b32 s63, v197\n" \
  "s_cmp_eq_u32 s63, s55\n" \
  "s_cbranch_scc0 .LW_go_%=\n" \
  "s_branch .LW_go_x_%=\n" \
  ".LW_go_set_%=:\n" \
  "v_mov_b32 v196, 1\n" \
  "v_mov_b32 v197, s55\n" \
  "buffer_store_dwordx2 v[196:197], off, s[40:43], %48 offset:128 sc1\n" \
  "s_waitcnt vmcnt(0)\n" \
  ".LW_go_x_%=:\n" \
  "s_lshr_b32 s60, s96, 0\n" \
  "s_and_b32 s60, s60, 7\n" \
  "s_lshl_b32 s60, s60, 12\n" \
  "s_xor_b32 s74, %43, s60\n" \
  "s_xor_b32 s75, %46, s60\n" \
  "s_bfe_u32 s56, s96, 0x30010\n" \
  "s_lshl_b32 s56, s56, 12\n" \
  "s_or_b32 s56, s56, 0x200000\n" \
  "s_mov_b32 s57, 1052672\n" \
  "s_lshl_b32 s61, %42, 2\n" \
  "s_lshr_b32 s51, s96, s61\n" \
  "s_and_b32 s51, s51, 7\n" \
  "s_lshl_b32 s51, s51, 12\n" \
  "s_xor_b32 s51, s51, 4210688\n" \
  "s_mov_b32 s71, 0\n" \
  "v_mov_b64 v[48:49], -1\n" \
  "v_mov_b64 v[50:51], -1\n" \
  "v_and_b32 v194, 3, v251\n" \
  "v_lshlrev_b32 v194, 2, v194\n" \
  "v_lshrrev_b32 v194, v194, s96\n" \
  "v_and_b32 v194, 7, v194\n" \
  "v_lshlrev_b32 v194, 12, v194\n" \
  "v_and_b32 v251, -4, v251\n" \
  "v_xor_b32 v251, v251, v194\n" \
  "v_and_b32 v194, 3, v252\n" \
  "v_lshlrev_b32 v194, 2, v194\n" \
  "v_lshrrev_b32 v194, v194, s96\n" \
  "v_and_b32 v194, 7, v194\n" \
  "v_lshlrev_b32 v194, 12, v194\n" \
  "v_and_b32 v252, -4, v252\n" \
  "v_xor_b32 v252, v252, v194\n" \
  "v_and_b32 v194, 3, v253\n" \
  "v_lshlrev_b32 v194, 2, v194\n" \
  "v_lshrrev_b32 v194, v194, s96\n" \
  "v_and_b32 v194, 7, v194\n" \
  "v_lshlrev_b32 v194, 12, v194\n" \
  "v_and_b32 v253, -4, v253\n" \
  "v_xor_b32 v253, v253, v194\n" \
  "v_xor_b32 v185, s56, v251\n" \
  "buffer_store_dwordx2 v[48:49], v185, s[40:43], 0 offen sc1\n" \
  "v_xor_b32 v186, s56, v252\n" \
  "buffer_store_dwordx2 v[48:49], v186, s[40:43], 0 offen sc1\n" \
  "v_xor_b32 v187, s56, v253\n" \
  "buffer_store_dwordx2 v[48:49], v187, s[40:43], 0 offen sc1\n" \
  "s_xor_b32 s62, s56, s57\n" \
  "s_nop 1\n" \
  "v_xor_b32 v185, s62, v251\n" \
  "buffer_store_dwordx2 v[48:49], v185, s[40:43], 0 offen sc1\n" \
  "v_xor_b32 v186, s62, v252\n" \
  "buffer_store_dwordx2 v[48:49], v186, s[40:43], 0 offen sc1\n" \
  "v_xor_b32 v187, s62, v253\n" \
  "buffer_store_dwordx2 v[48:49], v187, s[40:43], 0 offen sc1\n" \
  "s_andn2_b32 s60, 2, s96\n" \
  "s_lshl_b32 s60, s60, 12\n" \
  "s_xor_b32 s60, s60, 8192\n" \
  "s_xor_b32 s58, %39, s60\n" \
  "s_mov_b32 s49, 0\n" \
  "s_add_u32 s47, %53, s60\n" \
  "s_add_u32 s97, s60, 8388608\n" \
  "s_xor_b32 s50, %40, s60\n" \
  "s_mov_b32 s48, 0\n" \
  "s_mov_b32 s54, 0\n" \
  "s_cmp_eq_u32 %36, 0\n" \
  "s_cselect_b64 s[88:89], -1, 0\n" \
  "s_mov_b32 s68, 0\n" \
  "s_mov_b32 s70, 1\n" \
  "s_mov_b32 s73, 0\n" \
  "s_mov_b32 s44, 0\n" \
  "s_mov_b32 s53, 0\n" \
  "s_memrealtime s[86:87]\n" \
  "s_memtime s[76:77]\n" \
  "s_waitcnt lgkmcnt(0)\n" \
  "s_mov_b32 s90, s76\n" \
  "s_mov_b32 s87, 340\n" \
  "s_mov_b32 s64, 0x3e0293ee\n" \
  "s_mov_b32 s65, 0xbe0293ee\n" \
  "v_mov_b64 v[56:57], 0\n" \
  "v_mov_b64 v[58:59], 0\n" \
  "v_mov_b64 v[60:61], 0\n" \
  "v_mov_b64 v[62:63], 0\n" \
  "v_mov_b64 v[64:65], 0\n" \
  "v_mov_b64 v[66:67], 0\n" \
  "v_mov_b64 v[68:69], 0\n" \
  "v_mov_b64 v[70:71], 0\n" \
  "v_mov_b64 v[72:73], 0\n" \
  "v_mov_b64 v[74:75], 0\n" \
  "v_mov_b32 v52, 0\n" \
  "v_mov_b32 v53, s55\n" \
  "v_mov_b64 v[54:55], v[52:53]\n" \
  "v_mov_b64 v[78:79], 0\n" \
  "s_mov_b32 s67, 2130706432\n" \
  "s_waitcnt vmcnt(0) lgkmcnt(0)\n" \
  "s_barrier\n" \
  "s_branch .LW_p_%=\n" \
  "s_nop 7\n" \
  ".p2align 6\n" \
  ".fill (((16 - (.LW_p_%= - .LW_rot_%=)) & 63) >> 2), 4, 0xBF800000\n" \
  ".LW_rot_%=:\n" \
  "s_xor_b32 s51, s51, s56\n" \
  "s_xor_b32 s71, s71, s56\n" \
  "v_xor_b32 v251, s56, v251\n" \
  "v_xor_b32 v252, s56, v252\n" \
  "v_xor_b32 v253, s56, v253\n" \
  "s_mov_b32 s60, s56\n" \
  "s_mov_b32 s56, s57\n" \
  "s_mov_b32 s57, s60\n" \
  "s_add_u32 s59, s51, 819200\n" \
  "s_mov_b32 m0, 0\n" \
  "s_mov_b32 s70, 0\n" \
  "s_mov_b32 s68, 0\n" \
  "s_waitcnt lgkmcnt(0)\n" \
  "s_mov_b32 s69, 0\n" \
  ".LW_tw_%=:\n" \
  "v_readfirstlane_b32 s63, v80\n" \
  "s_cmp_lg_u32 s63, s48\n" \
  "s_cbranch_scc1 .LW_tw_re_%=\n" \
  "v_readfirstlane_b32 s53, v82\n" \
  ".LW_rt_%=:\n" \
  "s_memrealtime s[60:61]\n" \
  "s_add_u32 s73, s53, s44\n" \
  "s_waitcnt lgkmcnt(0)\n" \
  "s_sub_u32 s62, s73, s60\n" \
  "s_cmp_ge_u32 s62, 2000\n" \
  "s_cbranch_scc1 .LW_lat_%=\n" \
  "s_sub_u32 s62, s62, 30\n" \
  ".LW_slp_%=:\n" \
  "s_cmp_le_i32 s62, 0\n" \
  "s_cbranch_scc1 .LW_clk_%=\n" \
  "s_sleep 1\n" \
  "s_sub_u32 s62, s62, 3\n" \
  "s_branch .LW_slp_%=\n" \
  ".LW_clk_%=:\n" \
  "s_memrealtime s[60:61]\n" \
  "s_waitcnt lgkmcnt(0)\n" \
  "s_sub_u32 s62, s73, s60\n" \
  "s_cmp_gt_i32 s62, 21\n" \
  "s_cbranch_scc1 .LW_clk_%=\n" \
  "s_cmp_le_i32 s62, 11\n" \
  "s_cbranch_scc1 .LW_clk2_%=\n" \
  "s_memrealtime s[76:77]\n" \
  "s_nop 4\n" \
  "s_memrealtime s[78:79]\n" \
  "s_nop 4\n" \
  "s_memrealtime s[80:81]\n" \
  "s_nop 4\n" \
  "s_memrealtime s[82:83]\n" \
  "s_waitcnt lgkmcnt(0)\n" \
  "s_mov_b32 s77, 1\n" \
  "s_cmp_eq_u32 s78, s76\n" \
  "s_addc_u32 s77, s77, 0\n" \
  "s_cmp_eq_u32 s80, s76\n" \
  "s_addc_u32 s77, s77, 0\n" \
  "s_cmp_eq_u32 s82, s76\n" \
  "s_addc_u32 s77, s77, 0\n" \
  "s_mul_i32 s77, s77, 24\n" \
  "s_lshr_b32 s78, s87, 2\n" \
  "s_sub_u32 s77, s78, s77\n" \
  "s_max_i32 s77, s77, 0\n" \
  "s_sub_u32 s76, s73, s76\n" \
  "s_mul_i32 s76, s76, s87\n" \
  "s_ashr_i32 s76, s76, 4\n" \
  "s_sub_u32 s76, s76, s77\n" \
  "s_sub_u32 s76, s76, 200\n" \
  "s_max_i32 s76, s76, 0\n" \
  "s_min_i32 s76, s76, 320\n" \
  "s_lshr_b32 s76, s76, 3\n" \
  "s_lshl_b32 s76, s76, 2\n" \
  "s_mov_b32 s68, 1\n" \
  "s_getpc_b64 s[78:79]\n" \
  "s_sub_u32 s76, 180, s76\n" \
  "s_add_u32 s78, s78, s76\n" \
  "s_addc_u32 s79, s79, 0\n" \
  "s_setpc_b64 s[78:79]\n" \
  "s_nop 1\n" \
  "s_nop 1\n" \
  "s_nop 1\n" \
  "s_nop 1\n" \
  "s_nop 1\n" \
  "s_nop 1\n" \
  "s_nop 1\n" \
  "s_nop 1\n" \
  "s_nop 1\n" \
  "s_nop 1\n" \
  "s_nop 1\n" \
  "s_nop 1\n" \
  "s_nop 1\n" \
  "s_nop 1\n" \
  "s_nop 1\n" \
  "s_nop 1\n" \
  "s_nop 1\n" \
  "s_nop 1\n" \
  "s_nop 1\n" \
  "s_nop 1\n" \
  "s_nop 1\n" \
  "s_nop 1\n" \
  "s_nop 1\n" \
  "s_nop 1\n" \
  "s_nop 1\n" \
  "s_nop 1\n" \
  "s_nop 1\n" \
  "s_nop 1\n" \
  "s_nop 1\n" \
  "s_nop 1\n" \
  "s_nop 1\n" \
  "s_nop 1\n" \
  "s_nop 1\n" \
  "s_nop 1\n" \
  "s_nop 1\n" \
  "s_nop 1\n" \
  "s_nop 1\n" \
  "s_nop 1\n" \
  "s_nop 1\n" \
  "s_nop 1\n" \
  ".LW_noclk_%=:\n" \
  "buffer_load_dwordx4 v243, s[40:43], s59 offen sc1 lds\n" \
  "buffer_load_dwordx4 v243, s[40:43], s59 offen offset:1024 sc1 lds\n" \
  "buffer_load_dwordx4 v243, s[40:43], s59 offen offset:2048 sc1 lds\n" \
  "buffer_load_dwordx4 v243, s[40:43], s59 offen offset:3072 sc1 lds\n" \
  "s_add_u32 s60, s59, 32768\n" \
  "s_movk_i32 m0, 4096\n" \
  "s_nop 0\n" \
  "buffer_load_dwordx4 v243, s[40:43], s60 offen sc1 lds\n" \
  "buffer_load_dwordx4 v243, s[40:43], s60 offen offset:1024 sc1 lds\n" \
  "buffer_load_dwordx4 v243, s[40:43], s60 offen offset:2048 sc1 lds\n" \
  "buffer_load_dwordx4 v243, s[40:43], s60 offen offset:3072 sc1 lds\n" \
  "s_add_u32 s60, s59, 65536\n" \
  "s_movk_i32 m0, 8192\n" \
  "s_nop 0\n" \
  "buffer_load_dwordx4 v243, s[40:43], s60 offen sc1 lds\n" \
  ".LW_g3_%=:\n" \
  "s_waitcnt vmcnt(4)\n" \
  "s_barrier\n" \
  ".LW_p_%=:\n" \
  "ds_read_b128 v[80:83], v243\n" \
  "ds_read_b128 v[84:87], v243 offset:1024\n" \
  "ds_read_b128 v[88:91], v243 offset:2048\n" \
  "ds_read_b128 v[92:95], v243 offset:3072\n" \
  "ds_read_b128 v[96:99], v243 offset:4096\n" \
  "s_waitcnt lgkmcnt(4)\n" \
  "v_smfmac_f32_16x16x64_bf16 v[56:59], v[80:83], a[0:7], v242\n" \
  "s_waitcnt vmcnt(0)\n" \
  "s_barrier\n" \
  "ds_read_b128 v[100:103], v243 offset:5120\n" \
  "ds_read_b128 v[104:107], v243 offset:6144\n" \
  "s_waitcnt lgkmcnt(5)\n" \
  "v_smfmac_f32_16x16x64_bf16 v[56:59], v[84:87], a[8:15], v242\n" \
  "ds_read_b128 v[108:111], v243 offset:7168\n" \
  "s_waitcnt lgkmcnt(5)\n" \
  "v_smfmac_f32_16x16x64_bf16 v[56:59], v[88:91], a[16:23], v242\n" \
  "ds_read_b128 v[112:115], v243 offset:8192\n" \
  "s_waitcnt lgkmcnt(5)\n" \
  "v_smfmac_f32_16x16x64_bf16 v[56:59], v[92:95], a[24:31], v242\n" \
  "s_waitcnt lgkmcnt(4)\n" \
  "v_smfmac_f32_16x16x64_bf16 v[56:59], v[96:99], a[32:39], v242\n" \
  "s_waitcnt lgkmcnt(3)\n" \
  "v_smfmac_f32_16x16x64_bf16 v[56:59], v[100:103], a[40:47], v242\n" \
  "s_waitcnt lgkmcnt(2)\n" \
  "v_smfmac_f32_16x16x64_bf16 v[56:59], v[104:107], a[48:55], v242\n" \
  "s_waitcnt lgkmcnt(1)\n" \
  "v_smfmac_f32_16x16x64_bf16 v[56:59], v[108:111], a[56:63], v242\n" \
  "s_waitcnt lgkmcnt(0)\n" \
  "v_smfmac_f32_16x16x64_bf16 v[56:59], v[112:115], a[64:71], v242\n" \
  "s_nop 2\n" \
  "v_smfmac_f32_16x16x64_bf16 v[60:63], v[80:83], a[72:79], v242\n" \
  "s_nop 2\n" \
  "v_smfmac_f32_16x16x64_bf16 v[60:63], v[84:87], a[80:87], v242\n" \
  "v_add_f32 v56, v56, v58\n" \
  "v_add_f32 v57, v57, v59\n" \
  "v_cmp_u_f32 vcc, v56, v57\n" \
  "v_cvt_pk_bf16_f32 v76, v56, v57\n" \
  "v_smfmac_f32_16x16x64_bf16 v[60:63], v[88:91], a[88:95], v242\n" \
  "s_cbranch_vccnz .LW_slow_%=\n" \
  ".LW_back_%=:\n" \
  "ds_write_b32 v244, v76\n" \
  "v_smfmac_f32_16x16x64_bf16 v[60:63], v[92:95], a[96:103], v242\n" \
  "v_smfmac_f32_16x16x64_bf16 v[60:63], v[96:99], a[104:111], v242\n" \
  "v_smfmac_f32_16x16x64_bf16 v[60:63], v[100:103], a[112:119], v242\n" \
  "s_waitcnt lgkmcnt(0)\n" \
  "s_barrier\n" \
  "ds_read_b64_tr_b16 v[80:81], v245\n" \
  "ds_read_b64_tr_b16 v[82:83], v245 offset:512\n" \
  "v_smfmac_f32_16x16x64_bf16 v[60:63], v[104:107], a[120:127], v242\n" \
  "ds_read_b64_tr_b16 v[84:85], v245 offset:1024\n" \
  "ds_read_b64_tr_b16 v[86:87], v245 offset:1536\n" \
  "v_smfmac_f32_16x16x64_bf16 v[60:63], v[108:111], a[128:135], v242\n" \
  "v_smfmac_f32_16x16x64_bf16 v[60:63], v[112:115], a[136:143], v242\n" \
  "s_nop 1\n" \
  "s_waitcnt lgkmcnt(2)\n" \
  "v_smfmac_f32_16x16x64_bf16 v[64:67], v[80:83], a[144:151], v248\n" \
  "s_nop 3\n" \
  "v_add_f32 v60, v60, v62\n" \
  "v_add_f32 v61, v61, v63\n" \
  "v_cvt_pk_bf16_f32 v77, v60, v61\n" \
  "s_waitcnt lgkmcnt(0)\n" \
  "v_smfmac_f32_16x16x64_bf16 v[64:67], v[84:87], a[152:159], v248\n" \
  "ds_write_b32 v244, v77 offset:256\n" \
  "v_smfmac_f32_16x16x64_bf16 v[68:71], v[80:83], a[176:183], v248\n" \
  "v_smfmac_f32_16x16x64_bf16 v[68:71], v[84:87], a[184:191], v248\n" \
  "v_smfmac_f32_16x16x64_bf16 v[72:75], v[80:83], a[208:215], v248\n" \
  "v_smfmac_f32_16x16x64_bf16 v[72:75], v[84:87], a[216:223], v248\n" \
  "s_waitcnt lgkmcnt(0)\n" \
  "s_barrier\n" \
  "ds_read_b64_tr_b16 v[80:81], v245 offset:256\n" \
  "ds_read_b64_tr_b16 v[82:83], v245 offset:768\n" \
  "ds_read_b64_tr_b16 v[84:85], v245 offset:1280\n" \
  "ds_read_b64_tr_b16 v[86:87], v245 offset:1792\n" \
  "s_waitcnt lgkmcnt(2)\n" \
  "v_smfmac_f32_16x16x64_bf16 v[64:67], v[80:83], a[160:167], v248\n" \
  "s_waitcnt lgkmcnt(0)\n" \
  "v_smfmac_f32_16x16x64_bf16 v[64:67], v[84:87], a[168:175], v248\n" \
  "s_nop 2\n" \
  "v_smfmac_f32_16x16x64_bf16 v[68:71], v[80:83], a[192:199], v248\n" \
  "s_nop 2\n" \
  "v_smfmac_f32_16x16x64_bf16 v[68:71], v[84:87], a[200:207], v248\n" \
  "s_nop 1\n" \
  "v_cvt_pk_bf16_f32 v64, v64, v65\n" \
  "v_smfmac_f32_16x16x64_bf16 v[72:75], v[80:83], a[224:231], v248\n" \
  "s_nop 0\n" \
  "v_cvt_pk_bf16_f32 v65, v66, v67\n" \
  "v_smfmac_f32_16x16x64_bf16 v[72:75], v[84:87], a[232:239], v248\n" \
  "buffer_store_dwordx2 v[64:65], v251, s[40:43], 0 offen sc1\n" \
  "v_cvt_pk_bf16_f32 v68, v68, v69\n" \
  "v_cvt_pk_bf16_f32 v69, v70, v71\n" \
  "buffer_store_dwordx2 v[68:69], v252, s[40:43], 0 offen sc1\n" \
  "s_nop 3\n" \
  "v_cvt_pk_bf16_f32 v72, v72, v73\n" \
  "v_cvt_pk_bf16_f32 v73, v74, v75\n" \
  "buffer_store_dwordx2 v[72:73], v253, s[40:43], 0 offen sc1\n" \
  ".LW_loop_%=:\n" \
  "s_memrealtime s[98:99]\n" \
  "s_memtime s[84:85]\n" \
  "v_mov_b64 v[56:57], 0\n" \
  "v_mov_b64 v[58:59], 0\n" \
  "v_mov_b64 v[60:61], 0\n" \
  "v_mov_b64 v[62:63], 0\n" \
  "v_mov_b64 v[64:65], 0\n" \
  "v_mov_b64 v[66:67], 0\n" \
  "v_mov_b64 v[68:69], 0\n" \
  "v_mov_b64 v[70:71], 0\n" \
  "v_mov_b64 v[72:73], 0\n" \
  "v_mov_b64 v[74:75], 0\n" \
  "s_add_u32 s95, s51, 885760\n" \
  "buffer_load_dwordx4 v[80:83], off, s[40:43], s95 sc1\n" \
  "s_add_u32 s48, s48, 1\n" \
  "v_mov_b32 v52, s48\n" \
  "s_waitcnt lgkmcnt(0)\n" \
  "s_cmp_gt_u32 s48, 59\n" \
  "s_cbranch_scc1 .LW_ef_%=\n" \
  ".LW_ef_b_%=:\n" \
  "s_sub_u32 s76, s84, s90\n" \
  "s_sub_u32 s77, s98, s86\n" \
  "v_cvt_f32_u32 v216, s76\n" \
  "v_cvt_f32_u32 v217, s77\n" \
  "v_rcp_f32 v217, v217\n" \
  "v_mul_f32 v216, 16.0, v216\n" \
  "v_fma_f32 v216, v216, v217, 0.5\n" \
  "v_cvt_u32_f32 v216, v216\n" \
  "s_nop 0\n" \
  "v_readfirstlane_b32 s76, v216\n" \
  "s_max_u32 s76, s76, 160\n" \
  "s_min_u32 s76, s76, 400\n" \
  "s_add_u32 s77, s48, 3\n" \
  "s_and_b32 s77, s77, 3\n" \
  "s_cselect_b32 s87, s87, s76\n" \
  "s_cselect_b32 s86, s86, s98\n" \
  "s_cselect_b32 s90, s90, s84\n" \
  "s_waitcnt vmcnt(0)\n" \
  "s_branch .LW_rot_%=\n" \
  "s_nop 7\n" \
  ".LW_slow_%=:\n" \
  "s_bitcmp1_b32 s70, 0\n" \
  "s_cbranch_scc1 .LW_back_%=\n" \
  "s_waitcnt lgkmcnt(0)\n" \
  "s_cmp_lt_u32 s70, 10\n" \
  "s_nop 3\n" \
  "s_cbranch_scc1 .LW_nol_%=\n" \
  "v_max_u32 v217, v80, v81\n" \
  "v_max3_u32 v217, v217, v82, v83\n" \
  "v_max3_u32 v217, v217, v84, v85\n" \
  "v_max3_u32 v217, v217, v86, v87\n" \
  "v_max3_u32 v217, v217, v88, v89\n" \
  "v_max3_u32 v217, v217, v90, v91\n" \
  "v_max3_u32 v217, v217, v92, v93\n" \
  "v_max3_u32 v217, v217, v94, v95\n" \
  "v_max3_u32 v217, v217, v96, v97\n" \
  "v_max3_u32 v217, v217, v98, v99\n" \
  "v_max3_u32 v217, v217, v100, v101\n" \
  "v_max3_u32 v217, v217, v102, v103\n" \
  "v_max3_u32 v217, v217, v104, v105\n" \
  "v_max3_u32 v217, v217, v106, v107\n" \
  "v_max3_u32 v217, v217, v108, v109\n" \
  "v_max3_u32 v217, v217, v110, v111\n" \
  "v_max3_u32 v217, v217, v112, v113\n" \
  "v_max3_u32 v217, v217, v114, v115\n" \
  "v_cmp_eq_u32 vcc, -1, v217\n" \
  "s_cbranch_vccz .LW_back_%=\n" \
  ".LW_nol_%=:\n" \
  "s_add_u32 s70, s70, 2\n" \
  "s_cmp_gt_u32 s70, 65536\n" \
  "s_cbranch_scc1 .LW_dset_%=\n" \
  "s_barrier\n" \
  "v_mov_b64 v[56:57], 0\n" \
  "v_mov_b64 v[58:59], 0\n" \
  "v_mov_b64 v[60:61], 0\n" \
  "v_mov_b64 v[62:63], 0\n" \
  "v_mov_b64 v[64:65], 0\n" \
  "v_mov_b64 v[66:67], 0\n" \
  "v_mov_b64 v[68:69], 0\n" \
  "v_mov_b64 v[70:71], 0\n" \
  "v_mov_b64 v[72:73], 0\n" \
  "v_mov_b64 v[74:75], 0\n" \
  "s_and_b32 s62, %51, 0xffff\n" \
  "s_add_u32 s44, s44, s62\n" \
  "s_lshr_b32 s62, s70, 1\n" \
  "s_add_u32 s76, %42, s62\n" \
  "s_add_u32 s77, s76, 3\n" \
  "s_and_b32 s76, s76, 3\n" \
  "s_and_b32 s77, s77, 3\n" \
  "s_lshl_b32 s76, s76, 2\n" \
  "s_lshl_b32 s77, s77, 2\n" \
  "s_lshr_b32 s76, s96, s76\n" \
  "s_lshr_b32 s77, s96, s77\n" \
  "s_xor_b32 s76, s76, s77\n" \
  "s_and_b32 s76, s76, 7\n" \
  "s_lshl_b32 s76, s76, 12\n" \
  "s_xor_b32 s59, s59, 0\n" \
  "s_mov_b32 s68, 0\n" \
  "s_mov_b32 m0, 0\n" \
  "s_branch .LW_rt_%=\n" \
  ".LW_dset_%=:\n" \
  "v_mov_b32 v100, 1\n" \
  "v_mov_b32 v101, s55\n" \
  "buffer_store_dwordx2 v[100:101], off, s[40:43], %48 offset:128 sc1\n" \
  "s_mov_b32 s70, 1\n" \
  "v_mov_b64 v[56:57], 0\n" \
  "v_mov_b64 v[58:59], 0\n" \
  "v_mov_b64 v[60:61], 0\n" \
  "v_mov_b64 v[62:63], 0\n" \
  "v_mov_b64 v[64:65], 0\n" \
  "v_mov_b64 v[66:67], 0\n" \
  "v_mov_b64 v[68:69], 0\n" \
  "v_mov_b64 v[70:71], 0\n" \
  "v_mov_b64 v[72:73], 0\n" \
  "v_mov_b64 v[74:75], 0\n" \
  "s_waitcnt vmcnt(0) lgkmcnt(0)\n" \
  "s_barrier\n" \
  "s_branch .LW_p_%=\n" \
  ".LW_ef_%=:\n" \
  "s_cmp_eq_u32 s48, 64\n" \
  "s_cbranch_scc1 .LW_end_%=\n" \
  "s_waitcnt vmcnt(0)\n" \
  "s_barrier\n" \
  "s_branch .LW_ef_b_%=\n" \
  ".LW_tw_re_%=:\n" \
  "s_add_u32 s69, s69, 1\n" \
  "s_cmp_gt_u32 s69, 64\n" \
  "s_cbranch_scc1 .LW_noclk_%=\n" \
  "buffer_load_dwordx4 v[80:83], off, s[40:43], s95 sc1\n" \
  "s_waitcnt vmcnt(0)\n" \
  "s_branch .LW_tw_%=\n" \
  ".LW_lat_%=:\n" \
  "s_lshr_b32 s63, %51, 16\n" \
  "s_add_u32 s62, s62, s63\n" \
  "s_cmp_lt_u32 s62, 4294937296\n" \
  "s_cbranch_scc1 .LW_noclk_%=\n" \
  "s_and_b32 s63, %51, 0xffff\n" \
  ".LW_lat1_%=:\n" \
  "s_add_u32 s44, s44, s63\n" \
  "s_add_u32 s62, s62, s63\n" \
  "s_cbranch_scc0 .LW_lat1_%=\n" \
  "s_branch .LW_rt_%=\n" \
  ".LW_clk2_%=:\n" \
  "s_cmp_le_i32 s62, 3\n" \
  "s_cbranch_scc1 .LW_clk_done_%=\n" \
  "s_memrealtime s[60:61]\n" \
  "s_waitcnt lgkmcnt(0)\n" \
  "s_sub_u32 s62, s73, s60\n" \
  "s_branch .LW_clk2_%=\n" \
  ".LW_clk_done_%=:\n" \
  "s_branch .LW_noclk_%=\n" \
  ".LW_end_%=:\n" \
  "s_waitcnt vmcnt(0)\n" \
  "s_waitcnt vmcnt(0)\n" \
  "s_nop 7\n" \
  "s_endpgm\n" \
  ".LW_other_%=:\n"
#define W_ASM_VOPS "+{v242}"(ci.sidx), "+{v243}"(ci.l16), "+{v244}"(ci.viw), "+{v245}"(ci.fva), "+{v246}"(ci.pk3), "+{v247}"(ci.fvs), "+{v248}"(ci.sid2), "+{v249}"(ci.uo), "+{v250}"(ci.tlb), "+{v251}"(ci.po0), "+{v252}"(ci.po1), "+{v253}"(ci.po2), "+{v254}"(ci.po3), "+{v255}"(ci.po4), "+{v200}"(ci.vq0), "+{v201}"(ci.vq1), "+{v202}"(ci.vq2), "+{v203}"(ci.vq3), "+{v204}"(ci.vo0), "+{v205}"(ci.vo1), "+{v206}"(ci.vo2), "+{v207}"(ci.vo3), "+{v208}"(ci.v50), "+{v209}"(ci.v51), "+{v210}"(ci.v52), "+{v211}"(ci.v53), "+{v212}"(ci.tro0), "+{v213}"(ci.tro1), "+{v214}"(ci.m0o), "+{v215}"(ci.tro2), "+{v216}"(ci.tro3), "+{v217}"(ci.tro4)
#define W_ASM_SOPS "s"(ci.ws), "s"(ci.Wa), "s"(ci.Wb), "s"(ci.mem0), "s"(ci.w), "s"(ci.o_a), "s"(ci.o_b), "s"(ci.o_nx), "s"(ci.o_exp), "s"(ci.o_u), "s"(ci.lab), "s"(ci.f_p), "s"(ci.f_m), "s"(ci.f_u), "s"(ci.f_dur), "s"(ci.f_go), "s"(ci.o_ctl), "s"(ci.o_dbg), "s"(ci.push), "s"(ci.dret), "s"(ci.u5), "s"(ci.o_qx), "s"(ci.f_s), "s"(ci.f_r)
#define W_ASM_CLOB "v0", "v1", "v2", "v3", "v4", "v5", "v6", "v7", "v8", "v9", "v10", "v11", "v12", "v13", "v14", "v15", "v16", "v17", "v18", "v19", "v20", "v21", "v22", "v23", "v24", "v25", "v26", "v27", "v28", "v29", "v30", "v31", "v32", "v33", "v34", "v35", "v36", "v37", "v38", "v39", "v40", "v41", "v42", "v43", "v44", "v45", "v46", "v47", "v48", "v49", "v50", "v51", "v52", "v53", "v54", "v55", "v56", "v57", "v58", "v59", "v60", "v61", "v62", "v63", "v64", "v65", "v66", "v67", "v68", "v69", "v70", "v71", "v72", "v73", "v74", "v75", "v76", "v77", "v78", "v79", "v80", "v81", "v82", "v83", "v84", "v85", "v86", "v87", "v88", "v89", "v90", "v91", "v92", "v93", "v94", "v95", "v96", "v97", "v98", "v99", "v100", "v101", "v102", "v103", "v104", "v105", "v106", "v107", "v108", "v109", "v110", "v111", "v112", "v113", "v114", "v115", "v116", "v117", "v118", "v119", "v120", "v121", "v122", "v123", "v124", "v125", "v126", "v127", "v128", "v129", "v130", "v131", "v132", "v133", "v134", "v135", "v136", "v137", "v138", "v139", "v140", "v141", "v142", "v143", "v144", "v145", "v146", "v147", "v148", "v149", "v150", "v151", "v152", "v153", "v154", "v155", "v156", "v157", "v158", "v159", "v160", "v161", "v162", "v163", "v164", "v165", "v166", "v167", "v168", "v169", "v170", "v171", "v172", "v173", "v174", "v175", "v176", "v177", "v178", "v179", "v180", "v181", "v182", "v183", "v184", "v185", "v186", "v187", "v188", "v189", "v190", "v191", "v192", "v193", "v194", "v195", "v196", "v197", "v198", "v199", "a0", "a1", "a2", "a3", "a4", "a5", "a6", "a7", "a8", "a9", "a10", "a11", "a12", "a13", "a14", "a15", "a16", "a17", "a18", "a19", "a20", "a21", "a22", "a23", "a24", "a25", "a26", "a27", "a28", "a29", "a30", "a31", "a32", "a33", "a34", "a35", "a36", "a37", "a38", "a39", "a40", "a41", "a42", "a43", "a44", "a45", "a46", "a47", "a48", "a49", "a50", "a51", "a52", "a53", "a54", "a55", "a56", "a57", "a58", "a59", "a60", "a61", "a62", "a63", "a64", "a65", "a66", "a67", "a68", "a69", "a70", "a71", "a72", "a73", "a74", "a75", "a76", "a77", "a78", "a79", "a80", "a81", "a82", "a83", "a84", "a85", "a86", "a87", "a88", "a89", "a90", "a91", "a92", "a93", "a94", "a95", "a96", "a97", "a98", "a99", "a100", "a101", "a102", "a103", "a104", "a105", "a106", "a107", "a108", "a109", "a110", "a111", "a112", "a113", "a114", "a115", "a116", "a117", "a118", "a119", "a120", "a121", "a122", "a123", "a124", "a125", "a126", "a127", "a128", "a129", "a130", "a131", "a132", "a133", "a134", "a135", "a136", "a137", "a138", "a139", "a140", "a141", "a142", "a143", "a144", "a145", "a146", "a147", "a148", "a149", "a150", "a151", "a152", "a153", "a154", "a155", "a156", "a157", "a158", "a159", "a160", "a161", "a162", "a163", "a164", "a165", "a166", "a167", "a168", "a169", "a170", "a171", "a172", "a173", "a174", "a175", "a176", "a177", "a178", "a179", "a180", "a181", "a182", "a183", "a184", "a185", "a186", "a187", "a188", "a189", "a190", "a191", "a192", "a193", "a194", "a195", "a196", "a197", "a198", "a199", "a200", "a201", "a202", "a203", "a204", "a205", "a206", "a207", "a208", "a209", "a210", "a211", "a212", "a213", "a214", "a215", "a216", "a217", "a218", "a219", "a220", "a221", "a222", "a223", "a224", "a225", "a226", "a227", "a228", "a229", "a230", "a231", "a232", "a233", "a234", "a235", "a236", "a237", "a238", "a239", "a240", "a241", "a242", "a243", "a244", "a245", "a246", "a247", "a248", "a249", "a250", "a251", "a252", "a253", "a254", "a255", "s40", "s41", "s42", "s43", "s44", "s45", "s46", "s47", "s48", "s49", "s50", "s51", "s52", "s53", "s54", "s55", "s56", "s57", "s58", "s59", "s60", "s61", "s62", "s63", "s64", "s65", "s66", "s67", "s68", "s69", "s70", "s71", "s72", "s73", "s74", "s75", "s76", "s77", "s78", "s79", "s80", "s81", "s82", "s83", "s84", "s85", "s86", "s87", "s88", "s89", "s90", "s91", "s92", "s93", "s94", "s95", "s96", "s97", "s98", "s99", "vcc", "scc", "memory"
#define R_ASM_TEXT \
  "s_mov_b64 s[40:41], %17\n" \
  "s_and_b32 s41, s41, 0xffff\n" \
  "s_mov_b32 s42, 255852544\n" \
  "s_mov_b32 s43, 0x00020000\n" \
  "s_bfe_u32 s56, %19, 0x30010\n" \
  "s_lshl_b32 s56, s56, 12\n" \
  "s_or_b32 s56, s56, 0x200000\n" \
  "s_mov_b32 s57, 1052672\n" \
  "s_mov_b32 s48, 0\n" \
  "s_cmp_lg_u32 %18, 0\n" \
  "s_cbranch_scc1 .LR_help_%=\n" \
  "s_lshl_b32 s61, %23, 2\n" \
  "s_lshr_b32 s51, %19, s61\n" \
  "s_and_b32 s51, s51, 7\n" \
  "s_lshl_b32 s51, s51, 12\n" \
  "s_xor_b32 s51, s51, 4210688\n" \
  "s_mov_b32 s71, s56\n" \
  "s_lshr_b32 s52, %19, 0\n" \
  "s_and_b32 s52, s52, 7\n" \
  "s_lshl_b32 s52, s52, 12\n" \
  "s_xor_b32 s52, s52, 4210688\n" \
  "s_xor_b32 s52, s52, s56\n" \
  "s_lshr_b32 s64, %19, 4\n" \
  "s_and_b32 s64, s64, 7\n" \
  "s_lshl_b32 s64, s64, 12\n" \
  "s_xor_b32 s64, s64, 4210688\n" \
  "s_xor_b32 s64, s64, s56\n" \
  "s_lshr_b32 s65, %19, 8\n" \
  "s_and_b32 s65, s65, 7\n" \
  "s_lshl_b32 s65, s65, 12\n" \
  "s_xor_b32 s65, s65, 4210688\n" \
  "s_xor_b32 s65, s65, s56\n" \
  "s_lshr_b32 s67, %19, 12\n" \
  "s_and_b32 s67, s67, 7\n" \
  "s_lshl_b32 s67, s67, 12\n" \
  "s_xor_b32 s67, s67, 4210688\n" \
  "s_xor_b32 s67, s67, s56\n" \
  "v_add_u32 v208, 32768, v215\n" \
  "v_add_u32 v209, 65536, v215\n" \
  "v_add_u32 v210, 98304, v215\n" \
  "v_add_u32 v211, 40960, v214\n" \
  "s_mov_b32 s49, %21\n" \
  "s_mov_b32 s58, %22\n" \
  "s_memrealtime s[86:87]\n" \
  "s_memtime s[90:91]\n" \
  "s_waitcnt lgkmcnt(0)\n" \
  "s_mov_b32 s87, 340\n" \
  "s_mov_b32 s50, 0\n" \
  "s_mov_b32 s44, 0\n" \
  "s_mov_b32 s88, 0\n" \
  "v_mov_b32 v199, %20\n" \
  "s_mov_b32 s89, 2130706432\n" \
  "s_xor_b32 s51, s51, 0x40000000\n" \
  "s_xor_b32 s49, s49, 0x40000000\n" \
  "s_xor_b32 s58, s58, 0x40000000\n" \
  "s_xor_b32 s52, s52, 0x40000000\n" \
  "s_xor_b32 s64, s64, 0x40000000\n" \
  "s_xor_b32 s65, s65, 0x40000000\n" \
  "s_xor_b32 s67, s67, 0x40000000\n" \
  ".LR_loop_%=:\n" \
  "s_add_u32 s95, s51, 885760\n" \
  "s_mov_b32 s54, s51\n" \
  "s_add_u32 s94, s51, 32768\n" \
  "s_mov_b32 s70, 0\n" \
  "s_mov_b32 s96, 0\n" \
  "buffer_load_dwordx4 v[200:203], off, s[40:43], s95 offset:128 sc1\n" \
  "s_sub_u32 s76, s84, s90\n" \
  "s_sub_u32 s77, s98, s86\n" \
  "v_cvt_f32_u32 v196, s76\n" \
  "v_cvt_f32_u32 v197, s77\n" \
  "v_rcp_f32 v197, v197\n" \
  "v_mul_f32 v196, 16.0, v196\n" \
  "v_fma_f32 v196, v196, v197, 0.5\n" \
  "v_cvt_u32_f32 v196, v196\n" \
  "s_nop 0\n" \
  "v_readfirstlane_b32 s76, v196\n" \
  "s_max_u32 s76, s76, 160\n" \
  "s_min_u32 s76, s76, 400\n" \
  "s_add_u32 s77, s48, 2\n" \
  "s_and_b32 s77, s77, 3\n" \
  "s_cselect_b32 s87, s87, s76\n" \
  "s_cselect_b32 s86, s86, s98\n" \
  "s_cselect_b32 s90, s90, s84\n" \
  "buffer_store_dwordx2 v[198:199], off, s[40:43], s89 sc1\n" \
  "s_barrier\n" \
  "ds_read_b128 a[0:3], v211 offset:0\n" \
  "ds_read_b128 a[4:7], v211 offset:1024\n" \
  "ds_read_b128 a[8:11], v211 offset:2048\n" \
  "ds_read_b128 a[12:15], v211 offset:3072\n" \
  "ds_read_b128 a[16:19], v211 offset:4096\n" \
  "ds_read_b128 a[20:23], v211 offset:5120\n" \
  "ds_read_b128 a[24:27], v211 offset:6144\n" \
  "ds_read_b128 a[28:31], v211 offset:7168\n" \
  "ds_read_b128 a[32:35], v211 offset:8192\n" \
  "ds_read_b128 a[36:39], v211 offset:9216\n" \
  "ds_read_b128 a[40:43], v211 offset:10240\n" \
  "ds_read_b128 a[44:47], v211 offset:11264\n" \
  "ds_read_b128 a[48:51], v211 offset:12288\n" \
  "ds_read_b128 a[52:55], v211 offset:13312\n" \
  "ds_read_b128 a[56:59], v211 offset:14336\n" \
  "ds_read_b128 a[60:63], v211 offset:15360\n" \
  "ds_read_b128 a[64:67], v211 offset:16384\n" \
  "ds_read_b128 a[68:71], v211 offset:17408\n" \
  "ds_read_b128 a[72:75], v211 offset:18432\n" \
  "ds_read_b128 a[76:79], v211 offset:19456\n" \
  "ds_read_b128 a[80:83], v211 offset:20480\n" \
  "ds_read_b128 a[84:87], v211 offset:21504\n" \
  "ds_read_b128 a[88:91], v211 offset:22528\n" \
  "ds_read_b128 a[92:95], v211 offset:23552\n" \
  "ds_read_b128 a[96:99], v211 offset:24576\n" \
  "ds_read_b128 a[100:103], v211 offset:25600\n" \
  "ds_read_b128 a[104:107], v211 offset:26624\n" \
  "ds_read_b128 a[108:111], v211 offset:27648\n" \
  "ds_read_b128 a[112:115], v211 offset:28672\n" \
  "ds_read_b128 a[116:119], v211 offset:29696\n" \
  "ds_read_b128 a[120:123], v211 offset:30720\n" \
  "ds_read_b128 a[124:127], v211 offset:31744\n" \
  "v_xor_b32 v211, 32768, v211\n" \
  "s_mov_b32 s69, 0\n" \
  "s_waitcnt vmcnt(1)\n" \
  ".LR_tw_%=:\n" \
  "v_readfirstlane_b32 s60, v200\n" \
  "s_cmp_lg_u32 s60, s48\n" \
  "s_cbranch_scc1 .LR_tw_re_%=\n" \
  "v_readfirstlane_b32 s50, v201\n" \
  ".LR_again_%=:\n" \
  "s_memrealtime s[60:61]\n" \
  "s_add_u32 s53, s50, s44\n" \
  "s_bfe_u32 s63, %27, 0x80010\n" \
  "s_waitcnt lgkmcnt(0)\n" \
  "s_sub_u32 s62, s53, s60\n" \
  "s_sub_u32 s61, s62, 14\n" \
  "s_cmp_ge_u32 s61, 2000\n" \
  "s_cbranch_scc1 .LR_near_%=\n" \
  "s_sub_u32 s62, s62, s63\n" \
  "s_cmp_le_i32 s62, 0\n" \
  "s_cbranch_scc1 .LR_fin_%=\n" \
  "s_sub_u32 s62, s62, 9\n" \
  ".LR_slp_%=:\n" \
  "s_cmp_le_i32 s62, 0\n" \
  "s_cbranch_scc1 .LR_clk_%=\n" \
  "s_sleep 1\n" \
  "s_sub_u32 s62, s62, 3\n" \
  "s_branch .LR_slp_%=\n" \
  ".LR_clk_%=:\n" \
  "s_memrealtime s[60:61]\n" \
  "s_waitcnt lgkmcnt(0)\n" \
  "s_sub_u32 s62, s53, s60\n" \
  "s_cmp_gt_i32 s62, s63\n" \
  "s_cbranch_scc1 .LR_clk_%=\n" \
  ".LR_fin_%=:\n" \
  "s_memrealtime s[76:77]\n" \
  "s_nop 4\n" \
  "s_memrealtime s[78:79]\n" \
  "s_nop 4\n" \
  "s_memrealtime s[80:81]\n" \
  "s_nop 4\n" \
  "s_memrealtime s[82:83]\n" \
  "s_waitcnt lgkmcnt(0)\n" \
  "s_mov_b32 s77, 1\n" \
  "s_cmp_eq_u32 s78, s76\n" \
  "s_addc_u32 s77, s77, 0\n" \
  "s_cmp_eq_u32 s80, s76\n" \
  "s_addc_u32 s77, s77, 0\n" \
  "s_cmp_eq_u32 s82, s76\n" \
  "s_addc_u32 s77, s77, 0\n" \
  "s_mul_i32 s77, s77, 24\n" \
  "s_lshr_b32 s78, s87, 2\n" \
  "s_sub_u32 s77, s78, s77\n" \
  "s_max_i32 s77, s77, 0\n" \
  "s_sub_u32 s76, s53, s76\n" \
  "s_mul_i32 s76, s76, s87\n" \
  "s_ashr_i32 s76, s76, 4\n" \
  "s_sub_u32 s76, s76, s77\n" \
  "s_sub_u32 s76, s76, 220\n" \
  "s_max_i32 s76, s76, 0\n" \
  "s_min_i32 s76, s76, 400\n" \
  "s_lshr_b32 s76, s76, 3\n" \
  "s_lshl_b32 s76, s76, 2\n" \
  "s_getpc_b64 s[78:79]\n" \
  "s_sub_u32 s76, 220, s76\n" \
  "s_add_u32 s78, s78, s76\n" \
  "s_addc_u32 s79, s79, 0\n" \
  "s_setpc_b64 s[78:79]\n" \
  "s_nop 1\n" \
  "s_nop 1\n" \
  "s_nop 1\n" \
  "s_nop 1\n" \
  "s_nop 1\n" \
  "s_nop 1\n" \
  "s_nop 1\n" \
  "s_nop 1\n" \
  "s_nop 1\n" \
  "s_nop 1\n" \
  "s_nop 1\n" \
  "s_nop 1\n" \
  "s_nop 1\n" \
  "s_nop 1\n" \
  "s_nop 1\n" \
  "s_nop 1\n" \
  "s_nop 1\n" \
  "s_nop 1\n" \
  "s_nop 1\n" \
  "s_nop 1\n" \
  "s_nop 1\n" \
  "s_nop 1\n" \
  "s_nop 1\n" \
  "s_nop 1\n" \
  "s_nop 1\n" \
  "s_nop 1\n" \
  "s_nop 1\n" \
  "s_nop 1\n" \
  "s_nop 1\n" \
  "s_nop 1\n" \
  "s_nop 1\n" \
  "s_nop 1\n" \
  "s_nop 1\n" \
  "s_nop 1\n" \
  "s_nop 1\n" \
  "s_nop 1\n" \
  "s_nop 1\n" \
  "s_nop 1\n" \
  "s_nop 1\n" \
  "s_nop 1\n" \
  "s_nop 1\n" \
  "s_nop 1\n" \
  "s_nop 1\n" \
  "s_nop 1\n" \
  "s_nop 1\n" \
  "s_nop 1\n" \
  "s_nop 1\n" \
  "s_nop 1\n" \
  "s_nop 1\n" \
  "s_nop 1\n" \
  ".LR_b2_%=:\n" \
  "buffer_load_dwordx4 v[0:3], v215, s[40:43], s54 offen sc1\n" \
  "s_memtime s[92:93]\n" \
  "s_cmp_lg_u32 s96, 0\n" \
  "buffer_load_dwordx4 v[4:7], v215, s[40:43], s54 offen offset:1024 sc1\n" \
  "buffer_load_dwordx4 v[8:11], v215, s[40:43], s54 offen offset:2048 sc1\n" \
  "buffer_load_dwordx4 v[12:15], v215, s[40:43], s54 offen offset:3072 sc1\n" \
  "buffer_load_dwordx4 v[16:19], v208, s[40:43], s54 offen sc1\n" \
  "buffer_load_dwordx4 v[20:23], v208, s[40:43], s54 offen offset:1024 sc1\n" \
  "buffer_load_dwordx4 v[24:27], v208, s[40:43], s54 offen offset:2048 sc1\n" \
  "buffer_load_dwordx4 v[28:31], v208, s[40:43], s54 offen offset:3072 sc1\n" \
  "buffer_load_dwordx4 v[32:35], v209, s[40:43], s54 offen sc1\n" \
  "buffer_load_dwordx4 v[36:39], v209, s[40:43], s54 offen offset:1024 sc1\n" \
  "buffer_load_dwordx4 v[40:43], v209, s[40:43], s54 offen offset:2048 sc1\n" \
  "buffer_load_dwordx4 v[44:47], v209, s[40:43], s54 offen offset:3072 sc1\n" \
  "buffer_load_dwordx4 v[48:51], v210, s[40:43], s54 offen sc1\n" \
  "buffer_load_dwordx4 v[52:55], v210, s[40:43], s54 offen offset:1024 sc1\n" \
  "buffer_load_dwordx4 v[56:59], v210, s[40:43], s54 offen offset:2048 sc1\n" \
  "buffer_load_dwordx4 v[60:63], v210, s[40:43], s54 offen offset:3072 sc1\n" \
  "v_mov_b64 v[64:65], 0\n" \
  "v_mov_b64 v[66:67], 0\n" \
  "buffer_load_dwordx4 v[128:131], v214, s[40:43], s49 offen sc1\n" \
  "v_mov_b64 v[68:69], 0\n" \
  "v_mov_b64 v[70:71], 0\n" \
  "buffer_load_dwordx4 v[132:135], v214, s[40:43], s49 offen offset:1024 sc1\n" \
  "v_mov_b64 v[72:73], 0\n" \
  "v_mov_b64 v[74:75], 0\n" \
  "buffer_load_dwordx4 v[136:139], v214, s[40:43], s49 offen offset:2048 sc1\n" \
  "v_mov_b64 v[76:77], 0\n" \
  "v_mov_b64 v[78:79], 0\n" \
  "buffer_load_dwordx4 v[140:143], v214, s[40:43], s49 offen offset:3072 sc1\n" \
  "v_mov_b64 v[80:81], 0\n" \
  "v_mov_b64 v[82:83], 0\n" \
  "buffer_load_dwordx4 v[144:147], v214, s[40:43], s58 offen sc1\n" \
  "v_mov_b64 v[84:85], 0\n" \
  "v_mov_b64 v[86:87], 0\n" \
  "buffer_load_dwordx4 v[148:151], v214, s[40:43], s58 offen offset:1024 sc1\n" \
  "v_mov_b64 v[88:89], 0\n" \
  "v_mov_b64 v[90:91], 0\n" \
  "buffer_load_dwordx4 v[152:155], v214, s[40:43], s58 offen offset:2048 sc1\n" \
  "v_mov_b64 v[92:93], 0\n" \
  "v_mov_b64 v[94:95], 0\n" \
  "buffer_load_dwordx4 v[156:159], v214, s[40:43], s58 offen offset:3072 sc1\n" \
  "v_mov_b64 v[96:97], 0\n" \
  "v_mov_b64 v[98:99], 0\n" \
  "buffer_load_dwordx4 v[160:163], v216, s[40:43], s54 offen sc1\n" \
  "v_mov_b64 v[100:101], 0\n" \
  "v_mov_b64 v[102:103], 0\n" \
  "buffer_load_dwordx4 v[164:167], v216, s[40:43], s54 offen offset:1024 sc1\n" \
  "v_mov_b64 v[104:105], 0\n" \
  "v_mov_b64 v[106:107], 0\n" \
  "buffer_load_dwordx4 v[168:171], v216, s[40:43], s54 offen offset:2048 sc1\n" \
  "v_mov_b64 v[108:109], 0\n" \
  "v_mov_b64 v[110:111], 0\n" \
  "buffer_load_dwordx4 v[172:175], v216, s[40:43], s54 offen offset:3072 sc1\n" \
  "v_mov_b64 v[112:113], 0\n" \
  "v_mov_b64 v[114:115], 0\n" \
  "buffer_load_dwordx4 v[176:179], v216, s[40:43], s94 offen sc1\n" \
  "v_mov_b64 v[116:117], 0\n" \
  "v_mov_b64 v[118:119], 0\n" \
  "buffer_load_dwordx4 v[180:183], v216, s[40:43], s94 offen offset:1024 sc1\n" \
  "v_mov_b64 v[120:121], 0\n" \
  "v_mov_b64 v[122:123], 0\n" \
  "buffer_load_dwordx4 v[184:187], v216, s[40:43], s94 offen offset:2048 sc1\n" \
  "v_mov_b64 v[124:125], 0\n" \
  "v_mov_b64 v[126:127], 0\n" \
  "buffer_load_dwordx4 v[188:191], v216, s[40:43], s94 offen offset:3072 sc1\n" \
  "v_mov_b64 v[192:193], 0\n" \
  "v_mov_b64 v[194:195], 0\n" \
  "s_cbranch_scc1 .LR_nb_%=\n" \
  "s_barrier\n" \
  ".LR_nb_%=:\n" \
  "s_waitcnt vmcnt(31)\n" \
  "v_smfmac_f32_16x16x64_bf16 v[64:67], v[0:3], a[0:7], v212\n" \
  "s_waitcnt vmcnt(30)\n" \
  "s_nop 1\n" \
  "v_smfmac_f32_16x16x64_bf16 v[68:71], v[4:7], a[8:15], v212\n" \
  "s_waitcnt vmcnt(29)\n" \
  "s_nop 1\n" \
  "v_smfmac_f32_16x16x64_bf16 v[72:75], v[8:11], a[16:23], v212\n" \
  "s_waitcnt vmcnt(28)\n" \
  "v_cvt_pk_bf16_f32 v64, v64, v65\n" \
  "v_cvt_pk_bf16_f32 v65, v66, v67\n" \
  "v_smfmac_f32_16x16x64_bf16 v[76:79], v[12:15], a[24:31], v212\n" \
  "s_waitcnt vmcnt(27)\n" \
  "v_cvt_pk_bf16_f32 v66, v68, v69\n" \
  "v_cvt_pk_bf16_f32 v67, v70, v71\n" \
  "v_smfmac_f32_16x16x64_bf16 v[80:83], v[16:19], a[32:39], v212\n" \
  "s_waitcnt vmcnt(26)\n" \
  "v_cvt_pk_bf16_f32 v68, v72, v73\n" \
  "v_cvt_pk_bf16_f32 v69, v74, v75\n" \
  "v_smfmac_f32_16x16x64_bf16 v[84:87], v[20:23], a[40:47], v212\n" \
  "s_waitcnt vmcnt(25)\n" \
  "v_cvt_pk_bf16_f32 v70, v76, v77\n" \
  "v_cvt_pk_bf16_f32 v71, v78, v79\n" \
  "v_smfmac_f32_16x16x64_bf16 v[88:91], v[24:27], a[48:55], v212\n" \
  "s_waitcnt vmcnt(24)\n" \
  "v_cvt_pk_bf16_f32 v80, v80, v81\n" \
  "v_cvt_pk_bf16_f32 v81, v82, v83\n" \
  "v_smfmac_f32_16x16x64_bf16 v[92:95], v[28:31], a[56:63], v212\n" \
  "s_waitcnt vmcnt(23)\n" \
  "v_cvt_pk_bf16_f32 v82, v84, v85\n" \
  "v_cvt_pk_bf16_f32 v83, v86, v87\n" \
  "v_smfmac_f32_16x16x64_bf16 v[96:99], v[32:35], a[64:71], v212\n" \
  "s_waitcnt vmcnt(22)\n" \
  "v_cvt_pk_bf16_f32 v84, v88, v89\n" \
  "v_cvt_pk_bf16_f32 v85, v90, v91\n" \
  "v_smfmac_f32_16x16x64_bf16 v[100:103], v[36:39], a[72:79], v212\n" \
  "s_waitcnt vmcnt(21)\n" \
  "v_cvt_pk_bf16_f32 v86, v92, v93\n" \
  "v_cvt_pk_bf16_f32 v87, v94, v95\n" \
  "v_smfmac_f32_16x16x64_bf16 v[104:107], v[40:43], a[80:87], v212\n" \
  "s_waitcnt vmcnt(20)\n" \
  "v_cvt_pk_bf16_f32 v96, v96, v97\n" \
  "v_cvt_pk_bf16_f32 v97, v98, v99\n" \
  "v_smfmac_f32_16x16x64_bf16 v[108:111], v[44:47], a[88:95], v212\n" \
  "s_waitcnt vmcnt(19)\n" \
  "v_cvt_pk_bf16_f32 v98, v100, v101\n" \
  "v_cvt_pk_bf16_f32 v99, v102, v103\n" \
  "v_smfmac_f32_16x16x64_bf16 v[112:115], v[48:51], a[96:103], v212\n" \
  "s_waitcnt vmcnt(18)\n" \
  "v_cvt_pk_bf16_f32 v100, v104, v105\n" \
  "v_cvt_pk_bf16_f32 v101, v106, v107\n" \
  "v_smfmac_f32_16x16x64_bf16 v[116:119], v[52:55], a[104:111], v212\n" \
  "s_waitcnt vmcnt(17)\n" \
  "v_cvt_pk_bf16_f32 v102, v108, v109\n" \
  "v_cvt_pk_bf16_f32 v103, v110, v111\n" \
  "v_smfmac_f32_16x16x64_bf16 v[120:123], v[56:59], a[112:119], v212\n" \
  "s_waitcnt vmcnt(16)\n" \
  "v_cvt_pk_bf16_f32 v112, v112, v113\n" \
  "v_cvt_pk_bf16_f32 v113, v114, v115\n" \
  "v_smfmac_f32_16x16x64_bf16 v[124:127], v[60:63], a[120:127], v212\n" \
  "s_mov_b32 s96, 1\n" \
  "v_cvt_pk_bf16_f32 v114, v116, v117\n" \
  "v_cvt_pk_bf16_f32 v115, v118, v119\n" \
  "s_nop 0\n" \
  "v_cvt_pk_bf16_f32 v116, v120, v121\n" \
  "v_cvt_pk_bf16_f32 v117, v122, v123\n" \
  "s_nop 1\n" \
  "v_cvt_pk_bf16_f32 v118, v124, v125\n" \
  "v_cvt_pk_bf16_f32 v119, v126, v127\n" \
  "s_waitcnt vmcnt(7)\n" \
  "v_smfmac_f32_16x16x64_bf16 v[192:195], v[160:163], v[128:135], v212\n" \
  "s_waitcnt vmcnt(6)\n" \
  "v_smfmac_f32_16x16x64_bf16 v[192:195], v[164:167], v[136:143], v212\n" \
  "s_waitcnt vmcnt(5)\n" \
  "v_smfmac_f32_16x16x64_bf16 v[192:195], v[168:171], v[144:151], v212\n" \
  "s_waitcnt vmcnt(4)\n" \
  "v_smfmac_f32_16x16x64_bf16 v[192:195], v[172:175], v[152:159], v212\n" \
  "s_waitcnt vmcnt(3)\n" \
  "v_smfmac_f32_16x16x64_bf16 v[192:195], v[176:179], v[64:71], v213\n" \
  "s_waitcnt vmcnt(2)\n" \
  "v_smfmac_f32_16x16x64_bf16 v[192:195], v[180:183], v[80:87], v213\n" \
  "s_waitcnt vmcnt(1)\n" \
  "v_smfmac_f32_16x16x64_bf16 v[192:195], v[184:187], v[96:103], v213\n" \
  "s_waitcnt vmcnt(0)\n" \
  "v_smfmac_f32_16x16x64_bf16 v[192:195], v[188:191], v[112:119], v213\n" \
  "s_nop 7\n" \
  "v_permlane32_swap_b32 v192, v194\n" \
  "v_permlane32_swap_b32 v193, v195\n" \
  "v_add_f32 v192, v192, v194\n" \
  "v_add_f32 v193, v193, v195\n" \
  "v_cvt_pk_bf16_f32 v194, v192, v193\n" \
  "v_cmp_u_f32 vcc, v192, v193\n" \
  "s_nop 0\n" \
  "v_mov_b32_dpp v195, v194 quad_perm:[1,0,3,2] row_mask:0xf bank_mask:0xf\n" \
  "v_perm_b32 v194, v195, v194, v218\n" \
  "s_cbranch_vccnz .LR_bad_%=\n" \
  ".LR_good_%=:\n" \
  "buffer_store_dword v194, v217, s[40:43], s52 offen sc1\n" \
  "buffer_store_dword v194, v217, s[40:43], s64 offen sc1\n" \
  "buffer_store_dword v194, v217, s[40:43], s65 offen sc1\n" \
  "buffer_store_dword v194, v217, s[40:43], s67 offen sc1\n" \
  "s_memrealtime s[98:99]\n" \
  "s_memtime s[84:85]\n" \
  "s_waitcnt lgkmcnt(0)\n" \
  "s_sub_u32 s60, s84, s92\n" \
  "s_cmp_eq_u32 s70, 0\n" \
  "s_cselect_b32 s89, %30, 2130706432\n" \
  "s_cmp_lt_u32 s60, 4000\n" \
  "s_cselect_b32 s89, s89, 2130706432\n" \
  "s_lshl_b32 s60, s60, 10\n" \
  "s_or_b32 s60, s60, s87\n" \
  "v_mov_b32 v198, s60\n" \
  "s_cmp_eq_u32 s48, 0\n" \
  "s_cbranch_scc1 .LR_go0_%=\n" \
  "s_cmp_lg_u32 %25, 0\n" \
  "s_cbranch_scc1 .LR_extra_%=\n" \
  ".LR_extra_b_%=:\n" \
  "s_xor_b32 s51, s51, s56\n" \
  "s_xor_b32 s49, s49, s56\n" \
  "s_xor_b32 s58, s58, s56\n" \
  "s_xor_b32 s52, s52, s57\n" \
  "s_xor_b32 s64, s64, s57\n" \
  "s_xor_b32 s65, s65, s57\n" \
  "s_xor_b32 s67, s67, s57\n" \
  "s_xor_b32 s71, s71, s57\n" \
  "s_mov_b32 s60, s56\n" \
  "s_mov_b32 s56, s57\n" \
  "s_mov_b32 s57, s60\n" \
  ".LR_go_%=:\n" \
  "s_add_u32 s48, s48, 1\n" \
  "s_cmp_lt_u32 s48, 64\n" \
  "s_cbranch_scc1 .LR_loop_%=\n" \
  "s_branch .LR_end_%=\n" \
  ".LR_tw_re_%=:\n" \
  "s_add_u32 s69, s69, 1\n" \
  "s_cmp_gt_u32 s69, 64\n" \
  "s_cbranch_scc1 .LR_now_%=\n" \
  "buffer_load_dwordx4 v[200:203], off, s[40:43], s95 offset:128 sc1\n" \
  "s_waitcnt vmcnt(0)\n" \
  "s_branch .LR_tw_%=\n" \
  ".LR_now_%=:\n" \
  "s_waitcnt lgkmcnt(0)\n" \
  "s_branch .LR_b2_%=\n" \
  ".LR_near_%=:\n" \
  "s_cmp_ge_u32 s62, 14\n" \
  "s_cbranch_scc1 .LR_lat_%=\n" \
  "s_cmp_le_u32 s62, 4\n" \
  "s_cbranch_scc1 .LR_b2_%=\n" \
  "s_memrealtime s[60:61]\n" \
  "s_waitcnt lgkmcnt(0)\n" \
  "s_sub_u32 s62, s53, s60\n" \
  "s_branch .LR_near_%=\n" \
  ".LR_lat_%=:\n" \
  "s_lshr_b32 s63, %28, 16\n" \
  "s_add_u32 s62, s62, s63\n" \
  "s_cmp_lt_u32 s62, 4294937296\n" \
  "s_cbranch_scc1 .LR_b2_%=\n" \
  "s_and_b32 s63, %28, 0xffff\n" \
  ".LR_lat1_%=:\n" \
  "s_add_u32 s44, s44, s63\n" \
  "s_add_u32 s62, s62, s63\n" \
  "s_cbranch_scc0 .LR_lat1_%=\n" \
  "s_branch .LR_again_%=\n" \
  ".LR_go0_%=:\n" \
  "s_xor_b32 s51, s51, 0x40000000\n" \
  "s_xor_b32 s49, s49, 0x40000000\n" \
  "s_xor_b32 s58, s58, 0x40000000\n" \
  "s_xor_b32 s52, s52, 0x40000000\n" \
  "s_xor_b32 s64, s64, 0x40000000\n" \
  "s_xor_b32 s65, s65, 0x40000000\n" \
  "s_xor_b32 s67, s67, 0x40000000\n" \
  "s_mov_b32 s44, 0\n" \
  "s_branch .LR_go_%=\n" \
  ".LR_extra_%=:\n" \
  "s_xor_b32 s62, s71, s56\n" \
  "v_mov_b32 v196, s48\n" \
  "v_add_u32 v197, s44, v202\n" \
  "v_xor_b32 v204, s62, v219\n" \
  "v_writelane_b32 v197, %20, 4\n" \
  "v_writelane_b32 v204, %25, 4\n" \
  "s_nop 1\n" \
  "buffer_store_dwordx2 v[196:197], v204, s[40:43], 0 offen sc1\n" \
  "s_branch .LR_extra_b_%=\n" \
  ".LR_bad_%=:\n" \
  "s_add_u32 s70, s70, 1\n" \
  "s_cmp_gt_u32 s70, 5\n" \
  "s_cbranch_scc1 .LR_bad1_%=\n" \
  ".LR_redo_%=:\n" \
  "s_and_b32 s62, %28, 0xffff\n" \
  "s_add_u32 s44, s44, s62\n" \
  "s_add_u32 s76, %23, s70\n" \
  "s_add_u32 s77, s76, 3\n" \
  "s_and_b32 s76, s76, 3\n" \
  "s_and_b32 s77, s77, 3\n" \
  "s_lshl_b32 s76, s76, 2\n" \
  "s_lshl_b32 s77, s77, 2\n" \
  "s_lshr_b32 s76, %19, s76\n" \
  "s_lshr_b32 s77, %19, s77\n" \
  "s_xor_b32 s76, s76, s77\n" \
  "s_and_b32 s76, s76, 7\n" \
  "s_lshl_b32 s76, s76, 12\n" \
  "s_mov_b32 s76, 0\n" \
  "s_xor_b32 s54, s54, s76\n" \
  "s_xor_b32 s94, s94, s76\n" \
  "s_branch .LR_again_%=\n" \
  ".LR_bad1_%=:\n" \
  "v_max_u32 v196, v0, v1\n" \
  "v_max3_u32 v196, v196, v2, v3\n" \
  "v_max3_u32 v196, v196, v4, v5\n" \
  "v_max3_u32 v196, v196, v6, v7\n" \
  "v_max3_u32 v196, v196, v8, v9\n" \
  "v_max3_u32 v196, v196, v10, v11\n" \
  "v_max3_u32 v196, v196, v12, v13\n" \
  "v_max3_u32 v196, v196, v14, v15\n" \
  "v_max3_u32 v196, v196, v16, v17\n" \
  "v_max3_u32 v196, v196, v18, v19\n" \
  "v_max3_u32 v196, v196, v20, v21\n" \
  "v_max3_u32 v196, v196, v22, v23\n" \
  "v_max3_u32 v196, v196, v24, v25\n" \
  "v_max3_u32 v196, v196, v26, v27\n" \
  "v_max3_u32 v196, v196, v28, v29\n" \
  "v_max3_u32 v196, v196, v30, v31\n" \
  "v_max3_u32 v196, v196, v32, v33\n" \
  "v_max3_u32 v196, v196, v34, v35\n" \
  "v_max3_u32 v196, v196, v36, v37\n" \
  "v_max3_u32 v196, v196, v38, v39\n" \
  "v_max3_u32 v196, v196, v40, v41\n" \
  "v_max3_u32 v196, v196, v42, v43\n" \
  "v_max3_u32 v196, v196, v44, v45\n" \
  "v_max3_u32 v196, v196, v46, v47\n" \
  "v_max3_u32 v196, v196, v48, v49\n" \
  "v_max3_u32 v196, v196, v50, v51\n" \
  "v_max3_u32 v196, v196, v52, v53\n" \
  "v_max3_u32 v196, v196, v54, v55\n" \
  "v_max3_u32 v196, v196, v56, v57\n" \
  "v_max3_u32 v196, v196, v58, v59\n" \
  "v_max3_u32 v196, v196, v60, v61\n" \
  "v_max3_u32 v196, v196, v62, v63\n" \
  "v_max_u32 v197, v128, v129\n" \
  "v_max3_u32 v197, v197, v130, v131\n" \
  "v_max3_u32 v197, v197, v132, v133\n" \
  "v_max3_u32 v197, v197, v134, v135\n" \
  "v_max3_u32 v197, v197, v136, v137\n" \
  "v_max3_u32 v197, v197, v138, v139\n" \
  "v_max3_u32 v197, v197, v140, v141\n" \
  "v_max3_u32 v197, v197, v142, v143\n" \
  "v_max3_u32 v197, v197, v144, v145\n" \
  "v_max3_u32 v197, v197, v146, v147\n" \
  "v_max3_u32 v197, v197, v148, v149\n" \
  "v_max3_u32 v197, v197, v150, v151\n" \
  "v_max3_u32 v197, v197, v152, v153\n" \
  "v_max3_u32 v197, v197, v154, v155\n" \
  "v_max3_u32 v197, v197, v156, v157\n" \
  "v_max3_u32 v197, v197, v158, v159\n" \
  "v_max_u32 v204, v160, v161\n" \
  "v_max3_u32 v204, v204, v162, v163\n" \
  "v_max3_u32 v204, v204, v164, v165\n" \
  "v_max3_u32 v204, v204, v166, v167\n" \
  "v_max3_u32 v204, v204, v168, v169\n" \
  "v_max3_u32 v204, v204, v170, v171\n" \
  "v_max3_u32 v204, v204, v172, v173\n" \
  "v_max3_u32 v204, v204, v174, v175\n" \
  "v_max3_u32 v204, v204, v176, v177\n" \
  "v_max3_u32 v204, v204, v178, v179\n" \
  "v_max3_u32 v204, v204, v180, v181\n" \
  "v_max3_u32 v204, v204, v182, v183\n" \
  "v_max3_u32 v204, v204, v184, v185\n" \
  "v_max3_u32 v204, v204, v186, v187\n" \
  "v_max3_u32 v204, v204, v188, v189\n" \
  "v_max3_u32 v204, v204, v190, v191\n" \
  "v_max3_u32 v196, v196, v197, v204\n" \
  "v_cmp_eq_u32 vcc, -1, v196\n" \
  "s_cbranch_vccz .LR_nan_%=\n" \
  "s_add_u32 s88, s88, 1\n" \
  "v_mov_b32 v196, s88\n" \
  "v_mov_b32 v197, %20\n" \
  "buffer_store_dwordx2 v[196:197], off, s[40:43], %31 sc1\n" \
  "s_waitcnt vmcnt(0)\n" \
  "s_and_b32 s63, s70, 255\n" \
  "s_cbranch_scc1 .LR_redo_%=\n" \
  "buffer_load_dwordx2 v[196:197], off, s[40:43], %26 offset:128 sc1\n" \
  "s_waitcnt vmcnt(0)\n" \
  "v_readfirstlane_b32 s63, v197\n" \
  "s_cmp_eq_u32 s63, %20\n" \
  "s_cbranch_scc1 .LR_nan_%=\n" \
  "s_cmp_lt_u32 s70, 262144\n" \
  "s_cbranch_scc1 .LR_redo_%=\n" \
  "v_mov_b32 v196, 1\n" \
  "v_mov_b32 v197, %20\n" \
  "buffer_store_dwordx2 v[196:197], off, s[40:43], %26 offset:128 sc1\n" \
  ".LR_nan_%=:\n" \
  "v_min_u32 v194, -2, v194\n" \
  "s_branch .LR_good_%=\n" \
  ".LR_fu_%=:\n" \
  "buffer_load_dwordx2 v[140:141], v220, s[40:43], 0 offen sc1\n" \
  "s_waitcnt vmcnt(0)\n" \
  "v_cmp_ne_u32 vcc, %20, v141\n" \
  "s_nop 1\n" \
  "v_cndmask_b32 v140, v140, 0, vcc\n" \
  "v_cmp_ge_i32 vcc, s48, v140\n" \
  "s_sub_u32 s60, s48, 2\n" \
  "s_cmp_lt_u32 s60, 8\n" \
  "s_cselect_b32 vcc_lo, 0, vcc_lo\n" \
  "s_cselect_b32 vcc_hi, vcc_hi, 0\n" \
  "s_mov_b64 vcc, vcc\n" \
  "s_cbranch_vccnz .LR_fu0_%=\n" \
  "s_xor_b32 s60, s61, s68\n" \
  "buffer_load_dwordx4 v[24:27], v214, s[40:43], s60 offen sc1\n" \
  "s_xor_b32 s60, s60, 1024\n" \
  "buffer_load_dwordx4 v[28:31], v214, s[40:43], s60 offen sc1\n" \
  "s_xor_b32 s60, s61, s74\n" \
  "buffer_load_dwordx4 v[32:35], v214, s[40:43], s60 offen sc1\n" \
  "s_xor_b32 s60, s60, 1024\n" \
  "buffer_load_dwordx4 v[36:39], v214, s[40:43], s60 offen sc1\n" \
  "s_cmp_lg_u32 %18, 3\n" \
  "s_cbranch_scc1 .LR_h8_%=\n" \
  "s_xor_b32 s60, s61, s75\n" \
  "buffer_load_dwordx4 v[40:43], v214, s[40:43], s60 offen sc1\n" \
  "s_xor_b32 s60, s60, 1024\n" \
  "buffer_load_dwordx4 v[44:47], v214, s[40:43], s60 offen sc1\n" \
  ".LR_h8_%=:\n" \
  "s_xor_b32 s60, s61, s64\n" \
  "buffer_load_dwordx4 v[0:3], v214, s[40:43], s60 offen sc1\n" \
  "s_xor_b32 s60, s60, 1024\n" \
  "buffer_load_dwordx4 v[4:7], v214, s[40:43], s60 offen sc1\n" \
  "s_xor_b32 s60, s61, s65\n" \
  "buffer_load_dwordx4 v[8:11], v214, s[40:43], s60 offen sc1\n" \
  "s_xor_b32 s60, s60, 1024\n" \
  "buffer_load_dwordx4 v[12:15], v214, s[40:43], s60 offen sc1\n" \
  "s_xor_b32 s60, s61, s67\n" \
  "buffer_load_dwordx4 v[16:19], v214, s[40:43], s60 offen sc1\n" \
  "s_xor_b32 s60, s60, 1024\n" \
  "buffer_load_dwordx4 v[20:23], v214, s[40:43], s60 offen sc1\n" \
  "s_cmp_lt_u32 s48, 62\n" \
  "s_cselect_b32 s60, s71, 2130706432\n" \
  "v_xor_b32 v142, s60, v221\n" \
  "v_xor_b32 v143, s60, v222\n" \
  "v_xor_b32 v144, s60, v223\n" \
  "buffer_store_dwordx4 v[136:139], v142, s[40:43], 0 offen sc1\n" \
  "buffer_store_dwordx4 v[136:139], v143, s[40:43], 0 offen sc1\n" \
  "buffer_store_dwordx4 v[136:139], v144, s[40:43], 0 offen sc1\n" \
  "s_sleep 8\n" \
  "buffer_load_dwordx2 v[140:141], v220, s[40:43], 0 offen sc1\n" \
  "s_barrier\n" \
  "s_waitcnt vmcnt(0)\n" \
  "v_mov_b32 v72, v24\n" \
  "v_mov_b32 v73, v28\n" \
  "v_mov_b32 v74, v25\n" \
  "v_mov_b32 v75, v29\n" \
  "v_mov_b32 v76, v26\n" \
  "v_mov_b32 v77, v30\n" \
  "v_mov_b32 v78, v27\n" \
  "v_mov_b32 v79, v31\n" \
  "ds_write_b128 v211, v[72:75] offset:6144\n" \
  "ds_write_b128 v211, v[76:79] offset:7168\n" \
  "v_mov_b32 v80, v32\n" \
  "v_mov_b32 v81, v36\n" \
  "v_mov_b32 v82, v33\n" \
  "v_mov_b32 v83, v37\n" \
  "v_mov_b32 v84, v34\n" \
  "v_mov_b32 v85, v38\n" \
  "v_mov_b32 v86, v35\n" \
  "v_mov_b32 v87, v39\n" \
  "ds_write_b128 v211, v[80:83] offset:8192\n" \
  "ds_write_b128 v211, v[84:87] offset:9216\n" \
  "s_branch .LR_fu_b_%=\n" \
  ".LR_fu0_%=:\n" \
  "s_cmp_lt_u32 s48, 62\n" \
  "s_cselect_b32 s60, s71, 2130706432\n" \
  "v_xor_b32 v142, s60, v221\n" \
  "v_xor_b32 v143, s60, v222\n" \
  "v_xor_b32 v144, s60, v223\n" \
  "buffer_store_dwordx4 v[136:139], v142, s[40:43], 0 offen sc1\n" \
  "buffer_store_dwordx4 v[136:139], v143, s[40:43], 0 offen sc1\n" \
  "buffer_store_dwordx4 v[136:139], v144, s[40:43], 0 offen sc1\n" \
  "s_barrier\n" \
  "s_mov_b32 s69, 0\n" \
  ".LR_fu1_%=:\n" \
  "buffer_load_dwordx2 v[140:141], v220, s[40:43], 0 offen sc1\n" \
  "s_waitcnt vmcnt(0)\n" \
  "v_cmp_ne_u32 vcc, %20, v141\n" \
  "s_nop 1\n" \
  "v_cndmask_b32 v140, v140, 0, vcc\n" \
  "v_cmp_ge_i32 vcc, s48, v140\n" \
  "s_sub_u32 s60, s48, 2\n" \
  "s_cmp_lt_u32 s60, 8\n" \
  "s_cselect_b32 vcc_lo, 0, vcc_lo\n" \
  "s_cselect_b32 vcc_hi, vcc_hi, 0\n" \
  "s_mov_b64 vcc, vcc\n" \
  "s_cbranch_vccz .LR_fu_x_%=\n" \
  "s_sleep 8\n" \
  "s_add_u32 s69, s69, 1\n" \
  "s_and_b32 s63, s69, 1023\n" \
  "s_cbranch_scc1 .LR_fu1_%=\n" \
  "s_cmp_gt_u32 s69, 2097152\n" \
  "s_cbranch_scc1 .LR_fu1_set_%=\n" \
  "buffer_load_dwordx2 v[196:197], off, s[40:43], %26 offset:128 sc1\n" \
  "s_waitcnt vmcnt(0)\n" \
  "v_readfirstlane_b32 s63, v197\n" \
  "s_cmp_eq_u32 s63, %20\n" \
  "s_cbranch_scc0 .LR_fu1_%=\n" \
  "s_branch .LR_fu_x_%=\n" \
  ".LR_fu1_set_%=:\n" \
  "v_mov_b32 v196, 1\n" \
  "v_mov_b32 v197, %20\n" \
  "buffer_store_dwordx2 v[196:197], off, s[40:43], %26 offset:128 sc1\n" \
  "s_waitcnt vmcnt(0)\n" \
  "s_branch .LR_fu_x_%=\n" \
  ".LR_fu_x_%=:\n" \
  "s_xor_b32 s60, s61, s68\n" \
  "buffer_load_dwordx4 v[24:27], v214, s[40:43], s60 offen sc1\n" \
  "s_xor_b32 s60, s60, 1024\n" \
  "buffer_load_dwordx4 v[28:31], v214, s[40:43], s60 offen sc1\n" \
  "s_xor_b32 s60, s61, s74\n" \
  "buffer_load_dwordx4 v[32:35], v214, s[40:43], s60 offen sc1\n" \
  "s_xor_b32 s60, s60, 1024\n" \
  "buffer_load_dwordx4 v[36:39], v214, s[40:43], s60 offen sc1\n" \
  "s_cmp_lg_u32 %18, 3\n" \
  "s_cbranch_scc1 .LR_h7_%=\n" \
  "s_xor_b32 s60, s61, s75\n" \
  "buffer_load_dwordx4 v[40:43], v214, s[40:43], s60 offen sc1\n" \
  "s_xor_b32 s60, s60, 1024\n" \
  "buffer_load_dwordx4 v[44:47], v214, s[40:43], s60 offen sc1\n" \
  ".LR_h7_%=:\n" \
  "s_xor_b32 s60, s61, s64\n" \
  "buffer_load_dwordx4 v[0:3], v214, s[40:43], s60 offen sc1\n" \
  "s_xor_b32 s60, s60, 1024\n" \
  "buffer_load_dwordx4 v[4:7], v214, s[40:43], s60 offen sc1\n" \
  "s_xor_b32 s60, s61, s65\n" \
  "buffer_load_dwordx4 v[8:11], v214, s[40:43], s60 offen sc1\n" \
  "s_xor_b32 s60, s60, 1024\n" \
  "buffer_load_dwordx4 v[12:15], v214, s[40:43], s60 offen sc1\n" \
  "s_xor_b32 s60, s61, s67\n" \
  "buffer_load_dwordx4 v[16:19], v214, s[40:43], s60 offen sc1\n" \
  "s_xor_b32 s60, s60, 1024\n" \
  "buffer_load_dwordx4 v[20:23], v214, s[40:43], s60 offen sc1\n" \
  "buffer_load_dwordx2 v[140:141], v220, s[40:43], 0 offen sc1\n" \
  "s_waitcnt vmcnt(7)\n" \
  "v_mov_b32 v72, v24\n" \
  "v_mov_b32 v73, v28\n" \
  "v_mov_b32 v74, v25\n" \
  "v_mov_b32 v75, v29\n" \
  "v_mov_b32 v76, v26\n" \
  "v_mov_b32 v77, v30\n" \
  "v_mov_b32 v78, v27\n" \
  "v_mov_b32 v79, v31\n" \
  "ds_write_b128 v211, v[72:75] offset:6144\n" \
  "ds_write_b128 v211, v[76:79] offset:7168\n" \
  "v_mov_b32 v80, v32\n" \
  "v_mov_b32 v81, v36\n" \
  "v_mov_b32 v82, v33\n" \
  "v_mov_b32 v83, v37\n" \
  "v_mov_b32 v84, v34\n" \
  "v_mov_b32 v85, v38\n" \
  "v_mov_b32 v86, v35\n" \
  "v_mov_b32 v87, v39\n" \
  "ds_write_b128 v211, v[80:83] offset:8192\n" \
  "ds_write_b128 v211, v[84:87] offset:9216\n" \
  "s_waitcnt vmcnt(0)\n" \
  "s_branch .LR_fu_b_%=\n" \
  ".LR_nb0_%=:\n" \
  "s_waitcnt vmcnt(0)\n" \
  "s_branch .LR_nb1_%=\n" \
  ".LR_help_%=:\n" \
  "s_mul_i32 s60, %18, 5\n" \
  "s_sub_u32 s60, s60, 5\n" \
  "s_lshl_b32 s61, s60, 11\n" \
  "s_add_u32 s61, s61, 8192\n" \
  "v_add_u32 v211, s61, v214\n" \
  "s_lshr_b32 s61, s60, 2\n" \
  "s_lshl_b32 s61, s61, 14\n" \
  "s_and_b32 s62, s60, 3\n" \
  "s_lshl_b32 s62, s62, 11\n" \
  "s_xor_b32 s61, s61, s62\n" \
  "s_xor_b32 s64, s61, %24\n" \
  "s_add_u32 s60, s60, 1\n" \
  "s_lshr_b32 s61, s60, 2\n" \
  "s_lshl_b32 s61, s61, 14\n" \
  "s_and_b32 s62, s60, 3\n" \
  "s_lshl_b32 s62, s62, 11\n" \
  "s_xor_b32 s61, s61, s62\n" \
  "s_xor_b32 s65, s61, %24\n" \
  "s_add_u32 s60, s60, 1\n" \
  "s_lshr_b32 s61, s60, 2\n" \
  "s_lshl_b32 s61, s61, 14\n" \
  "s_and_b32 s62, s60, 3\n" \
  "s_lshl_b32 s62, s62, 11\n" \
  "s_xor_b32 s61, s61, s62\n" \
  "s_xor_b32 s67, s61, %24\n" \
  "s_add_u32 s60, s60, 1\n" \
  "s_lshr_b32 s61, s60, 2\n" \
  "s_lshl_b32 s61, s61, 14\n" \
  "s_and_b32 s62, s60, 3\n" \
  "s_lshl_b32 s62, s62, 11\n" \
  "s_xor_b32 s61, s61, s62\n" \
  "s_xor_b32 s68, s61, %24\n" \
  "s_add_u32 s60, s60, 1\n" \
  "s_lshr_b32 s61, s60, 2\n" \
  "s_lshl_b32 s61, s61, 14\n" \
  "s_and_b32 s62, s60, 3\n" \
  "s_lshl_b32 s62, s62, 11\n" \
  "s_xor_b32 s61, s61, s62\n" \
  "s_xor_b32 s74, s61, %24\n" \
  "s_add_u32 s60, s60, 1\n" \
  "s_lshr_b32 s61, s60, 2\n" \
  "s_lshl_b32 s61, s61, 14\n" \
  "s_and_b32 s62, s60, 3\n" \
  "s_lshl_b32 s62, s62, 11\n" \
  "s_xor_b32 s61, s61, s62\n" \
  "s_xor_b32 s75, s61, %24\n" \
  "s_add_u32 s60, s60, 1\n" \
  "s_xor_b32 s71, s56, s57\n" \
  "s_movk_i32 s88, -32768\n" \
  "v_mov_b64 v[136:137], -1\n" \
  "v_mov_b64 v[138:139], -1\n" \
  "v_mov_b64 v[140:141], 0\n" \
  "s_mov_b64 vcc, -1\n" \
  "s_barrier\n" \
  ".LR_hl_%=:\n" \
  "s_and_b32 s61, s48, 15\n" \
  "s_lshl_b32 s61, s61, 16\n" \
  "s_bfe_u32 s60, %32, 524288\n" \
  ".LR_hs_%=:\n" \
  "s_cmp_eq_u32 s60, 0\n" \
  "s_cbranch_scc1 .LR_hs_x_%=\n" \
  "s_sleep 1\n" \
  "s_sub_u32 s60, s60, 1\n" \
  "s_branch .LR_hs_%=\n" \
  ".LR_hs_x_%=:\n" \
  "s_cbranch_vccnz .LR_fu_%=\n" \
  "s_cmp_lg_u32 %18, 3\n" \
  "s_cbranch_scc1 .LR_h5_%=\n" \
  "s_xor_b32 s60, s61, s75\n" \
  "buffer_load_dwordx4 v[40:43], v214, s[40:43], s60 offen sc1\n" \
  "s_xor_b32 s60, s60, 1024\n" \
  "buffer_load_dwordx4 v[44:47], v214, s[40:43], s60 offen sc1\n" \
  ".LR_h5_%=:\n" \
  "s_xor_b32 s60, s61, s64\n" \
  "buffer_load_dwordx4 v[0:3], v214, s[40:43], s60 offen sc1\n" \
  "s_xor_b32 s60, s60, 1024\n" \
  "buffer_load_dwordx4 v[4:7], v214, s[40:43], s60 offen sc1\n" \
  "s_xor_b32 s60, s61, s65\n" \
  "buffer_load_dwordx4 v[8:11], v214, s[40:43], s60 offen sc1\n" \
  "s_xor_b32 s60, s60, 1024\n" \
  "buffer_load_dwordx4 v[12:15], v214, s[40:43], s60 offen sc1\n" \
  "s_xor_b32 s60, s61, s67\n" \
  "buffer_load_dwordx4 v[16:19], v214, s[40:43], s60 offen sc1\n" \
  "s_xor_b32 s60, s60, 1024\n" \
  "buffer_load_dwordx4 v[20:23], v214, s[40:43], s60 offen sc1\n" \
  "s_cmp_lt_u32 s48, 62\n" \
  "s_cselect_b32 s60, s71, 2130706432\n" \
  "v_xor_b32 v142, s60, v221\n" \
  "v_xor_b32 v143, s60, v222\n" \
  "v_xor_b32 v144, s60, v223\n" \
  "buffer_store_dwordx4 v[136:139], v142, s[40:43], 0 offen sc1\n" \
  "buffer_store_dwordx4 v[136:139], v143, s[40:43], 0 offen sc1\n" \
  "buffer_store_dwordx4 v[136:139], v144, s[40:43], 0 offen sc1\n" \
  "s_waitcnt vmcnt(9)\n" \
  "v_mov_b32 v72, v24\n" \
  "v_mov_b32 v73, v28\n" \
  "v_mov_b32 v74, v25\n" \
  "v_mov_b32 v75, v29\n" \
  "v_mov_b32 v76, v26\n" \
  "v_mov_b32 v77, v30\n" \
  "v_mov_b32 v78, v27\n" \
  "v_mov_b32 v79, v31\n" \
  "ds_write_b128 v211, v[72:75] offset:6144\n" \
  "ds_write_b128 v211, v[76:79] offset:7168\n" \
  "v_mov_b32 v80, v32\n" \
  "v_mov_b32 v81, v36\n" \
  "v_mov_b32 v82, v33\n" \
  "v_mov_b32 v83, v37\n" \
  "v_mov_b32 v84, v34\n" \
  "v_mov_b32 v85, v38\n" \
  "v_mov_b32 v86, v35\n" \
  "v_mov_b32 v87, v39\n" \
  "ds_write_b128 v211, v[80:83] offset:8192\n" \
  "ds_write_b128 v211, v[84:87] offset:9216\n" \
  "s_barrier\n" \
  ".LR_fu_b_%=:\n" \
  "s_add_u32 s48, s48, 1\n" \
  "v_cmp_ne_u32 vcc, %20, v141\n" \
  "s_nop 1\n" \
  "v_cndmask_b32 v140, v140, 0, vcc\n" \
  "v_cmp_ge_i32 vcc, s48, v140\n" \
  "s_sub_u32 s60, s48, 2\n" \
  "s_cmp_lt_u32 s60, 8\n" \
  "s_cselect_b32 vcc_lo, 0, vcc_lo\n" \
  "s_cselect_b32 vcc_hi, vcc_hi, 0\n" \
  "s_mov_b64 vcc, vcc\n" \
  "s_and_b32 s61, s48, 15\n" \
  "s_lshl_b32 s61, s61, 16\n" \
  "s_cbranch_vccnz .LR_nb0_%=\n" \
  "s_bfe_u32 s60, %32, 524296\n" \
  ".LR_ht_%=:\n" \
  "s_cmp_eq_u32 s60, 0\n" \
  "s_cbranch_scc1 .LR_ht_x_%=\n" \
  "s_sleep 1\n" \
  "s_sub_u32 s60, s60, 1\n" \
  "s_branch .LR_ht_%=\n" \
  ".LR_ht_x_%=:\n" \
  "s_xor_b32 s60, s61, s68\n" \
  "buffer_load_dwordx4 v[24:27], v214, s[40:43], s60 offen sc1\n" \
  "s_xor_b32 s60, s60, 1024\n" \
  "buffer_load_dwordx4 v[28:31], v214, s[40:43], s60 offen sc1\n" \
  "s_xor_b32 s60, s61, s74\n" \
  "buffer_load_dwordx4 v[32:35], v214, s[40:43], s60 offen sc1\n" \
  "s_xor_b32 s60, s60, 1024\n" \
  "buffer_load_dwordx4 v[36:39], v214, s[40:43], s60 offen sc1\n" \
  "s_waitcnt vmcnt(4)\n" \
  ".LR_nb1_%=:\n" \
  "s_add_u32 s60, s48, 3\n" \
  "v_cmp_ge_i32 s[76:77], s60, v140\n" \
  "s_cmp_lt_u32 s48, 10\n" \
  "s_cselect_b32 s77, s77, 0\n" \
  "s_cmp_eq_u64 s[76:77], 0\n" \
  "s_cbranch_scc1 .LR_nf_%=\n" \
  "buffer_load_dwordx2 v[140:141], v220, s[40:43], 0 offen sc1\n" \
  ".LR_nf_%=:\n" \
  "v_mov_b32 v48, v0\n" \
  "v_mov_b32 v49, v4\n" \
  "v_mov_b32 v50, v1\n" \
  "v_mov_b32 v51, v5\n" \
  "v_mov_b32 v52, v2\n" \
  "v_mov_b32 v53, v6\n" \
  "v_mov_b32 v54, v3\n" \
  "v_mov_b32 v55, v7\n" \
  "ds_write_b128 v211, v[48:51] offset:0\n" \
  "ds_write_b128 v211, v[52:55] offset:1024\n" \
  "v_mov_b32 v56, v8\n" \
  "v_mov_b32 v57, v12\n" \
  "v_mov_b32 v58, v9\n" \
  "v_mov_b32 v59, v13\n" \
  "v_mov_b32 v60, v10\n" \
  "v_mov_b32 v61, v14\n" \
  "v_mov_b32 v62, v11\n" \
  "v_mov_b32 v63, v15\n" \
  "ds_write_b128 v211, v[56:59] offset:2048\n" \
  "ds_write_b128 v211, v[60:63] offset:3072\n" \
  "v_mov_b32 v64, v16\n" \
  "v_mov_b32 v65, v20\n" \
  "v_mov_b32 v66, v17\n" \
  "v_mov_b32 v67, v21\n" \
  "v_mov_b32 v68, v18\n" \
  "v_mov_b32 v69, v22\n" \
  "v_mov_b32 v70, v19\n" \
  "v_mov_b32 v71, v23\n" \
  "ds_write_b128 v211, v[64:67] offset:4096\n" \
  "ds_write_b128 v211, v[68:71] offset:5120\n" \
  "s_cmp_lg_u32 %18, 3\n" \
  "s_cbranch_scc1 .LR_h6_%=\n" \
  "v_mov_b32 v88, v40\n" \
  "v_mov_b32 v89, v44\n" \
  "v_mov_b32 v90, v41\n" \
  "v_mov_b32 v91, v45\n" \
  "v_mov_b32 v92, v42\n" \
  "v_mov_b32 v93, v46\n" \
  "v_mov_b32 v94, v43\n" \
  "v_mov_b32 v95, v47\n" \
  "ds_write_b128 v211, v[88:91] offset:10240\n" \
  "ds_write_b128 v211, v[92:95] offset:11264\n" \
  ".LR_h6_%=:\n" \
  "s_sub_u32 s88, 0, s88\n" \
  "s_xor_b32 s71, s71, s56\n" \
  "s_mov_b32 s61, s56\n" \
  "s_mov_b32 s56, s57\n" \
  "s_mov_b32 s57, s61\n" \
  "s_waitcnt lgkmcnt(0)\n" \
  "v_add_u32 v211, s88, v211\n" \
  "s_barrier\n" \
  "s_cmp_lt_u32 s48, 63\n" \
  "s_cbranch_scc1 .LR_hl_%=\n" \
  "s_barrier\n" \
  ".LR_end_%=:\n" \
  "s_waitcnt vmcnt(0)\n"
#define R_ASM_VOPS "+{v212}"(ri.sidx), "+{v213}"(ri.cidx), "+{v214}"(ri.l16), "+{v215}"(ri.ax), "+{v216}"(ri.am), "+{v217}"(ri.mo), "+{v218}"(ri.sel), "+{v219}"(ri.to), "+{v220}"(ri.fu), "+{v221}"(ri.p0), "+{v222}"(ri.p1), "+{v223}"(ri.p2), "+{v224}"(ri.d0), "+{v225}"(ri.d1), "+{v226}"(ri.d2), "+{v227}"(ri.d3), "+{v228}"(ri.d4)
#define R_ASM_SOPS "s"(ri.ws), "s"(ri.w), "s"(ri.go), "s"(ri.tag), "s"(ri.o_va), "s"(ri.o_vb), "s"(ri.lab), "s"(ri.o_u), "s"(ri.kind), "s"(ri.o_ctl), "s"(ri.pa), "s"(ri.pb), "s"(ri.pc), "s"(ri.f_d), "s"(ri.f_r), "s"(ri.ph)
#define R_ASM_CLOB "v0", "v1", "v2", "v3", "v4", "v5", "v6", "v7", "v8", "v9", "v10", "v11", "v12", "v13", "v14", "v15", "v16", "v17", "v18", "v19", "v20", "v21", "v22", "v23", "v24", "v25", "v26", "v27", "v28", "v29", "v30", "v31", "v32", "v33", "v34", "v35", "v36", "v37", "v38", "v39", "v40", "v41", "v42", "v43", "v44", "v45", "v46", "v47", "v48", "v49", "v50", "v51", "v52", "v53", "v54", "v55", "v56", "v57", "v58", "v59", "v60", "v61", "v62", "v63", "v64", "v65", "v66", "v67", "v68", "v69", "v70", "v71", "v72", "v73", "v74", "v75", "v76", "v77", "v78", "v79", "v80", "v81", "v82", "v83", "v84", "v85", "v86", "v87", "v88", "v89", "v90", "v91", "v92", "v93", "v94", "v95", "v96", "v97", "v98", "v99", "v100", "v101", "v102", "v103", "v104", "v105", "v106", "v107", "v108", "v109", "v110", "v111", "v112", "v113", "v114", "v115", "v116", "v117", "v118", "v119", "v120", "v121", "v122", "v123", "v124", "v125", "v126", "v127", "v128", "v129", "v130", "v131", "v132", "v133", "v134", "v135", "v136", "v137", "v138", "v139", "v140", "v141", "v142", "v143", "v144", "v145", "v146", "v147", "v148", "v149", "v150", "v151", "v152", "v153", "v154", "v155", "v156", "v157", "v158", "v159", "v160", "v161", "v162", "v163", "v164", "v165", "v166", "v167", "v168", "v169", "v170", "v171", "v172", "v173", "v174", "v175", "v176", "v177", "v178", "v179", "v180", "v181", "v182", "v183", "v184", "v185", "v186", "v187", "v188", "v189", "v190", "v191", "v192", "v193", "v194", "v195", "v196", "v197", "v198", "v199", "v200", "v201", "v202", "v203", "v204", "v205", "v206", "v207", "v208", "v209", "v210", "v211", "a0", "a1", "a2", "a3", "a4", "a5", "a6", "a7", "a8", "a9", "a10", "a11", "a12", "a13", "a14", "a15", "a16", "a17", "a18", "a19", "a20", "a21", "a22", "a23", "a24", "a25", "a26", "a27", "a28", "a29", "a30", "a31", "a32", "a33", "a34", "a35", "a36", "a37", "a38", "a39", "a40", "a41", "a42", "a43", "a44", "a45", "a46", "a47", "a48", "a49", "a50", "a51", "a52", "a53", "a54", "a55", "a56", "a57", "a58", "a59", "a60", "a61", "a62", "a63", "a64", "a65", "a66", "a67", "a68", "a69", "a70", "a71", "a72", "a73", "a74", "a75", "a76", "a77", "a78", "a79", "a80", "a81", "a82", "a83", "a84", "a85", "a86", "a87", "a88", "a89", "a90", "a91", "a92", "a93", "a94", "a95", "a96", "a97", "a98", "a99", "a100", "a101", "a102", "a103", "a104", "a105", "a106", "a107", "a108", "a109", "a110", "a111", "a112", "a113", "a114", "a115", "a116", "a117", "a118", "a119", "a120", "a121", "a122", "a123", "a124", "a125", "a126", "a127", "s40", "s41", "s42", "s43", "s44", "s45", "s46", "s47", "s48", "s49", "s50", "s51", "s52", "s53", "s54", "s55", "s56", "s57", "s58", "s59", "s60", "s61", "s62", "s63", "s64", "s65", "s66", "s67", "s68", "s69", "s70", "s71", "s72", "s73", "s74", "s75", "s76", "s77", "s78", "s79", "s80", "s81", "s82", "s83", "s84", "s85", "s86", "s87", "s88", "s89", "s90", "s91", "s92", "s93", "s94", "s95", "s96", "s97", "s98", "s99", "vcc", "scc", "memory"
#define K_ASM_TEXT \
  "s_mov_b64 s[40:41], %30\n" \
  "s_and_b32 s41, s41, 0xffff\n" \
  "s_mov_b32 s42, 255852544\n" \
  "s_mov_b32 s43, 0x00020000\n" \
  "s_mov_b32 s47, 0x00020000\n" \
  "s_lshl_b32 s72, %34, 12\n" \
  "buffer_load_dword v184, off, s[40:43], %42 sc1\n" \
  "s_mov_b64 s[44:45], %31\n" \
  "s_and_b32 s45, s45, 0xffff\n" \
  "s_mov_b32 s46, 4718592\n" \
  "s_add_u32 s60, %35, 0\n" \
  "s_add_u32 m0, s72, 16384\n" \
  "s_nop 0\n" \
  "buffer_load_dwordx4 v170, s[44:47], s60 offen lds\n" \
  "buffer_load_dwordx4 v171, s[44:47], s60 offen offset:1024 lds\n" \
  "buffer_load_dwordx4 v172, s[44:47], s60 offen offset:2048 lds\n" \
  "buffer_load_dwordx4 v173, s[44:47], s60 offen offset:3072 lds\n" \
  "s_add_u32 s60, %35, 524288\n" \
  "s_add_u32 m0, s72, 32768\n" \
  "s_nop 0\n" \
  "buffer_load_dwordx4 v170, s[44:47], s60 offen lds\n" \
  "buffer_load_dwordx4 v171, s[44:47], s60 offen offset:1024 lds\n" \
  "buffer_load_dwordx4 v172, s[44:47], s60 offen offset:2048 lds\n" \
  "buffer_load_dwordx4 v173, s[44:47], s60 offen offset:3072 lds\n" \
  "s_add_u32 s60, %35, 1048576\n" \
  "s_add_u32 m0, s72, 49152\n" \
  "s_nop 0\n" \
  "buffer_load_dwordx4 v170, s[44:47], s60 offen lds\n" \
  "buffer_load_dwordx4 v171, s[44:47], s60 offen offset:1024 lds\n" \
  "buffer_load_dwordx4 v172, s[44:47], s60 offen offset:2048 lds\n" \
  "buffer_load_dwordx4 v173, s[44:47], s60 offen offset:3072 lds\n" \
  "s_add_u32 s60, %35, 1572864\n" \
  "s_add_u32 m0, s72, 65536\n" \
  "s_nop 0\n" \
  "buffer_load_dwordx4 v170, s[44:47], s60 offen lds\n" \
  "buffer_load_dwordx4 v171, s[44:47], s60 offen offset:1024 lds\n" \
  "buffer_load_dwordx4 v172, s[44:47], s60 offen offset:2048 lds\n" \
  "buffer_load_dwordx4 v173, s[44:47], s60 offen offset:3072 lds\n" \
  "s_add_u32 s60, %35, 2097152\n" \
  "s_add_u32 m0, s72, 81920\n" \
  "s_nop 0\n" \
  "buffer_load_dwordx4 v170, s[44:47], s60 offen lds\n" \
  "buffer_load_dwordx4 v171, s[44:47], s60 offen offset:1024 lds\n" \
  "buffer_load_dwordx4 v172, s[44:47], s60 offen offset:2048 lds\n" \
  "buffer_load_dwordx4 v173, s[44:47], s60 offen offset:3072 lds\n" \
  "s_add_u32 s60, %35, 2621440\n" \
  "s_add_u32 m0, s72, 98304\n" \
  "s_nop 0\n" \
  "buffer_load_dwordx4 v170, s[44:47], s60 offen lds\n" \
  "buffer_load_dwordx4 v171, s[44:47], s60 offen offset:1024 lds\n" \
  "buffer_load_dwordx4 v172, s[44:47], s60 offen offset:2048 lds\n" \
  "buffer_load_dwordx4 v173, s[44:47], s60 offen offset:3072 lds\n" \
  "s_add_u32 s60, %35, 3145728\n" \
  "s_add_u32 m0, s72, 114688\n" \
  "s_nop 0\n" \
  "buffer_load_dwordx4 v170, s[44:47], s60 offen lds\n" \
  "buffer_load_dwordx4 v171, s[44:47], s60 offen offset:1024 lds\n" \
  "buffer_load_dwordx4 v172, s[44:47], s60 offen offset:2048 lds\n" \
  "buffer_load_dwordx4 v173, s[44:47], s60 offen offset:3072 lds\n" \
  "s_add_u32 s60, %35, 3670016\n" \
  "s_add_u32 m0, s72, 131072\n" \
  "s_nop 0\n" \
  "buffer_load_dwordx4 v170, s[44:47], s60 offen lds\n" \
  "buffer_load_dwordx4 v171, s[44:47], s60 offen offset:1024 lds\n" \
  "buffer_load_dwordx4 v172, s[44:47], s60 offen offset:2048 lds\n" \
  "buffer_load_dwordx4 v173, s[44:47], s60 offen offset:3072 lds\n" \
  "s_add_u32 s60, %35, 4194304\n" \
  "s_add_u32 m0, s72, 147456\n" \
  "s_nop 0\n" \
  "buffer_load_dwordx4 v170, s[44:47], s60 offen lds\n" \
  "buffer_load_dwordx4 v171, s[44:47], s60 offen offset:1024 lds\n" \
  "buffer_load_dwordx4 v172, s[44:47], s60 offen offset:2048 lds\n" \
  "buffer_load_dwordx4 v173, s[44:47], s60 offen offset:3072 lds\n" \
  "s_waitcnt vmcnt(24)\n" \
  "s_barrier\n" \
  "v_add_u32 v186, 16384, v182\n" \
  "ds_read_b64_tr_b16 a[0:1], v186\n" \
  "ds_read_b64_tr_b16 a[2:3], v186 offset:1024\n" \
  "ds_read_b64_tr_b16 a[4:5], v186 offset:8192\n" \
  "ds_read_b64_tr_b16 a[6:7], v186 offset:9216\n" \
  "v_add_u32 v187, 16384, v183\n" \
  "ds_read_b64_tr_b16 a[72:73], v187\n" \
  "ds_read_b64_tr_b16 a[74:75], v187 offset:1024\n" \
  "ds_read_b64_tr_b16 a[76:77], v187 offset:8192\n" \
  "ds_read_b64_tr_b16 a[78:79], v187 offset:9216\n" \
  "v_add_u32 v186, 32768, v182\n" \
  "ds_read_b64_tr_b16 a[8:9], v186\n" \
  "ds_read_b64_tr_b16 a[10:11], v186 offset:1024\n" \
  "ds_read_b64_tr_b16 a[12:13], v186 offset:8192\n" \
  "ds_read_b64_tr_b16 a[14:15], v186 offset:9216\n" \
  "v_add_u32 v187, 32768, v183\n" \
  "ds_read_b64_tr_b16 a[80:81], v187\n" \
  "ds_read_b64_tr_b16 a[82:83], v187 offset:1024\n" \
  "ds_read_b64_tr_b16 a[84:85], v187 offset:8192\n" \
  "ds_read_b64_tr_b16 a[86:87], v187 offset:9216\n" \
  "v_add_u32 v186, 49152, v182\n" \
  "ds_read_b64_tr_b16 a[16:17], v186\n" \
  "ds_read_b64_tr_b16 a[18:19], v186 offset:1024\n" \
  "ds_read_b64_tr_b16 a[20:21], v186 offset:8192\n" \
  "ds_read_b64_tr_b16 a[22:23], v186 offset:9216\n" \
  "v_add_u32 v187, 49152, v183\n" \
  "ds_read_b64_tr_b16 a[88:89], v187\n" \
  "ds_read_b64_tr_b16 a[90:91], v187 offset:1024\n" \
  "ds_read_b64_tr_b16 a[92:93], v187 offset:8192\n" \
  "ds_read_b64_tr_b16 a[94:95], v187 offset:9216\n" \
  "s_waitcnt lgkmcnt(0)\n" \
  "s_barrier\n" \
  "v_readfirstlane_b32 s55, v184\n" \
  "s_add_u32 s55, s55, 1\n" \
  "s_mov_b64 s[44:45], %33\n" \
  "s_and_b32 s45, s45, 0xffff\n" \
  "s_mov_b32 s46, 2359296\n" \
  "s_mov_b32 s51, 0\n" \
  "s_mul_i32 s52, %34, 9216\n" \
  "s_add_u32 s52, s52, 16384\n" \
  "s_add_u32 m0, s52, 0\n" \
  "s_nop 0\n" \
  "buffer_load_dwordx4 v196, s[44:47], s51 offen lds\n" \
  "s_add_u32 m0, s52, 1024\n" \
  "s_nop 0\n" \
  "buffer_load_dwordx4 v197, s[44:47], s51 offen lds\n" \
  "s_add_u32 m0, s52, 2048\n" \
  "s_nop 0\n" \
  "buffer_load_dwordx4 v198, s[44:47], s51 offen lds\n" \
  "s_add_u32 m0, s52, 3072\n" \
  "s_nop 0\n" \
  "buffer_load_dwordx4 v199, s[44:47], s51 offen lds\n" \
  "s_add_u32 m0, s52, 4096\n" \
  "s_nop 0\n" \
  "buffer_load_dwordx4 v200, s[44:47], s51 offen lds\n" \
  "s_add_u32 m0, s52, 5120\n" \
  "s_nop 0\n" \
  "buffer_load_dwordx4 v201, s[44:47], s51 offen lds\n" \
  "s_add_u32 m0, s52, 6144\n" \
  "s_nop 0\n" \
  "buffer_load_dwordx4 v202, s[44:47], s51 offen lds\n" \
  "s_add_u32 m0, s52, 7168\n" \
  "s_nop 0\n" \
  "buffer_load_dwordx4 v203, s[44:47], s51 offen lds\n" \
  "s_add_u32 m0, s52, 8192\n" \
  "s_nop 0\n" \
  "buffer_load_dwordx4 v204, s[44:47], s51 offen lds\n" \
  "s_add_u32 s51, s51, 36864\n" \
  "s_min_u32 s51, s51, 2322432\n" \
  "s_add_u32 s52, s52, 36864\n" \
  "s_cmp_ge_u32 s52, 163840\n" \
  "s_cselect_b32 s60, 147456, 0\n" \
  "s_sub_u32 s52, s52, s60\n" \
  "s_waitcnt vmcnt(21)\n" \
  "s_barrier\n" \
  "v_add_u32 v186, 65536, v182\n" \
  "ds_read_b64_tr_b16 a[24:25], v186\n" \
  "ds_read_b64_tr_b16 a[26:27], v186 offset:1024\n" \
  "ds_read_b64_tr_b16 a[28:29], v186 offset:8192\n" \
  "ds_read_b64_tr_b16 a[30:31], v186 offset:9216\n" \
  "v_add_u32 v187, 65536, v183\n" \
  "ds_read_b64_tr_b16 a[96:97], v187\n" \
  "ds_read_b64_tr_b16 a[98:99], v187 offset:1024\n" \
  "ds_read_b64_tr_b16 a[100:101], v187 offset:8192\n" \
  "ds_read_b64_tr_b16 a[102:103], v187 offset:9216\n" \
  "v_add_u32 v186, 81920, v182\n" \
  "ds_read_b64_tr_b16 a[32:33], v186\n" \
  "ds_read_b64_tr_b16 a[34:35], v186 offset:1024\n" \
  "ds_read_b64_tr_b16 a[36:37], v186 offset:8192\n" \
  "ds_read_b64_tr_b16 a[38:39], v186 offset:9216\n" \
  "v_add_u32 v187, 81920, v183\n" \
  "ds_read_b64_tr_b16 a[104:105], v187\n" \
  "ds_read_b64_tr_b16 a[106:107], v187 offset:1024\n" \
  "ds_read_b64_tr_b16 a[108:109], v187 offset:8192\n" \
  "ds_read_b64_tr_b16 a[110:111], v187 offset:9216\n" \
  "v_add_u32 v186, 98304, v182\n" \
  "ds_read_b64_tr_b16 a[40:41], v186\n" \
  "ds_read_b64_tr_b16 a[42:43], v186 offset:1024\n" \
  "ds_read_b64_tr_b16 a[44:45], v186 offset:8192\n" \
  "ds_read_b64_tr_b16 a[46:47], v186 offset:9216\n" \
  "v_add_u32 v187, 98304, v183\n" \
  "ds_read_b64_tr_b16 a[112:113], v187\n" \
  "ds_read_b64_tr_b16 a[114:115], v187 offset:1024\n" \
  "ds_read_b64_tr_b16 a[116:117], v187 offset:8192\n" \
  "ds_read_b64_tr_b16 a[118:119], v187 offset:9216\n" \
  "s_waitcnt lgkmcnt(0)\n" \
  "s_barrier\n" \
  "buffer_load_dwordx2 v[184:185], off, s[40:43], %41 sc1\n" \
  "s_add_u32 m0, s52, 0\n" \
  "s_nop 0\n" \
  "buffer_load_dwordx4 v196, s[44:47], s51 offen lds\n" \
  "s_add_u32 m0, s52, 1024\n" \
  "s_nop 0\n" \
  "buffer_load_dwordx4 v197, s[44:47], s51 offen lds\n" \
  "s_add_u32 m0, s52, 2048\n" \
  "s_nop 0\n" \
  "buffer_load_dwordx4 v198, s[44:47], s51 offen lds\n" \
  "s_add_u32 m0, s52, 3072\n" \
  "s_nop 0\n" \
  "buffer_load_dwordx4 v199, s[44:47], s51 offen lds\n" \
  "s_add_u32 m0, s52, 4096\n" \
  "s_nop 0\n" \
  "buffer_load_dwordx4 v200, s[44:47], s51 offen lds\n" \
  "s_add_u32 m0, s52, 5120\n" \
  "s_nop 0\n" \
  "buffer_load_dwordx4 v201, s[44:47], s51 offen lds\n" \
  "s_add_u32 m0, s52, 6144\n" \
  "s_nop 0\n" \
  "buffer_load_dwordx4 v202, s[44:47], s51 offen lds\n" \
  "s_add_u32 m0, s52, 7168\n" \
  "s_nop 0\n" \
  "buffer_load_dwordx4 v203, s[44:47], s51 offen lds\n" \
  "s_add_u32 m0, s52, 8192\n" \
  "s_nop 0\n" \
  "buffer_load_dwordx4 v204, s[44:47], s51 offen lds\n" \
  "s_add_u32 s51, s51, 36864\n" \
  "s_min_u32 s51, s51, 2322432\n" \
  "s_add_u32 s52, s52, 36864\n" \
  "s_cmp_ge_u32 s52, 163840\n" \
  "s_cselect_b32 s60, 147456, 0\n" \
  "s_sub_u32 s52, s52, s60\n" \
  "s_waitcnt vmcnt(19)\n" \
  "s_barrier\n" \
  "v_add_u32 v186, 114688, v182\n" \
  "ds_read_b64_tr_b16 a[48:49], v186\n" \
  "ds_read_b64_tr_b16 a[50:51], v186 offset:1024\n" \
  "ds_read_b64_tr_b16 a[52:53], v186 offset:8192\n" \
  "ds_read_b64_tr_b16 a[54:55], v186 offset:9216\n" \
  "v_add_u32 v187, 114688, v183\n" \
  "ds_read_b64_tr_b16 a[120:121], v187\n" \
  "ds_read_b64_tr_b16 a[122:123], v187 offset:1024\n" \
  "ds_read_b64_tr_b16 a[124:125], v187 offset:8192\n" \
  "ds_read_b64_tr_b16 a[126:127], v187 offset:9216\n" \
  "v_add_u32 v186, 131072, v182\n" \
  "ds_read_b64_tr_b16 a[56:57], v186\n" \
  "ds_read_b64_tr_b16 a[58:59], v186 offset:1024\n" \
  "ds_read_b64_tr_b16 a[60:61], v186 offset:8192\n" \
  "ds_read_b64_tr_b16 a[62:63], v186 offset:9216\n" \
  "v_add_u32 v187, 131072, v183\n" \
  "ds_read_b64_tr_b16 a[128:129], v187\n" \
  "ds_read_b64_tr_b16 a[130:131], v187 offset:1024\n" \
  "ds_read_b64_tr_b16 a[132:133], v187 offset:8192\n" \
  "ds_read_b64_tr_b16 a[134:135], v187 offset:9216\n" \
  "v_add_u32 v186, 147456, v182\n" \
  "ds_read_b64_tr_b16 a[64:65], v186\n" \
  "ds_read_b64_tr_b16 a[66:67], v186 offset:1024\n" \
  "ds_read_b64_tr_b16 a[68:69], v186 offset:8192\n" \
  "ds_read_b64_tr_b16 a[70:71], v186 offset:9216\n" \
  "v_add_u32 v187, 147456, v183\n" \
  "ds_read_b64_tr_b16 a[136:137], v187\n" \
  "ds_read_b64_tr_b16 a[138:139], v187 offset:1024\n" \
  "ds_read_b64_tr_b16 a[140:141], v187 offset:8192\n" \
  "ds_read_b64_tr_b16 a[142:143], v187 offset:9216\n" \
  "s_waitcnt lgkmcnt(0)\n" \
  "s_mov_b32 s69, 0\n" \
  "s_waitcnt vmcnt(9)\n" \
  "s_branch .LK_go1_%=\n" \
  ".LK_go_%=:\n" \
  "buffer_load_dwordx2 v[184:185], off, s[40:43], %41 sc1\n" \
  "s_waitcnt vmcnt(0)\n" \
  ".LK_go1_%=:\n" \
  "v_readfirstlane_b32 s96, v184\n" \
  "v_readfirstlane_b32 s63, v185\n" \
  "s_cmp_eq_u32 s63, s55\n" \
  "s_cselect_b32 s96, s96, 0\n" \
  "s_cmp_ge_u32 s96, 0x80000000\n" \
  "s_cbranch_scc1 .LK_go_x_%=\n" \
  "s_sleep 4\n" \
  "s_add_u32 s69, s69, 1\n" \
  "s_cmp_lt_u32 s69, 2097152\n" \
  "s_cbranch_scc1 .LK_go_%=\n" \
  "v_mov_b32 v184, 1\n" \
  "v_mov_b32 v185, s55\n" \
  "buffer_store_dwordx2 v[184:185], off, s[40:43], %42 offset:128 sc1\n" \
  ".LK_go_x_%=:\n" \
  "s_and_b32 s60, s96, 7\n" \
  "s_lshl_b32 s60, s60, 12\n" \
  "s_xor_b32 s53, %39, s60\n" \
  "s_mov_b32 s54, %40\n" \
  "s_andn2_b32 s60, 2, s96\n" \
  "s_lshl_b32 s60, s60, 12\n" \
  "s_add_u32 s49, %37, s60\n" \
  "s_add_u32 s50, %38, s60\n" \
  "s_xor_b32 s66, %38, s60\n" \
  "s_xor_b32 s59, %44, s60\n" \
  "s_or_b32 s50, s50, 0x40000000\n" \
  "s_mov_b32 s57, 2130706432\n" \
  "s_mov_b32 s48, 0\n" \
  "v_mov_b32 v169, s55\n" \
  "v_mov_b32 v167, 2130706432\n" \
  "v_mov_b32 v166, -1\n" \
  "s_mov_b64 exec, 3\n" \
  "v_mov_b32 v167, s54\n" \
  "s_mov_b64 exec, 1\n" \
  "v_mov_b32 v167, s53\n" \
  "v_mov_b32 v166, 0\n" \
  "s_mov_b64 exec, -1\n" \
  "s_barrier\n" \
  "ds_read_b128 v[32:35], v191\n" \
  "ds_read_b128 v[36:39], v191 offset:18432\n" \
  "ds_read_b128 v[40:43], v192\n" \
  "ds_read_b128 v[44:47], v192 offset:18432\n" \
  "ds_read_b128 v[48:51], v191 offset:128\n" \
  "ds_read_b128 v[52:55], v191 offset:18560\n" \
  "s_waitcnt lgkmcnt(0)\n" \
  "s_sub_u32 s49, s49, 524288\n" \
  "s_or_b32 s49, s49, 0x40000000\n" \
  "s_mov_b32 s56, -1\n" \
  "s_mov_b32 s57, 0x10000\n" \
  ".LK_ma_%=:\n" \
  "v_mfma_f32_16x16x32_bf16 v[64:67], a[0:3], v[32:35], 0\n" \
  "ds_read_b128 v[56:59], v192 offset:128\n" \
  "v_mfma_f32_16x16x32_bf16 v[72:75], a[0:3], v[36:39], 0\n" \
  "ds_read_b128 v[60:63], v192 offset:18560\n" \
  "s_lshr_b32 s62, s49, 8\n" \
  "v_mfma_f32_16x16x32_bf16 v[68:71], a[72:75], v[32:35], 0\n" \
  "s_add_u32 m0, s52, 0\n" \
  "s_and_b32 s62, s62, 8192\n" \
  "s_xor_b32 s62, s62, s49\n" \
  "v_mfma_f32_16x16x32_bf16 v[76:79], a[72:75], v[36:39], 0\n" \
  "buffer_load_dwordx4 v196, s[44:47], s51 offen lds\n" \
  "v_mfma_f32_16x16x32_bf16 v[64:67], a[4:7], v[40:43], v[64:67]\n" \
  "ds_read_b128 v[32:35], v191 offset:256\n" \
  "v_cvt_pk_bf16_f32 v96, v80, v81\n" \
  "v_mfma_f32_16x16x32_bf16 v[72:75], a[4:7], v[44:47], v[72:75]\n" \
  "ds_read_b128 v[36:39], v191 offset:18688\n" \
  "v_cvt_pk_bf16_f32 v97, v82, v83\n" \
  "v_mfma_f32_16x16x32_bf16 v[68:71], a[76:79], v[40:43], v[68:71]\n" \
  "v_cvt_pk_bf16_f32 v98, v84, v85\n" \
  "v_cvt_pk_bf16_f32 v99, v86, v87\n" \
  "s_lshl_b32 s63, s57, 14\n" \
  "v_mfma_f32_16x16x32_bf16 v[76:79], a[76:79], v[44:47], v[76:79]\n" \
  "s_xor_b32 s63, s63, s59\n" \
  "v_mfma_f32_16x16x32_bf16 v[64:67], a[8:11], v[48:51], v[64:67]\n" \
  "ds_read_b128 v[40:43], v192 offset:256\n" \
  "v_mfma_f32_16x16x32_bf16 v[72:75], a[8:11], v[52:55], v[72:75]\n" \
  "ds_read_b128 v[44:47], v192 offset:18688\n" \
  "v_mfma_f32_16x16x32_bf16 v[68:71], a[80:83], v[48:51], v[68:71]\n" \
  "s_add_u32 m0, s52, 1024\n" \
  "v_mfma_f32_16x16x32_bf16 v[76:79], a[80:83], v[52:55], v[76:79]\n" \
  "buffer_load_dwordx4 v197, s[44:47], s51 offen lds\n" \
  "s_waitcnt lgkmcnt(5)\n" \
  "v_mfma_f32_16x16x32_bf16 v[64:67], a[12:15], v[56:59], v[64:67]\n" \
  "ds_read_b128 v[48:51], v191 offset:384\n" \
  "s_waitcnt lgkmcnt(5)\n" \
  "v_mfma_f32_16x16x32_bf16 v[72:75], a[12:15], v[60:63], v[72:75]\n" \
  "ds_read_b128 v[52:55], v191 offset:18816\n" \
  "v_mfma_f32_16x16x32_bf16 v[68:71], a[84:87], v[56:59], v[68:71]\n" \
  "buffer_store_dwordx4 v[96:99], v190, s[40:43], s62 offen sc1\n" \
  "v_mfma_f32_16x16x32_bf16 v[76:79], a[84:87], v[60:63], v[76:79]\n" \
  "buffer_store_dwordx4 v[96:99], v190, s[40:43], s63 offen sc1\n" \
  "s_waitcnt lgkmcnt(5)\n" \
  "v_mfma_f32_16x16x32_bf16 v[64:67], a[16:19], v[32:35], v[64:67]\n" \
  "ds_read_b128 v[56:59], v192 offset:384\n" \
  "s_waitcnt lgkmcnt(5)\n" \
  "v_cvt_pk_bf16_f32 v100, v88, v89\n" \
  "v_mfma_f32_16x16x32_bf16 v[72:75], a[16:19], v[36:39], v[72:75]\n" \
  "ds_read_b128 v[60:63], v192 offset:18816\n" \
  "v_cvt_pk_bf16_f32 v101, v90, v91\n" \
  "v_mfma_f32_16x16x32_bf16 v[68:71], a[88:91], v[32:35], v[68:71]\n" \
  "s_add_u32 m0, s52, 2048\n" \
  "v_cvt_pk_bf16_f32 v102, v92, v93\n" \
  "v_cvt_pk_bf16_f32 v103, v94, v95\n" \
  "v_mfma_f32_16x16x32_bf16 v[76:79], a[88:91], v[36:39], v[76:79]\n" \
  "buffer_load_dwordx4 v198, s[44:47], s51 offen lds\n" \
  "s_waitcnt lgkmcnt(5)\n" \
  "v_mfma_f32_16x16x32_bf16 v[64:67], a[20:23], v[40:43], v[64:67]\n" \
  "ds_read_b128 v[32:35], v191 offset:512\n" \
  "s_waitcnt lgkmcnt(5)\n" \
  "v_mfma_f32_16x16x32_bf16 v[72:75], a[20:23], v[44:47], v[72:75]\n" \
  "ds_read_b128 v[36:39], v191 offset:18944\n" \
  "v_mfma_f32_16x16x32_bf16 v[68:71], a[92:95], v[40:43], v[68:71]\n" \
  "buffer_store_dwordx4 v[100:103], v190, s[40:43], s62 offen offset:1024 sc1\n" \
  "v_mfma_f32_16x16x32_bf16 v[76:79], a[92:95], v[44:47], v[76:79]\n" \
  "buffer_store_dwordx4 v[100:103], v190, s[40:43], s63 offen offset:1024 sc1\n" \
  "s_waitcnt lgkmcnt(5)\n" \
  "v_mfma_f32_16x16x32_bf16 v[64:67], a[24:27], v[48:51], v[64:67]\n" \
  "ds_read_b128 v[40:43], v192 offset:512\n" \
  "s_add_u32 s49, s49, 524288\n" \
  "s_waitcnt lgkmcnt(5)\n" \
  "v_mfma_f32_16x16x32_bf16 v[72:75], a[24:27], v[52:55], v[72:75]\n" \
  "ds_read_b128 v[44:47], v192 offset:18944\n" \
  "s_bitset0_b32 s49, 30\n" \
  "v_mfma_f32_16x16x32_bf16 v[68:71], a[96:99], v[48:51], v[68:71]\n" \
  "s_add_u32 m0, s52, 3072\n" \
  "s_add_u32 s58, s48, 1\n" \
  "v_mfma_f32_16x16x32_bf16 v[76:79], a[96:99], v[52:55], v[76:79]\n" \
  "buffer_load_dwordx4 v199, s[44:47], s51 offen lds\n" \
  "s_waitcnt lgkmcnt(5)\n" \
  "v_mfma_f32_16x16x32_bf16 v[64:67], a[28:31], v[56:59], v[64:67]\n" \
  "ds_read_b128 v[48:51], v191 offset:640\n" \
  "s_waitcnt lgkmcnt(5)\n" \
  "v_mfma_f32_16x16x32_bf16 v[72:75], a[28:31], v[60:63], v[72:75]\n" \
  "ds_read_b128 v[52:55], v191 offset:19072\n" \
  "v_mfma_f32_16x16x32_bf16 v[68:71], a[100:103], v[56:59], v[68:71]\n" \
  "s_and_b32 s58, s58, 3\n" \
  "s_cselect_b32 s58, 0, 147456\n" \
  "s_sub_u32 s58, 36864, s58\n" \
  "v_mfma_f32_16x16x32_bf16 v[76:79], a[100:103], v[60:63], v[76:79]\n" \
  "s_waitcnt lgkmcnt(5)\n" \
  "v_add_u32 v165, s58, v191\n" \
  "v_add_u32 v164, s58, v192\n" \
  "v_mfma_f32_16x16x32_bf16 v[64:67], a[32:35], v[32:35], v[64:67]\n" \
  "ds_read_b128 v[56:59], v192 offset:640\n" \
  "s_waitcnt lgkmcnt(5)\n" \
  "v_mfma_f32_16x16x32_bf16 v[72:75], a[32:35], v[36:39], v[72:75]\n" \
  "ds_read_b128 v[60:63], v192 offset:19072\n" \
  "v_mfma_f32_16x16x32_bf16 v[68:71], a[104:107], v[32:35], v[68:71]\n" \
  "s_add_u32 m0, s52, 4096\n" \
  "v_mfma_f32_16x16x32_bf16 v[76:79], a[104:107], v[36:39], v[76:79]\n" \
  "buffer_load_dwordx4 v200, s[44:47], s51 offen lds\n" \
  "s_waitcnt lgkmcnt(5)\n" \
  "v_mfma_f32_16x16x32_bf16 v[64:67], a[36:39], v[40:43], v[64:67]\n" \
  "ds_read_b128 v[32:35], v191 offset:768\n" \
  "s_waitcnt lgkmcnt(5)\n" \
  "v_mfma_f32_16x16x32_bf16 v[72:75], a[36:39], v[44:47], v[72:75]\n" \
  "ds_read_b128 v[36:39], v191 offset:19200\n" \
  "v_mfma_f32_16x16x32_bf16 v[68:71], a[108:111], v[40:43], v[68:71]\n" \
  "v_mfma_f32_16x16x32_bf16 v[76:79], a[108:111], v[44:47], v[76:79]\n" \
  "s_waitcnt lgkmcnt(5)\n" \
  "v_mfma_f32_16x16x32_bf16 v[64:67], a[40:43], v[48:51], v[64:67]\n" \
  "ds_read_b128 v[40:43], v192 offset:768\n" \
  "s_waitcnt lgkmcnt(5)\n" \
  "v_mfma_f32_16x16x32_bf16 v[72:75], a[40:43], v[52:55], v[72:75]\n" \
  "ds_read_b128 v[44:47], v192 offset:19200\n" \
  "v_mfma_f32_16x16x32_bf16 v[68:71], a[112:115], v[48:51], v[68:71]\n" \
  "s_add_u32 m0, s52, 5120\n" \
  "v_mfma_f32_16x16x32_bf16 v[76:79], a[112:115], v[52:55], v[76:79]\n" \
  "buffer_load_dwordx4 v201, s[44:47], s51 offen lds\n" \
  "s_waitcnt lgkmcnt(5)\n" \
  "v_mfma_f32_16x16x32_bf16 v[64:67], a[44:47], v[56:59], v[64:67]\n" \
  "ds_read_b128 v[48:51], v191 offset:896\n" \
  "s_waitcnt lgkmcnt(5)\n" \
  "v_mfma_f32_16x16x32_bf16 v[72:75], a[44:47], v[60:63], v[72:75]\n" \
  "ds_read_b128 v[52:55], v191 offset:19328\n" \
  "v_mfma_f32_16x16x32_bf16 v[68:71], a[116:119], v[56:59], v[68:71]\n" \
  "v_mfma_f32_16x16x32_bf16 v[76:79], a[116:119], v[60:63], v[76:79]\n" \
  "s_waitcnt lgkmcnt(5)\n" \
  "v_mfma_f32_16x16x32_bf16 v[64:67], a[48:51], v[32:35], v[64:67]\n" \
  "ds_read_b128 v[56:59], v192 offset:896\n" \
  "s_waitcnt lgkmcnt(5)\n" \
  "v_mfma_f32_16x16x32_bf16 v[72:75], a[48:51], v[36:39], v[72:75]\n" \
  "ds_read_b128 v[60:63], v192 offset:19328\n" \
  "v_mfma_f32_16x16x32_bf16 v[68:71], a[120:123], v[32:35], v[68:71]\n" \
  "s_add_u32 m0, s52, 6144\n" \
  "v_mfma_f32_16x16x32_bf16 v[76:79], a[120:123], v[36:39], v[76:79]\n" \
  "buffer_load_dwordx4 v202, s[44:47], s51 offen lds\n" \
  "s_waitcnt lgkmcnt(5)\n" \
  "v_mfma_f32_16x16x32_bf16 v[64:67], a[52:55], v[40:43], v[64:67]\n" \
  "ds_read_b128 v[32:35], v191 offset:1024\n" \
  "s_waitcnt lgkmcnt(5)\n" \
  "v_mfma_f32_16x16x32_bf16 v[72:75], a[52:55], v[44:47], v[72:75]\n" \
  "ds_read_b128 v[36:39], v191 offset:19456\n" \
  "v_mfma_f32_16x16x32_bf16 v[68:71], a[124:127], v[40:43], v[68:71]\n" \
  "s_waitcnt vmcnt(11)\n" \
  "s_barrier\n" \
  "v_mfma_f32_16x16x32_bf16 v[76:79], a[124:127], v[44:47], v[76:79]\n" \
  "v_mov_b32 v168, s56\n" \
  "s_mov_b64 exec, 1\n" \
  "buffer_store_dwordx2 v[168:169], v167, s[40:43], 0 offen sc1\n" \
  "s_mov_b64 exec, -1\n" \
  "s_waitcnt lgkmcnt(5)\n" \
  "v_mfma_f32_16x16x32_bf16 v[64:67], a[56:59], v[48:51], v[64:67]\n" \
  "ds_read_b128 v[40:43], v192 offset:1024\n" \
  "s_waitcnt lgkmcnt(5)\n" \
  "v_mfma_f32_16x16x32_bf16 v[72:75], a[56:59], v[52:55], v[72:75]\n" \
  "ds_read_b128 v[44:47], v192 offset:19456\n" \
  "v_mfma_f32_16x16x32_bf16 v[68:71], a[128:131], v[48:51], v[68:71]\n" \
  "s_add_u32 m0, s52, 7168\n" \
  "v_mfma_f32_16x16x32_bf16 v[76:79], a[128:131], v[52:55], v[76:79]\n" \
  "buffer_load_dwordx4 v203, s[44:47], s51 offen lds\n" \
  "s_waitcnt lgkmcnt(5)\n" \
  "v_mfma_f32_16x16x32_bf16 v[64:67], a[60:63], v[56:59], v[64:67]\n" \
  "ds_read_b128 v[48:51], v165\n" \
  "s_waitcnt lgkmcnt(5)\n" \
  "v_mfma_f32_16x16x32_bf16 v[72:75], a[60:63], v[60:63], v[72:75]\n" \
  "ds_read_b128 v[52:55], v165 offset:18432\n" \
  "v_mfma_f32_16x16x32_bf16 v[68:71], a[132:135], v[56:59], v[68:71]\n" \
  "v_mfma_f32_16x16x32_bf16 v[76:79], a[132:135], v[60:63], v[76:79]\n" \
  "s_waitcnt lgkmcnt(5)\n" \
  "v_mfma_f32_16x16x32_bf16 v[64:67], a[64:67], v[32:35], v[64:67]\n" \
  "ds_read_b128 v[56:59], v164\n" \
  "s_waitcnt lgkmcnt(5)\n" \
  "v_mfma_f32_16x16x32_bf16 v[72:75], a[64:67], v[36:39], v[72:75]\n" \
  "ds_read_b128 v[60:63], v164 offset:18432\n" \
  "v_mfma_f32_16x16x32_bf16 v[68:71], a[136:139], v[32:35], v[68:71]\n" \
  "s_add_u32 m0, s52, 8192\n" \
  "v_mfma_f32_16x16x32_bf16 v[76:79], a[136:139], v[36:39], v[76:79]\n" \
  "buffer_load_dwordx4 v204, s[44:47], s51 offen lds\n" \
  "s_waitcnt lgkmcnt(5)\n" \
  "v_mfma_f32_16x16x32_bf16 v[64:67], a[68:71], v[40:43], v[64:67]\n" \
  "ds_read_b128 v[32:35], v165 offset:128\n" \
  "s_add_u32 s51, s51, 36864\n" \
  "s_waitcnt lgkmcnt(5)\n" \
  "v_mfma_f32_16x16x32_bf16 v[72:75], a[68:71], v[44:47], v[72:75]\n" \
  "ds_read_b128 v[36:39], v165 offset:18560\n" \
  "s_min_u32 s51, s51, 2322432\n" \
  "v_mfma_f32_16x16x32_bf16 v[68:71], a[140:143], v[40:43], v[68:71]\n" \
  "s_add_u32 s52, s52, 36864\n" \
  "v_mfma_f32_16x16x32_bf16 v[76:79], a[140:143], v[44:47], v[76:79]\n" \
  "s_cmp_ge_u32 s52, 163840\n" \
  "s_cselect_b32 s60, 147456, 0\n" \
  "s_sub_u32 s52, s52, s60\n" \
  "v_mov_b32 v191, v165\n" \
  "v_mov_b32 v192, v164\n" \
  "s_cmp_le_u32 s48, 3\n" \
  "s_cbranch_scc1 .LK_mea_%=\n" \
  ".LK_mea_b_%=:\n" \
  "s_mov_b32 s56, s48\n" \
  "s_mov_b32 s57, s48\n" \
  "s_add_u32 s48, s48, 1\n" \
  "s_waitcnt lgkmcnt(5)\n" \
  "v_mfma_f32_16x16x32_bf16 v[80:83], a[0:3], v[48:51], 0\n" \
  "ds_read_b128 v[40:43], v192 offset:128\n" \
  "s_waitcnt lgkmcnt(5)\n" \
  "v_mfma_f32_16x16x32_bf16 v[88:91], a[0:3], v[52:55], 0\n" \
  "ds_read_b128 v[44:47], v192 offset:18560\n" \
  "s_lshr_b32 s62, s49, 8\n" \
  "v_mfma_f32_16x16x32_bf16 v[84:87], a[72:75], v[48:51], 0\n" \
  "s_add_u32 m0, s52, 0\n" \
  "s_and_b32 s62, s62, 8192\n" \
  "s_xor_b32 s62, s62, s49\n" \
  "v_mfma_f32_16x16x32_bf16 v[92:95], a[72:75], v[52:55], 0\n" \
  "buffer_load_dwordx4 v196, s[44:47], s51 offen lds\n" \
  "s_waitcnt lgkmcnt(5)\n" \
  "v_mfma_f32_16x16x32_bf16 v[80:83], a[4:7], v[56:59], v[80:83]\n" \
  "ds_read_b128 v[48:51], v191 offset:256\n" \
  "s_waitcnt lgkmcnt(5)\n" \
  "v_cvt_pk_bf16_f32 v96, v64, v65\n" \
  "v_mfma_f32_16x16x32_bf16 v[88:91], a[4:7], v[60:63], v[88:91]\n" \
  "ds_read_b128 v[52:55], v191 offset:18688\n" \
  "v_cvt_pk_bf16_f32 v97, v66, v67\n" \
  "v_mfma_f32_16x16x32_bf16 v[84:87], a[76:79], v[56:59], v[84:87]\n" \
  "v_cvt_pk_bf16_f32 v98, v68, v69\n" \
  "v_cvt_pk_bf16_f32 v99, v70, v71\n" \
  "s_lshl_b32 s63, s57, 14\n" \
  "v_mfma_f32_16x16x32_bf16 v[92:95], a[76:79], v[60:63], v[92:95]\n" \
  "s_xor_b32 s63, s63, s59\n" \
  "s_waitcnt lgkmcnt(5)\n" \
  "v_mfma_f32_16x16x32_bf16 v[80:83], a[8:11], v[32:35], v[80:83]\n" \
  "ds_read_b128 v[56:59], v192 offset:256\n" \
  "s_waitcnt lgkmcnt(5)\n" \
  "v_mfma_f32_16x16x32_bf16 v[88:91], a[8:11], v[36:39], v[88:91]\n" \
  "ds_read_b128 v[60:63], v192 offset:18688\n" \
  "v_mfma_f32_16x16x32_bf16 v[84:87], a[80:83], v[32:35], v[84:87]\n" \
  "s_add_u32 m0, s52, 1024\n" \
  "v_mfma_f32_16x16x32_bf16 v[92:95], a[80:83], v[36:39], v[92:95]\n" \
  "buffer_load_dwordx4 v197, s[44:47], s51 offen lds\n" \
  "s_waitcnt lgkmcnt(5)\n" \
  "v_mfma_f32_16x16x32_bf16 v[80:83], a[12:15], v[40:43], v[80:83]\n" \
  "ds_read_b128 v[32:35], v191 offset:384\n" \
  "s_waitcnt lgkmcnt(5)\n" \
  "v_mfma_f32_16x16x32_bf16 v[88:91], a[12:15], v[44:47], v[88:91]\n" \
  "ds_read_b128 v[36:39], v191 offset:18816\n" \
  "v_mfma_f32_16x16x32_bf16 v[84:87], a[84:87], v[40:43], v[84:87]\n" \
  "buffer_store_dwordx4 v[96:99], v190, s[40:43], s62 offen sc1\n" \
  "v_mfma_f32_16x16x32_bf16 v[92:95], a[84:87], v[44:47], v[92:95]\n" \
  "buffer_store_dwordx4 v[96:99], v190, s[40:43], s63 offen sc1\n" \
  "s_waitcnt lgkmcnt(5)\n" \
  "v_mfma_f32_16x16x32_bf16 v[80:83], a[16:19], v[48:51], v[80:83]\n" \
  "ds_read_b128 v[40:43], v192 offset:384\n" \
  "s_waitcnt lgkmcnt(5)\n" \
  "v_cvt_pk_bf16_f32 v100, v72, v73\n" \
  "v_mfma_f32_16x16x32_bf16 v[88:91], a[16:19], v[52:55], v[88:91]\n" \
  "ds_read_b128 v[44:47], v192 offset:18816\n" \
  "v_cvt_pk_bf16_f32 v101, v74, v75\n" \
  "v_mfma_f32_16x16x32_bf16 v[84:87], a[88:91], v[48:51], v[84:87]\n" \
  "s_add_u32 m0, s52, 2048\n" \
  "v_cvt_pk_bf16_f32 v102, v76, v77\n" \
  "v_cvt_pk_bf16_f32 v103, v78, v79\n" \
  "v_mfma_f32_16x16x32_bf16 v[92:95], a[88:91], v[52:55], v[92:95]\n" \
  "buffer_load_dwordx4 v198, s[44:47], s51 offen lds\n" \
  "s_waitcnt lgkmcnt(5)\n" \
  "v_mfma_f32_16x16x32_bf16 v[80:83], a[20:23], v[56:59], v[80:83]\n" \
  "ds_read_b128 v[48:51], v191 offset:512\n" \
  "s_waitcnt lgkmcnt(5)\n" \
  "v_mfma_f32_16x16x32_bf16 v[88:91], a[20:23], v[60:63], v[88:91]\n" \
  "ds_read_b128 v[52:55], v191 offset:18944\n" \
  "v_mfma_f32_16x16x32_bf16 v[84:87], a[92:95], v[56:59], v[84:87]\n" \
  "buffer_store_dwordx4 v[100:103], v190, s[40:43], s62 offen offset:1024 sc1\n" \
  "v_mfma_f32_16x16x32_bf16 v[92:95], a[92:95], v[60:63], v[92:95]\n" \
  "buffer_store_dwordx4 v[100:103], v190, s[40:43], s63 offen offset:1024 sc1\n" \
  "s_waitcnt lgkmcnt(5)\n" \
  "v_mfma_f32_16x16x32_bf16 v[80:83], a[24:27], v[32:35], v[80:83]\n" \
  "ds_read_b128 v[56:59], v192 offset:512\n" \
  "s_add_u32 s49, s49, 524288\n" \
  "s_waitcnt lgkmcnt(5)\n" \
  "v_mfma_f32_16x16x32_bf16 v[88:91], a[24:27], v[36:39], v[88:91]\n" \
  "ds_read_b128 v[60:63], v192 offset:18944\n" \
  "s_bitset0_b32 s49, 30\n" \
  "v_mfma_f32_16x16x32_bf16 v[84:87], a[96:99], v[32:35], v[84:87]\n" \
  "s_add_u32 m0, s52, 3072\n" \
  "s_add_u32 s58, s48, 1\n" \
  "v_mfma_f32_16x16x32_bf16 v[92:95], a[96:99], v[36:39], v[92:95]\n" \
  "buffer_load_dwordx4 v199, s[44:47], s51 offen lds\n" \
  "s_waitcnt lgkmcnt(5)\n" \
  "v_mfma_f32_16x16x32_bf16 v[80:83], a[28:31], v[40:43], v[80:83]\n" \
  "ds_read_b128 v[32:35], v191 offset:640\n" \
  "s_waitcnt lgkmcnt(5)\n" \
  "v_mfma_f32_16x16x32_bf16 v[88:91], a[28:31], v[44:47], v[88:91]\n" \
  "ds_read_b128 v[36:39], v191 offset:19072\n" \
  "v_mfma_f32_16x16x32_bf16 v[84:87], a[100:103], v[40:43], v[84:87]\n" \
  "s_and_b32 s58, s58, 3\n" \
  "s_cselect_b32 s58, 0, 147456\n" \
  "s_sub_u32 s58, 36864, s58\n" \
  "v_mfma_f32_16x16x32_bf16 v[92:95], a[100:103], v[44:47], v[92:95]\n" \
  "s_waitcnt lgkmcnt(5)\n" \
  "v_add_u32 v165, s58, v191\n" \
  "v_add_u32 v164, s58, v192\n" \
  "v_mfma_f32_16x16x32_bf16 v[80:83], a[32:35], v[48:51], v[80:83]\n" \
  "ds_read_b128 v[40:43], v192 offset:640\n" \
  "s_waitcnt lgkmcnt(5)\n" \
  "v_mfma_f32_16x16x32_bf16 v[88:91], a[32:35], v[52:55], v[88:91]\n" \
  "ds_read_b128 v[44:47], v192 offset:19072\n" \
  "v_mfma_f32_16x16x32_bf16 v[84:87], a[104:107], v[48:51], v[84:87]\n" \
  "s_add_u32 m0, s52, 4096\n" \
  "v_mfma_f32_16x16x32_bf16 v[92:95], a[104:107], v[52:55], v[92:95]\n" \
  "buffer_load_dwordx4 v200, s[44:47], s51 offen lds\n" \
  "s_waitcnt lgkmcnt(5)\n" \
  "v_mfma_f32_16x16x32_bf16 v[80:83], a[36:39], v[56:59], v[80:83]\n" \
  "ds_read_b128 v[48:51], v191 offset:768\n" \
  "s_waitcnt lgkmcnt(5)\n" \
  "v_mfma_f32_16x16x32_bf16 v[88:91], a[36:39], v[60:63], v[88:91]\n" \
  "ds_read_b128 v[52:55], v191 offset:19200\n" \
  "v_mfma_f32_16x16x32_bf16 v[84:87], a[108:111], v[56:59], v[84:87]\n" \
  "v_mfma_f32_16x16x32_bf16 v[92:95], a[108:111], v[60:63], v[92:95]\n" \
  "s_waitcnt lgkmcnt(5)\n" \
  "v_mfma_f32_16x16x32_bf16 v[80:83], a[40:43], v[32:35], v[80:83]\n" \
  "ds_read_b128 v[56:59], v192 offset:768\n" \
  "s_waitcnt lgkmcnt(5)\n" \
  "v_mfma_f32_16x16x32_bf16 v[88:91], a[40:43], v[36:39], v[88:91]\n" \
  "ds_read_b128 v[60:63], v192 offset:19200\n" \
  "v_mfma_f32_16x16x32_bf16 v[84:87], a[112:115], v[32:35], v[84:87]\n" \
  "s_add_u32 m0, s52, 5120\n" \
  "v_mfma_f32_16x16x32_bf16 v[92:95], a[112:115], v[36:39], v[92:95]\n" \
  "buffer_load_dwordx4 v201, s[44:47], s51 offen lds\n" \
  "s_waitcnt lgkmcnt(5)\n" \
  "v_mfma_f32_16x16x32_bf16 v[80:83], a[44:47], v[40:43], v[80:83]\n" \
  "ds_read_b128 v[32:35], v191 offset:896\n" \
  "s_waitcnt lgkmcnt(5)\n" \
  "v_mfma_f32_16x16x32_bf16 v[88:91], a[44:47], v[44:47], v[88:91]\n" \
  "ds_read_b128 v[36:39], v191 offset:19328\n" \
  "v_mfma_f32_16x16x32_bf16 v[84:87], a[116:119], v[40:43], v[84:87]\n" \
  "v_mfma_f32_16x16x32_bf16 v[92:95], a[116:119], v[44:47], v[92:95]\n" \
  "s_waitcnt lgkmcnt(5)\n" \
  "v_mfma_f32_16x16x32_bf16 v[80:83], a[48:51], v[48:51], v[80:83]\n" \
  "ds_read_b128 v[40:43], v192 offset:896\n" \
  "s_waitcnt lgkmcnt(5)\n" \
  "v_mfma_f32_16x16x32_bf16 v[88:91], a[48:51], v[52:55], v[88:91]\n" \
  "ds_read_b128 v[44:47], v192 offset:19328\n" \
  "v_mfma_f32_16x16x32_bf16 v[84:87], a[120:123], v[48:51], v[84:87]\n" \
  "s_add_u32 m0, s52, 6144\n" \
  "v_mfma_f32_16x16x32_bf16 v[92:95], a[120:123], v[52:55], v[92:95]\n" \
  "buffer_load_dwordx4 v202, s[44:47], s51 offen lds\n" \
  "s_waitcnt lgkmcnt(5)\n" \
  "v_mfma_f32_16x16x32_bf16 v[80:83], a[52:55], v[56:59], v[80:83]\n" \
  "ds_read_b128 v[48:51], v191 offset:1024\n" \
  "s_waitcnt lgkmcnt(5)\n" \
  "v_mfma_f32_16x16x32_bf16 v[88:91], a[52:55], v[60:63], v[88:91]\n" \
  "ds_read_b128 v[52:55], v191 offset:19456\n" \
  "v_mfma_f32_16x16x32_bf16 v[84:87], a[124:127], v[56:59], v[84:87]\n" \
  "s_waitcnt vmcnt(11)\n" \
  "s_barrier\n" \
  "v_mfma_f32_16x16x32_bf16 v[92:95], a[124:127], v[60:63], v[92:95]\n" \
  "v_mov_b32 v168, s56\n" \
  "s_mov_b64 exec, 1\n" \
  "buffer_store_dwordx2 v[168:169], v167, s[40:43], 0 offen sc1\n" \
  "s_mov_b64 exec, -1\n" \
  "s_waitcnt lgkmcnt(5)\n" \
  "v_mfma_f32_16x16x32_bf16 v[80:83], a[56:59], v[32:35], v[80:83]\n" \
  "ds_read_b128 v[56:59], v192 offset:1024\n" \
  "s_waitcnt lgkmcnt(5)\n" \
  "v_mfma_f32_16x16x32_bf16 v[88:91], a[56:59], v[36:39], v[88:91]\n" \
  "ds_read_b128 v[60:63], v192 offset:19456\n" \
  "v_mfma_f32_16x16x32_bf16 v[84:87], a[128:131], v[32:35], v[84:87]\n" \
  "s_add_u32 m0, s52, 7168\n" \
  "v_mfma_f32_16x16x32_bf16 v[92:95], a[128:131], v[36:39], v[92:95]\n" \
  "buffer_load_dwordx4 v203, s[44:47], s51 offen lds\n" \
  "s_waitcnt lgkmcnt(5)\n" \
  "v_mfma_f32_16x16x32_bf16 v[80:83], a[60:63], v[40:43], v[80:83]\n" \
  "ds_read_b128 v[32:35], v165\n" \
  "s_waitcnt lgkmcnt(5)\n" \
  "v_mfma_f32_16x16x32_bf16 v[88:91], a[60:63], v[44:47], v[88:91]\n" \
  "ds_read_b128 v[36:39], v165 offset:18432\n" \
  "v_mfma_f32_16x16x32_bf16 v[84:87], a[132:135], v[40:43], v[84:87]\n" \
  "v_mfma_f32_16x16x32_bf16 v[92:95], a[132:135], v[44:47], v[92:95]\n" \
  "s_waitcnt lgkmcnt(5)\n" \
  "v_mfma_f32_16x16x32_bf16 v[80:83], a[64:67], v[48:51], v[80:83]\n" \
  "ds_read_b128 v[40:43], v164\n" \
  "s_waitcnt lgkmcnt(5)\n" \
  "v_mfma_f32_16x16x32_bf16 v[88:91], a[64:67], v[52:55], v[88:91]\n" \
  "ds_read_b128 v[44:47], v164 offset:18432\n" \
  "v_mfma_f32_16x16x32_bf16 v[84:87], a[136:139], v[48:51], v[84:87]\n" \
  "s_add_u32 m0, s52, 8192\n" \
  "v_mfma_f32_16x16x32_bf16 v[92:95], a[136:139], v[52:55], v[92:95]\n" \
  "buffer_load_dwordx4 v204, s[44:47], s51 offen lds\n" \
  "s_waitcnt lgkmcnt(5)\n" \
  "v_mfma_f32_16x16x32_bf16 v[80:83], a[68:71], v[56:59], v[80:83]\n" \
  "ds_read_b128 v[48:51], v165 offset:128\n" \
  "s_add_u32 s51, s51, 36864\n" \
  "s_waitcnt lgkmcnt(5)\n" \
  "v_mfma_f32_16x16x32_bf16 v[88:91], a[68:71], v[60:63], v[88:91]\n" \
  "ds_read_b128 v[52:55], v165 offset:18560\n" \
  "s_min_u32 s51, s51, 2322432\n" \
  "v_mfma_f32_16x16x32_bf16 v[84:87], a[140:143], v[56:59], v[84:87]\n" \
  "s_add_u32 s52, s52, 36864\n" \
  "v_mfma_f32_16x16x32_bf16 v[92:95], a[140:143], v[60:63], v[92:95]\n" \
  "s_cmp_ge_u32 s52, 163840\n" \
  "s_cselect_b32 s60, 147456, 0\n" \
  "s_sub_u32 s52, s52, s60\n" \
  "v_mov_b32 v191, v165\n" \
  "v_mov_b32 v192, v164\n" \
  "s_cmp_le_u32 s48, 3\n" \
  "s_cbranch_scc1 .LK_meb_%=\n" \
  ".LK_meb_b_%=:\n" \
  "s_mov_b32 s56, s48\n" \
  "s_mov_b32 s57, s48\n" \
  "s_add_u32 s48, s48, 1\n" \
  "s_cmp_lt_u32 s48, 16\n" \
  "s_waitcnt lgkmcnt(0)\n" \
  "s_cbranch_scc1 .LK_ma_%=\n" \
  "s_nop 7\n" \
  "s_lshr_b32 s62, s49, 8\n" \
  "s_and_b32 s62, s62, 8192\n" \
  "s_xor_b32 s62, s62, s49\n" \
  "v_cvt_pk_bf16_f32 v96, v80, v81\n" \
  "v_cvt_pk_bf16_f32 v97, v82, v83\n" \
  "v_cvt_pk_bf16_f32 v98, v84, v85\n" \
  "v_cvt_pk_bf16_f32 v99, v86, v87\n" \
  "s_lshl_b32 s63, s56, 14\n" \
  "s_xor_b32 s63, s63, s59\n" \
  "buffer_store_dwordx4 v[96:99], v190, s[40:43], s62 offen sc1\n" \
  "buffer_store_dwordx4 v[96:99], v190, s[40:43], s63 offen sc1\n" \
  "v_cvt_pk_bf16_f32 v100, v88, v89\n" \
  "v_cvt_pk_bf16_f32 v101, v90, v91\n" \
  "v_cvt_pk_bf16_f32 v102, v92, v93\n" \
  "v_cvt_pk_bf16_f32 v103, v94, v95\n" \
  "buffer_store_dwordx4 v[100:103], v190, s[40:43], s62 offen offset:1024 sc1\n" \
  "buffer_store_dwordx4 v[100:103], v190, s[40:43], s63 offen offset:1024 sc1\n" \
  "s_add_u32 s49, s49, 524288\n" \
  "s_waitcnt vmcnt(0)\n" \
  "s_barrier\n" \
  "v_mov_b32 v168, s48\n" \
  "s_mov_b64 exec, 1\n" \
  "buffer_store_dwordx2 v[168:169], v167, s[40:43], 0 offen sc1\n" \
  "s_mov_b64 exec, -1\n" \
  "s_waitcnt vmcnt(0)\n" \
  "s_cmp_eq_u32 %43, 77\n" \
  "s_cbranch_scc1 .LK_end_%=\n" \
  "s_mov_b64 s[44:45], %32\n" \
  "s_and_b32 s45, s45, 0xffff\n" \
  "s_mov_b32 s46, 4718592\n" \
  "s_add_u32 s60, %35, 0\n" \
  "s_add_u32 m0, s72, 16384\n" \
  "s_nop 0\n" \
  "buffer_load_dwordx4 v170, s[44:47], s60 offen lds\n" \
  "buffer_load_dwordx4 v171, s[44:47], s60 offen offset:1024 lds\n" \
  "buffer_load_dwordx4 v172, s[44:47], s60 offen offset:2048 lds\n" \
  "buffer_load_dwordx4 v173, s[44:47], s60 offen offset:3072 lds\n" \
  "s_add_u32 s60, %35, 524288\n" \
  "s_add_u32 m0, s72, 32768\n" \
  "s_nop 0\n" \
  "buffer_load_dwordx4 v170, s[44:47], s60 offen lds\n" \
  "buffer_load_dwordx4 v171, s[44:47], s60 offen offset:1024 lds\n" \
  "buffer_load_dwordx4 v172, s[44:47], s60 offen offset:2048 lds\n" \
  "buffer_load_dwordx4 v173, s[44:47], s60 offen offset:3072 lds\n" \
  "s_add_u32 s60, %35, 1048576\n" \
  "s_add_u32 m0, s72, 49152\n" \
  "s_nop 0\n" \
  "buffer_load_dwordx4 v170, s[44:47], s60 offen lds\n" \
  "buffer_load_dwordx4 v171, s[44:47], s60 offen offset:1024 lds\n" \
  "buffer_load_dwordx4 v172, s[44:47], s60 offen offset:2048 lds\n" \
  "buffer_load_dwordx4 v173, s[44:47], s60 offen offset:3072 lds\n" \
  "s_add_u32 s60, %35, 1572864\n" \
  "s_add_u32 m0, s72, 65536\n" \
  "s_nop 0\n" \
  "buffer_load_dwordx4 v170, s[44:47], s60 offen lds\n" \
  "buffer_load_dwordx4 v171, s[44:47], s60 offen offset:1024 lds\n" \
  "buffer_load_dwordx4 v172, s[44:47], s60 offen offset:2048 lds\n" \
  "buffer_load_dwordx4 v173, s[44:47], s60 offen offset:3072 lds\n" \
  "s_add_u32 s60, %35, 2097152\n" \
  "s_add_u32 m0, s72, 81920\n" \
  "s_nop 0\n" \
  "buffer_load_dwordx4 v170, s[44:47], s60 offen lds\n" \
  "buffer_load_dwordx4 v171, s[44:47], s60 offen offset:1024 lds\n" \
  "buffer_load_dwordx4 v172, s[44:47], s60 offen offset:2048 lds\n" \
  "buffer_load_dwordx4 v173, s[44:47], s60 offen offset:3072 lds\n" \
  "s_add_u32 s60, %35, 2621440\n" \
  "s_add_u32 m0, s72, 98304\n" \
  "s_nop 0\n" \
  "buffer_load_dwordx4 v170, s[44:47], s60 offen lds\n" \
  "buffer_load_dwordx4 v171, s[44:47], s60 offen offset:1024 lds\n" \
  "buffer_load_dwordx4 v172, s[44:47], s60 offen offset:2048 lds\n" \
  "buffer_load_dwordx4 v173, s[44:47], s60 offen offset:3072 lds\n" \
  "s_add_u32 s60, %35, 3145728\n" \
  "s_add_u32 m0, s72, 114688\n" \
  "s_nop 0\n" \
  "buffer_load_dwordx4 v170, s[44:47], s60 offen lds\n" \
  "buffer_load_dwordx4 v171, s[44:47], s60 offen offset:1024 lds\n" \
  "buffer_load_dwordx4 v172, s[44:47], s60 offen offset:2048 lds\n" \
  "buffer_load_dwordx4 v173, s[44:47], s60 offen offset:3072 lds\n" \
  "s_add_u32 s60, %35, 3670016\n" \
  "s_add_u32 m0, s72, 131072\n" \
  "s_nop 0\n" \
  "buffer_load_dwordx4 v170, s[44:47], s60 offen lds\n" \
  "buffer_load_dwordx4 v171, s[44:47], s60 offen offset:1024 lds\n" \
  "buffer_load_dwordx4 v172, s[44:47], s60 offen offset:2048 lds\n" \
  "buffer_load_dwordx4 v173, s[44:47], s60 offen offset:3072 lds\n" \
  "s_add_u32 s60, %35, 4194304\n" \
  "s_add_u32 m0, s72, 147456\n" \
  "s_nop 0\n" \
  "buffer_load_dwordx4 v170, s[44:47], s60 offen lds\n" \
  "buffer_load_dwordx4 v171, s[44:47], s60 offen offset:1024 lds\n" \
  "buffer_load_dwordx4 v172, s[44:47], s60 offen offset:2048 lds\n" \
  "buffer_load_dwordx4 v173, s[44:47], s60 offen offset:3072 lds\n" \
  "s_waitcnt vmcnt(24)\n" \
  "s_barrier\n" \
  "v_add_u32 v186, 16384, v182\n" \
  "ds_read_b64_tr_b16 a[144:145], v186\n" \
  "ds_read_b64_tr_b16 a[146:147], v186 offset:1024\n" \
  "ds_read_b64_tr_b16 a[148:149], v186 offset:8192\n" \
  "ds_read_b64_tr_b16 a[150:151], v186 offset:9216\n" \
  "v_add_u32 v187, 16384, v183\n" \
  "ds_read_b64_tr_b16 a[216:217], v187\n" \
  "ds_read_b64_tr_b16 a[218:219], v187 offset:1024\n" \
  "ds_read_b64_tr_b16 a[220:221], v187 offset:8192\n" \
  "ds_read_b64_tr_b16 a[222:223], v187 offset:9216\n" \
  "v_add_u32 v186, 32768, v182\n" \
  "ds_read_b64_tr_b16 a[152:153], v186\n" \
  "ds_read_b64_tr_b16 a[154:155], v186 offset:1024\n" \
  "ds_read_b64_tr_b16 a[156:157], v186 offset:8192\n" \
  "ds_read_b64_tr_b16 a[158:159], v186 offset:9216\n" \
  "v_add_u32 v187, 32768, v183\n" \
  "ds_read_b64_tr_b16 a[224:225], v187\n" \
  "ds_read_b64_tr_b16 a[226:227], v187 offset:1024\n" \
  "ds_read_b64_tr_b16 a[228:229], v187 offset:8192\n" \
  "ds_read_b64_tr_b16 a[230:231], v187 offset:9216\n" \
  "v_add_u32 v186, 49152, v182\n" \
  "ds_read_b64_tr_b16 a[160:161], v186\n" \
  "ds_read_b64_tr_b16 a[162:163], v186 offset:1024\n" \
  "ds_read_b64_tr_b16 a[164:165], v186 offset:8192\n" \
  "ds_read_b64_tr_b16 a[166:167], v186 offset:9216\n" \
  "v_add_u32 v187, 49152, v183\n" \
  "ds_read_b64_tr_b16 a[232:233], v187\n" \
  "ds_read_b64_tr_b16 a[234:235], v187 offset:1024\n" \
  "ds_read_b64_tr_b16 a[236:237], v187 offset:8192\n" \
  "ds_read_b64_tr_b16 a[238:239], v187 offset:9216\n" \
  "s_waitcnt lgkmcnt(0)\n" \
  "s_barrier\n" \
  "s_mov_b64 s[44:45], %33\n" \
  "s_and_b32 s45, s45, 0xffff\n" \
  "s_mov_b32 s46, 2359296\n" \
  "s_mov_b32 s51, 589824\n" \
  "s_mul_i32 s52, %34, 9216\n" \
  "s_add_u32 s52, s52, 16384\n" \
  "s_add_u32 m0, s52, 0\n" \
  "s_nop 0\n" \
  "buffer_load_dwordx4 v196, s[44:47], s51 offen lds\n" \
  "s_add_u32 m0, s52, 1024\n" \
  "s_nop 0\n" \
  "buffer_load_dwordx4 v197, s[44:47], s51 offen lds\n" \
  "s_add_u32 m0, s52, 2048\n" \
  "s_nop 0\n" \
  "buffer_load_dwordx4 v198, s[44:47], s51 offen lds\n" \
  "s_add_u32 m0, s52, 3072\n" \
  "s_nop 0\n" \
  "buffer_load_dwordx4 v199, s[44:47], s51 offen lds\n" \
  "s_add_u32 m0, s52, 4096\n" \
  "s_nop 0\n" \
  "buffer_load_dwordx4 v200, s[44:47], s51 offen lds\n" \
  "s_add_u32 m0, s52, 5120\n" \
  "s_nop 0\n" \
  "buffer_load_dwordx4 v201, s[44:47], s51 offen lds\n" \
  "s_add_u32 m0, s52, 6144\n" \
  "s_nop 0\n" \
  "buffer_load_dwordx4 v202, s[44:47], s51 offen lds\n" \
  "s_add_u32 m0, s52, 7168\n" \
  "s_nop 0\n" \
  "buffer_load_dwordx4 v203, s[44:47], s51 offen lds\n" \
  "s_add_u32 m0, s52, 8192\n" \
  "s_nop 0\n" \
  "buffer_load_dwordx4 v204, s[44:47], s51 offen lds\n" \
  "s_add_u32 s51, s51, 36864\n" \
  "s_min_u32 s51, s51, 2322432\n" \
  "s_add_u32 s52, s52, 36864\n" \
  "s_cmp_ge_u32 s52, 163840\n" \
  "s_cselect_b32 s60, 147456, 0\n" \
  "s_sub_u32 s52, s52, s60\n" \
  "s_waitcnt vmcnt(21)\n" \
  "s_barrier\n" \
  "v_add_u32 v186, 65536, v182\n" \
  "ds_read_b64_tr_b16 a[168:169], v186\n" \
  "ds_read_b64_tr_b16 a[170:171], v186 offset:1024\n" \
  "ds_read_b64_tr_b16 a[172:173], v186 offset:8192\n" \
  "ds_read_b64_tr_b16 a[174:175], v186 offset:9216\n" \
  "v_add_u32 v187, 65536, v183\n" \
  "ds_read_b64_tr_b16 a[240:241], v187\n" \
  "ds_read_b64_tr_b16 a[242:243], v187 offset:1024\n" \
  "ds_read_b64_tr_b16 a[244:245], v187 offset:8192\n" \
  "ds_read_b64_tr_b16 a[246:247], v187 offset:9216\n" \
  "v_add_u32 v186, 81920, v182\n" \
  "ds_read_b64_tr_b16 a[176:177], v186\n" \
  "ds_read_b64_tr_b16 a[178:179], v186 offset:1024\n" \
  "ds_read_b64_tr_b16 a[180:181], v186 offset:8192\n" \
  "ds_read_b64_tr_b16 a[182:183], v186 offset:9216\n" \
  "v_add_u32 v187, 81920, v183\n" \
  "ds_read_b64_tr_b16 a[248:249], v187\n" \
  "ds_read_b64_tr_b16 a[250:251], v187 offset:1024\n" \
  "ds_read_b64_tr_b16 a[252:253], v187 offset:8192\n" \
  "ds_read_b64_tr_b16 a[254:255], v187 offset:9216\n" \
  "v_add_u32 v186, 98304, v182\n" \
  "ds_read_b64_tr_b16 a[184:185], v186\n" \
  "ds_read_b64_tr_b16 a[186:187], v186 offset:1024\n" \
  "ds_read_b64_tr_b16 a[188:189], v186 offset:8192\n" \
  "ds_read_b64_tr_b16 a[190:191], v186 offset:9216\n" \
  "v_add_u32 v187, 98304, v183\n" \
  "ds_read_b64_tr_b16 v[0:1], v187\n" \
  "ds_read_b64_tr_b16 v[2:3], v187 offset:1024\n" \
  "ds_read_b64_tr_b16 v[4:5], v187 offset:8192\n" \
  "ds_read_b64_tr_b16 v[6:7], v187 offset:9216\n" \
  "s_waitcnt lgkmcnt(0)\n" \
  "s_barrier\n" \
  "s_add_u32 m0, s52, 0\n" \
  "s_nop 0\n" \
  "buffer_load_dwordx4 v196, s[44:47], s51 offen lds\n" \
  "s_add_u32 m0, s52, 1024\n" \
  "s_nop 0\n" \
  "buffer_load_dwordx4 v197, s[44:47], s51 offen lds\n" \
  "s_add_u32 m0, s52, 2048\n" \
  "s_nop 0\n" \
  "buffer_load_dwordx4 v198, s[44:47], s51 offen lds\n" \
  "s_add_u32 m0, s52, 3072\n" \
  "s_nop 0\n" \
  "buffer_load_dwordx4 v199, s[44:47], s51 offen lds\n" \
  "s_add_u32 m0, s52, 4096\n" \
  "s_nop 0\n" \
  "buffer_load_dwordx4 v200, s[44:47], s51 offen lds\n" \
  "s_add_u32 m0, s52, 5120\n" \
  "s_nop 0\n" \
  "buffer_load_dwordx4 v201, s[44:47], s51 offen lds\n" \
  "s_add_u32 m0, s52, 6144\n" \
  "s_nop 0\n" \
  "buffer_load_dwordx4 v202, s[44:47], s51 offen lds\n" \
  "s_add_u32 m0, s52, 7168\n" \
  "s_nop 0\n" \
  "buffer_load_dwordx4 v203, s[44:47], s51 offen lds\n" \
  "s_add_u32 m0, s52, 8192\n" \
  "s_nop 0\n" \
  "buffer_load_dwordx4 v204, s[44:47], s51 offen lds\n" \
  "s_add_u32 s51, s51, 36864\n" \
  "s_min_u32 s51, s51, 2322432\n" \
  "s_add_u32 s52, s52, 36864\n" \
  "s_cmp_ge_u32 s52, 163840\n" \
  "s_cselect_b32 s60, 147456, 0\n" \
  "s_sub_u32 s52, s52, s60\n" \
  "s_waitcnt vmcnt(18)\n" \
  "s_barrier\n" \
  "v_add_u32 v186, 114688, v182\n" \
  "ds_read_b64_tr_b16 a[192:193], v186\n" \
  "ds_read_b64_tr_b16 a[194:195], v186 offset:1024\n" \
  "ds_read_b64_tr_b16 a[196:197], v186 offset:8192\n" \
  "ds_read_b64_tr_b16 a[198:199], v186 offset:9216\n" \
  "v_add_u32 v187, 114688, v183\n" \
  "ds_read_b64_tr_b16 v[8:9], v187\n" \
  "ds_read_b64_tr_b16 v[10:11], v187 offset:1024\n" \
  "ds_read_b64_tr_b16 v[12:13], v187 offset:8192\n" \
  "ds_read_b64_tr_b16 v[14:15], v187 offset:9216\n" \
  "v_add_u32 v186, 131072, v182\n" \
  "ds_read_b64_tr_b16 a[200:201], v186\n" \
  "ds_read_b64_tr_b16 a[202:203], v186 offset:1024\n" \
  "ds_read_b64_tr_b16 a[204:205], v186 offset:8192\n" \
  "ds_read_b64_tr_b16 a[206:207], v186 offset:9216\n" \
  "v_add_u32 v187, 131072, v183\n" \
  "ds_read_b64_tr_b16 v[16:17], v187\n" \
  "ds_read_b64_tr_b16 v[18:19], v187 offset:1024\n" \
  "ds_read_b64_tr_b16 v[20:21], v187 offset:8192\n" \
  "ds_read_b64_tr_b16 v[22:23], v187 offset:9216\n" \
  "v_add_u32 v186, 147456, v182\n" \
  "ds_read_b64_tr_b16 a[208:209], v186\n" \
  "ds_read_b64_tr_b16 a[210:211], v186 offset:1024\n" \
  "ds_read_b64_tr_b16 a[212:213], v186 offset:8192\n" \
  "ds_read_b64_tr_b16 a[214:215], v186 offset:9216\n" \
  "v_add_u32 v187, 147456, v183\n" \
  "ds_read_b64_tr_b16 v[24:25], v187\n" \
  "ds_read_b64_tr_b16 v[26:27], v187 offset:1024\n" \
  "ds_read_b64_tr_b16 v[28:29], v187 offset:8192\n" \
  "ds_read_b64_tr_b16 v[30:31], v187 offset:9216\n" \
  "s_waitcnt lgkmcnt(0)\n" \
  "s_waitcnt vmcnt(9)\n" \
  "s_barrier\n" \
  "ds_read_b128 v[32:35], v191\n" \
  "ds_read_b128 v[36:39], v191 offset:18432\n" \
  "ds_read_b128 v[40:43], v192\n" \
  "ds_read_b128 v[44:47], v192 offset:18432\n" \
  "ds_read_b128 v[48:51], v191 offset:128\n" \
  "ds_read_b128 v[52:55], v191 offset:18560\n" \
  "s_waitcnt lgkmcnt(0)\n" \
  ".LK_loop_%=:\n" \
  "v_mfma_f32_16x16x32_bf16 v[64:67], a[0:3], v[32:35], 0\n" \
  "ds_read_b128 v[56:59], v192 offset:128\n" \
  "v_mfma_f32_16x16x32_bf16 v[72:75], a[0:3], v[36:39], 0\n" \
  "ds_read_b128 v[60:63], v192 offset:18560\n" \
  "s_lshr_b32 s62, s50, 8\n" \
  "v_mfma_f32_16x16x32_bf16 v[68:71], a[72:75], v[32:35], 0\n" \
  "s_and_b32 s62, s62, 8192\n" \
  "s_xor_b32 s62, s62, s50\n" \
  "v_cvt_pk_bf16_f32 v96, v80, v81\n" \
  "v_mfma_f32_16x16x32_bf16 v[76:79], a[72:75], v[36:39], 0\n" \
  "v_cvt_pk_bf16_f32 v97, v82, v83\n" \
  "v_cvt_pk_bf16_f32 v98, v84, v85\n" \
  "v_mfma_f32_16x16x32_bf16 v[64:67], a[4:7], v[40:43], v[64:67]\n" \
  "ds_read_b128 v[32:35], v191 offset:256\n" \
  "v_cvt_pk_bf16_f32 v99, v86, v87\n" \
  "v_mfma_f32_16x16x32_bf16 v[72:75], a[4:7], v[44:47], v[72:75]\n" \
  "ds_read_b128 v[36:39], v191 offset:18688\n" \
  "v_mfma_f32_16x16x32_bf16 v[68:71], a[76:79], v[40:43], v[68:71]\n" \
  "buffer_store_dwordx4 v[96:99], v190, s[40:43], s62 offen sc1\n" \
  "v_mfma_f32_16x16x32_bf16 v[76:79], a[76:79], v[44:47], v[76:79]\n" \
  "v_cvt_pk_bf16_f32 v100, v88, v89\n" \
  "v_cvt_pk_bf16_f32 v101, v90, v91\n" \
  "v_mfma_f32_16x16x32_bf16 v[64:67], a[8:11], v[48:51], v[64:67]\n" \
  "ds_read_b128 v[40:43], v192 offset:256\n" \
  "v_cvt_pk_bf16_f32 v102, v92, v93\n" \
  "v_mfma_f32_16x16x32_bf16 v[72:75], a[8:11], v[52:55], v[72:75]\n" \
  "ds_read_b128 v[44:47], v192 offset:18688\n" \
  "v_cvt_pk_bf16_f32 v103, v94, v95\n" \
  "v_mfma_f32_16x16x32_bf16 v[68:71], a[80:83], v[48:51], v[68:71]\n" \
  "buffer_store_dwordx4 v[100:103], v190, s[40:43], s62 offen offset:1024 sc1\n" \
  "v_mfma_f32_16x16x32_bf16 v[76:79], a[80:83], v[52:55], v[76:79]\n" \
  "s_add_u32 s50, s50, 524288\n" \
  "s_bitset0_b32 s50, 30\n" \
  "s_waitcnt lgkmcnt(5)\n" \
  "v_mfma_f32_16x16x32_bf16 v[64:67], a[12:15], v[56:59], v[64:67]\n" \
  "ds_read_b128 v[48:51], v191 offset:384\n" \
  "s_waitcnt lgkmcnt(5)\n" \
  "v_mfma_f32_16x16x32_bf16 v[72:75], a[12:15], v[60:63], v[72:75]\n" \
  "ds_read_b128 v[52:55], v191 offset:18816\n" \
  "v_mfma_f32_16x16x32_bf16 v[68:71], a[84:87], v[56:59], v[68:71]\n" \
  "v_mfma_f32_16x16x32_bf16 v[76:79], a[84:87], v[60:63], v[76:79]\n" \
  "s_waitcnt lgkmcnt(5)\n" \
  "v_mfma_f32_16x16x32_bf16 v[64:67], a[16:19], v[32:35], v[64:67]\n" \
  "ds_read_b128 v[56:59], v192 offset:384\n" \
  "s_waitcnt lgkmcnt(5)\n" \
  "v_mfma_f32_16x16x32_bf16 v[72:75], a[16:19], v[36:39], v[72:75]\n" \
  "ds_read_b128 v[60:63], v192 offset:18816\n" \
  "v_mfma_f32_16x16x32_bf16 v[68:71], a[88:91], v[32:35], v[68:71]\n" \
  "v_mfma_f32_16x16x32_bf16 v[76:79], a[88:91], v[36:39], v[76:79]\n" \
  "s_waitcnt lgkmcnt(5)\n" \
  "v_mfma_f32_16x16x32_bf16 v[64:67], a[20:23], v[40:43], v[64:67]\n" \
  "ds_read_b128 v[32:35], v191 offset:512\n" \
  "s_waitcnt lgkmcnt(5)\n" \
  "v_mfma_f32_16x16x32_bf16 v[72:75], a[20:23], v[44:47], v[72:75]\n" \
  "ds_read_b128 v[36:39], v191 offset:18944\n" \
  "v_mfma_f32_16x16x32_bf16 v[68:71], a[92:95], v[40:43], v[68:71]\n" \
  "v_mfma_f32_16x16x32_bf16 v[76:79], a[92:95], v[44:47], v[76:79]\n" \
  "s_waitcnt lgkmcnt(5)\n" \
  "v_mfma_f32_16x16x32_bf16 v[64:67], a[24:27], v[48:51], v[64:67]\n" \
  "ds_read_b128 v[40:43], v192 offset:512\n" \
  "s_waitcnt lgkmcnt(5)\n" \
  "v_mfma_f32_16x16x32_bf16 v[72:75], a[24:27], v[52:55], v[72:75]\n" \
  "ds_read_b128 v[44:47], v192 offset:18944\n" \
  "v_mfma_f32_16x16x32_bf16 v[68:71], a[96:99], v[48:51], v[68:71]\n" \
  "v_mfma_f32_16x16x32_bf16 v[76:79], a[96:99], v[52:55], v[76:79]\n" \
  "s_waitcnt lgkmcnt(5)\n" \
  "v_mfma_f32_16x16x32_bf16 v[64:67], a[28:31], v[56:59], v[64:67]\n" \
  "ds_read_b128 v[48:51], v191 offset:640\n" \
  "s_waitcnt lgkmcnt(5)\n" \
  "v_mfma_f32_16x16x32_bf16 v[72:75], a[28:31], v[60:63], v[72:75]\n" \
  "ds_read_b128 v[52:55], v191 offset:19072\n" \
  "v_mfma_f32_16x16x32_bf16 v[68:71], a[100:103], v[56:59], v[68:71]\n" \
  "s_add_u32 m0, s52, 0\n" \
  "v_mfma_f32_16x16x32_bf16 v[76:79], a[100:103], v[60:63], v[76:79]\n" \
  "buffer_load_dwordx4 v196, s[44:47], s51 offen lds\n" \
  "s_waitcnt lgkmcnt(5)\n" \
  "v_mfma_f32_16x16x32_bf16 v[64:67], a[32:35], v[32:35], v[64:67]\n" \
  "ds_read_b128 v[56:59], v192 offset:640\n" \
  "s_waitcnt lgkmcnt(5)\n" \
  "v_mfma_f32_16x16x32_bf16 v[72:75], a[32:35], v[36:39], v[72:75]\n" \
  "ds_read_b128 v[60:63], v192 offset:19072\n" \
  "v_mfma_f32_16x16x32_bf16 v[68:71], a[104:107], v[32:35], v[68:71]\n" \
  "v_mfma_f32_16x16x32_bf16 v[76:79], a[104:107], v[36:39], v[76:79]\n" \
  "s_waitcnt lgkmcnt(5)\n" \
  "v_mfma_f32_16x16x32_bf16 v[64:67], a[36:39], v[40:43], v[64:67]\n" \
  "ds_read_b128 v[32:35], v191 offset:768\n" \
  "s_waitcnt lgkmcnt(5)\n" \
  "v_mfma_f32_16x16x32_bf16 v[72:75], a[36:39], v[44:47], v[72:75]\n" \
  "ds_read_b128 v[36:39], v191 offset:19200\n" \
  "v_mfma_f32_16x16x32_bf16 v[68:71], a[108:111], v[40:43], v[68:71]\n" \
  "s_add_u32 m0, s52, 1024\n" \
  "v_mfma_f32_16x16x32_bf16 v[76:79], a[108:111], v[44:47], v[76:79]\n" \
  "buffer_load_dwordx4 v197, s[44:47], s51 offen lds\n" \
  "s_waitcnt lgkmcnt(5)\n" \
  "v_mfma_f32_16x16x32_bf16 v[64:67], a[40:43], v[48:51], v[64:67]\n" \
  "ds_read_b128 v[40:43], v192 offset:768\n" \
  "s_waitcnt lgkmcnt(5)\n" \
  "v_mfma_f32_16x16x32_bf16 v[72:75], a[40:43], v[52:55], v[72:75]\n" \
  "ds_read_b128 v[44:47], v192 offset:19200\n" \
  "v_mfma_f32_16x16x32_bf16 v[68:71], a[112:115], v[48:51], v[68:71]\n" \
  "v_mfma_f32_16x16x32_bf16 v[76:79], a[112:115], v[52:55], v[76:79]\n" \
  "s_waitcnt lgkmcnt(5)\n" \
  "v_mfma_f32_16x16x32_bf16 v[64:67], a[44:47], v[56:59], v[64:67]\n" \
  "ds_read_b128 v[48:51], v191 offset:896\n" \
  "s_waitcnt lgkmcnt(5)\n" \
  "v_mfma_f32_16x16x32_bf16 v[72:75], a[44:47], v[60:63], v[72:75]\n" \
  "ds_read_b128 v[52:55], v191 offset:19328\n" \
  "v_mfma_f32_16x16x32_bf16 v[68:71], a[116:119], v[56:59], v[68:71]\n" \
  "s_add_u32 m0, s52, 2048\n" \
  "v_add_u32 v168, s48, v166\n" \
  "v_mfma_f32_16x16x32_bf16 v[76:79], a[116:119], v[60:63], v[76:79]\n" \
  "buffer_load_dwordx4 v198, s[44:47], s51 offen lds\n" \
  "s_waitcnt lgkmcnt(5)\n" \
  "v_mfma_f32_16x16x32_bf16 v[64:67], a[48:51], v[32:35], v[64:67]\n" \
  "ds_read_b128 v[56:59], v192 offset:896\n" \
  "s_waitcnt lgkmcnt(5)\n" \
  "v_mfma_f32_16x16x32_bf16 v[72:75], a[48:51], v[36:39], v[72:75]\n" \
  "ds_read_b128 v[60:63], v192 offset:19328\n" \
  "v_mfma_f32_16x16x32_bf16 v[68:71], a[120:123], v[32:35], v[68:71]\n" \
  "s_waitcnt vmcnt(5)\n" \
  "s_barrier\n" \
  "v_mfma_f32_16x16x32_bf16 v[76:79], a[120:123], v[36:39], v[76:79]\n" \
  "buffer_store_dwordx2 v[168:169], v167, s[40:43], 0 offen sc1\n" \
  "s_waitcnt lgkmcnt(5)\n" \
  "v_mfma_f32_16x16x32_bf16 v[64:67], a[52:55], v[40:43], v[64:67]\n" \
  "ds_read_b128 v[32:35], v191 offset:1024\n" \
  "s_waitcnt lgkmcnt(5)\n" \
  "v_mfma_f32_16x16x32_bf16 v[72:75], a[52:55], v[44:47], v[72:75]\n" \
  "ds_read_b128 v[36:39], v191 offset:19456\n" \
  "v_mfma_f32_16x16x32_bf16 v[68:71], a[124:127], v[40:43], v[68:71]\n" \
  "s_add_u32 m0, s52, 3072\n" \
  "v_mfma_f32_16x16x32_bf16 v[76:79], a[124:127], v[44:47], v[76:79]\n" \
  "buffer_load_dwordx4 v199, s[44:47], s51 offen lds\n" \
  "s_waitcnt lgkmcnt(5)\n" \
  "v_mfma_f32_16x16x32_bf16 v[64:67], a[56:59], v[48:51], v[64:67]\n" \
  "ds_read_b128 v[40:43], v192 offset:1024\n" \
  "s_waitcnt lgkmcnt(5)\n" \
  "v_mfma_f32_16x16x32_bf16 v[72:75], a[56:59], v[52:55], v[72:75]\n" \
  "ds_read_b128 v[44:47], v192 offset:19456\n" \
  "v_mfma_f32_16x16x32_bf16 v[68:71], a[128:131], v[48:51], v[68:71]\n" \
  "v_mfma_f32_16x16x32_bf16 v[76:79], a[128:131], v[52:55], v[76:79]\n" \
  "s_waitcnt lgkmcnt(5)\n" \
  "v_mfma_f32_16x16x32_bf16 v[64:67], a[60:63], v[56:59], v[64:67]\n" \
  "ds_read_b128 v[48:51], v191\n" \
  "s_waitcnt lgkmcnt(5)\n" \
  "v_mfma_f32_16x16x32_bf16 v[72:75], a[60:63], v[60:63], v[72:75]\n" \
  "ds_read_b128 v[52:55], v191 offset:18432\n" \
  "v_mfma_f32_16x16x32_bf16 v[68:71], a[132:135], v[56:59], v[68:71]\n" \
  "s_add_u32 m0, s52, 4096\n" \
  "v_mfma_f32_16x16x32_bf16 v[76:79], a[132:135], v[60:63], v[76:79]\n" \
  "buffer_load_dwordx4 v200, s[44:47], s51 offen lds\n" \
  "s_waitcnt lgkmcnt(5)\n" \
  "v_mfma_f32_16x16x32_bf16 v[64:67], a[64:67], v[32:35], v[64:67]\n" \
  "ds_read_b128 v[56:59], v192\n" \
  "s_waitcnt lgkmcnt(5)\n" \
  "v_mfma_f32_16x16x32_bf16 v[72:75], a[64:67], v[36:39], v[72:75]\n" \
  "ds_read_b128 v[60:63], v192 offset:18432\n" \
  "v_mfma_f32_16x16x32_bf16 v[68:71], a[136:139], v[32:35], v[68:71]\n" \
  "v_mfma_f32_16x16x32_bf16 v[76:79], a[136:139], v[36:39], v[76:79]\n" \
  "s_waitcnt lgkmcnt(5)\n" \
  "v_mfma_f32_16x16x32_bf16 v[64:67], a[68:71], v[40:43], v[64:67]\n" \
  "ds_read_b128 v[32:35], v191 offset:128\n" \
  "s_waitcnt lgkmcnt(5)\n" \
  "v_mfma_f32_16x16x32_bf16 v[72:75], a[68:71], v[44:47], v[72:75]\n" \
  "ds_read_b128 v[36:39], v191 offset:18560\n" \
  "v_mfma_f32_16x16x32_bf16 v[68:71], a[140:143], v[40:43], v[68:71]\n" \
  "v_mfma_f32_16x16x32_bf16 v[76:79], a[140:143], v[44:47], v[76:79]\n" \
  "s_waitcnt lgkmcnt(5)\n" \
  "v_mfma_f32_16x16x32_bf16 v[80:83], a[144:147], v[48:51], 0\n" \
  "ds_read_b128 v[40:43], v192 offset:128\n" \
  "s_waitcnt lgkmcnt(5)\n" \
  "v_mfma_f32_16x16x32_bf16 v[88:91], a[144:147], v[52:55], 0\n" \
  "ds_read_b128 v[44:47], v192 offset:18560\n" \
  "s_lshr_b32 s62, s49, 8\n" \
  "v_mfma_f32_16x16x32_bf16 v[84:87], a[216:219], v[48:51], 0\n" \
  "s_and_b32 s62, s62, 8192\n" \
  "s_xor_b32 s62, s62, s49\n" \
  "v_cvt_pk_bf16_f32 v96, v64, v65\n" \
  "v_mfma_f32_16x16x32_bf16 v[92:95], a[216:219], v[52:55], 0\n" \
  "s_waitcnt lgkmcnt(5)\n" \
  "v_cvt_pk_bf16_f32 v97, v66, v67\n" \
  "v_cvt_pk_bf16_f32 v98, v68, v69\n" \
  "v_mfma_f32_16x16x32_bf16 v[80:83], a[148:151], v[56:59], v[80:83]\n" \
  "ds_read_b128 v[48:51], v191 offset:256\n" \
  "s_waitcnt lgkmcnt(5)\n" \
  "v_cvt_pk_bf16_f32 v99, v70, v71\n" \
  "v_mfma_f32_16x16x32_bf16 v[88:91], a[148:151], v[60:63], v[88:91]\n" \
  "ds_read_b128 v[52:55], v191 offset:18688\n" \
  "s_lshl_b32 s63, s48, 14\n" \
  "v_mfma_f32_16x16x32_bf16 v[84:87], a[220:223], v[56:59], v[84:87]\n" \
  "s_xor_b32 s63, s63, s59\n" \
  "v_mfma_f32_16x16x32_bf16 v[92:95], a[220:223], v[60:63], v[92:95]\n" \
  "buffer_store_dwordx4 v[96:99], v190, s[40:43], s62 offen sc1\n" \
  "s_waitcnt lgkmcnt(5)\n" \
  "v_mfma_f32_16x16x32_bf16 v[80:83], a[152:155], v[32:35], v[80:83]\n" \
  "ds_read_b128 v[56:59], v192 offset:256\n" \
  "s_waitcnt lgkmcnt(5)\n" \
  "v_mfma_f32_16x16x32_bf16 v[88:91], a[152:155], v[36:39], v[88:91]\n" \
  "ds_read_b128 v[60:63], v192 offset:18688\n" \
  "v_mfma_f32_16x16x32_bf16 v[84:87], a[224:227], v[32:35], v[84:87]\n" \
  "buffer_store_dwordx4 v[96:99], v190, s[40:43], s63 offen sc1\n" \
  "v_mfma_f32_16x16x32_bf16 v[92:95], a[224:227], v[36:39], v[92:95]\n" \
  "s_waitcnt lgkmcnt(5)\n" \
  "v_cvt_pk_bf16_f32 v100, v72, v73\n" \
  "v_cvt_pk_bf16_f32 v101, v74, v75\n" \
  "v_mfma_f32_16x16x32_bf16 v[80:83], a[156:159], v[40:43], v[80:83]\n" \
  "ds_read_b128 v[32:35], v191 offset:384\n" \
  "s_waitcnt lgkmcnt(5)\n" \
  "v_cvt_pk_bf16_f32 v102, v76, v77\n" \
  "v_mfma_f32_16x16x32_bf16 v[88:91], a[156:159], v[44:47], v[88:91]\n" \
  "ds_read_b128 v[36:39], v191 offset:18816\n" \
  "v_cvt_pk_bf16_f32 v103, v78, v79\n" \
  "v_mfma_f32_16x16x32_bf16 v[84:87], a[228:231], v[40:43], v[84:87]\n" \
  "buffer_store_dwordx4 v[100:103], v190, s[40:43], s62 offen offset:1024 sc1\n" \
  "v_mfma_f32_16x16x32_bf16 v[92:95], a[228:231], v[44:47], v[92:95]\n" \
  "buffer_store_dwordx4 v[100:103], v190, s[40:43], s63 offen offset:1024 sc1\n" \
  "s_waitcnt lgkmcnt(5)\n" \
  "v_mfma_f32_16x16x32_bf16 v[80:83], a[160:163], v[48:51], v[80:83]\n" \
  "ds_read_b128 v[40:43], v192 offset:384\n" \
  "s_add_u32 s49, s49, 524288\n" \
  "s_waitcnt lgkmcnt(5)\n" \
  "v_mfma_f32_16x16x32_bf16 v[88:91], a[160:163], v[52:55], v[88:91]\n" \
  "ds_read_b128 v[44:47], v192 offset:18816\n" \
  "s_add_u32 s58, s48, 1\n" \
  "v_mfma_f32_16x16x32_bf16 v[84:87], a[232:235], v[48:51], v[84:87]\n" \
  "s_and_b32 s58, s58, 3\n" \
  "s_cselect_b32 s58, 0, 147456\n" \
  "s_sub_u32 s58, 36864, s58\n" \
  "v_mfma_f32_16x16x32_bf16 v[92:95], a[232:235], v[52:55], v[92:95]\n" \
  "s_waitcnt lgkmcnt(5)\n" \
  "v_add_u32 v165, s58, v191\n" \
  "v_add_u32 v164, s58, v192\n" \
  "v_mfma_f32_16x16x32_bf16 v[80:83], a[164:167], v[56:59], v[80:83]\n" \
  "ds_read_b128 v[48:51], v191 offset:512\n" \
  "s_waitcnt lgkmcnt(5)\n" \
  "v_mfma_f32_16x16x32_bf16 v[88:91], a[164:167], v[60:63], v[88:91]\n" \
  "ds_read_b128 v[52:55], v191 offset:18944\n" \
  "v_mfma_f32_16x16x32_bf16 v[84:87], a[236:239], v[56:59], v[84:87]\n" \
  "v_mfma_f32_16x16x32_bf16 v[92:95], a[236:239], v[60:63], v[92:95]\n" \
  "s_waitcnt lgkmcnt(5)\n" \
  "v_mfma_f32_16x16x32_bf16 v[80:83], a[168:171], v[32:35], v[80:83]\n" \
  "ds_read_b128 v[56:59], v192 offset:512\n" \
  "s_waitcnt lgkmcnt(5)\n" \
  "v_mfma_f32_16x16x32_bf16 v[88:91], a[168:171], v[36:39], v[88:91]\n" \
  "ds_read_b128 v[60:63], v192 offset:18944\n" \
  "v_mfma_f32_16x16x32_bf16 v[84:87], a[240:243], v[32:35], v[84:87]\n" \
  "v_mfma_f32_16x16x32_bf16 v[92:95], a[240:243], v[36:39], v[92:95]\n" \
  "s_waitcnt lgkmcnt(5)\n" \
  "v_mfma_f32_16x16x32_bf16 v[80:83], a[172:175], v[40:43], v[80:83]\n" \
  "ds_read_b128 v[32:35], v191 offset:640\n" \
  "s_waitcnt lgkmcnt(5)\n" \
  "v_mfma_f32_16x16x32_bf16 v[88:91], a[172:175], v[44:47], v[88:91]\n" \
  "ds_read_b128 v[36:39], v191 offset:19072\n" \
  "v_mfma_f32_16x16x32_bf16 v[84:87], a[244:247], v[40:43], v[84:87]\n" \
  "v_mfma_f32_16x16x32_bf16 v[92:95], a[244:247], v[44:47], v[92:95]\n" \
  "s_waitcnt lgkmcnt(5)\n" \
  "v_mfma_f32_16x16x32_bf16 v[80:83], a[176:179], v[48:51], v[80:83]\n" \
  "ds_read_b128 v[40:43], v192 offset:640\n" \
  "s_waitcnt lgkmcnt(5)\n" \
  "v_mfma_f32_16x16x32_bf16 v[88:91], a[176:179], v[52:55], v[88:91]\n" \
  "ds_read_b128 v[44:47], v192 offset:19072\n" \
  "v_mfma_f32_16x16x32_bf16 v[84:87], a[248:251], v[48:51], v[84:87]\n" \
  "s_add_u32 m0, s52, 5120\n" \
  "v_mfma_f32_16x16x32_bf16 v[92:95], a[248:251], v[52:55], v[92:95]\n" \
  "buffer_load_dwordx4 v201, s[44:47], s51 offen lds\n" \
  "s_waitcnt lgkmcnt(5)\n" \
  "v_mfma_f32_16x16x32_bf16 v[80:83], a[180:183], v[56:59], v[80:83]\n" \
  "ds_read_b128 v[48:51], v191 offset:768\n" \
  "s_waitcnt lgkmcnt(5)\n" \
  "v_mfma_f32_16x16x32_bf16 v[88:91], a[180:183], v[60:63], v[88:91]\n" \
  "ds_read_b128 v[52:55], v191 offset:19200\n" \
  "v_mfma_f32_16x16x32_bf16 v[84:87], a[252:255], v[56:59], v[84:87]\n" \
  "v_mfma_f32_16x16x32_bf16 v[92:95], a[252:255], v[60:63], v[92:95]\n" \
  "s_waitcnt lgkmcnt(5)\n" \
  "v_mfma_f32_16x16x32_bf16 v[80:83], a[184:187], v[32:35], v[80:83]\n" \
  "ds_read_b128 v[56:59], v192 offset:768\n" \
  "s_waitcnt lgkmcnt(5)\n" \
  "v_mfma_f32_16x16x32_bf16 v[88:91], a[184:187], v[36:39], v[88:91]\n" \
  "ds_read_b128 v[60:63], v192 offset:19200\n" \
  "v_mfma_f32_16x16x32_bf16 v[84:87], v[0:3], v[32:35], v[84:87]\n" \
  "s_add_u32 m0, s52, 6144\n" \
  "v_mfma_f32_16x16x32_bf16 v[92:95], v[0:3], v[36:39], v[92:95]\n" \
  "buffer_load_dwordx4 v202, s[44:47], s51 offen lds\n" \
  "s_waitcnt lgkmcnt(5)\n" \
  "v_mfma_f32_16x16x32_bf16 v[80:83], a[188:191], v[40:43], v[80:83]\n" \
  "ds_read_b128 v[32:35], v191 offset:896\n" \
  "s_waitcnt lgkmcnt(5)\n" \
  "v_mfma_f32_16x16x32_bf16 v[88:91], a[188:191], v[44:47], v[88:91]\n" \
  "ds_read_b128 v[36:39], v191 offset:19328\n" \
  "v_mfma_f32_16x16x32_bf16 v[84:87], v[4:7], v[40:43], v[84:87]\n" \
  "v_mfma_f32_16x16x32_bf16 v[92:95], v[4:7], v[44:47], v[92:95]\n" \
  "s_waitcnt lgkmcnt(5)\n" \
  "v_mfma_f32_16x16x32_bf16 v[80:83], a[192:195], v[48:51], v[80:83]\n" \
  "ds_read_b128 v[40:43], v192 offset:896\n" \
  "s_waitcnt lgkmcnt(5)\n" \
  "v_mfma_f32_16x16x32_bf16 v[88:91], a[192:195], v[52:55], v[88:91]\n" \
  "ds_read_b128 v[44:47], v192 offset:19328\n" \
  "v_mfma_f32_16x16x32_bf16 v[84:87], v[8:11], v[48:51], v[84:87]\n" \
  "s_add_u32 m0, s52, 7168\n" \
  "v_mfma_f32_16x16x32_bf16 v[92:95], v[8:11], v[52:55], v[92:95]\n" \
  "buffer_load_dwordx4 v203, s[44:47], s51 offen lds\n" \
  "s_waitcnt lgkmcnt(5)\n" \
  "v_mfma_f32_16x16x32_bf16 v[80:83], a[196:199], v[56:59], v[80:83]\n" \
  "ds_read_b128 v[48:51], v191 offset:1024\n" \
  "s_waitcnt lgkmcnt(5)\n" \
  "v_mfma_f32_16x16x32_bf16 v[88:91], a[196:199], v[60:63], v[88:91]\n" \
  "ds_read_b128 v[52:55], v191 offset:19456\n" \
  "v_mfma_f32_16x16x32_bf16 v[84:87], v[12:15], v[56:59], v[84:87]\n" \
  "v_mfma_f32_16x16x32_bf16 v[92:95], v[12:15], v[60:63], v[92:95]\n" \
  "s_waitcnt lgkmcnt(5)\n" \
  "v_mfma_f32_16x16x32_bf16 v[80:83], a[200:203], v[32:35], v[80:83]\n" \
  "ds_read_b128 v[56:59], v192 offset:1024\n" \
  "s_waitcnt lgkmcnt(5)\n" \
  "v_mfma_f32_16x16x32_bf16 v[88:91], a[200:203], v[36:39], v[88:91]\n" \
  "ds_read_b128 v[60:63], v192 offset:19456\n" \
  "v_mfma_f32_16x16x32_bf16 v[84:87], v[16:19], v[32:35], v[84:87]\n" \
  "s_add_u32 m0, s52, 8192\n" \
  "v_mfma_f32_16x16x32_bf16 v[92:95], v[16:19], v[36:39], v[92:95]\n" \
  "buffer_load_dwordx4 v204, s[44:47], s51 offen lds\n" \
  "s_waitcnt lgkmcnt(5)\n" \
  "v_mfma_f32_16x16x32_bf16 v[80:83], a[204:207], v[40:43], v[80:83]\n" \
  "ds_read_b128 v[32:35], v165\n" \
  "s_add_u32 s51, s51, 36864\n" \
  "s_waitcnt lgkmcnt(5)\n" \
  "v_mfma_f32_16x16x32_bf16 v[88:91], a[204:207], v[44:47], v[88:91]\n" \
  "ds_read_b128 v[36:39], v165 offset:18432\n" \
  "s_min_u32 s51, s51, 2322432\n" \
  "v_mfma_f32_16x16x32_bf16 v[84:87], v[20:23], v[40:43], v[84:87]\n" \
  "s_add_u32 s52, s52, 36864\n" \
  "v_mfma_f32_16x16x32_bf16 v[92:95], v[20:23], v[44:47], v[92:95]\n" \
  "s_cmp_ge_u32 s52, 163840\n" \
  "s_cselect_b32 s60, 147456, 0\n" \
  "s_sub_u32 s52, s52, s60\n" \
  "s_waitcnt lgkmcnt(5)\n" \
  "v_mfma_f32_16x16x32_bf16 v[80:83], a[208:211], v[48:51], v[80:83]\n" \
  "ds_read_b128 v[40:43], v164\n" \
  "s_waitcnt lgkmcnt(5)\n" \
  "v_mfma_f32_16x16x32_bf16 v[88:91], a[208:211], v[52:55], v[88:91]\n" \
  "ds_read_b128 v[44:47], v164 offset:18432\n" \
  "v_mfma_f32_16x16x32_bf16 v[84:87], v[24:27], v[48:51], v[84:87]\n" \
  "v_mfma_f32_16x16x32_bf16 v[92:95], v[24:27], v[52:55], v[92:95]\n" \
  "s_waitcnt lgkmcnt(5)\n" \
  "v_mfma_f32_16x16x32_bf16 v[80:83], a[212:215], v[56:59], v[80:83]\n" \
  "ds_read_b128 v[48:51], v165 offset:128\n" \
  "s_waitcnt lgkmcnt(5)\n" \
  "v_mfma_f32_16x16x32_bf16 v[88:91], a[212:215], v[60:63], v[88:91]\n" \
  "ds_read_b128 v[52:55], v165 offset:18560\n" \
  "v_mfma_f32_16x16x32_bf16 v[84:87], v[28:31], v[56:59], v[84:87]\n" \
  "v_mov_b32 v191, v165\n" \
  "v_mov_b32 v192, v164\n" \
  "v_mfma_f32_16x16x32_bf16 v[92:95], v[28:31], v[60:63], v[92:95]\n" \
  "s_add_u32 s48, s48, 1\n" \
  "s_cmp_lt_u32 s48, 64\n" \
  "s_waitcnt lgkmcnt(0)\n" \
  "s_cbranch_scc1 .LK_loop_%=\n" \
  "s_nop 7\n" \
  "s_lshr_b32 s62, s50, 8\n" \
  "s_and_b32 s62, s62, 8192\n" \
  "s_xor_b32 s62, s62, s50\n" \
  "v_cvt_pk_bf16_f32 v96, v80, v81\n" \
  "v_cvt_pk_bf16_f32 v97, v82, v83\n" \
  "v_cvt_pk_bf16_f32 v98, v84, v85\n" \
  "v_cvt_pk_bf16_f32 v99, v86, v87\n" \
  "buffer_store_dwordx4 v[96:99], v190, s[40:43], s62 offen sc1\n" \
  "v_cvt_pk_bf16_f32 v100, v88, v89\n" \
  "v_cvt_pk_bf16_f32 v101, v90, v91\n" \
  "v_cvt_pk_bf16_f32 v102, v92, v93\n" \
  "v_cvt_pk_bf16_f32 v103, v94, v95\n" \
  "buffer_store_dwordx4 v[100:103], v190, s[40:43], s62 offen offset:1024 sc1\n" \
  "s_waitcnt vmcnt(0)\n" \
  "s_barrier\n" \
  "s_mov_b32 s48, 64\n" \
  "s_mov_b32 s69, 0\n" \
  ".LK_ie_%=:\n" \
  "s_cmp_ge_u32 s96, 0xc0000000\n" \
  "s_cbranch_scc1 .LK_ie_x_%=\n" \
  "buffer_load_dwordx2 v[184:185], off, s[40:43], %41 sc1\n" \
  "s_waitcnt vmcnt(0)\n" \
  "v_readfirstlane_b32 s96, v184\n" \
  "s_add_u32 s69, s69, 1\n" \
  "s_cmp_lt_u32 s69, 2097152\n" \
  "s_cbranch_scc1 .LK_ie_%=\n" \
  ".LK_ie_x_%=:\n" \
  "v_mov_b32 v168, s48\n" \
  "buffer_store_dwordx2 v[168:169], v167, s[40:43], 0 offen sc1\n" \
  "s_branch .LK_end_%=\n" \
  ".LK_mea_%=:\n" \
  "s_waitcnt vmcnt(7)\n" \
  "s_barrier\n" \
  "v_mov_b32 v168, s48\n" \
  "s_mov_b64 exec, 1\n" \
  "buffer_store_dwordx2 v[168:169], v167, s[40:43], 0 offen sc1\n" \
  "s_mov_b64 exec, -1\n" \
  "s_branch .LK_mea_b_%=\n" \
  ".LK_meb_%=:\n" \
  "s_waitcnt vmcnt(7)\n" \
  "s_barrier\n" \
  "v_mov_b32 v168, s48\n" \
  "s_mov_b64 exec, 1\n" \
  "buffer_store_dwordx2 v[168:169], v167, s[40:43], 0 offen sc1\n" \
  "s_mov_b64 exec, -1\n" \
  "s_cmp_lg_u32 s48, 3\n" \
  "s_cbranch_scc1 .LK_in_x_%=\n" \
  "s_mov_b32 s69, 0\n" \
  ".LK_in_%=:\n" \
  "s_cmp_ge_u32 s96, 0xc0000000\n" \
  "s_cbranch_scc1 .LK_in_x_%=\n" \
  "buffer_load_dwordx2 v[184:185], off, s[40:43], %41 sc1\n" \
  "s_waitcnt vmcnt(0)\n" \
  "v_readfirstlane_b32 s96, v184\n" \
  "s_add_u32 s69, s69, 1\n" \
  "s_cmp_lt_u32 s69, 2097152\n" \
  "s_cbranch_scc1 .LK_in_%=\n" \
  ".LK_in_x_%=:\n" \
  "s_branch .LK_meb_b_%=\n" \
  ".LK_end_%=:\n" \
  "s_mov_b32 %0, s55\n"
#define K_FIRST 16
#define U_HELP 8
#define U_ASM_TEXT \
  "s_mov_b64 s[40:41], %30\n" \
  "s_and_b32 s41, s41, 0xffff\n" \
  "s_mov_b32 s42, 255852544\n" \
  "s_mov_b32 s43, 0x00020000\n" \
  "s_mov_b32 s47, 0x00020000\n" \
  "s_lshl_b32 s72, %34, 12\n" \
  "buffer_load_dword v184, off, s[40:43], %42 sc1\n" \
  "s_and_b32 s73, %45, 255\n" \
  "s_bfe_u32 s74, %45, 0x80008\n" \
  "s_lshr_b32 s75, %45, 16\n" \
  "s_mov_b64 s[44:45], %31\n" \
  "s_and_b32 s45, s45, 0xffff\n" \
  "s_mov_b32 s46, 4718592\n" \
  "s_add_u32 s60, %35, 0\n" \
  "s_add_u32 m0, s72, 16384\n" \
  "s_nop 0\n" \
  "buffer_load_dwordx4 v170, s[44:47], s60 offen lds\n" \
  "buffer_load_dwordx4 v171, s[44:47], s60 offen offset:1024 lds\n" \
  "buffer_load_dwordx4 v172, s[44:47], s60 offen offset:2048 lds\n" \
  "buffer_load_dwordx4 v173, s[44:47], s60 offen offset:3072 lds\n" \
  "s_add_u32 s60, %35, 524288\n" \
  "s_add_u32 m0, s72, 32768\n" \
  "s_nop 0\n" \
  "buffer_load_dwordx4 v170, s[44:47], s60 offen lds\n" \
  "buffer_load_dwordx4 v171, s[44:47], s60 offen offset:1024 lds\n" \
  "buffer_load_dwordx4 v172, s[44:47], s60 offen offset:2048 lds\n" \
  "buffer_load_dwordx4 v173, s[44:47], s60 offen offset:3072 lds\n" \
  "s_add_u32 s60, %35, 1048576\n" \
  "s_add_u32 m0, s72, 49152\n" \
  "s_nop 0\n" \
  "buffer_load_dwordx4 v170, s[44:47], s60 offen lds\n" \
  "buffer_load_dwordx4 v171, s[44:47], s60 offen offset:1024 lds\n" \
  "buffer_load_dwordx4 v172, s[44:47], s60 offen offset:2048 lds\n" \
  "buffer_load_dwordx4 v173, s[44:47], s60 offen offset:3072 lds\n" \
  "s_add_u32 s60, %35, 1572864\n" \
  "s_add_u32 m0, s72, 65536\n" \
  "s_nop 0\n" \
  "buffer_load_dwordx4 v170, s[44:47], s60 offen lds\n" \
  "buffer_load_dwordx4 v171, s[44:47], s60 offen offset:1024 lds\n" \
  "buffer_load_dwordx4 v172, s[44:47], s60 offen offset:2048 lds\n" \
  "buffer_load_dwordx4 v173, s[44:47], s60 offen offset:3072 lds\n" \
  "s_add_u32 s60, %35, 2097152\n" \
  "s_add_u32 m0, s72, 81920\n" \
  "s_nop 0\n" \
  "buffer_load_dwordx4 v170, s[44:47], s60 offen lds\n" \
  "buffer_load_dwordx4 v171, s[44:47], s60 offen offset:1024 lds\n" \
  "buffer_load_dwordx4 v172, s[44:47], s60 offen offset:2048 lds\n" \
  "buffer_load_dwordx4 v173, s[44:47], s60 offen offset:3072 lds\n" \
  "s_add_u32 s60, %35, 2621440\n" \
  "s_add_u32 m0, s72, 98304\n" \
  "s_nop 0\n" \
  "buffer_load_dwordx4 v170, s[44:47], s60 offen lds\n" \
  "buffer_load_dwordx4 v171, s[44:47], s60 offen offset:1024 lds\n" \
  "buffer_load_dwordx4 v172, s[44:47], s60 offen offset:2048 lds\n" \
  "buffer_load_dwordx4 v173, s[44:47], s60 offen offset:3072 lds\n" \
  "s_add_u32 s60, %35, 3145728\n" \
  "s_add_u32 m0, s72, 114688\n" \
  "s_nop 0\n" \
  "buffer_load_dwordx4 v170, s[44:47], s60 offen lds\n" \
  "buffer_load_dwordx4 v171, s[44:47], s60 offen offset:1024 lds\n" \
  "buffer_load_dwordx4 v172, s[44:47], s60 offen offset:2048 lds\n" \
  "buffer_load_dwordx4 v173, s[44:47], s60 offen offset:3072 lds\n" \
  "s_add_u32 s60, %35, 3670016\n" \
  "s_add_u32 m0, s72, 131072\n" \
  "s_nop 0\n" \
  "buffer_load_dwordx4 v170, s[44:47], s60 offen lds\n" \
  "buffer_load_dwordx4 v171, s[44:47], s60 offen offset:1024 lds\n" \
  "buffer_load_dwordx4 v172, s[44:47], s60 offen offset:2048 lds\n" \
  "buffer_load_dwordx4 v173, s[44:47], s60 offen offset:3072 lds\n" \
  "s_add_u32 s60, %35, 4194304\n" \
  "s_add_u32 m0, s72, 147456\n" \
  "s_nop 0\n" \
  "buffer_load_dwordx4 v170, s[44:47], s60 offen lds\n" \
  "buffer_load_dwordx4 v171, s[44:47], s60 offen offset:1024 lds\n" \
  "buffer_load_dwordx4 v172, s[44:47], s60 offen offset:2048 lds\n" \
  "buffer_load_dwordx4 v173, s[44:47], s60 offen offset:3072 lds\n" \
  "s_waitcnt vmcnt(24)\n" \
  "s_barrier\n" \
  "v_add_u32 v186, 16384, v182\n" \
  "ds_read_b64_tr_b16 a[0:1], v186\n" \
  "ds_read_b64_tr_b16 a[2:3], v186 offset:1024\n" \
  "ds_read_b64_tr_b16 a[4:5], v186 offset:8192\n" \
  "ds_read_b64_tr_b16 a[6:7], v186 offset:9216\n" \
  "v_add_u32 v187, 16384, v183\n" \
  "ds_read_b64_tr_b16 a[72:73], v187\n" \
  "ds_read_b64_tr_b16 a[74:75], v187 offset:1024\n" \
  "ds_read_b64_tr_b16 a[76:77], v187 offset:8192\n" \
  "ds_read_b64_tr_b16 a[78:79], v187 offset:9216\n" \
  "v_add_u32 v186, 32768, v182\n" \
  "ds_read_b64_tr_b16 a[8:9], v186\n" \
  "ds_read_b64_tr_b16 a[10:11], v186 offset:1024\n" \
  "ds_read_b64_tr_b16 a[12:13], v186 offset:8192\n" \
  "ds_read_b64_tr_b16 a[14:15], v186 offset:9216\n" \
  "v_add_u32 v187, 32768, v183\n" \
  "ds_read_b64_tr_b16 a[80:81], v187\n" \
  "ds_read_b64_tr_b16 a[82:83], v187 offset:1024\n" \
  "ds_read_b64_tr_b16 a[84:85], v187 offset:8192\n" \
  "ds_read_b64_tr_b16 a[86:87], v187 offset:9216\n" \
  "v_add_u32 v186, 49152, v182\n" \
  "ds_read_b64_tr_b16 a[16:17], v186\n" \
  "ds_read_b64_tr_b16 a[18:19], v186 offset:1024\n" \
  "ds_read_b64_tr_b16 a[20:21], v186 offset:8192\n" \
  "ds_read_b64_tr_b16 a[22:23], v186 offset:9216\n" \
  "v_add_u32 v187, 49152, v183\n" \
  "ds_read_b64_tr_b16 a[88:89], v187\n" \
  "ds_read_b64_tr_b16 a[90:91], v187 offset:1024\n" \
  "ds_read_b64_tr_b16 a[92:93], v187 offset:8192\n" \
  "ds_read_b64_tr_b16 a[94:95], v187 offset:9216\n" \
  "s_waitcnt lgkmcnt(0)\n" \
  "s_barrier\n" \
  "s_mov_b64 s[44:45], %32\n" \
  "s_and_b32 s45, s45, 0xffff\n" \
  "s_mov_b32 s46, 4718592\n" \
  "s_add_u32 s60, %36, 0\n" \
  "s_add_u32 m0, s72, 16384\n" \
  "s_nop 0\n" \
  "buffer_load_dwordx4 v174, s[44:47], s60 offen lds\n" \
  "buffer_load_dwordx4 v175, s[44:47], s60 offen offset:1024 lds\n" \
  "buffer_load_dwordx4 v176, s[44:47], s60 offen offset:2048 lds\n" \
  "buffer_load_dwordx4 v177, s[44:47], s60 offen offset:3072 lds\n" \
  "s_add_u32 s60, %36, 73728\n" \
  "s_add_u32 m0, s72, 32768\n" \
  "s_nop 0\n" \
  "buffer_load_dwordx4 v174, s[44:47], s60 offen lds\n" \
  "buffer_load_dwordx4 v175, s[44:47], s60 offen offset:1024 lds\n" \
  "buffer_load_dwordx4 v176, s[44:47], s60 offen offset:2048 lds\n" \
  "buffer_load_dwordx4 v177, s[44:47], s60 offen offset:3072 lds\n" \
  "s_add_u32 s60, %36, 256\n" \
  "s_add_u32 m0, s72, 49152\n" \
  "s_nop 0\n" \
  "buffer_load_dwordx4 v174, s[44:47], s60 offen lds\n" \
  "buffer_load_dwordx4 v175, s[44:47], s60 offen offset:1024 lds\n" \
  "buffer_load_dwordx4 v176, s[44:47], s60 offen offset:2048 lds\n" \
  "buffer_load_dwordx4 v177, s[44:47], s60 offen offset:3072 lds\n" \
  "v_readfirstlane_b32 s55, v184\n" \
  "s_add_u32 s55, s55, 1\n" \
  "s_waitcnt vmcnt(24)\n" \
  "s_barrier\n" \
  "v_add_u32 v186, 65536, v182\n" \
  "ds_read_b64_tr_b16 a[24:25], v186\n" \
  "ds_read_b64_tr_b16 a[26:27], v186 offset:1024\n" \
  "ds_read_b64_tr_b16 a[28:29], v186 offset:8192\n" \
  "ds_read_b64_tr_b16 a[30:31], v186 offset:9216\n" \
  "v_add_u32 v187, 65536, v183\n" \
  "ds_read_b64_tr_b16 a[96:97], v187\n" \
  "ds_read_b64_tr_b16 a[98:99], v187 offset:1024\n" \
  "ds_read_b64_tr_b16 a[100:101], v187 offset:8192\n" \
  "ds_read_b64_tr_b16 a[102:103], v187 offset:9216\n" \
  "v_add_u32 v186, 81920, v182\n" \
  "ds_read_b64_tr_b16 a[32:33], v186\n" \
  "ds_read_b64_tr_b16 a[34:35], v186 offset:1024\n" \
  "ds_read_b64_tr_b16 a[36:37], v186 offset:8192\n" \
  "ds_read_b64_tr_b16 a[38:39], v186 offset:9216\n" \
  "v_add_u32 v187, 81920, v183\n" \
  "ds_read_b64_tr_b16 a[104:105], v187\n" \
  "ds_read_b64_tr_b16 a[106:107], v187 offset:1024\n" \
  "ds_read_b64_tr_b16 a[108:109], v187 offset:8192\n" \
  "ds_read_b64_tr_b16 a[110:111], v187 offset:9216\n" \
  "v_add_u32 v186, 98304, v182\n" \
  "ds_read_b64_tr_b16 a[40:41], v186\n" \
  "ds_read_b64_tr_b16 a[42:43], v186 offset:1024\n" \
  "ds_read_b64_tr_b16 a[44:45], v186 offset:8192\n" \
  "ds_read_b64_tr_b16 a[46:47], v186 offset:9216\n" \
  "v_add_u32 v187, 98304, v183\n" \
  "ds_read_b64_tr_b16 a[112:113], v187\n" \
  "ds_read_b64_tr_b16 a[114:115], v187 offset:1024\n" \
  "ds_read_b64_tr_b16 a[116:117], v187 offset:8192\n" \
  "ds_read_b64_tr_b16 a[118:119], v187 offset:9216\n" \
  "s_waitcnt lgkmcnt(0)\n" \
  "s_barrier\n" \
  "s_add_u32 s60, %36, 73984\n" \
  "s_add_u32 m0, s72, 65536\n" \
  "s_nop 0\n" \
  "buffer_load_dwordx4 v174, s[44:47], s60 offen lds\n" \
  "buffer_load_dwordx4 v175, s[44:47], s60 offen offset:1024 lds\n" \
  "buffer_load_dwordx4 v176, s[44:47], s60 offen offset:2048 lds\n" \
  "buffer_load_dwordx4 v177, s[44:47], s60 offen offset:3072 lds\n" \
  "s_add_u32 s60, %36, 512\n" \
  "s_add_u32 m0, s72, 81920\n" \
  "s_nop 0\n" \
  "buffer_load_dwordx4 v174, s[44:47], s60 offen lds\n" \
  "buffer_load_dwordx4 v175, s[44:47], s60 offen offset:1024 lds\n" \
  "buffer_load_dwordx4 v176, s[44:47], s60 offen offset:2048 lds\n" \
  "buffer_load_dwordx4 v177, s[44:47], s60 offen offset:3072 lds\n" \
  "s_add_u32 s60, %36, 74240\n" \
  "s_add_u32 m0, s72, 98304\n" \
  "s_nop 0\n" \
  "buffer_load_dwordx4 v174, s[44:47], s60 offen lds\n" \
  "buffer_load_dwordx4 v175, s[44:47], s60 offen offset:1024 lds\n" \
  "buffer_load_dwordx4 v176, s[44:47], s60 offen offset:2048 lds\n" \
  "buffer_load_dwordx4 v177, s[44:47], s60 offen offset:3072 lds\n" \
  "s_waitcnt vmcnt(24)\n" \
  "s_barrier\n" \
  "v_add_u32 v186, 114688, v182\n" \
  "ds_read_b64_tr_b16 a[48:49], v186\n" \
  "ds_read_b64_tr_b16 a[50:51], v186 offset:1024\n" \
  "ds_read_b64_tr_b16 a[52:53], v186 offset:8192\n" \
  "ds_read_b64_tr_b16 a[54:55], v186 offset:9216\n" \
  "v_add_u32 v187, 114688, v183\n" \
  "ds_read_b64_tr_b16 a[120:121], v187\n" \
  "ds_read_b64_tr_b16 a[122:123], v187 offset:1024\n" \
  "ds_read_b64_tr_b16 a[124:125], v187 offset:8192\n" \
  "ds_read_b64_tr_b16 a[126:127], v187 offset:9216\n" \
  "v_add_u32 v186, 131072, v182\n" \
  "ds_read_b64_tr_b16 a[56:57], v186\n" \
  "ds_read_b64_tr_b16 a[58:59], v186 offset:1024\n" \
  "ds_read_b64_tr_b16 a[60:61], v186 offset:8192\n" \
  "ds_read_b64_tr_b16 a[62:63], v186 offset:9216\n" \
  "v_add_u32 v187, 131072, v183\n" \
  "ds_read_b64_tr_b16 a[128:129], v187\n" \
  "ds_read_b64_tr_b16 a[130:131], v187 offset:1024\n" \
  "ds_read_b64_tr_b16 a[132:133], v187 offset:8192\n" \
  "ds_read_b64_tr_b16 a[134:135], v187 offset:9216\n" \
  "v_add_u32 v186, 147456, v182\n" \
  "ds_read_b64_tr_b16 a[64:65], v186\n" \
  "ds_read_b64_tr_b16 a[66:67], v186 offset:1024\n" \
  "ds_read_b64_tr_b16 a[68:69], v186 offset:8192\n" \
  "ds_read_b64_tr_b16 a[70:71], v186 offset:9216\n" \
  "v_add_u32 v187, 147456, v183\n" \
  "ds_read_b64_tr_b16 a[136:137], v187\n" \
  "ds_read_b64_tr_b16 a[138:139], v187 offset:1024\n" \
  "ds_read_b64_tr_b16 a[140:141], v187 offset:8192\n" \
  "ds_read_b64_tr_b16 a[142:143], v187 offset:9216\n" \
  "s_waitcnt lgkmcnt(0)\n" \
  "s_barrier\n" \
  "s_add_u32 s60, %36, 768\n" \
  "s_add_u32 m0, s72, 114688\n" \
  "s_nop 0\n" \
  "buffer_load_dwordx4 v174, s[44:47], s60 offen lds\n" \
  "buffer_load_dwordx4 v175, s[44:47], s60 offen offset:1024 lds\n" \
  "buffer_load_dwordx4 v176, s[44:47], s60 offen offset:2048 lds\n" \
  "buffer_load_dwordx4 v177, s[44:47], s60 offen offset:3072 lds\n" \
  "s_add_u32 s60, %36, 74496\n" \
  "s_add_u32 m0, s72, 131072\n" \
  "s_nop 0\n" \
  "buffer_load_dwordx4 v174, s[44:47], s60 offen lds\n" \
  "buffer_load_dwordx4 v175, s[44:47], s60 offen offset:1024 lds\n" \
  "buffer_load_dwordx4 v176, s[44:47], s60 offen offset:2048 lds\n" \
  "buffer_load_dwordx4 v177, s[44:47], s60 offen offset:3072 lds\n" \
  "s_add_u32 s60, %36, 0\n" \
  "s_add_u32 m0, s72, 147456\n" \
  "s_nop 0\n" \
  "buffer_load_dwordx4 v178, s[44:47], s60 offen lds\n" \
  "buffer_load_dwordx4 v179, s[44:47], s60 offen offset:1024 lds\n" \
  "buffer_load_dwordx4 v180, s[44:47], s60 offen offset:2048 lds\n" \
  "buffer_load_dwordx4 v181, s[44:47], s60 offen offset:3072 lds\n" \
  "s_waitcnt vmcnt(24)\n" \
  "s_barrier\n" \
  "v_add_u32 v186, 16384, v182\n" \
  "ds_read_b64_tr_b16 a[144:145], v186\n" \
  "ds_read_b64_tr_b16 a[146:147], v186 offset:1024\n" \
  "ds_read_b64_tr_b16 a[148:149], v186 offset:8192\n" \
  "ds_read_b64_tr_b16 a[150:151], v186 offset:9216\n" \
  "v_add_u32 v187, 16384, v183\n" \
  "ds_read_b64_tr_b16 a[160:161], v187\n" \
  "ds_read_b64_tr_b16 a[162:163], v187 offset:1024\n" \
  "ds_read_b64_tr_b16 a[164:165], v187 offset:8192\n" \
  "ds_read_b64_tr_b16 a[166:167], v187 offset:9216\n" \
  "v_add_u32 v186, 32768, v182\n" \
  "ds_read_b64_tr_b16 a[152:153], v186\n" \
  "ds_read_b64_tr_b16 a[154:155], v186 offset:1024\n" \
  "ds_read_b64_tr_b16 a[156:157], v186 offset:8192\n" \
  "ds_read_b64_tr_b16 a[158:159], v186 offset:9216\n" \
  "v_add_u32 v187, 32768, v183\n" \
  "ds_read_b64_tr_b16 a[168:169], v187\n" \
  "ds_read_b64_tr_b16 a[170:171], v187 offset:1024\n" \
  "ds_read_b64_tr_b16 a[172:173], v187 offset:8192\n" \
  "ds_read_b64_tr_b16 a[174:175], v187 offset:9216\n" \
  "v_add_u32 v186, 49152, v182\n" \
  "ds_read_b64_tr_b16 a[176:177], v186\n" \
  "ds_read_b64_tr_b16 a[178:179], v186 offset:1024\n" \
  "ds_read_b64_tr_b16 a[180:181], v186 offset:8192\n" \
  "ds_read_b64_tr_b16 a[182:183], v186 offset:9216\n" \
  "v_add_u32 v187, 49152, v183\n" \
  "ds_read_b64_tr_b16 a[192:193], v187\n" \
  "ds_read_b64_tr_b16 a[194:195], v187 offset:1024\n" \
  "ds_read_b64_tr_b16 a[196:197], v187 offset:8192\n" \
  "ds_read_b64_tr_b16 a[198:199], v187 offset:9216\n" \
  "s_waitcnt lgkmcnt(0)\n" \
  "s_barrier\n" \
  "s_mov_b64 s[44:45], %33\n" \
  "s_and_b32 s45, s45, 0xffff\n" \
  "s_mov_b32 s46, 2359296\n" \
  "s_mul_i32 s51, s73, 36864\n" \
  "s_mul_i32 s52, %34, 9216\n" \
  "s_add_u32 s52, s52, 16384\n" \
  "s_add_u32 m0, s52, 0\n" \
  "s_nop 0\n" \
  "buffer_load_dwordx4 v196, s[44:47], s51 offen lds\n" \
  "s_add_u32 m0, s52, 1024\n" \
  "s_nop 0\n" \
  "buffer_load_dwordx4 v197, s[44:47], s51 offen lds\n" \
  "s_add_u32 m0, s52, 2048\n" \
  "s_nop 0\n" \
  "buffer_load_dwordx4 v198, s[44:47], s51 offen lds\n" \
  "s_add_u32 m0, s52, 3072\n" \
  "s_nop 0\n" \
  "buffer_load_dwordx4 v199, s[44:47], s51 offen lds\n" \
  "s_add_u32 m0, s52, 4096\n" \
  "s_nop 0\n" \
  "buffer_load_dwordx4 v200, s[44:47], s51 offen lds\n" \
  "s_add_u32 m0, s52, 5120\n" \
  "s_nop 0\n" \
  "buffer_load_dwordx4 v201, s[44:47], s51 offen lds\n" \
  "s_add_u32 m0, s52, 6144\n" \
  "s_nop 0\n" \
  "buffer_load_dwordx4 v202, s[44:47], s51 offen lds\n" \
  "s_add_u32 m0, s52, 7168\n" \
  "s_nop 0\n" \
  "buffer_load_dwordx4 v203, s[44:47], s51 offen lds\n" \
  "s_add_u32 m0, s52, 8192\n" \
  "s_nop 0\n" \
  "buffer_load_dwordx4 v204, s[44:47], s51 offen lds\n" \
  "s_add_u32 s51, s51, 36864\n" \
  "s_min_u32 s51, s51, 2322432\n" \
  "s_add_u32 s52, s52, 36864\n" \
  "s_cmp_ge_u32 s52, 163840\n" \
  "s_cselect_b32 s60, 147456, 0\n" \
  "s_sub_u32 s52, s52, s60\n" \
  "s_waitcnt vmcnt(21)\n" \
  "s_barrier\n" \
  "v_add_u32 v186, 65536, v182\n" \
  "ds_read_b64_tr_b16 a[184:185], v186\n" \
  "ds_read_b64_tr_b16 a[186:187], v186 offset:1024\n" \
  "ds_read_b64_tr_b16 a[188:189], v186 offset:8192\n" \
  "ds_read_b64_tr_b16 a[190:191], v186 offset:9216\n" \
  "v_add_u32 v187, 65536, v183\n" \
  "ds_read_b64_tr_b16 a[200:201], v187\n" \
  "ds_read_b64_tr_b16 a[202:203], v187 offset:1024\n" \
  "ds_read_b64_tr_b16 a[204:205], v187 offset:8192\n" \
  "ds_read_b64_tr_b16 a[206:207], v187 offset:9216\n" \
  "v_add_u32 v186, 81920, v182\n" \
  "ds_read_b64_tr_b16 a[208:209], v186\n" \
  "ds_read_b64_tr_b16 a[210:211], v186 offset:1024\n" \
  "ds_read_b64_tr_b16 a[212:213], v186 offset:8192\n" \
  "ds_read_b64_tr_b16 a[214:215], v186 offset:9216\n" \
  "v_add_u32 v187, 81920, v183\n" \
  "ds_read_b64_tr_b16 a[224:225], v187\n" \
  "ds_read_b64_tr_b16 a[226:227], v187 offset:1024\n" \
  "ds_read_b64_tr_b16 a[228:229], v187 offset:8192\n" \
  "ds_read_b64_tr_b16 a[230:231], v187 offset:9216\n" \
  "v_add_u32 v186, 98304, v182\n" \
  "ds_read_b64_tr_b16 a[216:217], v186\n" \
  "ds_read_b64_tr_b16 a[218:219], v186 offset:1024\n" \
  "ds_read_b64_tr_b16 a[220:221], v186 offset:8192\n" \
  "ds_read_b64_tr_b16 a[222:223], v186 offset:9216\n" \
  "v_add_u32 v187, 98304, v183\n" \
  "ds_read_b64_tr_b16 a[232:233], v187\n" \
  "ds_read_b64_tr_b16 a[234:235], v187 offset:1024\n" \
  "ds_read_b64_tr_b16 a[236:237], v187 offset:8192\n" \
  "ds_read_b64_tr_b16 a[238:239], v187 offset:9216\n" \
  "s_waitcnt lgkmcnt(0)\n" \
  "s_barrier\n" \
  "buffer_load_dwordx2 v[184:185], off, s[40:43], %41 sc1\n" \
  "s_add_u32 m0, s52, 0\n" \
  "s_nop 0\n" \
  "buffer_load_dwordx4 v196, s[44:47], s51 offen lds\n" \
  "s_add_u32 m0, s52, 1024\n" \
  "s_nop 0\n" \
  "buffer_load_dwordx4 v197, s[44:47], s51 offen lds\n" \
  "s_add_u32 m0, s52, 2048\n" \
  "s_nop 0\n" \
  "buffer_load_dwordx4 v198, s[44:47], s51 offen lds\n" \
  "s_add_u32 m0, s52, 3072\n" \
  "s_nop 0\n" \
  "buffer_load_dwordx4 v199, s[44:47], s51 offen lds\n" \
  "s_add_u32 m0, s52, 4096\n" \
  "s_nop 0\n" \
  "buffer_load_dwordx4 v200, s[44:47], s51 offen lds\n" \
  "s_add_u32 m0, s52, 5120\n" \
  "s_nop 0\n" \
  "buffer_load_dwordx4 v201, s[44:47], s51 offen lds\n" \
  "s_add_u32 m0, s52, 6144\n" \
  "s_nop 0\n" \
  "buffer_load_dwordx4 v202, s[44:47], s51 offen lds\n" \
  "s_add_u32 m0, s52, 7168\n" \
  "s_nop 0\n" \
  "buffer_load_dwordx4 v203, s[44:47], s51 offen lds\n" \
  "s_add_u32 m0, s52, 8192\n" \
  "s_nop 0\n" \
  "buffer_load_dwordx4 v204, s[44:47], s51 offen lds\n" \
  "s_add_u32 s51, s51, 36864\n" \
  "s_min_u32 s51, s51, 2322432\n" \
  "s_add_u32 s52, s52, 36864\n" \
  "s_cmp_ge_u32 s52, 163840\n" \
  "s_cselect_b32 s60, 147456, 0\n" \
  "s_sub_u32 s52, s52, s60\n" \
  "s_waitcnt vmcnt(19)\n" \
  "s_barrier\n" \
  "v_add_u32 v186, 114688, v182\n" \
  "ds_read_b64_tr_b16 a[240:241], v186\n" \
  "ds_read_b64_tr_b16 a[242:243], v186 offset:1024\n" \
  "ds_read_b64_tr_b16 a[244:245], v186 offset:8192\n" \
  "ds_read_b64_tr_b16 a[246:247], v186 offset:9216\n" \
  "v_add_u32 v187, 114688, v183\n" \
  "ds_read_b64_tr_b16 v[0:1], v187\n" \
  "ds_read_b64_tr_b16 v[2:3], v187 offset:1024\n" \
  "ds_read_b64_tr_b16 v[4:5], v187 offset:8192\n" \
  "ds_read_b64_tr_b16 v[6:7], v187 offset:9216\n" \
  "v_add_u32 v186, 131072, v182\n" \
  "ds_read_b64_tr_b16 a[248:249], v186\n" \
  "ds_read_b64_tr_b16 a[250:251], v186 offset:1024\n" \
  "ds_read_b64_tr_b16 a[252:253], v186 offset:8192\n" \
  "ds_read_b64_tr_b16 a[254:255], v186 offset:9216\n" \
  "v_add_u32 v187, 131072, v183\n" \
  "ds_read_b64_tr_b16 v[8:9], v187\n" \
  "ds_read_b64_tr_b16 v[10:11], v187 offset:1024\n" \
  "ds_read_b64_tr_b16 v[12:13], v187 offset:8192\n" \
  "ds_read_b64_tr_b16 v[14:15], v187 offset:9216\n" \
  "v_add_u32 v186, 147456, v182\n" \
  "ds_read_b64_tr_b16 v[16:17], v186\n" \
  "ds_read_b64_tr_b16 v[18:19], v186 offset:1024\n" \
  "ds_read_b64_tr_b16 v[20:21], v186 offset:8192\n" \
  "ds_read_b64_tr_b16 v[22:23], v186 offset:9216\n" \
  "v_add_u32 v187, 147456, v183\n" \
  "ds_read_b64_tr_b16 v[24:25], v187\n" \
  "ds_read_b64_tr_b16 v[26:27], v187 offset:1024\n" \
  "ds_read_b64_tr_b16 v[28:29], v187 offset:8192\n" \
  "ds_read_b64_tr_b16 v[30:31], v187 offset:9216\n" \
  "s_waitcnt lgkmcnt(0)\n" \
  "s_mul_i32 s60, s75, 36864\n" \
  "s_add_u32 s51, s51, s60\n" \
  "s_add_u32 s75, s75, 1\n" \
  "s_mov_b32 s69, 0\n" \
  "s_waitcnt vmcnt(9)\n" \
  "s_branch .LU_go1_%=\n" \
  ".LU_go_%=:\n" \
  "buffer_load_dwordx2 v[184:185], off, s[40:43], %41 sc1\n" \
  "s_waitcnt vmcnt(0)\n" \
  ".LU_go1_%=:\n" \
  "v_readfirstlane_b32 s96, v184\n" \
  "v_readfirstlane_b32 s63, v185\n" \
  "s_cmp_eq_u32 s63, s55\n" \
  "s_cselect_b32 s96, s96, 0\n" \
  "s_cmp_ge_u32 s96, 0x80000000\n" \
  "s_cbranch_scc1 .LU_go_x_%=\n" \
  "s_sleep 4\n" \
  "s_add_u32 s69, s69, 1\n" \
  "s_cmp_lt_u32 s69, 2097152\n" \
  "s_cbranch_scc1 .LU_go_%=\n" \
  "v_mov_b32 v184, 1\n" \
  "v_mov_b32 v185, s55\n" \
  "buffer_store_dwordx2 v[184:185], off, s[40:43], %42 offset:128 sc1\n" \
  ".LU_go_x_%=:\n" \
  "s_and_b32 s60, s96, 7\n" \
  "s_lshl_b32 s60, s60, 12\n" \
  "s_xor_b32 s53, %39, s60\n" \
  "s_xor_b32 s54, %40, s60\n" \
  "s_andn2_b32 s60, 2, s96\n" \
  "s_lshl_b32 s60, s60, 12\n" \
  "s_add_u32 s49, %37, s60\n" \
  "s_add_u32 s50, %38, s60\n" \
  "s_xor_b32 s66, %38, s60\n" \
  "s_xor_b32 s59, %44, s60\n" \
  "s_or_b32 s50, s50, 0x40000000\n" \
  "s_mov_b32 s57, 2130706432\n" \
  "s_mov_b32 s48, 0\n" \
  "v_mov_b32 v169, s55\n" \
  "v_mov_b32 v167, 2130706432\n" \
  "v_mov_b32 v166, 0\n" \
  "s_mov_b64 exec, 3\n" \
  "v_mov_b32 v167, s54\n" \
  "s_mov_b64 exec, 1\n" \
  "v_mov_b32 v167, s53\n" \
  "v_mov_b32 v166, 0\n" \
  "s_mov_b64 exec, -1\n" \
  "s_barrier\n" \
  "ds_read_b128 v[32:35], v191\n" \
  "ds_read_b128 v[36:39], v191 offset:18432\n" \
  "ds_read_b128 v[40:43], v192\n" \
  "ds_read_b128 v[44:47], v192 offset:18432\n" \
  "ds_read_b128 v[48:51], v191 offset:128\n" \
  "ds_read_b128 v[52:55], v191 offset:18560\n" \
  "s_waitcnt lgkmcnt(0)\n" \
  "v_mov_b64 v[164:165], 0\n" \
  ".LU_loop_%=:\n" \
  "v_mfma_f32_16x16x32_bf16 v[64:67], v[32:35], a[0:3], 0\n" \
  "ds_read_b128 v[56:59], v192 offset:128\n" \
  "v_mfma_f32_16x16x32_bf16 v[72:75], v[36:39], a[0:3], 0\n" \
  "ds_read_b128 v[60:63], v192 offset:18560\n" \
  "v_mfma_f32_16x16x32_bf16 v[68:71], v[32:35], a[72:75], 0\n" \
  "v_cvt_pk_bf16_f32 v104, v88, v89\n" \
  "v_cvt_pk_bf16_f32 v105, v90, v91\n" \
  "v_mfma_f32_16x16x32_bf16 v[76:79], v[36:39], a[72:75], 0\n" \
  "v_cvt_pk_bf16_f32 v106, v92, v93\n" \
  "v_cvt_pk_bf16_f32 v107, v94, v95\n" \
  "s_mov_b32 s63, s57\n" \
  "v_mfma_f32_16x16x32_bf16 v[64:67], v[40:43], a[4:7], v[64:67]\n" \
  "ds_read_b128 v[32:35], v191 offset:256\n" \
  "v_mfma_f32_16x16x32_bf16 v[72:75], v[44:47], a[4:7], v[72:75]\n" \
  "ds_read_b128 v[36:39], v191 offset:18688\n" \
  "v_mfma_f32_16x16x32_bf16 v[68:71], v[40:43], a[76:79], v[68:71]\n" \
  "buffer_store_dwordx4 v[104:107], v190, s[40:43], s63 offen sc1\n" \
  "v_mfma_f32_16x16x32_bf16 v[76:79], v[44:47], a[76:79], v[76:79]\n" \
  "v_mfma_f32_16x16x32_bf16 v[64:67], v[48:51], a[8:11], v[64:67]\n" \
  "ds_read_b128 v[40:43], v192 offset:256\n" \
  "v_mfma_f32_16x16x32_bf16 v[72:75], v[52:55], a[8:11], v[72:75]\n" \
  "ds_read_b128 v[44:47], v192 offset:18688\n" \
  "v_mfma_f32_16x16x32_bf16 v[68:71], v[48:51], a[80:83], v[68:71]\n" \
  "s_add_u32 m0, s52, 0\n" \
  "v_mfma_f32_16x16x32_bf16 v[76:79], v[52:55], a[80:83], v[76:79]\n" \
  "buffer_load_dwordx4 v196, s[44:47], s51 offen lds\n" \
  "s_waitcnt lgkmcnt(5)\n" \
  "v_mfma_f32_16x16x32_bf16 v[64:67], v[56:59], a[12:15], v[64:67]\n" \
  "ds_read_b128 v[48:51], v191 offset:384\n" \
  "s_add_u32 s58, s73, 15\n" \
  "s_waitcnt lgkmcnt(5)\n" \
  "v_mfma_f32_16x16x32_bf16 v[72:75], v[60:63], a[12:15], v[72:75]\n" \
  "ds_read_b128 v[52:55], v191 offset:18816\n" \
  "s_and_b32 s58, s58, 15\n" \
  "v_mfma_f32_16x16x32_bf16 v[68:71], v[56:59], a[84:87], v[68:71]\n" \
  "s_lshl_b32 s58, s58, 16\n" \
  "s_xor_b32 s50, s58, s66\n" \
  "s_xor_b32 s57, s58, s59\n" \
  "v_mfma_f32_16x16x32_bf16 v[76:79], v[60:63], a[84:87], v[76:79]\n" \
  "s_waitcnt lgkmcnt(5)\n" \
  "v_mfma_f32_16x16x32_bf16 v[64:67], v[32:35], a[16:19], v[64:67]\n" \
  "ds_read_b128 v[56:59], v192 offset:384\n" \
  "s_waitcnt lgkmcnt(5)\n" \
  "v_mfma_f32_16x16x32_bf16 v[72:75], v[36:39], a[16:19], v[72:75]\n" \
  "ds_read_b128 v[60:63], v192 offset:18816\n" \
  "v_mfma_f32_16x16x32_bf16 v[68:71], v[32:35], a[88:91], v[68:71]\n" \
  "s_add_u32 m0, s52, 1024\n" \
  "v_mfma_f32_16x16x32_bf16 v[76:79], v[36:39], a[88:91], v[76:79]\n" \
  "buffer_load_dwordx4 v197, s[44:47], s51 offen lds\n" \
  "s_waitcnt lgkmcnt(5)\n" \
  "v_mfma_f32_16x16x32_bf16 v[64:67], v[40:43], a[20:23], v[64:67]\n" \
  "ds_read_b128 v[32:35], v191 offset:512\n" \
  "s_waitcnt lgkmcnt(5)\n" \
  "v_mfma_f32_16x16x32_bf16 v[72:75], v[44:47], a[20:23], v[72:75]\n" \
  "ds_read_b128 v[36:39], v191 offset:18944\n" \
  "v_mfma_f32_16x16x32_bf16 v[68:71], v[40:43], a[92:95], v[68:71]\n" \
  "v_mfma_f32_16x16x32_bf16 v[76:79], v[44:47], a[92:95], v[76:79]\n" \
  "s_waitcnt lgkmcnt(5)\n" \
  "v_mfma_f32_16x16x32_bf16 v[64:67], v[48:51], a[24:27], v[64:67]\n" \
  "ds_read_b128 v[40:43], v192 offset:512\n" \
  "s_waitcnt lgkmcnt(5)\n" \
  "v_xor_b32 v161, 8, v194\n" \
  "v_mfma_f32_16x16x32_bf16 v[72:75], v[52:55], a[24:27], v[72:75]\n" \
  "ds_read_b128 v[44:47], v192 offset:18944\n" \
  "v_mfma_f32_16x16x32_bf16 v[68:71], v[48:51], a[96:99], v[68:71]\n" \
  "s_add_u32 m0, s52, 2048\n" \
  "v_add_u32 v168, s73, v166\n" \
  "v_mfma_f32_16x16x32_bf16 v[76:79], v[52:55], a[96:99], v[76:79]\n" \
  "buffer_load_dwordx4 v198, s[44:47], s51 offen lds\n" \
  "s_waitcnt lgkmcnt(5)\n" \
  "v_mfma_f32_16x16x32_bf16 v[64:67], v[56:59], a[28:31], v[64:67]\n" \
  "ds_read_b128 v[48:51], v191 offset:640\n" \
  "s_waitcnt lgkmcnt(5)\n" \
  "v_mfma_f32_16x16x32_bf16 v[72:75], v[60:63], a[28:31], v[72:75]\n" \
  "ds_read_b128 v[52:55], v191 offset:19072\n" \
  "v_mfma_f32_16x16x32_bf16 v[68:71], v[56:59], a[100:103], v[68:71]\n" \
  "s_waitcnt vmcnt(11)\n" \
  "s_barrier\n" \
  "v_mfma_f32_16x16x32_bf16 v[76:79], v[60:63], a[100:103], v[76:79]\n" \
  "ds_read_b64_tr_b16 v[104:105], v194\n" \
  "s_waitcnt lgkmcnt(6)\n" \
  "v_mfma_f32_16x16x32_bf16 v[64:67], v[32:35], a[32:35], v[64:67]\n" \
  "ds_read_b128 v[56:59], v192 offset:640\n" \
  "s_waitcnt lgkmcnt(6)\n" \
  "v_mfma_f32_16x16x32_bf16 v[72:75], v[36:39], a[32:35], v[72:75]\n" \
  "ds_read_b128 v[60:63], v192 offset:19072\n" \
  "v_mfma_f32_16x16x32_bf16 v[68:71], v[32:35], a[104:107], v[68:71]\n" \
  "s_add_u32 m0, s52, 3072\n" \
  "ds_read_b64_tr_b16 v[106:107], v194 offset:256\n" \
  "v_mfma_f32_16x16x32_bf16 v[76:79], v[36:39], a[104:107], v[76:79]\n" \
  "buffer_load_dwordx4 v199, s[44:47], s51 offen lds\n" \
  "s_waitcnt lgkmcnt(7)\n" \
  "v_mfma_f32_16x16x32_bf16 v[64:67], v[40:43], a[36:39], v[64:67]\n" \
  "ds_read_b128 v[32:35], v191 offset:768\n" \
  "s_waitcnt lgkmcnt(7)\n" \
  "v_mfma_f32_16x16x32_bf16 v[72:75], v[44:47], a[36:39], v[72:75]\n" \
  "ds_read_b128 v[36:39], v191 offset:19200\n" \
  "v_mfma_f32_16x16x32_bf16 v[68:71], v[40:43], a[108:111], v[68:71]\n" \
  "s_cmp_le_u32 s48, 7\n" \
  "s_cbranch_scc1 .LU_e2_%=\n" \
  ".LU_e2_b_%=:\n" \
  "v_mfma_f32_16x16x32_bf16 v[76:79], v[44:47], a[108:111], v[76:79]\n" \
  "ds_read_b64_tr_b16 v[108:109], v161\n" \
  "s_waitcnt lgkmcnt(8)\n" \
  "v_mfma_f32_16x16x32_bf16 v[64:67], v[48:51], a[40:43], v[64:67]\n" \
  "ds_read_b128 v[40:43], v192 offset:768\n" \
  "s_waitcnt lgkmcnt(8)\n" \
  "v_mfma_f32_16x16x32_bf16 v[72:75], v[52:55], a[40:43], v[72:75]\n" \
  "ds_read_b128 v[44:47], v192 offset:19200\n" \
  "v_mfma_f32_16x16x32_bf16 v[68:71], v[48:51], a[112:115], v[68:71]\n" \
  "s_add_u32 m0, s52, 4096\n" \
  "ds_read_b64_tr_b16 v[110:111], v161 offset:256\n" \
  "v_mfma_f32_16x16x32_bf16 v[76:79], v[52:55], a[112:115], v[76:79]\n" \
  "buffer_load_dwordx4 v200, s[44:47], s51 offen lds\n" \
  "s_waitcnt lgkmcnt(8)\n" \
  "v_mfma_f32_16x16x32_bf16 v[64:67], v[56:59], a[44:47], v[64:67]\n" \
  "ds_read_b128 v[48:51], v191 offset:896\n" \
  "s_waitcnt lgkmcnt(8)\n" \
  "v_mfma_f32_16x16x32_bf16 v[72:75], v[60:63], a[44:47], v[72:75]\n" \
  "ds_read_b128 v[52:55], v191 offset:19328\n" \
  "v_mfma_f32_16x16x32_bf16 v[68:71], v[56:59], a[116:119], v[68:71]\n" \
  "ds_read_b64_tr_b16 v[112:113], v194 offset:2048\n" \
  "v_mfma_f32_16x16x32_bf16 v[76:79], v[60:63], a[116:119], v[76:79]\n" \
  "ds_read_b64_tr_b16 v[114:115], v194 offset:2304\n" \
  "s_waitcnt lgkmcnt(9)\n" \
  "v_mfma_f32_16x16x32_bf16 v[64:67], v[32:35], a[48:51], v[64:67]\n" \
  "ds_read_b128 v[56:59], v192 offset:896\n" \
  "s_waitcnt lgkmcnt(9)\n" \
  "v_mfma_f32_16x16x32_bf16 v[72:75], v[36:39], a[48:51], v[72:75]\n" \
  "ds_read_b128 v[60:63], v192 offset:19328\n" \
  "v_mfma_f32_16x16x32_bf16 v[68:71], v[32:35], a[120:123], v[68:71]\n" \
  "s_add_u32 m0, s52, 5120\n" \
  "ds_read_b64_tr_b16 v[116:117], v161 offset:2048\n" \
  "v_mfma_f32_16x16x32_bf16 v[76:79], v[36:39], a[120:123], v[76:79]\n" \
  "buffer_load_dwordx4 v201, s[44:47], s51 offen lds\n" \
  "s_waitcnt lgkmcnt(9)\n" \
  "v_mfma_f32_16x16x32_bf16 v[64:67], v[40:43], a[52:55], v[64:67]\n" \
  "ds_read_b128 v[32:35], v191 offset:1024\n" \
  "s_waitcnt lgkmcnt(9)\n" \
  "v_mfma_f32_16x16x32_bf16 v[72:75], v[44:47], a[52:55], v[72:75]\n" \
  "ds_read_b128 v[36:39], v191 offset:19456\n" \
  "v_mfma_f32_16x16x32_bf16 v[68:71], v[40:43], a[124:127], v[68:71]\n" \
  "ds_read_b64_tr_b16 v[118:119], v161 offset:2304\n" \
  "v_mfma_f32_16x16x32_bf16 v[76:79], v[44:47], a[124:127], v[76:79]\n" \
  "ds_read_b64_tr_b16 v[120:121], v194 offset:4096\n" \
  "s_waitcnt lgkmcnt(10)\n" \
  "v_mfma_f32_16x16x32_bf16 v[64:67], v[48:51], a[56:59], v[64:67]\n" \
  "ds_read_b128 v[40:43], v192 offset:1024\n" \
  "s_waitcnt lgkmcnt(10)\n" \
  "v_mfma_f32_16x16x32_bf16 v[72:75], v[52:55], a[56:59], v[72:75]\n" \
  "ds_read_b128 v[44:47], v192 offset:19456\n" \
  "v_mfma_f32_16x16x32_bf16 v[68:71], v[48:51], a[128:131], v[68:71]\n" \
  "s_add_u32 m0, s52, 6144\n" \
  "ds_read_b64_tr_b16 v[122:123], v194 offset:4352\n" \
  "v_mfma_f32_16x16x32_bf16 v[76:79], v[52:55], a[128:131], v[76:79]\n" \
  "buffer_load_dwordx4 v202, s[44:47], s51 offen lds\n" \
  "s_waitcnt lgkmcnt(9)\n" \
  "v_mfma_f32_16x16x32_bf16 v[64:67], v[56:59], a[60:63], v[64:67]\n" \
  "ds_read_b64_tr_b16 v[124:125], v161 offset:4096\n" \
  "s_waitcnt lgkmcnt(9)\n" \
  "v_mfma_f32_16x16x32_bf16 v[72:75], v[60:63], a[60:63], v[72:75]\n" \
  "ds_read_b64_tr_b16 v[126:127], v161 offset:4352\n" \
  "v_mfma_f32_16x16x32_bf16 v[68:71], v[56:59], a[132:135], v[68:71]\n" \
  "s_add_u32 m0, s52, 7168\n" \
  "ds_read_b64_tr_b16 v[128:129], v194 offset:6144\n" \
  "v_mfma_f32_16x16x32_bf16 v[76:79], v[60:63], a[132:135], v[76:79]\n" \
  "buffer_load_dwordx4 v203, s[44:47], s51 offen lds\n" \
  "s_waitcnt lgkmcnt(9)\n" \
  "v_mfma_f32_16x16x32_bf16 v[64:67], v[32:35], a[64:67], v[64:67]\n" \
  "ds_read_b64_tr_b16 v[130:131], v194 offset:6400\n" \
  "s_waitcnt lgkmcnt(9)\n" \
  "v_mfma_f32_16x16x32_bf16 v[72:75], v[36:39], a[64:67], v[72:75]\n" \
  "ds_read_b64_tr_b16 v[132:133], v161 offset:6144\n" \
  "v_mfma_f32_16x16x32_bf16 v[68:71], v[32:35], a[136:139], v[68:71]\n" \
  "ds_read_b64_tr_b16 v[134:135], v161 offset:6400\n" \
  "v_mfma_f32_16x16x32_bf16 v[76:79], v[36:39], a[136:139], v[76:79]\n" \
  "s_waitcnt lgkmcnt(8)\n" \
  "v_mfma_f32_16x16x32_bf16 v[64:67], v[40:43], a[68:71], v[64:67]\n" \
  "s_waitcnt lgkmcnt(7)\n" \
  "v_mfma_f32_16x16x32_bf16 v[72:75], v[44:47], a[68:71], v[72:75]\n" \
  "s_waitcnt vmcnt(8)\n" \
  "buffer_store_dwordx2 v[168:169], v167, s[40:43], 0 offen sc1\n" \
  "v_mfma_f32_16x16x32_bf16 v[68:71], v[40:43], a[140:143], v[68:71]\n" \
  "s_add_u32 m0, s52, 8192\n" \
  "v_mfma_f32_16x16x32_bf16 v[76:79], v[44:47], a[140:143], v[76:79]\n" \
  "buffer_load_dwordx4 v204, s[44:47], s51 offen lds\n" \
  "s_cmp_le_u32 s48, 2\n" \
  "s_cbranch_scc1 .LU_first_%=\n" \
  ".LU_phb_%=:\n" \
  "v_mfma_f32_16x16x32_bf16 v[88:91], v[104:107], a[144:147], 0\n" \
  "v_mfma_f32_16x16x32_bf16 v[92:95], v[108:111], a[144:147], 0\n" \
  "s_lshr_b32 s62, s49, 8\n" \
  "s_and_b32 s62, s62, 8192\n" \
  "s_xor_b32 s62, s62, s49\n" \
  "v_mfma_f32_16x16x32_bf16 v[88:91], v[112:115], a[148:151], v[88:91]\n" \
  "v_cvt_pk_bf16_f32 v80, v64, v65\n" \
  "v_cvt_pk_bf16_f32 v81, v66, v67\n" \
  "v_mfma_f32_16x16x32_bf16 v[92:95], v[116:119], a[148:151], v[92:95]\n" \
  "s_waitcnt lgkmcnt(6)\n" \
  "v_cvt_pk_bf16_f32 v82, v72, v73\n" \
  "v_cvt_pk_bf16_f32 v83, v74, v75\n" \
  "v_mfma_f32_16x16x32_bf16 v[88:91], v[120:123], a[152:155], v[88:91]\n" \
  "buffer_store_dwordx4 v[80:83], v190, s[40:43], s62 offen sc1\n" \
  "s_waitcnt lgkmcnt(4)\n" \
  "v_mfma_f32_16x16x32_bf16 v[92:95], v[124:127], a[152:155], v[92:95]\n" \
  "ds_write_b128 v193, v[80:83]\n" \
  "s_waitcnt lgkmcnt(3)\n" \
  "v_mfma_f32_16x16x32_bf16 v[88:91], v[128:131], a[156:159], v[88:91]\n" \
  "s_waitcnt lgkmcnt(1)\n" \
  "v_cvt_pk_bf16_f32 v84, v76, v77\n" \
  "v_cvt_pk_bf16_f32 v85, v78, v79\n" \
  "v_mfma_f32_16x16x32_bf16 v[92:95], v[132:135], a[156:159], v[92:95]\n" \
  "v_cvt_pk_bf16_f32 v86, v68, v69\n" \
  "v_cvt_pk_bf16_f32 v87, v70, v71\n" \
  "v_mfma_f32_16x16x32_bf16 v[96:99], v[104:107], a[160:163], 0\n" \
  "buffer_store_dwordx4 v[84:87], v190, s[40:43], s62 offen offset:1024 sc1\n" \
  "v_mfma_f32_16x16x32_bf16 v[100:103], v[108:111], a[160:163], 0\n" \
  "ds_write_b128 v193, v[84:87] offset:1024\n" \
  "v_mfma_f32_16x16x32_bf16 v[96:99], v[112:115], a[164:167], v[96:99]\n" \
  "v_cvt_pk_bf16_f32 v136, v88, v89\n" \
  "v_cvt_pk_bf16_f32 v137, v90, v91\n" \
  "s_add_u32 s58, s48, 1\n" \
  "v_mfma_f32_16x16x32_bf16 v[100:103], v[116:119], a[164:167], v[100:103]\n" \
  "v_cvt_pk_bf16_f32 v138, v92, v93\n" \
  "v_cvt_pk_bf16_f32 v139, v94, v95\n" \
  "s_xor_b32 s63, s50, 0\n" \
  "v_mfma_f32_16x16x32_bf16 v[96:99], v[120:123], a[168:171], v[96:99]\n" \
  "s_add_u32 s63, s63, 0\n" \
  "s_and_b32 s58, s58, 3\n" \
  "s_cselect_b32 s58, 0, 147456\n" \
  "v_mfma_f32_16x16x32_bf16 v[100:103], v[124:127], a[168:171], v[100:103]\n" \
  "buffer_store_dwordx4 v[136:139], v190, s[40:43], s63 offen sc1\n" \
  "v_mfma_f32_16x16x32_bf16 v[96:99], v[128:131], a[172:175], v[96:99]\n" \
  "s_sub_u32 s58, 36864, s58\n" \
  "v_add_u32 v163, s58, v191\n" \
  "v_add_u32 v162, s58, v192\n" \
  "v_mfma_f32_16x16x32_bf16 v[100:103], v[132:135], a[172:175], v[100:103]\n" \
  "s_add_u32 s51, s51, 36864\n" \
  "s_min_u32 s51, s51, 2322432\n" \
  "s_add_u32 s52, s52, 36864\n" \
  "v_mfma_f32_16x16x32_bf16 v[88:91], v[104:107], a[176:179], 0\n" \
  "s_cmp_ge_u32 s52, 163840\n" \
  "s_cselect_b32 s60, 147456, 0\n" \
  "s_sub_u32 s52, s52, s60\n" \
  "v_mfma_f32_16x16x32_bf16 v[92:95], v[108:111], a[176:179], 0\n" \
  "v_mfma_f32_16x16x32_bf16 v[88:91], v[112:115], a[180:183], v[88:91]\n" \
  "v_cvt_pk_bf16_f32 v136, v96, v97\n" \
  "v_cvt_pk_bf16_f32 v137, v98, v99\n" \
  "v_mfma_f32_16x16x32_bf16 v[92:95], v[116:119], a[180:183], v[92:95]\n" \
  "v_cvt_pk_bf16_f32 v138, v100, v101\n" \
  "v_cvt_pk_bf16_f32 v139, v102, v103\n" \
  "s_xor_b32 s63, s50, 1024\n" \
  "v_mfma_f32_16x16x32_bf16 v[88:91], v[120:123], a[184:187], v[88:91]\n" \
  "s_add_u32 s63, s63, 1048576\n" \
  "v_mfma_f32_16x16x32_bf16 v[92:95], v[124:127], a[184:187], v[92:95]\n" \
  "buffer_store_dwordx4 v[136:139], v190, s[40:43], s63 offen sc1\n" \
  "v_mfma_f32_16x16x32_bf16 v[88:91], v[128:131], a[188:191], v[88:91]\n" \
  "s_nop 2\n" \
  "v_mfma_f32_16x16x32_bf16 v[92:95], v[132:135], a[188:191], v[92:95]\n" \
  "s_nop 0\n" \
  "s_nop 0\n" \
  "v_mfma_f32_16x16x32_bf16 v[96:99], v[104:107], a[192:195], 0\n" \
  "v_mfma_f32_16x16x32_bf16 v[100:103], v[108:111], a[192:195], 0\n" \
  "v_mfma_f32_16x16x32_bf16 v[96:99], v[112:115], a[196:199], v[96:99]\n" \
  "v_cvt_pk_bf16_f32 v136, v88, v89\n" \
  "v_cvt_pk_bf16_f32 v137, v90, v91\n" \
  "v_mfma_f32_16x16x32_bf16 v[100:103], v[116:119], a[196:199], v[100:103]\n" \
  "v_cvt_pk_bf16_f32 v138, v92, v93\n" \
  "v_cvt_pk_bf16_f32 v139, v94, v95\n" \
  "s_xor_b32 s63, s50, 278528\n" \
  "v_mfma_f32_16x16x32_bf16 v[96:99], v[120:123], a[200:203], v[96:99]\n" \
  "s_add_u32 s63, s63, 8388608\n" \
  "v_mfma_f32_16x16x32_bf16 v[100:103], v[124:127], a[200:203], v[100:103]\n" \
  "buffer_store_dwordx4 v[136:139], v190, s[40:43], s63 offen sc1\n" \
  "v_mfma_f32_16x16x32_bf16 v[96:99], v[128:131], a[204:207], v[96:99]\n" \
  "v_readfirstlane_b32 s58, v164\n" \
  "v_readfirstlane_b32 s61, v165\n" \
  "v_mfma_f32_16x16x32_bf16 v[100:103], v[132:135], a[204:207], v[100:103]\n" \
  "s_cmp_eq_u32 s61, s55\n" \
  "s_cselect_b32 s58, s58, 0\n" \
  "s_add_u32 s58, s58, 14\n" \
  "v_mfma_f32_16x16x32_bf16 v[88:91], v[104:107], a[208:211], 0\n" \
  "s_cmp_lt_u32 s58, s73\n" \
  "s_cbranch_scc1 .LU_fc_%=\n" \
  ".LU_fc_b_%=:\n" \
  "v_mfma_f32_16x16x32_bf16 v[92:95], v[108:111], a[208:211], 0\n" \
  "buffer_load_dwordx2 v[164:165], off, s[40:43], %43 sc1\n" \
  "v_mfma_f32_16x16x32_bf16 v[88:91], v[112:115], a[212:215], v[88:91]\n" \
  "v_cvt_pk_bf16_f32 v136, v96, v97\n" \
  "v_cvt_pk_bf16_f32 v137, v98, v99\n" \
  "v_mfma_f32_16x16x32_bf16 v[92:95], v[116:119], a[212:215], v[92:95]\n" \
  "v_cvt_pk_bf16_f32 v138, v100, v101\n" \
  "v_cvt_pk_bf16_f32 v139, v102, v103\n" \
  "s_xor_b32 s63, s50, 279552\n" \
  "v_mfma_f32_16x16x32_bf16 v[88:91], v[120:123], a[216:219], v[88:91]\n" \
  "s_add_u32 s63, s63, 9437184\n" \
  "v_mfma_f32_16x16x32_bf16 v[92:95], v[124:127], a[216:219], v[92:95]\n" \
  "buffer_store_dwordx4 v[136:139], v190, s[40:43], s63 offen sc1\n" \
  "v_mfma_f32_16x16x32_bf16 v[88:91], v[128:131], a[220:223], v[88:91]\n" \
  "s_nop 2\n" \
  "v_mfma_f32_16x16x32_bf16 v[92:95], v[132:135], a[220:223], v[92:95]\n" \
  "s_nop 0\n" \
  "s_nop 0\n" \
  "v_mfma_f32_16x16x32_bf16 v[96:99], v[104:107], a[224:227], 0\n" \
  "v_mfma_f32_16x16x32_bf16 v[100:103], v[108:111], a[224:227], 0\n" \
  "v_mfma_f32_16x16x32_bf16 v[96:99], v[112:115], a[228:231], v[96:99]\n" \
  "v_cvt_pk_bf16_f32 v136, v88, v89\n" \
  "v_cvt_pk_bf16_f32 v137, v90, v91\n" \
  "v_mfma_f32_16x16x32_bf16 v[100:103], v[116:119], a[228:231], v[100:103]\n" \
  "v_cvt_pk_bf16_f32 v138, v92, v93\n" \
  "v_cvt_pk_bf16_f32 v139, v94, v95\n" \
  "s_xor_b32 s63, s50, 557056\n" \
  "v_mfma_f32_16x16x32_bf16 v[96:99], v[120:123], a[232:235], v[96:99]\n" \
  "s_add_u32 s63, s63, 16777216\n" \
  "v_mfma_f32_16x16x32_bf16 v[100:103], v[124:127], a[232:235], v[100:103]\n" \
  "buffer_store_dwordx4 v[136:139], v190, s[40:43], s63 offen sc1\n" \
  "v_mfma_f32_16x16x32_bf16 v[96:99], v[128:131], a[236:239], v[96:99]\n" \
  "s_nop 2\n" \
  "v_mfma_f32_16x16x32_bf16 v[100:103], v[132:135], a[236:239], v[100:103]\n" \
  "s_nop 0\n" \
  "s_nop 0\n" \
  "v_mfma_f32_16x16x32_bf16 v[88:91], v[104:107], a[240:243], 0\n" \
  "v_mfma_f32_16x16x32_bf16 v[92:95], v[108:111], a[240:243], 0\n" \
  "v_mfma_f32_16x16x32_bf16 v[88:91], v[112:115], a[244:247], v[88:91]\n" \
  "v_cvt_pk_bf16_f32 v136, v96, v97\n" \
  "v_cvt_pk_bf16_f32 v137, v98, v99\n" \
  "v_mfma_f32_16x16x32_bf16 v[92:95], v[116:119], a[244:247], v[92:95]\n" \
  "v_cvt_pk_bf16_f32 v138, v100, v101\n" \
  "v_cvt_pk_bf16_f32 v139, v102, v103\n" \
  "s_xor_b32 s63, s50, 558080\n" \
  "v_mfma_f32_16x16x32_bf16 v[88:91], v[120:123], a[248:251], v[88:91]\n" \
  "s_add_u32 s63, s63, 17825792\n" \
  "v_mfma_f32_16x16x32_bf16 v[92:95], v[124:127], a[248:251], v[92:95]\n" \
  "buffer_store_dwordx4 v[136:139], v190, s[40:43], s63 offen sc1\n" \
  "v_mfma_f32_16x16x32_bf16 v[88:91], v[128:131], a[252:255], v[88:91]\n" \
  "v_mfma_f32_16x16x32_bf16 v[92:95], v[132:135], a[252:255], v[92:95]\n" \
  "v_mfma_f32_16x16x32_bf16 v[96:99], v[104:107], v[0:3], 0\n" \
  "ds_read_b128 v[32:35], v163\n" \
  "ds_read_b128 v[36:39], v163 offset:18432\n" \
  "v_mfma_f32_16x16x32_bf16 v[100:103], v[108:111], v[0:3], 0\n" \
  "ds_read_b128 v[40:43], v162\n" \
  "ds_read_b128 v[44:47], v162 offset:18432\n" \
  "v_mfma_f32_16x16x32_bf16 v[96:99], v[112:115], v[4:7], v[96:99]\n" \
  "v_cvt_pk_bf16_f32 v136, v88, v89\n" \
  "v_cvt_pk_bf16_f32 v137, v90, v91\n" \
  "v_mfma_f32_16x16x32_bf16 v[100:103], v[116:119], v[4:7], v[100:103]\n" \
  "v_cvt_pk_bf16_f32 v138, v92, v93\n" \
  "v_cvt_pk_bf16_f32 v139, v94, v95\n" \
  "s_xor_b32 s63, s50, 835584\n" \
  "v_mfma_f32_16x16x32_bf16 v[96:99], v[120:123], v[8:11], v[96:99]\n" \
  "s_add_u32 s63, s63, 25165824\n" \
  "ds_read_b128 v[48:51], v163 offset:128\n" \
  "v_mfma_f32_16x16x32_bf16 v[100:103], v[124:127], v[8:11], v[100:103]\n" \
  "buffer_store_dwordx4 v[136:139], v190, s[40:43], s63 offen sc1\n" \
  "v_mfma_f32_16x16x32_bf16 v[96:99], v[128:131], v[12:15], v[96:99]\n" \
  "ds_read_b128 v[52:55], v163 offset:18560\n" \
  "v_mfma_f32_16x16x32_bf16 v[100:103], v[132:135], v[12:15], v[100:103]\n" \
  "v_mov_b32 v191, v163\n" \
  "v_mov_b32 v192, v162\n" \
  "v_mfma_f32_16x16x32_bf16 v[88:91], v[104:107], v[16:19], 0\n" \
  "v_xor_b32 v193, 8192, v193\n" \
  "v_xor_b32 v194, 8192, v194\n" \
  "s_add_u32 s49, s49, s71\n" \
  "v_mfma_f32_16x16x32_bf16 v[92:95], v[108:111], v[16:19], 0\n" \
  "v_mfma_f32_16x16x32_bf16 v[88:91], v[112:115], v[20:23], v[88:91]\n" \
  "v_cvt_pk_bf16_f32 v104, v96, v97\n" \
  "v_cvt_pk_bf16_f32 v105, v98, v99\n" \
  "v_mfma_f32_16x16x32_bf16 v[92:95], v[116:119], v[20:23], v[92:95]\n" \
  "v_cvt_pk_bf16_f32 v106, v100, v101\n" \
  "v_cvt_pk_bf16_f32 v107, v102, v103\n" \
  "s_xor_b32 s63, s50, 836608\n" \
  "v_mfma_f32_16x16x32_bf16 v[88:91], v[120:123], v[24:27], v[88:91]\n" \
  "s_add_u32 s63, s63, 26214400\n" \
  "v_mfma_f32_16x16x32_bf16 v[92:95], v[124:127], v[24:27], v[92:95]\n" \
  "buffer_store_dwordx4 v[104:107], v190, s[40:43], s63 offen sc1\n" \
  "v_mfma_f32_16x16x32_bf16 v[88:91], v[128:131], v[28:31], v[88:91]\n" \
  "v_mfma_f32_16x16x32_bf16 v[92:95], v[132:135], v[28:31], v[92:95]\n" \
  ".LU_first_b_%=:\n" \
  "s_add_u32 s48, s48, 1\n" \
  "s_add_u32 s73, s73, s70\n" \
  "s_cmp_eq_u32 s73, s74\n" \
  "s_cselect_b32 s49, 2130706432, s49\n" \
  "s_cmp_le_u32 s73, s74\n" \
  "s_waitcnt lgkmcnt(0)\n" \
  "s_cbranch_scc1 .LU_loop_%=\n" \
  "s_nop 7\n" \
  "v_cvt_pk_bf16_f32 v104, v88, v89\n" \
  "v_cvt_pk_bf16_f32 v105, v90, v91\n" \
  "v_cvt_pk_bf16_f32 v106, v92, v93\n" \
  "v_cvt_pk_bf16_f32 v107, v94, v95\n" \
  "s_mov_b32 s63, s57\n" \
  "buffer_store_dwordx4 v[104:107], v190, s[40:43], s63 offen sc1\n" \
  "s_waitcnt vmcnt(0)\n" \
  "s_barrier\n" \
  "s_mov_b32 s48, s74\n" \
  "s_mov_b32 s69, 0\n" \
  ".LU_ie_%=:\n" \
  "s_cmp_ge_u32 s96, 0xc0000000\n" \
  "s_cbranch_scc1 .LU_ie_x_%=\n" \
  "buffer_load_dwordx2 v[184:185], off, s[40:43], %41 sc1\n" \
  "s_waitcnt vmcnt(0)\n" \
  "v_readfirstlane_b32 s96, v184\n" \
  "s_add_u32 s69, s69, 1\n" \
  "s_cmp_lt_u32 s69, 2097152\n" \
  "s_cbranch_scc1 .LU_ie_%=\n" \
  ".LU_ie_x_%=:\n" \
  "v_mov_b32 v168, s48\n" \
  "buffer_store_dwordx2 v[168:169], v167, s[40:43], 0 offen sc1\n" \
  "s_branch .LU_end_%=\n" \
  ".LU_e2_%=:\n" \
  "s_waitcnt vmcnt(4)\n" \
  "v_add_u32 v168, s73, v166\n" \
  "s_mov_b64 exec, 2\n" \
  "buffer_store_dwordx2 v[168:169], v167, s[40:43], 0 offen sc1\n" \
  "s_mov_b64 exec, -1\n" \
  "v_readfirstlane_b32 s96, v184\n" \
  "s_cmp_lg_u32 s48, 7\n" \
  "s_cbranch_scc1 .LU_in_x_%=\n" \
  "s_mov_b32 s69, 0\n" \
  ".LU_in_%=:\n" \
  "s_cmp_ge_u32 s96, 0xc0000000\n" \
  "s_cbranch_scc1 .LU_in_x_%=\n" \
  "buffer_load_dwordx2 v[184:185], off, s[40:43], %41 sc1\n" \
  "s_waitcnt vmcnt(0)\n" \
  "v_readfirstlane_b32 s96, v184\n" \
  "s_add_u32 s69, s69, 1\n" \
  "s_cmp_lt_u32 s69, 2097152\n" \
  "s_cbranch_scc1 .LU_in_%=\n" \
  ".LU_in_x_%=:\n" \
  "buffer_load_dwordx2 v[184:185], off, s[40:43], %41 sc1\n" \
  "s_branch .LU_e2_b_%=\n" \
  ".LU_fc_%=:\n" \
  "s_mov_b32 s69, 0\n" \
  ".LU_fc1_%=:\n" \
  "s_sleep 8\n" \
  "buffer_load_dwordx2 v[164:165], off, s[40:43], %43 sc1\n" \
  "s_waitcnt vmcnt(0)\n" \
  "v_readfirstlane_b32 s58, v164\n" \
  "v_readfirstlane_b32 s61, v165\n" \
  "s_cmp_eq_u32 s61, s55\n" \
  "s_cselect_b32 s58, s58, 0\n" \
  "s_add_u32 s58, s58, 14\n" \
  "s_cmp_lt_u32 s58, s73\n" \
  "s_cbranch_scc0 .LU_fc_b_%=\n" \
  "s_add_u32 s69, s69, 1\n" \
  "s_cmp_lt_u32 s69, 524288\n" \
  "s_cbranch_scc1 .LU_fc1_%=\n" \
  "v_mov_b32 v164, 1\n" \
  "v_mov_b32 v165, s55\n" \
  "buffer_store_dwordx2 v[164:165], off, s[40:43], %42 offset:128 sc1\n" \
  "s_waitcnt vmcnt(0)\n" \
  "v_mov_b32 v164, 100\n" \
  "s_branch .LU_fc_b_%=\n" \
  ".LU_first_%=:\n" \
  "s_waitcnt lgkmcnt(0)\n" \
  "s_cmp_eq_u32 s48, 1\n" \
  "s_cselect_b32 s70, s75, 1\n" \
  "s_lshl_b32 s71, s70, 19\n" \
  "s_nop 4\n" \
  "s_lshr_b32 s62, s49, 8\n" \
  "s_and_b32 s62, s62, 8192\n" \
  "s_xor_b32 s62, s62, s49\n" \
  "v_cvt_pk_bf16_f32 v80, v64, v65\n" \
  "v_cvt_pk_bf16_f32 v81, v66, v67\n" \
  "v_cvt_pk_bf16_f32 v82, v72, v73\n" \
  "v_cvt_pk_bf16_f32 v83, v74, v75\n" \
  "buffer_store_dwordx4 v[80:83], v190, s[40:43], s62 offen sc1\n" \
  "ds_write_b128 v193, v[80:83]\n" \
  "v_cvt_pk_bf16_f32 v84, v76, v77\n" \
  "v_cvt_pk_bf16_f32 v85, v78, v79\n" \
  "v_cvt_pk_bf16_f32 v86, v68, v69\n" \
  "v_cvt_pk_bf16_f32 v87, v70, v71\n" \
  "buffer_store_dwordx4 v[84:87], v190, s[40:43], s62 offen offset:1024 sc1\n" \
  "ds_write_b128 v193, v[84:87] offset:1024\n" \
  "s_cmp_eq_u32 s48, 2\n" \
  "s_cbranch_scc1 .LU_skip_%=\n" \
  "v_xor_b32 v161, 8, v194\n" \
  "s_waitcnt lgkmcnt(0)\n" \
  "s_barrier\n" \
  "v_xor_b32 v194, 8192, v194\n" \
  "v_xor_b32 v161, 8192, v161\n" \
  "ds_read_b64_tr_b16 v[104:105], v194\n" \
  "ds_read_b64_tr_b16 v[106:107], v194 offset:256\n" \
  "ds_read_b64_tr_b16 v[108:109], v161\n" \
  "ds_read_b64_tr_b16 v[110:111], v161 offset:256\n" \
  "ds_read_b64_tr_b16 v[112:113], v194 offset:2048\n" \
  "ds_read_b64_tr_b16 v[114:115], v194 offset:2304\n" \
  "ds_read_b64_tr_b16 v[116:117], v161 offset:2048\n" \
  "ds_read_b64_tr_b16 v[118:119], v161 offset:2304\n" \
  "ds_read_b64_tr_b16 v[120:121], v194 offset:4096\n" \
  "ds_read_b64_tr_b16 v[122:123], v194 offset:4352\n" \
  "ds_read_b64_tr_b16 v[124:125], v161 offset:4096\n" \
  "ds_read_b64_tr_b16 v[126:127], v161 offset:4352\n" \
  "ds_read_b64_tr_b16 v[128:129], v194 offset:6144\n" \
  "ds_read_b64_tr_b16 v[130:131], v194 offset:6400\n" \
  "ds_read_b64_tr_b16 v[132:133], v161 offset:6144\n" \
  "ds_read_b64_tr_b16 v[134:135], v161 offset:6400\n" \
  "v_xor_b32 v194, 8192, v194\n" \
  "s_mov_b32 s58, s73\n" \
  "s_and_b32 s58, s58, 15\n" \
  "s_lshl_b32 s58, s58, 16\n" \
  "s_xor_b32 s50, s58, s66\n" \
  "s_xor_b32 s57, s58, s59\n" \
  "s_waitcnt lgkmcnt(0)\n" \
  "s_branch .LU_phb_%=\n" \
  ".LU_skip_%=:\n" \
  "s_mov_b64 exec, 2\n" \
  "v_mov_b32 v166, -1\n" \
  "s_mov_b64 exec, -1\n" \
  "s_add_u32 s58, s48, 1\n" \
  "s_and_b32 s58, s58, 3\n" \
  "s_cselect_b32 s58, 0, 147456\n" \
  "s_sub_u32 s58, 36864, s58\n" \
  "v_add_u32 v163, s58, v191\n" \
  "v_add_u32 v162, s58, v192\n" \
  "s_add_u32 s51, s51, 36864\n" \
  "s_min_u32 s51, s51, 2322432\n" \
  "s_add_u32 s52, s52, 36864\n" \
  "s_cmp_ge_u32 s52, 163840\n" \
  "s_cselect_b32 s60, 147456, 0\n" \
  "s_sub_u32 s52, s52, s60\n" \
  "ds_read_b128 v[32:35], v163\n" \
  "ds_read_b128 v[36:39], v163 offset:18432\n" \
  "ds_read_b128 v[40:43], v162\n" \
  "ds_read_b128 v[44:47], v162 offset:18432\n" \
  "ds_read_b128 v[48:51], v163 offset:128\n" \
  "ds_read_b128 v[52:55], v163 offset:18560\n" \
  "v_mov_b32 v191, v163\n" \
  "v_mov_b32 v192, v162\n" \
  "v_xor_b32 v193, 8192, v193\n" \
  "v_xor_b32 v194, 8192, v194\n" \
  "s_add_u32 s49, s49, s71\n" \
  "s_mov_b32 s57, 2130706432\n" \
  "s_mov_b32 s69, 0\n" \
  ".LU_sk1_%=:\n" \
  "buffer_load_dwordx2 v[164:165], off, s[40:43], %43 sc1\n" \
  "s_waitcnt vmcnt(0)\n" \
  "v_readfirstlane_b32 s58, v164\n" \
  "v_readfirstlane_b32 s61, v165\n" \
  "s_cmp_eq_u32 s61, s55\n" \
  "s_cselect_b32 s58, s58, 0\n" \
  "s_add_u32 s58, s58, 8\n" \
  "s_cmp_lt_u32 s58, s73\n" \
  "s_cbranch_scc0 .LU_first_b_%=\n" \
  "v_mov_b32 v168, s73\n" \
  "s_mov_b64 exec, 2\n" \
  "buffer_store_dwordx2 v[168:169], v167, s[40:43], 0 offen sc1\n" \
  "s_mov_b64 exec, -1\n" \
  "s_sleep 16\n" \
  "s_add_u32 s69, s69, 1\n" \
  "s_cmp_lt_u32 s69, 524288\n" \
  "s_cbranch_scc1 .LU_sk1_%=\n" \
  "v_mov_b32 v164, 1\n" \
  "v_mov_b32 v165, s55\n" \
  "buffer_store_dwordx2 v[164:165], off, s[40:43], %42 offset:128 sc1\n" \
  "s_waitcnt vmcnt(0)\n" \
  "s_branch .LU_first_b_%=\n" \
  ".LU_end_%=:\n" \
  "s_mov_b32 %0, s55\n"
#define P_ASM_VOPS "=&s"(tag), "+{v190}"(pi.l16), "+{v191}"(pi.xr0), "+{v192}"(pi.xr1), "+{v193}"(pi.ew), "+{v194}"(pi.er), "+{v195}"(pi.uo), "+{v196}"(pi.xs0), "+{v197}"(pi.xs1), "+{v198}"(pi.xs2), "+{v199}"(pi.xs3), "+{v200}"(pi.xs4), "+{v201}"(pi.xs5), "+{v202}"(pi.xs6), "+{v203}"(pi.xs7), "+{v204}"(pi.xs8), "+{v170}"(pi.vq0), "+{v171}"(pi.vq1), "+{v172}"(pi.vq2), "+{v173}"(pi.vq3), "+{v174}"(pi.vo0), "+{v175}"(pi.vo1), "+{v176}"(pi.vo2), "+{v177}"(pi.vo3), "+{v178}"(pi.v50), "+{v179}"(pi.v51), "+{v180}"(pi.v52), "+{v181}"(pi.v53), "+{v182}"(pi.tro0), "+{v183}"(pi.tro1)
#define P_ASM_SOPS "s"(pi.ws), "s"(pi.Wa), "s"(pi.Wb), "s"(pi.x), "s"(pi.w), "s"(pi.o_a), "s"(pi.o_b), "s"(pi.o_p), "s"(pi.o_q), "s"(pi.f_a), "s"(pi.f_b), "s"(pi.f_go), "s"(pi.o_ctl), "s"(pi.f_c), "s"(pi.u8), "s"(pi.rng)
#define P_ASM_CLOB "v0", "v1", "v2", "v3", "v4", "v5", "v6", "v7", "v8", "v9", "v10", "v11", "v12", "v13", "v14", "v15", "v16", "v17", "v18", "v19", "v20", "v21", "v22", "v23", "v24", "v25", "v26", "v27", "v28", "v29", "v30", "v31", "v32", "v33", "v34", "v35", "v36", "v37", "v38", "v39", "v40", "v41", "v42", "v43", "v44", "v45", "v46", "v47", "v48", "v49", "v50", "v51", "v52", "v53", "v54", "v55", "v56", "v57", "v58", "v59", "v60", "v61", "v62", "v63", "v64", "v65", "v66", "v67", "v68", "v69", "v70", "v71", "v72", "v73", "v74", "v75", "v76", "v77", "v78", "v79", "v80", "v81", "v82", "v83", "v84", "v85", "v86", "v87", "v88", "v89", "v90", "v91", "v92", "v93", "v94", "v95", "v96", "v97", "v98", "v99", "v100", "v101", "v102", "v103", "v104", "v105", "v106", "v107", "v108", "v109", "v110", "v111", "v112", "v113", "v114", "v115", "v116", "v117", "v118", "v119", "v120", "v121", "v122", "v123", "v124", "v125", "v126", "v127", "v128", "v129", "v130", "v131", "v132", "v133", "v134", "v135", "v136", "v137", "v138", "v139", "v140", "v141", "v142", "v143", "v144", "v145", "v146", "v147", "v148", "v149", "v150", "v151", "v152", "v153", "v154", "v155", "v156", "v157", "v158", "v159", "v160", "v161", "v162", "v163", "v164", "v165", "v166", "v167", "v168", "v169", "v184", "v185", "v186", "v187", "v188", "v189", "a0", "a1", "a2", "a3", "a4", "a5", "a6", "a7", "a8", "a9", "a10", "a11", "a12", "a13", "a14", "a15", "a16", "a17", "a18", "a19", "a20", "a21", "a22", "a23", "a24", "a25", "a26", "a27", "a28", "a29", "a30", "a31", "a32", "a33", "a34", "a35", "a36", "a37", "a38", "a39", "a40", "a41", "a42", "a43", "a44", "a45", "a46", "a47", "a48", "a49", "a50", "a51", "a52", "a53", "a54", "a55", "a56", "a57", "a58", "a59", "a60", "a61", "a62", "a63", "a64", "a65", "a66", "a67", "a68", "a69", "a70", "a71", "a72", "a73", "a74", "a75", "a76", "a77", "a78", "a79", "a80", "a81", "a82", "a83", "a84", "a85", "a86", "a87", "a88", "a89", "a90", "a91", "a92", "a93", "a94", "a95", "a96", "a97", "a98", "a99", "a100", "a101", "a102", "a103", "a104", "a105", "a106", "a107", "a108", "a109", "a110", "a111", "a112", "a113", "a114", "a115", "a116", "a117", "a118", "a119", "a120", "a121", "a122", "a123", "a124", "a125", "a126", "a127", "a128", "a129", "a130", "a131", "a132", "a133", "a134", "a135", "a136", "a137", "a138", "a139", "a140", "a141", "a142", "a143", "a144", "a145", "a146", "a147", "a148", "a149", "a150", "a151", "a152", "a153", "a154", "a155", "a156", "a157", "a158", "a159", "a160", "a161", "a162", "a163", "a164", "a165", "a166", "a167", "a168", "a169", "a170", "a171", "a172", "a173", "a174", "a175", "a176", "a177", "a178", "a179", "a180", "a181", "a182", "a183", "a184", "a185", "a186", "a187", "a188", "a189", "a190", "a191", "a192", "a193", "a194", "a195", "a196", "a197", "a198", "a199", "a200", "a201", "a202", "a203", "a204", "a205", "a206", "a207", "a208", "a209", "a210", "a211", "a212", "a213", "a214", "a215", "a216", "a217", "a218", "a219", "a220", "a221", "a222", "a223", "a224", "a225", "a226", "a227", "a228", "a229", "a230", "a231", "a232", "a233", "a234", "a235", "a236", "a237", "a238", "a239", "a240", "a241", "a242", "a243", "a244", "a245", "a246", "a247", "a248", "a249", "a250", "a251", "a252", "a253", "a254", "a255", "s40", "s41", "s42", "s43", "s44", "s45", "s46", "s47", "s48", "s49", "s50", "s51", "s52", "s53", "s54", "s55", "s56", "s57", "s58", "s59", "s60", "s61", "s62", "s63", "s64", "s65", "s66", "s67", "s68", "s69", "s70", "s71", "s72", "s73", "s74", "s75", "s76", "s77", "s78", "s79", "s80", "s81", "s82", "s83", "s84", "s85", "s86", "s87", "s88", "s89", "s90", "s91", "s92", "s93", "s94", "s95", "s96", "s97", "s98", "s99", "vcc", "scc", "memory"
#define O_ASM_TEXT \
  "s_mov_b64 s[40:41], %8\n" \
  "s_and_b32 s41, s41, 0xffff\n" \
  "s_mov_b32 s42, 255852544\n" \
  "s_mov_b32 s43, 0x00020000\n" \
  "s_mov_b64 s[44:45], %9\n" \
  "s_and_b32 s45, s45, 0xffff\n" \
  "s_mov_b32 s46, 4718592\n" \
  "s_mov_b32 s47, 0x00020000\n" \
  "s_mul_i32 s62, %10, 24576\n" \
  "s_add_u32 s60, %12, 0\n" \
  "s_add_u32 m0, s62, 0\n" \
  "s_nop 0\n" \
  "buffer_load_dwordx4 v194, s[44:47], s60 offen lds\n" \
  "s_add_u32 m0, s62, 1024\n" \
  "s_nop 0\n" \
  "buffer_load_dwordx4 v195, s[44:47], s60 offen lds\n" \
  "s_add_u32 m0, s62, 2048\n" \
  "s_nop 0\n" \
  "buffer_load_dwordx4 v196, s[44:47], s60 offen lds\n" \
  "s_add_u32 s60, %12, 18432\n" \
  "s_add_u32 m0, s62, 3072\n" \
  "s_nop 0\n" \
  "buffer_load_dwordx4 v194, s[44:47], s60 offen lds\n" \
  "s_add_u32 m0, s62, 4096\n" \
  "s_nop 0\n" \
  "buffer_load_dwordx4 v195, s[44:47], s60 offen lds\n" \
  "s_add_u32 m0, s62, 5120\n" \
  "s_nop 0\n" \
  "buffer_load_dwordx4 v196, s[44:47], s60 offen lds\n" \
  "s_add_u32 s60, %12, 36864\n" \
  "s_add_u32 m0, s62, 6144\n" \
  "s_nop 0\n" \
  "buffer_load_dwordx4 v194, s[44:47], s60 offen lds\n" \
  "s_add_u32 m0, s62, 7168\n" \
  "s_nop 0\n" \
  "buffer_load_dwordx4 v195, s[44:47], s60 offen lds\n" \
  "s_add_u32 m0, s62, 8192\n" \
  "s_nop 0\n" \
  "buffer_load_dwordx4 v196, s[44:47], s60 offen lds\n" \
  "s_add_u32 s60, %12, 55296\n" \
  "s_add_u32 m0, s62, 9216\n" \
  "s_nop 0\n" \
  "buffer_load_dwordx4 v194, s[44:47], s60 offen lds\n" \
  "s_add_u32 m0, s62, 10240\n" \
  "s_nop 0\n" \
  "buffer_load_dwordx4 v195, s[44:47], s60 offen lds\n" \
  "s_add_u32 m0, s62, 11264\n" \
  "s_nop 0\n" \
  "buffer_load_dwordx4 v196, s[44:47], s60 offen lds\n" \
  "s_add_u32 s60, %12, 73728\n" \
  "s_add_u32 m0, s62, 12288\n" \
  "s_nop 0\n" \
  "buffer_load_dwordx4 v194, s[44:47], s60 offen lds\n" \
  "s_add_u32 m0, s62, 13312\n" \
  "s_nop 0\n" \
  "buffer_load_dwordx4 v195, s[44:47], s60 offen lds\n" \
  "s_add_u32 m0, s62, 14336\n" \
  "s_nop 0\n" \
  "buffer_load_dwordx4 v196, s[44:47], s60 offen lds\n" \
  "s_add_u32 s60, %12, 92160\n" \
  "s_add_u32 m0, s62, 15360\n" \
  "s_nop 0\n" \
  "buffer_load_dwordx4 v194, s[44:47], s60 offen lds\n" \
  "s_add_u32 m0, s62, 16384\n" \
  "s_nop 0\n" \
  "buffer_load_dwordx4 v195, s[44:47], s60 offen lds\n" \
  "s_add_u32 m0, s62, 17408\n" \
  "s_nop 0\n" \
  "buffer_load_dwordx4 v196, s[44:47], s60 offen lds\n" \
  "s_add_u32 s60, %12, 110592\n" \
  "s_add_u32 m0, s62, 18432\n" \
  "s_nop 0\n" \
  "buffer_load_dwordx4 v194, s[44:47], s60 offen lds\n" \
  "s_add_u32 m0, s62, 19456\n" \
  "s_nop 0\n" \
  "buffer_load_dwordx4 v195, s[44:47], s60 offen lds\n" \
  "s_add_u32 m0, s62, 20480\n" \
  "s_nop 0\n" \
  "buffer_load_dwordx4 v196, s[44:47], s60 offen lds\n" \
  "s_add_u32 s60, %12, 129024\n" \
  "s_add_u32 m0, s62, 21504\n" \
  "s_nop 0\n" \
  "buffer_load_dwordx4 v194, s[44:47], s60 offen lds\n" \
  "s_add_u32 m0, s62, 22528\n" \
  "s_nop 0\n" \
  "buffer_load_dwordx4 v195, s[44:47], s60 offen lds\n" \
  "s_add_u32 m0, s62, 23552\n" \
  "s_nop 0\n" \
  "buffer_load_dwordx4 v196, s[44:47], s60 offen lds\n" \
  "s_waitcnt vmcnt(0)\n" \
  "ds_read_b64_tr_b16 a[0:1], v197\n" \
  "ds_read_b64_tr_b16 a[2:3], v197 offset:3072\n" \
  "ds_read_b64_tr_b16 a[4:5], v197 offset:32\n" \
  "ds_read_b64_tr_b16 a[6:7], v197 offset:3104\n" \
  "ds_read_b64_tr_b16 a[8:9], v197 offset:64\n" \
  "ds_read_b64_tr_b16 a[10:11], v197 offset:3136\n" \
  "ds_read_b64_tr_b16 a[12:13], v197 offset:96\n" \
  "ds_read_b64_tr_b16 a[14:15], v197 offset:3168\n" \
  "ds_read_b64_tr_b16 a[16:17], v197 offset:128\n" \
  "ds_read_b64_tr_b16 a[18:19], v197 offset:3200\n" \
  "ds_read_b64_tr_b16 a[20:21], v197 offset:160\n" \
  "ds_read_b64_tr_b16 a[22:23], v197 offset:3232\n" \
  "ds_read_b64_tr_b16 a[24:25], v197 offset:6144\n" \
  "ds_read_b64_tr_b16 a[26:27], v197 offset:9216\n" \
  "ds_read_b64_tr_b16 a[28:29], v197 offset:6176\n" \
  "ds_read_b64_tr_b16 a[30:31], v197 offset:9248\n" \
  "ds_read_b64_tr_b16 a[32:33], v197 offset:6208\n" \
  "ds_read_b64_tr_b16 a[34:35], v197 offset:9280\n" \
  "ds_read_b64_tr_b16 a[36:37], v197 offset:6240\n" \
  "ds_read_b64_tr_b16 a[38:39], v197 offset:9312\n" \
  "ds_read_b64_tr_b16 a[40:41], v197 offset:6272\n" \
  "ds_read_b64_tr_b16 a[42:43], v197 offset:9344\n" \
  "ds_read_b64_tr_b16 a[44:45], v197 offset:6304\n" \
  "ds_read_b64_tr_b16 a[46:47], v197 offset:9376\n" \
  "ds_read_b64_tr_b16 a[48:49], v197 offset:12288\n" \
  "ds_read_b64_tr_b16 a[50:51], v197 offset:15360\n" \
  "ds_read_b64_tr_b16 a[52:53], v197 offset:12320\n" \
  "ds_read_b64_tr_b16 a[54:55], v197 offset:15392\n" \
  "ds_read_b64_tr_b16 a[56:57], v197 offset:12352\n" \
  "ds_read_b64_tr_b16 a[58:59], v197 offset:15424\n" \
  "ds_read_b64_tr_b16 a[60:61], v197 offset:12384\n" \
  "ds_read_b64_tr_b16 a[62:63], v197 offset:15456\n" \
  "ds_read_b64_tr_b16 a[64:65], v197 offset:12416\n" \
  "ds_read_b64_tr_b16 a[66:67], v197 offset:15488\n" \
  "ds_read_b64_tr_b16 a[68:69], v197 offset:12448\n" \
  "ds_read_b64_tr_b16 a[70:71], v197 offset:15520\n" \
  "ds_read_b64_tr_b16 a[72:73], v197 offset:18432\n" \
  "ds_read_b64_tr_b16 a[74:75], v197 offset:21504\n" \
  "ds_read_b64_tr_b16 a[76:77], v197 offset:18464\n" \
  "ds_read_b64_tr_b16 a[78:79], v197 offset:21536\n" \
  "ds_read_b64_tr_b16 a[80:81], v197 offset:18496\n" \
  "ds_read_b64_tr_b16 a[82:83], v197 offset:21568\n" \
  "ds_read_b64_tr_b16 a[84:85], v197 offset:18528\n" \
  "ds_read_b64_tr_b16 a[86:87], v197 offset:21600\n" \
  "ds_read_b64_tr_b16 a[88:89], v197 offset:18560\n" \
  "ds_read_b64_tr_b16 a[90:91], v197 offset:21632\n" \
  "ds_read_b64_tr_b16 a[92:93], v197 offset:18592\n" \
  "ds_read_b64_tr_b16 a[94:95], v197 offset:21664\n" \
  "s_waitcnt lgkmcnt(0)\n" \
  "s_add_u32 s60, %12, 147456\n" \
  "s_add_u32 m0, s62, 0\n" \
  "s_nop 0\n" \
  "buffer_load_dwordx4 v194, s[44:47], s60 offen lds\n" \
  "s_add_u32 m0, s62, 1024\n" \
  "s_nop 0\n" \
  "buffer_load_dwordx4 v195, s[44:47], s60 offen lds\n" \
  "s_add_u32 m0, s62, 2048\n" \
  "s_nop 0\n" \
  "buffer_load_dwordx4 v196, s[44:47], s60 offen lds\n" \
  "s_add_u32 s60, %12, 165888\n" \
  "s_add_u32 m0, s62, 3072\n" \
  "s_nop 0\n" \
  "buffer_load_dwordx4 v194, s[44:47], s60 offen lds\n" \
  "s_add_u32 m0, s62, 4096\n" \
  "s_nop 0\n" \
  "buffer_load_dwordx4 v195, s[44:47], s60 offen lds\n" \
  "s_add_u32 m0, s62, 5120\n" \
  "s_nop 0\n" \
  "buffer_load_dwordx4 v196, s[44:47], s60 offen lds\n" \
  "s_add_u32 s60, %12, 184320\n" \
  "s_add_u32 m0, s62, 6144\n" \
  "s_nop 0\n" \
  "buffer_load_dwordx4 v194, s[44:47], s60 offen lds\n" \
  "s_add_u32 m0, s62, 7168\n" \
  "s_nop 0\n" \
  "buffer_load_dwordx4 v195, s[44:47], s60 offen lds\n" \
  "s_add_u32 m0, s62, 8192\n" \
  "s_nop 0\n" \
  "buffer_load_dwordx4 v196, s[44:47], s60 offen lds\n" \
  "s_add_u32 s60, %12, 202752\n" \
  "s_add_u32 m0, s62, 9216\n" \
  "s_nop 0\n" \
  "buffer_load_dwordx4 v194, s[44:47], s60 offen lds\n" \
  "s_add_u32 m0, s62, 10240\n" \
  "s_nop 0\n" \
  "buffer_load_dwordx4 v195, s[44:47], s60 offen lds\n" \
  "s_add_u32 m0, s62, 11264\n" \
  "s_nop 0\n" \
  "buffer_load_dwordx4 v196, s[44:47], s60 offen lds\n" \
  "s_add_u32 s60, %12, 221184\n" \
  "s_add_u32 m0, s62, 12288\n" \
  "s_nop 0\n" \
  "buffer_load_dwordx4 v194, s[44:47], s60 offen lds\n" \
  "s_add_u32 m0, s62, 13312\n" \
  "s_nop 0\n" \
  "buffer_load_dwordx4 v195, s[44:47], s60 offen lds\n" \
  "s_add_u32 m0, s62, 14336\n" \
  "s_nop 0\n" \
  "buffer_load_dwordx4 v196, s[44:47], s60 offen lds\n" \
  "s_add_u32 s60, %12, 239616\n" \
  "s_add_u32 m0, s62, 15360\n" \
  "s_nop 0\n" \
  "buffer_load_dwordx4 v194, s[44:47], s60 offen lds\n" \
  "s_add_u32 m0, s62, 16384\n" \
  "s_nop 0\n" \
  "buffer_load_dwordx4 v195, s[44:47], s60 offen lds\n" \
  "s_add_u32 m0, s62, 17408\n" \
  "s_nop 0\n" \
  "buffer_load_dwordx4 v196, s[44:47], s60 offen lds\n" \
  "s_add_u32 s60, %12, 258048\n" \
  "s_add_u32 m0, s62, 18432\n" \
  "s_nop 0\n" \
  "buffer_load_dwordx4 v194, s[44:47], s60 offen lds\n" \
  "s_add_u32 m0, s62, 19456\n" \
  "s_nop 0\n" \
  "buffer_load_dwordx4 v195, s[44:47], s60 offen lds\n" \
  "s_add_u32 m0, s62, 20480\n" \
  "s_nop 0\n" \
  "buffer_load_dwordx4 v196, s[44:47], s60 offen lds\n" \
  "s_add_u32 s60, %12, 276480\n" \
  "s_add_u32 m0, s62, 21504\n" \
  "s_nop 0\n" \
  "buffer_load_dwordx4 v194, s[44:47], s60 offen lds\n" \
  "s_add_u32 m0, s62, 22528\n" \
  "s_nop 0\n" \
  "buffer_load_dwordx4 v195, s[44:47], s60 offen lds\n" \
  "s_add_u32 m0, s62, 23552\n" \
  "s_nop 0\n" \
  "buffer_load_dwordx4 v196, s[44:47], s60 offen lds\n" \
  "s_waitcnt vmcnt(0)\n" \
  "ds_read_b64_tr_b16 a[96:97], v197\n" \
  "ds_read_b64_tr_b16 a[98:99], v197 offset:3072\n" \
  "ds_read_b64_tr_b16 a[100:101], v197 offset:32\n" \
  "ds_read_b64_tr_b16 a[102:103], v197 offset:3104\n" \
  "ds_read_b64_tr_b16 a[104:105], v197 offset:64\n" \
  "ds_read_b64_tr_b16 a[106:107], v197 offset:3136\n" \
  "ds_read_b64_tr_b16 a[108:109], v197 offset:96\n" \
  "ds_read_b64_tr_b16 a[110:111], v197 offset:3168\n" \
  "ds_read_b64_tr_b16 a[112:113], v197 offset:128\n" \
  "ds_read_b64_tr_b16 a[114:115], v197 offset:3200\n" \
  "ds_read_b64_tr_b16 a[116:117], v197 offset:160\n" \
  "ds_read_b64_tr_b16 a[118:119], v197 offset:3232\n" \
  "ds_read_b64_tr_b16 a[120:121], v197 offset:6144\n" \
  "ds_read_b64_tr_b16 a[122:123], v197 offset:9216\n" \
  "ds_read_b64_tr_b16 a[124:125], v197 offset:6176\n" \
  "ds_read_b64_tr_b16 a[126:127], v197 offset:9248\n" \
  "ds_read_b64_tr_b16 a[128:129], v197 offset:6208\n" \
  "ds_read_b64_tr_b16 a[130:131], v197 offset:9280\n" \
  "ds_read_b64_tr_b16 a[132:133], v197 offset:6240\n" \
  "ds_read_b64_tr_b16 a[134:135], v197 offset:9312\n" \
  "ds_read_b64_tr_b16 a[136:137], v197 offset:6272\n" \
  "ds_read_b64_tr_b16 a[138:139], v197 offset:9344\n" \
  "ds_read_b64_tr_b16 a[140:141], v197 offset:6304\n" \
  "ds_read_b64_tr_b16 a[142:143], v197 offset:9376\n" \
  "ds_read_b64_tr_b16 a[144:145], v197 offset:12288\n" \
  "ds_read_b64_tr_b16 a[146:147], v197 offset:15360\n" \
  "ds_read_b64_tr_b16 a[148:149], v197 offset:12320\n" \
  "ds_read_b64_tr_b16 a[150:151], v197 offset:15392\n" \
  "ds_read_b64_tr_b16 a[152:153], v197 offset:12352\n" \
  "ds_read_b64_tr_b16 a[154:155], v197 offset:15424\n" \
  "ds_read_b64_tr_b16 a[156:157], v197 offset:12384\n" \
  "ds_read_b64_tr_b16 a[158:159], v197 offset:15456\n" \
  "ds_read_b64_tr_b16 a[160:161], v197 offset:12416\n" \
  "ds_read_b64_tr_b16 a[162:163], v197 offset:15488\n" \
  "ds_read_b64_tr_b16 a[164:165], v197 offset:12448\n" \
  "ds_read_b64_tr_b16 a[166:167], v197 offset:15520\n" \
  "ds_read_b64_tr_b16 a[168:169], v197 offset:18432\n" \
  "ds_read_b64_tr_b16 a[170:171], v197 offset:21504\n" \
  "ds_read_b64_tr_b16 a[172:173], v197 offset:18464\n" \
  "ds_read_b64_tr_b16 a[174:175], v197 offset:21536\n" \
  "ds_read_b64_tr_b16 a[176:177], v197 offset:18496\n" \
  "ds_read_b64_tr_b16 a[178:179], v197 offset:21568\n" \
  "ds_read_b64_tr_b16 a[180:181], v197 offset:18528\n" \
  "ds_read_b64_tr_b16 a[182:183], v197 offset:21600\n" \
  "ds_read_b64_tr_b16 a[184:185], v197 offset:18560\n" \
  "ds_read_b64_tr_b16 a[186:187], v197 offset:21632\n" \
  "ds_read_b64_tr_b16 a[188:189], v197 offset:18592\n" \
  "ds_read_b64_tr_b16 a[190:191], v197 offset:21664\n" \
  "s_waitcnt lgkmcnt(0)\n" \
  "s_barrier\n" \
  "s_mov_b32 s49, %13\n" \
  "s_mov_b32 s50, %14\n" \
  "s_mov_b32 s48, 0\n" \
  "s_mov_b32 s53, 2130706432\n" \
  "s_mov_b32 s94, 0\n" \
  "v_mov_b32 v187, %11\n" \
  "s_branch .LO_fl_%=\n" \
  ".LO_fl_b_%=:\n" \
  "s_mov_b32 s69, 0\n" \
  ".LO_p0_%=:\n" \
  "buffer_load_dwordx2 v[188:189], v193, s[40:43], 0 offen sc1\n" \
  "s_waitcnt vmcnt(0)\n" \
  "v_cmp_ne_u32 vcc, %11, v189\n" \
  "s_nop 1\n" \
  "v_cndmask_b32 v188, v188, 0, vcc\n" \
  "v_cmp_ge_i32 vcc, s94, v188\n" \
  "s_cbranch_vccz .LO_p0_x_%=\n" \
  "s_sleep 2\n" \
  "s_add_u32 s69, s69, 1\n" \
  "s_and_b32 s63, s69, 1023\n" \
  "s_cbranch_scc1 .LO_p0_%=\n" \
  "buffer_load_dwordx2 v[188:189], off, s[40:43], %16 offset:128 sc1\n" \
  "s_waitcnt vmcnt(0)\n" \
  "v_readfirstlane_b32 s63, v189\n" \
  "s_cmp_eq_u32 s63, %11\n" \
  "s_cbranch_scc1 .LO_p0_x_%=\n" \
  "s_cmp_le_u32 s69, 2097152\n" \
  "s_cbranch_scc1 .LO_p0_%=\n" \
  "v_mov_b32 v188, 1\n" \
  "v_mov_b32 v189, %11\n" \
  "buffer_store_dwordx2 v[188:189], off, s[40:43], %16 offset:128 sc1\n" \
  "s_waitcnt vmcnt(0)\n" \
  ".LO_p0_x_%=:\n" \
  "s_lshr_b32 s76, s49, 8\n" \
  "s_and_b32 s76, s76, 8192\n" \
  "s_xor_b32 s76, s76, s49\n" \
  "s_add_u32 s77, s76, 4096\n" \
  "s_add_u32 s78, s76, 16384\n" \
  "s_add_u32 s79, s76, 20480\n" \
  "s_add_u32 s49, s49, 524288\n" \
  "buffer_load_dwordx4 v[0:3], v190, s[40:43], s76 offen sc1\n" \
  "buffer_load_dwordx4 v[8:11], v190, s[40:43], s76 offen offset:2048 sc1\n" \
  "buffer_load_dwordx4 v[16:19], v190, s[40:43], s77 offen sc1\n" \
  "buffer_load_dwordx4 v[24:27], v190, s[40:43], s77 offen offset:2048 sc1\n" \
  "buffer_load_dwordx4 v[32:35], v190, s[40:43], s78 offen sc1\n" \
  "buffer_load_dwordx4 v[40:43], v190, s[40:43], s78 offen offset:2048 sc1\n" \
  "buffer_load_dwordx4 v[48:51], v190, s[40:43], s79 offen sc1\n" \
  "buffer_load_dwordx4 v[56:59], v190, s[40:43], s79 offen offset:2048 sc1\n" \
  "buffer_load_dwordx4 v[4:7], v190, s[40:43], s76 offen offset:1024 sc1\n" \
  "buffer_load_dwordx4 v[12:15], v190, s[40:43], s76 offen offset:3072 sc1\n" \
  "buffer_load_dwordx4 v[20:23], v190, s[40:43], s77 offen offset:1024 sc1\n" \
  "buffer_load_dwordx4 v[28:31], v190, s[40:43], s77 offen offset:3072 sc1\n" \
  "buffer_load_dwordx4 v[36:39], v190, s[40:43], s78 offen offset:1024 sc1\n" \
  "buffer_load_dwordx4 v[44:47], v190, s[40:43], s78 offen offset:3072 sc1\n" \
  "buffer_load_dwordx4 v[52:55], v190, s[40:43], s79 offen offset:1024 sc1\n" \
  "buffer_load_dwordx4 v[60:63], v190, s[40:43], s79 offen offset:3072 sc1\n" \
  "s_lshr_b32 s76, s49, 8\n" \
  "s_and_b32 s76, s76, 8192\n" \
  "s_xor_b32 s76, s76, s49\n" \
  "s_add_u32 s77, s76, 4096\n" \
  "s_add_u32 s78, s76, 16384\n" \
  "s_add_u32 s79, s76, 20480\n" \
  "s_add_u32 s49, s49, 524288\n" \
  "s_mov_b32 s94, 1\n" \
  "s_waitcnt vmcnt(0)\n" \
  ".LO_loop_%=:\n" \
  "s_waitcnt vmcnt(20)\n" \
  "v_cmp_ne_u32 vcc, %11, v189\n" \
  "s_nop 1\n" \
  "v_cndmask_b32 v188, v188, 0, vcc\n" \
  "v_cmp_ge_i32 vcc, s94, v188\n" \
  "s_mov_b32 s54, 0\n" \
  "s_cbranch_vccnz .LO_pl_%=\n" \
  ".LO_pl_b_%=:\n" \
  "s_waitcnt vmcnt(19)\n" \
  "v_mfma_f32_16x16x32_bf16 v[64:67], a[0:3], v[0:3], 0\n" \
  "buffer_load_dwordx2 v[188:189], v193, s[40:43], 0 offen sc1\n" \
  "v_mfma_f32_16x16x32_bf16 v[68:71], a[4:7], v[0:3], 0\n" \
  "s_waitcnt lgkmcnt(3)\n" \
  "v_mfma_f32_16x16x32_bf16 v[72:75], a[8:11], v[0:3], 0\n" \
  "ds_write_b128 v191, v[88:91] offset:6144\n" \
  "s_waitcnt lgkmcnt(3)\n" \
  "v_mfma_f32_16x16x32_bf16 v[76:79], a[12:15], v[0:3], 0\n" \
  "s_waitcnt lgkmcnt(2)\n" \
  "v_mfma_f32_16x16x32_bf16 v[80:83], a[16:19], v[0:3], 0\n" \
  "s_waitcnt lgkmcnt(1)\n" \
  "v_mfma_f32_16x16x32_bf16 v[84:87], a[20:23], v[0:3], 0\n" \
  "buffer_load_dwordx4 v[0:3], v190, s[40:43], s76 offen sc1\n" \
  "s_waitcnt vmcnt(20)\n" \
  "v_mfma_f32_16x16x32_bf16 v[64:67], a[24:27], v[8:11], v[64:67]\n" \
  "v_mfma_f32_16x16x32_bf16 v[68:71], a[28:31], v[8:11], v[68:71]\n" \
  "v_mfma_f32_16x16x32_bf16 v[72:75], a[32:35], v[8:11], v[72:75]\n" \
  "ds_write_b128 v191, v[92:95] offset:7168\n" \
  "v_mfma_f32_16x16x32_bf16 v[76:79], a[36:39], v[8:11], v[76:79]\n" \
  "v_mfma_f32_16x16x32_bf16 v[80:83], a[40:43], v[8:11], v[80:83]\n" \
  "v_mfma_f32_16x16x32_bf16 v[84:87], a[44:47], v[8:11], v[84:87]\n" \
  "buffer_load_dwordx4 v[8:11], v190, s[40:43], s76 offen offset:2048 sc1\n" \
  "s_waitcnt vmcnt(20)\n" \
  "v_mfma_f32_16x16x32_bf16 v[64:67], a[48:51], v[16:19], v[64:67]\n" \
  "v_mfma_f32_16x16x32_bf16 v[68:71], a[52:55], v[16:19], v[68:71]\n" \
  "v_mfma_f32_16x16x32_bf16 v[72:75], a[56:59], v[16:19], v[72:75]\n" \
  "ds_write_b128 v191, v[96:99] offset:8192\n" \
  "v_mfma_f32_16x16x32_bf16 v[76:79], a[60:63], v[16:19], v[76:79]\n" \
  "v_mfma_f32_16x16x32_bf16 v[80:83], a[64:67], v[16:19], v[80:83]\n" \
  "v_mfma_f32_16x16x32_bf16 v[84:87], a[68:71], v[16:19], v[84:87]\n" \
  "buffer_load_dwordx4 v[16:19], v190, s[40:43], s77 offen sc1\n" \
  "s_waitcnt vmcnt(20)\n" \
  "v_mfma_f32_16x16x32_bf16 v[64:67], a[72:75], v[24:27], v[64:67]\n" \
  "v_mfma_f32_16x16x32_bf16 v[68:71], a[76:79], v[24:27], v[68:71]\n" \
  "v_mfma_f32_16x16x32_bf16 v[72:75], a[80:83], v[24:27], v[72:75]\n" \
  "ds_write_b128 v191, v[100:103] offset:9216\n" \
  "v_mfma_f32_16x16x32_bf16 v[76:79], a[84:87], v[24:27], v[76:79]\n" \
  "v_mfma_f32_16x16x32_bf16 v[80:83], a[88:91], v[24:27], v[80:83]\n" \
  "v_mfma_f32_16x16x32_bf16 v[84:87], a[92:95], v[24:27], v[84:87]\n" \
  "buffer_load_dwordx4 v[24:27], v190, s[40:43], s77 offen offset:2048 sc1\n" \
  "s_waitcnt vmcnt(20)\n" \
  "v_mfma_f32_16x16x32_bf16 v[64:67], a[96:99], v[32:35], v[64:67]\n" \
  "v_mfma_f32_16x16x32_bf16 v[68:71], a[100:103], v[32:35], v[68:71]\n" \
  "v_mfma_f32_16x16x32_bf16 v[72:75], a[104:107], v[32:35], v[72:75]\n" \
  "ds_write_b128 v191, v[104:107] offset:10240\n" \
  "v_mfma_f32_16x16x32_bf16 v[76:79], a[108:111], v[32:35], v[76:79]\n" \
  "v_mfma_f32_16x16x32_bf16 v[80:83], a[112:115], v[32:35], v[80:83]\n" \
  "v_mfma_f32_16x16x32_bf16 v[84:87], a[116:119], v[32:35], v[84:87]\n" \
  "buffer_load_dwordx4 v[32:35], v190, s[40:43], s78 offen sc1\n" \
  "s_waitcnt vmcnt(20)\n" \
  "v_mfma_f32_16x16x32_bf16 v[64:67], a[120:123], v[40:43], v[64:67]\n" \
  "v_mfma_f32_16x16x32_bf16 v[68:71], a[124:127], v[40:43], v[68:71]\n" \
  "v_mfma_f32_16x16x32_bf16 v[72:75], a[128:131], v[40:43], v[72:75]\n" \
  "ds_write_b128 v191, v[108:111] offset:11264\n" \
  "v_mfma_f32_16x16x32_bf16 v[76:79], a[132:135], v[40:43], v[76:79]\n" \
  "v_xor_b32 v191, 65536, v191\n" \
  "v_mfma_f32_16x16x32_bf16 v[80:83], a[136:139], v[40:43], v[80:83]\n" \
  "v_mfma_f32_16x16x32_bf16 v[84:87], a[140:143], v[40:43], v[84:87]\n" \
  "buffer_load_dwordx4 v[40:43], v190, s[40:43], s78 offen offset:2048 sc1\n" \
  "s_waitcnt vmcnt(20)\n" \
  "v_mfma_f32_16x16x32_bf16 v[64:67], a[144:147], v[48:51], v[64:67]\n" \
  "v_mfma_f32_16x16x32_bf16 v[68:71], a[148:151], v[48:51], v[68:71]\n" \
  "v_mfma_f32_16x16x32_bf16 v[72:75], a[152:155], v[48:51], v[72:75]\n" \
  "v_mfma_f32_16x16x32_bf16 v[76:79], a[156:159], v[48:51], v[76:79]\n" \
  "s_waitcnt lgkmcnt(0)\n" \
  "s_waitcnt vmcnt(9)\n" \
  "s_barrier\n" \
  "v_mfma_f32_16x16x32_bf16 v[80:83], a[160:163], v[48:51], v[80:83]\n" \
  "ds_read_b128 v[112:115], v192\n" \
  "v_mfma_f32_16x16x32_bf16 v[84:87], a[164:167], v[48:51], v[84:87]\n" \
  "buffer_load_dwordx4 v[48:51], v190, s[40:43], s79 offen sc1\n" \
  "v_mfma_f32_16x16x32_bf16 v[64:67], a[168:171], v[56:59], v[64:67]\n" \
  "ds_read_b128 v[116:119], v192 offset:12288\n" \
  "v_mfma_f32_16x16x32_bf16 v[68:71], a[172:175], v[56:59], v[68:71]\n" \
  "s_add_i32 s60, s48, -1\n" \
  "v_mov_b32 v186, s60\n" \
  "s_mov_b64 exec, 1\n" \
  "buffer_store_dwordx2 v[186:187], off, s[40:43], %15 sc1\n" \
  "s_mov_b64 exec, -1\n" \
  "v_mfma_f32_16x16x32_bf16 v[72:75], a[176:179], v[56:59], v[72:75]\n" \
  "ds_read_b128 v[120:123], v192 offset:24576\n" \
  "v_mfma_f32_16x16x32_bf16 v[76:79], a[180:183], v[56:59], v[76:79]\n" \
  "ds_read_b128 v[124:127], v192 offset:36864\n" \
  "v_mfma_f32_16x16x32_bf16 v[80:83], a[184:187], v[56:59], v[80:83]\n" \
  "ds_read_b128 v[128:131], v192 offset:1024\n" \
  "v_mfma_f32_16x16x32_bf16 v[84:87], a[188:191], v[56:59], v[84:87]\n" \
  "buffer_load_dwordx4 v[56:59], v190, s[40:43], s79 offen offset:2048 sc1\n" \
  "v_mfma_f32_16x16x32_bf16 v[88:91], a[0:3], v[4:7], 0\n" \
  "ds_read_b128 v[132:135], v192 offset:13312\n" \
  "v_mfma_f32_16x16x32_bf16 v[92:95], a[4:7], v[4:7], 0\n" \
  "ds_read_b128 v[136:139], v192 offset:25600\n" \
  "v_mfma_f32_16x16x32_bf16 v[96:99], a[8:11], v[4:7], 0\n" \
  "ds_write_b128 v191, v[64:67]\n" \
  "v_mfma_f32_16x16x32_bf16 v[100:103], a[12:15], v[4:7], 0\n" \
  "ds_read_b128 v[140:143], v192 offset:37888\n" \
  "v_mfma_f32_16x16x32_bf16 v[104:107], a[16:19], v[4:7], 0\n" \
  "ds_read_b128 v[144:147], v192 offset:2048\n" \
  "v_mfma_f32_16x16x32_bf16 v[108:111], a[20:23], v[4:7], 0\n" \
  "buffer_load_dwordx4 v[4:7], v190, s[40:43], s76 offen offset:1024 sc1\n" \
  "v_mfma_f32_16x16x32_bf16 v[88:91], a[24:27], v[12:15], v[88:91]\n" \
  "ds_read_b128 v[148:151], v192 offset:14336\n" \
  "v_mfma_f32_16x16x32_bf16 v[92:95], a[28:31], v[12:15], v[92:95]\n" \
  "ds_read_b128 v[152:155], v192 offset:26624\n" \
  "v_mfma_f32_16x16x32_bf16 v[96:99], a[32:35], v[12:15], v[96:99]\n" \
  "ds_write_b128 v191, v[68:71] offset:1024\n" \
  "v_mfma_f32_16x16x32_bf16 v[100:103], a[36:39], v[12:15], v[100:103]\n" \
  "ds_read_b128 v[156:159], v192 offset:38912\n" \
  "v_mfma_f32_16x16x32_bf16 v[104:107], a[40:43], v[12:15], v[104:107]\n" \
  "s_waitcnt lgkmcnt(12)\n" \
  "v_add_f32 v112, v112, v116\n" \
  "v_add_f32 v113, v113, v117\n" \
  "v_mfma_f32_16x16x32_bf16 v[108:111], a[44:47], v[12:15], v[108:111]\n" \
  "buffer_load_dwordx4 v[12:15], v190, s[40:43], s76 offen offset:3072 sc1\n" \
  "v_mfma_f32_16x16x32_bf16 v[88:91], a[48:51], v[20:23], v[88:91]\n" \
  "v_add_f32 v114, v114, v118\n" \
  "v_add_f32 v115, v115, v119\n" \
  "v_mfma_f32_16x16x32_bf16 v[92:95], a[52:55], v[20:23], v[92:95]\n" \
  "s_waitcnt lgkmcnt(10)\n" \
  "v_add_f32 v120, v120, v124\n" \
  "v_add_f32 v121, v121, v125\n" \
  "v_mfma_f32_16x16x32_bf16 v[96:99], a[56:59], v[20:23], v[96:99]\n" \
  "ds_write_b128 v191, v[72:75] offset:2048\n" \
  "v_mfma_f32_16x16x32_bf16 v[100:103], a[60:63], v[20:23], v[100:103]\n" \
  "v_add_f32 v122, v122, v126\n" \
  "v_add_f32 v123, v123, v127\n" \
  "v_mfma_f32_16x16x32_bf16 v[104:107], a[64:67], v[20:23], v[104:107]\n" \
  "v_add_f32 v112, v112, v120\n" \
  "v_add_f32 v113, v113, v121\n" \
  "v_mfma_f32_16x16x32_bf16 v[108:111], a[68:71], v[20:23], v[108:111]\n" \
  "buffer_load_dwordx4 v[20:23], v190, s[40:43], s77 offen offset:1024 sc1\n" \
  "v_mfma_f32_16x16x32_bf16 v[88:91], a[72:75], v[28:31], v[88:91]\n" \
  "v_add_f32 v114, v114, v122\n" \
  "v_add_f32 v115, v115, v123\n" \
  "v_mfma_f32_16x16x32_bf16 v[92:95], a[76:79], v[28:31], v[92:95]\n" \
  "buffer_store_dwordx4 v[112:115], v190, s[40:43], s53 offen sc1\n" \
  "v_mfma_f32_16x16x32_bf16 v[96:99], a[80:83], v[28:31], v[96:99]\n" \
  "ds_write_b128 v191, v[76:79] offset:3072\n" \
  "v_mfma_f32_16x16x32_bf16 v[100:103], a[84:87], v[28:31], v[100:103]\n" \
  "s_waitcnt lgkmcnt(10)\n" \
  "v_add_f32 v128, v128, v132\n" \
  "v_add_f32 v129, v129, v133\n" \
  "v_mfma_f32_16x16x32_bf16 v[104:107], a[88:91], v[28:31], v[104:107]\n" \
  "v_add_f32 v130, v130, v134\n" \
  "v_add_f32 v131, v131, v135\n" \
  "v_mfma_f32_16x16x32_bf16 v[108:111], a[92:95], v[28:31], v[108:111]\n" \
  "buffer_load_dwordx4 v[28:31], v190, s[40:43], s77 offen offset:3072 sc1\n" \
  "v_mfma_f32_16x16x32_bf16 v[88:91], a[96:99], v[36:39], v[88:91]\n" \
  "s_waitcnt lgkmcnt(7)\n" \
  "v_add_f32 v136, v136, v140\n" \
  "v_add_f32 v137, v137, v141\n" \
  "v_mfma_f32_16x16x32_bf16 v[92:95], a[100:103], v[36:39], v[92:95]\n" \
  "v_add_f32 v138, v138, v142\n" \
  "v_add_f32 v139, v139, v143\n" \
  "v_mfma_f32_16x16x32_bf16 v[96:99], a[104:107], v[36:39], v[96:99]\n" \
  "ds_write_b128 v191, v[80:83] offset:4096\n" \
  "v_mfma_f32_16x16x32_bf16 v[100:103], a[108:111], v[36:39], v[100:103]\n" \
  "v_add_f32 v128, v128, v136\n" \
  "v_add_f32 v129, v129, v137\n" \
  "v_mfma_f32_16x16x32_bf16 v[104:107], a[112:115], v[36:39], v[104:107]\n" \
  "v_add_f32 v130, v130, v138\n" \
  "v_add_f32 v131, v131, v139\n" \
  "v_mfma_f32_16x16x32_bf16 v[108:111], a[116:119], v[36:39], v[108:111]\n" \
  "buffer_load_dwordx4 v[36:39], v190, s[40:43], s78 offen offset:1024 sc1\n" \
  "v_mfma_f32_16x16x32_bf16 v[88:91], a[120:123], v[44:47], v[88:91]\n" \
  "buffer_store_dwordx4 v[128:131], v190, s[40:43], s53 offen offset:1024 sc1\n" \
  "v_mfma_f32_16x16x32_bf16 v[92:95], a[124:127], v[44:47], v[92:95]\n" \
  "s_waitcnt lgkmcnt(6)\n" \
  "v_add_f32 v144, v144, v148\n" \
  "v_add_f32 v145, v145, v149\n" \
  "v_mfma_f32_16x16x32_bf16 v[96:99], a[128:131], v[44:47], v[96:99]\n" \
  "ds_write_b128 v191, v[84:87] offset:5120\n" \
  "v_mfma_f32_16x16x32_bf16 v[100:103], a[132:135], v[44:47], v[100:103]\n" \
  "v_add_f32 v146, v146, v150\n" \
  "v_add_f32 v147, v147, v151\n" \
  "v_mfma_f32_16x16x32_bf16 v[104:107], a[136:139], v[44:47], v[104:107]\n" \
  "s_waitcnt lgkmcnt(4)\n" \
  "v_add_f32 v152, v152, v156\n" \
  "v_add_f32 v153, v153, v157\n" \
  "v_mfma_f32_16x16x32_bf16 v[108:111], a[140:143], v[44:47], v[108:111]\n" \
  "buffer_load_dwordx4 v[44:47], v190, s[40:43], s78 offen offset:3072 sc1\n" \
  "s_waitcnt vmcnt(19)\n" \
  "v_mfma_f32_16x16x32_bf16 v[88:91], a[144:147], v[52:55], v[88:91]\n" \
  "v_add_f32 v154, v154, v158\n" \
  "v_add_f32 v155, v155, v159\n" \
  "v_mfma_f32_16x16x32_bf16 v[92:95], a[148:151], v[52:55], v[92:95]\n" \
  "v_add_f32 v144, v144, v152\n" \
  "v_add_f32 v145, v145, v153\n" \
  "v_mfma_f32_16x16x32_bf16 v[96:99], a[152:155], v[52:55], v[96:99]\n" \
  "v_mfma_f32_16x16x32_bf16 v[100:103], a[156:159], v[52:55], v[100:103]\n" \
  "v_add_f32 v146, v146, v154\n" \
  "v_add_f32 v147, v147, v155\n" \
  "v_mfma_f32_16x16x32_bf16 v[104:107], a[160:163], v[52:55], v[104:107]\n" \
  "buffer_store_dwordx4 v[144:147], v190, s[40:43], s53 offen offset:2048 sc1\n" \
  "v_mfma_f32_16x16x32_bf16 v[108:111], a[164:167], v[52:55], v[108:111]\n" \
  "buffer_load_dwordx4 v[52:55], v190, s[40:43], s79 offen offset:1024 sc1\n" \
  "s_waitcnt vmcnt(20)\n" \
  "v_mfma_f32_16x16x32_bf16 v[88:91], a[168:171], v[60:63], v[88:91]\n" \
  "v_mfma_f32_16x16x32_bf16 v[92:95], a[172:175], v[60:63], v[92:95]\n" \
  "v_mfma_f32_16x16x32_bf16 v[96:99], a[176:179], v[60:63], v[96:99]\n" \
  "s_lshr_b32 s53, s50, 8\n" \
  "s_and_b32 s53, s53, 8192\n" \
  "s_xor_b32 s53, s53, s50\n" \
  "s_add_u32 s50, s50, 786432\n" \
  "v_xor_b32 v192, 65536, v192\n" \
  "v_mfma_f32_16x16x32_bf16 v[100:103], a[180:183], v[60:63], v[100:103]\n" \
  "v_mfma_f32_16x16x32_bf16 v[104:107], a[184:187], v[60:63], v[104:107]\n" \
  "v_mfma_f32_16x16x32_bf16 v[108:111], a[188:191], v[60:63], v[108:111]\n" \
  "buffer_load_dwordx4 v[60:63], v190, s[40:43], s79 offen offset:3072 sc1\n" \
  "s_cmp_lg_u32 s54, 0\n" \
  "s_cbranch_scc1 .LO_ft_%=\n" \
  ".LO_ft_b_%=:\n" \
  "s_lshr_b32 s76, s49, 8\n" \
  "s_and_b32 s76, s76, 8192\n" \
  "s_xor_b32 s76, s76, s49\n" \
  "s_add_u32 s77, s76, 4096\n" \
  "s_add_u32 s78, s76, 16384\n" \
  "s_add_u32 s79, s76, 20480\n" \
  "s_add_u32 s49, s49, 524288\n" \
  "s_add_u32 s48, s48, 1\n" \
  "s_add_u32 s94, s48, 1\n" \
  "s_cmp_lt_u32 s94, %17\n" \
  "s_cselect_b32 s94, s94, -1\n" \
  "s_cmp_lt_u32 s48, %17\n" \
  "s_cbranch_scc1 .LO_loop_%=\n" \
  ".LO_fl_%=:\n" \
  "s_nop 7\n" \
  "ds_write_b128 v191, v[88:91] offset:6144\n" \
  "ds_write_b128 v191, v[92:95] offset:7168\n" \
  "ds_write_b128 v191, v[96:99] offset:8192\n" \
  "ds_write_b128 v191, v[100:103] offset:9216\n" \
  "ds_write_b128 v191, v[104:107] offset:10240\n" \
  "ds_write_b128 v191, v[108:111] offset:11264\n" \
  "s_waitcnt lgkmcnt(0)\n" \
  "s_barrier\n" \
  "ds_read_b128 v[112:115], v192\n" \
  "ds_read_b128 v[116:119], v192 offset:12288\n" \
  "ds_read_b128 v[120:123], v192 offset:24576\n" \
  "ds_read_b128 v[124:127], v192 offset:36864\n" \
  "ds_read_b128 v[128:131], v192 offset:1024\n" \
  "ds_read_b128 v[132:135], v192 offset:13312\n" \
  "ds_read_b128 v[136:139], v192 offset:25600\n" \
  "ds_read_b128 v[140:143], v192 offset:37888\n" \
  "ds_read_b128 v[144:147], v192 offset:2048\n" \
  "ds_read_b128 v[148:151], v192 offset:14336\n" \
  "ds_read_b128 v[152:155], v192 offset:26624\n" \
  "ds_read_b128 v[156:159], v192 offset:38912\n" \
  "s_waitcnt lgkmcnt(10)\n" \
  "v_add_f32 v112, v112, v116\n" \
  "v_add_f32 v113, v113, v117\n" \
  "v_add_f32 v114, v114, v118\n" \
  "v_add_f32 v115, v115, v119\n" \
  "s_waitcnt lgkmcnt(8)\n" \
  "v_add_f32 v120, v120, v124\n" \
  "v_add_f32 v121, v121, v125\n" \
  "v_add_f32 v122, v122, v126\n" \
  "v_add_f32 v123, v123, v127\n" \
  "v_add_f32 v112, v112, v120\n" \
  "v_add_f32 v113, v113, v121\n" \
  "v_add_f32 v114, v114, v122\n" \
  "v_add_f32 v115, v115, v123\n" \
  "buffer_store_dwordx4 v[112:115], v190, s[40:43], s53 offen sc1\n" \
  "s_waitcnt lgkmcnt(6)\n" \
  "v_add_f32 v128, v128, v132\n" \
  "v_add_f32 v129, v129, v133\n" \
  "v_add_f32 v130, v130, v134\n" \
  "v_add_f32 v131, v131, v135\n" \
  "s_waitcnt lgkmcnt(4)\n" \
  "v_add_f32 v136, v136, v140\n" \
  "v_add_f32 v137, v137, v141\n" \
  "v_add_f32 v138, v138, v142\n" \
  "v_add_f32 v139, v139, v143\n" \
  "v_add_f32 v128, v128, v136\n" \
  "v_add_f32 v129, v129, v137\n" \
  "v_add_f32 v130, v130, v138\n" \
  "v_add_f32 v131, v131, v139\n" \
  "buffer_store_dwordx4 v[128:131], v190, s[40:43], s53 offen offset:1024 sc1\n" \
  "s_waitcnt lgkmcnt(2)\n" \
  "v_add_f32 v144, v144, v148\n" \
  "v_add_f32 v145, v145, v149\n" \
  "v_add_f32 v146, v146, v150\n" \
  "v_add_f32 v147, v147, v151\n" \
  "s_waitcnt lgkmcnt(0)\n" \
  "v_add_f32 v152, v152, v156\n" \
  "v_add_f32 v153, v153, v157\n" \
  "v_add_f32 v154, v154, v158\n" \
  "v_add_f32 v155, v155, v159\n" \
  "v_add_f32 v144, v144, v152\n" \
  "v_add_f32 v145, v145, v153\n" \
  "v_add_f32 v146, v146, v154\n" \
  "v_add_f32 v147, v147, v155\n" \
  "buffer_store_dwordx4 v[144:147], v190, s[40:43], s53 offen offset:2048 sc1\n" \
  "s_waitcnt vmcnt(0)\n" \
  "s_barrier\n" \
  "s_add_i32 s60, s48, 0\n" \
  "v_mov_b32 v186, s60\n" \
  "s_mov_b64 exec, 1\n" \
  "buffer_store_dwordx2 v[186:187], off, s[40:43], %15 sc1\n" \
  "s_mov_b64 exec, -1\n" \
  "s_cmp_eq_u32 s48, 0\n" \
  "s_cbranch_scc1 .LO_fl_b_%=\n" \
  "s_branch .LO_end_%=\n" \
  ".LO_pl_%=:\n" \
  "s_add_u32 s60, s48, 2\n" \
  "s_cmp_lg_u32 s60, %17\n" \
  "s_cbranch_scc1 .LO_ft_%=\n" \
  "s_mov_b32 s54, s76\n" \
  "s_mov_b32 s76, 2130706432\n" \
  "s_mov_b32 s77, 2130706432\n" \
  "s_mov_b32 s78, 2130706432\n" \
  "s_mov_b32 s79, 2130706432\n" \
  "s_branch .LO_pl_b_%=\n" \
  ".LO_ft_%=:\n" \
  "s_mov_b32 s69, 0\n" \
  ".LO_p1_%=:\n" \
  "buffer_load_dwordx2 v[188:189], v193, s[40:43], 0 offen sc1\n" \
  "s_waitcnt vmcnt(0)\n" \
  "v_cmp_ne_u32 vcc, %11, v189\n" \
  "s_nop 1\n" \
  "v_cndmask_b32 v188, v188, 0, vcc\n" \
  "v_cmp_ge_i32 vcc, s94, v188\n" \
  "s_cbranch_vccz .LO_p1_x_%=\n" \
  "s_sleep 2\n" \
  "s_add_u32 s69, s69, 1\n" \
  "s_and_b32 s63, s69, 1023\n" \
  "s_cbranch_scc1 .LO_p1_%=\n" \
  "buffer_load_dwordx2 v[188:189], off, s[40:43], %16 offset:128 sc1\n" \
  "s_waitcnt vmcnt(0)\n" \
  "v_readfirstlane_b32 s63, v189\n" \
  "s_cmp_eq_u32 s63, %11\n" \
  "s_cbranch_scc1 .LO_p1_x_%=\n" \
  "s_cmp_le_u32 s69, 2097152\n" \
  "s_cbranch_scc1 .LO_p1_%=\n" \
  "v_mov_b32 v188, 1\n" \
  "v_mov_b32 v189, %11\n" \
  "buffer_store_dwordx2 v[188:189], off, s[40:43], %16 offset:128 sc1\n" \
  "s_waitcnt vmcnt(0)\n" \
  ".LO_p1_x_%=:\n" \
  "s_cmp_eq_u32 s54, 0\n" \
  "s_cbranch_scc1 .LO_pl_b_%=\n" \
  "s_mov_b32 s76, s54\n" \
  "s_add_u32 s77, s76, 4096\n" \
  "s_add_u32 s78, s76, 16384\n" \
  "s_add_u32 s79, s76, 20480\n" \
  "buffer_load_dwordx4 v[0:3], v190, s[40:43], s76 offen sc1\n" \
  "buffer_load_dwordx4 v[8:11], v190, s[40:43], s76 offen offset:2048 sc1\n" \
  "buffer_load_dwordx4 v[16:19], v190, s[40:43], s77 offen sc1\n" \
  "buffer_load_dwordx4 v[24:27], v190, s[40:43], s77 offen offset:2048 sc1\n" \
  "buffer_load_dwordx4 v[32:35], v190, s[40:43], s78 offen sc1\n" \
  "buffer_load_dwordx4 v[40:43], v190, s[40:43], s78 offen offset:2048 sc1\n" \
  "buffer_load_dwordx4 v[48:51], v190, s[40:43], s79 offen sc1\n" \
  "buffer_load_dwordx4 v[56:59], v190, s[40:43], s79 offen offset:2048 sc1\n" \
  "buffer_load_dwordx4 v[4:7], v190, s[40:43], s76 offen offset:1024 sc1\n" \
  "buffer_load_dwordx4 v[12:15], v190, s[40:43], s76 offen offset:3072 sc1\n" \
  "buffer_load_dwordx4 v[20:23], v190, s[40:43], s77 offen offset:1024 sc1\n" \
  "buffer_load_dwordx4 v[28:31], v190, s[40:43], s77 offen offset:3072 sc1\n" \
  "buffer_load_dwordx4 v[36:39], v190, s[40:43], s78 offen offset:1024 sc1\n" \
  "buffer_load_dwordx4 v[44:47], v190, s[40:43], s78 offen offset:3072 sc1\n" \
  "buffer_load_dwordx4 v[52:55], v190, s[40:43], s79 offen offset:1024 sc1\n" \
  "buffer_load_dwordx4 v[60:63], v190, s[40:43], s79 offen offset:3072 sc1\n" \
  "s_waitcnt vmcnt(8)\n" \
  "s_branch .LO_ft_b_%=\n" \
  ".LO_end_%=:\n"
#define O_ASM_VOPS "+{v190}"(oi.l16), "+{v191}"(oi.rw), "+{v192}"(oi.rr), "+{v193}"(oi.fo), "+{v194}"(oi.wv0), "+{v195}"(oi.wv1), "+{v196}"(oi.wv2), "+{v197}"(oi.tro)
#define O_ASM_SOPS "s"(oi.ws), "s"(oi.Wo), "s"(oi.w), "s"(oi.tag), "s"(oi.o_w), "s"(oi.o_o), "s"(oi.o_a), "s"(oi.f_a), "s"(oi.o_ctl), "s"(oi.ns)
#define O_ASM_CLOB "v0", "v1", "v2", "v3", "v4", "v5", "v6", "v7", "v8", "v9", "v10", "v11", "v12", "v13", "v14", "v15", "v16", "v17", "v18", "v19", "v20", "v21", "v22", "v23", "v24", "v25", "v26", "v27", "v28", "v29", "v30", "v31", "v32", "v33", "v34", "v35", "v36", "v37", "v38", "v39", "v40", "v41", "v42", "v43", "v44", "v45", "v46", "v47", "v48", "v49", "v50", "v51", "v52", "v53", "v54", "v55", "v56", "v57", "v58", "v59", "v60", "v61", "v62", "v63", "v64", "v65", "v66", "v67", "v68", "v69", "v70", "v71", "v72", "v73", "v74", "v75", "v76", "v77", "v78", "v79", "v80", "v81", "v82", "v83", "v84", "v85", "v86", "v87", "v88", "v89", "v90", "v91", "v92", "v93", "v94", "v95", "v96", "v97", "v98", "v99", "v100", "v101", "v102", "v103", "v104", "v105", "v106", "v107", "v108", "v109", "v110", "v111", "v112", "v113", "v114", "v115", "v116", "v117", "v118", "v119", "v120", "v121", "v122", "v123", "v124", "v125", "v126", "v127", "v128", "v129", "v130", "v131", "v132", "v133", "v134", "v135", "v136", "v137", "v138", "v139", "v140", "v141", "v142", "v143", "v144", "v145", "v146", "v147", "v148", "v149", "v150", "v151", "v152", "v153", "v154", "v155", "v156", "v157", "v158", "v159", "v160", "v161", "v162", "v163", "v164", "v165", "v166", "v167", "v168", "v169", "v170", "v171", "v172", "v173", "v174", "v175", "v176", "v177", "v178", "v179", "v180", "v181", "v182", "v183", "v184", "v185", "v186", "v187", "v188", "v189", "a0", "a1", "a2", "a3", "a4", "a5", "a6", "a7", "a8", "a9", "a10", "a11", "a12", "a13", "a14", "a15", "a16", "a17", "a18", "a19", "a20", "a21", "a22", "a23", "a24", "a25", "a26", "a27", "a28", "a29", "a30", "a31", "a32", "a33", "a34", "a35", "a36", "a37", "a38", "a39", "a40", "a41", "a42", "a43", "a44", "a45", "a46", "a47", "a48", "a49", "a50", "a51", "a52", "a53", "a54", "a55", "a56", "a57", "a58", "a59", "a60", "a61", "a62", "a63", "a64", "a65", "a66", "a67", "a68", "a69", "a70", "a71", "a72", "a73", "a74", "a75", "a76", "a77", "a78", "a79", "a80", "a81", "a82", "a83", "a84", "a85", "a86", "a87", "a88", "a89", "a90", "a91", "a92", "a93", "a94", "a95", "a96", "a97", "a98", "a99", "a100", "a101", "a102", "a103", "a104", "a105", "a106", "a107", "a108", "a109", "a110", "a111", "a112", "a113", "a114", "a115", "a116", "a117", "a118", "a119", "a120", "a121", "a122", "a123", "a124", "a125", "a126", "a127", "a128", "a129", "a130", "a131", "a132", "a133", "a134", "a135", "a136", "a137", "a138", "a139", "a140", "a141", "a142", "a143", "a144", "a145", "a146", "a147", "a148", "a149", "a150", "a151", "a152", "a153", "a154", "a155", "a156", "a157", "a158", "a159", "a160", "a161", "a162", "a163", "a164", "a165", "a166", "a167", "a168", "a169", "a170", "a171", "a172", "a173", "a174", "a175", "a176", "a177", "a178", "a179", "a180", "a181", "a182", "a183", "a184", "a185", "a186", "a187", "a188", "a189", "a190", "a191", "s40", "s41", "s42", "s43", "s44", "s45", "s46", "s47", "s48", "s49", "s50", "s51", "s52", "s53", "s54", "s55", "s56", "s57", "s58", "s59", "s60", "s61", "s62", "s63", "s64", "s65", "s66", "s67", "s68", "s69", "s70", "s71", "s72", "s73", "s74", "s75", "s76", "s77", "s78", "s79", "s80", "s81", "s82", "s83", "s84", "s85", "s86", "s87", "s88", "s89", "s90", "s91", "s92", "s93", "s94", "s95", "s96", "s97", "s98", "s99", "vcc", "scc", "memory"
#define F_ASM_TEXT \
  "s_mov_b64 s[40:41], %22\n" \
  "s_and_b32 s41, s41, 0xffff\n" \
  "s_mov_b32 s42, 255852544\n" \
  "s_mov_b32 s43, 0x00020000\n" \
  "s_mov_b64 s[44:45], %23\n" \
  "s_and_b32 s45, s45, 0xffff\n" \
  "s_mov_b32 s46, 2359296\n" \
  "s_mov_b32 s47, 0x00020000\n" \
  "s_mov_b32 s48, 0\n" \
  ".LF_pre_%=:\n" \
  "s_add_u32 s61, s48, 12\n" \
  "s_lshl_b32 s61, s61, 16\n" \
  "s_xor_b32 s61, s61, %25\n" \
  "s_lshl_b32 s62, s48, 17\n" \
  "s_add_u32 s62, s62, %26\n" \
  "buffer_load_dwordx4 v[64:67], v202, s[40:43], s62 offen sc1\n" \
  "s_sleep 1\n" \
  "buffer_load_dwordx4 v[68:71], v202, s[40:43], s62 offen offset:1024 sc1\n" \
  "s_sleep 1\n" \
  "buffer_load_dwordx4 v[72:75], v202, s[40:43], s62 offen offset:2048 sc1\n" \
  "s_sleep 1\n" \
  "buffer_load_dwordx4 v[76:79], v202, s[40:43], s62 offen offset:3072 sc1\n" \
  "s_sleep 1\n" \
  "s_add_u32 s62, s62, 32768\n" \
  "buffer_load_dwordx4 v[80:83], v202, s[40:43], s62 offen sc1\n" \
  "s_sleep 1\n" \
  "buffer_load_dwordx4 v[84:87], v202, s[40:43], s62 offen offset:1024 sc1\n" \
  "s_sleep 1\n" \
  "buffer_load_dwordx4 v[88:91], v202, s[40:43], s62 offen offset:2048 sc1\n" \
  "s_sleep 1\n" \
  "buffer_load_dwordx4 v[92:95], v202, s[40:43], s62 offen offset:3072 sc1\n" \
  "s_sleep 1\n" \
  "s_add_u32 s62, s62, 32768\n" \
  "buffer_load_dwordx4 v[96:99], v202, s[40:43], s62 offen sc1\n" \
  "s_sleep 1\n" \
  "buffer_load_dwordx4 v[100:103], v202, s[40:43], s62 offen offset:1024 sc1\n" \
  "s_sleep 1\n" \
  "buffer_load_dwordx4 v[104:107], v202, s[40:43], s62 offen offset:2048 sc1\n" \
  "s_sleep 1\n" \
  "buffer_load_dwordx4 v[108:111], v202, s[40:43], s62 offen offset:3072 sc1\n" \
  "s_sleep 1\n" \
  "s_add_u32 s62, s62, 32768\n" \
  "buffer_load_dwordx4 v[112:115], v202, s[40:43], s62 offen sc1\n" \
  "s_sleep 1\n" \
  "buffer_load_dwordx4 v[116:119], v202, s[40:43], s62 offen offset:1024 sc1\n" \
  "s_sleep 1\n" \
  "buffer_load_dwordx4 v[120:123], v202, s[40:43], s62 offen offset:2048 sc1\n" \
  "s_sleep 1\n" \
  "buffer_load_dwordx4 v[124:127], v202, s[40:43], s62 offen offset:3072 sc1\n" \
  "s_sleep 1\n" \
  "s_add_u32 s62, s62, 32768\n" \
  "s_add_u32 s53, s48, 60\n" \
  "s_mov_b32 s69, 0\n" \
  ".LF_fu_%=:\n" \
  "buffer_load_dwordx2 v[32:33], v209, s[40:43], 0 offen sc1\n" \
  "s_waitcnt vmcnt(0)\n" \
  "v_cmp_ne_u32 vcc, %24, v33\n" \
  "s_nop 1\n" \
  "v_cndmask_b32 v32, v32, 0, vcc\n" \
  "v_cmp_ge_i32 vcc, s53, v32\n" \
  "s_cbranch_vccz .LF_fu_x_%=\n" \
  "s_sleep 24\n" \
  "s_add_u32 s69, s69, 1\n" \
  "s_cmp_lt_u32 s69, 131072\n" \
  "s_cbranch_scc1 .LF_fu_%=\n" \
  "v_mov_b32 v32, 1\n" \
  "v_mov_b32 v33, %24\n" \
  "buffer_store_dwordx2 v[32:33], off, s[40:43], %31 offset:128 sc1\n" \
  "s_waitcnt vmcnt(0)\n" \
  ".LF_fu_x_%=:\n" \
  "s_xor_b32 s60, s61, 0\n" \
  "buffer_load_dwordx4 v[32:35], v201, s[40:43], s60 offen sc1\n" \
  "s_sleep 1\n" \
  "s_xor_b32 s60, s61, 1024\n" \
  "buffer_load_dwordx4 v[36:39], v201, s[40:43], s60 offen sc1\n" \
  "s_sleep 1\n" \
  "s_xor_b32 s60, s61, 2048\n" \
  "buffer_load_dwordx4 v[40:43], v201, s[40:43], s60 offen sc1\n" \
  "s_sleep 1\n" \
  "s_xor_b32 s60, s61, 3072\n" \
  "buffer_load_dwordx4 v[44:47], v201, s[40:43], s60 offen sc1\n" \
  "s_sleep 1\n" \
  "s_xor_b32 s60, s61, 4096\n" \
  "buffer_load_dwordx4 v[48:51], v201, s[40:43], s60 offen sc1\n" \
  "s_sleep 1\n" \
  "s_xor_b32 s60, s61, 5120\n" \
  "buffer_load_dwordx4 v[52:55], v201, s[40:43], s60 offen sc1\n" \
  "s_sleep 1\n" \
  "s_xor_b32 s60, s61, 6144\n" \
  "buffer_load_dwordx4 v[56:59], v201, s[40:43], s60 offen sc1\n" \
  "s_sleep 1\n" \
  "s_xor_b32 s60, s61, 7168\n" \
  "buffer_load_dwordx4 v[60:63], v201, s[40:43], s60 offen sc1\n" \
  "s_sleep 1\n" \
  "v_mov_b64 v[128:129], 0\n" \
  "v_mov_b64 v[130:131], 0\n" \
  "v_mov_b64 v[132:133], 0\n" \
  "v_mov_b64 v[134:135], 0\n" \
  "v_mov_b64 v[136:137], 0\n" \
  "v_mov_b64 v[138:139], 0\n" \
  "v_mov_b64 v[140:141], 0\n" \
  "v_mov_b64 v[142:143], 0\n" \
  "v_mov_b64 v[144:145], 0\n" \
  "v_mov_b64 v[146:147], 0\n" \
  "v_mov_b64 v[148:149], 0\n" \
  "v_mov_b64 v[150:151], 0\n" \
  "v_mov_b64 v[152:153], 0\n" \
  "v_mov_b64 v[154:155], 0\n" \
  "v_mov_b64 v[156:157], 0\n" \
  "v_mov_b64 v[158:159], 0\n" \
  "v_mov_b64 v[160:161], 0\n" \
  "v_mov_b64 v[162:163], 0\n" \
  "v_mov_b64 v[164:165], 0\n" \
  "v_mov_b64 v[166:167], 0\n" \
  "v_mov_b64 v[168:169], 0\n" \
  "v_mov_b64 v[170:171], 0\n" \
  "v_mov_b64 v[172:173], 0\n" \
  "v_mov_b64 v[174:175], 0\n" \
  "v_mov_b64 v[176:177], 0\n" \
  "v_mov_b64 v[178:179], 0\n" \
  "v_mov_b64 v[180:181], 0\n" \
  "v_mov_b64 v[182:183], 0\n" \
  "v_mov_b64 v[184:185], 0\n" \
  "v_mov_b64 v[186:187], 0\n" \
  "v_mov_b64 v[188:189], 0\n" \
  "v_mov_b64 v[190:191], 0\n" \
  "s_waitcnt vmcnt(0)\n" \
  "v_mov_b32 v0, v32\n" \
  "v_mov_b32 v1, v36\n" \
  "v_mov_b32 v2, v33\n" \
  "v_mov_b32 v3, v37\n" \
  "v_mov_b32 v4, v34\n" \
  "v_mov_b32 v5, v38\n" \
  "v_mov_b32 v6, v35\n" \
  "v_mov_b32 v7, v39\n" \
  "v_mov_b32 v8, v40\n" \
  "v_mov_b32 v9, v44\n" \
  "v_mov_b32 v10, v41\n" \
  "v_mov_b32 v11, v45\n" \
  "v_mov_b32 v12, v42\n" \
  "v_mov_b32 v13, v46\n" \
  "v_mov_b32 v14, v43\n" \
  "v_mov_b32 v15, v47\n" \
  "v_mov_b32 v16, v48\n" \
  "v_mov_b32 v17, v52\n" \
  "v_mov_b32 v18, v49\n" \
  "v_mov_b32 v19, v53\n" \
  "v_mov_b32 v20, v50\n" \
  "v_mov_b32 v21, v54\n" \
  "v_mov_b32 v22, v51\n" \
  "v_mov_b32 v23, v55\n" \
  "v_mov_b32 v24, v56\n" \
  "v_mov_b32 v25, v60\n" \
  "v_mov_b32 v26, v57\n" \
  "v_mov_b32 v27, v61\n" \
  "v_mov_b32 v28, v58\n" \
  "v_mov_b32 v29, v62\n" \
  "v_mov_b32 v30, v59\n" \
  "v_mov_b32 v31, v63\n" \
  "s_waitcnt vmcnt(0)\n" \
  "s_nop 1\n" \
  "v_smfmac_f32_16x16x64_bf16 v[128:131], v[64:67], v[0:7], v200\n" \
  "v_smfmac_f32_16x16x64_bf16 v[132:135], v[68:71], v[8:15], v200\n" \
  "v_smfmac_f32_16x16x64_bf16 v[136:139], v[72:75], v[16:23], v200\n" \
  "v_smfmac_f32_16x16x64_bf16 v[140:143], v[76:79], v[24:31], v200\n" \
  "v_smfmac_f32_16x16x64_bf16 v[144:147], v[80:83], v[0:7], v200\n" \
  "v_smfmac_f32_16x16x64_bf16 v[148:151], v[84:87], v[8:15], v200\n" \
  "v_smfmac_f32_16x16x64_bf16 v[152:155], v[88:91], v[16:23], v200\n" \
  "v_smfmac_f32_16x16x64_bf16 v[156:159], v[92:95], v[24:31], v200\n" \
  "v_smfmac_f32_16x16x64_bf16 v[160:163], v[96:99], v[0:7], v200\n" \
  "v_smfmac_f32_16x16x64_bf16 v[164:167], v[100:103], v[8:15], v200\n" \
  "v_smfmac_f32_16x16x64_bf16 v[168:171], v[104:107], v[16:23], v200\n" \
  "v_smfmac_f32_16x16x64_bf16 v[172:175], v[108:111], v[24:31], v200\n" \
  "v_smfmac_f32_16x16x64_bf16 v[176:179], v[112:115], v[0:7], v200\n" \
  "v_smfmac_f32_16x16x64_bf16 v[180:183], v[116:119], v[8:15], v200\n" \
  "v_smfmac_f32_16x16x64_bf16 v[184:187], v[120:123], v[16:23], v200\n" \
  "v_smfmac_f32_16x16x64_bf16 v[188:191], v[124:127], v[24:31], v200\n" \
  "s_nop 7\n" \
  "s_cmp_eq_u32 s48, 1\n" \
  "s_cbranch_scc1 .LF_w1_%=\n" \
  "s_cmp_eq_u32 s48, 2\n" \
  "s_cbranch_scc1 .LF_w2_%=\n" \
  "s_cmp_eq_u32 s48, 3\n" \
  "s_cbranch_scc1 .LF_w3_%=\n" \
  "v_accvgpr_write_b32 a0, v128\n" \
  "v_accvgpr_write_b32 a1, v129\n" \
  "v_accvgpr_write_b32 a2, v130\n" \
  "v_accvgpr_write_b32 a3, v131\n" \
  "v_accvgpr_write_b32 a4, v132\n" \
  "v_accvgpr_write_b32 a5, v133\n" \
  "v_accvgpr_write_b32 a6, v134\n" \
  "v_accvgpr_write_b32 a7, v135\n" \
  "v_accvgpr_write_b32 a8, v136\n" \
  "v_accvgpr_write_b32 a9, v137\n" \
  "v_accvgpr_write_b32 a10, v138\n" \
  "v_accvgpr_write_b32 a11, v139\n" \
  "v_accvgpr_write_b32 a12, v140\n" \
  "v_accvgpr_write_b32 a13, v141\n" \
  "v_accvgpr_write_b32 a14, v142\n" \
  "v_accvgpr_write_b32 a15, v143\n" \
  "v_accvgpr_write_b32 a16, v144\n" \
  "v_accvgpr_write_b32 a17, v145\n" \
  "v_accvgpr_write_b32 a18, v146\n" \
  "v_accvgpr_write_b32 a19, v147\n" \
  "v_accvgpr_write_b32 a20, v148\n" \
  "v_accvgpr_write_b32 a21, v149\n" \
  "v_accvgpr_write_b32 a22, v150\n" \
  "v_accvgpr_write_b32 a23, v151\n" \
  "v_accvgpr_write_b32 a24, v152\n" \
  "v_accvgpr_write_b32 a25, v153\n" \
  "v_accvgpr_write_b32 a26, v154\n" \
  "v_accvgpr_write_b32 a27, v155\n" \
  "v_accvgpr_write_b32 a28, v156\n" \
  "v_accvgpr_write_b32 a29, v157\n" \
  "v_accvgpr_write_b32 a30, v158\n" \
  "v_accvgpr_write_b32 a31, v159\n" \
  "v_accvgpr_write_b32 a32, v160\n" \
  "v_accvgpr_write_b32 a33, v161\n" \
  "v_accvgpr_write_b32 a34, v162\n" \
  "v_accvgpr_write_b32 a35, v163\n" \
  "v_accvgpr_write_b32 a36, v164\n" \
  "v_accvgpr_write_b32 a37, v165\n" \
  "v_accvgpr_write_b32 a38, v166\n" \
  "v_accvgpr_write_b32 a39, v167\n" \
  "v_accvgpr_write_b32 a40, v168\n" \
  "v_accvgpr_write_b32 a41, v169\n" \
  "v_accvgpr_write_b32 a42, v170\n" \
  "v_accvgpr_write_b32 a43, v171\n" \
  "v_accvgpr_write_b32 a44, v172\n" \
  "v_accvgpr_write_b32 a45, v173\n" \
  "v_accvgpr_write_b32 a46, v174\n" \
  "v_accvgpr_write_b32 a47, v175\n" \
  "v_accvgpr_write_b32 a48, v176\n" \
  "v_accvgpr_write_b32 a49, v177\n" \
  "v_accvgpr_write_b32 a50, v178\n" \
  "v_accvgpr_write_b32 a51, v179\n" \
  "v_accvgpr_write_b32 a52, v180\n" \
  "v_accvgpr_write_b32 a53, v181\n" \
  "v_accvgpr_write_b32 a54, v182\n" \
  "v_accvgpr_write_b32 a55, v183\n" \
  "v_accvgpr_write_b32 a56, v184\n" \
  "v_accvgpr_write_b32 a57, v185\n" \
  "v_accvgpr_write_b32 a58, v186\n" \
  "v_accvgpr_write_b32 a59, v187\n" \
  "v_accvgpr_write_b32 a60, v188\n" \
  "v_accvgpr_write_b32 a61, v189\n" \
  "v_accvgpr_write_b32 a62, v190\n" \
  "v_accvgpr_write_b32 a63, v191\n" \
  ".LF_w_b_%=:\n" \
  "s_add_u32 s48, s48, 1\n" \
  "s_cmp_lt_u32 s48, 4\n" \
  "s_cbranch_scc1 .LF_pre_%=\n" \
  "s_mov_b32 s48, 0\n" \
  "s_mov_b32 s49, %28\n" \
  "s_mov_b32 s50, %29\n" \
  "s_mov_b32 s51, %27\n" \
  "s_mov_b32 s56, %30\n" \
  "s_mov_b32 s57, 1052672\n" \
  "s_mov_b32 s52, 1\n" \
  ".LF_loop_%=:\n" \
  "s_cmp_eq_u32 s48, 1\n" \
  "s_cbranch_scc1 .LF_r1_%=\n" \
  "s_cmp_eq_u32 s48, 2\n" \
  "s_cbranch_scc1 .LF_r2_%=\n" \
  "s_cmp_eq_u32 s48, 3\n" \
  "s_cbranch_scc1 .LF_r3_%=\n" \
  "v_accvgpr_read_b32 v128, a0\n" \
  "v_accvgpr_read_b32 v129, a1\n" \
  "v_accvgpr_read_b32 v130, a2\n" \
  "v_accvgpr_read_b32 v131, a3\n" \
  "v_accvgpr_read_b32 v132, a4\n" \
  "v_accvgpr_read_b32 v133, a5\n" \
  "v_accvgpr_read_b32 v134, a6\n" \
  "v_accvgpr_read_b32 v135, a7\n" \
  "v_accvgpr_read_b32 v136, a8\n" \
  "v_accvgpr_read_b32 v137, a9\n" \
  "v_accvgpr_read_b32 v138, a10\n" \
  "v_accvgpr_read_b32 v139, a11\n" \
  "v_accvgpr_read_b32 v140, a12\n" \
  "v_accvgpr_read_b32 v141, a13\n" \
  "v_accvgpr_read_b32 v142, a14\n" \
  "v_accvgpr_read_b32 v143, a15\n" \
  "v_accvgpr_read_b32 v144, a16\n" \
  "v_accvgpr_read_b32 v145, a17\n" \
  "v_accvgpr_read_b32 v146, a18\n" \
  "v_accvgpr_read_b32 v147, a19\n" \
  "v_accvgpr_read_b32 v148, a20\n" \
  "v_accvgpr_read_b32 v149, a21\n" \
  "v_accvgpr_read_b32 v150, a22\n" \
  "v_accvgpr_read_b32 v151, a23\n" \
  "v_accvgpr_read_b32 v152, a24\n" \
  "v_accvgpr_read_b32 v153, a25\n" \
  "v_accvgpr_read_b32 v154, a26\n" \
  "v_accvgpr_read_b32 v155, a27\n" \
  "v_accvgpr_read_b32 v156, a28\n" \
  "v_accvgpr_read_b32 v157, a29\n" \
  "v_accvgpr_read_b32 v158, a30\n" \
  "v_accvgpr_read_b32 v159, a31\n" \
  "v_accvgpr_read_b32 v160, a32\n" \
  "v_accvgpr_read_b32 v161, a33\n" \
  "v_accvgpr_read_b32 v162, a34\n" \
  "v_accvgpr_read_b32 v163, a35\n" \
  "v_accvgpr_read_b32 v164, a36\n" \
  "v_accvgpr_read_b32 v165, a37\n" \
  "v_accvgpr_read_b32 v166, a38\n" \
  "v_accvgpr_read_b32 v167, a39\n" \
  "v_accvgpr_read_b32 v168, a40\n" \
  "v_accvgpr_read_b32 v169, a41\n" \
  "v_accvgpr_read_b32 v170, a42\n" \
  "v_accvgpr_read_b32 v171, a43\n" \
  "v_accvgpr_read_b32 v172, a44\n" \
  "v_accvgpr_read_b32 v173, a45\n" \
  "v_accvgpr_read_b32 v174, a46\n" \
  "v_accvgpr_read_b32 v175, a47\n" \
  "v_accvgpr_read_b32 v176, a48\n" \
  "v_accvgpr_read_b32 v177, a49\n" \
  "v_accvgpr_read_b32 v178, a50\n" \
  "v_accvgpr_read_b32 v179, a51\n" \
  "v_accvgpr_read_b32 v180, a52\n" \
  "v_accvgpr_read_b32 v181, a53\n" \
  "v_accvgpr_read_b32 v182, a54\n" \
  "v_accvgpr_read_b32 v183, a55\n" \
  "v_accvgpr_read_b32 v184, a56\n" \
  "v_accvgpr_read_b32 v185, a57\n" \
  "v_accvgpr_read_b32 v186, a58\n" \
  "v_accvgpr_read_b32 v187, a59\n" \
  "v_accvgpr_read_b32 v188, a60\n" \
  "v_accvgpr_read_b32 v189, a61\n" \
  "v_accvgpr_read_b32 v190, a62\n" \
  "v_accvgpr_read_b32 v191, a63\n" \
  "v_mov_b32 v192, v210\n" \
  "v_mov_b32 v193, v211\n" \
  "v_mov_b32 v194, v212\n" \
  ".LF_r_b_%=:\n" \
  "v_mov_b64 v[40:41], 0\n" \
  "v_mov_b64 v[42:43], 0\n" \
  "v_mov_b64 v[44:45], 0\n" \
  "v_mov_b64 v[46:47], 0\n" \
  "v_mov_b64 v[48:49], 0\n" \
  "v_mov_b64 v[50:51], 0\n" \
  "v_mov_b64 v[52:53], 0\n" \
  "v_mov_b64 v[54:55], 0\n" \
  "s_cmp_lg_u32 %33, 0\n" \
  "s_cbranch_scc1 .LF_go_%=\n" \
  "s_mov_b32 s69, 0\n" \
  "s_add_u32 s53, s48, 60\n" \
  "s_cmp_eq_u32 s48, 3\n" \
  "s_cselect_b32 s54, 16, 0\n" \
  "s_lshr_b32 s54, %32, s54\n" \
  "s_and_b32 s54, s54, 0xffff\n" \
  ".LF_tw_%=:\n" \
  "buffer_load_dwordx2 v[60:61], off, s[40:43], s50 offset:256 sc1\n" \
  "s_waitcnt vmcnt(0)\n" \
  "s_memrealtime s[76:77]\n" \
  "v_readfirstlane_b32 s60, v60\n" \
  "v_readfirstlane_b32 s61, v61\n" \
  "s_and_b32 s62, s60, 255\n" \
  "s_lshr_b32 s60, s60, 8\n" \
  "s_add_u32 s61, s61, s60\n" \
  "s_add_u32 s55, s61, s54\n" \
  "s_waitcnt lgkmcnt(0)\n" \
  "s_sub_u32 s60, s55, s76\n" \
  "s_cmp_gt_u32 s62, s53\n" \
  "s_cbranch_scc1 .LF_go_%=\n" \
  "s_cmp_eq_u32 s62, s53\n" \
  "s_cselect_b32 s60, s60, 999\n" \
  "s_cmp_lt_i32 s60, 90\n" \
  "s_cbranch_scc1 .LF_clk_%=\n" \
  "s_sleep 14\n" \
  "s_add_u32 s69, s69, 1\n" \
  "s_cmp_lt_u32 s69, 20000\n" \
  "s_cbranch_scc1 .LF_tw_%=\n" \
  "s_branch .LF_go_%=\n" \
  ".LF_clk_%=:\n" \
  "s_sleep 1\n" \
  "s_memrealtime s[76:77]\n" \
  "s_waitcnt lgkmcnt(0)\n" \
  "s_sub_u32 s60, s55, s76\n" \
  "s_cmp_gt_i32 s60, 3\n" \
  "s_cbranch_scc1 .LF_clk_%=\n" \
  ".LF_go_%=:\n" \
  "s_barrier\n" \
  "s_mov_b32 s69, 0\n" \
  ".LF_rd_%=:\n" \
  "buffer_load_dwordx4 v[0:3], v201, s[40:43], s49 offen sc1\n" \
  "buffer_load_dwordx4 v[4:7], v201, s[40:43], s49 offen offset:1024 sc1\n" \
  "s_mov_b32 s60, s51\n" \
  "buffer_load_dwordx4 v[8:11], v203, s[40:43], s60 offen sc1\n" \
  "buffer_load_dwordx2 v[24:25], v204, s[40:43], s60 offen sc1\n" \
  "s_add_u32 s60, s60, 16384\n" \
  "buffer_load_dwordx4 v[12:15], v203, s[40:43], s60 offen sc1\n" \
  "buffer_load_dwordx2 v[26:27], v204, s[40:43], s60 offen sc1\n" \
  "s_add_u32 s60, s60, 16384\n" \
  "buffer_load_dwordx4 v[16:19], v203, s[40:43], s60 offen sc1\n" \
  "buffer_load_dwordx2 v[28:29], v204, s[40:43], s60 offen sc1\n" \
  "s_add_u32 s60, s60, 16384\n" \
  "buffer_load_dwordx4 v[20:23], v203, s[40:43], s60 offen sc1\n" \
  "buffer_load_dwordx2 v[30:31], v204, s[40:43], s60 offen sc1\n" \
  "s_waitcnt vmcnt(8)\n" \
  "v_permlane16_swap_b32 v0, v1\n" \
  "v_permlane16_swap_b32 v2, v3\n" \
  "v_permlane16_swap_b32 v4, v5\n" \
  "v_permlane16_swap_b32 v6, v7\n" \
  "s_mov_b64 s[78:79], 0\n" \
  "s_waitcnt vmcnt(6)\n" \
  "v_smfmac_f32_16x16x64_bf16 v[40:43], v[8:11], v[0:7], v200\n" \
  "v_mul_f32_dpp v56, v24, v128 row_newbcast:0 row_mask:0xf bank_mask:0xf\n" \
  "v_mul_f32_dpp v57, v24, v129 row_newbcast:1 row_mask:0xf bank_mask:0xf\n" \
  "v_mul_f32_dpp v58, v24, v130 row_newbcast:2 row_mask:0xf bank_mask:0xf\n" \
  "v_mul_f32_dpp v59, v24, v131 row_newbcast:3 row_mask:0xf bank_mask:0xf\n" \
  "v_fmac_f32_dpp v56, v24, v132 row_newbcast:4 row_mask:0xf bank_mask:0xf\n" \
  "v_fmac_f32_dpp v57, v24, v133 row_newbcast:5 row_mask:0xf bank_mask:0xf\n" \
  "v_fmac_f32_dpp v58, v24, v134 row_newbcast:6 row_mask:0xf bank_mask:0xf\n" \
  "v_fmac_f32_dpp v59, v24, v135 row_newbcast:7 row_mask:0xf bank_mask:0xf\n" \
  "v_fmac_f32_dpp v40, v24, v136 row_newbcast:8 row_mask:0xf bank_mask:0xf\n" \
  "v_fmac_f32_dpp v41, v24, v137 row_newbcast:9 row_mask:0xf bank_mask:0xf\n" \
  "v_fmac_f32_dpp v42, v24, v138 row_newbcast:10 row_mask:0xf bank_mask:0xf\n" \
  "v_fmac_f32_dpp v43, v24, v139 row_newbcast:11 row_mask:0xf bank_mask:0xf\n" \
  "v_fmac_f32_dpp v40, v24, v140 row_newbcast:12 row_mask:0xf bank_mask:0xf\n" \
  "v_fmac_f32_dpp v41, v24, v141 row_newbcast:13 row_mask:0xf bank_mask:0xf\n" \
  "v_fmac_f32_dpp v42, v24, v142 row_newbcast:14 row_mask:0xf bank_mask:0xf\n" \
  "v_fmac_f32_dpp v43, v24, v143 row_newbcast:15 row_mask:0xf bank_mask:0xf\n" \
  "v_add_f32 v40, v40, v56\n" \
  "v_add_f32 v42, v42, v58\n" \
  "v_add_f32 v41, v41, v57\n" \
  "v_add_f32 v43, v43, v59\n" \
  "v_permlane32_swap_b32 v40, v42\n" \
  "v_add_f32 v40, v40, v42\n" \
  "v_permlane32_swap_b32 v41, v43\n" \
  "v_add_f32 v41, v41, v43\n" \
  "v_cmp_u_f32 vcc, v40, v41\n" \
  "ds_write2st64_b32 v205, v40, v41 offset0:0 offset1:4\n" \
  "s_or_b64 s[78:79], s[78:79], vcc\n" \
  "s_waitcnt vmcnt(4)\n" \
  "v_smfmac_f32_16x16x64_bf16 v[44:47], v[12:15], v[0:7], v200\n" \
  "v_mul_f32_dpp v56, v26, v144 row_newbcast:0 row_mask:0xf bank_mask:0xf\n" \
  "v_mul_f32_dpp v57, v26, v145 row_newbcast:1 row_mask:0xf bank_mask:0xf\n" \
  "v_mul_f32_dpp v58, v26, v146 row_newbcast:2 row_mask:0xf bank_mask:0xf\n" \
  "v_mul_f32_dpp v59, v26, v147 row_newbcast:3 row_mask:0xf bank_mask:0xf\n" \
  "v_fmac_f32_dpp v56, v26, v148 row_newbcast:4 row_mask:0xf bank_mask:0xf\n" \
  "v_fmac_f32_dpp v57, v26, v149 row_newbcast:5 row_mask:0xf bank_mask:0xf\n" \
  "v_fmac_f32_dpp v58, v26, v150 row_newbcast:6 row_mask:0xf bank_mask:0xf\n" \
  "v_fmac_f32_dpp v59, v26, v151 row_newbcast:7 row_mask:0xf bank_mask:0xf\n" \
  "v_fmac_f32_dpp v44, v26, v152 row_newbcast:8 row_mask:0xf bank_mask:0xf\n" \
  "v_fmac_f32_dpp v45, v26, v153 row_newbcast:9 row_mask:0xf bank_mask:0xf\n" \
  "v_fmac_f32_dpp v46, v26, v154 row_newbcast:10 row_mask:0xf bank_mask:0xf\n" \
  "v_fmac_f32_dpp v47, v26, v155 row_newbcast:11 row_mask:0xf bank_mask:0xf\n" \
  "v_fmac_f32_dpp v44, v26, v156 row_newbcast:12 row_mask:0xf bank_mask:0xf\n" \
  "v_fmac_f32_dpp v45, v26, v157 row_newbcast:13 row_mask:0xf bank_mask:0xf\n" \
  "v_fmac_f32_dpp v46, v26, v158 row_newbcast:14 row_mask:0xf bank_mask:0xf\n" \
  "v_fmac_f32_dpp v47, v26, v159 row_newbcast:15 row_mask:0xf bank_mask:0xf\n" \
  "v_add_f32 v44, v44, v56\n" \
  "v_add_f32 v46, v46, v58\n" \
  "v_add_f32 v45, v45, v57\n" \
  "v_add_f32 v47, v47, v59\n" \
  "v_permlane32_swap_b32 v44, v46\n" \
  "v_add_f32 v44, v44, v46\n" \
  "v_permlane32_swap_b32 v45, v47\n" \
  "v_add_f32 v45, v45, v47\n" \
  "v_cmp_u_f32 vcc, v44, v45\n" \
  "ds_write2st64_b32 v205, v44, v45 offset0:8 offset1:12\n" \
  "s_or_b64 s[78:79], s[78:79], vcc\n" \
  "s_waitcnt vmcnt(2)\n" \
  "v_smfmac_f32_16x16x64_bf16 v[48:51], v[16:19], v[0:7], v200\n" \
  "v_mul_f32_dpp v56, v28, v160 row_newbcast:0 row_mask:0xf bank_mask:0xf\n" \
  "v_mul_f32_dpp v57, v28, v161 row_newbcast:1 row_mask:0xf bank_mask:0xf\n" \
  "v_mul_f32_dpp v58, v28, v162 row_newbcast:2 row_mask:0xf bank_mask:0xf\n" \
  "v_mul_f32_dpp v59, v28, v163 row_newbcast:3 row_mask:0xf bank_mask:0xf\n" \
  "v_fmac_f32_dpp v56, v28, v164 row_newbcast:4 row_mask:0xf bank_mask:0xf\n" \
  "v_fmac_f32_dpp v57, v28, v165 row_newbcast:5 row_mask:0xf bank_mask:0xf\n" \
  "v_fmac_f32_dpp v58, v28, v166 row_newbcast:6 row_mask:0xf bank_mask:0xf\n" \
  "v_fmac_f32_dpp v59, v28, v167 row_newbcast:7 row_mask:0xf bank_mask:0xf\n" \
  "v_fmac_f32_dpp v48, v28, v168 row_newbcast:8 row_mask:0xf bank_mask:0xf\n" \
  "v_fmac_f32_dpp v49, v28, v169 row_newbcast:9 row_mask:0xf bank_mask:0xf\n" \
  "v_fmac_f32_dpp v50, v28, v170 row_newbcast:10 row_mask:0xf bank_mask:0xf\n" \
  "v_fmac_f32_dpp v51, v28, v171 row_newbcast:11 row_mask:0xf bank_mask:0xf\n" \
  "v_fmac_f32_dpp v48, v28, v172 row_newbcast:12 row_mask:0xf bank_mask:0xf\n" \
  "v_fmac_f32_dpp v49, v28, v173 row_newbcast:13 row_mask:0xf bank_mask:0xf\n" \
  "v_fmac_f32_dpp v50, v28, v174 row_newbcast:14 row_mask:0xf bank_mask:0xf\n" \
  "v_fmac_f32_dpp v51, v28, v175 row_newbcast:15 row_mask:0xf bank_mask:0xf\n" \
  "v_add_f32 v48, v48, v56\n" \
  "v_add_f32 v50, v50, v58\n" \
  "v_add_f32 v49, v49, v57\n" \
  "v_add_f32 v51, v51, v59\n" \
  "v_permlane32_swap_b32 v48, v50\n" \
  "v_add_f32 v48, v48, v50\n" \
  "v_permlane32_swap_b32 v49, v51\n" \
  "v_add_f32 v49, v49, v51\n" \
  "v_cmp_u_f32 vcc, v48, v49\n" \
  "ds_write2st64_b32 v205, v48, v49 offset0:16 offset1:20\n" \
  "s_or_b64 s[78:79], s[78:79], vcc\n" \
  "s_waitcnt vmcnt(0)\n" \
  "v_smfmac_f32_16x16x64_bf16 v[52:55], v[20:23], v[0:7], v200\n" \
  "v_mul_f32_dpp v56, v30, v176 row_newbcast:0 row_mask:0xf bank_mask:0xf\n" \
  "v_mul_f32_dpp v57, v30, v177 row_newbcast:1 row_mask:0xf bank_mask:0xf\n" \
  "v_mul_f32_dpp v58, v30, v178 row_newbcast:2 row_mask:0xf bank_mask:0xf\n" \
  "v_mul_f32_dpp v59, v30, v179 row_newbcast:3 row_mask:0xf bank_mask:0xf\n" \
  "v_fmac_f32_dpp v56, v30, v180 row_newbcast:4 row_mask:0xf bank_mask:0xf\n" \
  "v_fmac_f32_dpp v57, v30, v181 row_newbcast:5 row_mask:0xf bank_mask:0xf\n" \
  "v_fmac_f32_dpp v58, v30, v182 row_newbcast:6 row_mask:0xf bank_mask:0xf\n" \
  "v_fmac_f32_dpp v59, v30, v183 row_newbcast:7 row_mask:0xf bank_mask:0xf\n" \
  "v_fmac_f32_dpp v52, v30, v184 row_newbcast:8 row_mask:0xf bank_mask:0xf\n" \
  "v_fmac_f32_dpp v53, v30, v185 row_newbcast:9 row_mask:0xf bank_mask:0xf\n" \
  "v_fmac_f32_dpp v54, v30, v186 row_newbcast:10 row_mask:0xf bank_mask:0xf\n" \
  "v_fmac_f32_dpp v55, v30, v187 row_newbcast:11 row_mask:0xf bank_mask:0xf\n" \
  "v_fmac_f32_dpp v52, v30, v188 row_newbcast:12 row_mask:0xf bank_mask:0xf\n" \
  "v_fmac_f32_dpp v53, v30, v189 row_newbcast:13 row_mask:0xf bank_mask:0xf\n" \
  "v_fmac_f32_dpp v54, v30, v190 row_newbcast:14 row_mask:0xf bank_mask:0xf\n" \
  "v_fmac_f32_dpp v55, v30, v191 row_newbcast:15 row_mask:0xf bank_mask:0xf\n" \
  "v_add_f32 v52, v52, v56\n" \
  "v_add_f32 v54, v54, v58\n" \
  "v_add_f32 v53, v53, v57\n" \
  "v_add_f32 v55, v55, v59\n" \
  "v_permlane32_swap_b32 v52, v54\n" \
  "v_add_f32 v52, v52, v54\n" \
  "v_permlane32_swap_b32 v53, v55\n" \
  "v_add_f32 v53, v53, v55\n" \
  "v_cmp_u_f32 vcc, v52, v53\n" \
  "ds_write2st64_b32 v205, v52, v53 offset0:24 offset1:28\n" \
  "s_or_b64 s[78:79], s[78:79], vcc\n" \
  "s_cbranch_scc1 .LF_bad_%=\n" \
  ".LF_ok_%=:\n" \
  "s_cmp_lg_u32 s52, 0\n" \
  "s_cbranch_scc1 .LF_ag_%=\n" \
  "s_waitcnt lgkmcnt(0)\n" \
  "s_barrier\n" \
  "ds_read_b128 v[40:43], v206\n" \
  "ds_read_b128 v[44:47], v206 offset:512\n" \
  "s_waitcnt lgkmcnt(1)\n" \
  "v_add_f32 v40, v40, v42\n" \
  "v_add_f32 v41, v41, v43\n" \
  "s_waitcnt lgkmcnt(0)\n" \
  "v_add_f32 v44, v44, v46\n" \
  "v_add_f32 v45, v45, v47\n" \
  "v_add_f32 v40, v40, v44\n" \
  "v_add_f32 v41, v41, v45\n" \
  "v_cvt_pk_bf16_f32 v40, v40, v41\n" \
  "v_and_b32 v41, 0xffff0000, v40\n" \
  "v_lshlrev_b32 v40, 16, v40\n" \
  "v_mul_f32 v40, v192, v40\n" \
  "v_mul_f32 v41, v192, v41\n" \
  "v_cvt_pk_bf16_f32 v40, v40, v41\n" \
  "v_and_b32 v41, 0xffff0000, v40\n" \
  "v_lshlrev_b32 v40, 16, v40\n" \
  "v_add_f32 v40, v40, v193\n" \
  "v_add_f32 v41, v41, v194\n" \
  "v_cvt_pk_bf16_f32 v123, v40, v41\n" \
  "s_cmp_eq_u32 s48, 3\n" \
  "s_cbranch_scc1 .LF_out_%=\n" \
  "v_mov_b32 v120, v121\n" \
  "v_mov_b32 v121, v122\n" \
  "v_mov_b32 v122, v123\n" \
  "v_xor_b32 v205, 8192, v205\n" \
  "v_xor_b32 v206, 8192, v206\n" \
  "s_xor_b32 s49, s49, s56\n" \
  "s_xor_b32 s50, s50, s57\n" \
  "s_mov_b32 s60, s56\n" \
  "s_mov_b32 s56, s57\n" \
  "s_mov_b32 s57, s60\n" \
  "s_add_u32 s51, s51, 65536\n" \
  "s_add_u32 s48, s48, 1\n" \
  "s_branch .LF_loop_%=\n" \
  ".LF_out_%=:\n" \
  "s_mov_b32 s60, 110592\n" \
  "buffer_store_dword v123, v207, s[44:47], s60 offen\n" \
  "s_mov_b32 s60, 73728\n" \
  "buffer_store_dword v122, v207, s[44:47], s60 offen\n" \
  "s_mov_b32 s60, 36864\n" \
  "buffer_store_dword v121, v207, s[44:47], s60 offen\n" \
  "s_mov_b32 s60, 0\n" \
  "buffer_store_dword v120, v207, s[44:47], s60 offen\n" \
  "s_branch .LF_end_%=\n" \
  ".LF_w1_%=:\n" \
  "v_accvgpr_write_b32 a64, v128\n" \
  "v_accvgpr_write_b32 a65, v129\n" \
  "v_accvgpr_write_b32 a66, v130\n" \
  "v_accvgpr_write_b32 a67, v131\n" \
  "v_accvgpr_write_b32 a68, v132\n" \
  "v_accvgpr_write_b32 a69, v133\n" \
  "v_accvgpr_write_b32 a70, v134\n" \
  "v_accvgpr_write_b32 a71, v135\n" \
  "v_accvgpr_write_b32 a72, v136\n" \
  "v_accvgpr_write_b32 a73, v137\n" \
  "v_accvgpr_write_b32 a74, v138\n" \
  "v_accvgpr_write_b32 a75, v139\n" \
  "v_accvgpr_write_b32 a76, v140\n" \
  "v_accvgpr_write_b32 a77, v141\n" \
  "v_accvgpr_write_b32 a78, v142\n" \
  "v_accvgpr_write_b32 a79, v143\n" \
  "v_accvgpr_write_b32 a80, v144\n" \
  "v_accvgpr_write_b32 a81, v145\n" \
  "v_accvgpr_write_b32 a82, v146\n" \
  "v_accvgpr_write_b32 a83, v147\n" \
  "v_accvgpr_write_b32 a84, v148\n" \
  "v_accvgpr_write_b32 a85, v149\n" \
  "v_accvgpr_write_b32 a86, v150\n" \
  "v_accvgpr_write_b32 a87, v151\n" \
  "v_accvgpr_write_b32 a88, v152\n" \
  "v_accvgpr_write_b32 a89, v153\n" \
  "v_accvgpr_write_b32 a90, v154\n" \
  "v_accvgpr_write_b32 a91, v155\n" \
  "v_accvgpr_write_b32 a92, v156\n" \
  "v_accvgpr_write_b32 a93, v157\n" \
  "v_accvgpr_write_b32 a94, v158\n" \
  "v_accvgpr_write_b32 a95, v159\n" \
  "v_accvgpr_write_b32 a96, v160\n" \
  "v_accvgpr_write_b32 a97, v161\n" \
  "v_accvgpr_write_b32 a98, v162\n" \
  "v_accvgpr_write_b32 a99, v163\n" \
  "v_accvgpr_write_b32 a100, v164\n" \
  "v_accvgpr_write_b32 a101, v165\n" \
  "v_accvgpr_write_b32 a102, v166\n" \
  "v_accvgpr_write_b32 a103, v167\n" \
  "v_accvgpr_write_b32 a104, v168\n" \
  "v_accvgpr_write_b32 a105, v169\n" \
  "v_accvgpr_write_b32 a106, v170\n" \
  "v_accvgpr_write_b32 a107, v171\n" \
  "v_accvgpr_write_b32 a108, v172\n" \
  "v_accvgpr_write_b32 a109, v173\n" \
  "v_accvgpr_write_b32 a110, v174\n" \
  "v_accvgpr_write_b32 a111, v175\n" \
  "v_accvgpr_write_b32 a112, v176\n" \
  "v_accvgpr_write_b32 a113, v177\n" \
  "v_accvgpr_write_b32 a114, v178\n" \
  "v_accvgpr_write_b32 a115, v179\n" \
  "v_accvgpr_write_b32 a116, v180\n" \
  "v_accvgpr_write_b32 a117, v181\n" \
  "v_accvgpr_write_b32 a118, v182\n" \
  "v_accvgpr_write_b32 a119, v183\n" \
  "v_accvgpr_write_b32 a120, v184\n" \
  "v_accvgpr_write_b32 a121, v185\n" \
  "v_accvgpr_write_b32 a122, v186\n" \
  "v_accvgpr_write_b32 a123, v187\n" \
  "v_accvgpr_write_b32 a124, v188\n" \
  "v_accvgpr_write_b32 a125, v189\n" \
  "v_accvgpr_write_b32 a126, v190\n" \
  "v_accvgpr_write_b32 a127, v191\n" \
  "s_branch .LF_w_b_%=\n" \
  ".LF_w2_%=:\n" \
  "v_accvgpr_write_b32 a128, v128\n" \
  "v_accvgpr_write_b32 a129, v129\n" \
  "v_accvgpr_write_b32 a130, v130\n" \
  "v_accvgpr_write_b32 a131, v131\n" \
  "v_accvgpr_write_b32 a132, v132\n" \
  "v_accvgpr_write_b32 a133, v133\n" \
  "v_accvgpr_write_b32 a134, v134\n" \
  "v_accvgpr_write_b32 a135, v135\n" \
  "v_accvgpr_write_b32 a136, v136\n" \
  "v_accvgpr_write_b32 a137, v137\n" \
  "v_accvgpr_write_b32 a138, v138\n" \
  "v_accvgpr_write_b32 a139, v139\n" \
  "v_accvgpr_write_b32 a140, v140\n" \
  "v_accvgpr_write_b32 a141, v141\n" \
  "v_accvgpr_write_b32 a142, v142\n" \
  "v_accvgpr_write_b32 a143, v143\n" \
  "v_accvgpr_write_b32 a144, v144\n" \
  "v_accvgpr_write_b32 a145, v145\n" \
  "v_accvgpr_write_b32 a146, v146\n" \
  "v_accvgpr_write_b32 a147, v147\n" \
  "v_accvgpr_write_b32 a148, v148\n" \
  "v_accvgpr_write_b32 a149, v149\n" \
  "v_accvgpr_write_b32 a150, v150\n" \
  "v_accvgpr_write_b32 a151, v151\n" \
  "v_accvgpr_write_b32 a152, v152\n" \
  "v_accvgpr_write_b32 a153, v153\n" \
  "v_accvgpr_write_b32 a154, v154\n" \
  "v_accvgpr_write_b32 a155, v155\n" \
  "v_accvgpr_write_b32 a156, v156\n" \
  "v_accvgpr_write_b32 a157, v157\n" \
  "v_accvgpr_write_b32 a158, v158\n" \
  "v_accvgpr_write_b32 a159, v159\n" \
  "v_accvgpr_write_b32 a160, v160\n" \
  "v_accvgpr_write_b32 a161, v161\n" \
  "v_accvgpr_write_b32 a162, v162\n" \
  "v_accvgpr_write_b32 a163, v163\n" \
  "v_accvgpr_write_b32 a164, v164\n" \
  "v_accvgpr_write_b32 a165, v165\n" \
  "v_accvgpr_write_b32 a166, v166\n" \
  "v_accvgpr_write_b32 a167, v167\n" \
  "v_accvgpr_write_b32 a168, v168\n" \
  "v_accvgpr_write_b32 a169, v169\n" \
  "v_accvgpr_write_b32 a170, v170\n" \
  "v_accvgpr_write_b32 a171, v171\n" \
  "v_accvgpr_write_b32 a172, v172\n" \
  "v_accvgpr_write_b32 a173, v173\n" \
  "v_accvgpr_write_b32 a174, v174\n" \
  "v_accvgpr_write_b32 a175, v175\n" \
  "v_accvgpr_write_b32 a176, v176\n" \
  "v_accvgpr_write_b32 a177, v177\n" \
  "v_accvgpr_write_b32 a178, v178\n" \
  "v_accvgpr_write_b32 a179, v179\n" \
  "v_accvgpr_write_b32 a180, v180\n" \
  "v_accvgpr_write_b32 a181, v181\n" \
  "v_accvgpr_write_b32 a182, v182\n" \
  "v_accvgpr_write_b32 a183, v183\n" \
  "v_accvgpr_write_b32 a184, v184\n" \
  "v_accvgpr_write_b32 a185, v185\n" \
  "v_accvgpr_write_b32 a186, v186\n" \
  "v_accvgpr_write_b32 a187, v187\n" \
  "v_accvgpr_write_b32 a188, v188\n" \
  "v_accvgpr_write_b32 a189, v189\n" \
  "v_accvgpr_write_b32 a190, v190\n" \
  "v_accvgpr_write_b32 a191, v191\n" \
  "s_branch .LF_w_b_%=\n" \
  ".LF_w3_%=:\n" \
  "v_accvgpr_write_b32 a192, v128\n" \
  "v_accvgpr_write_b32 a193, v129\n" \
  "v_accvgpr_write_b32 a194, v130\n" \
  "v_accvgpr_write_b32 a195, v131\n" \
  "v_accvgpr_write_b32 a196, v132\n" \
  "v_accvgpr_write_b32 a197, v133\n" \
  "v_accvgpr_write_b32 a198, v134\n" \
  "v_accvgpr_write_b32 a199, v135\n" \
  "v_accvgpr_write_b32 a200, v136\n" \
  "v_accvgpr_write_b32 a201, v137\n" \
  "v_accvgpr_write_b32 a202, v138\n" \
  "v_accvgpr_write_b32 a203, v139\n" \
  "v_accvgpr_write_b32 a204, v140\n" \
  "v_accvgpr_write_b32 a205, v141\n" \
  "v_accvgpr_write_b32 a206, v142\n" \
  "v_accvgpr_write_b32 a207, v143\n" \
  "v_accvgpr_write_b32 a208, v144\n" \
  "v_accvgpr_write_b32 a209, v145\n" \
  "v_accvgpr_write_b32 a210, v146\n" \
  "v_accvgpr_write_b32 a211, v147\n" \
  "v_accvgpr_write_b32 a212, v148\n" \
  "v_accvgpr_write_b32 a213, v149\n" \
  "v_accvgpr_write_b32 a214, v150\n" \
  "v_accvgpr_write_b32 a215, v151\n" \
  "v_accvgpr_write_b32 a216, v152\n" \
  "v_accvgpr_write_b32 a217, v153\n" \
  "v_accvgpr_write_b32 a218, v154\n" \
  "v_accvgpr_write_b32 a219, v155\n" \
  "v_accvgpr_write_b32 a220, v156\n" \
  "v_accvgpr_write_b32 a221, v157\n" \
  "v_accvgpr_write_b32 a222, v158\n" \
  "v_accvgpr_write_b32 a223, v159\n" \
  "v_accvgpr_write_b32 a224, v160\n" \
  "v_accvgpr_write_b32 a225, v161\n" \
  "v_accvgpr_write_b32 a226, v162\n" \
  "v_accvgpr_write_b32 a227, v163\n" \
  "v_accvgpr_write_b32 a228, v164\n" \
  "v_accvgpr_write_b32 a229, v165\n" \
  "v_accvgpr_write_b32 a230, v166\n" \
  "v_accvgpr_write_b32 a231, v167\n" \
  "v_accvgpr_write_b32 a232, v168\n" \
  "v_accvgpr_write_b32 a233, v169\n" \
  "v_accvgpr_write_b32 a234, v170\n" \
  "v_accvgpr_write_b32 a235, v171\n" \
  "v_accvgpr_write_b32 a236, v172\n" \
  "v_accvgpr_write_b32 a237, v173\n" \
  "v_accvgpr_write_b32 a238, v174\n" \
  "v_accvgpr_write_b32 a239, v175\n" \
  "v_accvgpr_write_b32 a240, v176\n" \
  "v_accvgpr_write_b32 a241, v177\n" \
  "v_accvgpr_write_b32 a242, v178\n" \
  "v_accvgpr_write_b32 a243, v179\n" \
  "v_accvgpr_write_b32 a244, v180\n" \
  "v_accvgpr_write_b32 a245, v181\n" \
  "v_accvgpr_write_b32 a246, v182\n" \
  "v_accvgpr_write_b32 a247, v183\n" \
  "v_accvgpr_write_b32 a248, v184\n" \
  "v_accvgpr_write_b32 a249, v185\n" \
  "v_accvgpr_write_b32 a250, v186\n" \
  "v_accvgpr_write_b32 a251, v187\n" \
  "v_accvgpr_write_b32 a252, v188\n" \
  "v_accvgpr_write_b32 a253, v189\n" \
  "v_accvgpr_write_b32 a254, v190\n" \
  "v_accvgpr_write_b32 a255, v191\n" \
  "s_branch .LF_w_b_%=\n" \
  ".LF_r1_%=:\n" \
  "v_accvgpr_read_b32 v128, a64\n" \
  "v_accvgpr_read_b32 v129, a65\n" \
  "v_accvgpr_read_b32 v130, a66\n" \
  "v_accvgpr_read_b32 v131, a67\n" \
  "v_accvgpr_read_b32 v132, a68\n" \
  "v_accvgpr_read_b32 v133, a69\n" \
  "v_accvgpr_read_b32 v134, a70\n" \
  "v_accvgpr_read_b32 v135, a71\n" \
  "v_accvgpr_read_b32 v136, a72\n" \
  "v_accvgpr_read_b32 v137, a73\n" \
  "v_accvgpr_read_b32 v138, a74\n" \
  "v_accvgpr_read_b32 v139, a75\n" \
  "v_accvgpr_read_b32 v140, a76\n" \
  "v_accvgpr_read_b32 v141, a77\n" \
  "v_accvgpr_read_b32 v142, a78\n" \
  "v_accvgpr_read_b32 v143, a79\n" \
  "v_accvgpr_read_b32 v144, a80\n" \
  "v_accvgpr_read_b32 v145, a81\n" \
  "v_accvgpr_read_b32 v146, a82\n" \
  "v_accvgpr_read_b32 v147, a83\n" \
  "v_accvgpr_read_b32 v148, a84\n" \
  "v_accvgpr_read_b32 v149, a85\n" \
  "v_accvgpr_read_b32 v150, a86\n" \
  "v_accvgpr_read_b32 v151, a87\n" \
  "v_accvgpr_read_b32 v152, a88\n" \
  "v_accvgpr_read_b32 v153, a89\n" \
  "v_accvgpr_read_b32 v154, a90\n" \
  "v_accvgpr_read_b32 v155, a91\n" \
  "v_accvgpr_read_b32 v156, a92\n" \
  "v_accvgpr_read_b32 v157, a93\n" \
  "v_accvgpr_read_b32 v158, a94\n" \
  "v_accvgpr_read_b32 v159, a95\n" \
  "v_accvgpr_read_b32 v160, a96\n" \
  "v_accvgpr_read_b32 v161, a97\n" \
  "v_accvgpr_read_b32 v162, a98\n" \
  "v_accvgpr_read_b32 v163, a99\n" \
  "v_accvgpr_read_b32 v164, a100\n" \
  "v_accvgpr_read_b32 v165, a101\n" \
  "v_accvgpr_read_b32 v166, a102\n" \
  "v_accvgpr_read_b32 v167, a103\n" \
  "v_accvgpr_read_b32 v168, a104\n" \
  "v_accvgpr_read_b32 v169, a105\n" \
  "v_accvgpr_read_b32 v170, a106\n" \
  "v_accvgpr_read_b32 v171, a107\n" \
  "v_accvgpr_read_b32 v172, a108\n" \
  "v_accvgpr_read_b32 v173, a109\n" \
  "v_accvgpr_read_b32 v174, a110\n" \
  "v_accvgpr_read_b32 v175, a111\n" \
  "v_accvgpr_read_b32 v176, a112\n" \
  "v_accvgpr_read_b32 v177, a113\n" \
  "v_accvgpr_read_b32 v178, a114\n" \
  "v_accvgpr_read_b32 v179, a115\n" \
  "v_accvgpr_read_b32 v180, a116\n" \
  "v_accvgpr_read_b32 v181, a117\n" \
  "v_accvgpr_read_b32 v182, a118\n" \
  "v_accvgpr_read_b32 v183, a119\n" \
  "v_accvgpr_read_b32 v184, a120\n" \
  "v_accvgpr_read_b32 v185, a121\n" \
  "v_accvgpr_read_b32 v186, a122\n" \
  "v_accvgpr_read_b32 v187, a123\n" \
  "v_accvgpr_read_b32 v188, a124\n" \
  "v_accvgpr_read_b32 v189, a125\n" \
  "v_accvgpr_read_b32 v190, a126\n" \
  "v_accvgpr_read_b32 v191, a127\n" \
  "v_mov_b32 v192, v213\n" \
  "v_mov_b32 v193, v214\n" \
  "v_mov_b32 v194, v215\n" \
  "s_branch .LF_r_b_%=\n" \
  ".LF_r2_%=:\n" \
  "v_accvgpr_read_b32 v128, a128\n" \
  "v_accvgpr_read_b32 v129, a129\n" \
  "v_accvgpr_read_b32 v130, a130\n" \
  "v_accvgpr_read_b32 v131, a131\n" \
  "v_accvgpr_read_b32 v132, a132\n" \
  "v_accvgpr_read_b32 v133, a133\n" \
  "v_accvgpr_read_b32 v134, a134\n" \
  "v_accvgpr_read_b32 v135, a135\n" \
  "v_accvgpr_read_b32 v136, a136\n" \
  "v_accvgpr_read_b32 v137, a137\n" \
  "v_accvgpr_read_b32 v138, a138\n" \
  "v_accvgpr_read_b32 v139, a139\n" \
  "v_accvgpr_read_b32 v140, a140\n" \
  "v_accvgpr_read_b32 v141, a141\n" \
  "v_accvgpr_read_b32 v142, a142\n" \
  "v_accvgpr_read_b32 v143, a143\n" \
  "v_accvgpr_read_b32 v144, a144\n" \
  "v_accvgpr_read_b32 v145, a145\n" \
  "v_accvgpr_read_b32 v146, a146\n" \
  "v_accvgpr_read_b32 v147, a147\n" \
  "v_accvgpr_read_b32 v148, a148\n" \
  "v_accvgpr_read_b32 v149, a149\n" \
  "v_accvgpr_read_b32 v150, a150\n" \
  "v_accvgpr_read_b32 v151, a151\n" \
  "v_accvgpr_read_b32 v152, a152\n" \
  "v_accvgpr_read_b32 v153, a153\n" \
  "v_accvgpr_read_b32 v154, a154\n" \
  "v_accvgpr_read_b32 v155, a155\n" \
  "v_accvgpr_read_b32 v156, a156\n" \
  "v_accvgpr_read_b32 v157, a157\n" \
  "v_accvgpr_read_b32 v158, a158\n" \
  "v_accvgpr_read_b32 v159, a159\n" \
  "v_accvgpr_read_b32 v160, a160\n" \
  "v_accvgpr_read_b32 v161, a161\n" \
  "v_accvgpr_read_b32 v162, a162\n" \
  "v_accvgpr_read_b32 v163, a163\n" \
  "v_accvgpr_read_b32 v164, a164\n" \
  "v_accvgpr_read_b32 v165, a165\n" \
  "v_accvgpr_read_b32 v166, a166\n" \
  "v_accvgpr_read_b32 v167, a167\n" \
  "v_accvgpr_read_b32 v168, a168\n" \
  "v_accvgpr_read_b32 v169, a169\n" \
  "v_accvgpr_read_b32 v170, a170\n" \
  "v_accvgpr_read_b32 v171, a171\n" \
  "v_accvgpr_read_b32 v172, a172\n" \
  "v_accvgpr_read_b32 v173, a173\n" \
  "v_accvgpr_read_b32 v174, a174\n" \
  "v_accvgpr_read_b32 v175, a175\n" \
  "v_accvgpr_read_b32 v176, a176\n" \
  "v_accvgpr_read_b32 v177, a177\n" \
  "v_accvgpr_read_b32 v178, a178\n" \
  "v_accvgpr_read_b32 v179, a179\n" \
  "v_accvgpr_read_b32 v180, a180\n" \
  "v_accvgpr_read_b32 v181, a181\n" \
  "v_accvgpr_read_b32 v182, a182\n" \
  "v_accvgpr_read_b32 v183, a183\n" \
  "v_accvgpr_read_b32 v184, a184\n" \
  "v_accvgpr_read_b32 v185, a185\n" \
  "v_accvgpr_read_b32 v186, a186\n" \
  "v_accvgpr_read_b32 v187, a187\n" \
  "v_accvgpr_read_b32 v188, a188\n" \
  "v_accvgpr_read_b32 v189, a189\n" \
  "v_accvgpr_read_b32 v190, a190\n" \
  "v_accvgpr_read_b32 v191, a191\n" \
  "v_mov_b32 v192, v216\n" \
  "v_mov_b32 v193, v217\n" \
  "v_mov_b32 v194, v218\n" \
  "s_branch .LF_r_b_%=\n" \
  ".LF_r3_%=:\n" \
  "v_accvgpr_read_b32 v128, a192\n" \
  "v_accvgpr_read_b32 v129, a193\n" \
  "v_accvgpr_read_b32 v130, a194\n" \
  "v_accvgpr_read_b32 v131, a195\n" \
  "v_accvgpr_read_b32 v132, a196\n" \
  "v_accvgpr_read_b32 v133, a197\n" \
  "v_accvgpr_read_b32 v134, a198\n" \
  "v_accvgpr_read_b32 v135, a199\n" \
  "v_accvgpr_read_b32 v136, a200\n" \
  "v_accvgpr_read_b32 v137, a201\n" \
  "v_accvgpr_read_b32 v138, a202\n" \
  "v_accvgpr_read_b32 v139, a203\n" \
  "v_accvgpr_read_b32 v140, a204\n" \
  "v_accvgpr_read_b32 v141, a205\n" \
  "v_accvgpr_read_b32 v142, a206\n" \
  "v_accvgpr_read_b32 v143, a207\n" \
  "v_accvgpr_read_b32 v144, a208\n" \
  "v_accvgpr_read_b32 v145, a209\n" \
  "v_accvgpr_read_b32 v146, a210\n" \
  "v_accvgpr_read_b32 v147, a211\n" \
  "v_accvgpr_read_b32 v148, a212\n" \
  "v_accvgpr_read_b32 v149, a213\n" \
  "v_accvgpr_read_b32 v150, a214\n" \
  "v_accvgpr_read_b32 v151, a215\n" \
  "v_accvgpr_read_b32 v152, a216\n" \
  "v_accvgpr_read_b32 v153, a217\n" \
  "v_accvgpr_read_b32 v154, a218\n" \
  "v_accvgpr_read_b32 v155, a219\n" \
  "v_accvgpr_read_b32 v156, a220\n" \
  "v_accvgpr_read_b32 v157, a221\n" \
  "v_accvgpr_read_b32 v158, a222\n" \
  "v_accvgpr_read_b32 v159, a223\n" \
  "v_accvgpr_read_b32 v160, a224\n" \
  "v_accvgpr_read_b32 v161, a225\n" \
  "v_accvgpr_read_b32 v162, a226\n" \
  "v_accvgpr_read_b32 v163, a227\n" \
  "v_accvgpr_read_b32 v164, a228\n" \
  "v_accvgpr_read_b32 v165, a229\n" \
  "v_accvgpr_read_b32 v166, a230\n" \
  "v_accvgpr_read_b32 v167, a231\n" \
  "v_accvgpr_read_b32 v168, a232\n" \
  "v_accvgpr_read_b32 v169, a233\n" \
  "v_accvgpr_read_b32 v170, a234\n" \
  "v_accvgpr_read_b32 v171, a235\n" \
  "v_accvgpr_read_b32 v172, a236\n" \
  "v_accvgpr_read_b32 v173, a237\n" \
  "v_accvgpr_read_b32 v174, a238\n" \
  "v_accvgpr_read_b32 v175, a239\n" \
  "v_accvgpr_read_b32 v176, a240\n" \
  "v_accvgpr_read_b32 v177, a241\n" \
  "v_accvgpr_read_b32 v178, a242\n" \
  "v_accvgpr_read_b32 v179, a243\n" \
  "v_accvgpr_read_b32 v180, a244\n" \
  "v_accvgpr_read_b32 v181, a245\n" \
  "v_accvgpr_read_b32 v182, a246\n" \
  "v_accvgpr_read_b32 v183, a247\n" \
  "v_accvgpr_read_b32 v184, a248\n" \
  "v_accvgpr_read_b32 v185, a249\n" \
  "v_accvgpr_read_b32 v186, a250\n" \
  "v_accvgpr_read_b32 v187, a251\n" \
  "v_accvgpr_read_b32 v188, a252\n" \
  "v_accvgpr_read_b32 v189, a253\n" \
  "v_accvgpr_read_b32 v190, a254\n" \
  "v_accvgpr_read_b32 v191, a255\n" \
  "v_mov_b32 v192, v219\n" \
  "v_mov_b32 v193, v220\n" \
  "v_mov_b32 v194, v221\n" \
  "s_branch .LF_r_b_%=\n" \
  ".LF_ag_%=:\n" \
  "s_mov_b32 s52, 0\n" \
  "v_mov_b64 v[40:41], 0\n" \
  "v_mov_b64 v[42:43], 0\n" \
  "v_mov_b64 v[44:45], 0\n" \
  "v_mov_b64 v[46:47], 0\n" \
  "v_mov_b64 v[48:49], 0\n" \
  "v_mov_b64 v[50:51], 0\n" \
  "v_mov_b64 v[52:53], 0\n" \
  "v_mov_b64 v[54:55], 0\n" \
  "s_branch .LF_rd_%=\n" \
  ".LF_bad_%=:\n" \
  "v_max_u32 v60, v0, v1\n" \
  "v_max3_u32 v60, v60, v2, v3\n" \
  "v_max3_u32 v60, v60, v4, v5\n" \
  "v_max3_u32 v60, v60, v6, v7\n" \
  "v_max3_u32 v60, v60, v8, v9\n" \
  "v_max3_u32 v60, v60, v10, v11\n" \
  "v_max3_u32 v60, v60, v12, v13\n" \
  "v_max3_u32 v60, v60, v14, v15\n" \
  "v_max3_u32 v60, v60, v16, v17\n" \
  "v_max3_u32 v60, v60, v18, v19\n" \
  "v_max3_u32 v60, v60, v20, v21\n" \
  "v_max3_u32 v60, v60, v22, v23\n" \
  "v_max3_u32 v60, v60, v24, v26\n" \
  "v_max3_u32 v60, v60, v28, v30\n" \
  "v_cmp_eq_u32 vcc, -1, v60\n" \
  "s_cbranch_vccz .LF_ok_%=\n" \
  "v_mov_b64 v[40:41], 0\n" \
  "v_mov_b64 v[42:43], 0\n" \
  "v_mov_b64 v[44:45], 0\n" \
  "v_mov_b64 v[46:47], 0\n" \
  "v_mov_b64 v[48:49], 0\n" \
  "v_mov_b64 v[50:51], 0\n" \
  "v_mov_b64 v[52:53], 0\n" \
  "v_mov_b64 v[54:55], 0\n" \
  "s_sleep 4\n" \
  "s_add_u32 s69, s69, 1\n" \
  "s_and_b32 s63, s69, 1023\n" \
  "s_cbranch_scc1 .LF_rd_%=\n" \
  "buffer_load_dwordx2 v[60:61], off, s[40:43], %31 offset:128 sc1\n" \
  "s_waitcnt vmcnt(0)\n" \
  "v_readfirstlane_b32 s63, v61\n" \
  "s_cmp_eq_u32 s63, %24\n" \
  "s_cbranch_scc1 .LF_ok_%=\n" \
  "s_cmp_le_u32 s69, 2097152\n" \
  "s_cbranch_scc1 .LF_rd_%=\n" \
  "v_mov_b32 v60, 1\n" \
  "v_mov_b32 v61, %24\n" \
  "buffer_store_dwordx2 v[60:61], off, s[40:43], %31 offset:128 sc1\n" \
  "s_branch .LF_ok_%=\n" \
  ".LF_end_%=:\n"
#define F_ASM_VOPS "+{v200}"(fi.sidx), "+{v201}"(fi.l16), "+{v202}"(fi.ae), "+{v203}"(fi.am), "+{v204}"(fi.ac), "+{v205}"(fi.lw), "+{v206}"(fi.lr), "+{v207}"(fi.oo), "+{v208}"(fi.d0), "+{v209}"(fi.fu), "+{v210}"(fi.g[0]), "+{v211}"(fi.b[0]), "+{v212}"(fi.c[0]), "+{v213}"(fi.g[1]), "+{v214}"(fi.b[1]), "+{v215}"(fi.c[1]), "+{v216}"(fi.g[2]), "+{v217}"(fi.b[2]), "+{v218}"(fi.c[2]), "+{v219}"(fi.g[3]), "+{v220}"(fi.b[3]), "+{v221}"(fi.c[3])
#define F_ASM_SOPS "s"(fi.ws), "s"(fi.out), "s"(fi.tag), "s"(fi.o_u), "s"(fi.o_e), "s"(fi.o_x), "s"(fi.o_va), "s"(fi.o_tw), "s"(fi.xa), "s"(fi.o_ctl), "s"(fi.pa), "s"(fi.w)
#define F_ASM_CLOB "v0", "v1", "v2", "v3", "v4", "v5", "v6", "v7", "v8", "v9", "v10", "v11", "v12", "v13", "v14", "v15", "v16", "v17", "v18", "v19", "v20", "v21", "v22", "v23", "v24", "v25", "v26", "v27", "v28", "v29", "v30", "v31", "v32", "v33", "v34", "v35", "v36", "v37", "v38", "v39", "v40", "v41", "v42", "v43", "v44", "v45", "v46", "v47", "v48", "v49", "v50", "v51", "v52", "v53", "v54", "v55", "v56", "v57", "v58", "v59", "v60", "v61", "v62", "v63", "v64", "v65", "v66", "v67", "v68", "v69", "v70", "v71", "v72", "v73", "v74", "v75", "v76", "v77", "v78", "v79", "v80", "v81", "v82", "v83", "v84", "v85", "v86", "v87", "v88", "v89", "v90", "v91", "v92", "v93", "v94", "v95", "v96", "v97", "v98", "v99", "v100", "v101", "v102", "v103", "v104", "v105", "v106", "v107", "v108", "v109", "v110", "v111", "v112", "v113", "v114", "v115", "v116", "v117", "v118", "v119", "v120", "v121", "v122", "v123", "v124", "v125", "v126", "v127", "v128", "v129", "v130", "v131", "v132", "v133", "v134", "v135", "v136", "v137", "v138", "v139", "v140", "v141", "v142", "v143", "v144", "v145", "v146", "v147", "v148", "v149", "v150", "v151", "v152", "v153", "v154", "v155", "v156", "v157", "v158", "v159", "v160", "v161", "v162", "v163", "v164", "v165", "v166", "v167", "v168", "v169", "v170", "v171", "v172", "v173", "v174", "v175", "v176", "v177", "v178", "v179", "v180", "v181", "v182", "v183", "v184", "v185", "v186", "v187", "v188", "v189", "v190", "v191", "v192", "v193", "v194", "v195", "a0", "a1", "a2", "a3", "a4", "a5", "a6", "a7", "a8", "a9", "a10", "a11", "a12", "a13", "a14", "a15", "a16", "a17", "a18", "a19", "a20", "a21", "a22", "a23", "a24", "a25", "a26", "a27", "a28", "a29", "a30", "a31", "a32", "a33", "a34", "a35", "a36", "a37", "a38", "a39", "a40", "a41", "a42", "a43", "a44", "a45", "a46", "a47", "a48", "a49", "a50", "a51", "a52", "a53", "a54", "a55", "a56", "a57", "a58", "a59", "a60", "a61", "a62", "a63", "a64", "a65", "a66", "a67", "a68", "a69", "a70", "a71", "a72", "a73", "a74", "a75", "a76", "a77", "a78", "a79", "a80", "a81", "a82", "a83", "a84", "a85", "a86", "a87", "a88", "a89", "a90", "a91", "a92", "a93", "a94", "a95", "a96", "a97", "a98", "a99", "a100", "a101", "a102", "a103", "a104", "a105", "a106", "a107", "a108", "a109", "a110", "a111", "a112", "a113", "a114", "a115", "a116", "a117", "a118", "a119", "a120", "a121", "a122", "a123", "a124", "a125", "a126", "a127", "a128", "a129", "a130", "a131", "a132", "a133", "a134", "a135", "a136", "a137", "a138", "a139", "a140", "a141", "a142", "a143", "a144", "a145", "a146", "a147", "a148", "a149", "a150", "a151", "a152", "a153", "a154", "a155", "a156", "a157", "a158", "a159", "a160", "a161", "a162", "a163", "a164", "a165", "a166", "a167", "a168", "a169", "a170", "a171", "a172", "a173", "a174", "a175", "a176", "a177", "a178", "a179", "a180", "a181", "a182", "a183", "a184", "a185", "a186", "a187", "a188", "a189", "a190", "a191", "a192", "a193", "a194", "a195", "a196", "a197", "a198", "a199", "a200", "a201", "a202", "a203", "a204", "a205", "a206", "a207", "a208", "a209", "a210", "a211", "a212", "a213", "a214", "a215", "a216", "a217", "a218", "a219", "a220", "a221", "a222", "a223", "a224", "a225", "a226", "a227", "a228", "a229", "a230", "a231", "a232", "a233", "a234", "a235", "a236", "a237", "a238", "a239", "a240", "a241", "a242", "a243", "a244", "a245", "a246", "a247", "a248", "a249", "a250", "a251", "a252", "a253", "a254", "a255", "s40", "s41", "s42", "s43", "s44", "s45", "s46", "s47", "s48", "s49", "s50", "s51", "s52", "s53", "s54", "s55", "s56", "s57", "s58", "s59", "s60", "s61", "s62", "s63", "s64", "s65", "s66", "s67", "s68", "s69", "s70", "s71", "s72", "s73", "s74", "s75", "s76", "s77", "s78", "s79", "s80", "s81", "s82", "s83", "s84", "s85", "s86", "s87", "s88", "s89", "s90", "s91", "s92", "s93", "s94", "s95", "s96", "s97", "s98", "s99", "vcc", "scc", "memory"

struct RedIn {
  unsigned sidx, cidx, l16, ax, am, mo, sel, to, fu, p0, p1, p2, d0, d1, d2, d3, d4;
  const void* ws;
  unsigned w, go, tag, o_va, o_vb, lab, o_u, kind, o_ctl, pa, pb, pc, f_d, f_r, ph;
};
__device__ __forceinline__ void red_role(const Params& p, int w, int T, char* smem) {
  const int grp = T & 3;
  CODE_PAD(PAD_RED);
  char* const ws = p.ws;
  Ctx cx = {ws, 0u};
  const int l = lane_id(), n = l & 15, g = l >> 4;
  comm_init(cx, grp * 9 + (T >> 2), w, smem);
  Bulk bk;
  bk.init(cx, grp);
  bk.pl.load(cx, grp);
  const Place& pl = bk.pl;
  char* const base = comm_base(ws) - CO;
  const unsigned dl = (unsigned)(ws - base);
  RedIn ri;
  ri.sidx = (n & 8) ? 0xEEEEu : 0x4444u;
  ri.cidx = (n & 3) * 0x5555u;
  ri.l16 = l * 16;
  ri.ax = W_PX * 32768 + (n >> 3) * 512 + g * 128 + (n & 7) * 16;
  ri.am = W_PM * 32768 + l * 16;
  const unsigned mt = (W_MEM + (T >> 4)) * 32768 + (T & 15) * 256;
  ri.mo = mt + (2 * (g & 1) + (g >> 1)) * 64 + (n >> 3) * 32 + (n & 1) * 16 + ((n >> 1) & 3) * 4;
  ri.sel = (n & 1) ? 0x03020706u : 0x05040100u;
  ri.to = l < 4 ? CO + TW_OFF + (16384u ^ pl.dyn(l)) + 256 : 0x7F000000u;
  ri.fu = CO + pl.near(N_U + min(10 * w - 10 + min(n, 11), w == 3 ? 31 : 10 * w - 1)) + (T < 32 ? (T >> 1) & 3 : T & 3) * 8 + (l & 32);
  ri.d0 = 0; ri.d1 = 0; ri.d2 = 0; ri.d3 = 0; ri.d4 = 0;
  ri.ws = base; ri.w = w; ri.go = pl.go; ri.tag = cx.tag; ri.lab = grp; ri.o_ctl = dl;
  ri.o_va = __builtin_amdgcn_readfirstlane(CO + (win_static(W_VO + 2 * (T >> 2)) ^ pl.dyn(grp)));
  ri.o_vb = __builtin_amdgcn_readfirstlane(CO + (win_static(W_VO + 2 * (T >> 2) + 1) ^ pl.dyn(grp)));
  auto dead = [&](int i) { return i < 0 ? CO + (16384u ^ pl.dyn(g)) + mt + n * 16 : (i < 4 ? ri.o_va : ri.o_vb) + (i & 3) * 1024 + l * 16; };
  ri.p0 = dead(3 * w - 4); ri.p1 = dead(3 * w - 3); ri.p2 = dead(3 * w - 2);
  ri.o_u = __builtin_amdgcn_readfirstlane(u_tile(T, 0, 0) ^ (~bk.w13 & 8192u));
  ri.kind = __builtin_amdgcn_readfirstlane(T == 35 ? CO + TW_OFF + (16384u ^ pl.dyn(0)) + 512 : 0u);
  ri.pa = __builtin_amdgcn_readfirstlane(0) + (p.slow & 255) | (p.vlead << 16) | (p.push3 << 24);
  ri.pb = p.dret | (p.late_r << 16);
  ri.pc = p.t_first | (p.look << 16);
  ri.ph = p.hdel | (p.hdl2 << 8) | 0x10000;
  ri.f_d = __builtin_amdgcn_readfirstlane(T < 32 ? CO + pl.near(N_RD + T) : 0x7F000000u);
  ri.f_r = dl + F_R * 128 + T * 8;
  asm volatile(R_ASM_TEXT : R_ASM_VOPS : R_ASM_SOPS : R_ASM_CLOB);
}

struct ChainIn {
  unsigned sidx, l16, qw, kw, fq, fk, kxo, pst, ps2, lx, sid2, qxo, sto, plx, mk, ck0, ck2, hx, viw, fva, pk3, fvs, uo, tlb, po0, po1, po2, po3, po4;
  unsigned vq0, vq1, vq2, vq3, vo0, vo1, vo2, vo3, v50, v51, v52, v53, tro0, tro1, tro2, tro3, tro4, m0o;
  const void *ws, *Wa, *Wb, *mem0;
  unsigned w, o_a, o_b, o_nx, o_exp, o_u, lab, f_p, f_m, f_u, f_dur, f_go, o_ctl, o_dbg, push, dret, u5, o_qx, f_s, f_r;
};

#define CODE_END ".Lcode_end:\n"
template <bool SC>
__device__ __forceinline__ void chain_role(const Params& p, int w, int h, int grp) {
  constexpr unsigned QT = 9216, KT = 13312, SX = 16384, VI = 9216, VX = 16384;
  const int l = lane_id(), n = l & 15, g = l >> 4, q4 = (l >> 2) & 3, p4 = l & 3;
  const int ty = (n >> 1) & 1, row = 2 * (n >> 2) + (n & 1), cid = h * 2 + (SC ? 0 : 1);
  char* const base = comm_base(p.ws) - CO;
  const unsigned dl = (unsigned)(p.ws - base);
  ChainIn ci;
  ci.sidx = (n & 2) ? 0xEEEEu : 0x4444u;
  ci.l16 = l * 16;
  ci.m0o = row * (DM * 2) + g * 32 + ty * 16;
  if (SC) {
    ci.qw = QT + (32 * w + n) * 16 + g * 4;
    ci.kw = KT + (32 * w + n) * 16 + (((g & 1) << 1) | (g >> 1)) * 4;
    ci.fq = QT + (4 * g + q4) * 16 + (p4 & 1) * 8;
    ci.fk = KT + (4 * g + q4) * 16 + (p4 >> 1) * 8;
    ci.kxo = (g * 16 + 8 * (q4 & 1) + 4 * (q4 >> 1) + p4) * 16;
    const unsigned ex = CO + win_static(W_PX + (h >> 3)) + (h & 7) * 512 + (g & 1) * 128 + (n & 7) * 16 + (g >> 1) * 8 + (n >> 3);
    if (w < 2) ci.pst = ex + w * 256;
    else if (g & 1) ci.pst = CO + win_static(W_PC) + (h >> 2) * 512 + (h & 1) * 256 + (h & 2) * 64 + (n & 7) * 16 + (((n >> 2) ^ (g >> 1)) & 1) * 8 + 2 * (w & 1) + (n >> 3);
    else ci.pst = CO + win_static(W_PM) + (h >> 1) * 256 + (g >> 1) * 128 + (n & 7) * 16 + (h & 1) * 8 + 2 * (w & 1) + (n >> 3);
    ci.ps2 = w < 2 ? ci.pst + 2 : ex + 2 * (w & 1);
    ci.mk = (g & 1) ? 0u : 0x3F800000u;
    ci.ck0 = g == 1 && !(h & 2) ? 0x3F800000u : 0u;
    ci.ck2 = g == 1 && (h & 2) ? 0x3F800000u : 0u;
    ci.hx = SX + 2048 + (w & 1) * 512 + l * 8;
    ci.lx = SX + l * 8 + (w == 1 ? 4 : 0);
    ci.qxo = (w >> 1) * 1024 + (g * 16 + 8 * (w & 1) + (n & 7)) * 16;
    ci.sto = 0x40000 + h * 256 + (8 * w + (n & 7)) * 8;
    ci.plx = n >= 8 || g == 3 ? 0x7F000000u : w * 16384 + (g & 1) * 4096 + h * 128 + n * 16 + (g >> 1) * 8;
  } else {
    ci.viw = VI + (32 * w + n) * 16 + g * 4;
    ci.fva = VI + (4 * g + q4) * 16 + (p4 & 1) * 8;
    ci.pk3 = VI + n * 16 + g * 4; ci.fvs = w * 1024 + l * 16;
    ci.uo = w == 1 ? l * 16 : 0x7F000000u;
    ci.tlb = 0;
    auto vo_at = [&](int T0) {
      const int T = T0 + 2 * w + (g >> 1);
      return CO + win_static(W_VO + 2 * (T >> 2) + (h >> 4)) + (h & 15) * 256 + n * 16 + (g & 1) * 8 + (T & 3);
    };
    ci.po0 = vo_at(0); ci.po1 = vo_at(8); ci.po2 = vo_at(16);
    ci.po3 = w ? vo_at(24) : 0x7F000000u; ci.po4 = w ? vo_at(w == 1 ? 22 : 28) : 0x7F000000u;
  }
  ci.sid2 = (n & 8) ? 0xEEEEu : 0x4444u;
  unsigned vq[4], vo[4], v5[4];
#pragma unroll
  for (int i = 0; i < 4; ++i) {
    const int ch = n ^ (2 * ((g & 1) | (i << 1))), tp = ch >> 1;
    vq[i] = (g + 4 * i) * (HD * 2) + ch * 16 - 1024 * i;
    vo[i] = (g + 4 * i) * (DM * 2) + ch * 16 - 1024 * i;
    v5[i] = (g + 4 * i + 16 - 16 * (tp >> 2)) * (DM * 2) + (32 + (tp & 3)) * 32 + (ch & 1) * 16 - 1024 * i;
  }
  ci.vq0 = vq[0]; ci.vq1 = vq[1]; ci.vq2 = vq[2]; ci.vq3 = vq[3];
  ci.vo0 = vo[0]; ci.vo1 = vo[1]; ci.vo2 = vo[2]; ci.vo3 = vo[3];
  ci.v50 = v5[0]; ci.v51 = v5[1]; ci.v52 = v5[2]; ci.v53 = v5[3];
  {
    const int rowl = 16 * (g >> 1) + 8 * (q4 >> 1) + 4 * (g & 1) + (q4 & 1), rho = (rowl & 1) | (((rowl >> 2) & 3) << 1);
    ci.tro0 = rowl * 256 + (((4 * w + (p4 >> 1)) ^ (2 * rho)) * 16) + (p4 & 1) * 8;
    ci.tro1 = rowl * 256 + (((4 * w + 2 + (p4 >> 1)) ^ (2 * rho)) * 16) + (p4 & 1) * 8;
    const int row2 = 16 * (g & 1) + 4 * (g >> 1) + (q4 & 1), rho2 = (row2 & 1) | (((row2 >> 2) & 3) << 1);
    ci.tro2 = row2 * 256 + (((4 * w + 2 * (q4 >> 1) + (p4 >> 1)) ^ (2 * rho2)) * 16) + (p4 & 1) * 8;
    ci.tro3 = w < 2 ? (ci.tro2 ^ (w * 64)) + 7 * 16384 : ci.tro2 + 9 * 16384;
    ci.tro4 = w < 2 ? ci.tro3 + 16384 : ci.tro3 ^ 128u;
  }
  ci.ws = base; ci.Wa = SC ? p.Wq : p.Wv; ci.Wb = SC ? p.Wk : p.Wo; ci.mem0 = p.mem0;
  ci.w = w;
  ci.o_a = 16 * w * (HD * 2) + h * 256;
  ci.o_b = (h * DH + 32 * w) * (DM * 2);
  ci.o_nx = near_head(B_K, h) + (w & 1) * 4096;
  ci.o_exp = near_head(B_K, h) + (SC ? (w & 1) * 2048 : 4096);
  ci.o_u = 0;
  ci.u5 = w == 1 ? 26 * 65536 + 3 * 16384 : 36 * 65536 + 8192;
  ci.o_qx = PB + 2 * B_O + h * 16384; ci.f_s = __builtin_amdgcn_readfirstlane(dl + (F_S + h) * 128);
  ci.f_r = dl + F_R * 128;
  ci.lab = grp;
  ci.f_p = __builtin_amdgcn_readfirstlane(CO + near_static(N_K + h) + (SC ? 0 : 8));
  ci.f_m = SC ? dl + (F_M + cid) * 128 + (w & 1) * 8 : w == 1 ? dl + (F_M + cid) * 128 : 0x7F000000u; ci.f_u = 0; ci.f_dur = __builtin_amdgcn_readfirstlane(CO + near_static(N_DUR + h));
  ci.f_go = dl + (F_GO + grp) * 128; ci.o_ctl = dl;
  ci.o_dbg = dl + (unsigned)WS_END + (cid * 4 + w) * 4096;
  ci.push = __builtin_amdgcn_readfirstlane((w == 0 && (SC ? h < 4 : h >= 24)) + (p.gate << 8));
  ci.dret = p.dret | (p.late_s << 16);
  if (SC) asm volatile(X_ASM_TEXT M_ASM_TEXT CODE_END : X_ASM_VOPS : X_ASM_SOPS : X_ASM_CLOB);
  else asm volatile(W_ASM_TEXT V_ASM_TEXT : V_ASM_VOPS : V_ASM_SOPS : V_ASM_CLOB);
}

struct ProjIn {
  unsigned l16, xr0, xr1, ew, er, uo, xs0, xs1, xs2, xs3, xs4, xs5, xs6, xs7, xs8;
  unsigned vq0, vq1, vq2, vq3, vo0, vo1, vo2, vo3, v50, v51, v52, v53, tro0, tro1;
  const void *ws, *Wa, *Wb, *x;
  unsigned w, o_a, o_b, o_p, o_q, f_a, f_b, f_go, o_ctl, f_c, u8, rng;
};
template <bool VU, bool QO = false>
__device__ __forceinline__ unsigned proj_role(const Params& p, int w, int h) {
  const int l = lane_id(), n = l & 15, g = l >> 4, q4 = (l >> 2) & 3, p4 = l & 3;
  char* const base = comm_base(p.ws) - CO;
  const unsigned dl = (unsigned)(p.ws - base);
  ProjIn pi;
  pi.l16 = l * 16; pi.uo = 0;
  unsigned xs[9];
#pragma unroll
  for (int i = 0; i < 9; ++i) {
    const int ci = (9 * w + i) * 64 + l, r = ci / 72, cc = ci - r * 72;
    xs[i] = (r * 72 + (cc ^ ((r >> 1) & 7))) * 16;
  }
  pi.xs0 = xs[0]; pi.xs1 = xs[1]; pi.xs2 = xs[2]; pi.xs3 = xs[3]; pi.xs4 = xs[4]; pi.xs5 = xs[5]; pi.xs6 = xs[6]; pi.xs7 = xs[7]; pi.xs8 = xs[8];
  pi.xr0 = 16384 + n * 1152 + ((g ^ ((n >> 1) & 7)) * 16); pi.xr1 = 16384 + n * 1152 + (((4 + g) ^ ((n >> 1) & 7)) * 16);
  pi.ew = w * 2048 + n * 64 + ((g ^ ((n >> 1) & 1) ^ ((n >> 2) & 2)) * 16);
  pi.er = 8192 + (g >> 1) * 1032 + (8 * (g & 1) + q4) * 64 + ((p4 ^ (q4 >> 1) ^ (2 * (g & 1))) * 16);
  unsigned vq[4], vo[4], v5[4];
#pragma unroll
  for (int i = 0; i < 4; ++i) {
    const int ch = n ^ (2 * (g | ((i >> 1) << 2))), tp = ch >> 1;
    vq[i] = (g + 4 * i) * (HD * 2) + ch * 16 - 1024 * i;
    vo[i] = (g + 4 * i) * (DM * 2) + ch * 16 - 1024 * i;
    v5[i] = (g + 4 * i + 64 * (tp & 1)) * (DM * 2) + (32 + (tp >> 1)) * 32 + (ch & 1) * 16 - 1024 * i;
  }
  pi.vq0 = vq[0]; pi.vq1 = vq[1]; pi.vq2 = vq[2]; pi.vq3 = vq[3];
  pi.vo0 = vo[0]; pi.vo1 = vo[1]; pi.vo2 = vo[2]; pi.vo3 = vo[3];
  pi.v50 = v5[0]; pi.v51 = v5[1]; pi.v52 = v5[2]; pi.v53 = v5[3];
  const int rho = q4 | ((g & 1) << 2);
  pi.tro0 = (8 * g + q4) * 256 + (((4 * w + (p4 >> 1)) ^ (2 * rho)) * 16) + (p4 & 1) * 8;
  pi.tro1 = (8 * g + q4) * 256 + (((4 * w + 2 + (p4 >> 1)) ^ (2 * rho)) * 16) + (p4 & 1) * 8;
  pi.ws = base; pi.Wa = VU ? p.Wv : QO ? p.Wq : p.Wk; pi.Wb = VU ? p.Wo : p.Wq; pi.x = p.x;
  pi.w = w;
  pi.o_a = 16 * w * (HD * 2) + h * 256;
  pi.o_b = (h * DH + 16 * w) * (DM * 2);
  pi.o_p = PB + 2 * (VU ? B_V : QO ? B_Q : B_K) + h * 16384 + w * 2048 + (VU && QO ? 2 * 524288 : 0);
  pi.o_q = VU ? u_tile(2 * w, h >> 3, h & 7) ^ 8192u : PB + 2 * B_Q + h * 16384 + w * 2048 + (K_FIRST - 1) * 524288;
  pi.f_a = __builtin_amdgcn_readfirstlane(w || (QO && !VU) ? 0x7F000000u : CO + near_static(N_K + h) + (VU ? 8 + 8 * QO : 0));
  pi.f_b = __builtin_amdgcn_readfirstlane(VU ? CO + near_static(N_U + h) + w * 8 + 32 * QO : w ? 0x7F000000u : dl + (F_Q + h) * 128);
  pi.f_go = dl + (F_GO + 4 + (h & 3)) * 128; pi.o_ctl = dl;
  pi.f_c = VU ? dl + (F_M + 2 * h + 1) * 128 : QO ? 77 : 78;
  pi.rng = QO ? 2 | (U_HELP + 2) << 8 : 64 << 8 | U_HELP << 16;
  pi.u8 = QO && !VU ? 0x7F000000u : (VU ? u_tile(32 + w, h >> 3, h & 7) : near_head(B_K, h) + w * 2048) ^ 8192u;
  unsigned tag;
  if (!VU) sleep_n(QO ? p.q0 : p.pj0);
  if (VU) asm volatile(U_ASM_TEXT : P_ASM_VOPS : P_ASM_SOPS : P_ASM_CLOB);
  else asm volatile(K_ASM_TEXT : P_ASM_VOPS : P_ASM_SOPS : P_ASM_CLOB);
  return tag;
}

__device__ __forceinline__ void att_role(const Params& p, unsigned tag, int w, int h) {
  CODE_PAD(PAD_ATT);
  char* const ws = p.ws;
  const int l = lane_id(), n = l & 15, g = l >> 4, qt = w & 1, par = w >> 1;
  const Ctx cx = {ws, tag};
  Bulk bk;
  bk.init(cx, 4 + (h & 3));
  const unsigned l16 = l * 16, vmo = (n + 16 * (((g & 1) << 1) | (g >> 1))) * 16;
  const float scale = 0.08838834764831845f;
  auto rnd2 = [&](float a, float b, float& ra, float& rb) {
    unsigned u = pk2(a, b);
    u = pk2(lo16(u) * scale, hi16(u) * scale);
    ra = lo16(u); rb = hi16(u);
  };
  auto soft = [&](const f32x4* st, int qi, u32x4& px, u32x2& pm) {
    const float neg = rbf(-1000000000.0f);
    float v12[12];
    rnd2((g & 1) ? st[0][2] : st[0][0], (g & 1) ? st[0][3] : st[0][1], v12[0], v12[1]);
    v12[2] = v12[0]; v12[3] = v12[1];
#pragma unroll
    for (int j = 0; j < 4; j += 2) { rnd2(st[1][j], st[1][j + 1], v12[4 + j], v12[5 + j]); rnd2(st[2][j], st[2][j + 1], v12[8 + j], v12[9 + j]); }
#pragma unroll
    for (int j = 0; j < 4; ++j) {
      v12[4 + j] = (4 * g + j <= qi) ? v12[4 + j] : neg;
      v12[8 + j] = (16 + 4 * g + j <= qi) ? v12[8 + j] : neg;
    }
    float mx = v12[0];
#pragma unroll
    for (int j = 1; j < 12; ++j) mx = fmaxf(mx, v12[j]);
    mx = red_max_g(mx);
#pragma unroll
    for (int j = 0; j < 12; ++j) v12[j] = __builtin_amdgcn_exp2f((v12[j] - mx) * 1.4426950408889634f);
    float sum = v12[0] + v12[1];
#pragma unroll
    for (int j = 4; j < 12; ++j) sum += v12[j];
    sum = red_sum_g(sum);
    const float inv = __builtin_amdgcn_rcpf(sum);
    px = (u32x4){pk2(v12[4] * inv, v12[5] * inv), pk2(v12[6] * inv, v12[7] * inv), pk2(v12[8] * inv, v12[9] * inv), pk2(v12[10] * inv, v12[11] * inv)};
    pm = (u32x2){pk2(v12[0] * inv, v12[1] * inv), 0u};
  };
  int seenp = 0, seenm = 0;
  auto wait_proj = [&](int target) {
    unsigned spins = 0;
    while (seenp < target) {
      const int v = (int)flag_get(cx, bk.proj_flag(cx, min(l, 3), h)), v2 = __builtin_amdgcn_readlane(v, 3);
      seenp = min(min(max(__builtin_amdgcn_readlane(v, 0), K_FIRST), __builtin_amdgcn_readlane(v, 1)), min(__builtin_amdgcn_readlane(v, 2), v2 < U_HELP + 2 ? max(v2, 2) : NSEG));
      if (seenp < target) { __builtin_amdgcn_s_sleep(4); if (spin_fail(spins, cx)) break; }
    }
    __builtin_amdgcn_fence(__ATOMIC_ACQUIRE, "wavefront");
  };
  GAS u64* const fm = flag_ptr(ws, F_M + 2 * h + ((l >> 1) & 1)) + (l & 1 & ~(l >> 1));
  auto min8 = [&](u64 f) {
    int v = (unsigned)(f >> 32) == cx.tag ? (int)(unsigned)f : 0;
#pragma unroll
    for (int m = 1; m < 4; m <<= 1) v = min(v, __shfl_xor(v, m));
    return __builtin_amdgcn_readfirstlane(v);
  };
  auto wait_m = [&](int target) {
    unsigned spins = 0;
    while (seenm < target) {
      seenm = min8(__hip_atomic_load(fm, RLX_AGENT));
      if (seenm < target) { __builtin_amdgcn_s_sleep(2); if (spin_fail(spins, cx)) break; }
    }
    __builtin_amdgcn_fence(__ATOMIC_ACQUIRE, "wavefront");
  };
  bf16x8 qn[4], kx[4][2], vn[8];
  auto ahead = [&](int s) {
    const unsigned so = (s * NH + h) * 8192, oq = bk.at(B_Q + so) + qt * 1024, ok = bk.at(B_K + so), ov = bk.at(B_V + so);
#pragma unroll
    for (int ks = 0; ks < 4; ++ks) {
      qn[ks] = ldfrag(bk.rs, l16, oq + ks * 2048);
      kx[ks][0] = ldfrag(bk.rs, l16, ok + ks * 2048);
      kx[ks][1] = ldfrag(bk.rs, l16, ok + ks * 2048 + 1024);
    }
#pragma unroll
    for (int et = 0; et < 8; ++et) vn[et] = ldfrag(bk.rs, l16, ov + et * 1024);
  };
  bf16x8 km[4];
  u32x2 vm[4];
  auto ldm = [&](int s) {
    const unsigned om = near_head(B_K, h) ^ (~bk.w13 & 8192u) ^ ((s ? s - 1 : 8192) << 14);
#pragma unroll
    for (int ks = 0; ks < 4; ++ks) km[ks] = ldfrag(bk.rs, l16, om + ks * 1024);
#pragma unroll
    for (int k = 0; k < 4; ++k) vm[k] = __builtin_amdgcn_raw_buffer_load_b64(bk.rs, vmo, om + 4096 + k * 1024, 16);
  };
  if (par && l == 0) flag_put(cx, flag_ptr(ws, F_O + h) + w, 1u);
  wait_proj(par + 1);
  ahead(par);
  wait_m(par + 1);
  ldm(par);
  bool pend = false, fq = false;
  u64 fw = 0;
  for (int s = par; s < SDIR; s += 2) {
    bf16x8 vx[8];
    u32x2 vc[4];
    f32x4 st[3] = {};
#pragma unroll
    for (int ks = 0; ks < 4; ++ks) { st[1] = MF(kx[ks][0], qn[ks], st[1]); st[2] = MF(kx[ks][1], qn[ks], st[2]); }
#pragma unroll
    for (int ks = 0; ks < 4; ++ks) { st[0] = MF(km[ks], qn[ks], st[0]); vc[ks] = vm[ks]; vx[ks] = vn[ks]; vx[ks + 4] = vn[ks + 4]; }
    SB();
    if (fq) {
      seenm = max(seenm, min8(fw));
      __builtin_amdgcn_fence(__ATOMIC_ACQUIRE, "wavefront");
    }
    const bool more = s + 2 < SDIR;
    bool have = more && seenm >= s + 3;
    fq = have;
    if (have) {
      ldm(s + 2);
      fw = __hip_atomic_load(fm, RLX_AGENT);
    }
    wait_proj(s + 3);
    ahead(more ? s + 2 : s);
    SB();
    u32x4 px;
    u32x2 pm;
    soft(st, qt * 16 + n, px, pm);
    const u32x4 pxs = {px[2], px[3], px[0], px[1]};
    f32x4 o[8];
#pragma unroll
    for (int et = 0; et < 8; ++et) {
      const f32x4 z = {};
      o[et] = MF(vx[et], __builtin_bit_cast(bf16x8, (et & 1) ? pxs : px), z);
      o[et] = MF(zext8((u32x2){vc[et >> 1][et & 1], 0u}), zext8(pm), o[et]);
    }
    if (pend) {
      WAIT_VM(20);
      if (l == 0) flag_put(cx, flag_ptr(ws, F_O + h) + w, s);
    }
    const unsigned oo = bk.at(B_O + (s * NH + h) * 8192) + qt * 1024;
#pragma unroll
    for (int k = 0; k < 4; ++k) st128(pack8(o[2 * k], o[2 * k + 1]), bk.rs, l16, oo + k * 2048);
    SB();
    pend = true;
    if (!have) {
      pend = false;
      if (more) seenm = min8(__hip_atomic_load(fm, RLX_AGENT));
      WAIT_VM(0);
      if (l == 0) flag_put(cx, flag_ptr(ws, F_O + h) + w, s + 2);
      if (more) { wait_m(s + 3); ldm(s + 2); }
    }
  }
}

__device__ __forceinline__ void xprep_role(const Params& p, unsigned tag, int w, int h) {
  const int l = lane_id(), n = l & 15, g = l >> 4, qt = w & 1, par = w >> 1;
  const Ctx cx = {p.ws, tag};
  Bulk bk;
  bk.init(cx, 4 + (h & 3));
  const unsigned l16 = l * 16;
  const unsigned xo = X_AT + ((bk.w13 ^ 8192u) & 8192u);
  bf16x8 qq[NDIR / 2][4], kk[NDIR / 2][8];
#pragma unroll
  for (int i2 = 0; i2 < NDIR; i2 += 2) {
    const unsigned so = ((SDIR + i2 + par) * NH + h) * 8192, oq = bk.at(B_Q + so) + qt * 1024, ok = bk.at(B_K + so);
#pragma unroll
    for (int ks = 0; ks < 4; ++ks) {
      qq[i2 >> 1][ks] = ldfrag(bk.rs, l16, oq + ks * 2048);
      kk[i2 >> 1][2 * ks] = ldfrag(bk.rs, l16, ok + ks * 2048); kk[i2 >> 1][2 * ks + 1] = ldfrag(bk.rs, l16, ok + ks * 2048 + 1024);
    }
  }
  SB();
#pragma unroll
  for (int i2 = 0; i2 < NDIR; i2 += 2) {
    const int i = i2 + par;
    const unsigned oc = (bk.at(B_O + ((SDIR + i) * NH + h) * 8192) ^ 8192u) + qt * 1024;
    f32x4 a = {}, b = {};
#pragma unroll
    for (int ks = 0; ks < 4; ++ks) {
      const bf16x8 q = qq[i2 >> 1][ks];
      st128(__builtin_bit_cast(u32x4, q), bk.rs, l16, oc + ks * 2048);
      a = MF(kk[i2 >> 1][2 * ks], q, a); b = MF(kk[i2 >> 1][2 * ks + 1], q, b);
    }
    float mx = -__builtin_inff(), sum = 0.f;
#pragma unroll
    for (int j = 0; j < 4; ++j) {
      a[j] = 4 * g + j <= 16 * qt + n ? a[j] : -__builtin_inff(); b[j] = 16 + 4 * g + j <= 16 * qt + n ? b[j] : -__builtin_inff();
      mx = fmaxf(mx, fmaxf(a[j], b[j]));
    }
    const float c = 0.08838834764831845f * 1.4426950408889634f, nx = -red_max_g(mx) * c;
#pragma unroll
    for (int j = 0; j < 4; ++j) { a[j] = __builtin_amdgcn_exp2f(__builtin_fmaf(a[j], c, nx)); b[j] = __builtin_amdgcn_exp2f(__builtin_fmaf(b[j], c, nx)); sum += a[j] + b[j]; }
    sum = red_sum_g(sum);
    const unsigned eo = xo + 0x80000 + (i * 4 + 2 * qt + (n >> 3)) * 32768 + (h >> 4) * 16384 + (h & 15) * 512 + (g >> 1) * 128 + (n & 7) * 16 + (g & 1) * 8;
    __builtin_amdgcn_raw_buffer_store_b64((u32x2){pk2(a[0], a[1]), pk2(a[2], a[3])}, bk.rs, eo, 0, 16);
    __builtin_amdgcn_raw_buffer_store_b64((u32x2){pk2(b[0], b[1]), pk2(b[2], b[3])}, bk.rs, eo, 256, 16);
    if (g == 0) __builtin_amdgcn_raw_buffer_store_b64((u32x2){__float_as_uint(nx), __float_as_uint(sum)}, bk.rs, h * 256 + (16 * qt + n) * 8, xo + 0x40000 + i * 65536, 16);
    __builtin_amdgcn_raw_buffer_store_b64((u32x2){0xFFFFFFFFu, 0xFFFFFFFFu}, bk.rs, (2 * qt + (g >> 1)) * 16384 + (g & 1) * 4096 + h * 128 + n * 8, xo + i * 65536, 16);
  }
  WAIT_VM(0);
  BARRIER();
  publish(cx, w, F_S + h, 1);
}

struct OutIn {
  unsigned l16, rw, rr, fo, wv0, wv1, wv2, tro;
  const void *ws, *Wo;
  unsigned w, tag, o_w, o_o, o_a, f_a, o_ctl, ns;
};
__device__ __forceinline__ void op_role(const Params& p, unsigned tag, int w, int ct, int kq, int grp) {
  const Ctx cx = {p.ws, tag};
  Bulk bk;
  bk.init(cx, grp);
  const int l = lane_id(), g = l >> 4, q4 = (l >> 2) & 3, p4 = l & 3, h0 = kq * 8 + w * 2;
  char* const base = comm_base(p.ws) - CO;
  const unsigned dl = (unsigned)(p.ws - base);
  OutIn oi;
  oi.l16 = l * 16;
  oi.rw = 65536 + w * 12288 + l * 16;
  oi.rr = 65536 + w * 3072 + l * 16;
  oi.fo = dl + (F_O + h0 + ((l >> 2) & 1)) * 128 + (l & 3) * 8;
  auto piece = [&](int i) { const int idx = i * 64 + l, row = idx / 12; return row * (DM * 2) + (idx - row * 12) * 16; };
  oi.wv0 = piece(0); oi.wv1 = piece(1); oi.wv2 = piece(2);
  oi.tro = w * 24576 + (4 * g + q4) * 192 + p4 * 8;
  oi.ws = base; oi.Wo = p.Wo; oi.w = w; oi.tag = tag; oi.o_ctl = dl; oi.ns = SDIR;
  oi.o_w = (kq * 1024 + w * 256) * (DM * 2) + ct * 192;
  oi.o_o = __builtin_amdgcn_readfirstlane(bk.w13 + 2 * B_O + h0 * 16384);
  oi.o_a = __builtin_amdgcn_readfirstlane(bk.w13 + 2 * B_AP + (ct * 4 + kq) * 32768 + (w >> 1) * 16384 + (w & 1) * 4096);
  oi.f_a = __builtin_amdgcn_readfirstlane(w ? 0x7F000000u : dl + (F_A + ct * 4 + kq) * 128);
  asm volatile(O_ASM_TEXT : O_ASM_VOPS : O_ASM_SOPS : O_ASM_CLOB);
}

typedef __attribute__((ext_vector_type(16))) float f32x16;
constexpr int S4_PRM = 0;
constexpr int S4_KD = S4_PRM + 2560;
constexpr int S4_KH = S4_KD + 2624;
constexpr int S4_U = S4_KH + 5200;
constexpr int S4_USTR = 4352 + 64;
constexpr int S4_WSZ = S4_U + 4 * S4_USTR;
constexpr int S4_RAW = 4 * S4_WSZ;

#define S4M(i) do {} while (0)
struct S4In { u32x2 u[8]; unsigned la, ai, cr, ci, dt; };
__device__ __forceinline__ float s4_params(LAS char* my, const S4In& in) {
  const int l = lane_id();
  const u16 pl_[2] = {(u16)in.la, (u16)(in.la >> 16)}, pa_[2] = {(u16)in.ai, (u16)(in.ai >> 16)}, pr_[2] = {(u16)in.cr, (u16)(in.cr >> 16)}, pi_[2] = {(u16)in.ci, (u16)(in.ci >> 16)};
  const float dt = rbf(expf(bf2f((u16)in.dt)));
  f32x4 pr[2];
#pragma unroll
  for (int k = 0; k < 2; ++k) {
    const float a = -rbf(expf(bf2f(pl_[k]))) * dt;
    const float b = bf2f(pa_[k]) * dt;
    const float c_hi = 0.15915494f, c_lo = (float)(0.15915494309189535 - (double)0.15915494f);
    const float bh = b * c_hi;
    const float bl = __builtin_fmaf(b, c_hi, -bh) + b * c_lo;
    const float cr = bf2f(pr_[k]), ci = bf2f(pi_[k]);
    pr[k] = (f32x4){a * 1.4426950408889634f, bh, bl, log2f(hypotf(cr, ci))};
    *(LAS float*)(my + S4_PRM + 2048 + (l + 64 * k) * 4) = -atan2f(ci, cr) * c_hi;
  }
  float top = fmaxf(fmaxf(pr[0][3], pr[1][3]), -100.f);
#pragma unroll
  for (int m = 1; m < 64; m <<= 1) top = fmaxf(top, __shfl_xor(top, m));
  top = ceilf(top);
#pragma unroll
  for (int k = 0; k < 2; ++k) { pr[k][3] += 14.f - top; *(LAS f32x4*)(my + S4_PRM + (l + 64 * k) * 16) = pr[k]; }
  return exp2f(top - 27.f) * dt;
}
typedef __attribute__((ext_vector_type(2))) _Float16 f16x2;
typedef __attribute__((ext_vector_type(8))) _Float16 f16x8;
__device__ __forceinline__ void s4_modes(LAS char* my, int q0, int q1, f32x4 (&D)[3]) {
  const int l = lane_id(), i = l & 15, kk = l >> 4;
  const f32x2 pe[2] = {{(float)(8 * i), (float)(128 * (i >> 3) + (i & 7))},
                       {(float)(i < 8 ? 256 + 128 * (i >> 2) + 2 * (i & 3) : 2048), (float)(i < 8 ? 512 + 128 * (i >> 1) + 4 * (i & 1) : 128 * i)}};
  const f32x2 pp[2] = {{-pe[0][0], pe[0][1]}, pe[1]};
#pragma unroll 1
  for (int q = q0; q < q1; ++q) {
    u32x4 op[4];
    const f32x4 pc = *(LAS f32x4*)(my + S4_PRM + 2048 + (4 * q + kk) * 16);
#pragma unroll
    for (int j = 0; j < 4; ++j) {
      const f32x4 pr = *(LAS f32x4*)(my + S4_PRM + (16 * q + 4 * kk + j) * 16);
#pragma unroll
      for (int m = 0; m < 2; ++m) {
        const f32x2 ea = __builtin_elementwise_fma((f32x2){pr[0], pr[0]}, pe[m], (f32x2){m ? 14.f : pr[3], 14.f});
        const f32x2 t = pp[m] * pr[1];
        f32x2 fr = __builtin_elementwise_fma((f32x2){pr[1], pr[1]}, pp[m], -(f32x2){__builtin_rintf(t[0]), __builtin_rintf(t[1])});
        fr = __builtin_elementwise_fma((f32x2){pr[2], pr[2]}, pp[m], fr);
        if (!m) fr[0] += pc[j];
#pragma unroll
        for (int h = 0; h < 2; ++h) {
          const f32x2 v = (f32x2){__builtin_amdgcn_cosf(fr[h]), __builtin_amdgcn_sinf(fr[h])} * __builtin_amdgcn_exp2f(ea[h]);
          op[2 * m + h][j] = __builtin_bit_cast(unsigned, __builtin_convertvector(v, f16x2));
        }
      }
    }
#pragma unroll
    for (int m = 0; m < 3; ++m) D[m] = __builtin_amdgcn_mfma_f32_16x16x32_f16(__builtin_bit_cast(f16x8, op[m + 1]), __builtin_bit_cast(f16x8, op[0]), D[m], 0, 0, 0);
  }
}
__device__ __forceinline__ void s4d_task(const Ctx& cx, const Bulk& bk, unsigned yflip, int gentle, int w, int task, char* smem, const S4In& in, char* ws, int q0, f32x4 (&D)[3], float ksc) {
  const int l = lane_id(), tid = w * 64 + l;
  LAS char* const lds = (LAS char*)smem;
  LAS char* const my = lds + w * S4_WSZ;
  S4M(2);
  s4_modes(my, q0, 8, D);
  {
    const int i = l & 15, kk = l >> 4;
#pragma unroll
    for (int r = 0; r < 4; ++r) {
      const int row = 4 * kk + r;
      *(LAS float*)(my + S4_KD + (128 * (row >> 3) + (row & 7) + 8 * i) * 4) = ksc * D[0][r];
      *(LAS float*)(my + S4_KD + (row < 8 ? 256 + 64 * kk + r + 4 * i : 640 + i) * 4) = ksc * D[1][r];
      *(LAS float*)(my + S4_KD + (row < 8 ? 384 + 32 * (row >> 1) + (row & 1) + 2 * i : 384 + 16 * row + i) * 4) = ksc * D[2][r];
    }
  }
  S4M(3);
#pragma unroll
  for (int k = 0; k < 2; ++k)
#pragma unroll
    for (int j = 0; j < 8; j += 4) *(LAS u32x4*)(lds + S4_RAW + k * 8208 + tid * 32 + j * 4) = (u32x4){in.u[j][k], in.u[j + 1][k], in.u[j + 2][k], in.u[j + 3][k]};
  BARRIER();
  u32x2 r[11];
#pragma unroll
  for (int k = 0; k < 2; ++k)
#pragma unroll
    for (int j = 0; j < 12; j += 4) {
      const u32x4 v = *(LAS u32x4*)(lds + S4_RAW + k * 8208 + tid * 32 + j * 4);
#pragma unroll
      for (int i = 0; i < 4; ++i) if (j + i < 11) r[j + i][k] = v[i];
    }
#pragma unroll
  for (int ch = 0; ch < 4; ++ch) {
    unsigned pr[10];
#pragma unroll
    for (int j = 0; j < 10; ++j) pr[j] = __builtin_amdgcn_perm(r[j + 1][ch >> 1], r[j][ch >> 1], (ch & 1) ? 0x07060302u : 0x05040100u);
    if (tid == 255) { pr[7] &= 0xFFFFu; pr[8] = 0u; pr[9] = 0u; }
    LAS char* ub = lds + ch * S4_WSZ + S4_U + 64 + tid * 16;
#pragma unroll
    for (int sft = 0; sft < 4; ++sft) *(LAS u32x4*)(ub + sft * S4_USTR) = (u32x4){pr[sft], pr[sft + 2], pr[sft + 4], pr[sft + 6]};
    if (tid < 4) {
#pragma unroll
      for (int sft = 0; sft < 4; ++sft) {
        const u32x4 v = {0u, 0u, sft == 3 ? pr[0] << 16 : 0u, sft == 0 ? 0u : sft == 1 ? pr[0] << 16 : pr[sft - 2]};
        *(LAS u32x4*)(lds + ch * S4_WSZ + S4_U + sft * S4_USTR + (3 - tid) * 16) = tid == 0 ? v : (u32x4){0u, 0u, 0u, 0u};
      }
    }
  }
  {
    if (l < 5) {
      *(LAS u32x4*)(my + S4_KH + l * 16) = (u32x4){0u, 0u, 0u, 0u};
    }
#pragma unroll
    for (int k = 0; k < 4; ++k) {
      const int grp = l + 64 * k;
      float v[8];
#pragma unroll
      for (int j = 0; j < 8; j += 2) {
        const unsigned c = pk2((float)(8 * grp + j), (float)(8 * grp + j + 1)) - 0x42804280u;
        const bool low = k == 0 && grp < 16;
        v[j] = *(LAS float*)(my + S4_KD + (low ? 8 * grp + j : c & 0xFFFFu) * 4);
        v[j + 1] = *(LAS float*)(my + S4_KD + (low ? 8 * grp + j + 1 : c >> 16) * 4);
      }
      *(LAS u32x4*)(my + S4_KH + 80 + (grp >> 2) * 80 + (grp & 3) * 16) = (u32x4){pk2(v[7], v[6]), pk2(v[5], v[4]), pk2(v[3], v[2]), pk2(v[1], v[0])};
    }
  }
  __syncthreads();
  S4M(4);
  {
    const int r = l & 31, hh = l >> 5, sft = (r + 1) & 3;
    unsigned ua = w * S4_WSZ + S4_U + sft * S4_USTR + (r + 25 - sft - 8 * hh - 16) * 2;
    const unsigned kz = w * S4_WSZ + S4_KH + hh * 16;
    unsigned kb1 = kz + (33 + r) * 80;
    unsigned kb0 = kz + (1 + r) * 80;
    f32x16 acc0 = {}, acc1 = {};
    unsigned ub = ua + 72;
    asm volatile("" : "+v"(ub));
    struct Ops { bf16x8 a0, a1, b0, b1; };
    auto ld = [&](int ks) {
      Ops o;
      const unsigned uo = ua + (1 - ks) * 32;
      o.a0 = cat2(*(LAS bf16x4*)(lds + uo), *(LAS bf16x4*)(lds + uo + 8));
      o.a1 = cat2(*(LAS bf16x4*)(lds + uo + 2048), *(LAS bf16x4*)(lds + uo + 2056));
      o.b0 = *(LAS bf16x8*)(lds + kb0 + ks * 32);
      o.b1 = *(LAS bf16x8*)(lds + kb1 + ks * 32);
      return o;
    };
    auto step = [&](const Ops& o, Ops& nx, int ks) {
      const unsigned uo = ua + (1 - ks) * 32, up = ub + (1 - ks) * 32;
      acc1 = __builtin_amdgcn_mfma_f32_32x32x16_bf16(o.a0, o.b1, acc1, 0, 0, 0); SB();
      nx.a0 = cat2(*(LAS bf16x4*)(lds + uo), *(LAS bf16x4*)(lds + up)); nx.b1 = *(LAS bf16x8*)(lds + kb1 + ks * 32); SB();
      acc0 = __builtin_amdgcn_mfma_f32_32x32x16_bf16(o.a0, o.b0, acc0, 0, 0, 0); SB();
      nx.b0 = *(LAS bf16x8*)(lds + kb0 + ks * 32);
      nx.a1 = cat2(*(LAS bf16x4*)(lds + uo + 2048), *(LAS bf16x4*)(lds + up + 2048));
      if (ks) {
        ub += 64;
        ua += 64;
        kb1 -= 80;
        kb0 = max(kb0 - 80, kz);
      }
      SB();
      acc1 = __builtin_amdgcn_mfma_f32_32x32x16_bf16(o.a1, o.b0, acc1, 0, 0, 0); SB();
    };
    Ops e0 = ld(0), e1 = ld(1), o0, o1;
    ua += 64; kb1 -= 80; kb0 = max(kb0 - 80, kz);
#pragma unroll 1
    for (int D = 0; D < 32; D += 2) {
      step(e0, o0, 0);
      step(e1, o1, 1);
      step(o0, e0, 0);
      step(o1, e1, 1);
      sleep_n(gentle);
    }
    S4M(5);
#pragma unroll
    for (int j = 0; j < 4; ++j) {
      const unsigned off = (32 * r + 8 * j + 4 * hh) * 2;
      *(LAS u32x2*)(my + off) = (u32x2){pk2(acc0[4 * j], acc0[4 * j + 1]), pk2(acc0[4 * j + 2], acc0[4 * j + 3])};
      *(LAS u32x2*)(my + off + 2048) = (u32x2){pk2(acc1[4 * j], acc1[4 * j + 1]), pk2(acc1[4 * j + 2], acc1[4 * j + 3])};
    }
    if (yflip && w == 0)
      for (unsigned spins = 0; __builtin_amdgcn_readfirstlane(flag_get(cx, flag_ptr(ws, F_Z) + (task - Y_NEAR))) == 0u && !spin_fail(spins, cx);) __builtin_amdgcn_s_sleep(30);
    __syncthreads();
#pragma unroll
    for (int j = 0; j < 2; ++j) {
      u32x2 c[4];
#pragma unroll
      for (int k = 0; k < 4; ++k) c[k] = *(LAS u32x2*)(lds + k * S4_WSZ + (j * 256 + tid) * 8);
      const unsigned o = bk.at(B_YT + task * 16384 + j * 8192) ^ yflip;
#pragma unroll
      for (int k = 0; k < 2; ++k)
        st128((u32x4){__builtin_amdgcn_perm(c[1][k], c[0][k], 0x05040100u), __builtin_amdgcn_perm(c[3][k], c[2][k], 0x05040100u),
                      __builtin_amdgcn_perm(c[1][k], c[0][k], 0x07060302u), __builtin_amdgcn_perm(c[3][k], c[2][k], 0x07060302u)}, bk.rs, tid * 32 + k * 16, o);
    }
  }
  __syncthreads();
  S4M(6);
}

__device__ __forceinline__ void warm_l2(const u16* W, unsigned off, bool rows256, int w) {
  const rsrc_t rw = mk_rsrc(W, (size_t)DM * HD * 2);
  const int l = lane_id();
  const unsigned vo = rows256 ? (l >> 4) * (HD * 2) + (l & 15) * 16 : l * 16, st = rows256 ? 4 * HD * 2 : 1024;
  u32x4 v[36];
#pragma unroll
  for (int i = 0; i < 36; ++i) v[i] = __builtin_amdgcn_raw_buffer_load_b128(rw, vo, off + (i * 4 + w) * st, 0);
#pragma unroll
  for (int i = 0; i < 36; ++i) asm volatile("" :: "v"(v[i]));
}

constexpr int NS4 = 67;
__device__ __forceinline__ unsigned y_at(const Bulk& bk, int quad, unsigned o) { return bk.at(B_YT + quad * 16384 + o) ^ (quad >= Y_NEAR && quad < Y_FAR ? 8192u : 0u); }
__device__ __forceinline__ void x_ahead(const Params& p, int w, int idx) {
  const rsrc_t rx = mk_rsrc(p.x, (size_t)SL * DM * 2);
  for (int i = 0; i < p.xpre; ++i) dma16(rx, lane_id() * 16, ((((idx - 35) >> 2) * 4 + w) + i * 32) * 1024, LDS_BYTES - 1024);
}
__device__ __forceinline__ unsigned s4_role(const Params& p, int w, int idx, char* smem) {
  CODE_PAD(PAD_S4);
  char* const ws = p.ws;
  const int l = lane_id(), tid = w * 64 + l;
  const bool near = idx >= 8 && idx < 35;
  const int r_ = idx - 8, t0 = idx < 8 ? 4 * idx : Y_NEAR + 4 * r_ + (r_ + 6) / 7;
  const int t1 = t0 + 4 + (near && r_ % 7 == 0) - (near ? p.s4skip : 0);
  const rsrc_t rx = mk_rsrc(p.x, (size_t)SL * DM * 2);
  S4In in[5];
#pragma unroll
  for (int k = 0; k < 5; ++k) {
    const int d = min(t0 + k, t1 - 1) * 4 + w, o = d * (NST / 2) + l;
    in[k].la = ((const unsigned*)p.lar)[o]; in[k].ai = ((const unsigned*)p.aim)[o]; in[k].cr = ((const unsigned*)p.cre)[o]; in[k].ci = ((const unsigned*)p.cim)[o];
    in[k].dt = __builtin_amdgcn_raw_buffer_load_b16(mk_rsrc(p.ldt, DM * 2), d * 2, 0, 0);
  }
  const unsigned gen = gen_load(ws);
  LAS char* const my = (LAS char*)smem + w * S4_WSZ;
  float ksc = s4_params(my, in[0]);
  f32x4 D[3] = {};
  const int qs = near ? p.s4q : p.s4f;
  s4_modes(my, 0, qs, D);
#pragma unroll
  for (int m = 0; m < 5; ++m)
#pragma unroll
    for (int j = 0; j < 8; ++j) in[m].u[j] = __builtin_amdgcn_raw_buffer_load_b64(rx, t0 + m < t1 ? (tid * 8 + j) * (DM * 2) : 0x7F000000u, (t0 + m) * 8, 0);
  if (near) sleep_n((r_ % 7) * p.stag);
  s4_modes(my, qs, 8, D);
  const Ctx cx = {ws, __builtin_amdgcn_readfirstlane(gen) + 1u};
  Bulk bk;
  bk.init(cx, idx & 7);
#pragma unroll 1
  for (int k = t0; k < t1; ++k) {
    s4d_task(cx, bk, near ? 8192u : 0u, near ? p.gentle : 0, w, k, smem, in[0], ws, k == t0 ? 8 : 0, D, ksc);
    in[0] = in[1]; in[1] = in[2]; in[2] = in[3]; in[3] = in[4];
    ksc = s4_params(my, in[0]);
    D[0] = D[1] = D[2] = (f32x4){0.f, 0.f, 0.f, 0.f};
  }
  WAIT_VM(0);
  BARRIER();
  if (tid == 0) flag_put(cx, flag_ptr(ws, F_Y) + idx, 1u);
  return cx.tag;
}
__device__ __forceinline__ void gate_role(const Params& p, int w, int idx) {
  char* const ws = p.ws;
  const int l = lane_id(), tid = w * 64 + l, n = l & 15, g = l >> 4;
  const unsigned gen = gen_load(ws);
  x_ahead(p, w, idx);
  sleep_n(p.gate0);
  const rsrc_t rx = mk_rsrc(p.x, (size_t)SL * DM * 2), rg = mk_rsrc(p.gw, DM * 2);
  const int t = ((idx - 35) * 4 + w) * 16 + g;
  u32x4 gw[4], xv[4][4];
  u32x2 gt, xt[4];
#pragma unroll
  for (int k = 0; k < 4; ++k) gw[k] = __builtin_amdgcn_raw_buffer_load_b128(rg, n * 16, k * 256, 0);
  gt = __builtin_amdgcn_raw_buffer_load_b64(rg, n * 8, 1024, 0);
#pragma unroll
  for (int r = 0; r < 4; ++r) {
#pragma unroll
    for (int k = 0; k < 4; ++k) xv[r][k] = __builtin_amdgcn_raw_buffer_load_b128(rx, t * (DM * 2) + n * 16, r * 4 * (DM * 2) + k * 256, 0);
    xt[r] = __builtin_amdgcn_raw_buffer_load_b64(rx, t * (DM * 2) + n * 8, r * 4 * (DM * 2) + 1024, 0);
  }
  SB();
  const Ctx cx = {ws, __builtin_amdgcn_readfirstlane(gen) + 1u};
  Bulk bk;
  bk.init(cx, idx & 7);
  for (int z = idx - 35; z < Y_FAR - Y_NEAR; z += 32)
#pragma unroll
    for (int m = 0; m < 4; ++m) st128((u32x4){0u, 0u, 0u, 0u}, bk.rs, tid * 16, (bk.at(B_YT + (Y_NEAR + z) * 16384 + m * 4096) ^ 8192u));
#pragma unroll
  for (int r = 0; r < 4; ++r) {
    float acc = 0.f;
#pragma unroll
    for (int k = 0; k < 4; ++k)
#pragma unroll
      for (int j = 0; j < 4; ++j) acc += rbf(lo16(xv[r][k][j]) * lo16(gw[k][j])) + rbf(hi16(xv[r][k][j]) * hi16(gw[k][j]));
#pragma unroll
    for (int j = 0; j < 2; ++j) acc += rbf(lo16(xt[r][j]) * lo16(gt[j])) + rbf(hi16(xt[r][j]) * hi16(gt[j]));
#pragma unroll
    for (int m = 8; m >= 1; m >>= 1) acc += __shfl_xor(acc, m);
    const float gg = rbf(1.0f / (1.0f + expf(-rbf(acc))));
    if (n == 0) __builtin_amdgcn_raw_buffer_store_b32(__float_as_uint(gg), bk.rs, (t + 4 * r) * 4, bk.at(B_G), 16);
  }
  WAIT_VM(0);
  BARRIER();
  if (tid < 4 && idx - 35 + 32 * tid < Y_FAR - Y_NEAR) flag_put(cx, flag_ptr(ws, F_Z) + (idx - 35 + 32 * tid), 1u);
}

struct FinIn {
  unsigned sidx, l16, ae, am, ac, lw, lr, oo, d0, fu;
  float g[NDIR], b[NDIR], c[NDIR];
  const void *ws, *out;
  unsigned tag, o_u, o_e, o_x, o_va, o_tw, xa, o_ctl, pa, w;
};
__device__ __forceinline__ void fin_role(const Params& p, unsigned tag, int w, int T) {
  static_assert(SDIR % 4 == 0, "ring slots");
  char* const ws = p.ws;
  const int l = lane_id(), n = l & 15, g = l >> 4;
  const Ctx cx = {ws, tag};
  Bulk bk;
  bk.init(cx, T & 7);
  const Place& pl = bk.pl;
  FinIn fi;
  if (w == 0) {
    wait_all<60>(cx, F_Y, 64);
    wait_all<60>(cx, F_Y + 4, NS4 - 64);
  }
  BARRIER();
  const int d = T * 16 + 8 * ((l >> 3) & 1) + 2 * (l & 3), r0 = SDIR * 32 + 8 * w + 2 * g + ((l >> 2) & 1);
  float (&gg)[NDIR] = fi.g, (&b0)[NDIR] = fi.b, (&b1)[NDIR] = fi.c;
#pragma unroll
  for (int i = 0; i < NDIR; ++i) {
    const int t = r0 + 32 * i;
    gg[i] = __uint_as_float(__builtin_amdgcn_raw_buffer_load_b32(bk.rs, t * 4, bk.at(B_G), 16));
    const unsigned yv = __builtin_amdgcn_raw_buffer_load_b32(bk.rs, y_at(bk, d >> 2, t * 8 + (d & 2) * 2), 0, 16);
    const unsigned xv = *(const unsigned*)(p.x + (size_t)t * DM + d), dv = *(const unsigned*)(p.dsk + d);
    b0[i] = rbf(rbf(1.0f - gg[i]) * rbf(lo16(yv) + rbf(lo16(xv) * lo16(dv)))); b1[i] = rbf(rbf(1.0f - gg[i]) * rbf(hi16(yv) + rbf(hi16(xv) * hi16(dv))));
  }
#pragma unroll
  for (int i = 0; i < NDIR; ++i) asm volatile("" : "+v"(b0[i]), "+v"(b1[i]));
  if (w == 0)
    for (unsigned spins = 0; __any(flag_get(cx, flag_ptr(ws, F_S + (l & 31))) == 0u) && !spin_fail(spins, cx);) __builtin_amdgcn_s_sleep(30);
  BARRIER();
  char* const base = comm_base(ws) - CO;
  fi.sidx = (n & 8) ? 0xEEEEu : 0x4444u;
  fi.l16 = l * 16; fi.d0 = 0;
  fi.ae = (n >> 3) * 512 + g * 128 + (n & 7) * 16;
  fi.am = w * 1024 + l * 16;
  fi.ac = 4096 + (8 * w + 2 * (n >> 2) + (g >> 1)) * 128 + (4 * (g & 1) + (n & 3)) * 16;
  fi.lw = (w >> 1) * 512 + (2 * (g & 1) + (g >> 1)) * 128 + (n >> 1) * 16 + (w & 1) * 8 + (n & 1) * 4;
  fi.lr = w * 2048 + ((l >> 2) & 1) * 1024 + g * 128 + (((l >> 3) & 1) * 4 + (l & 3)) * 16;
  fi.oo = (r0 * DM + d) * 2;
  fi.fu = CO + pl.near(N_U + 8 * w + (l & 7)) + (T < 32 ? (T >> 1) & 3 : T & 3) * 8;
  fi.ws = base; fi.out = p.out; fi.tag = __builtin_amdgcn_readfirstlane(tag); fi.o_ctl = (unsigned)(ws - base); fi.xa = pl.tog();
  fi.o_u = __builtin_amdgcn_readfirstlane(u_tile(T, w, 0) ^ (~bk.w13 & 8192u));
  fi.o_x = X_AT + ((bk.w13 ^ 8192u) & 8192u);
  fi.o_e = __builtin_amdgcn_readfirstlane(fi.o_x + 0x80000 + (w >> 1) * 16384 + (w & 1) * 4096);
  fi.o_va = __builtin_amdgcn_readfirstlane(CO + (win_static(W_VO + 2 * (T >> 2) + (w >> 1)) ^ pl.dyn(T & 3)) + (w & 1) * 2048);
  fi.o_tw = __builtin_amdgcn_readfirstlane(CO + TW_OFF + (16384u ^ pl.dyn(T & 3) ^ X1));
  fi.pa = p.xt0 | (p.xt1 << 16); fi.w = w;
  asm volatile(F_ASM_TEXT : F_ASM_VOPS : F_ASM_SOPS : F_ASM_CLOB);
  if (T == 0 && w == 0 && lane_id() == 0) __hip_atomic_store((GAS unsigned*)flag_ptr(ws, F_GEN), tag, RLX_AGENT);
}

__device__ __forceinline__ void mix_role(const Params& p, unsigned tag, int w, int idx) {
  char* const ws = p.ws;
  const int l = lane_id(), tid = w * 64 + l;
  const int n = l & 15, g = l >> 4;
  const Ctx cx = {ws, tag};
  Bulk bk;
  bk.init(cx, idx & 7);
  int seena = 0;
  wait_all<30>(cx, F_Y, 64);
  wait_all<30>(cx, F_Y + 4, NS4 - 64);
  const int ct = idx % 6, s0 = idx / 6;
  const int tr = (w >> 1) * 16 + n, dc = ct * 96 + (w & 1) * 48 + 4 * g;
  constexpr int MS = 5, MN = SDIR / MS;
  static_assert(SDIR % MS == 0, "segments per workgroup");
  unsigned bp[MN][3][2];
  float gg[MN];
#pragma unroll
  for (int k = 0; k < MN; ++k) {
    const int t = (s0 + MS * k) * 32 + tr;
    gg[k] = __uint_as_float(__builtin_amdgcn_raw_buffer_load_b32(bk.rs, t * 4, bk.at(B_G), 16));
#pragma unroll
    for (int i = 0; i < 3; ++i) {
      const int d = dc + 16 * i;
      const u32x2 xv = *(const u32x2*)(p.x + (size_t)t * DM + d), dv = *(const u32x2*)(p.dsk + d);
      const u32x2 yv = __builtin_amdgcn_raw_buffer_load_b64(bk.rs, y_at(bk, d >> 2, t * 8), 0, 16);
      float bb[4];
#pragma unroll
      for (int j = 0; j < 4; ++j) {
        const float y = (j & 1) ? hi16(yv[j >> 1]) : lo16(yv[j >> 1]);
        const float xf = (j & 1) ? hi16(xv[j >> 1]) : lo16(xv[j >> 1]), df = (j & 1) ? hi16(dv[j >> 1]) : lo16(dv[j >> 1]);
        bb[j] = rbf(1.0f - gg[k]) * rbf(y + rbf(xf * df));
      }
      bp[k][i][0] = pk2(bb[0], bb[1]); bp[k][i][1] = pk2(bb[2], bb[3]);
    }
  }
  const rsrc_t rout = mk_rsrc(p.out, (size_t)SL * DM * 2);
  f32x4 a[2][3][4];
  auto lda = [&](int k) {
#pragma unroll
    for (int i = 0; i < 3; ++i)
#pragma unroll
      for (int kq = 0; kq < 4; ++kq)
        a[k & 1][i][kq] = __builtin_bit_cast(f32x4, ld128(bk.rs, l * 16, bk.at(B_AP + (((((s0 + MS * k) & RG_AP) * 6 + ct) * 4 + kq) * 16 + w * 4 + i) * 1024)));
  };
  wait_ge_n<4>(cx, F_A + ct * 4, 1, 4, s0 + 1, seena);
  lda(0);
#pragma unroll
  for (int k = 0; k < MN; ++k) {
    const bool ahead = k + 1 < MN && seena > s0 + MS * k + MS;
    if (ahead) lda(k + 1);
    SB();
#pragma unroll
    for (int i = 0; i < 3; ++i) {
      const f32x4 av = (a[k & 1][i][0] + a[k & 1][i][1]) + (a[k & 1][i][2] + a[k & 1][i][3]);
      float o[4];
#pragma unroll
      for (int j = 0; j < 4; ++j) o[j] = rbf(gg[k] * rbf(av[j])) + ((j & 1) ? hi16(bp[k][i][j >> 1]) : lo16(bp[k][i][j >> 1]));
      __builtin_amdgcn_raw_buffer_store_b64((u32x2){pk2(o[0], o[1]), pk2(o[2], o[3])}, rout, (tr * DM + dc + 16 * i) * 2, (s0 + MS * k) * (32 * DM * 2), OUT_AUX);
    }
    if (!ahead && k + 1 < MN) { wait_ge_n<2>(cx, F_A + ct * 4, 1, 4, s0 + MS * k + MS + 1, seena); lda(k + 1); }
  }
}

__global__ void __launch_bounds__(256, 1) k_mega(Params p) {
  extern __shared__ __attribute__((aligned(16))) char smem[];
  const int x = blockIdx.x & 7, j = blockIdx.x >> 3;
  const int w = __builtin_amdgcn_readfirstlane(threadIdx.x >> 6);
  if (j >= p.jhi) return;
  if (x == 0 && (j < 16 || j > 17)) sleep_n(p.s0);
  if (x < 4 && j < 8) { chain_role<true>(p, w, x + 4 * j, x); return; }
  else if (x < 4 && j < 16) { chain_role<false>(p, w, x + 4 * (j - 8), x); return; }
  else if (x < 4 && j < 25) red_role(p, w, x + 4 * (j - 16), smem);
  else if (x == 0 && j == 25) {
    if (w == 0) keep_role(p);
    else if (p.cpf) {
      const char* pc;
      unsigned back, len;
      asm volatile("s_getpc_b64 %0\n.Lcp_%=:\n\ts_mov_b32 %1, .Lcp_%=-_Z6k_mega6Params\n\ts_mov_b32 %2, .Lcode_end-_Z6k_mega6Params" : "=s"(pc), "=s"(back), "=s"(len));
      pc -= back;
      sleep_n(p.cpf);
      const rsrc_t rc = mk_rsrc(pc, len);
      for (unsigned o = (w - 1) * 1024; o < len; o += 3072) dma16(rc, lane_id() * 16, o, w * 1024);
      WAIT_VM(0);
    }
  }
  else {
    unsigned tag;
    int T = -1;
    if (x >= 4 && j < 16) {
      const int h = (x & 3) + 4 * (j & 7);
      tag = j >= 8 ? proj_role<false>(p, w, h) : proj_role<true>(p, w, h);
      if (j >= 8) xprep_role(p, tag, w, h);
      if (j >= 8 && h < 30) mix_role(p, tag, w, h);
      if (j >= 8 && h == 30) T = 0;
    } else {
      if (x >= 4 && j < 24) {
        const int h = (x & 3) + 4 * (j & 7);
        if (p.wl2 & 1) warm_l2(p.Wo, h * DH * DM * 2, false, w);
        gate_role(p, w, 35 + h);
        tag = proj_role<false, true>(p, w, h);
        if (w == 0 && lane_id() == 0) flag_put({p.ws, tag}, flag_ptr(p.ws, F_Y) + 35 + h, 1u);
        att_role(p, tag, w, h);
      } else {
        if (x >= 4) tag = proj_role<true, true>(p, w, (x & 3) + 4 * (j & 7));
        if (x >= 4 && j < 30) op_role(p, tag, w, j - 24, x & 3, x);
        else {
          tag = s4_role(p, w, x < 4 ? 7 + x * 7 + (j - 25) : (x & 3) * 2 + j - 30, smem);
          T = x < 4 ? (j - 25) * 4 + x : (j - 23) * 4 + (x & 3);
        }
      }
    }
    if (T >= 0) fin_role(p, tag, w, T);
  }
}

extern "C" void kernel_launch(void* const* d_in, const int* in_sizes, int n_in, void* d_out, int out_size, void* d_ws, size_t ws_size,
                              hipStream_t stream) {
  Params p;
  p.x = (const u16*)d_in[0]; p.mem0 = (const u16*)d_in[1];
  p.Wq = (const u16*)d_in[2]; p.Wk = (const u16*)d_in[3]; p.Wv = (const u16*)d_in[4]; p.Wo = (const u16*)d_in[5];
  p.lar = (const u16*)d_in[8]; p.aim = (const u16*)d_in[9]; p.cre = (const u16*)d_in[10]; p.cim = (const u16*)d_in[11];
  p.ldt = (const u16*)d_in[12]; p.dsk = (const u16*)d_in[13]; p.gw = (const u16*)d_in[14];
  p.out = (u16*)d_out; p.ws = (char*)d_ws;
  (void)hipFuncSetAttribute((const void*)k_mega, hipFuncAttributeMaxDynamicSharedMemorySize, LDS_BYTES);
  {
    k_mega<<<256, 256, LDS_BYTES, stream>>>(p);
  }
}
```

```cpp
#include <hip/hip_runtime.h>
#include <hip/hip_bf16.h>
#include <stdint.h>
#include <type_traits>

typedef __attribute__((ext_vector_type(2))) __bf16 bf16x2;
typedef __attribute__((ext_vector_type(4))) __bf16 bf16x4;
typedef __attribute__((ext_vector_type(8))) __bf16 bf16x8;
typedef __attribute__((ext_vector_type(2))) float f32x2;
typedef __attribute__((ext_vector_type(4))) float f32x4;
typedef __attribute__((ext_vector_type(2))) unsigned u32x2;
typedef __attribute__((ext_vector_type(4))) unsigned u32x4;
typedef unsigned short u16;
typedef __amdgpu_buffer_rsrc_t rsrc_t;
#define LAS __attribute__((address_space(3)))
#define GAS __attribute__((address_space(1)))

constexpr int SL = 2048, DM = 576, NH = 32, DH = 128, HD = 4096, NSEG = 64, NST = 128;

constexpr size_t WS_CTL = 0;
constexpr int CTL_BYTES = 65536;
constexpr int F_GEN = 0, F_TMO = 1, F_GO = 2, F_XCC = 10, F_PRB = 14, F_INITW = 16, F_Y = 20;
constexpr int F_U = 320;
constexpr int F_W = 360;
constexpr int F_R = 300;
constexpr int F_M = 65, F_O = 129, F_A = 200, F_Q = 256;
constexpr int W_VO = 1, W_PX = 19, W_PM = 23, W_PC = 24, W_MEM = 25;
constexpr int N_DUR = 0, N_K = 32, N_U = 64, N_RD = 96;
constexpr size_t COMM_BYTES = (size_t)8 << 20;
constexpr unsigned FR_BYTES = SL * HD * 2;
constexpr unsigned B_K = 0;
constexpr unsigned B_V = FR_BYTES;
constexpr unsigned B_U = FR_BYTES;
constexpr unsigned B_O = 3 * FR_BYTES;
constexpr unsigned B_Q = 4 * FR_BYTES;
constexpr unsigned B_AP = 5 * FR_BYTES + NH * 8192;
constexpr unsigned B_YT = B_AP + NSEG * 24 * 16384;
constexpr unsigned B_G = B_YT + DM * SL * 2;
constexpr int SDIR = 58, NDIR = NSEG - SDIR;
constexpr unsigned X_AT = 8u << 20;
constexpr int Y_NEAR = 9, Y_FAR = 144, F_Z = 310;
constexpr int F_S = 26;
#define RG_U 3
#define UNEAR 8192u
#define NCOPY 4
#define RG_QKV 63
#define RG_M 63
#define RG_O 63
#define RG_AP 63
#define PB_MB 12u
constexpr unsigned CO = 4u << 20, PB = PB_MB << 20, BASE_BYTES = 244u << 20;
constexpr unsigned U_AT = (128u << 20) - PB;
constexpr size_t WS_END = (size_t)240 << 20;
constexpr int LDS_BYTES = 163840;
constexpr unsigned SPIN_CAP = 1u << 21;

struct Params {
  const u16 *x, *mem0, *Wq, *Wk, *Wv, *Wo, *lar, *aim, *cre, *cim, *ldt, *dsk, *gw;
  u16* out;
  char* ws;
#define TUNE_LIST(X) X(keep_at, "KD", 20) X(jhi, "JHI", 32) X(t_a, "TA", 12) X(t_first, "TY", 170) X(t_d, "TD", 12) X(rd0, "RD0", 52) X(phase, "SKIP", 3) X(gentle, "GENTLE", 0) X(spike, "SPIKE", 24) X(boost, "BOOST", 0) X(boost0, "BOOST0", 0) X(push, "PUSH", 165) X(slow, "SLOW", -4) X(dec0, "DEC0", 1) X(dec1, "DEC1", 3) X(dsw, "DSW", 26) X(look, "LOOK", 36) X(push2, "TY2", 25) X(bsa, "BSA", 8) X(push3, "TY3", 35) X(dur_dec, "DDEC", 8) X(dur_inc, "DINC", 8) X(dur0, "DP0", 63) X(calm, "CALM", 0) X(bmax, "BMAX", 16) X(op_sleep, "OSL", 2) X(stag, "STAG", 40) X(tail, "TAIL", 267) X(psl, "PSL", 0) X(xin, "XIN", 64) X(pushx, "PUSHX", 115) X(dret, "DRET", 76) X(dk, "DK", 115) X(dkv, "DKV", 115) X(rsl, "RSL", 1) X(cpf, "CPF", 3) X(yz, "YZ", 1) X(sa, "SA", 16) X(sb, "SB", 53) X(xg0, "XG0", 7) X(xg1, "XG1", 6) X(xtra, "XTRA", 6) X(s4q, "S4Q", 0) X(s4f, "S4F", 8) X(wl2, "WL2", 1) X(s0, "S0", 30) X(v0, "V0", 0) X(xl0, "XL0", 56) X(dsh, "DSH", 3) X(tlb, "TLB", 18) X(xprep, "XPREP", 0) X(xt0, "XT0", 145) X(xt1, "XT1", 80) X(gate, "GATE", 40) X(vlead, "VLEAD", 23) X(npush, "NPUSH", 8) X(near0, "NEAR0", 0) X(att0, "ATT0", 0) X(xpre, "XPRE", 7) X(pj0, "PJ0", 0) X(tail2, "TAIL2", 217) X(xlast, "XLAST", 5) X(defer, "DEFER", 4) X(apre, "APRE", 0) X(hdel, "HDEL", 0) X(hdl2, "HDL2", 0) X(gate0, "GATE0", 0) X(q0, "Q0", 0) X(s4skip, "S4SKIP", 0) X(mixs, "MIXS", 0) X(late_r, "LR", 8) X(late_s, "LS", 8)
#define TUNE_DECL(n, e, v) static constexpr int n = v;
  TUNE_LIST(TUNE_DECL)
};

__device__ __forceinline__ int lane_id() {
  int z = 0;
  asm volatile("" : "+v"(z));
  return __builtin_amdgcn_mbcnt_hi(~0u, __builtin_amdgcn_mbcnt_lo(~0u, z));
}
__device__ __forceinline__ float bf2f(u16 x) { return __uint_as_float((unsigned)x << 16); }
__device__ __forceinline__ float rbf(float x) { return (float)(__bf16)x; }
__device__ __forceinline__ unsigned pk2(float a, float b) {
  f32x2 v = {a, b};
  return __builtin_bit_cast(unsigned, __builtin_convertvector(v, bf16x2));
}
__device__ __forceinline__ float lo16(unsigned p) { return __uint_as_float(p << 16); }
__device__ __forceinline__ float hi16(unsigned p) { return __uint_as_float(p & 0xFFFF0000u); }
__device__ __forceinline__ bf16x8 zext8(u32x2 v) {
  u32x4 z = {v[0], v[1], 0u, 0u};
  return __builtin_bit_cast(bf16x8, z);
}
__device__ __forceinline__ bf16x4 tr_read(const char* smem_base, unsigned off) {
  return __builtin_amdgcn_ds_read_tr16_b64_v4bf16((LAS bf16x4*)((LAS char*)smem_base + off));
}
__device__ __forceinline__ float red_max_g(float x) {
  auto r = __builtin_amdgcn_permlane16_swap(__float_as_uint(x), __float_as_uint(x), false, false);
  x = fmaxf(__uint_as_float(r[0]), __uint_as_float(r[1]));
  auto q = __builtin_amdgcn_permlane32_swap(__float_as_uint(x), __float_as_uint(x), false, false);
  return fmaxf(__uint_as_float(q[0]), __uint_as_float(q[1]));
}
__device__ __forceinline__ float red_sum_g(float x) {
  auto r = __builtin_amdgcn_permlane16_swap(__float_as_uint(x), __float_as_uint(x), false, false);
  x = __uint_as_float(r[0]) + __uint_as_float(r[1]);
  auto q = __builtin_amdgcn_permlane32_swap(__float_as_uint(x), __float_as_uint(x), false, false);
  return __uint_as_float(q[0]) + __uint_as_float(q[1]);
}
__device__ __forceinline__ rsrc_t mk_rsrc(const void* p, size_t bytes) {
  return __builtin_amdgcn_make_buffer_rsrc((void*)p, 0, (int)bytes, 0x00020000);
}

#define STR2(x) #x
#define STR(x) STR2(x)
#define CODE_PAD(N) asm volatile("s_branch .Lpad_%=\n\t.fill " STR(N) ", 4, 0xBF800000\n\t.Lpad_%=:" ::: "memory")
#define PAD_RED 0
#define PAD_ATT 0
#define PAD_OP 0
#define PAD_S4 0
#define PAD_CH 0
#define WAIT_VM(n) asm volatile("s_waitcnt vmcnt(" #n ")" ::: "memory")
#define WAIT_LGKM0() asm volatile("s_waitcnt lgkmcnt(0)" ::: "memory")
#define BARRIER() do { WAIT_LGKM0(); __builtin_amdgcn_s_barrier(); asm volatile("" ::: "memory"); } while (0)
#define SB() __builtin_amdgcn_sched_barrier(0)
#define MF(a, b, c) __builtin_amdgcn_mfma_f32_16x16x32_bf16(a, b, c, 0, 0, 0)
#define RLX_AGENT __ATOMIC_RELAXED, __HIP_MEMORY_SCOPE_AGENT
#define DEV_COUNT(i, cond) do {} while (0)
#define DEV_COUNT_DECL do {} while (0)
#define DEV_COUNT_FLUSH() do {} while (0)
__device__ __forceinline__ bf16x8 cat2(bf16x4 a, bf16x4 b) { return __builtin_shufflevector(a, b, 0, 1, 2, 3, 4, 5, 6, 7); }
__device__ __forceinline__ u32x4 pack8(const f32x4& a, const f32x4& b) {
  return (u32x4){pk2(a[0], a[1]), pk2(a[2], a[3]), pk2(b[0], b[1]), pk2(b[2], b[3])};
}
__device__ __forceinline__ void dma16(rsrc_t rs, unsigned voff, unsigned soff, unsigned lds_addr) {
  unsigned keep;
  asm volatile("s_mov_b32 %0, m0\n\ts_mov_b32 m0, %3\n\ts_nop 0\n\tbuffer_load_dwordx4 %1, %2, %4 offen lds\n\ts_mov_b32 m0, %0"
               : "=&s"(keep) : "v"(voff), "s"(rs), "s"(lds_addr), "s"(soff) : "memory");
}
__device__ __forceinline__ void st128(u32x4 v, rsrc_t rs, unsigned voff, unsigned soff) {
  __builtin_amdgcn_raw_buffer_store_b128(v, rs, voff, soff, 16);
  asm volatile("s_nop 1" ::"v"(v) : "memory");
}
__device__ __forceinline__ u32x4 ld128(rsrc_t rs, unsigned voff, unsigned soff) { return __builtin_amdgcn_raw_buffer_load_b128(rs, voff, soff, 16); }
__device__ __forceinline__ bf16x8 ldfrag(rsrc_t rs, unsigned voff, unsigned soff) { return __builtin_bit_cast(bf16x8, ld128(rs, voff, soff)); }
__device__ __forceinline__ void sleep_n(int n) { for (int i = 0; i < n; ++i) __builtin_amdgcn_s_sleep(1); }

__device__ __forceinline__ char* comm_base(char* ws) {
  return (char*)(((uintptr_t)ws + CTL_BYTES + ((2u << 20) - 1)) & ~(uintptr_t)((2u << 20) - 1));
}
__device__ __forceinline__ unsigned rt_now() {
  unsigned long long t;
  asm volatile("s_memrealtime %0\n\ts_waitcnt lgkmcnt(0)" : "=s"(t) :: "memory");
  return (unsigned)t;
}
__device__ __forceinline__ int wait_rt(unsigned t) {
  int d = (int)(t - rt_now());
  if (d <= 0 || d > 2000) return -d;
  for (d -= 14; d > 0; d -= 3) __builtin_amdgcn_s_sleep(1);
  do { d = (int)(t - rt_now()); } while (d > 0);
  return -d;
}
__device__ __forceinline__ unsigned xcc_id() { return (unsigned)__builtin_amdgcn_s_getreg(20 | (3 << 11)) & 7u; }
typedef unsigned long long u64;
struct Ctx { char* ws; unsigned tag; };
__device__ __forceinline__ GAS u64* flag_ptr(char* ws, int idx) { return (GAS u64*)(ws + WS_CTL + idx * 128); }
__device__ __forceinline__ unsigned gen_load(char* ws) { return __hip_atomic_load((GAS unsigned*)flag_ptr(ws, F_GEN), RLX_AGENT); }
__device__ __forceinline__ unsigned flag_get(const Ctx& cx, GAS u64* p) {
  const u64 v = __hip_atomic_load(p, RLX_AGENT);
  return (unsigned)(v >> 32) == cx.tag ? (unsigned)v : 0u;
}
__device__ __forceinline__ void flag_put(const Ctx& cx, GAS u64* p, unsigned v) { __hip_atomic_store(p, ((u64)cx.tag << 32) | v, RLX_AGENT); }
__device__ __forceinline__ bool spin_fail(unsigned& spins, const Ctx& cx) {
  ++spins;
  if (spins > SPIN_CAP) { flag_put(cx, flag_ptr(cx.ws, F_TMO), 1u); return true; }
  return (spins & 1023u) == 0 && __builtin_amdgcn_readfirstlane(flag_get(cx, flag_ptr(cx.ws, F_TMO)));
}
template <int SLEEP = 1>
__device__ __forceinline__ void wait_ge_n(const Ctx& cx, int idx, int stride, int cnt, int target, int& seen) {
  unsigned spins = 0;
  const int k = lane_id() & 3;
  while (seen < target) {
    int v = (int)flag_get(cx, flag_ptr(cx.ws, idx + (k < cnt ? k : 0) * stride));
    seen = min(min(__builtin_amdgcn_readlane(v, 0), __builtin_amdgcn_readlane(v, 1)), min(__builtin_amdgcn_readlane(v, 2), __builtin_amdgcn_readlane(v, 3)));
    if (seen < target) {
      __builtin_amdgcn_s_sleep(SLEEP);
      if (spin_fail(spins, cx)) break;
    }
  }
  __builtin_amdgcn_fence(__ATOMIC_ACQUIRE, "wavefront");
}
template <int SLEEP>
__device__ __forceinline__ void wait_all(const Ctx& cx, int idx, int cnt) {
  unsigned spins = 0;
  const int k = lane_id();
  while (__any(flag_get(cx, flag_ptr(cx.ws, idx) + (k < cnt ? k : 0)) == 0u)) {
    __builtin_amdgcn_s_sleep(SLEEP);
    if (spin_fail(spins, cx)) break;
  }
  __builtin_amdgcn_fence(__ATOMIC_ACQUIRE, "wavefront");
}
__device__ __forceinline__ void publish(const Ctx& cx, int w, int idx, int value) {
  if (w == 0 && lane_id() == 0) flag_put(cx, flag_ptr(cx.ws, idx), (unsigned)value);
}

constexpr unsigned X1 = 0x101000u, TW_OFF = 27 * 32768 + 1024;
__device__ __forceinline__ unsigned win_static(int win) { return win * 32768 + (win >> 4) * 16384; }
__device__ __forceinline__ unsigned near_static(int i) { return (win_static(28 + (i >> 5)) ^ ((i & 32) << 7) ^ ((i & 64) << 8)) + (i & 31) * 128; }
struct Place {
  unsigned go;
  __device__ __forceinline__ void load(const Ctx& cx, int grp, unsigned need = 0xC0000000u) {
    unsigned spins = 0;
    while ((go = __builtin_amdgcn_readfirstlane(flag_get(cx, flag_ptr(cx.ws, F_GO + grp)))) < need && !spin_fail(spins, cx)) __builtin_amdgcn_s_sleep(8);
    __builtin_amdgcn_fence(__ATOMIC_ACQUIRE, "wavefront");
  }
  __device__ __forceinline__ unsigned dyn(int x) const { return ((go >> (4 * x)) & 7u) << 12; }
  __device__ __forceinline__ unsigned tog() const { return (((go >> 16) & 7u) << 12) | (2u << 20); }
  __device__ __forceinline__ unsigned slot(int s) const { return (((s ^ (s >> 1)) & 1) ? tog() : 0u) ^ ((s & 2) ? X1 : 0u); }
  __device__ __forceinline__ unsigned near(int i) const { return near_static(i) ^ (dyn(0) & 8192u); }
};
#define BULK_FLIP 0u
#define OUT_AUX 0
#define PROBE_N 6
struct Bulk {
  rsrc_t rs;
  unsigned w13;
  Place pl;
  __device__ __forceinline__ GAS u64* proj_flag(const Ctx& cx, int m, int h) const {
    return m == 0 ? flag_ptr(cx.ws, F_Q + h) : (GAS u64*)(comm_base(cx.ws) + pl.near(N_K + h) + (m - 1) * 8);
  }
  __device__ __forceinline__ void init(const Ctx& cx, int grp, u64 early = 0) {
    pl.go = __builtin_amdgcn_readfirstlane((unsigned)(early >> 32) == cx.tag ? (unsigned)early : 0u);
    if (pl.go < 0x80000000u) pl.load(cx, grp, 0x80000000u);
    rs = mk_rsrc(comm_base(cx.ws) - CO, BASE_BYTES);
    w13 = PB + ((~pl.go ^ BULK_FLIP) & 2u) * 4096;
  }
  __device__ __forceinline__ unsigned at(unsigned o) const {
    const unsigned q = w13 + ((o >> 13) << 14) + (o & 8191u);
    return q ^ ((q >> 8) & 8192u);
  }
};
__device__ __forceinline__ unsigned near_head(unsigned b, int h) { return PB + 2 * b + h * (1u << 20) + ((h >> 1) & 1) * 8192 + ((2 * h) & 63) * 16384; }
__device__ __forceinline__ unsigned u_tile(int T, int hb, int h7) {
  const int wv = T < 32 ? (T >> 1) & 3 : T & 3;
  return PB + 2 * B_U + T * (1u << 20) + ((T >> 1) & 1) * 8192 + (((T >> 3) & 3) * 4 + wv) * 65536 + (hb ^ ((T >> 3) & 3)) * 16384 + (h7 ^ (2 * wv + (T < 32 ? T & 1 : 0))) * 1024;
}
__device__ __forceinline__ unsigned xcc_page(unsigned xc) { return ((xc >> 1) & 1u) | ((xc >> 2) << 1) | ((xc & 1u) << 2); }

__device__ __forceinline__ void touch_pages(char* ws, int i) {
  const rsrc_t rs = mk_rsrc(comm_base(ws) - CO, BASE_BYTES);
  const unsigned o = lane_id() * (2u << 20) + (1u << 20) + (i & 63) * 128;
  const unsigned a = __builtin_amdgcn_raw_buffer_load_b32(rs, o, 4u << 20, 0), b = __builtin_amdgcn_raw_buffer_load_b32(rs, o, 132u << 20, 0);
  asm volatile("" :: "v"(a), "v"(b));
}

__device__ __forceinline__ void comm_init(Ctx& cx, int rid, int w, char* smem) {
  const int l = lane_id(), tid = w * 64 + l;
  const rsrc_t rs = mk_rsrc(comm_base(cx.ws), COMM_BYTES);
  const unsigned gen = gen_load(cx.ws);
  if (rid == 0) {
    LAS unsigned* const best = (LAS unsigned*)smem + 16;
    const unsigned base = w * 1024, r0 = rt_now(), c0 = (unsigned)__builtin_amdgcn_s_memtime();
    best[32] = 0u;
#pragma unroll
    for (int k = 0; k < 8; ++k) __builtin_amdgcn_raw_buffer_store_b128((u32x4){0u, 0u, 0u, 0u}, rs, base + k * 4096 + l * 16, 0, 16);
    WAIT_VM(0);
    BARRIER();
    unsigned mine = 0xFFFFu;
    auto sample = [&](int pg, int k) {
      unsigned long long t0, t1;
      asm volatile("s_memtime %0\n\ts_waitcnt lgkmcnt(0)" : "=s"(t0) :: "memory");
      const unsigned v = __builtin_amdgcn_raw_buffer_load_b32(rs, base + pg * 4096 + k * 128, 0, 16);
      asm volatile("s_waitcnt vmcnt(0)\n\ts_memtime %0\n\ts_waitcnt lgkmcnt(0)" : "=s"(t1) : "v"(v) : "memory");
      if (l == pg) mine = min(mine, (unsigned)(t1 - t0));
      return (unsigned)(t1 - t0);
    };
    auto all = [&]() {
      if (l < 8) best[w * 8 + l] = mine;
      __syncthreads();
      const int k8 = l & 7;
      const unsigned b = min(min(best[k8], best[8 + k8]), min(best[16 + k8], best[24 + k8]));
      __syncthreads();
      return b;
    };
    auto page = [](int j) { return (j & 1) | ((j & 2) << 1); };
    const unsigned lim = 25u * ((unsigned)__builtin_amdgcn_s_memtime() - c0) / max(rt_now() - r0, 1u);
    cx.tag = __builtin_amdgcn_readfirstlane(gen) + 1u;
    volatile LAS unsigned* const found = best + 32;
    for (int k = 0; k < 40 && !__builtin_amdgcn_readfirstlane(*found); ++k) {
      const int pg = page(w + (k >> 1)) | ((k & 1) << 1);
      if (sample(pg, k >> 3) < lim && l == 0) atomicCAS((unsigned*)found, 0u, 1u | (pg & 2));
    }
    unsigned b = all();
    b = min(b, (unsigned)__shfl_xor((int)b, 1)); b = min(b, (unsigned)__shfl_xor((int)b, 4));
    const unsigned fd = __builtin_amdgcn_readfirstlane(*found);
    const unsigned half = fd ? fd & 2u : __builtin_amdgcn_readlane(b, 2) < __builtin_amdgcn_readlane(b, 0) ? 2u : 0u;
    if (w == 3 && l < 8) flag_put(cx, flag_ptr(cx.ws, F_GO + l), 0x80000000u | half);
    unsigned xc = 0;
    if (w < 3) {
#pragma unroll
      for (int k = 0; k < PROBE_N; ++k) sample(page(w + k) | half, 5 + (k >> 2));
    } else {
      for (unsigned spins = 0; __any((xc = flag_get(cx, flag_ptr(cx.ws, F_INITW) + min(l, 35))) == 0u) && !spin_fail(spins, cx);) __builtin_amdgcn_s_sleep(2);
    }
    b = (all() << 3) | (l & 7);
    b = min(b, (unsigned)__shfl_xor((int)b, 1)); b = min(b, (unsigned)__shfl_xor((int)b, 4));
    if (w == 3) {
      const unsigned v = l ? __shfl((int)xc, min(l, 3) * 9 - 1) - 1u : xcc_id();
      const unsigned p0 = (__builtin_amdgcn_readlane(b, half) & 7u) ^ xcc_page(xcc_id());
      unsigned seen = 0, go = 0xC0020000u;
#pragma unroll
      for (int x = 0; x < 4; ++x) seen |= 1u << (__builtin_amdgcn_readlane(v, x) & 7u);
#pragma unroll
      for (int x = 0; x < 4; ++x)
        go |= (seen == 15u || seen == 240u ? (xcc_page(__builtin_amdgcn_readlane(v, x)) ^ p0) & 7u : page(x) | half) << (4 * x);
      if (l < 8) flag_put(cx, flag_ptr(cx.ws, F_GO + l), go);
    }
    return;
  }
  for (int j = (rid - 1) * 256 + tid; j < 34 * 2048; j += 35 * 256) {
    const int wi = 33 - (j >> 11), ch = j & 2047;
    const int win = wi < 24 ? wi + 1 : wi < 27 ? wi + 65 : wi == 27 ? 59 : wi == 28 ? 123 : wi - 2;
    const unsigned f = wi < 30 ? 0xFFFFFFFFu : 0u;
    __builtin_amdgcn_raw_buffer_store_b128((u32x4){f, f, f, f}, rs, win * 32768 + ch * 16, 0, 16);
  }
  cx.tag = __builtin_amdgcn_readfirstlane(gen) + 1u;
  WAIT_VM(0);
  BARRIER();
  if (tid == 0) flag_put(cx, flag_ptr(cx.ws, F_INITW) + rid - 1, xcc_id() + 1u);
}

__device__ __forceinline__ void keep_role(const Params& p) {
  char* const ws = p.ws;
  const int l = lane_id();
  const Ctx cx = {ws, __builtin_amdgcn_readfirstlane(gen_load(ws)) + 1u};
  if (l == 0) flag_put(cx, flag_ptr(ws, F_INITW) + 35, 1u);
  const rsrc_t crs = mk_rsrc(comm_base(ws), COMM_BYTES);
  Place pl;
  pl.load(cx, 0);
  int dp[2] = {4 * p.dur0, 4 * p.rd0};
  unsigned t = rt_now(), ta = 0, shift = 0;
  auto plan = [&](int b) {
    u32x4 wd = {0u, 0u, 0u, 0u};
#pragma unroll
    for (int k = 0; k < 4; ++k) {
      const int s = 4 * b + k;
      const unsigned t1 = t + ((dp[0] + k) >> 2) + p.t_a + (s < p.sa ? p.xtra : s > p.xl0 ? p.xlast : 0);
      t = t1 + ((dp[1] + 3 - k) >> 2) + p.t_d + (s < p.sa ? p.xg0 : s >= p.sb ? p.xg1 : 0);
      if ((l & 7) == k) wd = (u32x4){(unsigned)s, t1, t, 0u};
      if (k == 2) ta = t;
    }
    st128(wd, crs, l < 32 ? (16384u ^ pl.dyn(l >> 3)) + (l & 7) * 16 : 0x7F000000u, TW_OFF + 1024 + b * 128);
  };
  plan(0);
  for (int b = 1; b < NSEG / 4; ++b) {
    u32x2 dw;
    for (unsigned spins = 0; !spin_fail(spins, cx); shift += p.dret) {
      const int d = (int)(ta + shift + p.keep_at - rt_now());
      if (d < -20 && d > -30000) continue;
      if (d > 60 && d < 2000) sleep_n((d - 40) >> 2);
      while ((unsigned)(ta + shift + p.keep_at - rt_now() - 1u) < 2000u) __builtin_amdgcn_s_sleep(2);
      const unsigned f = flag_get(cx, (GAS u64*)(comm_base(ws) + TW_OFF + (16384u ^ pl.dyn(0)) + 512));
      dw = __builtin_amdgcn_raw_buffer_load_b64(crs, pl.near((l < 32 ? N_DUR : N_RD - 32) + l), 0, 16);
      if ((__builtin_amdgcn_readfirstlane(f) & 255u) >= (unsigned)(4 * b - 2)) { shift = __builtin_amdgcn_readfirstlane(f) >> 8; break; }
    }
    unsigned dur = dw[1] == cx.tag && (dw[0] & 1023u) ? (unsigned)((float)(dw[0] >> 10) * 64.f * __builtin_amdgcn_rcpf((float)(dw[0] & 1023u))) : 0u;
    const unsigned long long none = __ballot(dur == 0u);
#pragma unroll
    for (int m = 1; m < 32; m <<= 1) dur = max(dur, (unsigned)__shfl_xor((int)dur, m));
#pragma unroll
    for (int k = 0; k < 2; ++k) {
      const int dm = __builtin_amdgcn_readlane(dur, 32 * k), d = dp[k];
      if ((unsigned)(none >> (32 * k)) == 0u) dp[k] = max(min(dm, d + p.dur_inc), d - p.dur_dec);
    }
    plan(b);
  }
}

#define X_ASM_TEXT \
  "s_cmp_gt_u32 %28, 1\n" \
  "s_cbranch_scc1 .LX_other_%=\n" \
  "s_mov_b64 s[40:41], %24\n" \
  "s_and_b32 s41, s41, 0xffff\n" \
  "s_mov_b32 s42, 255852544\n" \
  "s_mov_b32 s43, 0x00020000\n" \
  "s_mov_b32 s46, 4718592\n" \
  "s_mov_b32 s47, 0x00020000\n" \
  "s_lshl_b32 s72, %28, 11\n" \
  "buffer_load_dword v196, off, s[40:43], %40 sc1\n" \
  "s_mov_b64 s[44:45], %25\n" \
  "s_and_b32 s45, s45, 0xffff\n" \
  "s_add_u32 s60, %29, 0\n" \
  "s_lshl_b32 s61, s72, 1\n" \
  "s_add_u32 m0, s61, 16384\n" \
  "s_nop 0\n" \
  "buffer_load_dwordx4 v200, s[44:47], s60 offen lds\n" \
  "buffer_load_dwordx4 v201, s[44:47], s60 offen offset:1024 lds\n" \
  "buffer_load_dwordx4 v202, s[44:47], s60 offen offset:2048 lds\n" \
  "buffer_load_dwordx4 v203, s[44:47], s60 offen offset:3072 lds\n" \
  "s_add_u32 s60, %29, 524288\n" \
  "s_lshl_b32 s61, s72, 1\n" \
  "s_add_u32 m0, s61, 32768\n" \
  "s_nop 0\n" \
  "buffer_load_dwordx4 v200, s[44:47], s60 offen lds\n" \
  "buffer_load_dwordx4 v201, s[44:47], s60 offen offset:1024 lds\n" \
  "buffer_load_dwordx4 v202, s[44:47], s60 offen offset:2048 lds\n" \
  "buffer_load_dwordx4 v203, s[44:47], s60 offen offset:3072 lds\n" \
  "s_add_u32 s60, %29, 1048576\n" \
  "s_lshl_b32 s61, s72, 1\n" \
  "s_add_u32 m0, s61, 49152\n" \
  "s_nop 0\n" \
  "buffer_load_dwordx4 v200, s[44:47], s60 offen lds\n" \
  "buffer_load_dwordx4 v201, s[44:47], s60 offen offset:1024 lds\n" \
  "buffer_load_dwordx4 v202, s[44:47], s60 offen offset:2048 lds\n" \
  "buffer_load_dwordx4 v203, s[44:47], s60 offen offset:3072 lds\n" \
  "s_add_u32 s60, %29, 1572864\n" \
  "s_lshl_b32 s61, s72, 1\n" \
  "s_add_u32 m0, s61, 65536\n" \
  "s_nop 0\n" \
  "buffer_load_dwordx4 v200, s[44:47], s60 offen lds\n" \
  "buffer_load_dwordx4 v201, s[44:47], s60 offen offset:1024 lds\n" \
  "buffer_load_dwordx4 v202, s[44:47], s60 offen offset:2048 lds\n" \
  "buffer_load_dwordx4 v203, s[44:47], s60 offen offset:3072 lds\n" \
  "s_add_u32 s60, %29, 2097152\n" \
  "s_lshl_b32 s61, s72, 1\n" \
  "s_add_u32 m0, s61, 81920\n" \
  "s_nop 0\n" \
  "buffer_load_dwordx4 v200, s[44:47], s60 offen lds\n" \
  "buffer_load_dwordx4 v201, s[44:47], s60 offen offset:1024 lds\n" \
  "buffer_load_dwordx4 v202, s[44:47], s60 offen offset:2048 lds\n" \
  "buffer_load_dwordx4 v203, s[44:47], s60 offen offset:3072 lds\n" \
  "s_add_u32 s60, %29, 2621440\n" \
  "s_lshl_b32 s61, s72, 1\n" \
  "s_add_u32 m0, s61, 98304\n" \
  "s_nop 0\n" \
  "buffer_load_dwordx4 v200, s[44:47], s60 offen lds\n" \
  "buffer_load_dwordx4 v201, s[44:47], s60 offen offset:1024 lds\n" \
  "buffer_load_dwordx4 v202, s[44:47], s60 offen offset:2048 lds\n" \
  "buffer_load_dwordx4 v203, s[44:47], s60 offen offset:3072 lds\n" \
  "s_add_u32 s60, %29, 3145728\n" \
  "s_lshl_b32 s61, s72, 1\n" \
  "s_add_u32 m0, s61, 114688\n" \
  "s_nop 0\n" \
  "buffer_load_dwordx4 v200, s[44:47], s60 offen lds\n" \
  "buffer_load_dwordx4 v201, s[44:47], s60 offen offset:1024 lds\n" \
  "buffer_load_dwordx4 v202, s[44:47], s60 offen offset:2048 lds\n" \
  "buffer_load_dwordx4 v203, s[44:47], s60 offen offset:3072 lds\n" \
  "s_add_u32 s60, %29, 3670016\n" \
  "s_lshl_b32 s61, s72, 1\n" \
  "s_add_u32 m0, s61, 131072\n" \
  "s_nop 0\n" \
  "buffer_load_dwordx4 v200, s[44:47], s60 offen lds\n" \
  "buffer_load_dwordx4 v201, s[44:47], s60 offen offset:1024 lds\n" \
  "buffer_load_dwordx4 v202, s[44:47], s60 offen offset:2048 lds\n" \
  "buffer_load_dwordx4 v203, s[44:47], s60 offen offset:3072 lds\n" \
  "s_add_u32 s60, %29, 4194304\n" \
  "s_lshl_b32 s61, s72, 1\n" \
  "s_add_u32 m0, s61, 147456\n" \
  "s_nop 0\n" \
  "buffer_load_dwordx4 v200, s[44:47], s60 offen lds\n" \
  "buffer_load_dwordx4 v201, s[44:47], s60 offen offset:1024 lds\n" \
  "buffer_load_dwordx4 v202, s[44:47], s60 offen offset:2048 lds\n" \
  "buffer_load_dwordx4 v203, s[44:47], s60 offen offset:3072 lds\n" \
  "s_mov_b64 s[44:45], %27\n" \
  "s_and_b32 s45, s45, 0xffff\n" \
  "s_lshr_b32 s60, s72, 3\n" \
  "s_mov_b32 m0, s72\n" \
  "s_nop 0\n" \
  "buffer_load_dwordx4 v206, s[44:47], s60 offen lds\n" \
  "s_add_u32 s60, s60, 128\n" \
  "s_add_u32 m0, s72, 1024\n" \
  "s_nop 0\n" \
  "buffer_load_dwordx4 v206, s[44:47], s60 offen lds\n" \
  "s_movk_i32 s60, 1024\n" \
  "s_movk_i32 m0, 8192\n" \
  "s_nop 0\n" \
  "buffer_load_dwordx4 v206, s[44:47], s60 offen lds\n" \
  "s_waitcnt vmcnt(27)\n" \
  "s_barrier\n" \
  "v_add_u32 v194, 16384, v204\n" \
  "ds_read_b64_tr_b16 a[0:1], v194\n" \
  "ds_read_b64_tr_b16 a[2:3], v194 offset:512\n" \
  "ds_read_b64_tr_b16 a[4:5], v194 offset:8192\n" \
  "ds_read_b64_tr_b16 a[6:7], v194 offset:8704\n" \
  "v_add_u32 v195, 16384, v205\n" \
  "ds_read_b64_tr_b16 a[72:73], v195\n" \
  "ds_read_b64_tr_b16 a[74:75], v195 offset:512\n" \
  "ds_read_b64_tr_b16 a[76:77], v195 offset:8192\n" \
  "ds_read_b64_tr_b16 a[78:79], v195 offset:8704\n" \
  "v_add_u32 v194, 32768, v204\n" \
  "ds_read_b64_tr_b16 a[8:9], v194\n" \
  "ds_read_b64_tr_b16 a[10:11], v194 offset:512\n" \
  "ds_read_b64_tr_b16 a[12:13], v194 offset:8192\n" \
  "ds_read_b64_tr_b16 a[14:15], v194 offset:8704\n" \
  "v_add_u32 v195, 32768, v205\n" \
  "ds_read_b64_tr_b16 a[80:81], v195\n" \
  "ds_read_b64_tr_b16 a[82:83], v195 offset:512\n" \
  "ds_read_b64_tr_b16 a[84:85], v195 offset:8192\n" \
  "ds_read_b64_tr_b16 a[86:87], v195 offset:8704\n" \
  "v_add_u32 v194, 49152, v204\n" \
  "ds_read_b64_tr_b16 a[16:17], v194\n" \
  "ds_read_b64_tr_b16 a[18:19], v194 offset:512\n" \
  "ds_read_b64_tr_b16 a[20:21], v194 offset:8192\n" \
  "ds_read_b64_tr_b16 a[22:23], v194 offset:8704\n" \
  "v_add_u32 v195, 49152, v205\n" \
  "ds_read_b64_tr_b16 a[88:89], v195\n" \
  "ds_read_b64_tr_b16 a[90:91], v195 offset:512\n" \
  "ds_read_b64_tr_b16 a[92:93], v195 offset:8192\n" \
  "ds_read_b64_tr_b16 a[94:95], v195 offset:8704\n" \
  "s_waitcnt lgkmcnt(0)\n" \
  "s_barrier\n" \
  "s_mov_b64 s[44:45], %26\n" \
  "s_and_b32 s45, s45, 0xffff\n" \
  "s_add_u32 s60, %29, 0\n" \
  "s_lshl_b32 s61, s72, 1\n" \
  "s_add_u32 m0, s61, 16384\n" \
  "s_nop 0\n" \
  "buffer_load_dwordx4 v200, s[44:47], s60 offen lds\n" \
  "buffer_load_dwordx4 v201, s[44:47], s60 offen offset:1024 lds\n" \
  "buffer_load_dwordx4 v202, s[44:47], s60 offen offset:2048 lds\n" \
  "buffer_load_dwordx4 v203, s[44:47], s60 offen offset:3072 lds\n" \
  "s_add_u32 s60, %29, 524288\n" \
  "s_lshl_b32 s61, s72, 1\n" \
  "s_add_u32 m0, s61, 32768\n" \
  "s_nop 0\n" \
  "buffer_load_dwordx4 v200, s[44:47], s60 offen lds\n" \
  "buffer_load_dwordx4 v201, s[44:47], s60 offen offset:1024 lds\n" \
  "buffer_load_dwordx4 v202, s[44:47], s60 offen offset:2048 lds\n" \
  "buffer_load_dwordx4 v203, s[44:47], s60 offen offset:3072 lds\n" \
  "s_add_u32 s60, %29, 1048576\n" \
  "s_lshl_b32 s61, s72, 1\n" \
  "s_add_u32 m0, s61, 49152\n" \
  "s_nop 0\n" \
  "buffer_load_dwordx4 v200, s[44:47], s60 offen lds\n" \
  "buffer_load_dwordx4 v201, s[44:47], s60 offen offset:1024 lds\n" \
  "buffer_load_dwordx4 v202, s[44:47], s60 offen offset:2048 lds\n" \
  "buffer_load_dwordx4 v203, s[44:47], s60 offen offset:3072 lds\n" \
  "v_readfirstlane_b32 s55, v196\n" \
  "s_add_u32 s55, s55, 1\n" \
  "s_waitcnt vmcnt(27)\n" \
  "s_barrier\n" \
  "v_add_u32 v194, 65536, v204\n" \
  "ds_read_b64_tr_b16 a[24:25], v194\n" \
  "ds_read_b64_tr_b16 a[26:27], v194 offset:512\n" \
  "ds_read_b64_tr_b16 a[28:29], v194 offset:8192\n" \
  "ds_read_b64_tr_b16 a[30:31], v194 offset:8704\n" \
  "v_add_u32 v195, 65536, v205\n" \
  "ds_read_b64_tr_b16 a[96:97], v195\n" \
  "ds_read_b64_tr_b16 a[98:99], v195 offset:512\n" \
  "ds_read_b64_tr_b16 a[100:101], v195 offset:8192\n" \
  "ds_read_b64_tr_b16 a[102:103], v195 offset:8704\n" \
  "v_add_u32 v194, 81920, v204\n" \
  "ds_read_b64_tr_b16 a[32:33], v194\n" \
  "ds_read_b64_tr_b16 a[34:35], v194 offset:512\n" \
  "ds_read_b64_tr_b16 a[36:37], v194 offset:8192\n" \
  "ds_read_b64_tr_b16 a[38:39], v194 offset:8704\n" \
  "v_add_u32 v195, 81920, v205\n" \
  "ds_read_b64_tr_b16 a[104:105], v195\n" \
  "ds_read_b64_tr_b16 a[106:107], v195 offset:512\n" \
  "ds_read_b64_tr_b16 a[108:109], v195 offset:8192\n" \
  "ds_read_b64_tr_b16 a[110:111], v195 offset:8704\n" \
  "v_add_u32 v194, 98304, v204\n" \
  "ds_read_b64_tr_b16 a[40:41], v194\n" \
  "ds_read_b64_tr_b16 a[42:43], v194 offset:512\n" \
  "ds_read_b64_tr_b16 a[44:45], v194 offset:8192\n" \
  "ds_read_b64_tr_b16 a[46:47], v194 offset:8704\n" \
  "v_add_u32 v195, 98304, v205\n" \
  "ds_read_b64_tr_b16 a[112:113], v195\n" \
  "ds_read_b64_tr_b16 a[114:115], v195 offset:512\n" \
  "ds_read_b64_tr_b16 a[116:117], v195 offset:8192\n" \
  "ds_read_b64_tr_b16 a[118:119], v195 offset:8704\n" \
  "s_waitcnt lgkmcnt(0)\n" \
  "s_barrier\n" \
  "s_add_u32 s60, %29, 1572864\n" \
  "s_lshl_b32 s61, s72, 1\n" \
  "s_add_u32 m0, s61, 65536\n" \
  "s_nop 0\n" \
  "buffer_load_dwordx4 v200, s[44:47], s60 offen lds\n" \
  "buffer_load_dwordx4 v201, s[44:47], s60 offen offset:1024 lds\n" \
  "buffer_load_dwordx4 v202, s[44:47], s60 offen offset:2048 lds\n" \
  "buffer_load_dwordx4 v203, s[44:47], s60 offen offset:3072 lds\n" \
  "s_add_u32 s60, %29, 2097152\n" \
  "s_lshl_b32 s61, s72, 1\n" \
  "s_add_u32 m0, s61, 81920\n" \
  "s_nop 0\n" \
  "buffer_load_dwordx4 v200, s[44:47], s60 offen lds\n" \
  "buffer_load_dwordx4 v201, s[44:47], s60 offen offset:1024 lds\n" \
  "buffer_load_dwordx4 v202, s[44:47], s60 offen offset:2048 lds\n" \
  "buffer_load_dwordx4 v203, s[44:47], s60 offen offset:3072 lds\n" \
  "s_add_u32 s60, %29, 2621440\n" \
  "s_lshl_b32 s61, s72, 1\n" \
  "s_add_u32 m0, s61, 98304\n" \
  "s_nop 0\n" \
  "buffer_load_dwordx4 v200, s[44:47], s60 offen lds\n" \
  "buffer_load_dwordx4 v201, s[44:47], s60 offen offset:1024 lds\n" \
  "buffer_load_dwordx4 v202, s[44:47], s60 offen offset:2048 lds\n" \
  "buffer_load_dwordx4 v203, s[44:47], s60 offen offset:3072 lds\n" \
  "s_waitcnt vmcnt(27)\n" \
  "s_barrier\n" \
  "v_add_u32 v194, 114688, v204\n" \
  "ds_read_b64_tr_b16 a[48:49], v194\n" \
  "ds_read_b64_tr_b16 a[50:51], v194 offset:512\n" \
  "ds_read_b64_tr_b16 a[52:53], v194 offset:8192\n" \
  "ds_read_b64_tr_b16 a[54:55], v194 offset:8704\n" \
  "v_add_u32 v195, 114688, v205\n" \
  "ds_read_b64_tr_b16 a[120:121], v195\n" \
  "ds_read_b64_tr_b16 a[122:123], v195 offset:512\n" \
  "ds_read_b64_tr_b16 a[124:125], v195 offset:8192\n" \
  "ds_read_b64_tr_b16 a[126:127], v195 offset:8704\n" \
  "v_add_u32 v194, 131072, v204\n" \
  "ds_read_b64_tr_b16 a[56:57], v194\n" \
  "ds_read_b64_tr_b16 a[58:59], v194 offset:512\n" \
  "ds_read_b64_tr_b16 a[60:61], v194 offset:8192\n" \
  "ds_read_b64_tr_b16 a[62:63], v194 offset:8704\n" \
  "v_add_u32 v195, 131072, v205\n" \
  "ds_read_b64_tr_b16 a[128:129], v195\n" \
  "ds_read_b64_tr_b16 a[130:131], v195 offset:512\n" \
  "ds_read_b64_tr_b16 a[132:133], v195 offset:8192\n" \
  "ds_read_b64_tr_b16 a[134:135], v195 offset:8704\n" \
  "v_add_u32 v194, 147456, v204\n" \
  "ds_read_b64_tr_b16 a[64:65], v194\n" \
  "ds_read_b64_tr_b16 a[66:67], v194 offset:512\n" \
  "ds_read_b64_tr_b16 a[68:69], v194 offset:8192\n" \
  "ds_read_b64_tr_b16 a[70:71], v194 offset:8704\n" \
  "v_add_u32 v195, 147456, v205\n" \
  "ds_read_b64_tr_b16 a[136:137], v195\n" \
  "ds_read_b64_tr_b16 a[138:139], v195 offset:512\n" \
  "ds_read_b64_tr_b16 a[140:141], v195 offset:8192\n" \
  "ds_read_b64_tr_b16 a[142:143], v195 offset:8704\n" \
  "s_waitcnt lgkmcnt(0)\n" \
  "s_barrier\n" \
  "s_add_u32 s60, %29, 3145728\n" \
  "s_lshl_b32 s61, s72, 1\n" \
  "s_add_u32 m0, s61, 114688\n" \
  "s_nop 0\n" \
  "buffer_load_dwordx4 v200, s[44:47], s60 offen lds\n" \
  "buffer_load_dwordx4 v201, s[44:47], s60 offen offset:1024 lds\n" \
  "buffer_load_dwordx4 v202, s[44:47], s60 offen offset:2048 lds\n" \
  "buffer_load_dwordx4 v203, s[44:47], s60 offen offset:3072 lds\n" \
  "s_add_u32 s60, %29, 3670016\n" \
  "s_lshl_b32 s61, s72, 1\n" \
  "s_add_u32 m0, s61, 131072\n" \
  "s_nop 0\n" \
  "buffer_load_dwordx4 v200, s[44:47], s60 offen lds\n" \
  "buffer_load_dwordx4 v201, s[44:47], s60 offen offset:1024 lds\n" \
  "buffer_load_dwordx4 v202, s[44:47], s60 offen offset:2048 lds\n" \
  "buffer_load_dwordx4 v203, s[44:47], s60 offen offset:3072 lds\n" \
  "s_add_u32 s60, %29, 4194304\n" \
  "s_lshl_b32 s61, s72, 1\n" \
  "s_add_u32 m0, s61, 147456\n" \
  "s_nop 0\n" \
  "buffer_load_dwordx4 v200, s[44:47], s60 offen lds\n" \
  "buffer_load_dwordx4 v201, s[44:47], s60 offen offset:1024 lds\n" \
  "buffer_load_dwordx4 v202, s[44:47], s60 offen offset:2048 lds\n" \
  "buffer_load_dwordx4 v203, s[44:47], s60 offen offset:3072 lds\n" \
  "buffer_load_dwordx2 v[198:199], off, s[40:43], %39 sc1\n" \
  "s_waitcnt vmcnt(25)\n" \
  "s_barrier\n" \
  "v_add_u32 v195, 16384, v205\n" \
  "ds_read_b64_tr_b16 a[216:217], v195\n" \
  "ds_read_b64_tr_b16 a[218:219], v195 offset:512\n" \
  "ds_read_b64_tr_b16 a[220:221], v195 offset:8192\n" \
  "ds_read_b64_tr_b16 a[222:223], v195 offset:8704\n" \
  "v_add_u32 v195, 32768, v205\n" \
  "ds_read_b64_tr_b16 a[224:225], v195\n" \
  "ds_read_b64_tr_b16 a[226:227], v195 offset:512\n" \
  "ds_read_b64_tr_b16 a[228:229], v195 offset:8192\n" \
  "ds_read_b64_tr_b16 a[230:231], v195 offset:8704\n" \
  "v_add_u32 v195, 49152, v205\n" \
  "ds_read_b64_tr_b16 a[232:233], v195\n" \
  "ds_read_b64_tr_b16 a[234:235], v195 offset:512\n" \
  "ds_read_b64_tr_b16 a[236:237], v195 offset:8192\n" \
  "ds_read_b64_tr_b16 a[238:239], v195 offset:8704\n" \
  "s_waitcnt lgkmcnt(0)\n" \
  "s_waitcnt vmcnt(13)\n" \
  "s_barrier\n" \
  "v_add_u32 v195, 65536, v205\n" \
  "ds_read_b64_tr_b16 a[240:241], v195\n" \
  "ds_read_b64_tr_b16 a[242:243], v195 offset:512\n" \
  "ds_read_b64_tr_b16 a[244:245], v195 offset:8192\n" \
  "ds_read_b64_tr_b16 a[246:247], v195 offset:8704\n" \
  "v_add_u32 v194, 81920, v204\n" \
  "ds_read_b64_tr_b16 a[176:177], v194\n" \
  "ds_read_b64_tr_b16 a[178:179], v194 offset:512\n" \
  "ds_read_b64_tr_b16 a[180:181], v194 offset:8192\n" \
  "ds_read_b64_tr_b16 a[182:183], v194 offset:8704\n" \
  "v_add_u32 v195, 81920, v205\n" \
  "ds_read_b64_tr_b16 a[248:249], v195\n" \
  "ds_read_b64_tr_b16 a[250:251], v195 offset:512\n" \
  "ds_read_b64_tr_b16 a[252:253], v195 offset:8192\n" \
  "ds_read_b64_tr_b16 a[254:255], v195 offset:8704\n" \
  "v_add_u32 v194, 98304, v204\n" \
  "ds_read_b64_tr_b16 a[184:185], v194\n" \
  "ds_read_b64_tr_b16 a[186:187], v194 offset:512\n" \
  "ds_read_b64_tr_b16 a[188:189], v194 offset:8192\n" \
  "ds_read_b64_tr_b16 a[190:191], v194 offset:8704\n" \
  "v_add_u32 v195, 98304, v205\n" \
  "ds_read_b64_tr_b16 v[0:1], v195\n" \
  "ds_read_b64_tr_b16 v[2:3], v195 offset:512\n" \
  "ds_read_b64_tr_b16 v[4:5], v195 offset:8192\n" \
  "ds_read_b64_tr_b16 v[6:7], v195 offset:8704\n" \
  "s_waitcnt lgkmcnt(0)\n" \
  "s_waitcnt vmcnt(1)\n" \
  "s_barrier\n" \
  "v_add_u32 v194, 114688, v204\n" \
  "ds_read_b64_tr_b16 a[192:193], v194\n" \
  "ds_read_b64_tr_b16 a[194:195], v194 offset:512\n" \
  "ds_read_b64_tr_b16 a[196:197], v194 offset:8192\n" \
  "ds_read_b64_tr_b16 a[198:199], v194 offset:8704\n" \
  "v_add_u32 v195, 114688, v205\n" \
  "ds_read_b64_tr_b16 v[8:9], v195\n" \
  "ds_read_b64_tr_b16 v[10:11], v195 offset:512\n" \
  "ds_read_b64_tr_b16 v[12:13], v195 offset:8192\n" \
  "ds_read_b64_tr_b16 v[14:15], v195 offset:8704\n" \
  "v_add_u32 v194, 131072, v204\n" \
  "ds_read_b64_tr_b16 a[200:201], v194\n" \
  "ds_read_b64_tr_b16 a[202:203], v194 offset:512\n" \
  "ds_read_b64_tr_b16 a[204:205], v194 offset:8192\n" \
  "ds_read_b64_tr_b16 a[206:207], v194 offset:8704\n" \
  "v_add_u32 v195, 131072, v205\n" \
  "ds_read_b64_tr_b16 v[16:17], v195\n" \
  "ds_read_b64_tr_b16 v[18:19], v195 offset:512\n" \
  "ds_read_b64_tr_b16 v[20:21], v195 offset:8192\n" \
  "ds_read_b64_tr_b16 v[22:23], v195 offset:8704\n" \
  "v_add_u32 v194, 147456, v204\n" \
  "ds_read_b64_tr_b16 a[208:209], v194\n" \
  "ds_read_b64_tr_b16 a[210:211], v194 offset:512\n" \
  "ds_read_b64_tr_b16 a[212:213], v194 offset:8192\n" \
  "ds_read_b64_tr_b16 a[214:215], v194 offset:8704\n" \
  "v_add_u32 v195, 147456, v205\n" \
  "ds_read_b64_tr_b16 v[24:25], v195\n" \
  "ds_read_b64_tr_b16 v[26:27], v195 offset:512\n" \
  "ds_read_b64_tr_b16 v[28:29], v195 offset:8192\n" \
  "ds_read_b64_tr_b16 v[30:31], v195 offset:8704\n" \
  "s_waitcnt lgkmcnt(0)\n" \
  "s_waitcnt vmcnt(0)\n" \
  "v_readfirstlane_b32 s96, v198\n" \
  "v_readfirstlane_b32 s63, v199\n" \
  "s_cmp_eq_u32 s63, s55\n" \
  "s_cselect_b32 s96, s96, 0\n" \
  "s_cmp_ge_u32 s96, 0xc0000000\n" \
  "s_cbranch_scc1 .LX_go_x_%=\n" \
  "s_mov_b32 s69, 0\n" \
  ".LX_go_%=:\n" \
  "buffer_load_dwordx2 v[196:197], off, s[40:43], %39 sc1\n" \
  "s_waitcnt vmcnt(0)\n" \
  "v_readfirstlane_b32 s96, v196\n" \
  "v_readfirstlane_b32 s63, v197\n" \
  "s_cmp_eq_u32 s63, s55\n" \
  "s_cselect_b32 s96, s96, 0\n" \
  "s_cmp_ge_u32 s96, 0xc0000000\n" \
  "s_cbranch_scc1 .LX_go_x_%=\n" \
  "s_sleep 8\n" \
  "s_add_u32 s69, s69, 1\n" \
  "s_and_b32 s63, s69, 1023\n" \
  "s_cbranch_scc1 .LX_go_%=\n" \
  "s_cmp_gt_u32 s69, 2097152\n" \
  "s_cbranch_scc1 .LX_go_set_%=\n" \
  "buffer_load_dwordx2 v[196:197], off, s[40:43], %40 offset:128 sc1\n" \
  "s_waitcnt vmcnt(0)\n" \
  "v_readfirstlane_b32 s63, v197\n" \
  "s_cmp_eq_u32 s63, s55\n" \
  "s_cbranch_scc0 .LX_go_%=\n" \
  "s_branch .LX_go_x_%=\n" \
  ".LX_go_set_%=:\n" \
  "v_mov_b32 v196, 1\n" \
  "v_mov_b32 v197, s55\n" \
  "buffer_store_dwordx2 v[196:197], off, s[40:43], %40 offset:128 sc1\n" \
  "s_waitcnt vmcnt(0)\n" \
  ".LX_go_x_%=:\n" \
  "s_and_b32 s60, s96, 2\n" \
  "s_lshl_b32 s60, s60, 12\n" \
  "s_xor_b32 s74, %35, s60\n" \
  "s_xor_b32 s75, %38, s60\n" \
  "s_bfe_u32 s56, s96, 0x30010\n" \
  "s_lshl_b32 s56, s56, 12\n" \
  "s_or_b32 s56, s56, 0x200000\n" \
  "s_mov_b32 s57, 1052672\n" \
  "s_lshl_b32 s61, %34, 2\n" \
  "s_lshr_b32 s51, s96, s61\n" \
  "s_and_b32 s51, s51, 7\n" \
  "s_lshl_b32 s51, s51, 12\n" \
  "s_xor_b32 s51, s51, 4210688\n" \
  "s_mov_b32 s71, 0\n" \
  "s_add_u32 s45, s51, 886784\n" \
  "s_mov_b32 s46, -1\n" \
  "v_mov_b64 v[32:33], -1\n" \
  "v_mov_b64 v[34:35], -1\n" \
  "v_and_b32 v194, 3, v246\n" \
  "v_lshlrev_b32 v194, 2, v194\n" \
  "v_lshrrev_b32 v194, v194, s96\n" \
  "v_and_b32 v194, 7, v194\n" \
  "v_lshlrev_b32 v194, 12, v194\n" \
  "v_and_b32 v246, -4, v246\n" \
  "v_xor_b32 v246, v246, v194\n" \
  "v_and_b32 v194, 3, v247\n" \
  "v_lshlrev_b32 v194, 2, v194\n" \
  "v_lshrrev_b32 v194, v194, s96\n" \
  "v_and_b32 v194, 7, v194\n" \
  "v_lshlrev_b32 v194, 12, v194\n" \
  "v_and_b32 v247, -4, v247\n" \
  "v_xor_b32 v247, v247, v194\n" \
  "s_xor_b32 s62, s56, s57\n" \
  "s_nop 1\n" \
  "s_andn2_b32 s60, 2, s96\n" \
  "s_lshl_b32 s60, s60, 12\n" \
  "s_xor_b32 s60, s60, 8192\n" \
  "s_xor_b32 s58, %31, s60\n" \
  "s_mov_b32 s49, 0\n" \
  "s_add_u32 s47, %45, s60\n" \
  "s_add_u32 s97, s60, 8388608\n" \
  "s_xor_b32 s50, %32, s60\n" \
  "s_mov_b32 s48, 0\n" \
  "s_mov_b32 s54, 0\n" \
  "s_cmp_eq_u32 %28, 0\n" \
  "s_cselect_b64 s[88:89], -1, 0\n" \
  "s_mov_b32 s68, 0\n" \
  "s_mov_b32 s70, 1\n" \
  "s_mov_b32 s73, 0\n" \
  "s_mov_b32 s44, 0\n" \
  "s_mov_b32 s53, 0\n" \
  "s_memrealtime s[86:87]\n" \
  "s_memtime s[76:77]\n" \
  "s_waitcnt lgkmcnt(0)\n" \
  "s_mov_b32 s90, s76\n" \
  "s_mov_b32 s87, 340\n" \
  "s_mov_b32 s64, 0x3e0293ee\n" \
  "s_mov_b32 s65, 0xbe0293ee\n" \
  "v_mov_b64 v[44:45], 0\n" \
  "v_mov_b64 v[46:47], 0\n" \
  "v_mov_b64 v[48:49], 0\n" \
  "v_mov_b64 v[50:51], 0\n" \
  "v_mov_b64 v[52:53], 0\n" \
  "v_mov_b64 v[54:55], 0\n" \
  "v_mov_b64 v[56:57], 0\n" \
  "v_mov_b64 v[58:59], 0\n" \
  "v_mov_b32 v40, 0\n" \
  "v_mov_b32 v41, s55\n" \
  "v_mov_b64 v[42:43], v[40:41]\n" \
  "s_mov_b64 s[92:93], 0\n" \
  "s_mov_b32 s92, 0xffff0000\n" \
  "s_lshr_b32 s60, s72, 1\n" \
  "s_add_u32 s60, s60, 32768\n" \
  "v_add_u32 v178, s60, v240\n" \
  "v_xor_b32 v246, 1073741824, v246\n" \
  "v_xor_b32 v247, 1073741824, v247\n" \
  "v_mov_b32 v182, 1065369472\n" \
  "v_mov_b32 v183, 1065369472\n" \
  "s_and_b32 s60, s72, 2048\n" \
  "s_add_u32 s50, s50, s60\n" \
  "s_lshr_b32 s60, s60, 1\n" \
  "v_add_u32 v179, s60, v244\n" \
  "s_lshr_b32 s60, s72, 9\n" \
  "s_add_u32 s60, s60, 17408\n" \
  "v_mov_b32 v184, s60\n" \
  "s_mov_b32 s52, 0\n" \
  "s_barrier\n" \
  "ds_write_b32 v184, v40\n" \
  "s_branch .LX_xr_%=\n" \
  ".LX_dry_b_%=:\n" \
  "s_waitcnt vmcnt(0)\n" \
  "s_barrier\n" \
  "ds_read_b128 v[64:67], v178\n" \
  "ds_read_b128 v[68:71], v178 offset:2048\n" \
  "ds_read_b128 v[72:75], v178 offset:4096\n" \
  "ds_read_b128 v[76:79], v178 offset:6144\n" \
  "v_xor_b32 v178, 8192, v178\n" \
  "s_mov_b32 s67, 2130706432\n" \
  "s_waitcnt vmcnt(0) lgkmcnt(0)\n" \
  "s_barrier\n" \
  "s_branch .LX_p_%=\n" \
  "s_nop 7\n" \
  ".p2align 6\n" \
  ".fill (((44 - (.LX_p_%= - .LX_rot_%=)) & 63) >> 2), 4, 0xBF800000\n" \
  ".LX_rot_%=:\n" \
  "s_xor_b32 s51, s51, s56\n" \
  "s_xor_b32 s71, s71, s56\n" \
  "v_xor_b32 v246, s56, v246\n" \
  "v_xor_b32 v247, s56, v247\n" \
  "s_mov_b32 s60, s56\n" \
  "s_mov_b32 s56, s57\n" \
  "s_mov_b32 s57, s60\n" \
  "s_add_u32 s59, s51, 819200\n" \
  "s_mov_b32 m0, 0\n" \
  "s_mov_b32 s70, 0\n" \
  "s_mov_b32 s68, 0\n" \
  "s_waitcnt lgkmcnt(0)\n" \
  "s_cmp_lg_u32 %28, 0\n" \
  "s_cbranch_scc1 .LX_g3_%=\n" \
  "s_mov_b32 s69, 0\n" \
  ".LX_tw_%=:\n" \
  "v_readfirstlane_b32 s63, v36\n" \
  "s_cmp_lg_u32 s63, s48\n" \
  "s_cbranch_scc1 .LX_tw_re_%=\n" \
  "v_readfirstlane_b32 s53, v38\n" \
  ".LX_rt_%=:\n" \
  "s_memrealtime s[60:61]\n" \
  "s_add_u32 s73, s53, s44\n" \
  "s_waitcnt lgkmcnt(0)\n" \
  "s_sub_u32 s62, s73, s60\n" \
  "s_cmp_ge_u32 s62, 2000\n" \
  "s_cbranch_scc1 .LX_lat_%=\n" \
  "s_sub_u32 s62, s62, 30\n" \
  ".LX_slp_%=:\n" \
  "s_cmp_le_i32 s62, 0\n" \
  "s_cbranch_scc1 .LX_clk_%=\n" \
  "s_sleep 1\n" \
  "s_sub_u32 s62, s62, 3\n" \
  "s_branch .LX_slp_%=\n" \
  ".LX_clk_%=:\n" \
  "s_memrealtime s[60:61]\n" \
  "s_waitcnt lgkmcnt(0)\n" \
  "s_sub_u32 s62, s73, s60\n" \
  "s_cmp_gt_i32 s62, 21\n" \
  "s_cbranch_scc1 .LX_clk_%=\n" \
  "s_cmp_le_i32 s62, 11\n" \
  "s_cbranch_scc1 .LX_clk2_%=\n" \
  "s_memrealtime s[76:77]\n" \
  "s_nop 4\n" \
  "s_memrealtime s[78:79]\n" \
  "s_nop 4\n" \
  "s_memrealtime s[80:81]\n" \
  "s_nop 4\n" \
  "s_memrealtime s[82:83]\n" \
  "s_waitcnt lgkmcnt(0)\n" \
  "s_mov_b32 s77, 1\n" \
  "s_cmp_eq_u32 s78, s76\n" \
  "s_addc_u32 s77, s77, 0\n" \
  "s_cmp_eq_u32 s80, s76\n" \
  "s_addc_u32 s77, s77, 0\n" \
  "s_cmp_eq_u32 s82, s76\n" \
  "s_addc_u32 s77, s77, 0\n" \
  "s_mul_i32 s77, s77, 24\n" \
  "s_lshr_b32 s78, s87, 2\n" \
  "s_sub_u32 s77, s78, s77\n" \
  "s_max_i32 s77, s77, 0\n" \
  "s_sub_u32 s76, s73, s76\n" \
  "s_mul_i32 s76, s76, s87\n" \
  "s_ashr_i32 s76, s76, 4\n" \
  "s_sub_u32 s76, s76, s77\n" \
  "s_sub_u32 s76, s76, 200\n" \
  "s_max_i32 s76, s76, 0\n" \
  "s_min_i32 s76, s76, 320\n" \
  "s_lshr_b32 s76, s76, 3\n" \
  "s_lshl_b32 s76, s76, 2\n" \
  "s_mov_b32 s68, 1\n" \
  "s_getpc_b64 s[78:79]\n" \
  "s_sub_u32 s76, 180, s76\n" \
  "s_add_u32 s78, s78, s76\n" \
  "s_addc_u32 s79, s79, 0\n" \
  "s_setpc_b64 s[78:79]\n" \
  "s_nop 1\n" \
  "s_nop 1\n" \
  "s_nop 1\n" \
  "s_nop 1\n" \
  "s_nop 1\n" \
  "s_nop 1\n" \
  "s_nop 1\n" \
  "s_nop 1\n" \
  "s_nop 1\n" \
  "s_nop 1\n" \
  "s_nop 1\n" \
  "s_nop 1\n" \
  "s_nop 1\n" \
  "s_nop 1\n" \
  "s_nop 1\n" \
  "s_nop 1\n" \
  "s_nop 1\n" \
  "s_nop 1\n" \
  "s_nop 1\n" \
  "s_nop 1\n" \
  "s_nop 1\n" \
  "s_nop 1\n" \
  "s_nop 1\n" \
  "s_nop 1\n" \
  "s_nop 1\n" \
  "s_nop 1\n" \
  "s_nop 1\n" \
  "s_nop 1\n" \
  "s_nop 1\n" \
  "s_nop 1\n" \
  "s_nop 1\n" \
  "s_nop 1\n" \
  "s_nop 1\n" \
  "s_nop 1\n" \
  "s_nop 1\n" \
  "s_nop 1\n" \
  "s_nop 1\n" \
  "s_nop 1\n" \
  "s_nop 1\n" \
  "s_nop 1\n" \
  ".LX_noclk_%=:\n" \
  "buffer_load_dwordx4 v240, s[40:43], s59 offen sc1 lds\n" \
  "s_memtime s[66:67]\n" \
  "buffer_load_dwordx4 v240, s[40:43], s59 offen offset:1024 sc1 lds\n" \
  "buffer_load_dwordx4 v240, s[40:43], s59 offen offset:2048 sc1 lds\n" \
  "buffer_load_dwordx4 v240, s[40:43], s59 offen offset:3072 sc1 lds\n" \
  "s_add_u32 s60, s59, 32768\n" \
  "s_movk_i32 m0, 4096\n" \
  "s_nop 0\n" \
  "buffer_load_dwordx4 v240, s[40:43], s60 offen sc1 lds\n" \
  "buffer_load_dwordx4 v240, s[40:43], s60 offen offset:1024 sc1 lds\n" \
  "buffer_load_dwordx4 v240, s[40:43], s60 offen offset:2048 sc1 lds\n" \
  "buffer_load_dwordx4 v240, s[40:43], s60 offen offset:3072 sc1 lds\n" \
  "s_add_u32 s60, s59, 65536\n" \
  "s_movk_i32 m0, 8192\n" \
  "s_nop 0\n" \
  "buffer_load_dwordx4 v240, s[40:43], s60 offen sc1 lds\n" \
  ".LX_g3_%=:\n" \
  "s_waitcnt vmcnt(6)\n" \
  "s_barrier\n" \
  ".LX_p_%=:\n" \
  "ds_read_b128 v[100:103], v240\n" \
  "ds_read_b128 v[104:107], v240 offset:1024\n" \
  "ds_read_b128 v[108:111], v240 offset:2048\n" \
  "s_waitcnt lgkmcnt(2)\n" \
  "v_smfmac_f32_16x16x64_bf16 v[44:47], v[100:103], a[0:7], v239\n" \
  "s_waitcnt lgkmcnt(1)\n" \
  "v_smfmac_f32_16x16x64_bf16 v[44:47], v[104:107], a[8:15], v239\n" \
  "s_waitcnt vmcnt(0)\n" \
  "s_barrier\n" \
  "v_smfmac_f32_16x16x64_bf16 v[48:51], v[100:103], a[72:79], v239\n" \
  "ds_read_b128 v[112:115], v240 offset:3072\n" \
  "ds_read_b128 v[116:119], v240 offset:4096\n" \
  "s_waitcnt lgkmcnt(2)\n" \
  "v_smfmac_f32_16x16x64_bf16 v[44:47], v[108:111], a[16:23], v239\n" \
  "ds_read_b128 v[120:123], v240 offset:6144\n" \
  "v_smfmac_f32_16x16x64_bf16 v[48:51], v[104:107], a[80:87], v239\n" \
  "ds_read_b128 v[124:127], v240 offset:7168\n" \
  "v_smfmac_f32_16x16x64_bf16 v[48:51], v[108:111], a[88:95], v239\n" \
  "ds_read_b128 v[128:131], v240 offset:5120\n" \
  "s_waitcnt lgkmcnt(4)\n" \
  "v_smfmac_f32_16x16x64_bf16 v[48:51], v[112:115], a[96:103], v239\n" \
  "s_waitcnt lgkmcnt(3)\n" \
  "v_smfmac_f32_16x16x64_bf16 v[48:51], v[116:119], a[104:111], v239\n" \
  "v_smfmac_f32_16x16x64_bf16 v[44:47], v[112:115], a[24:31], v239\n" \
  "ds_read_b128 v[132:135], v240 offset:8192\n" \
  "v_smfmac_f32_16x16x64_bf16 v[44:47], v[116:119], a[32:39], v239\n" \
  "s_waitcnt lgkmcnt(1)\n" \
  "v_smfmac_f32_16x16x64_bf16 v[44:47], v[128:131], a[40:47], v239\n" \
  "v_smfmac_f32_16x16x64_bf16 v[44:47], v[120:123], a[48:55], v239\n" \
  "v_smfmac_f32_16x16x64_bf16 v[44:47], v[124:127], a[56:63], v239\n" \
  "s_waitcnt lgkmcnt(0)\n" \
  "v_smfmac_f32_16x16x64_bf16 v[44:47], v[132:135], a[64:71], v239\n" \
  "s_nop 2\n" \
  "v_smfmac_f32_16x16x64_bf16 v[48:51], v[128:131], a[112:119], v239\n" \
  "s_nop 2\n" \
  "v_smfmac_f32_16x16x64_bf16 v[48:51], v[120:123], a[120:127], v239\n" \
  "v_add_f32 v45, v45, v47\n" \
  "v_smfmac_f32_16x16x64_bf16 v[48:51], v[124:127], a[128:135], v239\n" \
  "v_add_f32 v44, v44, v46\n" \
  "v_smfmac_f32_16x16x64_bf16 v[48:51], v[132:135], a[136:143], v239\n" \
  "s_nop 1\n" \
  "v_cmp_u_f32 vcc, v44, v45\n" \
  "v_smfmac_f32_16x16x64_bf16 v[52:55], v[116:119], a[176:183], v239\n" \
  "s_cbranch_vccnz .LX_slow_%=\n" \
  ".LX_back_%=:\n" \
  "s_cmp_eq_u32 s48, 63\n" \
  "v_mul_f32 v44, s64, v44\n" \
  "v_mul_f32 v45, s64, v45\n" \
  "v_add_f32 v48, v48, v50\n" \
  "v_add_f32 v49, v49, v51\n" \
  "v_cvt_pk_bf16_f32 v44, v44, v45\n" \
  "v_mul_f32 v48, s64, v48\n" \
  "v_smfmac_f32_16x16x64_bf16 v[56:59], v[100:103], a[216:223], v239\n" \
  "v_mul_f32 v49, s64, v49\n" \
  "v_smfmac_f32_16x16x64_bf16 v[52:55], v[128:131], a[184:191], v239\n" \
  "ds_write_b32 v241, v44\n" \
  "v_cvt_pk_bf16_f32 v48, v48, v49\n" \
  "v_smfmac_f32_16x16x64_bf16 v[52:55], v[120:123], a[192:199], v239\n" \
  "ds_write_b32 v241, v48 offset:256\n" \
  "v_smfmac_f32_16x16x64_bf16 v[52:55], v[124:127], a[200:207], v239\n" \
  "v_smfmac_f32_16x16x64_bf16 v[56:59], v[104:107], a[224:231], v239\n" \
  "v_smfmac_f32_16x16x64_bf16 v[56:59], v[108:111], a[232:239], v239\n" \
  "v_smfmac_f32_16x16x64_bf16 v[52:55], v[132:135], a[208:215], v239\n" \
  "s_waitcnt lgkmcnt(0)\n" \
  "s_barrier\n" \
  "v_smfmac_f32_16x16x64_bf16 v[56:59], v[112:115], a[240:247], v239\n" \
  "ds_read_b64_tr_b16 v[100:101], v243\n" \
  "v_smfmac_f32_16x16x64_bf16 v[56:59], v[116:119], a[248:255], v239\n" \
  "ds_read_b64_tr_b16 v[106:107], v243 offset:1280\n" \
  "ds_read_b64_tr_b16 v[110:111], v243 offset:1792\n" \
  "v_smfmac_f32_16x16x64_bf16 v[56:59], v[128:131], v[0:7], v239\n" \
  "ds_read_b64_tr_b16 v[112:113], v243 offset:512\n" \
  "ds_read_b64_tr_b16 v[102:103], v243 offset:256\n" \
  "v_smfmac_f32_16x16x64_bf16 v[56:59], v[120:123], v[8:15], v239\n" \
  "ds_read_b64_tr_b16 v[114:115], v243 offset:768\n" \
  "ds_read_b64_tr_b16 v[104:105], v243 offset:1024\n" \
  "v_smfmac_f32_16x16x64_bf16 v[56:59], v[124:127], v[16:23], v239\n" \
  "ds_read_b64_tr_b16 v[108:109], v243 offset:1536\n" \
  "v_add_f32 v52, v52, v54\n" \
  "v_smfmac_f32_16x16x64_bf16 v[56:59], v[132:135], v[24:31], v239\n" \
  "s_waitcnt lgkmcnt(3)\n" \
  "v_mfma_f32_16x16x32_bf16 v[116:119], v[64:67], v[100:103], 0\n" \
  "s_barrier\n" \
  "ds_read_b64 v[188:189], v255\n" \
  "s_waitcnt lgkmcnt(3)\n" \
  "v_mfma_f32_16x16x32_bf16 v[116:119], v[68:71], v[112:115], v[116:119]\n" \
  "s_waitcnt lgkmcnt(2)\n" \
  "v_add_f32 v53, v53, v55\n" \
  "v_add_f32 v56, v56, v58\n" \
  "v_mfma_f32_16x16x32_bf16 v[116:119], v[72:75], v[104:107], v[116:119]\n" \
  "s_waitcnt lgkmcnt(1)\n" \
  "v_add_f32 v57, v57, v59\n" \
  "v_cvt_pk_bf16_f32 v56, v56, v57\n" \
  "v_mfma_f32_16x16x32_bf16 v[116:119], v[76:79], v[108:111], v[116:119]\n" \
  "ds_write_b32 v242, v56 offset:256\n" \
  "s_waitcnt lgkmcnt(1)\n" \
  "v_add_f32 v53, v53, v189\n" \
  "v_add_f32 v52, v52, v188\n" \
  "v_cvt_pk_bf16_f32 v52, v52, v53\n" \
  "ds_write_b32 v242, v52\n" \
  "s_nop 1\n" \
  "v_exp_f32 v101, v117\n" \
  "v_exp_f32 v102, v118\n" \
  "v_exp_f32 v100, v116\n" \
  "v_exp_f32 v103, v119\n" \
  "v_cvt_pk_bf16_f32 v188, v100, v101\n" \
  "v_cvt_pk_bf16_f32 v189, v102, v103\n" \
  "s_nop 1\n" \
  "v_mfma_f32_16x16x16_bf16 v[100:103], v[182:183], v[188:189], 0\n" \
  "buffer_store_dwordx2 v[188:189], v246, s[40:43], 0 offen sc1\n" \
  "buffer_store_dwordx2 v[188:189], v247, s[40:43], 0 offen sc1\n" \
  "s_waitcnt lgkmcnt(0)\n" \
  "s_barrier\n" \
  "s_nop 3\n" \
  "ds_write_b32 v248, v100\n" \
  "s_waitcnt lgkmcnt(0)\n" \
  "s_barrier\n" \
  "s_cbranch_scc1 .LX_xr_%=\n" \
  "s_memrealtime s[98:99]\n" \
  "s_memtime s[84:85]\n" \
  ".LX_loop_%=:\n" \
  "s_cmp_ge_u32 s48, 55\n" \
  "s_cbranch_scc1 .LX_xl_%=\n" \
  ".LX_xr_b_%=:\n" \
  "s_add_u32 s95, s48, 1\n" \
  "s_and_b32 s76, s95, 3\n" \
  "s_lshl_b32 s95, s95, 5\n" \
  "s_mul_i32 s76, s76, 16\n" \
  "s_sub_u32 s95, s95, s76\n" \
  "s_add_u32 s95, s95, s45\n" \
  "s_mov_b64 exec, s[88:89]\n" \
  "buffer_load_dwordx4 v[36:39], off, s[40:43], s95 sc1\n" \
  "s_mov_b64 exec, -1\n" \
  "v_and_b32 v246, 3221225471, v246\n" \
  "v_and_b32 v247, 3221225471, v247\n" \
  "s_waitcnt lgkmcnt(0)\n" \
  "s_sub_u32 s60, s84, s66\n" \
  "s_cmp_lt_u32 s60, 4000\n" \
  "s_cselect_b32 s61, s68, 0\n" \
  "s_lshl_b32 s60, s60, 10\n" \
  "s_or_b32 s60, s60, s87\n" \
  "s_cmp_lg_u32 s61, 0\n" \
  "s_cselect_b32 s60, s60, 0\n" \
  "s_and_b32 s61, s48, 3\n" \
  "s_cmp_eq_u32 s61, 1\n" \
  "s_cselect_b32 s46, -1, s46\n" \
  "s_sub_u32 s60, s60, 1\n" \
  "s_min_u32 s46, s46, s60\n" \
  "v_add_u32 v187, s46, 1\n" \
  "s_sub_u32 s76, s84, s90\n" \
  "s_sub_u32 s77, s98, s86\n" \
  "v_cvt_f32_u32 v188, s76\n" \
  "v_cvt_f32_u32 v189, s77\n" \
  "v_rcp_f32 v189, v189\n" \
  "v_mul_f32 v188, 16.0, v188\n" \
  "v_fma_f32 v188, v188, v189, 0.5\n" \
  "v_cvt_u32_f32 v188, v188\n" \
  "s_nop 0\n" \
  "v_readfirstlane_b32 s76, v188\n" \
  "s_max_u32 s76, s76, 160\n" \
  "s_min_u32 s76, s76, 400\n" \
  "s_add_u32 s77, s48, 0\n" \
  "s_and_b32 s77, s77, 3\n" \
  "s_cselect_b32 s87, s87, s76\n" \
  "s_cselect_b32 s86, s86, s98\n" \
  "s_cselect_b32 s90, s90, s84\n" \
  "s_add_u32 s48, s48, 1\n" \
  "ds_read_b128 v[64:67], v178\n" \
  "ds_read_b128 v[68:71], v178 offset:2048\n" \
  "ds_read_b128 v[72:75], v178 offset:4096\n" \
  "ds_read_b128 v[76:79], v178 offset:6144\n" \
  "v_xor_b32 v178, 8192, v178\n" \
  "v_mov_b64 v[44:45], 0\n" \
  "v_mov_b64 v[46:47], 0\n" \
  "v_mov_b64 v[48:49], 0\n" \
  "v_mov_b64 v[50:51], 0\n" \
  "v_mov_b64 v[52:53], 0\n" \
  "v_mov_b64 v[54:55], 0\n" \
  "v_mov_b64 v[56:57], 0\n" \
  "v_mov_b64 v[58:59], 0\n" \
  "v_mov_b32 v40, s48\n" \
  "s_waitcnt vmcnt(0)\n" \
  "ds_write2_b32 v184, v40, v187 offset1:2\n" \
  "s_cmp_eq_u32 s48, 58\n" \
  "s_cbranch_scc1 .LX_ef_%=\n" \
  ".LX_ef_b_%=:\n" \
  "s_branch .LX_rot_%=\n" \
  "s_nop 7\n" \
  ".LX_slow_%=:\n" \
  "s_bitcmp1_b32 s70, 0\n" \
  "s_cbranch_scc1 .LX_back_%=\n" \
  "s_waitcnt lgkmcnt(0)\n" \
  "s_cmp_lt_u32 s70, 10\n" \
  "s_nop 3\n" \
  "s_cbranch_scc1 .LX_nol_%=\n" \
  "v_max_u32 v189, v100, v101\n" \
  "v_max3_u32 v189, v189, v102, v103\n" \
  "v_max3_u32 v189, v189, v104, v105\n" \
  "v_max3_u32 v189, v189, v106, v107\n" \
  "v_max3_u32 v189, v189, v108, v109\n" \
  "v_max3_u32 v189, v189, v110, v111\n" \
  "v_max3_u32 v189, v189, v112, v113\n" \
  "v_max3_u32 v189, v189, v114, v115\n" \
  "v_max3_u32 v189, v189, v116, v117\n" \
  "v_max3_u32 v189, v189, v118, v119\n" \
  "v_max3_u32 v189, v189, v128, v129\n" \
  "v_max3_u32 v189, v189, v130, v131\n" \
  "v_max3_u32 v189, v189, v120, v121\n" \
  "v_max3_u32 v189, v189, v122, v123\n" \
  "v_max3_u32 v189, v189, v124, v125\n" \
  "v_max3_u32 v189, v189, v126, v127\n" \
  "v_max3_u32 v189, v189, v132, v133\n" \
  "v_max3_u32 v189, v189, v134, v135\n" \
  "v_cmp_eq_u32 vcc, -1, v189\n" \
  "s_cbranch_vccz .LX_back_%=\n" \
  ".LX_nol_%=:\n" \
  "s_add_u32 s70, s70, 2\n" \
  "s_cmp_gt_u32 s70, 65536\n" \
  "s_cbranch_scc1 .LX_dset_%=\n" \
  "s_barrier\n" \
  "v_mov_b64 v[44:45], 0\n" \
  "v_mov_b64 v[46:47], 0\n" \
  "v_mov_b64 v[48:49], 0\n" \
  "v_mov_b64 v[50:51], 0\n" \
  "v_mov_b64 v[52:53], 0\n" \
  "v_mov_b64 v[54:55], 0\n" \
  "v_mov_b64 v[56:57], 0\n" \
  "v_mov_b64 v[58:59], 0\n" \
  "s_cmp_lg_u32 %28, 0\n" \
  "s_cbranch_scc1 .LX_g3_%=\n" \
  "s_and_b32 s62, %43, 0xffff\n" \
  "s_add_u32 s44, s44, s62\n" \
  "s_xor_b32 s59, s59, 0\n" \
  "s_mov_b32 s68, 0\n" \
  "s_mov_b32 m0, 0\n" \
  "s_branch .LX_rt_%=\n" \
  ".LX_dset_%=:\n" \
  "v_mov_b32 v128, 1\n" \
  "v_mov_b32 v129, s55\n" \
  "buffer_store_dwordx2 v[128:129], off, s[40:43], %40 offset:128 sc1\n" \
  "s_mov_b32 s70, 1\n" \
  "v_mov_b64 v[44:45], 0\n" \
  "v_mov_b64 v[46:47], 0\n" \
  "v_mov_b64 v[48:49], 0\n" \
  "v_mov_b64 v[50:51], 0\n" \
  "v_mov_b64 v[52:53], 0\n" \
  "v_mov_b64 v[54:55], 0\n" \
  "v_mov_b64 v[56:57], 0\n" \
  "v_mov_b64 v[58:59], 0\n" \
  "s_waitcnt vmcnt(0) lgkmcnt(0)\n" \
  "s_barrier\n" \
  "s_branch .LX_p_%=\n" \
  ".LX_xr_%=:\n" \
  "s_waitcnt lgkmcnt(0)\n" \
  "s_nop 7\n" \
  "ds_read_b64_tr_b16 v[132:133], v244\n" \
  "ds_read_b64_tr_b16 v[134:135], v244 offset:256\n" \
  "ds_read_b64_tr_b16 v[136:137], v244 offset:512\n" \
  "ds_read_b64_tr_b16 v[138:139], v244 offset:768\n" \
  "ds_read_b64_tr_b16 v[140:141], v244 offset:1024\n" \
  "ds_read_b64_tr_b16 v[142:143], v244 offset:1280\n" \
  "ds_read_b64_tr_b16 v[144:145], v244 offset:1536\n" \
  "ds_read_b64_tr_b16 v[146:147], v244 offset:1792\n" \
  "s_waitcnt lgkmcnt(6)\n" \
  "v_mfma_f32_16x16x32_bf16 v[148:151], v[132:135], v[84:87], 0\n" \
  "s_waitcnt lgkmcnt(4)\n" \
  "v_mfma_f32_16x16x32_bf16 v[148:151], v[136:139], v[88:91], v[148:151]\n" \
  "s_waitcnt lgkmcnt(2)\n" \
  "v_mfma_f32_16x16x32_bf16 v[148:151], v[140:143], v[92:95], v[148:151]\n" \
  "s_waitcnt lgkmcnt(0)\n" \
  "v_mfma_f32_16x16x32_bf16 v[148:151], v[144:147], v[96:99], v[148:151]\n" \
  "s_sub_u32 s60, s48, 58\n" \
  "s_lshl_b32 s60, s60, 16\n" \
  "s_add_u32 s60, s60, s97\n" \
  "s_cmp_eq_u32 s48, 0\n" \
  "s_cselect_b32 s60, 2130706432, s60\n" \
  "s_nop 2\n" \
  "v_max_f32 v158, v148, v149\n" \
  "v_max3_f32 v158, v150, v151, v158\n" \
  "v_mov_b32 v159, v158\n" \
  "s_nop 1\n" \
  "v_permlane32_swap_b32 v158, v159\n" \
  "v_max_f32 v158, v158, v159\n" \
  "v_mul_f32 v152, s65, v158\n" \
  "v_min_f32 v152, v152, v180\n" \
  "v_fma_f32 v148, v148, s64, v152\n" \
  "v_fma_f32 v149, v149, s64, v152\n" \
  "v_fma_f32 v150, v150, s64, v152\n" \
  "v_fma_f32 v151, v151, s64, v152\n" \
  "v_sub_f32 v160, v152, v180\n" \
  "v_exp_f32 v148, v148\n" \
  "v_exp_f32 v149, v149\n" \
  "v_exp_f32 v150, v150\n" \
  "v_exp_f32 v151, v151\n" \
  "v_exp_f32 v160, v160\n" \
  "v_add_f32 v153, v148, v149\n" \
  "v_add_f32 v158, v150, v151\n" \
  "v_add_f32 v153, v153, v158\n" \
  "v_mov_b32 v159, v153\n" \
  "s_nop 1\n" \
  "v_permlane32_swap_b32 v153, v159\n" \
  "v_add_f32 v153, v153, v159\n" \
  "v_fmac_f32 v153, v160, v181\n" \
  "v_rcp_f32 v154, v153\n" \
  "s_nop 0\n" \
  "v_mul_f32 v160, v160, v154\n" \
  "v_mul_f32 v154, v154, v252\n" \
  "v_mul_f32 v148, v148, v154\n" \
  "v_mul_f32 v149, v149, v154\n" \
  "v_mul_f32 v150, v150, v154\n" \
  "v_mul_f32 v151, v151, v154\n" \
  "v_fmac_f32 v148, v160, v253\n" \
  "v_fmac_f32 v150, v160, v254\n" \
  "v_cvt_pk_bf16_f32 v156, v148, v149\n" \
  "v_cvt_pk_bf16_f32 v157, v150, v151\n" \
  "buffer_store_dwordx2 v[156:157], v251, s[40:43], s60 offen sc1\n" \
  "s_cmp_eq_u32 s48, 63\n" \
  "s_cbranch_scc1 .LX_end_%=\n" \
  ".LX_xq_%=:\n" \
  "s_add_u32 s61, s48, 1\n" \
  "s_lshl_b32 s60, s61, 19\n" \
  "s_add_u32 s60, s60, s47\n" \
  "s_lshr_b32 s62, s60, 8\n" \
  "s_and_b32 s62, s62, 8192\n" \
  "s_xor_b32 s62, s62, s60\n" \
  "s_cmp_eq_u32 s48, 0\n" \
  "s_cselect_b32 s62, 2130706432, s62\n" \
  "s_add_u32 s63, s62, 4096\n" \
  "buffer_load_dwordx4 v[84:87], v249, s[40:43], s62 offen sc1\n" \
  "buffer_load_dwordx4 v[88:91], v249, s[40:43], s62 offen offset:2048 sc1\n" \
  "buffer_load_dwordx4 v[92:95], v249, s[40:43], s63 offen sc1\n" \
  "buffer_load_dwordx4 v[96:99], v249, s[40:43], s63 offen offset:2048 sc1\n" \
  "s_sub_u32 s60, s61, 58\n" \
  "s_lshl_b32 s60, s60, 16\n" \
  "s_add_u32 s60, s60, s97\n" \
  "s_cmp_eq_u32 s48, 0\n" \
  "s_cselect_b32 s60, 2130706432, s60\n" \
  "buffer_load_dwordx2 v[180:181], v250, s[40:43], s60 offen sc1\n" \
  "s_cbranch_scc1 .LX_dry_b_%=\n" \
  "s_branch .LX_xr_b_%=\n" \
  ".LX_xl_%=:\n" \
  "s_cmp_eq_u32 s48, 57\n" \
  "s_cbranch_scc1 .LX_xq_%=\n" \
  "s_cmp_eq_u32 s48, 56\n" \
  "s_cbranch_scc1 .LX_xc_%=\n" \
  "s_cmp_lg_u32 s48, 55\n" \
  "s_cbranch_scc1 .LX_xr_%=\n" \
  "buffer_load_dwordx2 v[180:181], off, s[40:43], %46 sc1\n" \
  "s_branch .LX_xr_b_%=\n" \
  ".LX_xc_%=:\n" \
  "v_readfirstlane_b32 s63, v181\n" \
  "s_cmp_eq_u32 s63, s55\n" \
  "s_cbranch_scc1 .LX_xr_b_%=\n" \
  "s_mov_b32 s69, 0\n" \
  ".LX_xs_%=:\n" \
  "buffer_load_dwordx2 v[180:181], off, s[40:43], %46 sc1\n" \
  "s_waitcnt vmcnt(0)\n" \
  "v_readfirstlane_b32 s62, v180\n" \
  "v_readfirstlane_b32 s63, v181\n" \
  "s_cmp_eq_u32 s63, s55\n" \
  "s_cselect_b32 s62, s62, 0\n" \
  "s_cmp_lg_u32 s62, 0\n" \
  "s_cbranch_scc1 .LX_xs_x_%=\n" \
  "s_sleep 2\n" \
  "s_add_u32 s69, s69, 1\n" \
  "s_and_b32 s63, s69, 1023\n" \
  "s_cbranch_scc1 .LX_xs_%=\n" \
  "s_cmp_gt_u32 s69, 2097152\n" \
  "s_cbranch_scc1 .LX_xs_set_%=\n" \
  "buffer_load_dwordx2 v[180:181], off, s[40:43], %40 offset:128 sc1\n" \
  "s_waitcnt vmcnt(0)\n" \
  "v_readfirstlane_b32 s63, v181\n" \
  "s_cmp_eq_u32 s63, s55\n" \
  "s_cbranch_scc0 .LX_xs_%=\n" \
  "s_branch .LX_xs_x_%=\n" \
  ".LX_xs_set_%=:\n" \
  "v_mov_b32 v180, 1\n" \
  "v_mov_b32 v181, s55\n" \
  "buffer_store_dwordx2 v[180:181], off, s[40:43], %40 offset:128 sc1\n" \
  "s_waitcnt vmcnt(0)\n" \
  ".LX_xs_x_%=:\n" \
  "s_branch .LX_xr_b_%=\n" \
  ".LX_ef_%=:\n" \
  "s_waitcnt vmcnt(0) lgkmcnt(0)\n" \
  "s_branch .LX_ef_b_%=\n" \
  ".LX_tw_re_%=:\n" \
  "s_add_u32 s69, s69, 1\n" \
  "s_cmp_gt_u32 s69, 64\n" \
  "s_cbranch_scc1 .LX_noclk_%=\n" \
  "buffer_load_dwordx4 v[36:39], off, s[40:43], s95 sc1\n" \
  "s_waitcnt vmcnt(0)\n" \
  "s_branch .LX_tw_%=\n" \
  ".LX_lat_%=:\n" \
  "s_lshr_b32 s63, %43, 16\n" \
  "s_add_u32 s62, s62, s63\n" \
  "s_cmp_lt_u32 s62, 4294937296\n" \
  "s_cbranch_scc1 .LX_noclk_%=\n" \
  "s_and_b32 s63, %43, 0xffff\n" \
  ".LX_lat1_%=:\n" \
  "s_add_u32 s44, s44, s63\n" \
  "s_add_u32 s62, s62, s63\n" \
  "s_cbranch_scc0 .LX_lat1_%=\n" \
  "s_branch .LX_rt_%=\n" \
  ".LX_clk2_%=:\n" \
  "s_cmp_le_i32 s62, 3\n" \
  "s_cbranch_scc1 .LX_clk_done_%=\n" \
  "s_memrealtime s[60:61]\n" \
  "s_waitcnt lgkmcnt(0)\n" \
  "s_sub_u32 s62, s73, s60\n" \
  "s_branch .LX_clk2_%=\n" \
  ".LX_clk_done_%=:\n" \
  "s_branch .LX_noclk_%=\n" \
  ".LX_end_%=:\n" \
  "s_waitcnt vmcnt(0)\n" \
  "s_waitcnt vmcnt(0)\n" \
  "s_nop 7\n" \
  "s_endpgm\n" \
  ".LX_other_%=:\n"
#define X_ASM_VOPS "+{v239}"(ci.sidx), "+{v240}"(ci.l16), "+{v241}"(ci.qw), "+{v242}"(ci.kw), "+{v243}"(ci.fq), "+{v244}"(ci.fk), "+{v245}"(ci.kxo), "+{v246}"(ci.pst), "+{v247}"(ci.ps2), "+{v248}"(ci.lx), "+{v249}"(ci.qxo), "+{v250}"(ci.sto), "+{v251}"(ci.plx), "+{v252}"(ci.mk), "+{v253}"(ci.ck0), "+{v254}"(ci.ck2), "+{v255}"(ci.hx), "+{v200}"(ci.vq0), "+{v201}"(ci.vq1), "+{v202}"(ci.vq2), "+{v203}"(ci.vq3), "+{v204}"(ci.tro0), "+{v205}"(ci.tro1), "+{v206}"(ci.m0o)
#define X_ASM_SOPS "s"(ci.ws), "s"(ci.Wa), "s"(ci.Wb), "s"(ci.mem0), "s"(ci.w), "s"(ci.o_a), "s"(ci.o_b), "s"(ci.o_nx), "s"(ci.o_exp), "s"(ci.o_u), "s"(ci.lab), "s"(ci.f_p), "s"(ci.f_m), "s"(ci.f_u), "s"(ci.f_dur), "s"(ci.f_go), "s"(ci.o_ctl), "s"(ci.o_dbg), "s"(ci.push), "s"(ci.dret), "s"(ci.u5), "s"(ci.o_qx), "s"(ci.f_s), "s"(ci.f_r)
#define X_ASM_CLOB "v0", "v1", "v2", "v3", "v4", "v5", "v6", "v7", "v8", "v9", "v10", "v11", "v12", "v13", "v14", "v15", "v16", "v17", "v18", "v19", "v20", "v21", "v22", "v23", "v24", "v25", "v26", "v27", "v28", "v29", "v30", "v31", "v32", "v33", "v34", "v35", "v36", "v37", "v38", "v39", "v40", "v41", "v42", "v43", "v44", "v45", "v46", "v47", "v48", "v49", "v50", "v51", "v52", "v53", "v54", "v55", "v56", "v57", "v58", "v59", "v60", "v61", "v62", "v63", "v64", "v65", "v66", "v67", "v68", "v69", "v70", "v71", "v72", "v73", "v74", "v75", "v76", "v77", "v78", "v79", "v80", "v81", "v82", "v83", "v84", "v85", "v86", "v87", "v88", "v89", "v90", "v91", "v92", "v93", "v94", "v95", "v96", "v97", "v98", "v99", "v100", "v101", "v102", "v103", "v104", "v105", "v106", "v107", "v108", "v109", "v110", "v111", "v112", "v113", "v114", "v115", "v116", "v117", "v118", "v119", "v120", "v121", "v122", "v123", "v124", "v125", "v126", "v127", "v128", "v129", "v130", "v131", "v132", "v133", "v134", "v135", "v136", "v137", "v138", "v139", "v140", "v141", "v142", "v143", "v144", "v145", "v146", "v147", "v148", "v149", "v150", "v151", "v152", "v153", "v154", "v155", "v156", "v157", "v158", "v159", "v160", "v161", "v162", "v163", "v164", "v165", "v166", "v167", "v168", "v169", "v170", "v171", "v172", "v173", "v174", "v175", "v176", "v177", "v178", "v179", "v180", "v181", "v182", "v183", "v184", "v185", "v186", "v187", "v188", "v189", "v190", "v191", "v192", "v193", "v194", "v195", "v196", "v197", "v198", "v199", "a0", "a1", "a2", "a3", "a4", "a5", "a6", "a7", "a8", "a9", "a10", "a11", "a12", "a13", "a14", "a15", "a16", "a17", "a18", "a19", "a20", "a21", "a22", "a23", "a24", "a25", "a26", "a27", "a28", "a29", "a30", "a31", "a32", "a33", "a34", "a35", "a36", "a37", "a38", "a39", "a40", "a41", "a42", "a43", "a44", "a45", "a46", "a47", "a48", "a49", "a50", "a51", "a52", "a53", "a54", "a55", "a56", "a57", "a58", "a59", "a60", "a61", "a62", "a63", "a64", "a65", "a66", "a67", "a68", "a69", "a70", "a71", "a72", "a73", "a74", "a75", "a76", "a77", "a78", "a79", "a80", "a81", "a82", "a83", "a84", "a85", "a86", "a87", "a88", "a89", "a90", "a91", "a92", "a93", "a94", "a95", "a96", "a97", "a98", "a99", "a100", "a101", "a102", "a103", "a104", "a105", "a106", "a107", "a108", "a109", "a110", "a111", "a112", "a113", "a114", "a115", "a116", "a117", "a118", "a119", "a120", "a121", "a122", "a123", "a124", "a125", "a126", "a127", "a128", "a129", "a130", "a131", "a132", "a133", "a134", "a135", "a136", "a137", "a138", "a139", "a140", "a141", "a142", "a143", "a144", "a145", "a146", "a147", "a148", "a149", "a150", "a151", "a152", "a153", "a154", "a155", "a156", "a157", "a158", "a159", "a160", "a161", "a162", "a163", "a164", "a165", "a166", "a167", "a168", "a169", "a170", "a171", "a172", "a173", "a174", "a175", "a176", "a177", "a178", "a179", "a180", "a181", "a182", "a183", "a184", "a185", "a186", "a187", "a188", "a189", "a190", "a191", "a192", "a193", "a194", "a195", "a196", "a197", "a198", "a199", "a200", "a201", "a202", "a203", "a204", "a205", "a206", "a207", "a208", "a209", "a210", "a211", "a212", "a213", "a214", "a215", "a216", "a217", "a218", "a219", "a220", "a221", "a222", "a223", "a224", "a225", "a226", "a227", "a228", "a229", "a230", "a231", "a232", "a233", "a234", "a235", "a236", "a237", "a238", "a239", "a240", "a241", "a242", "a243", "a244", "a245", "a246", "a247", "a248", "a249", "a250", "a251", "a252", "a253", "a254", "a255", "s40", "s41", "s42", "s43", "s44", "s45", "s46", "s47", "s48", "s49", "s50", "s51", "s52", "s53", "s54", "s55", "s56", "s57", "s58", "s59", "s60", "s61", "s62", "s63", "s64", "s65", "s66", "s67", "s68", "s69", "s70", "s71", "s72", "s73", "s74", "s75", "s76", "s77", "s78", "s79", "s80", "s81", "s82", "s83", "s84", "s85", "s86", "s87", "s88", "s89", "s90", "s91", "s92", "s93", "s94", "s95", "s96", "s97", "s98", "s99", "vcc", "scc", "memory"
#define M_ASM_TEXT \
  "s_mov_b64 s[40:41], %24\n" \
  "s_and_b32 s41, s41, 0xffff\n" \
  "s_mov_b32 s42, 255852544\n" \
  "s_mov_b32 s43, 0x00020000\n" \
  "s_mov_b32 s46, 4718592\n" \
  "s_mov_b32 s47, 0x00020000\n" \
  "s_lshl_b32 s72, %28, 11\n" \
  "buffer_load_dword v196, off, s[40:43], %40 sc1\n" \
  "s_mov_b64 s[44:45], %25\n" \
  "s_and_b32 s45, s45, 0xffff\n" \
  "s_add_u32 s60, %29, 0\n" \
  "s_lshl_b32 s61, s72, 1\n" \
  "s_add_u32 m0, s61, 16384\n" \
  "s_nop 0\n" \
  "buffer_load_dwordx4 v200, s[44:47], s60 offen lds\n" \
  "buffer_load_dwordx4 v201, s[44:47], s60 offen offset:1024 lds\n" \
  "buffer_load_dwordx4 v202, s[44:47], s60 offen offset:2048 lds\n" \
  "buffer_load_dwordx4 v203, s[44:47], s60 offen offset:3072 lds\n" \
  "s_add_u32 s60, %29, 524288\n" \
  "s_lshl_b32 s61, s72, 1\n" \
  "s_add_u32 m0, s61, 32768\n" \
  "s_nop 0\n" \
  "buffer_load_dwordx4 v200, s[44:47], s60 offen lds\n" \
  "buffer_load_dwordx4 v201, s[44:47], s60 offen offset:1024 lds\n" \
  "buffer_load_dwordx4 v202, s[44:47], s60 offen offset:2048 lds\n" \
  "buffer_load_dwordx4 v203, s[44:47], s60 offen offset:3072 lds\n" \
  "s_add_u32 s60, %29, 1048576\n" \
  "s_lshl_b32 s61, s72, 1\n" \
  "s_add_u32 m0, s61, 49152\n" \
  "s_nop 0\n" \
  "buffer_load_dwordx4 v200, s[44:47], s60 offen lds\n" \
  "buffer_load_dwordx4 v201, s[44:47], s60 offen offset:1024 lds\n" \
  "buffer_load_dwordx4 v202, s[44:47], s60 offen offset:2048 lds\n" \
  "buffer_load_dwordx4 v203, s[44:47], s60 offen offset:3072 lds\n" \
  "s_add_u32 s60, %29, 1572864\n" \
  "s_lshl_b32 s61, s72, 1\n" \
  "s_add_u32 m0, s61, 65536\n" \
  "s_nop 0\n" \
  "buffer_load_dwordx4 v200, s[44:47], s60 offen lds\n" \
  "buffer_load_dwordx4 v201, s[44:47], s60 offen offset:1024 lds\n" \
  "buffer_load_dwordx4 v202, s[44:47], s60 offen offset:2048 lds\n" \
  "buffer_load_dwordx4 v203, s[44:47], s60 offen offset:3072 lds\n" \
  "s_add_u32 s60, %29, 2097152\n" \
  "s_lshl_b32 s61, s72, 1\n" \
  "s_add_u32 m0, s61, 81920\n" \
  "s_nop 0\n" \
  "buffer_load_dwordx4 v200, s[44:47], s60 offen lds\n" \
  "buffer_load_dwordx4 v201, s[44:47], s60 offen offset:1024 lds\n" \
  "buffer_load_dwordx4 v202, s[44:47], s60 offen offset:2048 lds\n" \
  "buffer_load_dwordx4 v203, s[44:47], s60 offen offset:3072 lds\n" \
  "s_add_u32 s60, %29, 2621440\n" \
  "s_lshl_b32 s61, s72, 1\n" \
  "s_add_u32 m0, s61, 98304\n" \
  "s_nop 0\n" \
  "buffer_load_dwordx4 v200, s[44:47], s60 offen lds\n" \
  "buffer_load_dwordx4 v201, s[44:47], s60 offen offset:1024 lds\n" \
  "buffer_load_dwordx4 v202, s[44:47], s60 offen offset:2048 lds\n" \
  "buffer_load_dwordx4 v203, s[44:47], s60 offen offset:3072 lds\n" \
  "s_add_u32 s60, %29, 3145728\n" \
  "s_lshl_b32 s61, s72, 1\n" \
  "s_add_u32 m0, s61, 114688\n" \
  "s_nop 0\n" \
  "buffer_load_dwordx4 v200, s[44:47], s60 offen lds\n" \
  "buffer_load_dwordx4 v201, s[44:47], s60 offen offset:1024 lds\n" \
  "buffer_load_dwordx4 v202, s[44:47], s60 offen offset:2048 lds\n" \
  "buffer_load_dwordx4 v203, s[44:47], s60 offen offset:3072 lds\n" \
  "s_add_u32 s60, %29, 3670016\n" \
  "s_lshl_b32 s61, s72, 1\n" \
  "s_add_u32 m0, s61, 131072\n" \
  "s_nop 0\n" \
  "buffer_load_dwordx4 v200, s[44:47], s60 offen lds\n" \
  "buffer_load_dwordx4 v201, s[44:47], s60 offen offset:1024 lds\n" \
  "buffer_load_dwordx4 v202, s[44:47], s60 offen offset:2048 lds\n" \
  "buffer_load_dwordx4 v203, s[44:47], s60 offen offset:3072 lds\n" \
  "s_add_u32 s60, %29, 4194304\n" \
  "s_lshl_b32 s61, s72, 1\n" \
  "s_add_u32 m0, s61, 147456\n" \
  "s_nop 0\n" \
  "buffer_load_dwordx4 v200, s[44:47], s60 offen lds\n" \
  "buffer_load_dwordx4 v201, s[44:47], s60 offen offset:1024 lds\n" \
  "buffer_load_dwordx4 v202, s[44:47], s60 offen offset:2048 lds\n" \
  "buffer_load_dwordx4 v203, s[44:47], s60 offen offset:3072 lds\n" \
  "s_mov_b64 s[44:45], %27\n" \
  "s_and_b32 s45, s45, 0xffff\n" \
  "s_lshr_b32 s60, s72, 3\n" \
  "s_mov_b32 m0, s72\n" \
  "s_nop 0\n" \
  "buffer_load_dwordx4 v206, s[44:47], s60 offen lds\n" \
  "s_add_u32 s60, s60, 128\n" \
  "s_add_u32 m0, s72, 1024\n" \
  "s_nop 0\n" \
  "buffer_load_dwordx4 v206, s[44:47], s60 offen lds\n" \
  "s_movk_i32 s60, 1024\n" \
  "s_movk_i32 m0, 8192\n" \
  "s_nop 0\n" \
  "buffer_load_dwordx4 v206, s[44:47], s60 offen lds\n" \
  "s_waitcnt vmcnt(27)\n" \
  "s_barrier\n" \
  "v_add_u32 v194, 16384, v204\n" \
  "ds_read_b64_tr_b16 a[0:1], v194\n" \
  "ds_read_b64_tr_b16 a[2:3], v194 offset:512\n" \
  "ds_read_b64_tr_b16 a[4:5], v194 offset:8192\n" \
  "ds_read_b64_tr_b16 a[6:7], v194 offset:8704\n" \
  "v_add_u32 v195, 16384, v205\n" \
  "ds_read_b64_tr_b16 a[72:73], v195\n" \
  "ds_read_b64_tr_b16 a[74:75], v195 offset:512\n" \
  "ds_read_b64_tr_b16 a[76:77], v195 offset:8192\n" \
  "ds_read_b64_tr_b16 a[78:79], v195 offset:8704\n" \
  "v_add_u32 v194, 32768, v204\n" \
  "ds_read_b64_tr_b16 a[8:9], v194\n" \
  "ds_read_b64_tr_b16 a[10:11], v194 offset:512\n" \
  "ds_read_b64_tr_b16 a[12:13], v194 offset:8192\n" \
  "ds_read_b64_tr_b16 a[14:15], v194 offset:8704\n" \
  "v_add_u32 v195, 32768, v205\n" \
  "ds_read_b64_tr_b16 a[80:81], v195\n" \
  "ds_read_b64_tr_b16 a[82:83], v195 offset:512\n" \
  "ds_read_b64_tr_b16 a[84:85], v195 offset:8192\n" \
  "ds_read_b64_tr_b16 a[86:87], v195 offset:8704\n" \
  "v_add_u32 v194, 49152, v204\n" \
  "ds_read_b64_tr_b16 a[16:17], v194\n" \
  "ds_read_b64_tr_b16 a[18:19], v194 offset:512\n" \
  "ds_read_b64_tr_b16 a[20:21], v194 offset:8192\n" \
  "ds_read_b64_tr_b16 a[22:23], v194 offset:8704\n" \
  "v_add_u32 v195, 49152, v205\n" \
  "ds_read_b64_tr_b16 a[88:89], v195\n" \
  "ds_read_b64_tr_b16 a[90:91], v195 offset:512\n" \
  "ds_read_b64_tr_b16 a[92:93], v195 offset:8192\n" \
  "ds_read_b64_tr_b16 a[94:95], v195 offset:8704\n" \
  "s_waitcnt lgkmcnt(0)\n" \
  "s_barrier\n" \
  "s_mov_b64 s[44:45], %26\n" \
  "s_and_b32 s45, s45, 0xffff\n" \
  "s_add_u32 s60, %29, 0\n" \
  "s_lshl_b32 s61, s72, 1\n" \
  "s_add_u32 m0, s61, 16384\n" \
  "s_nop 0\n" \
  "buffer_load_dwordx4 v200, s[44:47], s60 offen lds\n" \
  "buffer_load_dwordx4 v201, s[44:47], s60 offen offset:1024 lds\n" \
  "buffer_load_dwordx4 v202, s[44:47], s60 offen offset:2048 lds\n" \
  "buffer_load_dwordx4 v203, s[44:47], s60 offen offset:3072 lds\n" \
  "s_add_u32 s60, %29, 524288\n" \
  "s_lshl_b32 s61, s72, 1\n" \
  "s_add_u32 m0, s61, 32768\n" \
  "s_nop 0\n" \
  "buffer_load_dwordx4 v200, s[44:47], s60 offen lds\n" \
  "buffer_load_dwordx4 v201, s[44:47], s60 offen offset:1024 lds\n" \
  "buffer_load_dwordx4 v202, s[44:47], s60 offen offset:2048 lds\n" \
  "buffer_load_dwordx4 v203, s[44:47], s60 offen offset:3072 lds\n" \
  "s_add_u32 s60, %29, 1048576\n" \
  "s_lshl_b32 s61, s72, 1\n" \
  "s_add_u32 m0, s61, 49152\n" \
  "s_nop 0\n" \
  "buffer_load_dwordx4 v200, s[44:47], s60 offen lds\n" \
  "buffer_load_dwordx4 v201, s[44:47], s60 offen offset:1024 lds\n" \
  "buffer_load_dwordx4 v202, s[44:47], s60 offen offset:2048 lds\n" \
  "buffer_load_dwordx4 v203, s[44:47], s60 offen offset:3072 lds\n" \
  "v_readfirstlane_b32 s55, v196\n" \
  "s_add_u32 s55, s55, 1\n" \
  "s_waitcnt vmcnt(27)\n" \
  "s_barrier\n" \
  "v_add_u32 v194, 65536, v204\n" \
  "ds_read_b64_tr_b16 a[24:25], v194\n" \
  "ds_read_b64_tr_b16 a[26:27], v194 offset:512\n" \
  "ds_read_b64_tr_b16 a[28:29], v194 offset:8192\n" \
  "ds_read_b64_tr_b16 a[30:31], v194 offset:8704\n" \
  "v_add_u32 v195, 65536, v205\n" \
  "ds_read_b64_tr_b16 a[96:97], v195\n" \
  "ds_read_b64_tr_b16 a[98:99], v195 offset:512\n" \
  "ds_read_b64_tr_b16 a[100:101], v195 offset:8192\n" \
  "ds_read_b64_tr_b16 a[102:103], v195 offset:8704\n" \
  "v_add_u32 v194, 81920, v204\n" \
  "ds_read_b64_tr_b16 a[32:33], v194\n" \
  "ds_read_b64_tr_b16 a[34:35], v194 offset:512\n" \
  "ds_read_b64_tr_b16 a[36:37], v194 offset:8192\n" \
  "ds_read_b64_tr_b16 a[38:39], v194 offset:8704\n" \
  "v_add_u32 v195, 81920, v205\n" \
  "ds_read_b64_tr_b16 a[104:105], v195\n" \
  "ds_read_b64_tr_b16 a[106:107], v195 offset:512\n" \
  "ds_read_b64_tr_b16 a[108:109], v195 offset:8192\n" \
  "ds_read_b64_tr_b16 a[110:111], v195 offset:8704\n" \
  "v_add_u32 v194, 98304, v204\n" \
  "ds_read_b64_tr_b16 a[40:41], v194\n" \
  "ds_read_b64_tr_b16 a[42:43], v194 offset:512\n" \
  "ds_read_b64_tr_b16 a[44:45], v194 offset:8192\n" \
  "ds_read_b64_tr_b16 a[46:47], v194 offset:8704\n" \
  "v_add_u32 v195, 98304, v205\n" \
  "ds_read_b64_tr_b16 a[112:113], v195\n" \
  "ds_read_b64_tr_b16 a[114:115], v195 offset:512\n" \
  "ds_read_b64_tr_b16 a[116:117], v195 offset:8192\n" \
  "ds_read_b64_tr_b16 a[118:119], v195 offset:8704\n" \
  "s_waitcnt lgkmcnt(0)\n" \
  "s_barrier\n" \
  "s_add_u32 s60, %29, 1572864\n" \
  "s_lshl_b32 s61, s72, 1\n" \
  "s_add_u32 m0, s61, 65536\n" \
  "s_nop 0\n" \
  "buffer_load_dwordx4 v200, s[44:47], s60 offen lds\n" \
  "buffer_load_dwordx4 v201, s[44:47], s60 offen offset:1024 lds\n" \
  "buffer_load_dwordx4 v202, s[44:47], s60 offen offset:2048 lds\n" \
  "buffer_load_dwordx4 v203, s[44:47], s60 offen offset:3072 lds\n" \
  "s_add_u32 s60, %29, 2097152\n" \
  "s_lshl_b32 s61, s72, 1\n" \
  "s_add_u32 m0, s61, 81920\n" \
  "s_nop 0\n" \
  "buffer_load_dwordx4 v200, s[44:47], s60 offen lds\n" \
  "buffer_load_dwordx4 v201, s[44:47], s60 offen offset:1024 lds\n" \
  "buffer_load_dwordx4 v202, s[44:47], s60 offen offset:2048 lds\n" \
  "buffer_load_dwordx4 v203, s[44:47], s60 offen offset:3072 lds\n" \
  "s_add_u32 s60, %29, 2621440\n" \
  "s_lshl_b32 s61, s72, 1\n" \
  "s_add_u32 m0, s61, 98304\n" \
  "s_nop 0\n" \
  "buffer_load_dwordx4 v200, s[44:47], s60 offen lds\n" \
  "buffer_load_dwordx4 v201, s[44:47], s60 offen offset:1024 lds\n" \
  "buffer_load_dwordx4 v202, s[44:47], s60 offen offset:2048 lds\n" \
  "buffer_load_dwordx4 v203, s[44:47], s60 offen offset:3072 lds\n" \
  "s_waitcnt vmcnt(27)\n" \
  "s_barrier\n" \
  "v_add_u32 v194, 114688, v204\n" \
  "ds_read_b64_tr_b16 a[48:49], v194\n" \
  "ds_read_b64_tr_b16 a[50:51], v194 offset:512\n" \
  "ds_read_b64_tr_b16 a[52:53], v194 offset:8192\n" \
  "ds_read_b64_tr_b16 a[54:55], v194 offset:8704\n" \
  "v_add_u32 v195, 114688, v205\n" \
  "ds_read_b64_tr_b16 a[120:121], v195\n" \
  "ds_read_b64_tr_b16 a[122:123], v195 offset:512\n" \
  "ds_read_b64_tr_b16 a[124:125], v195 offset:8192\n" \
  "ds_read_b64_tr_b16 a[126:127], v195 offset:8704\n" \
  "v_add_u32 v194, 131072, v204\n" \
  "ds_read_b64_tr_b16 a[56:57], v194\n" \
  "ds_read_b64_tr_b16 a[58:59], v194 offset:512\n" \
  "ds_read_b64_tr_b16 a[60:61], v194 offset:8192\n" \
  "ds_read_b64_tr_b16 a[62:63], v194 offset:8704\n" \
  "v_add_u32 v195, 131072, v205\n" \
  "ds_read_b64_tr_b16 a[128:129], v195\n" \
  "ds_read_b64_tr_b16 a[130:131], v195 offset:512\n" \
  "ds_read_b64_tr_b16 a[132:133], v195 offset:8192\n" \
  "ds_read_b64_tr_b16 a[134:135], v195 offset:8704\n" \
  "v_add_u32 v194, 147456, v204\n" \
  "ds_read_b64_tr_b16 a[64:65], v194\n" \
  "ds_read_b64_tr_b16 a[66:67], v194 offset:512\n" \
  "ds_read_b64_tr_b16 a[68:69], v194 offset:8192\n" \
  "ds_read_b64_tr_b16 a[70:71], v194 offset:8704\n" \
  "v_add_u32 v195, 147456, v205\n" \
  "ds_read_b64_tr_b16 a[136:137], v195\n" \
  "ds_read_b64_tr_b16 a[138:139], v195 offset:512\n" \
  "ds_read_b64_tr_b16 a[140:141], v195 offset:8192\n" \
  "ds_read_b64_tr_b16 a[142:143], v195 offset:8704\n" \
  "s_waitcnt lgkmcnt(0)\n" \
  "s_barrier\n" \
  "s_add_u32 s60, %29, 3145728\n" \
  "s_lshl_b32 s61, s72, 1\n" \
  "s_add_u32 m0, s61, 114688\n" \
  "s_nop 0\n" \
  "buffer_load_dwordx4 v200, s[44:47], s60 offen lds\n" \
  "buffer_load_dwordx4 v201, s[44:47], s60 offen offset:1024 lds\n" \
  "buffer_load_dwordx4 v202, s[44:47], s60 offen offset:2048 lds\n" \
  "buffer_load_dwordx4 v203, s[44:47], s60 offen offset:3072 lds\n" \
  "s_add_u32 s60, %29, 3670016\n" \
  "s_lshl_b32 s61, s72, 1\n" \
  "s_add_u32 m0, s61, 131072\n" \
  "s_nop 0\n" \
  "buffer_load_dwordx4 v200, s[44:47], s60 offen lds\n" \
  "buffer_load_dwordx4 v201, s[44:47], s60 offen offset:1024 lds\n" \
  "buffer_load_dwordx4 v202, s[44:47], s60 offen offset:2048 lds\n" \
  "buffer_load_dwordx4 v203, s[44:47], s60 offen offset:3072 lds\n" \
  "s_add_u32 s60, %29, 4194304\n" \
  "s_lshl_b32 s61, s72, 1\n" \
  "s_add_u32 m0, s61, 147456\n" \
  "s_nop 0\n" \
  "buffer_load_dwordx4 v200, s[44:47], s60 offen lds\n" \
  "buffer_load_dwordx4 v201, s[44:47], s60 offen offset:1024 lds\n" \
  "buffer_load_dwordx4 v202, s[44:47], s60 offen offset:2048 lds\n" \
  "buffer_load_dwordx4 v203, s[44:47], s60 offen offset:3072 lds\n" \
  "buffer_load_dwordx2 v[198:199], off, s[40:43], %39 sc1\n" \
  "s_waitcnt vmcnt(25)\n" \
  "s_barrier\n" \
  "v_add_u32 v194, 16384, v204\n" \
  "ds_read_b64_tr_b16 a[144:145], v194\n" \
  "ds_read_b64_tr_b16 a[146:147], v194 offset:512\n" \
  "ds_read_b64_tr_b16 a[148:149], v194 offset:8192\n" \
  "ds_read_b64_tr_b16 a[150:151], v194 offset:8704\n" \
  "v_add_u32 v195, 16384, v205\n" \
  "ds_read_b64_tr_b16 a[216:217], v195\n" \
  "ds_read_b64_tr_b16 a[218:219], v195 offset:512\n" \
  "ds_read_b64_tr_b16 a[220:221], v195 offset:8192\n" \
  "ds_read_b64_tr_b16 a[222:223], v195 offset:8704\n" \
  "v_xor_b32 v195, 128, v194\n" \
  "ds_read_b64_tr_b16 v[32:33], v195\n" \
  "ds_read_b64_tr_b16 v[34:35], v195 offset:512\n" \
  "ds_read_b64_tr_b16 v[36:37], v195 offset:8192\n" \
  "ds_read_b64_tr_b16 v[38:39], v195 offset:8704\n" \
  "v_add_u32 v194, 32768, v204\n" \
  "ds_read_b64_tr_b16 a[152:153], v194\n" \
  "ds_read_b64_tr_b16 a[154:155], v194 offset:512\n" \
  "ds_read_b64_tr_b16 a[156:157], v194 offset:8192\n" \
  "ds_read_b64_tr_b16 a[158:159], v194 offset:8704\n" \
  "v_add_u32 v195, 32768, v205\n" \
  "ds_read_b64_tr_b16 a[224:225], v195\n" \
  "ds_read_b64_tr_b16 a[226:227], v195 offset:512\n" \
  "ds_read_b64_tr_b16 a[228:229], v195 offset:8192\n" \
  "ds_read_b64_tr_b16 a[230:231], v195 offset:8704\n" \
  "v_xor_b32 v195, 128, v194\n" \
  "ds_read_b64_tr_b16 v[40:41], v195\n" \
  "ds_read_b64_tr_b16 v[42:43], v195 offset:512\n" \
  "ds_read_b64_tr_b16 v[44:45], v195 offset:8192\n" \
  "ds_read_b64_tr_b16 v[46:47], v195 offset:8704\n" \
  "v_add_u32 v194, 49152, v204\n" \
  "ds_read_b64_tr_b16 a[160:161], v194\n" \
  "ds_read_b64_tr_b16 a[162:163], v194 offset:512\n" \
  "ds_read_b64_tr_b16 a[164:165], v194 offset:8192\n" \
  "ds_read_b64_tr_b16 a[166:167], v194 offset:8704\n" \
  "v_add_u32 v195, 49152, v205\n" \
  "ds_read_b64_tr_b16 a[232:233], v195\n" \
  "ds_read_b64_tr_b16 a[234:235], v195 offset:512\n" \
  "ds_read_b64_tr_b16 a[236:237], v195 offset:8192\n" \
  "ds_read_b64_tr_b16 a[238:239], v195 offset:8704\n" \
  "v_xor_b32 v195, 128, v194\n" \
  "ds_read_b64_tr_b16 v[48:49], v195\n" \
  "ds_read_b64_tr_b16 v[50:51], v195 offset:512\n" \
  "ds_read_b64_tr_b16 v[52:53], v195 offset:8192\n" \
  "ds_read_b64_tr_b16 v[54:55], v195 offset:8704\n" \
  "s_waitcnt lgkmcnt(0)\n" \
  "s_waitcnt vmcnt(13)\n" \
  "s_barrier\n" \
  "v_add_u32 v194, 65536, v204\n" \
  "ds_read_b64_tr_b16 a[168:169], v194\n" \
  "ds_read_b64_tr_b16 a[170:171], v194 offset:512\n" \
  "ds_read_b64_tr_b16 a[172:173], v194 offset:8192\n" \
  "ds_read_b64_tr_b16 a[174:175], v194 offset:8704\n" \
  "v_add_u32 v195, 65536, v205\n" \
  "ds_read_b64_tr_b16 a[240:241], v195\n" \
  "ds_read_b64_tr_b16 a[242:243], v195 offset:512\n" \
  "ds_read_b64_tr_b16 a[244:245], v195 offset:8192\n" \
  "ds_read_b64_tr_b16 a[246:247], v195 offset:8704\n" \
  "v_xor_b32 v195, 128, v194\n" \
  "ds_read_b64_tr_b16 v[56:57], v195\n" \
  "ds_read_b64_tr_b16 v[58:59], v195 offset:512\n" \
  "ds_read_b64_tr_b16 v[60:61], v195 offset:8192\n" \
  "ds_read_b64_tr_b16 v[62:63], v195 offset:8704\n" \
  "v_add_u32 v194, 81920, v204\n" \
  "ds_read_b64_tr_b16 a[176:177], v194\n" \
  "ds_read_b64_tr_b16 a[178:179], v194 offset:512\n" \
  "ds_read_b64_tr_b16 a[180:181], v194 offset:8192\n" \
  "ds_read_b64_tr_b16 a[182:183], v194 offset:8704\n" \
  "v_add_u32 v195, 81920, v205\n" \
  "ds_read_b64_tr_b16 a[248:249], v195\n" \
  "ds_read_b64_tr_b16 a[250:251], v195 offset:512\n" \
  "ds_read_b64_tr_b16 a[252:253], v195 offset:8192\n" \
  "ds_read_b64_tr_b16 a[254:255], v195 offset:8704\n" \
  "v_add_u32 v194, 98304, v204\n" \
  "ds_read_b64_tr_b16 a[184:185], v194\n" \
  "ds_read_b64_tr_b16 a[186:187], v194 offset:512\n" \
  "ds_read_b64_tr_b16 a[188:189], v194 offset:8192\n" \
  "ds_read_b64_tr_b16 a[190:191], v194 offset:8704\n" \
  "v_add_u32 v195, 98304, v205\n" \
  "ds_read_b64_tr_b16 v[0:1], v195\n" \
  "ds_read_b64_tr_b16 v[2:3], v195 offset:512\n" \
  "ds_read_b64_tr_b16 v[4:5], v195 offset:8192\n" \
  "ds_read_b64_tr_b16 v[6:7], v195 offset:8704\n" \
  "s_waitcnt lgkmcnt(0)\n" \
  "s_waitcnt vmcnt(1)\n" \
  "s_barrier\n" \
  "v_add_u32 v194, 114688, v204\n" \
  "ds_read_b64_tr_b16 a[192:193], v194\n" \
  "ds_read_b64_tr_b16 a[194:195], v194 offset:512\n" \
  "ds_read_b64_tr_b16 a[196:197], v194 offset:8192\n" \
  "ds_read_b64_tr_b16 a[198:199], v194 offset:8704\n" \
  "v_add_u32 v195, 114688, v205\n" \
  "ds_read_b64_tr_b16 v[8:9], v195\n" \
  "ds_read_b64_tr_b16 v[10:11], v195 offset:512\n" \
  "ds_read_b64_tr_b16 v[12:13], v195 offset:8192\n" \
  "ds_read_b64_tr_b16 v[14:15], v195 offset:8704\n" \
  "v_add_u32 v194, 131072, v204\n" \
  "ds_read_b64_tr_b16 a[200:201], v194\n" \
  "ds_read_b64_tr_b16 a[202:203], v194 offset:512\n" \
  "ds_read_b64_tr_b16 a[204:205], v194 offset:8192\n" \
  "ds_read_b64_tr_b16 a[206:207], v194 offset:8704\n" \
  "v_add_u32 v195, 131072, v205\n" \
  "ds_read_b64_tr_b16 v[16:17], v195\n" \
  "ds_read_b64_tr_b16 v[18:19], v195 offset:512\n" \
  "ds_read_b64_tr_b16 v[20:21], v195 offset:8192\n" \
  "ds_read_b64_tr_b16 v[22:23], v195 offset:8704\n" \
  "v_add_u32 v194, 147456, v204\n" \
  "ds_read_b64_tr_b16 a[208:209], v194\n" \
  "ds_read_b64_tr_b16 a[210:211], v194 offset:512\n" \
  "ds_read_b64_tr_b16 a[212:213], v194 offset:8192\n" \
  "ds_read_b64_tr_b16 a[214:215], v194 offset:8704\n" \
  "v_add_u32 v195, 147456, v205\n" \
  "ds_read_b64_tr_b16 v[24:25], v195\n" \
  "ds_read_b64_tr_b16 v[26:27], v195 offset:512\n" \
  "ds_read_b64_tr_b16 v[28:29], v195 offset:8192\n" \
  "ds_read_b64_tr_b16 v[30:31], v195 offset:8704\n" \
  "s_waitcnt lgkmcnt(0)\n" \
  "s_waitcnt vmcnt(0)\n" \
  "v_readfirstlane_b32 s96, v198\n" \
  "v_readfirstlane_b32 s63, v199\n" \
  "s_cmp_eq_u32 s63, s55\n" \
  "s_cselect_b32 s96, s96, 0\n" \
  "s_cmp_ge_u32 s96, 0xc0000000\n" \
  "s_cbranch_scc1 .LM_go_x_%=\n" \
  "s_mov_b32 s69, 0\n" \
  ".LM_go_%=:\n" \
  "buffer_load_dwordx2 v[196:197], off, s[40:43], %39 sc1\n" \
  "s_waitcnt vmcnt(0)\n" \
  "v_readfirstlane_b32 s96, v196\n" \
  "v_readfirstlane_b32 s63, v197\n" \
  "s_cmp_eq_u32 s63, s55\n" \
  "s_cselect_b32 s96, s96, 0\n" \
  "s_cmp_ge_u32 s96, 0xc0000000\n" \
  "s_cbranch_scc1 .LM_go_x_%=\n" \
  "s_sleep 8\n" \
  "s_add_u32 s69, s69, 1\n" \
  "s_and_b32 s63, s69, 1023\n" \
  "s_cbranch_scc1 .LM_go_%=\n" \
  "s_cmp_gt_u32 s69, 2097152\n" \
  "s_cbranch_scc1 .LM_go_set_%=\n" \
  "buffer_load_dwordx2 v[196:197], off, s[40:43], %40 offset:128 sc1\n" \
  "s_waitcnt vmcnt(0)\n" \
  "v_readfirstlane_b32 s63, v197\n" \
  "s_cmp_eq_u32 s63, s55\n" \
  "s_cbranch_scc0 .LM_go_%=\n" \
  "s_branch .LM_go_x_%=\n" \
  ".LM_go_set_%=:\n" \
  "v_mov_b32 v196, 1\n" \
  "v_mov_b32 v197, s55\n" \
  "buffer_store_dwordx2 v[196:197], off, s[40:43], %40 offset:128 sc1\n" \
  "s_waitcnt vmcnt(0)\n" \
  ".LM_go_x_%=:\n" \
  "s_and_b32 s60, s96, 2\n" \
  "s_lshl_b32 s60, s60, 12\n" \
  "s_xor_b32 s74, %35, s60\n" \
  "s_xor_b32 s75, %38, s60\n" \
  "s_bfe_u32 s56, s96, 0x30010\n" \
  "s_lshl_b32 s56, s56, 12\n" \
  "s_or_b32 s56, s56, 0x200000\n" \
  "s_mov_b32 s57, 1052672\n" \
  "s_lshl_b32 s61, %34, 2\n" \
  "s_lshr_b32 s51, s96, s61\n" \
  "s_and_b32 s51, s51, 7\n" \
  "s_lshl_b32 s51, s51, 12\n" \
  "s_xor_b32 s51, s51, 4210688\n" \
  "s_mov_b32 s71, 0\n" \
  "s_add_u32 s45, s51, 886784\n" \
  "s_mov_b32 s46, -1\n" \
  "v_mov_b64 v[64:65], -1\n" \
  "v_mov_b64 v[66:67], -1\n" \
  "v_and_b32 v194, 3, v246\n" \
  "v_lshlrev_b32 v194, 2, v194\n" \
  "v_lshrrev_b32 v194, v194, s96\n" \
  "v_and_b32 v194, 7, v194\n" \
  "v_lshlrev_b32 v194, 12, v194\n" \
  "v_and_b32 v246, -4, v246\n" \
  "v_xor_b32 v246, v246, v194\n" \
  "v_and_b32 v194, 3, v247\n" \
  "v_lshlrev_b32 v194, 2, v194\n" \
  "v_lshrrev_b32 v194, v194, s96\n" \
  "v_and_b32 v194, 7, v194\n" \
  "v_lshlrev_b32 v194, 12, v194\n" \
  "v_and_b32 v247, -4, v247\n" \
  "v_xor_b32 v247, v247, v194\n" \
  "v_xor_b32 v190, s56, v246\n" \
  "buffer_store_dwordx2 v[64:65], v190, s[40:43], 0 offen sc1\n" \
  "v_xor_b32 v191, s56, v247\n" \
  "buffer_store_dwordx2 v[64:65], v191, s[40:43], 0 offen sc1\n" \
  "buffer_store_dwordx2 v[64:65], v191, s[40:43], 0 offen offset:256 sc1\n" \
  "s_xor_b32 s62, s56, s57\n" \
  "s_nop 1\n" \
  "v_xor_b32 v190, s62, v246\n" \
  "buffer_store_dwordx2 v[64:65], v190, s[40:43], 0 offen sc1\n" \
  "v_xor_b32 v191, s62, v247\n" \
  "buffer_store_dwordx2 v[64:65], v191, s[40:43], 0 offen sc1\n" \
  "buffer_store_dwordx2 v[64:65], v191, s[40:43], 0 offen offset:256 sc1\n" \
  "s_andn2_b32 s60, 2, s96\n" \
  "s_lshl_b32 s60, s60, 12\n" \
  "s_xor_b32 s60, s60, 8192\n" \
  "s_xor_b32 s58, %31, s60\n" \
  "s_mov_b32 s49, 0\n" \
  "s_add_u32 s47, %45, s60\n" \
  "s_add_u32 s97, s60, 8388608\n" \
  "s_xor_b32 s50, %32, s60\n" \
  "s_mov_b32 s48, 0\n" \
  "s_mov_b32 s54, 0\n" \
  "s_cmp_eq_u32 %28, 0\n" \
  "s_cselect_b64 s[88:89], -1, 0\n" \
  "s_mov_b32 s68, 0\n" \
  "s_mov_b32 s70, 1\n" \
  "s_mov_b32 s73, 0\n" \
  "s_mov_b32 s44, 0\n" \
  "s_mov_b32 s53, 0\n" \
  "s_memrealtime s[86:87]\n" \
  "s_memtime s[76:77]\n" \
  "s_waitcnt lgkmcnt(0)\n" \
  "s_mov_b32 s90, s76\n" \
  "s_mov_b32 s87, 340\n" \
  "s_mov_b32 s64, 0x3e0293ee\n" \
  "s_mov_b32 s65, 0xbe0293ee\n" \
  "v_mov_b64 v[76:77], 0\n" \
  "v_mov_b64 v[78:79], 0\n" \
  "v_mov_b64 v[80:81], 0\n" \
  "v_mov_b64 v[82:83], 0\n" \
  "v_mov_b64 v[84:85], 0\n" \
  "v_mov_b64 v[86:87], 0\n" \
  "v_mov_b64 v[88:89], 0\n" \
  "v_mov_b64 v[90:91], 0\n" \
  "v_mov_b64 v[92:93], 0\n" \
  "v_mov_b64 v[94:95], 0\n" \
  "v_mov_b32 v72, 0\n" \
  "v_mov_b32 v73, s55\n" \
  "v_mov_b64 v[74:75], v[72:73]\n" \
  "s_mov_b64 s[92:93], 0\n" \
  "s_mov_b32 s92, 0xffff0000\n" \
  "s_mov_b32 s60, 8192\n" \
  "s_add_u32 s60, s60, 32768\n" \
  "v_add_u32 v176, s60, v240\n" \
  "v_mov_b32 v180, v252\n" \
  "v_mov_b64 v[96:97], 0\n" \
  "v_mov_b64 v[98:99], 0\n" \
  "v_mov_b32 v96, v253\n" \
  "v_mov_b32 v98, v254\n" \
  "s_and_b32 s60, s72, 2048\n" \
  "s_add_u32 s50, s50, s60\n" \
  "s_lshr_b32 s60, s60, 1\n" \
  "v_add_u32 v177, s60, v244\n" \
  "s_lshr_b32 s60, s72, 9\n" \
  "s_add_u32 s60, s60, 17408\n" \
  "v_mov_b32 v184, s60\n" \
  "s_mov_b32 s52, 0\n" \
  "s_barrier\n" \
  "ds_write_b32 v184, v72\n" \
  "s_branch .LM_xr_%=\n" \
  ".LM_dry_b_%=:\n" \
  "s_and_b32 s60, s72, 2048\n" \
  "s_lshl_b32 s91, s60, 1\n" \
  "s_add_u32 s91, s91, 40960\n" \
  "v_mov_b64 v[182:183], 0\n" \
  "s_mov_b32 s69, 0\n" \
  ".LM_k0_%=:\n" \
  "buffer_load_dwordx2 v[196:197], off, s[40:43], s74 sc1\n" \
  "s_waitcnt vmcnt(0)\n" \
  "v_readfirstlane_b32 s54, v196\n" \
  "v_readfirstlane_b32 s63, v197\n" \
  "s_cmp_eq_u32 s63, s55\n" \
  "s_cselect_b32 s54, s54, 0\n" \
  "s_cmp_gt_i32 s54, 1\n" \
  "s_cbranch_scc1 .LM_k0_x_%=\n" \
  "s_sleep 2\n" \
  "s_add_u32 s69, s69, 1\n" \
  "s_and_b32 s63, s69, 1023\n" \
  "s_cbranch_scc1 .LM_k0_%=\n" \
  "s_cmp_gt_u32 s69, 2097152\n" \
  "s_cbranch_scc1 .LM_k0_set_%=\n" \
  "buffer_load_dwordx2 v[196:197], off, s[40:43], %40 offset:128 sc1\n" \
  "s_waitcnt vmcnt(0)\n" \
  "v_readfirstlane_b32 s63, v197\n" \
  "s_cmp_eq_u32 s63, s55\n" \
  "s_cbranch_scc0 .LM_k0_%=\n" \
  "s_branch .LM_k0_x_%=\n" \
  ".LM_k0_set_%=:\n" \
  "v_mov_b32 v196, 1\n" \
  "v_mov_b32 v197, s55\n" \
  "buffer_store_dwordx2 v[196:197], off, s[40:43], %40 offset:128 sc1\n" \
  "s_waitcnt vmcnt(0)\n" \
  ".LM_k0_x_%=:\n" \
  "s_xor_b32 s60, s49, s58\n" \
  "s_add_u32 s49, s49, 16384\n" \
  "s_xor_b32 s91, s91, 8192\n" \
  "s_mov_b32 m0, s91\n" \
  "s_nop 0\n" \
  "buffer_load_dwordx4 v245, s[40:43], s60 offen sc1 lds\n" \
  "buffer_load_dwordx4 v245, s[40:43], s60 offen offset:1024 sc1 lds\n" \
  "buffer_load_dwordx4 v245, s[40:43], s60 offen offset:2048 sc1 lds\n" \
  "buffer_load_dwordx4 v245, s[40:43], s60 offen offset:3072 sc1 lds\n" \
  "s_xor_b32 s60, s49, s58\n" \
  "s_add_u32 s49, s49, 16384\n" \
  "s_xor_b32 s91, s91, 8192\n" \
  "s_mov_b32 m0, s91\n" \
  "s_nop 0\n" \
  "buffer_load_dwordx4 v245, s[40:43], s60 offen sc1 lds\n" \
  "buffer_load_dwordx4 v245, s[40:43], s60 offen offset:1024 sc1 lds\n" \
  "buffer_load_dwordx4 v245, s[40:43], s60 offen offset:2048 sc1 lds\n" \
  "buffer_load_dwordx4 v245, s[40:43], s60 offen offset:3072 sc1 lds\n" \
  "s_waitcnt vmcnt(0)\n" \
  "s_barrier\n" \
  "v_xor_b32 v176, 8192, v176\n" \
  "s_mov_b32 s67, 2130706432\n" \
  "s_waitcnt vmcnt(0) lgkmcnt(0)\n" \
  "s_barrier\n" \
  "s_branch .LM_p_%=\n" \
  "s_nop 7\n" \
  ".p2align 6\n" \
  ".fill (((52 - (.LM_p_%= - .LM_rot_%=)) & 63) >> 2), 4, 0xBF800000\n" \
  ".LM_rot_%=:\n" \
  "s_xor_b32 s51, s51, s56\n" \
  "s_xor_b32 s71, s71, s56\n" \
  "v_xor_b32 v246, s56, v246\n" \
  "v_xor_b32 v247, s56, v247\n" \
  "s_mov_b32 s60, s56\n" \
  "s_mov_b32 s56, s57\n" \
  "s_mov_b32 s57, s60\n" \
  "s_add_u32 s59, s51, 819200\n" \
  "s_mov_b32 m0, 0\n" \
  "s_mov_b32 s70, 0\n" \
  "s_waitcnt lgkmcnt(0)\n" \
  ".LM_rt_%=:\n" \
  "s_barrier\n" \
  ".LM_p_%=:\n" \
  "ds_read_b128 v[116:119], v240\n" \
  "ds_read_b128 v[120:123], v240 offset:1024\n" \
  "ds_read_b128 v[124:127], v240 offset:2048\n" \
  "s_waitcnt lgkmcnt(2)\n" \
  "v_smfmac_f32_16x16x64_bf16 v[80:83], v[116:119], a[72:79], v239\n" \
  "s_waitcnt lgkmcnt(1)\n" \
  "v_smfmac_f32_16x16x64_bf16 v[80:83], v[120:123], a[80:87], v239\n" \
  "s_barrier\n" \
  "ds_read_b128 v[128:131], v240 offset:3072\n" \
  "v_smfmac_f32_16x16x64_bf16 v[76:79], v[116:119], a[0:7], v239\n" \
  "ds_read_b128 v[132:135], v240 offset:4096\n" \
  "s_waitcnt lgkmcnt(2)\n" \
  "v_smfmac_f32_16x16x64_bf16 v[80:83], v[124:127], a[88:95], v239\n" \
  "ds_read_b128 v[136:139], v240 offset:5120\n" \
  "v_smfmac_f32_16x16x64_bf16 v[76:79], v[120:123], a[8:15], v239\n" \
  "v_smfmac_f32_16x16x64_bf16 v[76:79], v[124:127], a[16:23], v239\n" \
  "s_waitcnt lgkmcnt(2)\n" \
  "v_smfmac_f32_16x16x64_bf16 v[76:79], v[128:131], a[24:31], v239\n" \
  "ds_read_b128 v[140:143], v240 offset:7168\n" \
  "ds_read_b128 v[144:147], v240 offset:6144\n" \
  "s_waitcnt lgkmcnt(3)\n" \
  "v_smfmac_f32_16x16x64_bf16 v[76:79], v[132:135], a[32:39], v239\n" \
  "v_smfmac_f32_16x16x64_bf16 v[80:83], v[128:131], a[96:103], v239\n" \
  "ds_read_b128 v[148:151], v240 offset:8192\n" \
  "s_waitcnt lgkmcnt(3)\n" \
  "v_smfmac_f32_16x16x64_bf16 v[76:79], v[136:139], a[40:47], v239\n" \
  "v_smfmac_f32_16x16x64_bf16 v[80:83], v[132:135], a[104:111], v239\n" \
  "v_smfmac_f32_16x16x64_bf16 v[80:83], v[136:139], a[112:119], v239\n" \
  "s_waitcnt lgkmcnt(1)\n" \
  "v_smfmac_f32_16x16x64_bf16 v[76:79], v[144:147], a[48:55], v239\n" \
  "v_smfmac_f32_16x16x64_bf16 v[80:83], v[144:147], a[120:127], v239\n" \
  "v_smfmac_f32_16x16x64_bf16 v[76:79], v[140:143], a[56:63], v239\n" \
  "s_waitcnt lgkmcnt(0)\n" \
  "v_smfmac_f32_16x16x64_bf16 v[76:79], v[148:151], a[64:71], v239\n" \
  "s_nop 2\n" \
  "v_smfmac_f32_16x16x64_bf16 v[80:83], v[140:143], a[128:135], v239\n" \
  "s_nop 2\n" \
  "v_smfmac_f32_16x16x64_bf16 v[80:83], v[148:151], a[136:143], v239\n" \
  "v_add_f32 v76, v76, v78\n" \
  "v_add_f32 v77, v77, v79\n" \
  "v_cmp_u_f32 vcc, v76, v77\n" \
  "v_mul_f32 v76, s64, v76\n" \
  "v_mul_f32 v77, s64, v77\n" \
  "v_cvt_pk_bf16_f32 v76, v76, v77\n" \
  "v_smfmac_f32_16x16x64_bf16 v[92:95], v[116:119], v[32:39], v239\n" \
  "s_cbranch_vccnz .LM_slow_%=\n" \
  ".LM_back_%=:\n" \
  "s_cmp_eq_u32 s48, 63\n" \
  "v_add_f32 v81, v81, v83\n" \
  "v_add_f32 v80, v80, v82\n" \
  "v_mul_f32 v80, s64, v80\n" \
  "v_mul_f32 v81, s64, v81\n" \
  "v_cvt_pk_bf16_f32 v80, v80, v81\n" \
  "v_smfmac_f32_16x16x64_bf16 v[92:95], v[120:123], v[40:47], v239\n" \
  "ds_write_b32 v241, v80 offset:256\n" \
  "v_smfmac_f32_16x16x64_bf16 v[92:95], v[124:127], v[48:55], v239\n" \
  "ds_write_b32 v241, v76\n" \
  "v_smfmac_f32_16x16x64_bf16 v[84:87], v[116:119], a[144:151], v239\n" \
  "v_smfmac_f32_16x16x64_bf16 v[92:95], v[128:131], v[56:63], v239\n" \
  "s_nop 2\n" \
  "v_smfmac_f32_16x16x64_bf16 v[84:87], v[120:123], a[152:159], v239\n" \
  "s_nop 0\n" \
  "v_smfmac_f32_16x16x64_bf16 v[84:87], v[124:127], a[160:167], v239\n" \
  "s_waitcnt lgkmcnt(0)\n" \
  "s_barrier\n" \
  "v_add_f32 v92, v92, v94\n" \
  "v_smfmac_f32_16x16x64_bf16 v[88:91], v[116:119], a[216:223], v239\n" \
  "ds_read_b64_tr_b16 v[118:119], v243 offset:1792\n" \
  "v_add_f32 v93, v93, v95\n" \
  "v_smfmac_f32_16x16x64_bf16 v[84:87], v[128:131], a[168:175], v239\n" \
  "ds_write_b64 v255, v[92:93]\n" \
  "ds_read_b64_tr_b16 v[152:153], v243 offset:256\n" \
  "v_smfmac_f32_16x16x64_bf16 v[84:87], v[132:135], a[176:183], v239\n" \
  "s_waitcnt vmcnt(0) \n" \
  "buffer_store_dwordx2 v[72:73], off, s[40:43], %36 sc1\n" \
  "v_smfmac_f32_16x16x64_bf16 v[84:87], v[136:139], a[184:191], v239\n" \
  "ds_read_b64_tr_b16 v[116:117], v243 offset:1280\n" \
  "v_smfmac_f32_16x16x64_bf16 v[84:87], v[144:147], a[192:199], v239\n" \
  "v_smfmac_f32_16x16x64_bf16 v[84:87], v[140:143], a[200:207], v239\n" \
  "ds_read_b64_tr_b16 v[156:157], v243\n" \
  "s_waitcnt lgkmcnt(3)\n" \
  "s_barrier\n" \
  "v_smfmac_f32_16x16x64_bf16 v[84:87], v[148:151], a[208:215], v239\n" \
  "s_nop 2\n" \
  "v_smfmac_f32_16x16x64_bf16 v[88:91], v[120:123], a[224:231], v239\n" \
  "s_nop 0\n" \
  "v_smfmac_f32_16x16x64_bf16 v[88:91], v[124:127], a[232:239], v239\n" \
  "ds_read_b64_tr_b16 v[120:121], v243 offset:1024\n" \
  "v_smfmac_f32_16x16x64_bf16 v[88:91], v[128:131], a[240:247], v239\n" \
  "v_add_f32 v85, v85, v87\n" \
  "v_smfmac_f32_16x16x64_bf16 v[88:91], v[132:135], a[248:255], v239\n" \
  "v_add_f32 v84, v84, v86\n" \
  "v_smfmac_f32_16x16x64_bf16 v[88:91], v[136:139], v[0:7], v239\n" \
  "v_cvt_pk_bf16_f32 v84, v84, v85\n" \
  "v_smfmac_f32_16x16x64_bf16 v[88:91], v[144:147], v[8:15], v239\n" \
  "ds_write_b32 v242, v84\n" \
  "v_smfmac_f32_16x16x64_bf16 v[88:91], v[140:143], v[16:23], v239\n" \
  "ds_read_b64_tr_b16 v[122:123], v243 offset:1536\n" \
  "v_smfmac_f32_16x16x64_bf16 v[88:91], v[148:151], v[24:31], v239\n" \
  "s_nop 7\n" \
  "v_add_f32 v88, v88, v90\n" \
  "v_add_f32 v89, v89, v91\n" \
  "v_cvt_pk_bf16_f32 v88, v88, v89\n" \
  "s_waitcnt lgkmcnt(1)\n" \
  "s_barrier\n" \
  "ds_write_b32 v242, v88 offset:256\n" \
  "ds_read_b64_tr_b16 v[124:125], v244\n" \
  "ds_read_b64_tr_b16 v[154:155], v243 offset:768\n" \
  "ds_read_b64_tr_b16 v[158:159], v243 offset:512\n" \
  "ds_read_b64_tr_b16 v[130:131], v244 offset:768\n" \
  "ds_read_b64_tr_b16 v[126:127], v244 offset:512\n" \
  "ds_read_b64_tr_b16 v[132:133], v244 offset:1024\n" \
  "ds_read_b64_tr_b16 v[134:135], v244 offset:1536\n" \
  "ds_read_b64_tr_b16 v[128:129], v244 offset:256\n" \
  "s_waitcnt lgkmcnt(8)\n" \
  "s_barrier\n" \
  "s_cbranch_scc1 .LM_xr_%=\n" \
  "ds_read_b64_tr_b16 v[138:139], v244 offset:1792\n" \
  "ds_read_b64 v[188:189], v248\n" \
  "ds_read_b64_tr_b16 v[136:137], v244 offset:1280\n" \
  "s_waitcnt lgkmcnt(6)\n" \
  "v_mfma_f32_16x16x32_bf16 v[140:143], v[124:127], v[156:159], 0\n" \
  "s_waitcnt lgkmcnt(4)\n" \
  "v_mfma_f32_16x16x32_bf16 v[140:143], v[132:135], v[120:123], v[140:143]\n" \
  "s_waitcnt lgkmcnt(3)\n" \
  "v_mfma_f32_16x16x32_bf16 v[140:143], v[128:131], v[152:155], v[140:143]\n" \
  "s_waitcnt lgkmcnt(0)\n" \
  "v_mfma_f32_16x16x32_bf16 v[140:143], v[136:139], v[116:119], v[140:143]\n" \
  "s_nop 7\n" \
  "v_exp_f32 v116, v140\n" \
  "v_exp_f32 v118, v142\n" \
  "v_exp_f32 v119, v143\n" \
  "v_exp_f32 v117, v141\n" \
  "s_nop 0\n" \
  "v_pk_add_f32 v[186:187], v[116:117], v[118:119]\n" \
  "v_pk_fma_f32 v[118:119], v[118:119], v[180:181], v[98:99] op_sel_hi:[1,0,1]\n" \
  "v_pk_add_f32 v[174:175], v[186:187], v[186:187] op_sel:[0,1] op_sel_hi:[1,0]\n" \
  "v_add_f32 v188, v188, v189\n" \
  "s_nop 0\n" \
  "v_permlane32_swap_b32 v174, v175\n" \
  "v_add_f32 v174, v174, v175\n" \
  "v_add_f32 v174, v174, v188\n" \
  "v_rcp_f32 v188, v174\n" \
  "v_pk_fma_f32 v[116:117], v[116:117], v[180:181], v[96:97] op_sel_hi:[1,0,1]\n" \
  "v_add_u32 v187, 3766484992, v174\n" \
  "v_pk_mul_f32 v[116:117], v[116:117], v[188:189] op_sel_hi:[1,0]\n" \
  "v_cmp_le_u32 vcc, s52, v187\n" \
  "v_pk_mul_f32 v[118:119], v[118:119], v[188:189] op_sel_hi:[1,0]\n" \
  "v_cvt_pk_bf16_f32 v188, v116, v117\n" \
  "v_cvt_pk_bf16_f32 v189, v118, v119\n" \
  "s_cbranch_vccnz .LM_ex_%=\n" \
  ".LM_ex_b_%=:\n" \
  "buffer_store_dwordx2 v[188:189], v246, s[40:43], 0 offen sc1\n" \
  "s_memrealtime s[98:99]\n" \
  "s_memtime s[84:85]\n" \
  ".LM_loop_%=:\n" \
  "s_cmp_ge_u32 s48, 55\n" \
  "s_cbranch_scc1 .LM_xl_%=\n" \
  ".LM_xr_b_%=:\n" \
  "ds_read_b64_tr_b16 v[116:117], v177\n" \
  "ds_read_b64_tr_b16 v[118:119], v177 offset:256\n" \
  "ds_read_b64_tr_b16 v[120:121], v177 offset:512\n" \
  "ds_read_b64_tr_b16 v[122:123], v177 offset:768\n" \
  "ds_read_b32 v188, v184\n" \
  "v_readfirstlane_b32 s63, v183\n" \
  "v_readfirstlane_b32 s62, v182\n" \
  "s_cmp_eq_u32 s63, s55\n" \
  "s_cselect_b32 s62, s62, 0\n" \
  "s_max_i32 s54, s54, s62\n" \
  "s_add_u32 s48, s48, 1\n" \
  "s_add_u32 s62, s48, 1\n" \
  "s_min_u32 s62, s62, 63\n" \
  "s_cmp_le_i32 s54, s62\n" \
  "s_cbranch_scc1 .LM_pk_go_%=\n" \
  ".LM_pk_ok_%=:\n" \
  "s_xor_b32 s60, s49, s58\n" \
  "s_add_u32 s49, s49, 16384\n" \
  "s_xor_b32 s91, s91, 8192\n" \
  "s_mov_b32 m0, s91\n" \
  "s_nop 0\n" \
  "buffer_load_dwordx4 v245, s[40:43], s60 offen sc1 lds\n" \
  "buffer_load_dwordx4 v245, s[40:43], s60 offen offset:1024 sc1 lds\n" \
  "buffer_load_dwordx4 v245, s[40:43], s60 offen offset:2048 sc1 lds\n" \
  "buffer_load_dwordx4 v245, s[40:43], s60 offen offset:3072 sc1 lds\n" \
  "s_add_u32 s62, s48, 2\n" \
  "s_cmp_le_i32 s54, s62\n" \
  "s_cselect_b32 s62, s74, 2130706432\n" \
  "buffer_load_dwordx2 v[182:183], off, s[40:43], s62 sc1\n" \
  "v_xor_b32 v176, 8192, v176\n" \
  "v_mov_b64 v[76:77], 0\n" \
  "v_mov_b64 v[78:79], 0\n" \
  "v_mov_b64 v[80:81], 0\n" \
  "v_mov_b64 v[82:83], 0\n" \
  "v_mov_b64 v[84:85], 0\n" \
  "v_mov_b64 v[86:87], 0\n" \
  "v_mov_b64 v[88:89], 0\n" \
  "v_mov_b64 v[90:91], 0\n" \
  "v_mov_b64 v[92:93], 0\n" \
  "v_mov_b64 v[94:95], 0\n" \
  "v_mov_b32 v72, s48\n" \
  "s_sub_u32 s60, s48, 2\n" \
  "s_cselect_b32 s60, 8192, s60\n" \
  "s_lshl_b32 s60, s60, 14\n" \
  "s_xor_b32 s60, s60, s50\n" \
  "s_cmp_le_u32 s48, 58\n" \
  "s_cselect_b32 s60, s60, 2130706432\n" \
  "s_waitcnt lgkmcnt(3)\n" \
  "buffer_store_dwordx4 v[116:119], v240, s[40:43], s60 offen sc1\n" \
  "s_waitcnt lgkmcnt(1)\n" \
  "buffer_store_dwordx4 v[120:123], v240, s[40:43], s60 offen offset:1024 sc1\n" \
  "s_waitcnt lgkmcnt(0)\n" \
  "v_readfirstlane_b32 s60, v188\n" \
  "v_mov_b32 v189, s55\n" \
  "s_cmp_lg_u32 s60, 0\n" \
  "s_cselect_b32 s61, s75, 2130706432\n" \
  "s_and_b32 s60, s48, 3\n" \
  "s_cmp_eq_u32 s60, 2\n" \
  "s_cselect_b32 s61, s61, 2130706432\n" \
  "buffer_store_dwordx2 v[188:189], off, s[40:43], s61 sc1\n" \
  "s_cmp_eq_u32 s48, 58\n" \
  "s_cbranch_scc1 .LM_ef_%=\n" \
  ".LM_ef_b_%=:\n" \
  "s_branch .LM_rot_%=\n" \
  "s_nop 7\n" \
  ".LM_slow_%=:\n" \
  "s_bitcmp1_b32 s70, 0\n" \
  "s_cbranch_scc1 .LM_back_%=\n" \
  "s_waitcnt lgkmcnt(0)\n" \
  "s_cmp_lt_u32 s70, 10\n" \
  "s_nop 3\n" \
  "s_cbranch_scc1 .LM_nol_%=\n" \
  "v_max_u32 v189, v116, v117\n" \
  "v_max3_u32 v189, v189, v118, v119\n" \
  "v_max3_u32 v189, v189, v120, v121\n" \
  "v_max3_u32 v189, v189, v122, v123\n" \
  "v_max3_u32 v189, v189, v124, v125\n" \
  "v_max3_u32 v189, v189, v126, v127\n" \
  "v_max3_u32 v189, v189, v128, v129\n" \
  "v_max3_u32 v189, v189, v130, v131\n" \
  "v_max3_u32 v189, v189, v132, v133\n" \
  "v_max3_u32 v189, v189, v134, v135\n" \
  "v_max3_u32 v189, v189, v136, v137\n" \
  "v_max3_u32 v189, v189, v138, v139\n" \
  "v_max3_u32 v189, v189, v144, v145\n" \
  "v_max3_u32 v189, v189, v146, v147\n" \
  "v_max3_u32 v189, v189, v140, v141\n" \
  "v_max3_u32 v189, v189, v142, v143\n" \
  "v_max3_u32 v189, v189, v148, v149\n" \
  "v_max3_u32 v189, v189, v150, v151\n" \
  "v_cmp_eq_u32 vcc, -1, v189\n" \
  "s_cbranch_vccz .LM_back_%=\n" \
  ".LM_nol_%=:\n" \
  "s_add_u32 s70, s70, 2\n" \
  "s_cmp_gt_u32 s70, 65536\n" \
  "s_cbranch_scc1 .LM_dset_%=\n" \
  "s_barrier\n" \
  "v_mov_b64 v[76:77], 0\n" \
  "v_mov_b64 v[78:79], 0\n" \
  "v_mov_b64 v[80:81], 0\n" \
  "v_mov_b64 v[82:83], 0\n" \
  "v_mov_b64 v[84:85], 0\n" \
  "v_mov_b64 v[86:87], 0\n" \
  "v_mov_b64 v[88:89], 0\n" \
  "v_mov_b64 v[90:91], 0\n" \
  "v_mov_b64 v[92:93], 0\n" \
  "v_mov_b64 v[94:95], 0\n" \
  "s_branch .LM_rt_%=\n" \
  ".LM_dset_%=:\n" \
  "v_mov_b32 v136, 1\n" \
  "v_mov_b32 v137, s55\n" \
  "buffer_store_dwordx2 v[136:137], off, s[40:43], %40 offset:128 sc1\n" \
  "s_mov_b32 s70, 1\n" \
  "v_mov_b64 v[76:77], 0\n" \
  "v_mov_b64 v[78:79], 0\n" \
  "v_mov_b64 v[80:81], 0\n" \
  "v_mov_b64 v[82:83], 0\n" \
  "v_mov_b64 v[84:85], 0\n" \
  "v_mov_b64 v[86:87], 0\n" \
  "v_mov_b64 v[88:89], 0\n" \
  "v_mov_b64 v[90:91], 0\n" \
  "v_mov_b64 v[92:93], 0\n" \
  "v_mov_b64 v[94:95], 0\n" \
  "s_waitcnt vmcnt(0) lgkmcnt(0)\n" \
  "s_barrier\n" \
  "s_branch .LM_p_%=\n" \
  ".LM_xr_%=:\n" \
  "s_waitcnt lgkmcnt(0)\n" \
  "s_nop 7\n" \
  "ds_read_b64_tr_b16 v[132:133], v244\n" \
  "ds_read_b64_tr_b16 v[134:135], v244 offset:256\n" \
  "ds_read_b64_tr_b16 v[136:137], v244 offset:512\n" \
  "ds_read_b64_tr_b16 v[138:139], v244 offset:768\n" \
  "ds_read_b64_tr_b16 v[140:141], v244 offset:1024\n" \
  "ds_read_b64_tr_b16 v[142:143], v244 offset:1280\n" \
  "ds_read_b64_tr_b16 v[144:145], v244 offset:1536\n" \
  "ds_read_b64_tr_b16 v[146:147], v244 offset:1792\n" \
  "s_waitcnt lgkmcnt(6)\n" \
  "v_mfma_f32_16x16x32_bf16 v[148:151], v[132:135], v[100:103], 0\n" \
  "s_waitcnt lgkmcnt(4)\n" \
  "v_mfma_f32_16x16x32_bf16 v[148:151], v[136:139], v[104:107], v[148:151]\n" \
  "s_waitcnt lgkmcnt(2)\n" \
  "v_mfma_f32_16x16x32_bf16 v[148:151], v[140:143], v[108:111], v[148:151]\n" \
  "s_waitcnt lgkmcnt(0)\n" \
  "v_mfma_f32_16x16x32_bf16 v[148:151], v[144:147], v[112:115], v[148:151]\n" \
  "s_sub_u32 s60, s48, 58\n" \
  "s_lshl_b32 s60, s60, 16\n" \
  "s_add_u32 s60, s60, s97\n" \
  "s_cmp_eq_u32 s48, 0\n" \
  "s_cselect_b32 s60, 2130706432, s60\n" \
  "s_nop 2\n" \
  "v_max_f32 v158, v148, v149\n" \
  "v_max3_f32 v158, v150, v151, v158\n" \
  "v_mov_b32 v159, v158\n" \
  "s_nop 1\n" \
  "v_permlane32_swap_b32 v158, v159\n" \
  "v_max_f32 v158, v158, v159\n" \
  "v_mul_f32 v152, s65, v158\n" \
  "v_min_f32 v152, v152, v178\n" \
  "v_fma_f32 v148, v148, s64, v152\n" \
  "v_fma_f32 v149, v149, s64, v152\n" \
  "v_fma_f32 v150, v150, s64, v152\n" \
  "v_fma_f32 v151, v151, s64, v152\n" \
  "v_sub_f32 v160, v152, v178\n" \
  "v_exp_f32 v148, v148\n" \
  "v_exp_f32 v149, v149\n" \
  "v_exp_f32 v150, v150\n" \
  "v_exp_f32 v151, v151\n" \
  "v_exp_f32 v160, v160\n" \
  "v_add_f32 v153, v148, v149\n" \
  "v_add_f32 v158, v150, v151\n" \
  "v_add_f32 v153, v153, v158\n" \
  "v_mov_b32 v159, v153\n" \
  "s_nop 1\n" \
  "v_permlane32_swap_b32 v153, v159\n" \
  "v_add_f32 v153, v153, v159\n" \
  "v_fmac_f32 v153, v160, v179\n" \
  "v_rcp_f32 v154, v153\n" \
  "s_nop 0\n" \
  "v_mul_f32 v160, v160, v154\n" \
  "v_mul_f32 v154, v154, v252\n" \
  "v_mul_f32 v148, v148, v154\n" \
  "v_mul_f32 v149, v149, v154\n" \
  "v_mul_f32 v150, v150, v154\n" \
  "v_mul_f32 v151, v151, v154\n" \
  "v_fmac_f32 v148, v160, v253\n" \
  "v_fmac_f32 v150, v160, v254\n" \
  "v_cvt_pk_bf16_f32 v156, v148, v149\n" \
  "v_cvt_pk_bf16_f32 v157, v150, v151\n" \
  "buffer_store_dwordx2 v[156:157], v251, s[40:43], s60 offen sc1\n" \
  "s_cmp_eq_u32 s48, 63\n" \
  "s_cbranch_scc1 .LM_end_%=\n" \
  ".LM_xq_%=:\n" \
  "s_add_u32 s61, s48, 1\n" \
  "s_lshl_b32 s60, s61, 19\n" \
  "s_add_u32 s60, s60, s47\n" \
  "s_lshr_b32 s62, s60, 8\n" \
  "s_and_b32 s62, s62, 8192\n" \
  "s_xor_b32 s62, s62, s60\n" \
  "s_cmp_eq_u32 s48, 0\n" \
  "s_cselect_b32 s62, 2130706432, s62\n" \
  "s_add_u32 s63, s62, 4096\n" \
  "buffer_load_dwordx4 v[100:103], v249, s[40:43], s62 offen sc1\n" \
  "buffer_load_dwordx4 v[104:107], v249, s[40:43], s62 offen offset:2048 sc1\n" \
  "buffer_load_dwordx4 v[108:111], v249, s[40:43], s63 offen sc1\n" \
  "buffer_load_dwordx4 v[112:115], v249, s[40:43], s63 offen offset:2048 sc1\n" \
  "s_sub_u32 s60, s61, 58\n" \
  "s_lshl_b32 s60, s60, 16\n" \
  "s_add_u32 s60, s60, s97\n" \
  "s_cmp_eq_u32 s48, 0\n" \
  "s_cselect_b32 s60, 2130706432, s60\n" \
  "buffer_load_dwordx2 v[178:179], v250, s[40:43], s60 offen sc1\n" \
  "s_cbranch_scc1 .LM_dry_b_%=\n" \
  "s_branch .LM_xr_b_%=\n" \
  ".LM_xl_%=:\n" \
  "s_cmp_eq_u32 s48, 57\n" \
  "s_cbranch_scc1 .LM_xq_%=\n" \
  "s_cmp_eq_u32 s48, 56\n" \
  "s_cbranch_scc1 .LM_xc_%=\n" \
  "s_cmp_lg_u32 s48, 55\n" \
  "s_cbranch_scc1 .LM_xr_%=\n" \
  "buffer_load_dwordx2 v[178:179], off, s[40:43], %46 sc1\n" \
  "s_branch .LM_xr_b_%=\n" \
  ".LM_xc_%=:\n" \
  "v_readfirstlane_b32 s63, v179\n" \
  "s_cmp_eq_u32 s63, s55\n" \
  "s_cbranch_scc1 .LM_xr_b_%=\n" \
  "s_mov_b32 s69, 0\n" \
  ".LM_xs_%=:\n" \
  "buffer_load_dwordx2 v[178:179], off, s[40:43], %46 sc1\n" \
  "s_waitcnt vmcnt(0)\n" \
  "v_readfirstlane_b32 s62, v178\n" \
  "v_readfirstlane_b32 s63, v179\n" \
  "s_cmp_eq_u32 s63, s55\n" \
  "s_cselect_b32 s62, s62, 0\n" \
  "s_cmp_lg_u32 s62, 0\n" \
  "s_cbranch_scc1 .LM_xs_x_%=\n" \
  "s_sleep 2\n" \
  "s_add_u32 s69, s69, 1\n" \
  "s_and_b32 s63, s69, 1023\n" \
  "s_cbranch_scc1 .LM_xs_%=\n" \
  "s_cmp_gt_u32 s69, 2097152\n" \
  "s_cbranch_scc1 .LM_xs_set_%=\n" \
  "buffer_load_dwordx2 v[178:179], off, s[40:43], %40 offset:128 sc1\n" \
  "s_waitcnt vmcnt(0)\n" \
  "v_readfirstlane_b32 s63, v179\n" \
  "s_cmp_eq_u32 s63, s55\n" \
  "s_cbranch_scc0 .LM_xs_%=\n" \
  "s_branch .LM_xs_x_%=\n" \
  ".LM_xs_set_%=:\n" \
  "v_mov_b32 v178, 1\n" \
  "v_mov_b32 v179, s55\n" \
  "buffer_store_dwordx2 v[178:179], off, s[40:43], %40 offset:128 sc1\n" \
  "s_waitcnt vmcnt(0)\n" \
  ".LM_xs_x_%=:\n" \
  "s_branch .LM_xr_b_%=\n" \
  ".LM_pk_go_%=:\n" \
  "s_mov_b32 s94, s62\n" \
  "s_mov_b32 s69, 0\n" \
  ".LM_pk_%=:\n" \
  "buffer_load_dwordx2 v[186:187], off, s[40:43], s74 sc1\n" \
  "s_waitcnt vmcnt(0)\n" \
  "v_readfirstlane_b32 s54, v186\n" \
  "v_readfirstlane_b32 s63, v187\n" \
  "s_cmp_eq_u32 s63, s55\n" \
  "s_cselect_b32 s54, s54, 0\n" \
  "s_cmp_gt_i32 s54, s94\n" \
  "s_cbranch_scc1 .LM_pk_x_%=\n" \
  "s_sleep 2\n" \
  "s_add_u32 s69, s69, 1\n" \
  "s_and_b32 s63, s69, 1023\n" \
  "s_cbranch_scc1 .LM_pk_%=\n" \
  "s_cmp_gt_u32 s69, 2097152\n" \
  "s_cbranch_scc1 .LM_pk_set_%=\n" \
  "buffer_load_dwordx2 v[186:187], off, s[40:43], %40 offset:128 sc1\n" \
  "s_waitcnt vmcnt(0)\n" \
  "v_readfirstlane_b32 s63, v187\n" \
  "s_cmp_eq_u32 s63, s55\n" \
  "s_cbranch_scc0 .LM_pk_%=\n" \
  "s_branch .LM_pk_x_%=\n" \
  ".LM_pk_set_%=:\n" \
  "v_mov_b32 v186, 1\n" \
  "v_mov_b32 v187, s55\n" \
  "buffer_store_dwordx2 v[186:187], off, s[40:43], %40 offset:128 sc1\n" \
  "s_waitcnt vmcnt(0)\n" \
  ".LM_pk_x_%=:\n" \
  "s_branch .LM_pk_ok_%=\n" \
  ".LM_ef_%=:\n" \
  "s_waitcnt vmcnt(0) lgkmcnt(0)\n" \
  "buffer_store_dwordx2 v[72:73], off, s[40:43], %36 sc1\n" \
  "s_branch .LM_ef_b_%=\n" \
  ".LM_ex_%=:\n" \
  "s_waitcnt vmcnt(0) lgkmcnt(0)\n" \
  "s_nop 7\n" \
  "ds_read_b64_tr_b16 v[120:121], v243\n" \
  "ds_read_b64_tr_b16 v[122:123], v243 offset:256\n" \
  "ds_read_b64_tr_b16 v[124:125], v243 offset:512\n" \
  "ds_read_b64_tr_b16 v[126:127], v243 offset:768\n" \
  "ds_read_b64_tr_b16 v[128:129], v243 offset:1024\n" \
  "ds_read_b64_tr_b16 v[130:131], v243 offset:1280\n" \
  "ds_read_b64_tr_b16 v[132:133], v243 offset:1536\n" \
  "ds_read_b64_tr_b16 v[134:135], v243 offset:1792\n" \
  "ds_read_b128 v[136:139], v176\n" \
  "ds_read_b128 v[144:147], v176 offset:2048\n" \
  "ds_read_b128 v[148:151], v176 offset:4096\n" \
  "ds_read_b128 v[152:155], v176 offset:6144\n" \
  "s_waitcnt lgkmcnt(3)\n" \
  "v_mfma_f32_16x16x32_bf16 v[156:159], v[136:139], v[120:123], 0\n" \
  "s_waitcnt lgkmcnt(2)\n" \
  "v_mfma_f32_16x16x32_bf16 v[156:159], v[144:147], v[124:127], v[156:159]\n" \
  "s_waitcnt lgkmcnt(1)\n" \
  "v_mfma_f32_16x16x32_bf16 v[156:159], v[148:151], v[128:131], v[156:159]\n" \
  "s_waitcnt lgkmcnt(0)\n" \
  "v_mfma_f32_16x16x32_bf16 v[156:159], v[152:155], v[132:135], v[156:159]\n" \
  "s_nop 7\n" \
  "ds_read_b128 v[136:139], v176 offset:1024\n" \
  "ds_read_b128 v[144:147], v176 offset:3072\n" \
  "ds_read_b128 v[148:151], v176 offset:5120\n" \
  "ds_read_b128 v[152:155], v176 offset:7168\n" \
  "s_waitcnt lgkmcnt(3)\n" \
  "v_mfma_f32_16x16x32_bf16 v[160:163], v[136:139], v[120:123], 0\n" \
  "s_waitcnt lgkmcnt(2)\n" \
  "v_mfma_f32_16x16x32_bf16 v[160:163], v[144:147], v[124:127], v[160:163]\n" \
  "s_waitcnt lgkmcnt(1)\n" \
  "v_mfma_f32_16x16x32_bf16 v[160:163], v[148:151], v[128:131], v[160:163]\n" \
  "s_waitcnt lgkmcnt(0)\n" \
  "v_mfma_f32_16x16x32_bf16 v[160:163], v[152:155], v[132:135], v[160:163]\n" \
  "s_nop 7\n" \
  "v_max_f32 v164, v140, v141\n" \
  "v_max3_f32 v164, v164, v142, v143\n" \
  "v_max3_f32 v164, v164, v156, v157\n" \
  "v_max3_f32 v164, v164, v158, v159\n" \
  "s_nop 2\n" \
  "v_max3_f32 v164, v164, v160, v161\n" \
  "v_max3_f32 v164, v164, v162, v163\n" \
  "v_mov_b32 v165, v164\n" \
  "s_nop 1\n" \
  "v_permlane32_swap_b32 v164, v165\n" \
  "v_max_f32 v164, v164, v165\n" \
  "v_mov_b32 v165, v164\n" \
  "s_nop 1\n" \
  "v_permlane16_swap_b32 v164, v165\n" \
  "v_max_f32 v164, v164, v165\n" \
  "v_sub_f32 v116, v140, v164\n" \
  "v_sub_f32 v117, v141, v164\n" \
  "v_sub_f32 v118, v142, v164\n" \
  "v_sub_f32 v119, v143, v164\n" \
  "v_sub_f32 v156, v156, v164\n" \
  "v_sub_f32 v157, v157, v164\n" \
  "v_sub_f32 v158, v158, v164\n" \
  "v_sub_f32 v159, v159, v164\n" \
  "v_sub_f32 v160, v160, v164\n" \
  "v_sub_f32 v161, v161, v164\n" \
  "v_sub_f32 v162, v162, v164\n" \
  "v_sub_f32 v163, v163, v164\n" \
  "v_exp_f32 v116, v116\n" \
  "v_exp_f32 v117, v117\n" \
  "v_exp_f32 v118, v118\n" \
  "v_exp_f32 v119, v119\n" \
  "v_exp_f32 v156, v156\n" \
  "v_exp_f32 v157, v157\n" \
  "v_exp_f32 v158, v158\n" \
  "v_exp_f32 v159, v159\n" \
  "v_exp_f32 v160, v160\n" \
  "v_exp_f32 v161, v161\n" \
  "v_exp_f32 v162, v162\n" \
  "v_exp_f32 v163, v163\n" \
  "v_add_f32 v164, v116, v117\n" \
  "v_add_f32 v164, v164, v118\n" \
  "v_add_f32 v164, v164, v119\n" \
  "v_mul_f32 v164, 0.5, v164\n" \
  "v_add_f32 v164, v164, v156\n" \
  "v_add_f32 v164, v164, v157\n" \
  "v_add_f32 v164, v164, v158\n" \
  "v_add_f32 v164, v164, v159\n" \
  "v_add_f32 v164, v164, v160\n" \
  "v_add_f32 v164, v164, v161\n" \
  "v_add_f32 v164, v164, v162\n" \
  "v_add_f32 v164, v164, v163\n" \
  "v_mov_b32 v165, v164\n" \
  "s_nop 1\n" \
  "v_permlane32_swap_b32 v164, v165\n" \
  "v_add_f32 v164, v164, v165\n" \
  "v_mov_b32 v165, v164\n" \
  "s_nop 1\n" \
  "v_permlane16_swap_b32 v164, v165\n" \
  "v_add_f32 v164, v164, v165\n" \
  "v_rcp_f32 v188, v164\n" \
  "v_cvt_pk_bf16_f32 v156, v156, v157\n" \
  "v_cvt_pk_bf16_f32 v157, v158, v159\n" \
  "v_cvt_pk_bf16_f32 v160, v160, v161\n" \
  "v_cvt_pk_bf16_f32 v161, v162, v163\n" \
  "v_mov_b32 v165, 17408\n" \
  "s_cmp_eq_u32 s48, 0\n" \
  "s_cbranch_scc1 .LM_ex2_%=\n" \
  ".LM_ex1_%=:\n" \
  "ds_read_b64 v[166:167], v165\n" \
  "s_waitcnt lgkmcnt(0)\n" \
  "v_min_u32 v166, v166, v167\n" \
  "v_cmp_ge_u32 vcc, s48, v166\n" \
  "s_cbranch_vccnz .LM_ex1_%=\n" \
  ".LM_ex2_%=:\n" \
  "buffer_store_dwordx2 v[156:157], v247, s[40:43], 0 offen sc1\n" \
  "buffer_store_dwordx2 v[160:161], v247, s[40:43], 0 offen offset:256 sc1\n" \
  "v_pk_fma_f32 v[116:117], v[116:117], v[180:181], v[96:97] op_sel_hi:[1,0,1]\n" \
  "v_pk_fma_f32 v[118:119], v[118:119], v[180:181], v[98:99] op_sel_hi:[1,0,1]\n" \
  "v_pk_mul_f32 v[116:117], v[116:117], v[188:189] op_sel_hi:[1,0]\n" \
  "v_pk_mul_f32 v[118:119], v[118:119], v[188:189] op_sel_hi:[1,0]\n" \
  "v_cvt_pk_bf16_f32 v188, v116, v117\n" \
  "v_cvt_pk_bf16_f32 v189, v118, v119\n" \
  "s_waitcnt vmcnt(0)\n" \
  "s_mov_b32 s52, 2.0\n" \
  "s_cmp_eq_u32 s48, 0\n" \
  "s_cbranch_scc1 .LM_ex_b_%=\n" \
  "v_lshrrev_b32 v165, 1, v240\n" \
  "v_min_u32 v165, 280, v165\n" \
  "buffer_load_dwordx2 v[166:167], v165, s[40:43], %47 offen sc1\n" \
  "s_waitcnt vmcnt(0)\n" \
  "v_cmp_eq_u32 vcc, s55, v167\n" \
  "s_nop 1\n" \
  "v_cndmask_b32 v166, 0, v166, vcc\n" \
  "v_add_u32 v164, 2, v166\n" \
  "s_mov_b32 s69, 0\n" \
  ".LM_ex3_%=:\n" \
  "s_sleep 8\n" \
  "buffer_load_dwordx2 v[166:167], v165, s[40:43], %47 offen sc1\n" \
  "s_waitcnt vmcnt(0)\n" \
  "v_cmp_eq_u32 vcc, s55, v167\n" \
  "s_nop 1\n" \
  "v_cndmask_b32 v166, 0, v166, vcc\n" \
  "v_cmp_gt_u32 vcc, v164, v166\n" \
  "s_cbranch_vccz .LM_ex_b_%=\n" \
  "s_add_u32 s69, s69, 1\n" \
  "s_and_b32 s63, s69, 1023\n" \
  "s_cbranch_scc1 .LM_ex3_%=\n" \
  "s_cmp_gt_u32 s69, 2097152\n" \
  "s_cbranch_scc1 .LM_ex3_set_%=\n" \
  "buffer_load_dwordx2 v[166:167], off, s[40:43], %40 offset:128 sc1\n" \
  "s_waitcnt vmcnt(0)\n" \
  "v_readfirstlane_b32 s63, v167\n" \
  "s_cmp_eq_u32 s63, s55\n" \
  "s_cbranch_scc0 .LM_ex3_%=\n" \
  "s_branch .LM_ex_b_%=\n" \
  ".LM_ex3_set_%=:\n" \
  "v_mov_b32 v166, 1\n" \
  "v_mov_b32 v167, s55\n" \
  "buffer_store_dwordx2 v[166:167], off, s[40:43], %40 offset:128 sc1\n" \
  "s_waitcnt vmcnt(0)\n" \
  "s_branch .LM_ex_b_%=\n" \
  ".LM_end_%=:\n" \
  "s_waitcnt vmcnt(0)\n" \
  "s_waitcnt vmcnt(0)\n" \
  "s_nop 7\n" \
  "s_endpgm\n"
#define M_ASM_VOPS "+{v239}"(ci.sidx), "+{v240}"(ci.l16), "+{v241}"(ci.qw), "+{v242}"(ci.kw), "+{v243}"(ci.fq), "+{v244}"(ci.fk), "+{v245}"(ci.kxo), "+{v246}"(ci.pst), "+{v247}"(ci.ps2), "+{v248}"(ci.lx), "+{v249}"(ci.qxo), "+{v250}"(ci.sto), "+{v251}"(ci.plx), "+{v252}"(ci.mk), "+{v253}"(ci.ck0), "+{v254}"(ci.ck2), "+{v255}"(ci.hx), "+{v200}"(ci.vq0), "+{v201}"(ci.vq1), "+{v202}"(ci.vq2), "+{v203}"(ci.vq3), "+{v204}"(ci.tro0), "+{v205}"(ci.tro1), "+{v206}"(ci.m0o)
#define M_ASM_SOPS "s"(ci.ws), "s"(ci.Wa), "s"(ci.Wb), "s"(ci.mem0), "s"(ci.w), "s"(ci.o_a), "s"(ci.o_b), "s"(ci.o_nx), "s"(ci.o_exp), "s"(ci.o_u), "s"(ci.lab), "s"(ci.f_p), "s"(ci.f_m), "s"(ci.f_u), "s"(ci.f_dur), "s"(ci.f_go), "s"(ci.o_ctl), "s"(ci.o_dbg), "s"(ci.push), "s"(ci.dret), "s"(ci.u5), "s"(ci.o_qx), "s"(ci.f_s), "s"(ci.f_r)
#define M_ASM_CLOB "v0", "v1", "v2", "v3", "v4", "v5", "v6", "v7", "v8", "v9", "v10", "v11", "v12", "v13", "v14", "v15", "v16", "v17", "v18", "v19", "v20", "v21", "v22", "v23", "v24", "v25", "v26", "v27", "v28", "v29", "v30", "v31", "v32", "v33", "v34", "v35", "v36", "v37", "v38", "v39", "v40", "v41", "v42", "v43", "v44", "v45", "v46", "v47", "v48", "v49", "v50", "v51", "v52", "v53", "v54", "v55", "v56", "v57", "v58", "v59", "v60", "v61", "v62", "v63", "v64", "v65", "v66", "v67", "v68", "v69", "v70", "v71", "v72", "v73", "v74", "v75", "v76", "v77", "v78", "v79", "v80", "v81", "v82", "v83", "v84", "v85", "v86", "v87", "v88", "v89", "v90", "v91", "v92", "v93", "v94", "v95", "v96", "v97", "v98", "v99", "v100", "v101", "v102", "v103", "v104", "v105", "v106", "v107", "v108", "v109", "v110", "v111", "v112", "v113", "v114", "v115", "v116", "v117", "v118", "v119", "v120", "v121", "v122", "v123", "v124", "v125", "v126", "v127", "v128", "v129", "v130", "v131", "v132", "v133", "v134", "v135", "v136", "v137", "v138", "v139", "v140", "v141", "v142", "v143", "v144", "v145", "v146", "v147", "v148", "v149", "v150", "v151", "v152", "v153", "v154", "v155", "v156", "v157", "v158", "v159", "v160", "v161", "v162", "v163", "v164", "v165", "v166", "v167", "v168", "v169", "v170", "v171", "v172", "v173", "v174", "v175", "v176", "v177", "v178", "v179", "v180", "v181", "v182", "v183", "v184", "v185", "v186", "v187", "v188", "v189", "v190", "v191", "v192", "v193", "v194", "v195", "v196", "v197", "v198", "v199", "a0", "a1", "a2", "a3", "a4", "a5", "a6", "a7", "a8", "a9", "a10", "a11", "a12", "a13", "a14", "a15", "a16", "a17", "a18", "a19", "a20", "a21", "a22", "a23", "a24", "a25", "a26", "a27", "a28", "a29", "a30", "a31", "a32", "a33", "a34", "a35", "a36", "a37", "a38", "a39", "a40", "a41", "a42", "a43", "a44", "a45", "a46", "a47", "a48", "a49", "a50", "a51", "a52", "a53", "a54", "a55", "a56", "a57", "a58", "a59", "a60", "a61", "a62", "a63", "a64", "a65", "a66", "a67", "a68", "a69", "a70", "a71", "a72", "a73", "a74", "a75", "a76", "a77", "a78", "a79", "a80", "a81", "a82", "a83", "a84", "a85", "a86", "a87", "a88", "a89", "a90", "a91", "a92", "a93", "a94", "a95", "a96", "a97", "a98", "a99", "a100", "a101", "a102", "a103", "a104", "a105", "a106", "a107", "a108", "a109", "a110", "a111", "a112", "a113", "a114", "a115", "a116", "a117", "a118", "a119", "a120", "a121", "a122", "a123", "a124", "a125", "a126", "a127", "a128", "a129", "a130", "a131", "a132", "a133", "a134", "a135", "a136", "a137", "a138", "a139", "a140", "a141", "a142", "a143", "a144", "a145", "a146", "a147", "a148", "a149", "a150", "a151", "a152", "a153", "a154", "a155", "a156", "a157", "a158", "a159", "a160", "a161", "a162", "a163", "a164", "a165", "a166", "a167", "a168", "a169", "a170", "a171", "a172", "a173", "a174", "a175", "a176", "a177", "a178", "a179", "a180", "a181", "a182", "a183", "a184", "a185", "a186", "a187", "a188", "a189", "a190", "a191", "a192", "a193", "a194", "a195", "a196", "a197", "a198", "a199", "a200", "a201", "a202", "a203", "a204", "a205", "a206", "a207", "a208", "a209", "a210", "a211", "a212", "a213", "a214", "a215", "a216", "a217", "a218", "a219", "a220", "a221", "a222", "a223", "a224", "a225", "a226", "a227", "a228", "a229", "a230", "a231", "a232", "a233", "a234", "a235", "a236", "a237", "a238", "a239", "a240", "a241", "a242", "a243", "a244", "a245", "a246", "a247", "a248", "a249", "a250", "a251", "a252", "a253", "a254", "a255", "s40", "s41", "s42", "s43", "s44", "s45", "s46", "s47", "s48", "s49", "s50", "s51", "s52", "s53", "s54", "s55", "s56", "s57", "s58", "s59", "s60", "s61", "s62", "s63", "s64", "s65", "s66", "s67", "s68", "s69", "s70", "s71", "s72", "s73", "s74", "s75", "s76", "s77", "s78", "s79", "s80", "s81", "s82", "s83", "s84", "s85", "s86", "s87", "s88", "s89", "s90", "s91", "s92", "s93", "s94", "s95", "s96", "s97", "s98", "s99", "vcc", "scc", "memory"
#define V_ASM_TEXT \
  "s_mov_b64 s[40:41], %32\n" \
  "s_and_b32 s41, s41, 0xffff\n" \
  "s_mov_b32 s42, 255852544\n" \
  "s_mov_b32 s43, 0x00020000\n" \
  "s_mov_b32 s46, 4718592\n" \
  "s_mov_b32 s47, 0x00020000\n" \
  "s_lshl_b32 s72, %36, 11\n" \
  "buffer_load_dword v196, off, s[40:43], %48 sc1\n" \
  "s_mov_b64 s[44:45], %34\n" \
  "s_and_b32 s45, s45, 0xffff\n" \
  "s_add_u32 s60, %38, 0\n" \
  "s_lshl_b32 s61, s72, 1\n" \
  "s_add_u32 m0, s61, 16384\n" \
  "s_nop 0\n" \
  "buffer_load_dwordx4 v204, s[44:47], s60 offen lds\n" \
  "buffer_load_dwordx4 v205, s[44:47], s60 offen offset:1024 lds\n" \
  "buffer_load_dwordx4 v206, s[44:47], s60 offen offset:2048 lds\n" \
  "buffer_load_dwordx4 v207, s[44:47], s60 offen offset:3072 lds\n" \
  "s_add_u32 s60, %38, 18432\n" \
  "s_lshl_b32 s61, s72, 1\n" \
  "s_add_u32 m0, s61, 32768\n" \
  "s_nop 0\n" \
  "buffer_load_dwordx4 v204, s[44:47], s60 offen lds\n" \
  "buffer_load_dwordx4 v205, s[44:47], s60 offen offset:1024 lds\n" \
  "buffer_load_dwordx4 v206, s[44:47], s60 offen offset:2048 lds\n" \
  "buffer_load_dwordx4 v207, s[44:47], s60 offen offset:3072 lds\n" \
  "s_add_u32 s60, %38, 256\n" \
  "s_lshl_b32 s61, s72, 1\n" \
  "s_add_u32 m0, s61, 49152\n" \
  "s_nop 0\n" \
  "buffer_load_dwordx4 v204, s[44:47], s60 offen lds\n" \
  "buffer_load_dwordx4 v205, s[44:47], s60 offen offset:1024 lds\n" \
  "buffer_load_dwordx4 v206, s[44:47], s60 offen offset:2048 lds\n" \
  "buffer_load_dwordx4 v207, s[44:47], s60 offen offset:3072 lds\n" \
  "s_add_u32 s60, %38, 18688\n" \
  "s_lshl_b32 s61, s72, 1\n" \
  "s_add_u32 m0, s61, 65536\n" \
  "s_nop 0\n" \
  "buffer_load_dwordx4 v204, s[44:47], s60 offen lds\n" \
  "buffer_load_dwordx4 v205, s[44:47], s60 offen offset:1024 lds\n" \
  "buffer_load_dwordx4 v206, s[44:47], s60 offen offset:2048 lds\n" \
  "buffer_load_dwordx4 v207, s[44:47], s60 offen offset:3072 lds\n" \
  "s_add_u32 s60, %38, 512\n" \
  "s_lshl_b32 s61, s72, 1\n" \
  "s_add_u32 m0, s61, 81920\n" \
  "s_nop 0\n" \
  "buffer_load_dwordx4 v204, s[44:47], s60 offen lds\n" \
  "buffer_load_dwordx4 v205, s[44:47], s60 offen offset:1024 lds\n" \
  "buffer_load_dwordx4 v206, s[44:47], s60 offen offset:2048 lds\n" \
  "buffer_load_dwordx4 v207, s[44:47], s60 offen offset:3072 lds\n" \
  "s_add_u32 s60, %38, 18944\n" \
  "s_lshl_b32 s61, s72, 1\n" \
  "s_add_u32 m0, s61, 98304\n" \
  "s_nop 0\n" \
  "buffer_load_dwordx4 v204, s[44:47], s60 offen lds\n" \
  "buffer_load_dwordx4 v205, s[44:47], s60 offen offset:1024 lds\n" \
  "buffer_load_dwordx4 v206, s[44:47], s60 offen offset:2048 lds\n" \
  "buffer_load_dwordx4 v207, s[44:47], s60 offen offset:3072 lds\n" \
  "s_add_u32 s60, %38, 768\n" \
  "s_lshl_b32 s61, s72, 1\n" \
  "s_add_u32 m0, s61, 114688\n" \
  "s_nop 0\n" \
  "buffer_load_dwordx4 v204, s[44:47], s60 offen lds\n" \
  "buffer_load_dwordx4 v205, s[44:47], s60 offen offset:1024 lds\n" \
  "buffer_load_dwordx4 v206, s[44:47], s60 offen offset:2048 lds\n" \
  "buffer_load_dwordx4 v207, s[44:47], s60 offen offset:3072 lds\n" \
  "s_add_u32 s60, %38, 19200\n" \
  "s_lshl_b32 s61, s72, 1\n" \
  "s_add_u32 m0, s61, 131072\n" \
  "s_nop 0\n" \
  "buffer_load_dwordx4 v204, s[44:47], s60 offen lds\n" \
  "buffer_load_dwordx4 v205, s[44:47], s60 offen offset:1024 lds\n" \
  "buffer_load_dwordx4 v206, s[44:47], s60 offen offset:2048 lds\n" \
  "buffer_load_dwordx4 v207, s[44:47], s60 offen offset:3072 lds\n" \
  "s_mov_b32 s60, %38\n" \
  "s_lshl_b32 s61, s72, 1\n" \
  "s_add_u32 m0, s61, 147456\n" \
  "s_nop 0\n" \
  "buffer_load_dwordx4 v208, s[44:47], s60 offen lds\n" \
  "buffer_load_dwordx4 v209, s[44:47], s60 offen offset:1024 lds\n" \
  "buffer_load_dwordx4 v210, s[44:47], s60 offen offset:2048 lds\n" \
  "buffer_load_dwordx4 v211, s[44:47], s60 offen offset:3072 lds\n" \
  "s_mov_b64 s[44:45], %35\n" \
  "s_and_b32 s45, s45, 0xffff\n" \
  "s_lshr_b32 s60, s72, 3\n" \
  "s_mov_b32 m0, s72\n" \
  "s_nop 0\n" \
  "buffer_load_dwordx4 v214, s[44:47], s60 offen lds\n" \
  "s_add_u32 s60, s60, 128\n" \
  "s_add_u32 m0, s72, 1024\n" \
  "s_nop 0\n" \
  "buffer_load_dwordx4 v214, s[44:47], s60 offen lds\n" \
  "s_movk_i32 s60, 1024\n" \
  "s_movk_i32 m0, 8192\n" \
  "s_nop 0\n" \
  "buffer_load_dwordx4 v214, s[44:47], s60 offen lds\n" \
  "s_waitcnt vmcnt(27)\n" \
  "s_barrier\n" \
  "v_add_u32 v194, 16384, v215\n" \
  "v_xor_b32 v195, 2176, v194\n" \
  "ds_read_b64_tr_b16 a[144:145], v194\n" \
  "ds_read_b64_tr_b16 a[146:147], v194 offset:512\n" \
  "ds_read_b64_tr_b16 a[148:149], v195\n" \
  "ds_read_b64_tr_b16 a[150:151], v195 offset:512\n" \
  "ds_read_b64_tr_b16 a[152:153], v194 offset:8192\n" \
  "ds_read_b64_tr_b16 a[154:155], v194 offset:8704\n" \
  "ds_read_b64_tr_b16 a[156:157], v195 offset:8192\n" \
  "ds_read_b64_tr_b16 a[158:159], v195 offset:8704\n" \
  "v_add_u32 v194, 32768, v215\n" \
  "v_xor_b32 v195, 2176, v194\n" \
  "ds_read_b64_tr_b16 a[160:161], v194\n" \
  "ds_read_b64_tr_b16 a[162:163], v194 offset:512\n" \
  "ds_read_b64_tr_b16 a[164:165], v195\n" \
  "ds_read_b64_tr_b16 a[166:167], v195 offset:512\n" \
  "ds_read_b64_tr_b16 a[168:169], v194 offset:8192\n" \
  "ds_read_b64_tr_b16 a[170:171], v194 offset:8704\n" \
  "ds_read_b64_tr_b16 a[172:173], v195 offset:8192\n" \
  "ds_read_b64_tr_b16 a[174:175], v195 offset:8704\n" \
  "v_add_u32 v194, 49152, v215\n" \
  "v_xor_b32 v195, 2176, v194\n" \
  "ds_read_b64_tr_b16 a[176:177], v194\n" \
  "ds_read_b64_tr_b16 a[178:179], v194 offset:512\n" \
  "ds_read_b64_tr_b16 a[180:181], v195\n" \
  "ds_read_b64_tr_b16 a[182:183], v195 offset:512\n" \
  "ds_read_b64_tr_b16 a[184:185], v194 offset:8192\n" \
  "ds_read_b64_tr_b16 a[186:187], v194 offset:8704\n" \
  "ds_read_b64_tr_b16 a[188:189], v195 offset:8192\n" \
  "ds_read_b64_tr_b16 a[190:191], v195 offset:8704\n" \
  "s_waitcnt lgkmcnt(0)\n" \
  "s_barrier\n" \
  "s_mov_b64 s[44:45], %33\n" \
  "s_and_b32 s45, s45, 0xffff\n" \
  "s_add_u32 s60, %37, 0\n" \
  "s_lshl_b32 s61, s72, 1\n" \
  "s_add_u32 m0, s61, 16384\n" \
  "s_nop 0\n" \
  "buffer_load_dwordx4 v200, s[44:47], s60 offen lds\n" \
  "buffer_load_dwordx4 v201, s[44:47], s60 offen offset:1024 lds\n" \
  "buffer_load_dwordx4 v202, s[44:47], s60 offen offset:2048 lds\n" \
  "buffer_load_dwordx4 v203, s[44:47], s60 offen offset:3072 lds\n" \
  "s_add_u32 s60, %37, 524288\n" \
  "s_lshl_b32 s61, s72, 1\n" \
  "s_add_u32 m0, s61, 32768\n" \
  "s_nop 0\n" \
  "buffer_load_dwordx4 v200, s[44:47], s60 offen lds\n" \
  "buffer_load_dwordx4 v201, s[44:47], s60 offen offset:1024 lds\n" \
  "buffer_load_dwordx4 v202, s[44:47], s60 offen offset:2048 lds\n" \
  "buffer_load_dwordx4 v203, s[44:47], s60 offen offset:3072 lds\n" \
  "s_add_u32 s60, %37, 1048576\n" \
  "s_lshl_b32 s61, s72, 1\n" \
  "s_add_u32 m0, s61, 49152\n" \
  "s_nop 0\n" \
  "buffer_load_dwordx4 v200, s[44:47], s60 offen lds\n" \
  "buffer_load_dwordx4 v201, s[44:47], s60 offen offset:1024 lds\n" \
  "buffer_load_dwordx4 v202, s[44:47], s60 offen offset:2048 lds\n" \
  "buffer_load_dwordx4 v203, s[44:47], s60 offen offset:3072 lds\n" \
  "v_readfirstlane_b32 s55, v196\n" \
  "s_add_u32 s55, s55, 1\n" \
  "s_waitcnt vmcnt(27)\n" \
  "s_barrier\n" \
  "v_add_u32 v194, 65536, v215\n" \
  "v_xor_b32 v195, 2176, v194\n" \
  "ds_read_b64_tr_b16 a[192:193], v194\n" \
  "ds_read_b64_tr_b16 a[194:195], v194 offset:512\n" \
  "ds_read_b64_tr_b16 a[196:197], v195\n" \
  "ds_read_b64_tr_b16 a[198:199], v195 offset:512\n" \
  "ds_read_b64_tr_b16 a[200:201], v194 offset:8192\n" \
  "ds_read_b64_tr_b16 a[202:203], v194 offset:8704\n" \
  "ds_read_b64_tr_b16 a[204:205], v195 offset:8192\n" \
  "ds_read_b64_tr_b16 a[206:207], v195 offset:8704\n" \
  "v_add_u32 v194, 81920, v215\n" \
  "v_xor_b32 v195, 2176, v194\n" \
  "ds_read_b64_tr_b16 a[208:209], v194\n" \
  "ds_read_b64_tr_b16 a[210:211], v194 offset:512\n" \
  "ds_read_b64_tr_b16 a[212:213], v195\n" \
  "ds_read_b64_tr_b16 a[214:215], v195 offset:512\n" \
  "ds_read_b64_tr_b16 a[216:217], v194 offset:8192\n" \
  "ds_read_b64_tr_b16 a[218:219], v194 offset:8704\n" \
  "ds_read_b64_tr_b16 a[220:221], v195 offset:8192\n" \
  "ds_read_b64_tr_b16 a[222:223], v195 offset:8704\n" \
  "v_add_u32 v194, 98304, v215\n" \
  "v_xor_b32 v195, 2176, v194\n" \
  "ds_read_b64_tr_b16 a[224:225], v194\n" \
  "ds_read_b64_tr_b16 a[226:227], v194 offset:512\n" \
  "ds_read_b64_tr_b16 a[228:229], v195\n" \
  "ds_read_b64_tr_b16 a[230:231], v195 offset:512\n" \
  "ds_read_b64_tr_b16 a[232:233], v194 offset:8192\n" \
  "ds_read_b64_tr_b16 a[234:235], v194 offset:8704\n" \
  "ds_read_b64_tr_b16 a[236:237], v195 offset:8192\n" \
  "ds_read_b64_tr_b16 a[238:239], v195 offset:8704\n" \
  "s_waitcnt lgkmcnt(0)\n" \
  "s_barrier\n" \
  "s_add_u32 s60, %37, 1572864\n" \
  "s_lshl_b32 s61, s72, 1\n" \
  "s_add_u32 m0, s61, 65536\n" \
  "s_nop 0\n" \
  "buffer_load_dwordx4 v200, s[44:47], s60 offen lds\n" \
  "buffer_load_dwordx4 v201, s[44:47], s60 offen offset:1024 lds\n" \
  "buffer_load_dwordx4 v202, s[44:47], s60 offen offset:2048 lds\n" \
  "buffer_load_dwordx4 v203, s[44:47], s60 offen offset:3072 lds\n" \
  "s_add_u32 s60, %37, 2097152\n" \
  "s_lshl_b32 s61, s72, 1\n" \
  "s_add_u32 m0, s61, 81920\n" \
  "s_nop 0\n" \
  "buffer_load_dwordx4 v200, s[44:47], s60 offen lds\n" \
  "buffer_load_dwordx4 v201, s[44:47], s60 offen offset:1024 lds\n" \
  "buffer_load_dwordx4 v202, s[44:47], s60 offen offset:2048 lds\n" \
  "buffer_load_dwordx4 v203, s[44:47], s60 offen offset:3072 lds\n" \
  "s_add_u32 s60, %37, 2621440\n" \
  "s_lshl_b32 s61, s72, 1\n" \
  "s_add_u32 m0, s61, 98304\n" \
  "s_nop 0\n" \
  "buffer_load_dwordx4 v200, s[44:47], s60 offen lds\n" \
  "buffer_load_dwordx4 v201, s[44:47], s60 offen offset:1024 lds\n" \
  "buffer_load_dwordx4 v202, s[44:47], s60 offen offset:2048 lds\n" \
  "buffer_load_dwordx4 v203, s[44:47], s60 offen offset:3072 lds\n" \
  "s_waitcnt vmcnt(27)\n" \
  "s_barrier\n" \
  "v_add_u32 v194, 114688, v215\n" \
  "v_xor_b32 v195, 2176, v194\n" \
  "ds_read_b64_tr_b16 a[240:241], v194\n" \
  "ds_read_b64_tr_b16 a[242:243], v194 offset:512\n" \
  "ds_read_b64_tr_b16 a[244:245], v195\n" \
  "ds_read_b64_tr_b16 a[246:247], v195 offset:512\n" \
  "ds_read_b64_tr_b16 a[248:249], v194 offset:8192\n" \
  "ds_read_b64_tr_b16 a[250:251], v194 offset:8704\n" \
  "ds_read_b64_tr_b16 a[252:253], v195 offset:8192\n" \
  "ds_read_b64_tr_b16 a[254:255], v195 offset:8704\n" \
  "v_add_u32 v194, 131072, v215\n" \
  "v_xor_b32 v195, 2176, v194\n" \
  "ds_read_b64_tr_b16 v[0:1], v194\n" \
  "ds_read_b64_tr_b16 v[2:3], v194 offset:512\n" \
  "ds_read_b64_tr_b16 v[4:5], v195\n" \
  "ds_read_b64_tr_b16 v[6:7], v195 offset:512\n" \
  "ds_read_b64_tr_b16 v[8:9], v194 offset:8192\n" \
  "ds_read_b64_tr_b16 v[10:11], v194 offset:8704\n" \
  "ds_read_b64_tr_b16 v[12:13], v195 offset:8192\n" \
  "ds_read_b64_tr_b16 v[14:15], v195 offset:8704\n" \
  "v_mov_b32 v194, v216\n" \
  "v_xor_b32 v195, 2176, v194\n" \
  "ds_read_b64_tr_b16 v[16:17], v194\n" \
  "ds_read_b64_tr_b16 v[18:19], v194 offset:512\n" \
  "ds_read_b64_tr_b16 v[20:21], v195\n" \
  "ds_read_b64_tr_b16 v[22:23], v195 offset:512\n" \
  "ds_read_b64_tr_b16 v[24:25], v194 offset:8192\n" \
  "ds_read_b64_tr_b16 v[26:27], v194 offset:8704\n" \
  "ds_read_b64_tr_b16 v[28:29], v195 offset:8192\n" \
  "ds_read_b64_tr_b16 v[30:31], v195 offset:8704\n" \
  "v_mov_b32 v194, v217\n" \
  "v_xor_b32 v195, 2176, v194\n" \
  "ds_read_b64_tr_b16 v[32:33], v194\n" \
  "ds_read_b64_tr_b16 v[34:35], v194 offset:512\n" \
  "ds_read_b64_tr_b16 v[36:37], v195\n" \
  "ds_read_b64_tr_b16 v[38:39], v195 offset:512\n" \
  "ds_read_b64_tr_b16 v[40:41], v194 offset:8192\n" \
  "ds_read_b64_tr_b16 v[42:43], v194 offset:8704\n" \
  "ds_read_b64_tr_b16 v[44:45], v195 offset:8192\n" \
  "ds_read_b64_tr_b16 v[46:47], v195 offset:8704\n" \
  "s_waitcnt lgkmcnt(0)\n" \
  "s_barrier\n" \
  "s_add_u32 s60, %37, 3145728\n" \
  "s_lshl_b32 s61, s72, 1\n" \
  "s_add_u32 m0, s61, 114688\n" \
  "s_nop 0\n" \
  "buffer_load_dwordx4 v200, s[44:47], s60 offen lds\n" \
  "buffer_load_dwordx4 v201, s[44:47], s60 offen offset:1024 lds\n" \
  "buffer_load_dwordx4 v202, s[44:47], s60 offen offset:2048 lds\n" \
  "buffer_load_dwordx4 v203, s[44:47], s60 offen offset:3072 lds\n" \
  "s_add_u32 s60, %37, 3670016\n" \
  "s_lshl_b32 s61, s72, 1\n" \
  "s_add_u32 m0, s61, 131072\n" \
  "s_nop 0\n" \
  "buffer_load_dwordx4 v200, s[44:47], s60 offen lds\n" \
  "buffer_load_dwordx4 v201, s[44:47], s60 offen offset:1024 lds\n" \
  "buffer_load_dwordx4 v202, s[44:47], s60 offen offset:2048 lds\n" \
  "buffer_load_dwordx4 v203, s[44:47], s60 offen offset:3072 lds\n" \
  "s_add_u32 s60, %37, 4194304\n" \
  "s_lshl_b32 s61, s72, 1\n" \
  "s_add_u32 m0, s61, 147456\n" \
  "s_nop 0\n" \
  "buffer_load_dwordx4 v200, s[44:47], s60 offen lds\n" \
  "buffer_load_dwordx4 v201, s[44:47], s60 offen offset:1024 lds\n" \
  "buffer_load_dwordx4 v202, s[44:47], s60 offen offset:2048 lds\n" \
  "buffer_load_dwordx4 v203, s[44:47], s60 offen offset:3072 lds\n" \
  "buffer_load_dwordx2 v[198:199], off, s[40:43], %47 sc1\n" \
  "s_waitcnt vmcnt(25)\n" \
  "s_barrier\n" \
  "v_add_u32 v194, 16384, v212\n" \
  "ds_read_b64_tr_b16 a[0:1], v194\n" \
  "ds_read_b64_tr_b16 a[2:3], v194 offset:512\n" \
  "ds_read_b64_tr_b16 a[4:5], v194 offset:8192\n" \
  "ds_read_b64_tr_b16 a[6:7], v194 offset:8704\n" \
  "v_add_u32 v195, 16384, v213\n" \
  "ds_read_b64_tr_b16 a[72:73], v195\n" \
  "ds_read_b64_tr_b16 a[74:75], v195 offset:512\n" \
  "ds_read_b64_tr_b16 a[76:77], v195 offset:8192\n" \
  "ds_read_b64_tr_b16 a[78:79], v195 offset:8704\n" \
  "v_add_u32 v194, 32768, v212\n" \
  "ds_read_b64_tr_b16 a[8:9], v194\n" \
  "ds_read_b64_tr_b16 a[10:11], v194 offset:512\n" \
  "ds_read_b64_tr_b16 a[12:13], v194 offset:8192\n" \
  "ds_read_b64_tr_b16 a[14:15], v194 offset:8704\n" \
  "v_add_u32 v195, 32768, v213\n" \
  "ds_read_b64_tr_b16 a[80:81], v195\n" \
  "ds_read_b64_tr_b16 a[82:83], v195 offset:512\n" \
  "ds_read_b64_tr_b16 a[84:85], v195 offset:8192\n" \
  "ds_read_b64_tr_b16 a[86:87], v195 offset:8704\n" \
  "v_add_u32 v194, 49152, v212\n" \
  "ds_read_b64_tr_b16 a[16:17], v194\n" \
  "ds_read_b64_tr_b16 a[18:19], v194 offset:512\n" \
  "ds_read_b64_tr_b16 a[20:21], v194 offset:8192\n" \
  "ds_read_b64_tr_b16 a[22:23], v194 offset:8704\n" \
  "v_add_u32 v195, 49152, v213\n" \
  "ds_read_b64_tr_b16 a[88:89], v195\n" \
  "ds_read_b64_tr_b16 a[90:91], v195 offset:512\n" \
  "ds_read_b64_tr_b16 a[92:93], v195 offset:8192\n" \
  "ds_read_b64_tr_b16 a[94:95], v195 offset:8704\n" \
  "s_waitcnt lgkmcnt(0)\n" \
  "s_waitcnt vmcnt(13)\n" \
  "s_barrier\n" \
  "v_add_u32 v194, 65536, v212\n" \
  "ds_read_b64_tr_b16 a[24:25], v194\n" \
  "ds_read_b64_tr_b16 a[26:27], v194 offset:512\n" \
  "ds_read_b64_tr_b16 a[28:29], v194 offset:8192\n" \
  "ds_read_b64_tr_b16 a[30:31], v194 offset:8704\n" \
  "v_add_u32 v195, 65536, v213\n" \
  "ds_read_b64_tr_b16 a[96:97], v195\n" \
  "ds_read_b64_tr_b16 a[98:99], v195 offset:512\n" \
  "ds_read_b64_tr_b16 a[100:101], v195 offset:8192\n" \
  "ds_read_b64_tr_b16 a[102:103], v195 offset:8704\n" \
  "v_add_u32 v194, 81920, v212\n" \
  "ds_read_b64_tr_b16 a[32:33], v194\n" \
  "ds_read_b64_tr_b16 a[34:35], v194 offset:512\n" \
  "ds_read_b64_tr_b16 a[36:37], v194 offset:8192\n" \
  "ds_read_b64_tr_b16 a[38:39], v194 offset:8704\n" \
  "v_add_u32 v195, 81920, v213\n" \
  "ds_read_b64_tr_b16 a[104:105], v195\n" \
  "ds_read_b64_tr_b16 a[106:107], v195 offset:512\n" \
  "ds_read_b64_tr_b16 a[108:109], v195 offset:8192\n" \
  "ds_read_b64_tr_b16 a[110:111], v195 offset:8704\n" \
  "v_add_u32 v194, 98304, v212\n" \
  "ds_read_b64_tr_b16 a[40:41], v194\n" \
  "ds_read_b64_tr_b16 a[42:43], v194 offset:512\n" \
  "ds_read_b64_tr_b16 a[44:45], v194 offset:8192\n" \
  "ds_read_b64_tr_b16 a[46:47], v194 offset:8704\n" \
  "v_add_u32 v195, 98304, v213\n" \
  "ds_read_b64_tr_b16 a[112:113], v195\n" \
  "ds_read_b64_tr_b16 a[114:115], v195 offset:512\n" \
  "ds_read_b64_tr_b16 a[116:117], v195 offset:8192\n" \
  "ds_read_b64_tr_b16 a[118:119], v195 offset:8704\n" \
  "s_waitcnt lgkmcnt(0)\n" \
  "s_waitcnt vmcnt(1)\n" \
  "s_barrier\n" \
  "v_add_u32 v194, 114688, v212\n" \
  "ds_read_b64_tr_b16 a[48:49], v194\n" \
  "ds_read_b64_tr_b16 a[50:51], v194 offset:512\n" \
  "ds_read_b64_tr_b16 a[52:53], v194 offset:8192\n" \
  "ds_read_b64_tr_b16 a[54:55], v194 offset:8704\n" \
  "v_add_u32 v195, 114688, v213\n" \
  "ds_read_b64_tr_b16 a[120:121], v195\n" \
  "ds_read_b64_tr_b16 a[122:123], v195 offset:512\n" \
  "ds_read_b64_tr_b16 a[124:125], v195 offset:8192\n" \
  "ds_read_b64_tr_b16 a[126:127], v195 offset:8704\n" \
  "v_add_u32 v194, 131072, v212\n" \
  "ds_read_b64_tr_b16 a[56:57], v194\n" \
  "ds_read_b64_tr_b16 a[58:59], v194 offset:512\n" \
  "ds_read_b64_tr_b16 a[60:61], v194 offset:8192\n" \
  "ds_read_b64_tr_b16 a[62:63], v194 offset:8704\n" \
  "v_add_u32 v195, 131072, v213\n" \
  "ds_read_b64_tr_b16 a[128:129], v195\n" \
  "ds_read_b64_tr_b16 a[130:131], v195 offset:512\n" \
  "ds_read_b64_tr_b16 a[132:133], v195 offset:8192\n" \
  "ds_read_b64_tr_b16 a[134:135], v195 offset:8704\n" \
  "v_add_u32 v194, 147456, v212\n" \
  "ds_read_b64_tr_b16 a[64:65], v194\n" \
  "ds_read_b64_tr_b16 a[66:67], v194 offset:512\n" \
  "ds_read_b64_tr_b16 a[68:69], v194 offset:8192\n" \
  "ds_read_b64_tr_b16 a[70:71], v194 offset:8704\n" \
  "v_add_u32 v195, 147456, v213\n" \
  "ds_read_b64_tr_b16 a[136:137], v195\n" \
  "ds_read_b64_tr_b16 a[138:139], v195 offset:512\n" \
  "ds_read_b64_tr_b16 a[140:141], v195 offset:8192\n" \
  "ds_read_b64_tr_b16 a[142:143], v195 offset:8704\n" \
  "s_waitcnt lgkmcnt(0)\n" \
  "s_waitcnt vmcnt(0)\n" \
  "v_readfirstlane_b32 s96, v198\n" \
  "v_readfirstlane_b32 s63, v199\n" \
  "s_cmp_eq_u32 s63, s55\n" \
  "s_cselect_b32 s96, s96, 0\n" \
  "s_cmp_ge_u32 s96, 0xc0000000\n" \
  "s_cbranch_scc1 .LV_go_x_%=\n" \
  "s_mov_b32 s69, 0\n" \
  ".LV_go_%=:\n" \
  "buffer_load_dwordx2 v[196:197], off, s[40:43], %47 sc1\n" \
  "s_waitcnt vmcnt(0)\n" \
  "v_readfirstlane_b32 s96, v196\n" \
  "v_readfirstlane_b32 s63, v197\n" \
  "s_cmp_eq_u32 s63, s55\n" \
  "s_cselect_b32 s96, s96, 0\n" \
  "s_cmp_ge_u32 s96, 0xc0000000\n" \
  "s_cbranch_scc1 .LV_go_x_%=\n" \
  "s_sleep 8\n" \
  "s_add_u32 s69, s69, 1\n" \
  "s_and_b32 s63, s69, 1023\n" \
  "s_cbranch_scc1 .LV_go_%=\n" \
  "s_cmp_gt_u32 s69, 2097152\n" \
  "s_cbranch_scc1 .LV_go_set_%=\n" \
  "buffer_load_dwordx2 v[196:197], off, s[40:43], %48 offset:128 sc1\n" \
  "s_waitcnt vmcnt(0)\n" \
  "v_readfirstlane_b32 s63, v197\n" \
  "s_cmp_eq_u32 s63, s55\n" \
  "s_cbranch_scc0 .LV_go_%=\n" \
  "s_branch .LV_go_x_%=\n" \
  ".LV_go_set_%=:\n" \
  "v_mov_b32 v196, 1\n" \
  "v_mov_b32 v197, s55\n" \
  "buffer_store_dwordx2 v[196:197], off, s[40:43], %48 offset:128 sc1\n" \
  "s_waitcnt vmcnt(0)\n" \
  ".LV_go_x_%=:\n" \
  "s_and_b32 s60, s96, 2\n" \
  "s_lshl_b32 s60, s60, 12\n" \
  "s_xor_b32 s74, %43, s60\n" \
  "s_xor_b32 s75, %46, s60\n" \
  "s_bfe_u32 s56, s96, 0x30010\n" \
  "s_lshl_b32 s56, s56, 12\n" \
  "s_or_b32 s56, s56, 0x200000\n" \
  "s_mov_b32 s57, 1052672\n" \
  "s_lshl_b32 s61, %42, 2\n" \
  "s_lshr_b32 s51, s96, s61\n" \
  "s_and_b32 s51, s51, 7\n" \
  "s_lshl_b32 s51, s51, 12\n" \
  "s_xor_b32 s51, s51, 4210688\n" \
  "s_mov_b32 s71, 0\n" \
  "s_add_u32 s45, s51, 886784\n" \
  "s_mov_b32 s46, -1\n" \
  "v_mov_b64 v[48:49], -1\n" \
  "v_mov_b64 v[50:51], -1\n" \
  "v_and_b32 v194, 3, v250\n" \
  "v_lshlrev_b32 v194, 2, v194\n" \
  "v_lshrrev_b32 v194, v194, s96\n" \
  "v_and_b32 v194, 7, v194\n" \
  "v_lshlrev_b32 v194, 12, v194\n" \
  "v_and_b32 v250, -4, v250\n" \
  "v_xor_b32 v250, v250, v194\n" \
  "v_and_b32 v194, 3, v251\n" \
  "v_lshlrev_b32 v194, 2, v194\n" \
  "v_lshrrev_b32 v194, v194, s96\n" \
  "v_and_b32 v194, 7, v194\n" \
  "v_lshlrev_b32 v194, 12, v194\n" \
  "v_and_b32 v251, -4, v251\n" \
  "v_xor_b32 v251, v251, v194\n" \
  "v_and_b32 v194, 3, v252\n" \
  "v_lshlrev_b32 v194, 2, v194\n" \
  "v_lshrrev_b32 v194, v194, s96\n" \
  "v_and_b32 v194, 7, v194\n" \
  "v_lshlrev_b32 v194, 12, v194\n" \
  "v_and_b32 v252, -4, v252\n" \
  "v_xor_b32 v252, v252, v194\n" \
  "v_xor_b32 v185, s56, v250\n" \
  "buffer_store_dwordx4 v[48:51], v185, s[40:43], 0 offen sc1\n" \
  "v_xor_b32 v186, s56, v251\n" \
  "buffer_store_dwordx4 v[48:51], v186, s[40:43], 0 offen sc1\n" \
  "v_xor_b32 v187, s56, v252\n" \
  "buffer_store_dwordx2 v[48:49], v187, s[40:43], 0 offen sc1\n" \
  "s_xor_b32 s62, s56, s57\n" \
  "s_nop 1\n" \
  "v_xor_b32 v185, s62, v250\n" \
  "buffer_store_dwordx4 v[48:51], v185, s[40:43], 0 offen sc1\n" \
  "v_xor_b32 v186, s62, v251\n" \
  "buffer_store_dwordx4 v[48:51], v186, s[40:43], 0 offen sc1\n" \
  "v_xor_b32 v187, s62, v252\n" \
  "buffer_store_dwordx2 v[48:49], v187, s[40:43], 0 offen sc1\n" \
  "s_andn2_b32 s60, 2, s96\n" \
  "s_lshl_b32 s60, s60, 12\n" \
  "s_xor_b32 s60, s60, 8192\n" \
  "s_xor_b32 s58, %39, s60\n" \
  "s_mov_b32 s49, 0\n" \
  "s_add_u32 s47, %53, s60\n" \
  "s_add_u32 s97, s60, 8388608\n" \
  "s_xor_b32 s50, %40, s60\n" \
  "s_mov_b32 s48, 0\n" \
  "s_mov_b32 s54, 0\n" \
  "s_cmp_eq_u32 %36, 0\n" \
  "s_cselect_b64 s[88:89], -1, 0\n" \
  "s_mov_b32 s68, 0\n" \
  "s_mov_b32 s70, 1\n" \
  "s_mov_b32 s73, 0\n" \
  "s_mov_b32 s44, 0\n" \
  "s_mov_b32 s53, 0\n" \
  "s_memrealtime s[86:87]\n" \
  "s_memtime s[76:77]\n" \
  "s_waitcnt lgkmcnt(0)\n" \
  "s_mov_b32 s90, s76\n" \
  "s_mov_b32 s87, 340\n" \
  "s_mov_b32 s64, 0x3e0293ee\n" \
  "s_mov_b32 s65, 0xbe0293ee\n" \
  "v_mov_b64 v[60:61], 0\n" \
  "v_mov_b64 v[62:63], 0\n" \
  "v_mov_b64 v[64:65], 0\n" \
  "v_mov_b64 v[66:67], 0\n" \
  "v_mov_b64 v[68:69], 0\n" \
  "v_mov_b64 v[70:71], 0\n" \
  "v_mov_b64 v[72:73], 0\n" \
  "v_mov_b64 v[74:75], 0\n" \
  "v_mov_b64 v[76:77], 0\n" \
  "v_mov_b64 v[78:79], 0\n" \
  "v_mov_b64 v[80:81], 0\n" \
  "v_mov_b64 v[82:83], 0\n" \
  "v_mov_b64 v[84:85], 0\n" \
  "v_mov_b64 v[86:87], 0\n" \
  "v_mov_b32 v56, 0\n" \
  "v_mov_b32 v57, s55\n" \
  "v_mov_b64 v[58:59], v[56:57]\n" \
  "v_mov_b64 v[90:91], 0\n" \
  "s_mov_b32 s67, 2130706432\n" \
  "s_waitcnt vmcnt(0) lgkmcnt(0)\n" \
  "s_barrier\n" \
  "s_branch .LV_p_%=\n" \
  "s_nop 7\n" \
  ".p2align 6\n" \
  ".fill (((20 - (.LV_p_%= - .LV_rot_%=)) & 63) >> 2), 4, 0xBF800000\n" \
  ".LV_rot_%=:\n" \
  "s_xor_b32 s51, s51, s56\n" \
  "s_xor_b32 s71, s71, s56\n" \
  "v_xor_b32 v250, s56, v250\n" \
  "v_xor_b32 v251, s56, v251\n" \
  "v_xor_b32 v252, s56, v252\n" \
  "s_mov_b32 s60, s56\n" \
  "s_mov_b32 s56, s57\n" \
  "s_mov_b32 s57, s60\n" \
  "s_add_u32 s59, s51, 819200\n" \
  "s_mov_b32 m0, 0\n" \
  "s_mov_b32 s70, 0\n" \
  "s_waitcnt lgkmcnt(0)\n" \
  ".LV_rt_%=:\n" \
  "s_barrier\n" \
  ".LV_p_%=:\n" \
  "ds_read_b128 v[92:95], v243\n" \
  "ds_read_b128 v[96:99], v243 offset:3072\n" \
  "ds_read_b128 v[100:103], v243 offset:1024\n" \
  "ds_read_b128 v[104:107], v243 offset:2048\n" \
  "s_waitcnt lgkmcnt(3)\n" \
  "v_smfmac_f32_16x16x64_bf16 v[60:63], v[92:95], a[0:7], v242\n" \
  "s_barrier\n" \
  "ds_read_b128 v[108:111], v243 offset:4096\n" \
  "v_smfmac_f32_16x16x64_bf16 v[64:67], v[92:95], a[72:79], v242\n" \
  "ds_read_b128 v[112:115], v243 offset:5120\n" \
  "s_waitcnt lgkmcnt(3)\n" \
  "v_smfmac_f32_16x16x64_bf16 v[60:63], v[100:103], a[8:15], v242\n" \
  "ds_read_b128 v[116:119], v243 offset:6144\n" \
  "s_waitcnt lgkmcnt(3)\n" \
  "v_smfmac_f32_16x16x64_bf16 v[60:63], v[104:107], a[16:23], v242\n" \
  "ds_read_b128 v[120:123], v243 offset:7168\n" \
  "v_smfmac_f32_16x16x64_bf16 v[60:63], v[96:99], a[24:31], v242\n" \
  "ds_read_b128 v[124:127], v243 offset:8192\n" \
  "s_waitcnt lgkmcnt(4)\n" \
  "v_smfmac_f32_16x16x64_bf16 v[60:63], v[108:111], a[32:39], v242\n" \
  "s_waitcnt lgkmcnt(3)\n" \
  "v_smfmac_f32_16x16x64_bf16 v[60:63], v[112:115], a[40:47], v242\n" \
  "s_waitcnt lgkmcnt(2)\n" \
  "v_smfmac_f32_16x16x64_bf16 v[60:63], v[116:119], a[48:55], v242\n" \
  "s_waitcnt lgkmcnt(1)\n" \
  "v_smfmac_f32_16x16x64_bf16 v[60:63], v[120:123], a[56:63], v242\n" \
  "s_waitcnt lgkmcnt(0)\n" \
  "v_smfmac_f32_16x16x64_bf16 v[60:63], v[124:127], a[64:71], v242\n" \
  "s_nop 2\n" \
  "v_smfmac_f32_16x16x64_bf16 v[64:67], v[100:103], a[80:87], v242\n" \
  "s_nop 2\n" \
  "v_smfmac_f32_16x16x64_bf16 v[64:67], v[104:107], a[88:95], v242\n" \
  "v_add_f32 v60, v60, v62\n" \
  "v_add_f32 v61, v61, v63\n" \
  "v_cvt_pk_bf16_f32 v88, v60, v61\n" \
  "v_cmp_u_f32 vcc, v60, v61\n" \
  "v_smfmac_f32_16x16x64_bf16 v[64:67], v[96:99], a[96:103], v242\n" \
  "s_cbranch_vccnz .LV_slow_%=\n" \
  ".LV_back_%=:\n" \
  "ds_write_b32 v244, v88\n" \
  "v_smfmac_f32_16x16x64_bf16 v[64:67], v[108:111], a[104:111], v242\n" \
  "v_smfmac_f32_16x16x64_bf16 v[64:67], v[112:115], a[112:119], v242\n" \
  "v_smfmac_f32_16x16x64_bf16 v[64:67], v[116:119], a[120:127], v242\n" \
  "v_smfmac_f32_16x16x64_bf16 v[64:67], v[120:123], a[128:135], v242\n" \
  "s_waitcnt lgkmcnt(0)\n" \
  "s_waitcnt vmcnt(0) \n" \
  "s_barrier\n" \
  "ds_read_b64_tr_b16 v[92:93], v245\n" \
  "ds_read_b64_tr_b16 v[94:95], v245 offset:512\n" \
  "v_smfmac_f32_16x16x64_bf16 v[64:67], v[124:127], a[136:143], v242\n" \
  "ds_read_b64_tr_b16 v[96:97], v245 offset:1024\n" \
  "ds_read_b64_tr_b16 v[98:99], v245 offset:1536\n" \
  "s_nop 5\n" \
  "v_add_f32 v64, v64, v66\n" \
  "v_add_f32 v65, v65, v67\n" \
  "v_cvt_pk_bf16_f32 v89, v64, v65\n" \
  "s_waitcnt lgkmcnt(2)\n" \
  "v_smfmac_f32_16x16x64_bf16 v[76:79], v[92:95], a[208:215], v248\n" \
  "ds_write_b32 v244, v89 offset:256\n" \
  "s_waitcnt lgkmcnt(1)\n" \
  "v_smfmac_f32_16x16x64_bf16 v[76:79], v[96:99], a[216:223], v248\n" \
  "v_smfmac_f32_16x16x64_bf16 v[68:71], v[92:95], a[144:151], v248\n" \
  "v_smfmac_f32_16x16x64_bf16 v[72:75], v[92:95], a[176:183], v248\n" \
  "buffer_store_dwordx2 v[56:57], off, s[40:43], %44 sc1\n" \
  "v_smfmac_f32_16x16x64_bf16 v[68:71], v[96:99], a[152:159], v248\n" \
  "s_waitcnt lgkmcnt(0)\n" \
  "s_barrier\n" \
  "v_smfmac_f32_16x16x64_bf16 v[80:83], v[92:95], a[240:247], v248\n" \
  "ds_read_b64_tr_b16 v[102:103], v245 offset:768\n" \
  "ds_read_b64_tr_b16 v[100:101], v245 offset:256\n" \
  "v_smfmac_f32_16x16x64_bf16 v[84:87], v[92:95], v[16:23], v248\n" \
  "ds_read_b64_tr_b16 v[94:95], v245 offset:1792\n" \
  "ds_read_b64_tr_b16 v[92:93], v245 offset:1280\n" \
  "v_smfmac_f32_16x16x64_bf16 v[80:83], v[96:99], a[248:255], v248\n" \
  "v_smfmac_f32_16x16x64_bf16 v[72:75], v[96:99], a[184:191], v248\n" \
  "s_waitcnt lgkmcnt(2)\n" \
  "v_smfmac_f32_16x16x64_bf16 v[68:71], v[100:103], a[160:167], v248\n" \
  "s_waitcnt lgkmcnt(0)\n" \
  "v_smfmac_f32_16x16x64_bf16 v[68:71], v[92:95], a[168:175], v248\n" \
  "s_nop 2\n" \
  "v_smfmac_f32_16x16x64_bf16 v[72:75], v[100:103], a[192:199], v248\n" \
  "s_nop 2\n" \
  "v_smfmac_f32_16x16x64_bf16 v[72:75], v[92:95], a[200:207], v248\n" \
  "s_nop 1\n" \
  "v_cvt_pk_bf16_f32 v68, v68, v69\n" \
  "v_smfmac_f32_16x16x64_bf16 v[84:87], v[96:99], v[24:31], v248\n" \
  "s_nop 0\n" \
  "v_cvt_pk_bf16_f32 v69, v70, v71\n" \
  "v_smfmac_f32_16x16x64_bf16 v[76:79], v[100:103], a[224:231], v248\n" \
  "v_smfmac_f32_16x16x64_bf16 v[80:83], v[100:103], v[0:7], v248\n" \
  "v_cvt_pk_bf16_f32 v71, v74, v75\n" \
  "v_smfmac_f32_16x16x64_bf16 v[84:87], v[100:103], v[32:39], v248\n" \
  "v_cvt_pk_bf16_f32 v70, v72, v73\n" \
  "v_smfmac_f32_16x16x64_bf16 v[76:79], v[92:95], a[232:239], v248\n" \
  "s_nop 1\n" \
  "v_permlane16_swap_b32 v69, v71\n" \
  "v_smfmac_f32_16x16x64_bf16 v[80:83], v[92:95], v[8:15], v248\n" \
  "s_nop 0\n" \
  "s_nop 0\n" \
  "v_permlane16_swap_b32 v68, v70\n" \
  "v_smfmac_f32_16x16x64_bf16 v[84:87], v[92:95], v[40:47], v248\n" \
  "buffer_store_dwordx4 v[68:71], v250, s[40:43], 0 offen sc1\n" \
  "v_cvt_pk_bf16_f32 v76, v76, v77\n" \
  "v_cvt_pk_bf16_f32 v77, v78, v79\n" \
  "s_nop 0\n" \
  "v_cvt_pk_bf16_f32 v78, v80, v81\n" \
  "v_cvt_pk_bf16_f32 v79, v82, v83\n" \
  "s_nop 0\n" \
  "v_permlane16_swap_b32 v76, v78\n" \
  "v_cvt_pk_bf16_f32 v84, v84, v85\n" \
  "v_cvt_pk_bf16_f32 v85, v86, v87\n" \
  "v_permlane16_swap_b32 v77, v79\n" \
  "buffer_store_dwordx4 v[76:79], v251, s[40:43], 0 offen sc1\n" \
  "buffer_store_dwordx2 v[84:85], v252, s[40:43], 0 offen sc1\n" \
  ".LV_loop_%=:\n" \
  "s_cmp_ge_u32 s48, 58\n" \
  "s_cbranch_scc1 .LV_cp_%=\n" \
  ".LV_cp_b_%=:\n" \
  "v_mov_b64 v[60:61], 0\n" \
  "v_mov_b64 v[62:63], 0\n" \
  "v_mov_b64 v[64:65], 0\n" \
  "v_mov_b64 v[66:67], 0\n" \
  "v_mov_b64 v[68:69], 0\n" \
  "v_mov_b64 v[70:71], 0\n" \
  "v_mov_b64 v[72:73], 0\n" \
  "v_mov_b64 v[74:75], 0\n" \
  "v_mov_b64 v[76:77], 0\n" \
  "v_mov_b64 v[78:79], 0\n" \
  "v_mov_b64 v[80:81], 0\n" \
  "v_mov_b64 v[82:83], 0\n" \
  "v_mov_b64 v[84:85], 0\n" \
  "v_mov_b64 v[86:87], 0\n" \
  "ds_read_b32 v92, v246\n" \
  "ds_read_b32 v93, v246 offset:256\n" \
  "v_mov_b64 v[94:95], 0\n" \
  "s_sub_u32 s60, s48, 1\n" \
  "s_cselect_b32 s60, 8192, s60\n" \
  "s_lshl_b32 s60, s60, 14\n" \
  "s_xor_b32 s60, s60, s50\n" \
  "buffer_store_dwordx4 v[88:91], v247, s[40:43], s60 offen sc1\n" \
  "s_waitcnt lgkmcnt(0)\n" \
  "buffer_store_dwordx4 v[92:95], v249, s[40:43], s60 offen sc1\n" \
  "s_add_u32 s48, s48, 1\n" \
  "v_mov_b32 v56, s48\n" \
  "s_waitcnt lgkmcnt(0)\n" \
  "s_cmp_gt_u32 s48, 57\n" \
  "s_cbranch_scc1 .LV_ef_%=\n" \
  ".LV_ef_b_%=:\n" \
  "s_branch .LV_rot_%=\n" \
  "s_nop 7\n" \
  ".LV_slow_%=:\n" \
  "s_bitcmp1_b32 s70, 0\n" \
  "s_cbranch_scc1 .LV_back_%=\n" \
  "s_waitcnt lgkmcnt(0)\n" \
  "s_cmp_lt_u32 s70, 10\n" \
  "s_nop 3\n" \
  "s_cbranch_scc1 .LV_nol_%=\n" \
  "v_max_u32 v217, v92, v93\n" \
  "v_max3_u32 v217, v217, v94, v95\n" \
  "v_max3_u32 v217, v217, v100, v101\n" \
  "v_max3_u32 v217, v217, v102, v103\n" \
  "v_max3_u32 v217, v217, v104, v105\n" \
  "v_max3_u32 v217, v217, v106, v107\n" \
  "v_max3_u32 v217, v217, v96, v97\n" \
  "v_max3_u32 v217, v217, v98, v99\n" \
  "v_max3_u32 v217, v217, v108, v109\n" \
  "v_max3_u32 v217, v217, v110, v111\n" \
  "v_max3_u32 v217, v217, v112, v113\n" \
  "v_max3_u32 v217, v217, v114, v115\n" \
  "v_max3_u32 v217, v217, v116, v117\n" \
  "v_max3_u32 v217, v217, v118, v119\n" \
  "v_max3_u32 v217, v217, v120, v121\n" \
  "v_max3_u32 v217, v217, v122, v123\n" \
  "v_max3_u32 v217, v217, v124, v125\n" \
  "v_max3_u32 v217, v217, v126, v127\n" \
  "v_cmp_eq_u32 vcc, -1, v217\n" \
  "s_cbranch_vccz .LV_back_%=\n" \
  ".LV_nol_%=:\n" \
  "s_add_u32 s70, s70, 2\n" \
  "s_cmp_gt_u32 s70, 65536\n" \
  "s_cbranch_scc1 .LV_dset_%=\n" \
  "s_barrier\n" \
  "v_mov_b64 v[60:61], 0\n" \
  "v_mov_b64 v[62:63], 0\n" \
  "v_mov_b64 v[64:65], 0\n" \
  "v_mov_b64 v[66:67], 0\n" \
  "v_mov_b64 v[68:69], 0\n" \
  "v_mov_b64 v[70:71], 0\n" \
  "v_mov_b64 v[72:73], 0\n" \
  "v_mov_b64 v[74:75], 0\n" \
  "v_mov_b64 v[76:77], 0\n" \
  "v_mov_b64 v[78:79], 0\n" \
  "v_mov_b64 v[80:81], 0\n" \
  "v_mov_b64 v[82:83], 0\n" \
  "v_mov_b64 v[84:85], 0\n" \
  "v_mov_b64 v[86:87], 0\n" \
  "s_branch .LV_rt_%=\n" \
  ".LV_dset_%=:\n" \
  "v_mov_b32 v112, 1\n" \
  "v_mov_b32 v113, s55\n" \
  "buffer_store_dwordx2 v[112:113], off, s[40:43], %48 offset:128 sc1\n" \
  "s_mov_b32 s70, 1\n" \
  "v_mov_b64 v[60:61], 0\n" \
  "v_mov_b64 v[62:63], 0\n" \
  "v_mov_b64 v[64:65], 0\n" \
  "v_mov_b64 v[66:67], 0\n" \
  "v_mov_b64 v[68:69], 0\n" \
  "v_mov_b64 v[70:71], 0\n" \
  "v_mov_b64 v[72:73], 0\n" \
  "v_mov_b64 v[74:75], 0\n" \
  "v_mov_b64 v[76:77], 0\n" \
  "v_mov_b64 v[78:79], 0\n" \
  "v_mov_b64 v[80:81], 0\n" \
  "v_mov_b64 v[82:83], 0\n" \
  "v_mov_b64 v[84:85], 0\n" \
  "v_mov_b64 v[86:87], 0\n" \
  "s_waitcnt vmcnt(0) lgkmcnt(0)\n" \
  "s_barrier\n" \
  "s_branch .LV_p_%=\n" \
  ".LV_cp_%=:\n" \
  "s_sub_u32 s60, s48, 58\n" \
  "s_lshl_b32 s60, s60, 20\n" \
  "s_add_u32 s60, s60, s47\n" \
  "s_lshr_b32 s62, s60, 8\n" \
  "s_and_b32 s62, s62, 8192\n" \
  "s_xor_b32 s62, s62, s60\n" \
  "buffer_store_dwordx4 v[68:71], v253, s[40:43], s62 offen sc1\n" \
  "buffer_store_dwordx4 v[76:79], v254, s[40:43], s62 offen sc1\n" \
  "buffer_store_dwordx2 v[84:85], v255, s[40:43], s62 offen sc1\n" \
  "s_nop 1\n" \
  "s_branch .LV_cp_b_%=\n" \
  ".LV_ef_%=:\n" \
  "s_cmp_eq_u32 s48, 64\n" \
  "s_cbranch_scc1 .LV_end_%=\n" \
  "s_waitcnt vmcnt(0)\n" \
  "s_barrier\n" \
  "buffer_store_dwordx2 v[56:57], off, s[40:43], %44 sc1\n" \
  "s_branch .LV_ef_b_%=\n" \
  ".LV_end_%=:\n" \
  "s_waitcnt vmcnt(0)\n" \
  "s_waitcnt vmcnt(0)\n" \
  "s_nop 7\n" \
  "s_endpgm\n"
#define V_ASM_VOPS "+{v242}"(ci.sidx), "+{v243}"(ci.l16), "+{v244}"(ci.viw), "+{v245}"(ci.fva), "+{v246}"(ci.pk3), "+{v247}"(ci.fvs), "+{v248}"(ci.sid2), "+{v249}"(ci.uo), "+{v250}"(ci.po0), "+{v251}"(ci.po1), "+{v252}"(ci.po2), "+{v253}"(ci.co0), "+{v254}"(ci.co1), "+{v255}"(ci.co2), "+{v200}"(ci.vq0), "+{v201}"(ci.vq1), "+{v202}"(ci.vq2), "+{v203}"(ci.vq3), "+{v204}"(ci.vo0), "+{v205}"(ci.vo1), "+{v206}"(ci.vo2), "+{v207}"(ci.vo3), "+{v208}"(ci.v50), "+{v209}"(ci.v51), "+{v210}"(ci.v52), "+{v211}"(ci.v53), "+{v212}"(ci.tro0), "+{v213}"(ci.tro1), "+{v214}"(ci.m0o), "+{v215}"(ci.tro2), "+{v216}"(ci.tro3), "+{v217}"(ci.tro4)
#define V_ASM_SOPS "s"(ci.ws), "s"(ci.Wa), "s"(ci.Wb), "s"(ci.mem0), "s"(ci.w), "s"(ci.o_a), "s"(ci.o_b), "s"(ci.o_nx), "s"(ci.o_exp), "s"(ci.o_u), "s"(ci.lab), "s"(ci.f_p), "s"(ci.f_m), "s"(ci.f_u), "s"(ci.f_dur), "s"(ci.f_go), "s"(ci.o_ctl), "s"(ci.o_dbg), "s"(ci.push), "s"(ci.dret), "s"(ci.u5), "s"(ci.o_qx), "s"(ci.f_s), "s"(ci.f_r)
#define V_ASM_CLOB "v0", "v1", "v2", "v3", "v4", "v5", "v6", "v7", "v8", "v9", "v10", "v11", "v12", "v13", "v14", "v15", "v16", "v17", "v18", "v19", "v20", "v21", "v22", "v23", "v24", "v25", "v26", "v27", "v28", "v29", "v30", "v31", "v32", "v33", "v34", "v35", "v36", "v37", "v38", "v39", "v40", "v41", "v42", "v43", "v44", "v45", "v46", "v47", "v48", "v49", "v50", "v51", "v52", "v53", "v54", "v55", "v56", "v57", "v58", "v59", "v60", "v61", "v62", "v63", "v64", "v65", "v66", "v67", "v68", "v69", "v70", "v71", "v72", "v73", "v74", "v75", "v76", "v77", "v78", "v79", "v80", "v81", "v82", "v83", "v84", "v85", "v86", "v87", "v88", "v89", "v90", "v91", "v92", "v93", "v94", "v95", "v96", "v97", "v98", "v99", "v100", "v101", "v102", "v103", "v104", "v105", "v106", "v107", "v108", "v109", "v110", "v111", "v112", "v113", "v114", "v115", "v116", "v117", "v118", "v119", "v120", "v121", "v122", "v123", "v124", "v125", "v126", "v127", "v128", "v129", "v130", "v131", "v132", "v133", "v134", "v135", "v136", "v137", "v138", "v139", "v140", "v141", "v142", "v143", "v144", "v145", "v146", "v147", "v148", "v149", "v150", "v151", "v152", "v153", "v154", "v155", "v156", "v157", "v158", "v159", "v160", "v161", "v162", "v163", "v164", "v165", "v166", "v167", "v168", "v169", "v170", "v171", "v172", "v173", "v174", "v175", "v176", "v177", "v178", "v179", "v180", "v181", "v182", "v183", "v184", "v185", "v186", "v187", "v188", "v189", "v190", "v191", "v192", "v193", "v194", "v195", "v196", "v197", "v198", "v199", "a0", "a1", "a2", "a3", "a4", "a5", "a6", "a7", "a8", "a9", "a10", "a11", "a12", "a13", "a14", "a15", "a16", "a17", "a18", "a19", "a20", "a21", "a22", "a23", "a24", "a25", "a26", "a27", "a28", "a29", "a30", "a31", "a32", "a33", "a34", "a35", "a36", "a37", "a38", "a39", "a40", "a41", "a42", "a43", "a44", "a45", "a46", "a47", "a48", "a49", "a50", "a51", "a52", "a53", "a54", "a55", "a56", "a57", "a58", "a59", "a60", "a61", "a62", "a63", "a64", "a65", "a66", "a67", "a68", "a69", "a70", "a71", "a72", "a73", "a74", "a75", "a76", "a77", "a78", "a79", "a80", "a81", "a82", "a83", "a84", "a85", "a86", "a87", "a88", "a89", "a90", "a91", "a92", "a93", "a94", "a95", "a96", "a97", "a98", "a99", "a100", "a101", "a102", "a103", "a104", "a105", "a106", "a107", "a108", "a109", "a110", "a111", "a112", "a113", "a114", "a115", "a116", "a117", "a118", "a119", "a120", "a121", "a122", "a123", "a124", "a125", "a126", "a127", "a128", "a129", "a130", "a131", "a132", "a133", "a134", "a135", "a136", "a137", "a138", "a139", "a140", "a141", "a142", "a143", "a144", "a145", "a146", "a147", "a148", "a149", "a150", "a151", "a152", "a153", "a154", "a155", "a156", "a157", "a158", "a159", "a160", "a161", "a162", "a163", "a164", "a165", "a166", "a167", "a168", "a169", "a170", "a171", "a172", "a173", "a174", "a175", "a176", "a177", "a178", "a179", "a180", "a181", "a182", "a183", "a184", "a185", "a186", "a187", "a188", "a189", "a190", "a191", "a192", "a193", "a194", "a195", "a196", "a197", "a198", "a199", "a200", "a201", "a202", "a203", "a204", "a205", "a206", "a207", "a208", "a209", "a210", "a211", "a212", "a213", "a214", "a215", "a216", "a217", "a218", "a219", "a220", "a221", "a222", "a223", "a224", "a225", "a226", "a227", "a228", "a229", "a230", "a231", "a232", "a233", "a234", "a235", "a236", "a237", "a238", "a239", "a240", "a241", "a242", "a243", "a244", "a245", "a246", "a247", "a248", "a249", "a250", "a251", "a252", "a253", "a254", "a255", "s40", "s41", "s42", "s43", "s44", "s45", "s46", "s47", "s48", "s49", "s50", "s51", "s52", "s53", "s54", "s55", "s56", "s57", "s58", "s59", "s60", "s61", "s62", "s63", "s64", "s65", "s66", "s67", "s68", "s69", "s70", "s71", "s72", "s73", "s74", "s75", "s76", "s77", "s78", "s79", "s80", "s81", "s82", "s83", "s84", "s85", "s86", "s87", "s88", "s89", "s90", "s91", "s92", "s93", "s94", "s95", "s96", "s97", "s98", "s99", "vcc", "scc", "memory"
#define W_ASM_TEXT \
  "s_cmp_gt_u32 %36, 0\n" \
  "s_cbranch_scc1 .LW_other_%=\n" \
  "s_mov_b64 s[40:41], %32\n" \
  "s_and_b32 s41, s41, 0xffff\n" \
  "s_mov_b32 s42, 255852544\n" \
  "s_mov_b32 s43, 0x00020000\n" \
  "s_mov_b32 s46, 4718592\n" \
  "s_mov_b32 s47, 0x00020000\n" \
  "s_lshl_b32 s72, %36, 11\n" \
  "buffer_load_dword v196, off, s[40:43], %48 sc1\n" \
  "s_mov_b64 s[44:45], %34\n" \
  "s_and_b32 s45, s45, 0xffff\n" \
  "s_add_u32 s60, %38, 0\n" \
  "s_lshl_b32 s61, s72, 1\n" \
  "s_add_u32 m0, s61, 16384\n" \
  "s_nop 0\n" \
  "buffer_load_dwordx4 v204, s[44:47], s60 offen lds\n" \
  "buffer_load_dwordx4 v205, s[44:47], s60 offen offset:1024 lds\n" \
  "buffer_load_dwordx4 v206, s[44:47], s60 offen offset:2048 lds\n" \
  "buffer_load_dwordx4 v207, s[44:47], s60 offen offset:3072 lds\n" \
  "s_add_u32 s60, %38, 18432\n" \
  "s_lshl_b32 s61, s72, 1\n" \
  "s_add_u32 m0, s61, 32768\n" \
  "s_nop 0\n" \
  "buffer_load_dwordx4 v204, s[44:47], s60 offen lds\n" \
  "buffer_load_dwordx4 v205, s[44:47], s60 offen offset:1024 lds\n" \
  "buffer_load_dwordx4 v206, s[44:47], s60 offen offset:2048 lds\n" \
  "buffer_load_dwordx4 v207, s[44:47], s60 offen offset:3072 lds\n" \
  "s_add_u32 s60, %38, 256\n" \
  "s_lshl_b32 s61, s72, 1\n" \
  "s_add_u32 m0, s61, 49152\n" \
  "s_nop 0\n" \
  "buffer_load_dwordx4 v204, s[44:47], s60 offen lds\n" \
  "buffer_load_dwordx4 v205, s[44:47], s60 offen offset:1024 lds\n" \
  "buffer_load_dwordx4 v206, s[44:47], s60 offen offset:2048 lds\n" \
  "buffer_load_dwordx4 v207, s[44:47], s60 offen offset:3072 lds\n" \
  "s_add_u32 s60, %38, 18688\n" \
  "s_lshl_b32 s61, s72, 1\n" \
  "s_add_u32 m0, s61, 65536\n" \
  "s_nop 0\n" \
  "buffer_load_dwordx4 v204, s[44:47], s60 offen lds\n" \
  "buffer_load_dwordx4 v205, s[44:47], s60 offen offset:1024 lds\n" \
  "buffer_load_dwordx4 v206, s[44:47], s60 offen offset:2048 lds\n" \
  "buffer_load_dwordx4 v207, s[44:47], s60 offen offset:3072 lds\n" \
  "s_add_u32 s60, %38, 512\n" \
  "s_lshl_b32 s61, s72, 1\n" \
  "s_add_u32 m0, s61, 81920\n" \
  "s_nop 0\n" \
  "buffer_load_dwordx4 v204, s[44:47], s60 offen lds\n" \
  "buffer_load_dwordx4 v205, s[44:47], s60 offen offset:1024 lds\n" \
  "buffer_load_dwordx4 v206, s[44:47], s60 offen offset:2048 lds\n" \
  "buffer_load_dwordx4 v207, s[44:47], s60 offen offset:3072 lds\n" \
  "s_add_u32 s60, %38, 18944\n" \
  "s_lshl_b32 s61, s72, 1\n" \
  "s_add_u32 m0, s61, 98304\n" \
  "s_nop 0\n" \
  "buffer_load_dwordx4 v204, s[44:47], s60 offen lds\n" \
  "buffer_load_dwordx4 v205, s[44:47], s60 offen offset:1024 lds\n" \
  "buffer_load_dwordx4 v206, s[44:47], s60 offen offset:2048 lds\n" \
  "buffer_load_dwordx4 v207, s[44:47], s60 offen offset:3072 lds\n" \
  "s_add_u32 s60, %38, 768\n" \
  "s_lshl_b32 s61, s72, 1\n" \
  "s_add_u32 m0, s61, 114688\n" \
  "s_nop 0\n" \
  "buffer_load_dwordx4 v204, s[44:47], s60 offen lds\n" \
  "buffer_load_dwordx4 v205, s[44:47], s60 offen offset:1024 lds\n" \
  "buffer_load_dwordx4 v206, s[44:47], s60 offen offset:2048 lds\n" \
  "buffer_load_dwordx4 v207, s[44:47], s60 offen offset:3072 lds\n" \
  "s_add_u32 s60, %38, 19200\n" \
  "s_lshl_b32 s61, s72, 1\n" \
  "s_add_u32 m0, s61, 131072\n" \
  "s_nop 0\n" \
  "buffer_load_dwordx4 v204, s[44:47], s60 offen lds\n" \
  "buffer_load_dwordx4 v205, s[44:47], s60 offen offset:1024 lds\n" \
  "buffer_load_dwordx4 v206, s[44:47], s60 offen offset:2048 lds\n" \
  "buffer_load_dwordx4 v207, s[44:47], s60 offen offset:3072 lds\n" \
  "s_mov_b32 s60, %38\n" \
  "s_lshl_b32 s61, s72, 1\n" \
  "s_add_u32 m0, s61, 147456\n" \
  "s_nop 0\n" \
  "buffer_load_dwordx4 v208, s[44:47], s60 offen lds\n" \
  "buffer_load_dwordx4 v209, s[44:47], s60 offen offset:1024 lds\n" \
  "buffer_load_dwordx4 v210, s[44:47], s60 offen offset:2048 lds\n" \
  "buffer_load_dwordx4 v211, s[44:47], s60 offen offset:3072 lds\n" \
  "s_mov_b64 s[44:45], %35\n" \
  "s_and_b32 s45, s45, 0xffff\n" \
  "s_lshr_b32 s60, s72, 3\n" \
  "s_mov_b32 m0, s72\n" \
  "s_nop 0\n" \
  "buffer_load_dwordx4 v214, s[44:47], s60 offen lds\n" \
  "s_add_u32 s60, s60, 128\n" \
  "s_add_u32 m0, s72, 1024\n" \
  "s_nop 0\n" \
  "buffer_load_dwordx4 v214, s[44:47], s60 offen lds\n" \
  "s_movk_i32 s60, 1024\n" \
  "s_movk_i32 m0, 8192\n" \
  "s_nop 0\n" \
  "buffer_load_dwordx4 v214, s[44:47], s60 offen lds\n" \
  "s_waitcnt vmcnt(27)\n" \
  "s_barrier\n" \
  "v_add_u32 v194, 16384, v215\n" \
  "v_xor_b32 v195, 2176, v194\n" \
  "ds_read_b64_tr_b16 a[144:145], v194\n" \
  "ds_read_b64_tr_b16 a[146:147], v194 offset:512\n" \
  "ds_read_b64_tr_b16 a[148:149], v195\n" \
  "ds_read_b64_tr_b16 a[150:151], v195 offset:512\n" \
  "ds_read_b64_tr_b16 a[152:153], v194 offset:8192\n" \
  "ds_read_b64_tr_b16 a[154:155], v194 offset:8704\n" \
  "ds_read_b64_tr_b16 a[156:157], v195 offset:8192\n" \
  "ds_read_b64_tr_b16 a[158:159], v195 offset:8704\n" \
  "v_add_u32 v194, 32768, v215\n" \
  "v_xor_b32 v195, 2176, v194\n" \
  "ds_read_b64_tr_b16 a[160:161], v194\n" \
  "ds_read_b64_tr_b16 a[162:163], v194 offset:512\n" \
  "ds_read_b64_tr_b16 a[164:165], v195\n" \
  "ds_read_b64_tr_b16 a[166:167], v195 offset:512\n" \
  "ds_read_b64_tr_b16 a[168:169], v194 offset:8192\n" \
  "ds_read_b64_tr_b16 a[170:171], v194 offset:8704\n" \
  "ds_read_b64_tr_b16 a[172:173], v195 offset:8192\n" \
  "ds_read_b64_tr_b16 a[174:175], v195 offset:8704\n" \
  "v_add_u32 v194, 49152, v215\n" \
  "v_xor_b32 v195, 2176, v194\n" \
  "ds_read_b64_tr_b16 a[176:177], v194\n" \
  "ds_read_b64_tr_b16 a[178:179], v194 offset:512\n" \
  "ds_read_b64_tr_b16 a[180:181], v195\n" \
  "ds_read_b64_tr_b16 a[182:183], v195 offset:512\n" \
  "ds_read_b64_tr_b16 a[184:185], v194 offset:8192\n" \
  "ds_read_b64_tr_b16 a[186:187], v194 offset:8704\n" \
  "ds_read_b64_tr_b16 a[188:189], v195 offset:8192\n" \
  "ds_read_b64_tr_b16 a[190:191], v195 offset:8704\n" \
  "s_waitcnt lgkmcnt(0)\n" \
  "s_barrier\n" \
  "s_mov_b64 s[44:45], %33\n" \
  "s_and_b32 s45, s45, 0xffff\n" \
  "s_add_u32 s60, %37, 0\n" \
  "s_lshl_b32 s61, s72, 1\n" \
  "s_add_u32 m0, s61, 16384\n" \
  "s_nop 0\n" \
  "buffer_load_dwordx4 v200, s[44:47], s60 offen lds\n" \
  "buffer_load_dwordx4 v201, s[44:47], s60 offen offset:1024 lds\n" \
  "buffer_load_dwordx4 v202, s[44:47], s60 offen offset:2048 lds\n" \
  "buffer_load_dwordx4 v203, s[44:47], s60 offen offset:3072 lds\n" \
  "s_add_u32 s60, %37, 524288\n" \
  "s_lshl_b32 s61, s72, 1\n" \
  "s_add_u32 m0, s61, 32768\n" \
  "s_nop 0\n" \
  "buffer_load_dwordx4 v200, s[44:47], s60 offen lds\n" \
  "buffer_load_dwordx4 v201, s[44:47], s60 offen offset:1024 lds\n" \
  "buffer_load_dwordx4 v202, s[44:47], s60 offen offset:2048 lds\n" \
  "buffer_load_dwordx4 v203, s[44:47], s60 offen offset:3072 lds\n" \
  "s_add_u32 s60, %37, 1048576\n" \
  "s_lshl_b32 s61, s72, 1\n" \
  "s_add_u32 m0, s61, 49152\n" \
  "s_nop 0\n" \
  "buffer_load_dwordx4 v200, s[44:47], s60 offen lds\n" \
  "buffer_load_dwordx4 v201, s[44:47], s60 offen offset:1024 lds\n" \
  "buffer_load_dwordx4 v202, s[44:47], s60 offen offset:2048 lds\n" \
  "buffer_load_dwordx4 v203, s[44:47], s60 offen offset:3072 lds\n" \
  "v_readfirstlane_b32 s55, v196\n" \
  "s_add_u32 s55, s55, 1\n" \
  "s_waitcnt vmcnt(27)\n" \
  "s_barrier\n" \
  "v_add_u32 v194, 65536, v215\n" \
  "v_xor_b32 v195, 2176, v194\n" \
  "ds_read_b64_tr_b16 a[192:193], v194\n" \
  "ds_read_b64_tr_b16 a[194:195], v194 offset:512\n" \
  "ds_read_b64_tr_b16 a[196:197], v195\n" \
  "ds_read_b64_tr_b16 a[198:199], v195 offset:512\n" \
  "ds_read_b64_tr_b16 a[200:201], v194 offset:8192\n" \
  "ds_read_b64_tr_b16 a[202:203], v194 offset:8704\n" \
  "ds_read_b64_tr_b16 a[204:205], v195 offset:8192\n" \
  "ds_read_b64_tr_b16 a[206:207], v195 offset:8704\n" \
  "v_add_u32 v194, 81920, v215\n" \
  "v_xor_b32 v195, 2176, v194\n" \
  "ds_read_b64_tr_b16 a[208:209], v194\n" \
  "ds_read_b64_tr_b16 a[210:211], v194 offset:512\n" \
  "ds_read_b64_tr_b16 a[212:213], v195\n" \
  "ds_read_b64_tr_b16 a[214:215], v195 offset:512\n" \
  "ds_read_b64_tr_b16 a[216:217], v194 offset:8192\n" \
  "ds_read_b64_tr_b16 a[218:219], v194 offset:8704\n" \
  "ds_read_b64_tr_b16 a[220:221], v195 offset:8192\n" \
  "ds_read_b64_tr_b16 a[222:223], v195 offset:8704\n" \
  "v_add_u32 v194, 98304, v215\n" \
  "v_xor_b32 v195, 2176, v194\n" \
  "ds_read_b64_tr_b16 a[224:225], v194\n" \
  "ds_read_b64_tr_b16 a[226:227], v194 offset:512\n" \
  "ds_read_b64_tr_b16 a[228:229], v195\n" \
  "ds_read_b64_tr_b16 a[230:231], v195 offset:512\n" \
  "ds_read_b64_tr_b16 a[232:233], v194 offset:8192\n" \
  "ds_read_b64_tr_b16 a[234:235], v194 offset:8704\n" \
  "ds_read_b64_tr_b16 a[236:237], v195 offset:8192\n" \
  "ds_read_b64_tr_b16 a[238:239], v195 offset:8704\n" \
  "s_waitcnt lgkmcnt(0)\n" \
  "s_barrier\n" \
  "s_add_u32 s60, %37, 1572864\n" \
  "s_lshl_b32 s61, s72, 1\n" \
  "s_add_u32 m0, s61, 65536\n" \
  "s_nop 0\n" \
  "buffer_load_dwordx4 v200, s[44:47], s60 offen lds\n" \
  "buffer_load_dwordx4 v201, s[44:47], s60 offen offset:1024 lds\n" \
  "buffer_load_dwordx4 v202, s[44:47], s60 offen offset:2048 lds\n" \
  "buffer_load_dwordx4 v203, s[44:47], s60 offen offset:3072 lds\n" \
  "s_add_u32 s60, %37, 2097152\n" \
  "s_lshl_b32 s61, s72, 1\n" \
  "s_add_u32 m0, s61, 81920\n" \
  "s_nop 0\n" \
  "buffer_load_dwordx4 v200, s[44:47], s60 offen lds\n" \
  "buffer_load_dwordx4 v201, s[44:47], s60 offen offset:1024 lds\n" \
  "buffer_load_dwordx4 v202, s[44:47], s60 offen offset:2048 lds\n" \
  "buffer_load_dwordx4 v203, s[44:47], s60 offen offset:3072 lds\n" \
  "s_add_u32 s60, %37, 2621440\n" \
  "s_lshl_b32 s61, s72, 1\n" \
  "s_add_u32 m0, s61, 98304\n" \
  "s_nop 0\n" \
  "buffer_load_dwordx4 v200, s[44:47], s60 offen lds\n" \
  "buffer_load_dwordx4 v201, s[44:47], s60 offen offset:1024 lds\n" \
  "buffer_load_dwordx4 v202, s[44:47], s60 offen offset:2048 lds\n" \
  "buffer_load_dwordx4 v203, s[44:47], s60 offen offset:3072 lds\n" \
  "s_waitcnt vmcnt(27)\n" \
  "s_barrier\n" \
  "s_waitcnt lgkmcnt(0)\n" \
  "s_barrier\n" \
  "s_add_u32 s60, %37, 3145728\n" \
  "s_lshl_b32 s61, s72, 1\n" \
  "s_add_u32 m0, s61, 114688\n" \
  "s_nop 0\n" \
  "buffer_load_dwordx4 v200, s[44:47], s60 offen lds\n" \
  "buffer_load_dwordx4 v201, s[44:47], s60 offen offset:1024 lds\n" \
  "buffer_load_dwordx4 v202, s[44:47], s60 offen offset:2048 lds\n" \
  "buffer_load_dwordx4 v203, s[44:47], s60 offen offset:3072 lds\n" \
  "s_add_u32 s60, %37, 3670016\n" \
  "s_lshl_b32 s61, s72, 1\n" \
  "s_add_u32 m0, s61, 131072\n" \
  "s_nop 0\n" \
  "buffer_load_dwordx4 v200, s[44:47], s60 offen lds\n" \
  "buffer_load_dwordx4 v201, s[44:47], s60 offen offset:1024 lds\n" \
  "buffer_load_dwordx4 v202, s[44:47], s60 offen offset:2048 lds\n" \
  "buffer_load_dwordx4 v203, s[44:47], s60 offen offset:3072 lds\n" \
  "s_add_u32 s60, %37, 4194304\n" \
  "s_lshl_b32 s61, s72, 1\n" \
  "s_add_u32 m0, s61, 147456\n" \
  "s_nop 0\n" \
  "buffer_load_dwordx4 v200, s[44:47], s60 offen lds\n" \
  "buffer_load_dwordx4 v201, s[44:47], s60 offen offset:1024 lds\n" \
  "buffer_load_dwordx4 v202, s[44:47], s60 offen offset:2048 lds\n" \
  "buffer_load_dwordx4 v203, s[44:47], s60 offen offset:3072 lds\n" \
  "buffer_load_dwordx2 v[198:199], off, s[40:43], %47 sc1\n" \
  "s_waitcnt vmcnt(25)\n" \
  "s_barrier\n" \
  "v_add_u32 v194, 16384, v212\n" \
  "ds_read_b64_tr_b16 a[0:1], v194\n" \
  "ds_read_b64_tr_b16 a[2:3], v194 offset:512\n" \
  "ds_read_b64_tr_b16 a[4:5], v194 offset:8192\n" \
  "ds_read_b64_tr_b16 a[6:7], v194 offset:8704\n" \
  "v_add_u32 v195, 16384, v213\n" \
  "ds_read_b64_tr_b16 a[72:73], v195\n" \
  "ds_read_b64_tr_b16 a[74:75], v195 offset:512\n" \
  "ds_read_b64_tr_b16 a[76:77], v195 offset:8192\n" \
  "ds_read_b64_tr_b16 a[78:79], v195 offset:8704\n" \
  "v_add_u32 v194, 32768, v212\n" \
  "ds_read_b64_tr_b16 a[8:9], v194\n" \
  "ds_read_b64_tr_b16 a[10:11], v194 offset:512\n" \
  "ds_read_b64_tr_b16 a[12:13], v194 offset:8192\n" \
  "ds_read_b64_tr_b16 a[14:15], v194 offset:8704\n" \
  "v_add_u32 v195, 32768, v213\n" \
  "ds_read_b64_tr_b16 a[80:81], v195\n" \
  "ds_read_b64_tr_b16 a[82:83], v195 offset:512\n" \
  "ds_read_b64_tr_b16 a[84:85], v195 offset:8192\n" \
  "ds_read_b64_tr_b16 a[86:87], v195 offset:8704\n" \
  "v_add_u32 v194, 49152, v212\n" \
  "ds_read_b64_tr_b16 a[16:17], v194\n" \
  "ds_read_b64_tr_b16 a[18:19], v194 offset:512\n" \
  "ds_read_b64_tr_b16 a[20:21], v194 offset:8192\n" \
  "ds_read_b64_tr_b16 a[22:23], v194 offset:8704\n" \
  "v_add_u32 v195, 49152, v213\n" \
  "ds_read_b64_tr_b16 a[88:89], v195\n" \
  "ds_read_b64_tr_b16 a[90:91], v195 offset:512\n" \
  "ds_read_b64_tr_b16 a[92:93], v195 offset:8192\n" \
  "ds_read_b64_tr_b16 a[94:95], v195 offset:8704\n" \
  "s_waitcnt lgkmcnt(0)\n" \
  "s_waitcnt vmcnt(13)\n" \
  "s_barrier\n" \
  "v_add_u32 v194, 65536, v212\n" \
  "ds_read_b64_tr_b16 a[24:25], v194\n" \
  "ds_read_b64_tr_b16 a[26:27], v194 offset:512\n" \
  "ds_read_b64_tr_b16 a[28:29], v194 offset:8192\n" \
  "ds_read_b64_tr_b16 a[30:31], v194 offset:8704\n" \
  "v_add_u32 v195, 65536, v213\n" \
  "ds_read_b64_tr_b16 a[96:97], v195\n" \
  "ds_read_b64_tr_b16 a[98:99], v195 offset:512\n" \
  "ds_read_b64_tr_b16 a[100:101], v195 offset:8192\n" \
  "ds_read_b64_tr_b16 a[102:103], v195 offset:8704\n" \
  "v_add_u32 v194, 81920, v212\n" \
  "ds_read_b64_tr_b16 a[32:33], v194\n" \
  "ds_read_b64_tr_b16 a[34:35], v194 offset:512\n" \
  "ds_read_b64_tr_b16 a[36:37], v194 offset:8192\n" \
  "ds_read_b64_tr_b16 a[38:39], v194 offset:8704\n" \
  "v_add_u32 v195, 81920, v213\n" \
  "ds_read_b64_tr_b16 a[104:105], v195\n" \
  "ds_read_b64_tr_b16 a[106:107], v195 offset:512\n" \
  "ds_read_b64_tr_b16 a[108:109], v195 offset:8192\n" \
  "ds_read_b64_tr_b16 a[110:111], v195 offset:8704\n" \
  "v_add_u32 v194, 98304, v212\n" \
  "ds_read_b64_tr_b16 a[40:41], v194\n" \
  "ds_read_b64_tr_b16 a[42:43], v194 offset:512\n" \
  "ds_read_b64_tr_b16 a[44:45], v194 offset:8192\n" \
  "ds_read_b64_tr_b16 a[46:47], v194 offset:8704\n" \
  "v_add_u32 v195, 98304, v213\n" \
  "ds_read_b64_tr_b16 a[112:113], v195\n" \
  "ds_read_b64_tr_b16 a[114:115], v195 offset:512\n" \
  "ds_read_b64_tr_b16 a[116:117], v195 offset:8192\n" \
  "ds_read_b64_tr_b16 a[118:119], v195 offset:8704\n" \
  "s_waitcnt lgkmcnt(0)\n" \
  "s_waitcnt vmcnt(1)\n" \
  "s_barrier\n" \
  "v_add_u32 v194, 114688, v212\n" \
  "ds_read_b64_tr_b16 a[48:49], v194\n" \
  "ds_read_b64_tr_b16 a[50:51], v194 offset:512\n" \
  "ds_read_b64_tr_b16 a[52:53], v194 offset:8192\n" \
  "ds_read_b64_tr_b16 a[54:55], v194 offset:8704\n" \
  "v_add_u32 v195, 114688, v213\n" \
  "ds_read_b64_tr_b16 a[120:121], v195\n" \
  "ds_read_b64_tr_b16 a[122:123], v195 offset:512\n" \
  "ds_read_b64_tr_b16 a[124:125], v195 offset:8192\n" \
  "ds_read_b64_tr_b16 a[126:127], v195 offset:8704\n" \
  "v_add_u32 v194, 131072, v212\n" \
  "ds_read_b64_tr_b16 a[56:57], v194\n" \
  "ds_read_b64_tr_b16 a[58:59], v194 offset:512\n" \
  "ds_read_b64_tr_b16 a[60:61], v194 offset:8192\n" \
  "ds_read_b64_tr_b16 a[62:63], v194 offset:8704\n" \
  "v_add_u32 v195, 131072, v213\n" \
  "ds_read_b64_tr_b16 a[128:129], v195\n" \
  "ds_read_b64_tr_b16 a[130:131], v195 offset:512\n" \
  "ds_read_b64_tr_b16 a[132:133], v195 offset:8192\n" \
  "ds_read_b64_tr_b16 a[134:135], v195 offset:8704\n" \
  "v_add_u32 v194, 147456, v212\n" \
  "ds_read_b64_tr_b16 a[64:65], v194\n" \
  "ds_read_b64_tr_b16 a[66:67], v194 offset:512\n" \
  "ds_read_b64_tr_b16 a[68:69], v194 offset:8192\n" \
  "ds_read_b64_tr_b16 a[70:71], v194 offset:8704\n" \
  "v_add_u32 v195, 147456, v213\n" \
  "ds_read_b64_tr_b16 a[136:137], v195\n" \
  "ds_read_b64_tr_b16 a[138:139], v195 offset:512\n" \
  "ds_read_b64_tr_b16 a[140:141], v195 offset:8192\n" \
  "ds_read_b64_tr_b16 a[142:143], v195 offset:8704\n" \
  "s_waitcnt lgkmcnt(0)\n" \
  "s_waitcnt vmcnt(0)\n" \
  "v_readfirstlane_b32 s96, v198\n" \
  "v_readfirstlane_b32 s63, v199\n" \
  "s_cmp_eq_u32 s63, s55\n" \
  "s_cselect_b32 s96, s96, 0\n" \
  "s_cmp_ge_u32 s96, 0xc0000000\n" \
  "s_cbranch_scc1 .LW_go_x_%=\n" \
  "s_mov_b32 s69, 0\n" \
  ".LW_go_%=:\n" \
  "buffer_load_dwordx2 v[196:197], off, s[40:43], %47 sc1\n" \
  "s_waitcnt vmcnt(0)\n" \
  "v_readfirstlane_b32 s96, v196\n" \
  "v_readfirstlane_b32 s63, v197\n" \
  "s_cmp_eq_u32 s63, s55\n" \
  "s_cselect_b32 s96, s96, 0\n" \
  "s_cmp_ge_u32 s96, 0xc0000000\n" \
  "s_cbranch_scc1 .LW_go_x_%=\n" \
  "s_sleep 8\n" \
  "s_add_u32 s69, s69, 1\n" \
  "s_and_b32 s63, s69, 1023\n" \
  "s_cbranch_scc1 .LW_go_%=\n" \
  "s_cmp_gt_u32 s69, 2097152\n" \
  "s_cbranch_scc1 .LW_go_set_%=\n" \
  "buffer_load_dwordx2 v[196:197], off, s[40:43], %48 offset:128 sc1\n" \
  "s_waitcnt vmcnt(0)\n" \
  "v_readfirstlane_b32 s63, v197\n" \
  "s_cmp_eq_u32 s63, s55\n" \
  "s_cbranch_scc0 .LW_go_%=\n" \
  "s_branch .LW_go_x_%=\n" \
  ".LW_go_set_%=:\n" \
  "v_mov_b32 v196, 1\n" \
  "v_mov_b32 v197, s55\n" \
  "buffer_store_dwordx2 v[196:197], off, s[40:43], %48 offset:128 sc1\n" \
  "s_waitcnt vmcnt(0)\n" \
  ".LW_go_x_%=:\n" \
  "s_and_b32 s60, s96, 2\n" \
  "s_lshl_b32 s60, s60, 12\n" \
  "s_xor_b32 s74, %43, s60\n" \
  "s_xor_b32 s75, %46, s60\n" \
  "s_bfe_u32 s56, s96, 0x30010\n" \
  "s_lshl_b32 s56, s56, 12\n" \
  "s_or_b32 s56, s56, 0x200000\n" \
  "s_mov_b32 s57, 1052672\n" \
  "s_lshl_b32 s61, %42, 2\n" \
  "s_lshr_b32 s51, s96, s61\n" \
  "s_and_b32 s51, s51, 7\n" \
  "s_lshl_b32 s51, s51, 12\n" \
  "s_xor_b32 s51, s51, 4210688\n" \
  "s_mov_b32 s71, 0\n" \
  "s_add_u32 s45, s51, 886784\n" \
  "s_mov_b32 s46, -1\n" \
  "v_mov_b64 v[48:49], -1\n" \
  "v_mov_b64 v[50:51], -1\n" \
  "v_and_b32 v194, 3, v250\n" \
  "v_lshlrev_b32 v194, 2, v194\n" \
  "v_lshrrev_b32 v194, v194, s96\n" \
  "v_and_b32 v194, 7, v194\n" \
  "v_lshlrev_b32 v194, 12, v194\n" \
  "v_and_b32 v250, -4, v250\n" \
  "v_xor_b32 v250, v250, v194\n" \
  "v_and_b32 v194, 3, v252\n" \
  "v_lshlrev_b32 v194, 2, v194\n" \
  "v_lshrrev_b32 v194, v194, s96\n" \
  "v_and_b32 v194, 7, v194\n" \
  "v_lshlrev_b32 v194, 12, v194\n" \
  "v_and_b32 v252, -4, v252\n" \
  "v_xor_b32 v252, v252, v194\n" \
  "v_and_b32 v194, 3, v251\n" \
  "v_lshlrev_b32 v194, 2, v194\n" \
  "v_lshrrev_b32 v194, v194, s96\n" \
  "v_and_b32 v194, 7, v194\n" \
  "v_lshlrev_b32 v194, 12, v194\n" \
  "v_and_b32 v251, -4, v251\n" \
  "v_xor_b32 v251, v251, v194\n" \
  "v_xor_b32 v185, s56, v250\n" \
  "buffer_store_dwordx4 v[48:51], v185, s[40:43], 0 offen sc1\n" \
  "v_xor_b32 v186, s56, v252\n" \
  "buffer_store_dwordx2 v[48:49], v186, s[40:43], 0 offen sc1\n" \
  "s_xor_b32 s62, s56, s57\n" \
  "s_nop 1\n" \
  "v_xor_b32 v185, s62, v250\n" \
  "buffer_store_dwordx4 v[48:51], v185, s[40:43], 0 offen sc1\n" \
  "v_xor_b32 v186, s62, v252\n" \
  "buffer_store_dwordx2 v[48:49], v186, s[40:43], 0 offen sc1\n" \
  "s_andn2_b32 s60, 2, s96\n" \
  "s_lshl_b32 s60, s60, 12\n" \
  "s_xor_b32 s60, s60, 8192\n" \
  "s_xor_b32 s58, %39, s60\n" \
  "s_mov_b32 s49, 0\n" \
  "s_add_u32 s47, %53, s60\n" \
  "s_add_u32 s97, s60, 8388608\n" \
  "s_xor_b32 s50, %40, s60\n" \
  "s_mov_b32 s48, 0\n" \
  "s_mov_b32 s54, 0\n" \
  "s_cmp_eq_u32 %36, 0\n" \
  "s_cselect_b64 s[88:89], -1, 0\n" \
  "s_mov_b32 s68, 0\n" \
  "s_mov_b32 s70, 1\n" \
  "s_mov_b32 s73, 0\n" \
  "s_mov_b32 s44, 0\n" \
  "s_mov_b32 s53, 0\n" \
  "s_memrealtime s[86:87]\n" \
  "s_memtime s[76:77]\n" \
  "s_waitcnt lgkmcnt(0)\n" \
  "s_mov_b32 s90, s76\n" \
  "s_mov_b32 s87, 340\n" \
  "s_mov_b32 s64, 0x3e0293ee\n" \
  "s_mov_b32 s65, 0xbe0293ee\n" \
  "v_mov_b64 v[60:61], 0\n" \
  "v_mov_b64 v[62:63], 0\n" \
  "v_mov_b64 v[64:65], 0\n" \
  "v_mov_b64 v[66:67], 0\n" \
  "v_mov_b64 v[68:69], 0\n" \
  "v_mov_b64 v[70:71], 0\n" \
  "v_mov_b64 v[72:73], 0\n" \
  "v_mov_b64 v[74:75], 0\n" \
  "v_mov_b64 v[76:77], 0\n" \
  "v_mov_b64 v[78:79], 0\n" \
  "v_mov_b32 v56, 0\n" \
  "v_mov_b32 v57, s55\n" \
  "v_mov_b64 v[58:59], v[56:57]\n" \
  "v_mov_b64 v[82:83], 0\n" \
  "s_mov_b32 s67, 2130706432\n" \
  "s_waitcnt vmcnt(0) lgkmcnt(0)\n" \
  "s_barrier\n" \
  "s_branch .LW_p_%=\n" \
  "s_nop 7\n" \
  ".p2align 6\n" \
  ".fill (((40 - (.LW_p_%= - .LW_rot_%=)) & 63) >> 2), 4, 0xBF800000\n" \
  ".LW_rot_%=:\n" \
  "s_xor_b32 s51, s51, s56\n" \
  "s_xor_b32 s71, s71, s56\n" \
  "v_xor_b32 v250, s56, v250\n" \
  "v_xor_b32 v252, s56, v252\n" \
  "s_mov_b32 s60, s56\n" \
  "s_mov_b32 s56, s57\n" \
  "s_mov_b32 s57, s60\n" \
  "s_add_u32 s59, s51, 819200\n" \
  "s_mov_b32 m0, 0\n" \
  "s_mov_b32 s70, 0\n" \
  "s_mov_b32 s68, 0\n" \
  "s_waitcnt lgkmcnt(0)\n" \
  "s_mov_b32 s69, 0\n" \
  ".LW_tw_%=:\n" \
  "v_readfirstlane_b32 s63, v52\n" \
  "s_cmp_lg_u32 s63, s48\n" \
  "s_cbranch_scc1 .LW_tw_re_%=\n" \
  "v_readfirstlane_b32 s53, v54\n" \
  ".LW_rt_%=:\n" \
  "s_memrealtime s[60:61]\n" \
  "s_add_u32 s73, s53, s44\n" \
  "s_waitcnt lgkmcnt(0)\n" \
  "s_sub_u32 s62, s73, s60\n" \
  "s_cmp_ge_u32 s62, 2000\n" \
  "s_cbranch_scc1 .LW_lat_%=\n" \
  "s_sub_u32 s62, s62, 30\n" \
  ".LW_slp_%=:\n" \
  "s_cmp_le_i32 s62, 0\n" \
  "s_cbranch_scc1 .LW_clk_%=\n" \
  "s_sleep 1\n" \
  "s_sub_u32 s62, s62, 3\n" \
  "s_branch .LW_slp_%=\n" \
  ".LW_clk_%=:\n" \
  "s_memrealtime s[60:61]\n" \
  "s_waitcnt lgkmcnt(0)\n" \
  "s_sub_u32 s62, s73, s60\n" \
  "s_cmp_gt_i32 s62, 21\n" \
  "s_cbranch_scc1 .LW_clk_%=\n" \
  "s_cmp_le_i32 s62, 11\n" \
  "s_cbranch_scc1 .LW_clk2_%=\n" \
  "s_memrealtime s[76:77]\n" \
  "s_nop 4\n" \
  "s_memrealtime s[78:79]\n" \
  "s_nop 4\n" \
  "s_memrealtime s[80:81]\n" \
  "s_nop 4\n" \
  "s_memrealtime s[82:83]\n" \
  "s_waitcnt lgkmcnt(0)\n" \
  "s_mov_b32 s77, 1\n" \
  "s_cmp_eq_u32 s78, s76\n" \
  "s_addc_u32 s77, s77, 0\n" \
  "s_cmp_eq_u32 s80, s76\n" \
  "s_addc_u32 s77, s77, 0\n" \
  "s_cmp_eq_u32 s82, s76\n" \
  "s_addc_u32 s77, s77, 0\n" \
  "s_mul_i32 s77, s77, 24\n" \
  "s_lshr_b32 s78, s87, 2\n" \
  "s_sub_u32 s77, s78, s77\n" \
  "s_max_i32 s77, s77, 0\n" \
  "s_sub_u32 s76, s73, s76\n" \
  "s_mul_i32 s76, s76, s87\n" \
  "s_ashr_i32 s76, s76, 4\n" \
  "s_sub_u32 s76, s76, s77\n" \
  "s_sub_u32 s76, s76, 200\n" \
  "s_max_i32 s76, s76, 0\n" \
  "s_min_i32 s76, s76, 320\n" \
  "s_lshr_b32 s76, s76, 3\n" \
  "s_lshl_b32 s76, s76, 2\n" \
  "s_mov_b32 s68, 1\n" \
  "s_getpc_b64 s[78:79]\n" \
  "s_sub_u32 s76, 180, s76\n" \
  "s_add_u32 s78, s78, s76\n" \
  "s_addc_u32 s79, s79, 0\n" \
  "s_setpc_b64 s[78:79]\n" \
  "s_nop 1\n" \
  "s_nop 1\n" \
  "s_nop 1\n" \
  "s_nop 1\n" \
  "s_nop 1\n" \
  "s_nop 1\n" \
  "s_nop 1\n" \
  "s_nop 1\n" \
  "s_nop 1\n" \
  "s_nop 1\n" \
  "s_nop 1\n" \
  "s_nop 1\n" \
  "s_nop 1\n" \
  "s_nop 1\n" \
  "s_nop 1\n" \
  "s_nop 1\n" \
  "s_nop 1\n" \
  "s_nop 1\n" \
  "s_nop 1\n" \
  "s_nop 1\n" \
  "s_nop 1\n" \
  "s_nop 1\n" \
  "s_nop 1\n" \
  "s_nop 1\n" \
  "s_nop 1\n" \
  "s_nop 1\n" \
  "s_nop 1\n" \
  "s_nop 1\n" \
  "s_nop 1\n" \
  "s_nop 1\n" \
  "s_nop 1\n" \
  "s_nop 1\n" \
  "s_nop 1\n" \
  "s_nop 1\n" \
  "s_nop 1\n" \
  "s_nop 1\n" \
  "s_nop 1\n" \
  "s_nop 1\n" \
  "s_nop 1\n" \
  "s_nop 1\n" \
  ".LW_noclk_%=:\n" \
  "buffer_load_dwordx4 v243, s[40:43], s59 offen sc1 lds\n" \
  "buffer_load_dwordx4 v243, s[40:43], s59 offen offset:1024 sc1 lds\n" \
  "buffer_load_dwordx4 v243, s[40:43], s59 offen offset:2048 sc1 lds\n" \
  "buffer_load_dwordx4 v243, s[40:43], s59 offen offset:3072 sc1 lds\n" \
  "s_add_u32 s60, s59, 32768\n" \
  "s_movk_i32 m0, 4096\n" \
  "s_nop 0\n" \
  "buffer_load_dwordx4 v243, s[40:43], s60 offen sc1 lds\n" \
  "buffer_load_dwordx4 v243, s[40:43], s60 offen offset:1024 sc1 lds\n" \
  "buffer_load_dwordx4 v243, s[40:43], s60 offen offset:2048 sc1 lds\n" \
  "buffer_load_dwordx4 v243, s[40:43], s60 offen offset:3072 sc1 lds\n" \
  "s_add_u32 s60, s59, 65536\n" \
  "s_movk_i32 m0, 8192\n" \
  "s_nop 0\n" \
  "buffer_load_dwordx4 v243, s[40:43], s60 offen sc1 lds\n" \
  ".LW_g3_%=:\n" \
  "s_waitcnt vmcnt(5)\n" \
  "s_barrier\n" \
  ".LW_p_%=:\n" \
  "ds_read_b128 v[84:87], v243\n" \
  "ds_read_b128 v[88:91], v243 offset:1024\n" \
  "ds_read_b128 v[92:95], v243 offset:2048\n" \
  "ds_read_b128 v[96:99], v243 offset:3072\n" \
  "s_waitcnt lgkmcnt(3)\n" \
  "v_smfmac_f32_16x16x64_bf16 v[60:63], v[84:87], a[0:7], v242\n" \
  "s_waitcnt vmcnt(0)\n" \
  "s_barrier\n" \
  "ds_read_b128 v[100:103], v243 offset:5120\n" \
  "ds_read_b128 v[104:107], v243 offset:4096\n" \
  "s_waitcnt lgkmcnt(4)\n" \
  "v_smfmac_f32_16x16x64_bf16 v[60:63], v[88:91], a[8:15], v242\n" \
  "ds_read_b128 v[108:111], v243 offset:7168\n" \
  "s_waitcnt lgkmcnt(4)\n" \
  "v_smfmac_f32_16x16x64_bf16 v[60:63], v[92:95], a[16:23], v242\n" \
  "ds_read_b128 v[112:115], v243 offset:6144\n" \
  "v_smfmac_f32_16x16x64_bf16 v[64:67], v[84:87], a[72:79], v242\n" \
  "ds_read_b128 v[116:119], v243 offset:8192\n" \
  "s_waitcnt lgkmcnt(5)\n" \
  "v_smfmac_f32_16x16x64_bf16 v[60:63], v[96:99], a[24:31], v242\n" \
  "s_waitcnt lgkmcnt(3)\n" \
  "v_smfmac_f32_16x16x64_bf16 v[60:63], v[104:107], a[32:39], v242\n" \
  "v_smfmac_f32_16x16x64_bf16 v[60:63], v[100:103], a[40:47], v242\n" \
  "s_waitcnt lgkmcnt(1)\n" \
  "v_smfmac_f32_16x16x64_bf16 v[60:63], v[112:115], a[48:55], v242\n" \
  "v_smfmac_f32_16x16x64_bf16 v[60:63], v[108:111], a[56:63], v242\n" \
  "s_waitcnt lgkmcnt(0)\n" \
  "v_smfmac_f32_16x16x64_bf16 v[60:63], v[116:119], a[64:71], v242\n" \
  "s_nop 2\n" \
  "v_smfmac_f32_16x16x64_bf16 v[64:67], v[88:91], a[80:87], v242\n" \
  "s_nop 3\n" \
  "v_add_f32 v60, v60, v62\n" \
  "v_add_f32 v61, v61, v63\n" \
  "v_cmp_u_f32 vcc, v60, v61\n" \
  "v_cvt_pk_bf16_f32 v80, v60, v61\n" \
  "v_smfmac_f32_16x16x64_bf16 v[64:67], v[92:95], a[88:95], v242\n" \
  "s_cbranch_vccnz .LW_slow_%=\n" \
  ".LW_back_%=:\n" \
  "ds_write_b32 v244, v80\n" \
  "v_smfmac_f32_16x16x64_bf16 v[64:67], v[96:99], a[96:103], v242\n" \
  "v_smfmac_f32_16x16x64_bf16 v[64:67], v[104:107], a[104:111], v242\n" \
  "v_smfmac_f32_16x16x64_bf16 v[64:67], v[100:103], a[112:119], v242\n" \
  "v_smfmac_f32_16x16x64_bf16 v[64:67], v[112:115], a[120:127], v242\n" \
  "s_waitcnt lgkmcnt(0)\n" \
  "s_barrier\n" \
  "v_smfmac_f32_16x16x64_bf16 v[64:67], v[108:111], a[128:135], v242\n" \
  "ds_read_b64_tr_b16 v[86:87], v245 offset:1536\n" \
  "ds_read_b64_tr_b16 v[90:91], v245 offset:512\n" \
  "v_smfmac_f32_16x16x64_bf16 v[64:67], v[116:119], a[136:143], v242\n" \
  "ds_read_b64_tr_b16 v[88:89], v245\n" \
  "s_nop 6\n" \
  "v_add_f32 v65, v65, v67\n" \
  "v_add_f32 v64, v64, v66\n" \
  "v_cvt_pk_bf16_f32 v81, v64, v65\n" \
  "ds_write_b32 v244, v81 offset:256\n" \
  "s_waitcnt lgkmcnt(1)\n" \
  "v_smfmac_f32_16x16x64_bf16 v[76:79], v[88:91], a[208:215], v248\n" \
  "v_smfmac_f32_16x16x64_bf16 v[72:75], v[88:91], a[176:183], v248\n" \
  "ds_read_b64_tr_b16 v[84:85], v245 offset:1024\n" \
  "v_smfmac_f32_16x16x64_bf16 v[68:71], v[88:91], a[144:151], v248\n" \
  "s_waitcnt lgkmcnt(1)\n" \
  "s_barrier\n" \
  "ds_read_b64_tr_b16 v[88:89], v245 offset:256\n" \
  "ds_read_b64_tr_b16 v[90:91], v245 offset:768\n" \
  "ds_read_b64_tr_b16 v[94:95], v245 offset:1792\n" \
  "s_waitcnt lgkmcnt(3)\n" \
  "v_smfmac_f32_16x16x64_bf16 v[68:71], v[84:87], a[152:159], v248\n" \
  "ds_read_b64_tr_b16 v[92:93], v245 offset:1280\n" \
  "v_smfmac_f32_16x16x64_bf16 v[72:75], v[84:87], a[184:191], v248\n" \
  "v_smfmac_f32_16x16x64_bf16 v[76:79], v[84:87], a[216:223], v248\n" \
  "s_waitcnt lgkmcnt(2)\n" \
  "v_smfmac_f32_16x16x64_bf16 v[68:71], v[88:91], a[160:167], v248\n" \
  "s_waitcnt lgkmcnt(0)\n" \
  "v_smfmac_f32_16x16x64_bf16 v[68:71], v[92:95], a[168:175], v248\n" \
  "s_nop 2\n" \
  "v_smfmac_f32_16x16x64_bf16 v[72:75], v[88:91], a[192:199], v248\n" \
  "s_nop 0\n" \
  "v_smfmac_f32_16x16x64_bf16 v[72:75], v[92:95], a[200:207], v248\n" \
  "s_nop 2\n" \
  "v_smfmac_f32_16x16x64_bf16 v[76:79], v[88:91], a[224:231], v248\n" \
  "s_nop 0\n" \
  "v_smfmac_f32_16x16x64_bf16 v[76:79], v[92:95], a[232:239], v248\n" \
  "v_cvt_pk_bf16_f32 v68, v68, v69\n" \
  "v_cvt_pk_bf16_f32 v69, v70, v71\n" \
  "v_cvt_pk_bf16_f32 v71, v74, v75\n" \
  "v_cvt_pk_bf16_f32 v70, v72, v73\n" \
  "s_nop 1\n" \
  "v_permlane16_swap_b32 v68, v70\n" \
  "v_permlane16_swap_b32 v69, v71\n" \
  "v_cvt_pk_bf16_f32 v76, v76, v77\n" \
  "v_cvt_pk_bf16_f32 v77, v78, v79\n" \
  "buffer_store_dwordx4 v[68:71], v250, s[40:43], 0 offen sc1\n" \
  "buffer_store_dwordx2 v[76:77], v252, s[40:43], 0 offen sc1\n" \
  ".LW_loop_%=:\n" \
  "s_memrealtime s[98:99]\n" \
  "s_memtime s[84:85]\n" \
  "s_cmp_ge_u32 s48, 58\n" \
  "s_cbranch_scc1 .LW_cp_%=\n" \
  ".LW_cp_b_%=:\n" \
  "v_mov_b64 v[60:61], 0\n" \
  "v_mov_b64 v[62:63], 0\n" \
  "v_mov_b64 v[64:65], 0\n" \
  "v_mov_b64 v[66:67], 0\n" \
  "v_mov_b64 v[68:69], 0\n" \
  "v_mov_b64 v[70:71], 0\n" \
  "v_mov_b64 v[72:73], 0\n" \
  "v_mov_b64 v[74:75], 0\n" \
  "v_mov_b64 v[76:77], 0\n" \
  "v_mov_b64 v[78:79], 0\n" \
  "s_add_u32 s95, s48, 1\n" \
  "s_and_b32 s76, s95, 3\n" \
  "s_lshl_b32 s95, s95, 5\n" \
  "s_mul_i32 s76, s76, 16\n" \
  "s_sub_u32 s95, s95, s76\n" \
  "s_add_u32 s95, s95, s45\n" \
  "buffer_load_dwordx4 v[52:55], off, s[40:43], s95 sc1\n" \
  "s_add_u32 s48, s48, 1\n" \
  "v_mov_b32 v56, s48\n" \
  "s_waitcnt lgkmcnt(0)\n" \
  "s_cmp_gt_u32 s48, 57\n" \
  "s_cbranch_scc1 .LW_ef_%=\n" \
  ".LW_ef_b_%=:\n" \
  "s_sub_u32 s76, s84, s90\n" \
  "s_sub_u32 s77, s98, s86\n" \
  "v_cvt_f32_u32 v216, s76\n" \
  "v_cvt_f32_u32 v217, s77\n" \
  "v_rcp_f32 v217, v217\n" \
  "v_mul_f32 v216, 16.0, v216\n" \
  "v_fma_f32 v216, v216, v217, 0.5\n" \
  "v_cvt_u32_f32 v216, v216\n" \
  "s_nop 0\n" \
  "v_readfirstlane_b32 s76, v216\n" \
  "s_max_u32 s76, s76, 160\n" \
  "s_min_u32 s76, s76, 400\n" \
  "s_add_u32 s77, s48, 3\n" \
  "s_and_b32 s77, s77, 3\n" \
  "s_cselect_b32 s87, s87, s76\n" \
  "s_cselect_b32 s86, s86, s98\n" \
  "s_cselect_b32 s90, s90, s84\n" \
  "s_waitcnt vmcnt(0)\n" \
  "s_branch .LW_rot_%=\n" \
  "s_nop 7\n" \
  ".LW_slow_%=:\n" \
  "s_bitcmp1_b32 s70, 0\n" \
  "s_cbranch_scc1 .LW_back_%=\n" \
  "s_waitcnt lgkmcnt(0)\n" \
  "s_cmp_lt_u32 s70, 10\n" \
  "s_nop 3\n" \
  "s_cbranch_scc1 .LW_nol_%=\n" \
  "v_max_u32 v217, v84, v85\n" \
  "v_max3_u32 v217, v217, v86, v87\n" \
  "v_max3_u32 v217, v217, v88, v89\n" \
  "v_max3_u32 v217, v217, v90, v91\n" \
  "v_max3_u32 v217, v217, v92, v93\n" \
  "v_max3_u32 v217, v217, v94, v95\n" \
  "v_max3_u32 v217, v217, v96, v97\n" \
  "v_max3_u32 v217, v217, v98, v99\n" \
  "v_max3_u32 v217, v217, v104, v105\n" \
  "v_max3_u32 v217, v217, v106, v107\n" \
  "v_max3_u32 v217, v217, v100, v101\n" \
  "v_max3_u32 v217, v217, v102, v103\n" \
  "v_max3_u32 v217, v217, v112, v113\n" \
  "v_max3_u32 v217, v217, v114, v115\n" \
  "v_max3_u32 v217, v217, v108, v109\n" \
  "v_max3_u32 v217, v217, v110, v111\n" \
  "v_max3_u32 v217, v217, v116, v117\n" \
  "v_max3_u32 v217, v217, v118, v119\n" \
  "v_cmp_eq_u32 vcc, -1, v217\n" \
  "s_cbranch_vccz .LW_back_%=\n" \
  ".LW_nol_%=:\n" \
  "s_add_u32 s70, s70, 2\n" \
  "s_cmp_gt_u32 s70, 65536\n" \
  "s_cbranch_scc1 .LW_dset_%=\n" \
  "s_barrier\n" \
  "v_mov_b64 v[60:61], 0\n" \
  "v_mov_b64 v[62:63], 0\n" \
  "v_mov_b64 v[64:65], 0\n" \
  "v_mov_b64 v[66:67], 0\n" \
  "v_mov_b64 v[68:69], 0\n" \
  "v_mov_b64 v[70:71], 0\n" \
  "v_mov_b64 v[72:73], 0\n" \
  "v_mov_b64 v[74:75], 0\n" \
  "v_mov_b64 v[76:77], 0\n" \
  "v_mov_b64 v[78:79], 0\n" \
  "s_and_b32 s62, %51, 0xffff\n" \
  "s_add_u32 s44, s44, s62\n" \
  "s_xor_b32 s59, s59, 0\n" \
  "s_mov_b32 s68, 0\n" \
  "s_mov_b32 m0, 0\n" \
  "s_branch .LW_rt_%=\n" \
  ".LW_dset_%=:\n" \
  "v_mov_b32 v100, 1\n" \
  "v_mov_b32 v101, s55\n" \
  "buffer_store_dwordx2 v[100:101], off, s[40:43], %48 offset:128 sc1\n" \
  "s_mov_b32 s70, 1\n" \
  "v_mov_b64 v[60:61], 0\n" \
  "v_mov_b64 v[62:63], 0\n" \
  "v_mov_b64 v[64:65], 0\n" \
  "v_mov_b64 v[66:67], 0\n" \
  "v_mov_b64 v[68:69], 0\n" \
  "v_mov_b64 v[70:71], 0\n" \
  "v_mov_b64 v[72:73], 0\n" \
  "v_mov_b64 v[74:75], 0\n" \
  "v_mov_b64 v[76:77], 0\n" \
  "v_mov_b64 v[78:79], 0\n" \
  "s_waitcnt vmcnt(0) lgkmcnt(0)\n" \
  "s_barrier\n" \
  "s_branch .LW_p_%=\n" \
  ".LW_cp_%=:\n" \
  "s_sub_u32 s60, s48, 58\n" \
  "s_lshl_b32 s60, s60, 20\n" \
  "s_add_u32 s60, s60, s47\n" \
  "s_lshr_b32 s62, s60, 8\n" \
  "s_and_b32 s62, s62, 8192\n" \
  "s_xor_b32 s62, s62, s60\n" \
  "buffer_store_dwordx4 v[68:71], v253, s[40:43], s62 offen sc1\n" \
  "buffer_store_dwordx2 v[76:77], v255, s[40:43], s62 offen sc1\n" \
  "s_nop 1\n" \
  "s_branch .LW_cp_b_%=\n" \
  ".LW_ef_%=:\n" \
  "s_cmp_eq_u32 s48, 64\n" \
  "s_cbranch_scc1 .LW_end_%=\n" \
  "s_waitcnt vmcnt(0)\n" \
  "s_barrier\n" \
  "s_branch .LW_ef_b_%=\n" \
  ".LW_tw_re_%=:\n" \
  "s_add_u32 s69, s69, 1\n" \
  "s_cmp_gt_u32 s69, 64\n" \
  "s_cbranch_scc1 .LW_noclk_%=\n" \
  "buffer_load_dwordx4 v[52:55], off, s[40:43], s95 sc1\n" \
  "s_waitcnt vmcnt(0)\n" \
  "s_branch .LW_tw_%=\n" \
  ".LW_lat_%=:\n" \
  "s_lshr_b32 s63, %51, 16\n" \
  "s_add_u32 s62, s62, s63\n" \
  "s_cmp_lt_u32 s62, 4294937296\n" \
  "s_cbranch_scc1 .LW_noclk_%=\n" \
  "s_and_b32 s63, %51, 0xffff\n" \
  ".LW_lat1_%=:\n" \
  "s_add_u32 s44, s44, s63\n" \
  "s_add_u32 s62, s62, s63\n" \
  "s_cbranch_scc0 .LW_lat1_%=\n" \
  "s_branch .LW_rt_%=\n" \
  ".LW_clk2_%=:\n" \
  "s_cmp_le_i32 s62, 3\n" \
  "s_cbranch_scc1 .LW_clk_done_%=\n" \
  "s_memrealtime s[60:61]\n" \
  "s_waitcnt lgkmcnt(0)\n" \
  "s_sub_u32 s62, s73, s60\n" \
  "s_branch .LW_clk2_%=\n" \
  ".LW_clk_done_%=:\n" \
  "s_branch .LW_noclk_%=\n" \
  ".LW_end_%=:\n" \
  "s_waitcnt vmcnt(0)\n" \
  "s_waitcnt vmcnt(0)\n" \
  "s_nop 7\n" \
  "s_endpgm\n" \
  ".LW_other_%=:\n"
#define W_ASM_VOPS "+{v242}"(ci.sidx), "+{v243}"(ci.l16), "+{v244}"(ci.viw), "+{v245}"(ci.fva), "+{v246}"(ci.pk3), "+{v247}"(ci.fvs), "+{v248}"(ci.sid2), "+{v249}"(ci.uo), "+{v250}"(ci.po0), "+{v251}"(ci.po1), "+{v252}"(ci.po2), "+{v253}"(ci.co0), "+{v254}"(ci.co1), "+{v255}"(ci.co2), "+{v200}"(ci.vq0), "+{v201}"(ci.vq1), "+{v202}"(ci.vq2), "+{v203}"(ci.vq3), "+{v204}"(ci.vo0), "+{v205}"(ci.vo1), "+{v206}"(ci.vo2), "+{v207}"(ci.vo3), "+{v208}"(ci.v50), "+{v209}"(ci.v51), "+{v210}"(ci.v52), "+{v211}"(ci.v53), "+{v212}"(ci.tro0), "+{v213}"(ci.tro1), "+{v214}"(ci.m0o), "+{v215}"(ci.tro2), "+{v216}"(ci.tro3), "+{v217}"(ci.tro4)
#define W_ASM_SOPS "s"(ci.ws), "s"(ci.Wa), "s"(ci.Wb), "s"(ci.mem0), "s"(ci.w), "s"(ci.o_a), "s"(ci.o_b), "s"(ci.o_nx), "s"(ci.o_exp), "s"(ci.o_u), "s"(ci.lab), "s"(ci.f_p), "s"(ci.f_m), "s"(ci.f_u), "s"(ci.f_dur), "s"(ci.f_go), "s"(ci.o_ctl), "s"(ci.o_dbg), "s"(ci.push), "s"(ci.dret), "s"(ci.u5), "s"(ci.o_qx), "s"(ci.f_s), "s"(ci.f_r)
#define W_ASM_CLOB "v0", "v1", "v2", "v3", "v4", "v5", "v6", "v7", "v8", "v9", "v10", "v11", "v12", "v13", "v14", "v15", "v16", "v17", "v18", "v19", "v20", "v21", "v22", "v23", "v24", "v25", "v26", "v27", "v28", "v29", "v30", "v31", "v32", "v33", "v34", "v35", "v36", "v37", "v38", "v39", "v40", "v41", "v42", "v43", "v44", "v45", "v46", "v47", "v48", "v49", "v50", "v51", "v52", "v53", "v54", "v55", "v56", "v57", "v58", "v59", "v60", "v61", "v62", "v63", "v64", "v65", "v66", "v67", "v68", "v69", "v70", "v71", "v72", "v73", "v74", "v75", "v76", "v77", "v78", "v79", "v80", "v81", "v82", "v83", "v84", "v85", "v86", "v87", "v88", "v89", "v90", "v91", "v92", "v93", "v94", "v95", "v96", "v97", "v98", "v99", "v100", "v101", "v102", "v103", "v104", "v105", "v106", "v107", "v108", "v109", "v110", "v111", "v112", "v113", "v114", "v115", "v116", "v117", "v118", "v119", "v120", "v121", "v122", "v123", "v124", "v125", "v126", "v127", "v128", "v129", "v130", "v131", "v132", "v133", "v134", "v135", "v136", "v137", "v138", "v139", "v140", "v141", "v142", "v143", "v144", "v145", "v146", "v147", "v148", "v149", "v150", "v151", "v152", "v153", "v154", "v155", "v156", "v157", "v158", "v159", "v160", "v161", "v162", "v163", "v164", "v165", "v166", "v167", "v168", "v169", "v170", "v171", "v172", "v173", "v174", "v175", "v176", "v177", "v178", "v179", "v180", "v181", "v182", "v183", "v184", "v185", "v186", "v187", "v188", "v189", "v190", "v191", "v192", "v193", "v194", "v195", "v196", "v197", "v198", "v199", "a0", "a1", "a2", "a3", "a4", "a5", "a6", "a7", "a8", "a9", "a10", "a11", "a12", "a13", "a14", "a15", "a16", "a17", "a18", "a19", "a20", "a21", "a22", "a23", "a24", "a25", "a26", "a27", "a28", "a29", "a30", "a31", "a32", "a33", "a34", "a35", "a36", "a37", "a38", "a39", "a40", "a41", "a42", "a43", "a44", "a45", "a46", "a47", "a48", "a49", "a50", "a51", "a52", "a53", "a54", "a55", "a56", "a57", "a58", "a59", "a60", "a61", "a62", "a63", "a64", "a65", "a66", "a67", "a68", "a69", "a70", "a71", "a72", "a73", "a74", "a75", "a76", "a77", "a78", "a79", "a80", "a81", "a82", "a83", "a84", "a85", "a86", "a87", "a88", "a89", "a90", "a91", "a92", "a93", "a94", "a95", "a96", "a97", "a98", "a99", "a100", "a101", "a102", "a103", "a104", "a105", "a106", "a107", "a108", "a109", "a110", "a111", "a112", "a113", "a114", "a115", "a116", "a117", "a118", "a119", "a120", "a121", "a122", "a123", "a124", "a125", "a126", "a127", "a128", "a129", "a130", "a131", "a132", "a133", "a134", "a135", "a136", "a137", "a138", "a139", "a140", "a141", "a142", "a143", "a144", "a145", "a146", "a147", "a148", "a149", "a150", "a151", "a152", "a153", "a154", "a155", "a156", "a157", "a158", "a159", "a160", "a161", "a162", "a163", "a164", "a165", "a166", "a167", "a168", "a169", "a170", "a171", "a172", "a173", "a174", "a175", "a176", "a177", "a178", "a179", "a180", "a181", "a182", "a183", "a184", "a185", "a186", "a187", "a188", "a189", "a190", "a191", "a192", "a193", "a194", "a195", "a196", "a197", "a198", "a199", "a200", "a201", "a202", "a203", "a204", "a205", "a206", "a207", "a208", "a209", "a210", "a211", "a212", "a213", "a214", "a215", "a216", "a217", "a218", "a219", "a220", "a221", "a222", "a223", "a224", "a225", "a226", "a227", "a228", "a229", "a230", "a231", "a232", "a233", "a234", "a235", "a236", "a237", "a238", "a239", "a240", "a241", "a242", "a243", "a244", "a245", "a246", "a247", "a248", "a249", "a250", "a251", "a252", "a253", "a254", "a255", "s40", "s41", "s42", "s43", "s44", "s45", "s46", "s47", "s48", "s49", "s50", "s51", "s52", "s53", "s54", "s55", "s56", "s57", "s58", "s59", "s60", "s61", "s62", "s63", "s64", "s65", "s66", "s67", "s68", "s69", "s70", "s71", "s72", "s73", "s74", "s75", "s76", "s77", "s78", "s79", "s80", "s81", "s82", "s83", "s84", "s85", "s86", "s87", "s88", "s89", "s90", "s91", "s92", "s93", "s94", "s95", "s96", "s97", "s98", "s99", "vcc", "scc", "memory"
#define R_ASM_TEXT \
  "s_mov_b64 s[40:41], %18\n" \
  "s_and_b32 s41, s41, 0xffff\n" \
  "s_mov_b32 s42, 255852544\n" \
  "s_mov_b32 s43, 0x00020000\n" \
  "s_bfe_u32 s56, %20, 0x30010\n" \
  "s_lshl_b32 s56, s56, 12\n" \
  "s_or_b32 s56, s56, 0x200000\n" \
  "s_mov_b32 s57, 1052672\n" \
  "s_mov_b32 s48, 0\n" \
  "s_cmp_lg_u32 %19, 0\n" \
  "s_cbranch_scc1 .LR_help_%=\n" \
  "s_lshl_b32 s61, %24, 2\n" \
  "s_lshr_b32 s51, %20, s61\n" \
  "s_and_b32 s51, s51, 7\n" \
  "s_lshl_b32 s51, s51, 12\n" \
  "s_xor_b32 s51, s51, 4210688\n" \
  "s_mov_b32 s71, s56\n" \
  "s_add_u32 s47, s51, 886784\n" \
  "s_lshr_b32 s52, %20, 0\n" \
  "s_and_b32 s52, s52, 7\n" \
  "s_lshl_b32 s52, s52, 12\n" \
  "s_xor_b32 s52, s52, 4210688\n" \
  "s_xor_b32 s52, s52, s56\n" \
  "s_lshr_b32 s64, %20, 4\n" \
  "s_and_b32 s64, s64, 7\n" \
  "s_lshl_b32 s64, s64, 12\n" \
  "s_xor_b32 s64, s64, 4210688\n" \
  "s_xor_b32 s64, s64, s56\n" \
  "s_lshr_b32 s65, %20, 8\n" \
  "s_and_b32 s65, s65, 7\n" \
  "s_lshl_b32 s65, s65, 12\n" \
  "s_xor_b32 s65, s65, 4210688\n" \
  "s_xor_b32 s65, s65, s56\n" \
  "s_lshr_b32 s67, %20, 12\n" \
  "s_and_b32 s67, s67, 7\n" \
  "s_lshl_b32 s67, s67, 12\n" \
  "s_xor_b32 s67, s67, 4210688\n" \
  "s_xor_b32 s67, s67, s56\n" \
  "v_add_u32 v208, 32768, v215\n" \
  "v_add_u32 v209, 65536, v215\n" \
  "v_add_u32 v210, 98304, v215\n" \
  "v_add_u32 v211, 40960, v214\n" \
  "s_mov_b32 s49, %22\n" \
  "s_mov_b32 s58, %23\n" \
  "s_memrealtime s[86:87]\n" \
  "s_memtime s[90:91]\n" \
  "s_waitcnt lgkmcnt(0)\n" \
  "s_mov_b32 s87, 900\n" \
  "s_mov_b32 s50, 0\n" \
  "s_mov_b32 s44, 0\n" \
  "s_mov_b32 s88, 0\n" \
  "s_mov_b32 s45, 0\n" \
  "s_mov_b32 s46, 4096\n" \
  "v_mov_b32 v199, %21\n" \
  "s_mov_b32 s89, 2130706432\n" \
  "s_xor_b32 s51, s51, 0x40000000\n" \
  "s_xor_b32 s49, s49, 0x40000000\n" \
  "s_xor_b32 s58, s58, 0x40000000\n" \
  "s_xor_b32 s47, s47, 0x40000000\n" \
  "s_xor_b32 s52, s52, 0x40000000\n" \
  "s_xor_b32 s64, s64, 0x40000000\n" \
  "s_xor_b32 s65, s65, 0x40000000\n" \
  "s_xor_b32 s67, s67, 0x40000000\n" \
  ".LR_loop_%=:\n" \
  "s_add_u32 s95, s48, 0\n" \
  "s_and_b32 s76, s95, 3\n" \
  "s_lshl_b32 s95, s95, 5\n" \
  "s_mul_i32 s76, s76, 16\n" \
  "s_sub_u32 s95, s95, s76\n" \
  "s_add_u32 s95, s95, s47\n" \
  "s_mov_b32 s54, s51\n" \
  "s_add_u32 s94, s51, 32768\n" \
  "s_mov_b32 s70, 0\n" \
  "s_mov_b32 s96, 0\n" \
  "buffer_load_dwordx4 v[200:203], off, s[40:43], s95 sc1\n" \
  "s_sub_u32 s76, s84, s90\n" \
  "s_sub_u32 s77, s98, s86\n" \
  "v_cvt_f32_u32 v196, s76\n" \
  "v_cvt_f32_u32 v197, s77\n" \
  "v_rcp_f32 v197, v197\n" \
  "v_mul_f32 v196, 16.0, v196\n" \
  "v_fma_f32 v196, v196, v197, 0.5\n" \
  "v_cvt_u32_f32 v196, v196\n" \
  "s_nop 0\n" \
  "v_readfirstlane_b32 s76, v196\n" \
  "s_max_u32 s76, s76, 160\n" \
  "s_min_u32 s76, s76, 400\n" \
  "s_add_u32 s77, s48, 2\n" \
  "s_and_b32 s77, s77, 3\n" \
  "s_cselect_b32 s87, s87, s76\n" \
  "s_cselect_b32 s86, s86, s98\n" \
  "s_cselect_b32 s90, s90, s84\n" \
  "v_mov_b64 v[64:65], 0\n" \
  "v_mov_b64 v[66:67], 0\n" \
  "v_mov_b64 v[68:69], 0\n" \
  "v_mov_b64 v[70:71], 0\n" \
  "v_mov_b64 v[72:73], 0\n" \
  "v_mov_b64 v[74:75], 0\n" \
  "v_mov_b64 v[76:77], 0\n" \
  "v_mov_b64 v[78:79], 0\n" \
  "v_mov_b64 v[80:81], 0\n" \
  "v_mov_b64 v[82:83], 0\n" \
  "v_mov_b64 v[84:85], 0\n" \
  "v_mov_b64 v[86:87], 0\n" \
  "v_mov_b64 v[88:89], 0\n" \
  "v_mov_b64 v[90:91], 0\n" \
  "v_mov_b64 v[92:93], 0\n" \
  "v_mov_b64 v[94:95], 0\n" \
  "v_mov_b64 v[96:97], 0\n" \
  "v_mov_b64 v[98:99], 0\n" \
  "v_mov_b64 v[100:101], 0\n" \
  "v_mov_b64 v[102:103], 0\n" \
  "v_mov_b64 v[104:105], 0\n" \
  "v_mov_b64 v[106:107], 0\n" \
  "v_mov_b64 v[108:109], 0\n" \
  "v_mov_b64 v[110:111], 0\n" \
  "v_mov_b64 v[112:113], 0\n" \
  "v_mov_b64 v[114:115], 0\n" \
  "v_mov_b64 v[116:117], 0\n" \
  "v_mov_b64 v[118:119], 0\n" \
  "v_mov_b64 v[120:121], 0\n" \
  "v_mov_b64 v[122:123], 0\n" \
  "v_mov_b64 v[124:125], 0\n" \
  "v_mov_b64 v[126:127], 0\n" \
  "v_mov_b64 v[192:193], 0\n" \
  "v_mov_b64 v[194:195], 0\n" \
  "buffer_store_dwordx2 v[198:199], off, s[40:43], s89 sc1\n" \
  "s_barrier\n" \
  "ds_read_b128 a[0:3], v211 offset:0\n" \
  "ds_read_b128 a[4:7], v211 offset:1024\n" \
  "ds_read_b128 a[8:11], v211 offset:2048\n" \
  "ds_read_b128 a[12:15], v211 offset:3072\n" \
  "ds_read_b128 a[16:19], v211 offset:4096\n" \
  "ds_read_b128 a[20:23], v211 offset:5120\n" \
  "ds_read_b128 a[24:27], v211 offset:6144\n" \
  "ds_read_b128 a[28:31], v211 offset:7168\n" \
  "ds_read_b128 a[32:35], v211 offset:8192\n" \
  "ds_read_b128 a[36:39], v211 offset:9216\n" \
  "ds_read_b128 a[40:43], v211 offset:10240\n" \
  "ds_read_b128 a[44:47], v211 offset:11264\n" \
  "ds_read_b128 a[48:51], v211 offset:12288\n" \
  "ds_read_b128 a[52:55], v211 offset:13312\n" \
  "ds_read_b128 a[56:59], v211 offset:14336\n" \
  "ds_read_b128 a[60:63], v211 offset:15360\n" \
  "ds_read_b128 a[64:67], v211 offset:16384\n" \
  "ds_read_b128 a[68:71], v211 offset:17408\n" \
  "ds_read_b128 a[72:75], v211 offset:18432\n" \
  "ds_read_b128 a[76:79], v211 offset:19456\n" \
  "ds_read_b128 a[80:83], v211 offset:20480\n" \
  "ds_read_b128 a[84:87], v211 offset:21504\n" \
  "ds_read_b128 a[88:91], v211 offset:22528\n" \
  "ds_read_b128 a[92:95], v211 offset:23552\n" \
  "ds_read_b128 a[96:99], v211 offset:24576\n" \
  "ds_read_b128 a[100:103], v211 offset:25600\n" \
  "ds_read_b128 a[104:107], v211 offset:26624\n" \
  "ds_read_b128 a[108:111], v211 offset:27648\n" \
  "ds_read_b128 a[112:115], v211 offset:28672\n" \
  "ds_read_b128 a[116:119], v211 offset:29696\n" \
  "ds_read_b128 a[120:123], v211 offset:30720\n" \
  "ds_read_b128 a[124:127], v211 offset:31744\n" \
  "v_xor_b32 v211, 32768, v211\n" \
  "s_mov_b32 s69, 0\n" \
  "s_waitcnt vmcnt(1)\n" \
  ".LR_tw_%=:\n" \
  "v_readfirstlane_b32 s60, v200\n" \
  "s_cmp_lg_u32 s60, s48\n" \
  "s_cbranch_scc1 .LR_tw_re_%=\n" \
  "v_readfirstlane_b32 s50, v201\n" \
  "s_cmp_eq_u32 s48, 0\n" \
  "s_cselect_b32 s50, s86, s50\n" \
  "s_cmp_lg_u32 s44, s45\n" \
  "s_cselect_b32 s60, s46, 0\n" \
  "s_mov_b32 s45, s44\n" \
  "s_xor_b32 s54, s54, s60\n" \
  "s_xor_b32 s94, s94, s60\n" \
  ".LR_again_%=:\n" \
  "s_memrealtime s[60:61]\n" \
  "s_add_u32 s53, s50, s44\n" \
  "s_bfe_u32 s63, %28, 0x80010\n" \
  "s_waitcnt lgkmcnt(0)\n" \
  "s_sub_u32 s62, s53, s60\n" \
  "s_sub_u32 s61, s62, 14\n" \
  "s_cmp_ge_u32 s61, 2000\n" \
  "s_cbranch_scc1 .LR_near_%=\n" \
  "s_sub_u32 s62, s62, s63\n" \
  "s_cmp_le_i32 s62, 0\n" \
  "s_cbranch_scc1 .LR_fin_%=\n" \
  "s_sub_u32 s62, s62, 9\n" \
  ".LR_slp_%=:\n" \
  "s_cmp_le_i32 s62, 0\n" \
  "s_cbranch_scc1 .LR_clk_%=\n" \
  "s_sleep 1\n" \
  "s_sub_u32 s62, s62, 3\n" \
  "s_branch .LR_slp_%=\n" \
  ".LR_clk_%=:\n" \
  "s_memrealtime s[60:61]\n" \
  "s_waitcnt lgkmcnt(0)\n" \
  "s_sub_u32 s62, s53, s60\n" \
  "s_cmp_gt_i32 s62, s63\n" \
  "s_cbranch_scc1 .LR_clk_%=\n" \
  ".LR_fin_%=:\n" \
  "s_memrealtime s[76:77]\n" \
  "s_nop 4\n" \
  "s_memrealtime s[78:79]\n" \
  "s_nop 4\n" \
  "s_memrealtime s[80:81]\n" \
  "s_nop 4\n" \
  "s_memrealtime s[82:83]\n" \
  "s_waitcnt lgkmcnt(0)\n" \
  "s_mov_b32 s77, 1\n" \
  "s_cmp_eq_u32 s78, s76\n" \
  "s_addc_u32 s77, s77, 0\n" \
  "s_cmp_eq_u32 s80, s76\n" \
  "s_addc_u32 s77, s77, 0\n" \
  "s_cmp_eq_u32 s82, s76\n" \
  "s_addc_u32 s77, s77, 0\n" \
  "s_mul_i32 s77, s77, 24\n" \
  "s_lshr_b32 s78, s87, 2\n" \
  "s_sub_u32 s77, s78, s77\n" \
  "s_max_i32 s77, s77, 0\n" \
  "s_sub_u32 s76, s53, s76\n" \
  "s_mul_i32 s76, s76, s87\n" \
  "s_ashr_i32 s76, s76, 4\n" \
  "s_sub_u32 s76, s76, s77\n" \
  "s_sub_u32 s76, s76, 220\n" \
  "s_max_i32 s76, s76, 0\n" \
  "s_min_i32 s76, s76, 400\n" \
  "s_lshr_b32 s76, s76, 3\n" \
  "s_lshl_b32 s76, s76, 2\n" \
  "s_getpc_b64 s[78:79]\n" \
  "s_sub_u32 s76, 220, s76\n" \
  "s_add_u32 s78, s78, s76\n" \
  "s_addc_u32 s79, s79, 0\n" \
  "s_setpc_b64 s[78:79]\n" \
  "s_nop 1\n" \
  "s_nop 1\n" \
  "s_nop 1\n" \
  "s_nop 1\n" \
  "s_nop 1\n" \
  "s_nop 1\n" \
  "s_nop 1\n" \
  "s_nop 1\n" \
  "s_nop 1\n" \
  "s_nop 1\n" \
  "s_nop 1\n" \
  "s_nop 1\n" \
  "s_nop 1\n" \
  "s_nop 1\n" \
  "s_nop 1\n" \
  "s_nop 1\n" \
  "s_nop 1\n" \
  "s_nop 1\n" \
  "s_nop 1\n" \
  "s_nop 1\n" \
  "s_nop 1\n" \
  "s_nop 1\n" \
  "s_nop 1\n" \
  "s_nop 1\n" \
  "s_nop 1\n" \
  "s_nop 1\n" \
  "s_nop 1\n" \
  "s_nop 1\n" \
  "s_nop 1\n" \
  "s_nop 1\n" \
  "s_nop 1\n" \
  "s_nop 1\n" \
  "s_nop 1\n" \
  "s_nop 1\n" \
  "s_nop 1\n" \
  "s_nop 1\n" \
  "s_nop 1\n" \
  "s_nop 1\n" \
  "s_nop 1\n" \
  "s_nop 1\n" \
  "s_nop 1\n" \
  "s_nop 1\n" \
  "s_nop 1\n" \
  "s_nop 1\n" \
  "s_nop 1\n" \
  "s_nop 1\n" \
  "s_nop 1\n" \
  "s_nop 1\n" \
  "s_nop 1\n" \
  "s_nop 1\n" \
  ".LR_b2_%=:\n" \
  "buffer_load_dwordx4 v[0:3], v215, s[40:43], s54 offen sc1\n" \
  "s_memtime s[92:93]\n" \
  "s_cmp_lg_u32 s96, 0\n" \
  "buffer_load_dwordx4 v[4:7], v215, s[40:43], s54 offen offset:1024 sc1\n" \
  "buffer_load_dwordx4 v[8:11], v215, s[40:43], s54 offen offset:2048 sc1\n" \
  "buffer_load_dwordx4 v[12:15], v215, s[40:43], s54 offen offset:3072 sc1\n" \
  "buffer_load_dwordx4 v[16:19], v208, s[40:43], s54 offen sc1\n" \
  "buffer_load_dwordx4 v[20:23], v208, s[40:43], s54 offen offset:1024 sc1\n" \
  "buffer_load_dwordx4 v[24:27], v208, s[40:43], s54 offen offset:2048 sc1\n" \
  "buffer_load_dwordx4 v[28:31], v208, s[40:43], s54 offen offset:3072 sc1\n" \
  "buffer_load_dwordx4 v[32:35], v209, s[40:43], s54 offen sc1\n" \
  "buffer_load_dwordx4 v[36:39], v209, s[40:43], s54 offen offset:1024 sc1\n" \
  "buffer_load_dwordx4 v[40:43], v209, s[40:43], s54 offen offset:2048 sc1\n" \
  "buffer_load_dwordx4 v[44:47], v209, s[40:43], s54 offen offset:3072 sc1\n" \
  "buffer_load_dwordx4 v[48:51], v210, s[40:43], s54 offen sc1\n" \
  "buffer_load_dwordx4 v[52:55], v210, s[40:43], s54 offen offset:1024 sc1\n" \
  "buffer_load_dwordx4 v[56:59], v210, s[40:43], s54 offen offset:2048 sc1\n" \
  "buffer_load_dwordx4 v[60:63], v210, s[40:43], s54 offen offset:3072 sc1\n" \
  "buffer_load_dwordx4 v[128:131], v214, s[40:43], s49 offen sc1\n" \
  "buffer_load_dwordx4 v[132:135], v214, s[40:43], s49 offen offset:1024 sc1\n" \
  "buffer_load_dwordx4 v[136:139], v214, s[40:43], s49 offen offset:2048 sc1\n" \
  "buffer_load_dwordx4 v[140:143], v214, s[40:43], s49 offen offset:3072 sc1\n" \
  "buffer_load_dwordx4 v[144:147], v214, s[40:43], s58 offen sc1\n" \
  "buffer_load_dwordx4 v[148:151], v214, s[40:43], s58 offen offset:1024 sc1\n" \
  "buffer_load_dwordx4 v[152:155], v214, s[40:43], s58 offen offset:2048 sc1\n" \
  "buffer_load_dwordx4 v[156:159], v214, s[40:43], s58 offen offset:3072 sc1\n" \
  "buffer_load_dwordx4 v[160:163], v216, s[40:43], s54 offen sc1\n" \
  "buffer_load_dwordx4 v[164:167], v216, s[40:43], s54 offen offset:1024 sc1\n" \
  "buffer_load_dwordx4 v[168:171], v216, s[40:43], s54 offen offset:2048 sc1\n" \
  "buffer_load_dwordx4 v[172:175], v216, s[40:43], s54 offen offset:3072 sc1\n" \
  "buffer_load_dwordx4 v[176:179], v216, s[40:43], s94 offen sc1\n" \
  "buffer_load_dwordx4 v[180:183], v216, s[40:43], s94 offen offset:1024 sc1\n" \
  "buffer_load_dwordx4 v[184:187], v216, s[40:43], s94 offen offset:2048 sc1\n" \
  "buffer_load_dwordx4 v[188:191], v216, s[40:43], s94 offen offset:3072 sc1\n" \
  "s_cbranch_scc1 .LR_nb_%=\n" \
  "s_barrier\n" \
  ".LR_nb_%=:\n" \
  "s_waitcnt vmcnt(31)\n" \
  "v_smfmac_f32_16x16x64_bf16 v[64:67], v[0:3], a[0:7], v212\n" \
  "s_waitcnt vmcnt(30)\n" \
  "s_nop 1\n" \
  "v_smfmac_f32_16x16x64_bf16 v[68:71], v[4:7], a[8:15], v212\n" \
  "s_waitcnt vmcnt(29)\n" \
  "s_nop 1\n" \
  "v_smfmac_f32_16x16x64_bf16 v[72:75], v[8:11], a[16:23], v212\n" \
  "s_waitcnt vmcnt(28)\n" \
  "v_cvt_pk_bf16_f32 v64, v64, v65\n" \
  "v_cvt_pk_bf16_f32 v65, v66, v67\n" \
  "v_smfmac_f32_16x16x64_bf16 v[76:79], v[12:15], a[24:31], v212\n" \
  "s_waitcnt vmcnt(27)\n" \
  "v_cvt_pk_bf16_f32 v66, v68, v69\n" \
  "v_cvt_pk_bf16_f32 v67, v70, v71\n" \
  "v_smfmac_f32_16x16x64_bf16 v[80:83], v[16:19], a[32:39], v212\n" \
  "s_waitcnt vmcnt(26)\n" \
  "v_cvt_pk_bf16_f32 v68, v72, v73\n" \
  "v_cvt_pk_bf16_f32 v69, v74, v75\n" \
  "v_smfmac_f32_16x16x64_bf16 v[84:87], v[20:23], a[40:47], v212\n" \
  "s_waitcnt vmcnt(25)\n" \
  "v_cvt_pk_bf16_f32 v70, v76, v77\n" \
  "v_cvt_pk_bf16_f32 v71, v78, v79\n" \
  "v_smfmac_f32_16x16x64_bf16 v[88:91], v[24:27], a[48:55], v212\n" \
  "s_waitcnt vmcnt(24)\n" \
  "v_cvt_pk_bf16_f32 v80, v80, v81\n" \
  "v_cvt_pk_bf16_f32 v81, v82, v83\n" \
  "v_smfmac_f32_16x16x64_bf16 v[92:95], v[28:31], a[56:63], v212\n" \
  "s_waitcnt vmcnt(23)\n" \
  "v_cvt_pk_bf16_f32 v82, v84, v85\n" \
  "v_cvt_pk_bf16_f32 v83, v86, v87\n" \
  "v_smfmac_f32_16x16x64_bf16 v[96:99], v[32:35], a[64:71], v212\n" \
  "s_waitcnt vmcnt(22)\n" \
  "v_cvt_pk_bf16_f32 v84, v88, v89\n" \
  "v_cvt_pk_bf16_f32 v85, v90, v91\n" \
  "v_smfmac_f32_16x16x64_bf16 v[100:103], v[36:39], a[72:79], v212\n" \
  "s_waitcnt vmcnt(21)\n" \
  "v_cvt_pk_bf16_f32 v86, v92, v93\n" \
  "v_cvt_pk_bf16_f32 v87, v94, v95\n" \
  "v_smfmac_f32_16x16x64_bf16 v[104:107], v[40:43], a[80:87], v212\n" \
  "s_waitcnt vmcnt(20)\n" \
  "v_cvt_pk_bf16_f32 v96, v96, v97\n" \
  "v_cvt_pk_bf16_f32 v97, v98, v99\n" \
  "v_smfmac_f32_16x16x64_bf16 v[108:111], v[44:47], a[88:95], v212\n" \
  "s_waitcnt vmcnt(19)\n" \
  "v_cvt_pk_bf16_f32 v98, v100, v101\n" \
  "v_cvt_pk_bf16_f32 v99, v102, v103\n" \
  "v_smfmac_f32_16x16x64_bf16 v[112:115], v[48:51], a[96:103], v212\n" \
  "s_waitcnt vmcnt(18)\n" \
  "v_cvt_pk_bf16_f32 v100, v104, v105\n" \
  "v_cvt_pk_bf16_f32 v101, v106, v107\n" \
  "v_smfmac_f32_16x16x64_bf16 v[116:119], v[52:55], a[104:111], v212\n" \
  "s_waitcnt vmcnt(17)\n" \
  "v_cvt_pk_bf16_f32 v102, v108, v109\n" \
  "v_cvt_pk_bf16_f32 v103, v110, v111\n" \
  "v_smfmac_f32_16x16x64_bf16 v[120:123], v[56:59], a[112:119], v212\n" \
  "s_waitcnt vmcnt(16)\n" \
  "v_cvt_pk_bf16_f32 v112, v112, v113\n" \
  "v_cvt_pk_bf16_f32 v113, v114, v115\n" \
  "v_smfmac_f32_16x16x64_bf16 v[124:127], v[60:63], a[120:127], v212\n" \
  "s_mov_b32 s96, 1\n" \
  "v_cvt_pk_bf16_f32 v114, v116, v117\n" \
  "v_cvt_pk_bf16_f32 v115, v118, v119\n" \
  "s_nop 0\n" \
  "v_cvt_pk_bf16_f32 v116, v120, v121\n" \
  "v_cvt_pk_bf16_f32 v117, v122, v123\n" \
  "s_nop 1\n" \
  "v_cvt_pk_bf16_f32 v118, v124, v125\n" \
  "v_cvt_pk_bf16_f32 v119, v126, v127\n" \
  "s_waitcnt vmcnt(7)\n" \
  "v_smfmac_f32_16x16x64_bf16 v[192:195], v[160:163], v[128:135], v212\n" \
  "s_waitcnt vmcnt(6)\n" \
  "v_smfmac_f32_16x16x64_bf16 v[192:195], v[164:167], v[136:143], v212\n" \
  "s_waitcnt vmcnt(5)\n" \
  "v_smfmac_f32_16x16x64_bf16 v[192:195], v[168:171], v[144:151], v212\n" \
  "s_waitcnt vmcnt(4)\n" \
  "v_smfmac_f32_16x16x64_bf16 v[192:195], v[172:175], v[152:159], v212\n" \
  "s_waitcnt vmcnt(3)\n" \
  "v_smfmac_f32_16x16x64_bf16 v[192:195], v[176:179], v[64:71], v213\n" \
  "s_waitcnt vmcnt(2)\n" \
  "v_smfmac_f32_16x16x64_bf16 v[192:195], v[180:183], v[80:87], v213\n" \
  "s_waitcnt vmcnt(1)\n" \
  "v_smfmac_f32_16x16x64_bf16 v[192:195], v[184:187], v[96:103], v213\n" \
  "s_waitcnt vmcnt(0)\n" \
  "v_smfmac_f32_16x16x64_bf16 v[192:195], v[188:191], v[112:119], v213\n" \
  "s_nop 7\n" \
  "v_permlane32_swap_b32 v192, v194\n" \
  "v_permlane32_swap_b32 v193, v195\n" \
  "v_add_f32 v192, v192, v194\n" \
  "v_add_f32 v193, v193, v195\n" \
  "v_cvt_pk_bf16_f32 v194, v192, v193\n" \
  "v_cmp_u_f32 vcc, v192, v193\n" \
  "s_nop 0\n" \
  "v_mov_b32_dpp v195, v194 quad_perm:[1,0,3,2] row_mask:0xf bank_mask:0xf\n" \
  "v_perm_b32 v194, v195, v194, v218\n" \
  "s_cbranch_vccnz .LR_bad_%=\n" \
  ".LR_good_%=:\n" \
  "buffer_store_dword v194, v217, s[40:43], s52 offen sc1\n" \
  "buffer_store_dword v194, v217, s[40:43], s64 offen sc1\n" \
  "buffer_store_dword v194, v217, s[40:43], s65 offen sc1\n" \
  "buffer_store_dword v194, v217, s[40:43], s67 offen sc1\n" \
  "s_memrealtime s[98:99]\n" \
  "s_memtime s[84:85]\n" \
  "s_waitcnt lgkmcnt(0)\n" \
  "s_sub_u32 s60, s84, s92\n" \
  "s_cmp_lt_u32 s60, 4000\n" \
  "s_cselect_b32 s60, s60, 4194304\n" \
  "s_cmp_eq_u32 s70, 0\n" \
  "s_cselect_b32 s60, s60, 4194304\n" \
  "s_min_u32 s66, s66, s60\n" \
  "s_lshl_b32 s60, s66, 10\n" \
  "s_cselect_b32 s89, %31, 2130706432\n" \
  "s_or_b32 s60, s60, s87\n" \
  "v_mov_b32 v198, s60\n" \
  "s_and_b32 s61, s48, 3\n" \
  "s_cmp_eq_u32 s61, 1\n" \
  "s_cselect_b32 s89, s89, 2130706432\n" \
  "s_cselect_b32 s66, 4194304, s66\n" \
  "s_cmp_eq_u32 s48, 0\n" \
  "s_cbranch_scc1 .LR_go0_%=\n" \
  "s_cmp_lg_u32 %26, 0\n" \
  "s_cbranch_scc1 .LR_extra_%=\n" \
  ".LR_extra_b_%=:\n" \
  "s_xor_b32 s51, s51, s56\n" \
  "s_xor_b32 s49, s49, s56\n" \
  "s_xor_b32 s58, s58, s56\n" \
  "s_xor_b32 s52, s52, s57\n" \
  "s_xor_b32 s64, s64, s57\n" \
  "s_xor_b32 s65, s65, s57\n" \
  "s_xor_b32 s67, s67, s57\n" \
  "s_xor_b32 s71, s71, s57\n" \
  "s_mov_b32 s60, s56\n" \
  "s_mov_b32 s56, s57\n" \
  "s_mov_b32 s57, s60\n" \
  ".LR_go_%=:\n" \
  "s_add_u32 s48, s48, 1\n" \
  "s_cmp_lt_u32 s48, 64\n" \
  "s_cbranch_scc1 .LR_loop_%=\n" \
  "s_branch .LR_end_%=\n" \
  ".LR_tw_re_%=:\n" \
  "s_add_u32 s69, s69, 1\n" \
  "s_cmp_gt_u32 s69, 64\n" \
  "s_cbranch_scc1 .LR_now_%=\n" \
  "buffer_load_dwordx4 v[200:203], off, s[40:43], s95 sc1\n" \
  "s_waitcnt vmcnt(0)\n" \
  "s_branch .LR_tw_%=\n" \
  ".LR_now_%=:\n" \
  "s_waitcnt lgkmcnt(0)\n" \
  "s_branch .LR_b2_%=\n" \
  ".LR_near_%=:\n" \
  "s_cmp_ge_u32 s62, 14\n" \
  "s_cbranch_scc1 .LR_lat_%=\n" \
  "s_cmp_le_u32 s62, 4\n" \
  "s_cbranch_scc1 .LR_b2_%=\n" \
  "s_memrealtime s[60:61]\n" \
  "s_waitcnt lgkmcnt(0)\n" \
  "s_sub_u32 s62, s53, s60\n" \
  "s_branch .LR_near_%=\n" \
  ".LR_lat_%=:\n" \
  "s_lshr_b32 s63, %29, 16\n" \
  "s_add_u32 s62, s62, s63\n" \
  "s_cmp_lt_u32 s62, 4294937296\n" \
  "s_cbranch_scc1 .LR_b2_%=\n" \
  "s_and_b32 s63, %29, 0xffff\n" \
  ".LR_lat1_%=:\n" \
  "s_add_u32 s44, s44, s63\n" \
  "s_add_u32 s62, s62, s63\n" \
  "s_cbranch_scc0 .LR_lat1_%=\n" \
  "s_branch .LR_again_%=\n" \
  ".LR_mv_%=:\n" \
  "s_sub_u32 s76, s77, s44\n" \
  "s_cmp_gt_u32 s76, 2000\n" \
  "s_cbranch_scc1 .LR_b2_%=\n" \
  "s_mov_b32 s44, s77\n" \
  "s_branch .LR_again_%=\n" \
  ".LR_go0_%=:\n" \
  "s_xor_b32 s51, s51, 0x40000000\n" \
  "s_xor_b32 s49, s49, 0x40000000\n" \
  "s_xor_b32 s58, s58, 0x40000000\n" \
  "s_xor_b32 s47, s47, 0x40000000\n" \
  "s_xor_b32 s52, s52, 0x40000000\n" \
  "s_xor_b32 s64, s64, 0x40000000\n" \
  "s_xor_b32 s65, s65, 0x40000000\n" \
  "s_xor_b32 s67, s67, 0x40000000\n" \
  "s_mov_b32 s44, 0\n" \
  "s_mov_b32 s89, 2130706432\n" \
  "s_mov_b32 s87, 340\n" \
  "s_mov_b32 s66, 4194304\n" \
  "s_add_u32 s95, s47, 16\n" \
  "s_mov_b32 s69, 0\n" \
  ".LR_go1_%=:\n" \
  "buffer_load_dwordx4 v[200:203], off, s[40:43], s95 sc1\n" \
  "s_add_u32 s69, s69, 1\n" \
  "s_waitcnt vmcnt(0)\n" \
  "v_readfirstlane_b32 s60, v200\n" \
  "s_cmp_gt_u32 s69, 64\n" \
  "s_cselect_b32 s60, 1, s60\n" \
  "s_cmp_lg_u32 s60, 1\n" \
  "s_cbranch_scc1 .LR_go1_%=\n" \
  "s_branch .LR_go_%=\n" \
  ".LR_extra_%=:\n" \
  "s_cmp_ge_u32 s48, 57\n" \
  "s_cselect_b32 s62, s71, 2130706432\n" \
  "s_cmp_eq_u32 s61, 2\n" \
  "s_cselect_b32 s61, %26, 2130706432\n" \
  "v_readfirstlane_b32 s63, v202\n" \
  "s_sub_u32 s60, s63, s68\n" \
  "s_mov_b32 s68, s63\n" \
  "s_add_u32 s63, s63, s60\n" \
  "s_add_u32 s63, s63, s44\n" \
  "s_lshl_b32 s60, s44, 8\n" \
  "s_or_b32 s60, s60, s48\n" \
  "v_add_u32_e64 v196, 1, s48\n" \
  "v_mov_b32 v197, s63\n" \
  "v_xor_b32 v204, s62, v219\n" \
  "v_or_b32 v204, v204, v222\n" \
  "v_writelane_b32 v196, s60, 4\n" \
  "v_writelane_b32 v197, %21, 4\n" \
  "v_writelane_b32 v204, s61, 4\n" \
  "s_nop 1\n" \
  "buffer_store_dwordx2 v[196:197], v204, s[40:43], 0 offen sc1\n" \
  "s_branch .LR_extra_b_%=\n" \
  ".LR_bad_%=:\n" \
  "s_cmp_eq_u32 s48, 0\n" \
  "s_cbranch_scc1 .LR_good_%=\n" \
  "s_add_u32 s70, s70, 1\n" \
  "s_cmp_gt_u32 s70, 5\n" \
  "s_cbranch_scc1 .LR_bad1_%=\n" \
  ".LR_redo_%=:\n" \
  "s_and_b32 s62, %29, 0xffff\n" \
  "s_add_u32 s44, s44, s62\n" \
  "v_mov_b32 v196, s44\n" \
  "v_xor_b32 v197, s71, v221\n" \
  "s_xor_b32 s54, s54, s46\n" \
  "s_xor_b32 s94, s94, s46\n" \
  "v_mov_b64 v[64:65], 0\n" \
  "v_mov_b64 v[66:67], 0\n" \
  "v_mov_b64 v[68:69], 0\n" \
  "v_mov_b64 v[70:71], 0\n" \
  "v_mov_b64 v[72:73], 0\n" \
  "v_mov_b64 v[74:75], 0\n" \
  "v_mov_b64 v[76:77], 0\n" \
  "v_mov_b64 v[78:79], 0\n" \
  "v_mov_b64 v[80:81], 0\n" \
  "v_mov_b64 v[82:83], 0\n" \
  "v_mov_b64 v[84:85], 0\n" \
  "v_mov_b64 v[86:87], 0\n" \
  "v_mov_b64 v[88:89], 0\n" \
  "v_mov_b64 v[90:91], 0\n" \
  "v_mov_b64 v[92:93], 0\n" \
  "v_mov_b64 v[94:95], 0\n" \
  "v_mov_b64 v[96:97], 0\n" \
  "v_mov_b64 v[98:99], 0\n" \
  "v_mov_b64 v[100:101], 0\n" \
  "v_mov_b64 v[102:103], 0\n" \
  "v_mov_b64 v[104:105], 0\n" \
  "v_mov_b64 v[106:107], 0\n" \
  "v_mov_b64 v[108:109], 0\n" \
  "v_mov_b64 v[110:111], 0\n" \
  "v_mov_b64 v[112:113], 0\n" \
  "v_mov_b64 v[114:115], 0\n" \
  "v_mov_b64 v[116:117], 0\n" \
  "v_mov_b64 v[118:119], 0\n" \
  "v_mov_b64 v[120:121], 0\n" \
  "v_mov_b64 v[122:123], 0\n" \
  "v_mov_b64 v[124:125], 0\n" \
  "v_mov_b64 v[126:127], 0\n" \
  "v_mov_b64 v[192:193], 0\n" \
  "v_mov_b64 v[194:195], 0\n" \
  "s_branch .LR_again_%=\n" \
  ".LR_bad1_%=:\n" \
  "v_max_u32 v196, v0, v1\n" \
  "v_max3_u32 v196, v196, v2, v3\n" \
  "v_max3_u32 v196, v196, v4, v5\n" \
  "v_max3_u32 v196, v196, v6, v7\n" \
  "v_max3_u32 v196, v196, v8, v9\n" \
  "v_max3_u32 v196, v196, v10, v11\n" \
  "v_max3_u32 v196, v196, v12, v13\n" \
  "v_max3_u32 v196, v196, v14, v15\n" \
  "v_max3_u32 v196, v196, v16, v17\n" \
  "v_max3_u32 v196, v196, v18, v19\n" \
  "v_max3_u32 v196, v196, v20, v21\n" \
  "v_max3_u32 v196, v196, v22, v23\n" \
  "v_max3_u32 v196, v196, v24, v25\n" \
  "v_max3_u32 v196, v196, v26, v27\n" \
  "v_max3_u32 v196, v196, v28, v29\n" \
  "v_max3_u32 v196, v196, v30, v31\n" \
  "v_max3_u32 v196, v196, v32, v33\n" \
  "v_max3_u32 v196, v196, v34, v35\n" \
  "v_max3_u32 v196, v196, v36, v37\n" \
  "v_max3_u32 v196, v196, v38, v39\n" \
  "v_max3_u32 v196, v196, v40, v41\n" \
  "v_max3_u32 v196, v196, v42, v43\n" \
  "v_max3_u32 v196, v196, v44, v45\n" \
  "v_max3_u32 v196, v196, v46, v47\n" \
  "v_max3_u32 v196, v196, v48, v49\n" \
  "v_max3_u32 v196, v196, v50, v51\n" \
  "v_max3_u32 v196, v196, v52, v53\n" \
  "v_max3_u32 v196, v196, v54, v55\n" \
  "v_max3_u32 v196, v196, v56, v57\n" \
  "v_max3_u32 v196, v196, v58, v59\n" \
  "v_max3_u32 v196, v196, v60, v61\n" \
  "v_max3_u32 v196, v196, v62, v63\n" \
  "v_max_u32 v197, v128, v129\n" \
  "v_max3_u32 v197, v197, v130, v131\n" \
  "v_max3_u32 v197, v197, v132, v133\n" \
  "v_max3_u32 v197, v197, v134, v135\n" \
  "v_max3_u32 v197, v197, v136, v137\n" \
  "v_max3_u32 v197, v197, v138, v139\n" \
  "v_max3_u32 v197, v197, v140, v141\n" \
  "v_max3_u32 v197, v197, v142, v143\n" \
  "v_max3_u32 v197, v197, v144, v145\n" \
  "v_max3_u32 v197, v197, v146, v147\n" \
  "v_max3_u32 v197, v197, v148, v149\n" \
  "v_max3_u32 v197, v197, v150, v151\n" \
  "v_max3_u32 v197, v197, v152, v153\n" \
  "v_max3_u32 v197, v197, v154, v155\n" \
  "v_max3_u32 v197, v197, v156, v157\n" \
  "v_max3_u32 v197, v197, v158, v159\n" \
  "v_max_u32 v204, v160, v161\n" \
  "v_max3_u32 v204, v204, v162, v163\n" \
  "v_max3_u32 v204, v204, v164, v165\n" \
  "v_max3_u32 v204, v204, v166, v167\n" \
  "v_max3_u32 v204, v204, v168, v169\n" \
  "v_max3_u32 v204, v204, v170, v171\n" \
  "v_max3_u32 v204, v204, v172, v173\n" \
  "v_max3_u32 v204, v204, v174, v175\n" \
  "v_max3_u32 v204, v204, v176, v177\n" \
  "v_max3_u32 v204, v204, v178, v179\n" \
  "v_max3_u32 v204, v204, v180, v181\n" \
  "v_max3_u32 v204, v204, v182, v183\n" \
  "v_max3_u32 v204, v204, v184, v185\n" \
  "v_max3_u32 v204, v204, v186, v187\n" \
  "v_max3_u32 v204, v204, v188, v189\n" \
  "v_max3_u32 v204, v204, v190, v191\n" \
  "v_max3_u32 v196, v196, v197, v204\n" \
  "v_cmp_eq_u32 vcc, -1, v196\n" \
  "s_cbranch_vccz .LR_nan_%=\n" \
  "s_add_u32 s88, s88, 1\n" \
  "v_mov_b32 v196, s88\n" \
  "v_mov_b32 v197, %21\n" \
  "buffer_store_dwordx2 v[196:197], off, s[40:43], %32 sc1\n" \
  "s_waitcnt vmcnt(0)\n" \
  "s_and_b32 s63, s70, 255\n" \
  "s_cbranch_scc1 .LR_redo_%=\n" \
  "buffer_load_dwordx2 v[196:197], off, s[40:43], %27 offset:128 sc1\n" \
  "s_waitcnt vmcnt(0)\n" \
  "v_readfirstlane_b32 s63, v197\n" \
  "s_cmp_eq_u32 s63, %21\n" \
  "s_cbranch_scc1 .LR_nan_%=\n" \
  "s_cmp_lt_u32 s70, 262144\n" \
  "s_cbranch_scc1 .LR_redo_%=\n" \
  "v_mov_b32 v196, 1\n" \
  "v_mov_b32 v197, %21\n" \
  "buffer_store_dwordx2 v[196:197], off, s[40:43], %27 offset:128 sc1\n" \
  ".LR_nan_%=:\n" \
  "v_min_u32 v194, -2, v194\n" \
  "s_branch .LR_good_%=\n" \
  ".LR_fu_%=:\n" \
  "buffer_load_dwordx2 v[140:141], v220, s[40:43], 0 offen sc1\n" \
  "s_waitcnt vmcnt(0)\n" \
  "v_cmp_ne_u32 vcc, %21, v141\n" \
  "s_nop 1\n" \
  "v_cndmask_b32 v140, v140, 0, vcc\n" \
  "v_cmp_ge_i32 vcc, s48, v140\n" \
  "s_min_u32 s60, s48, 31\n" \
  "s_bitcmp1_b32 43690, s60\n" \
  "s_cselect_b32 vcc_lo, 0, vcc_lo\n" \
  "s_cselect_b32 vcc_hi, vcc_hi, 0\n" \
  "s_mov_b64 vcc, vcc\n" \
  "s_cbranch_vccnz .LR_fu0_%=\n" \
  "s_cmp_lg_u32 %19, 3\n" \
  "s_cbranch_scc1 .LR_h8_%=\n" \
  "s_xor_b32 s60, s61, s75\n" \
  "buffer_load_dwordx4 v[40:43], v214, s[40:43], s60 offen sc1\n" \
  "s_xor_b32 s60, s60, 1024\n" \
  "buffer_load_dwordx4 v[44:47], v214, s[40:43], s60 offen sc1\n" \
  ".LR_h8_%=:\n" \
  "s_xor_b32 s60, s61, s64\n" \
  "buffer_load_dwordx4 v[0:3], v214, s[40:43], s60 offen sc1\n" \
  "s_xor_b32 s60, s60, 1024\n" \
  "buffer_load_dwordx4 v[4:7], v214, s[40:43], s60 offen sc1\n" \
  "s_xor_b32 s60, s61, s65\n" \
  "buffer_load_dwordx4 v[8:11], v214, s[40:43], s60 offen sc1\n" \
  "s_xor_b32 s60, s60, 1024\n" \
  "buffer_load_dwordx4 v[12:15], v214, s[40:43], s60 offen sc1\n" \
  "s_xor_b32 s60, s61, s67\n" \
  "buffer_load_dwordx4 v[16:19], v214, s[40:43], s60 offen sc1\n" \
  "s_xor_b32 s60, s60, 1024\n" \
  "buffer_load_dwordx4 v[20:23], v214, s[40:43], s60 offen sc1\n" \
  "s_cmp_lt_u32 s48, 62\n" \
  "s_cselect_b32 s60, s71, 2130706432\n" \
  "v_xor_b32 v142, s60, v221\n" \
  "v_xor_b32 v143, s60, v222\n" \
  "v_xor_b32 v144, s60, v223\n" \
  "buffer_store_dwordx4 v[136:139], v142, s[40:43], 0 offen sc1\n" \
  "buffer_store_dwordx4 v[136:139], v143, s[40:43], 0 offen sc1\n" \
  "buffer_store_dwordx4 v[136:139], v144, s[40:43], 0 offen sc1\n" \
  "s_barrier\n" \
  "s_xor_b32 s60, s61, s68\n" \
  "buffer_load_dwordx4 v[24:27], v214, s[40:43], s60 offen sc1\n" \
  "s_xor_b32 s60, s60, 1024\n" \
  "buffer_load_dwordx4 v[28:31], v214, s[40:43], s60 offen sc1\n" \
  "s_xor_b32 s60, s61, s74\n" \
  "buffer_load_dwordx4 v[32:35], v214, s[40:43], s60 offen sc1\n" \
  "s_xor_b32 s60, s60, 1024\n" \
  "buffer_load_dwordx4 v[36:39], v214, s[40:43], s60 offen sc1\n" \
  "buffer_load_dwordx2 v[140:141], v220, s[40:43], 0 offen sc1\n" \
  "s_waitcnt vmcnt(0)\n" \
  "v_mov_b32 v72, v24\n" \
  "v_mov_b32 v73, v28\n" \
  "v_mov_b32 v74, v25\n" \
  "v_mov_b32 v75, v29\n" \
  "v_mov_b32 v76, v26\n" \
  "v_mov_b32 v77, v30\n" \
  "v_mov_b32 v78, v27\n" \
  "v_mov_b32 v79, v31\n" \
  "ds_write_b128 v211, v[72:75] offset:6144\n" \
  "ds_write_b128 v211, v[76:79] offset:7168\n" \
  "v_mov_b32 v80, v32\n" \
  "v_mov_b32 v81, v36\n" \
  "v_mov_b32 v82, v33\n" \
  "v_mov_b32 v83, v37\n" \
  "v_mov_b32 v84, v34\n" \
  "v_mov_b32 v85, v38\n" \
  "v_mov_b32 v86, v35\n" \
  "v_mov_b32 v87, v39\n" \
  "ds_write_b128 v211, v[80:83] offset:8192\n" \
  "ds_write_b128 v211, v[84:87] offset:9216\n" \
  "s_branch .LR_fu_b_%=\n" \
  ".LR_fu0_%=:\n" \
  "s_cmp_lt_u32 s48, 62\n" \
  "s_cselect_b32 s60, s71, 2130706432\n" \
  "v_xor_b32 v142, s60, v221\n" \
  "v_xor_b32 v143, s60, v222\n" \
  "v_xor_b32 v144, s60, v223\n" \
  "buffer_store_dwordx4 v[136:139], v142, s[40:43], 0 offen sc1\n" \
  "buffer_store_dwordx4 v[136:139], v143, s[40:43], 0 offen sc1\n" \
  "buffer_store_dwordx4 v[136:139], v144, s[40:43], 0 offen sc1\n" \
  "s_barrier\n" \
  "s_sleep 0\n" \
  "s_mov_b32 s69, 0\n" \
  ".LR_fu1_%=:\n" \
  "buffer_load_dwordx2 v[140:141], v220, s[40:43], 0 offen sc1\n" \
  "s_waitcnt vmcnt(0)\n" \
  "v_cmp_ne_u32 vcc, %21, v141\n" \
  "s_nop 1\n" \
  "v_cndmask_b32 v140, v140, 0, vcc\n" \
  "v_cmp_ge_i32 vcc, s48, v140\n" \
  "s_min_u32 s60, s48, 31\n" \
  "s_bitcmp1_b32 43690, s60\n" \
  "s_cselect_b32 vcc_lo, 0, vcc_lo\n" \
  "s_cselect_b32 vcc_hi, vcc_hi, 0\n" \
  "s_mov_b64 vcc, vcc\n" \
  "s_cbranch_vccz .LR_fu_x_%=\n" \
  "s_sleep 2\n" \
  "s_add_u32 s69, s69, 1\n" \
  "s_and_b32 s63, s69, 1023\n" \
  "s_cbranch_scc1 .LR_fu1_%=\n" \
  "s_cmp_gt_u32 s69, 2097152\n" \
  "s_cbranch_scc1 .LR_fu1_set_%=\n" \
  "buffer_load_dwordx2 v[196:197], off, s[40:43], %27 offset:128 sc1\n" \
  "s_waitcnt vmcnt(0)\n" \
  "v_readfirstlane_b32 s63, v197\n" \
  "s_cmp_eq_u32 s63, %21\n" \
  "s_cbranch_scc0 .LR_fu1_%=\n" \
  "s_branch .LR_fu_x_%=\n" \
  ".LR_fu1_set_%=:\n" \
  "v_mov_b32 v196, 1\n" \
  "v_mov_b32 v197, %21\n" \
  "buffer_store_dwordx2 v[196:197], off, s[40:43], %27 offset:128 sc1\n" \
  "s_waitcnt vmcnt(0)\n" \
  "s_branch .LR_fu_x_%=\n" \
  ".LR_fu_x_%=:\n" \
  "s_xor_b32 s60, s61, s68\n" \
  "buffer_load_dwordx4 v[24:27], v214, s[40:43], s60 offen sc1\n" \
  "s_xor_b32 s60, s60, 1024\n" \
  "buffer_load_dwordx4 v[28:31], v214, s[40:43], s60 offen sc1\n" \
  "s_xor_b32 s60, s61, s74\n" \
  "buffer_load_dwordx4 v[32:35], v214, s[40:43], s60 offen sc1\n" \
  "s_xor_b32 s60, s60, 1024\n" \
  "buffer_load_dwordx4 v[36:39], v214, s[40:43], s60 offen sc1\n" \
  "s_cmp_lg_u32 %19, 3\n" \
  "s_cbranch_scc1 .LR_h7_%=\n" \
  "s_xor_b32 s60, s61, s75\n" \
  "buffer_load_dwordx4 v[40:43], v214, s[40:43], s60 offen sc1\n" \
  "s_xor_b32 s60, s60, 1024\n" \
  "buffer_load_dwordx4 v[44:47], v214, s[40:43], s60 offen sc1\n" \
  ".LR_h7_%=:\n" \
  "s_xor_b32 s60, s61, s64\n" \
  "buffer_load_dwordx4 v[0:3], v214, s[40:43], s60 offen sc1\n" \
  "s_xor_b32 s60, s60, 1024\n" \
  "buffer_load_dwordx4 v[4:7], v214, s[40:43], s60 offen sc1\n" \
  "s_xor_b32 s60, s61, s65\n" \
  "buffer_load_dwordx4 v[8:11], v214, s[40:43], s60 offen sc1\n" \
  "s_xor_b32 s60, s60, 1024\n" \
  "buffer_load_dwordx4 v[12:15], v214, s[40:43], s60 offen sc1\n" \
  "s_xor_b32 s60, s61, s67\n" \
  "buffer_load_dwordx4 v[16:19], v214, s[40:43], s60 offen sc1\n" \
  "s_xor_b32 s60, s60, 1024\n" \
  "buffer_load_dwordx4 v[20:23], v214, s[40:43], s60 offen sc1\n" \
  "buffer_load_dwordx2 v[140:141], v220, s[40:43], 0 offen sc1\n" \
  "s_waitcnt vmcnt(7)\n" \
  "v_mov_b32 v72, v24\n" \
  "v_mov_b32 v73, v28\n" \
  "v_mov_b32 v74, v25\n" \
  "v_mov_b32 v75, v29\n" \
  "v_mov_b32 v76, v26\n" \
  "v_mov_b32 v77, v30\n" \
  "v_mov_b32 v78, v27\n" \
  "v_mov_b32 v79, v31\n" \
  "ds_write_b128 v211, v[72:75] offset:6144\n" \
  "ds_write_b128 v211, v[76:79] offset:7168\n" \
  "v_mov_b32 v80, v32\n" \
  "v_mov_b32 v81, v36\n" \
  "v_mov_b32 v82, v33\n" \
  "v_mov_b32 v83, v37\n" \
  "v_mov_b32 v84, v34\n" \
  "v_mov_b32 v85, v38\n" \
  "v_mov_b32 v86, v35\n" \
  "v_mov_b32 v87, v39\n" \
  "ds_write_b128 v211, v[80:83] offset:8192\n" \
  "ds_write_b128 v211, v[84:87] offset:9216\n" \
  "s_waitcnt vmcnt(0)\n" \
  "s_branch .LR_fu_b_%=\n" \
  ".LR_nb0_%=:\n" \
  "s_waitcnt vmcnt(0)\n" \
  "s_branch .LR_nb1_%=\n" \
  ".LR_help_%=:\n" \
  "s_mul_i32 s60, %19, 5\n" \
  "s_sub_u32 s60, s60, 5\n" \
  "s_lshl_b32 s61, s60, 11\n" \
  "s_add_u32 s61, s61, 8192\n" \
  "v_add_u32 v211, s61, v214\n" \
  "s_lshr_b32 s61, s60, 2\n" \
  "s_lshl_b32 s61, s61, 14\n" \
  "s_and_b32 s62, s60, 3\n" \
  "s_lshl_b32 s62, s62, 11\n" \
  "s_xor_b32 s61, s61, s62\n" \
  "s_xor_b32 s64, s61, %25\n" \
  "s_add_u32 s60, s60, 1\n" \
  "s_lshr_b32 s61, s60, 2\n" \
  "s_lshl_b32 s61, s61, 14\n" \
  "s_and_b32 s62, s60, 3\n" \
  "s_lshl_b32 s62, s62, 11\n" \
  "s_xor_b32 s61, s61, s62\n" \
  "s_xor_b32 s65, s61, %25\n" \
  "s_add_u32 s60, s60, 1\n" \
  "s_lshr_b32 s61, s60, 2\n" \
  "s_lshl_b32 s61, s61, 14\n" \
  "s_and_b32 s62, s60, 3\n" \
  "s_lshl_b32 s62, s62, 11\n" \
  "s_xor_b32 s61, s61, s62\n" \
  "s_xor_b32 s67, s61, %25\n" \
  "s_add_u32 s60, s60, 1\n" \
  "s_lshr_b32 s61, s60, 2\n" \
  "s_lshl_b32 s61, s61, 14\n" \
  "s_and_b32 s62, s60, 3\n" \
  "s_lshl_b32 s62, s62, 11\n" \
  "s_xor_b32 s61, s61, s62\n" \
  "s_xor_b32 s68, s61, %25\n" \
  "s_add_u32 s60, s60, 1\n" \
  "s_lshr_b32 s61, s60, 2\n" \
  "s_lshl_b32 s61, s61, 14\n" \
  "s_and_b32 s62, s60, 3\n" \
  "s_lshl_b32 s62, s62, 11\n" \
  "s_xor_b32 s61, s61, s62\n" \
  "s_xor_b32 s74, s61, %25\n" \
  "s_add_u32 s60, s60, 1\n" \
  "s_lshr_b32 s61, s60, 2\n" \
  "s_lshl_b32 s61, s61, 14\n" \
  "s_and_b32 s62, s60, 3\n" \
  "s_lshl_b32 s62, s62, 11\n" \
  "s_xor_b32 s61, s61, s62\n" \
  "s_xor_b32 s75, s61, %25\n" \
  "s_add_u32 s60, s60, 1\n" \
  "s_xor_b32 s71, s56, s57\n" \
  "s_movk_i32 s88, -32768\n" \
  "v_mov_b64 v[136:137], -1\n" \
  "v_mov_b64 v[138:139], -1\n" \
  "v_mov_b64 v[140:141], 0\n" \
  "s_mov_b64 vcc, -1\n" \
  "s_barrier\n" \
  ".LR_hl_%=:\n" \
  "s_and_b32 s61, s48, 15\n" \
  "s_lshl_b32 s61, s61, 16\n" \
  "s_bfe_u32 s60, %33, 524288\n" \
  ".LR_hs_%=:\n" \
  "s_cmp_eq_u32 s60, 0\n" \
  "s_cbranch_scc1 .LR_hs_x_%=\n" \
  "s_sleep 1\n" \
  "s_sub_u32 s60, s60, 1\n" \
  "s_branch .LR_hs_%=\n" \
  ".LR_hs_x_%=:\n" \
  "s_xor_b32 s60, s71, s57\n" \
  "v_xor_b32 v145, s60, v224\n" \
  "buffer_store_dwordx4 v[136:139], v145, s[40:43], 0 offen sc1\n" \
  "s_cbranch_vccnz .LR_fu_%=\n" \
  "s_cmp_lg_u32 %19, 3\n" \
  "s_cbranch_scc1 .LR_h5_%=\n" \
  "s_xor_b32 s60, s61, s75\n" \
  "buffer_load_dwordx4 v[40:43], v214, s[40:43], s60 offen sc1\n" \
  "s_xor_b32 s60, s60, 1024\n" \
  "buffer_load_dwordx4 v[44:47], v214, s[40:43], s60 offen sc1\n" \
  ".LR_h5_%=:\n" \
  "s_xor_b32 s60, s61, s64\n" \
  "buffer_load_dwordx4 v[0:3], v214, s[40:43], s60 offen sc1\n" \
  "s_xor_b32 s60, s60, 1024\n" \
  "buffer_load_dwordx4 v[4:7], v214, s[40:43], s60 offen sc1\n" \
  "s_xor_b32 s60, s61, s65\n" \
  "buffer_load_dwordx4 v[8:11], v214, s[40:43], s60 offen sc1\n" \
  "s_xor_b32 s60, s60, 1024\n" \
  "buffer_load_dwordx4 v[12:15], v214, s[40:43], s60 offen sc1\n" \
  "s_xor_b32 s60, s61, s67\n" \
  "buffer_load_dwordx4 v[16:19], v214, s[40:43], s60 offen sc1\n" \
  "s_xor_b32 s60, s60, 1024\n" \
  "buffer_load_dwordx4 v[20:23], v214, s[40:43], s60 offen sc1\n" \
  "s_cmp_lt_u32 s48, 62\n" \
  "s_cselect_b32 s60, s71, 2130706432\n" \
  "v_xor_b32 v142, s60, v221\n" \
  "v_xor_b32 v143, s60, v222\n" \
  "v_xor_b32 v144, s60, v223\n" \
  "buffer_store_dwordx4 v[136:139], v142, s[40:43], 0 offen sc1\n" \
  "buffer_store_dwordx4 v[136:139], v143, s[40:43], 0 offen sc1\n" \
  "buffer_store_dwordx4 v[136:139], v144, s[40:43], 0 offen sc1\n" \
  "s_waitcnt vmcnt(9)\n" \
  "v_mov_b32 v72, v24\n" \
  "v_mov_b32 v73, v28\n" \
  "v_mov_b32 v74, v25\n" \
  "v_mov_b32 v75, v29\n" \
  "v_mov_b32 v76, v26\n" \
  "v_mov_b32 v77, v30\n" \
  "v_mov_b32 v78, v27\n" \
  "v_mov_b32 v79, v31\n" \
  "ds_write_b128 v211, v[72:75] offset:6144\n" \
  "ds_write_b128 v211, v[76:79] offset:7168\n" \
  "v_mov_b32 v80, v32\n" \
  "v_mov_b32 v81, v36\n" \
  "v_mov_b32 v82, v33\n" \
  "v_mov_b32 v83, v37\n" \
  "v_mov_b32 v84, v34\n" \
  "v_mov_b32 v85, v38\n" \
  "v_mov_b32 v86, v35\n" \
  "v_mov_b32 v87, v39\n" \
  "ds_write_b128 v211, v[80:83] offset:8192\n" \
  "ds_write_b128 v211, v[84:87] offset:9216\n" \
  "s_barrier\n" \
  ".LR_fu_b_%=:\n" \
  "s_add_u32 s48, s48, 1\n" \
  "v_cmp_ne_u32 vcc, %21, v141\n" \
  "s_nop 1\n" \
  "v_cndmask_b32 v140, v140, 0, vcc\n" \
  "v_cmp_ge_i32 vcc, s48, v140\n" \
  "s_min_u32 s60, s48, 31\n" \
  "s_bitcmp1_b32 43690, s60\n" \
  "s_cselect_b32 vcc_lo, 0, vcc_lo\n" \
  "s_cselect_b32 vcc_hi, vcc_hi, 0\n" \
  "s_mov_b64 vcc, vcc\n" \
  "s_and_b32 s61, s48, 15\n" \
  "s_lshl_b32 s61, s61, 16\n" \
  "s_cbranch_vccnz .LR_nb0_%=\n" \
  "s_bfe_u32 s60, %33, 524296\n" \
  ".LR_ht_%=:\n" \
  "s_cmp_eq_u32 s60, 0\n" \
  "s_cbranch_scc1 .LR_ht_x_%=\n" \
  "s_sleep 1\n" \
  "s_sub_u32 s60, s60, 1\n" \
  "s_branch .LR_ht_%=\n" \
  ".LR_ht_x_%=:\n" \
  "s_xor_b32 s60, s61, s68\n" \
  "buffer_load_dwordx4 v[24:27], v214, s[40:43], s60 offen sc1\n" \
  "s_xor_b32 s60, s60, 1024\n" \
  "buffer_load_dwordx4 v[28:31], v214, s[40:43], s60 offen sc1\n" \
  "s_xor_b32 s60, s61, s74\n" \
  "buffer_load_dwordx4 v[32:35], v214, s[40:43], s60 offen sc1\n" \
  "s_xor_b32 s60, s60, 1024\n" \
  "buffer_load_dwordx4 v[36:39], v214, s[40:43], s60 offen sc1\n" \
  "s_waitcnt vmcnt(4)\n" \
  ".LR_nb1_%=:\n" \
  "s_add_u32 s60, s48, 3\n" \
  "v_cmp_ge_i32 s[76:77], s60, v140\n" \
  "s_cmp_lt_u32 s48, 16\n" \
  "s_cselect_b32 s77, s77, 0\n" \
  "s_cmp_eq_u64 s[76:77], 0\n" \
  "s_cbranch_scc1 .LR_nf_%=\n" \
  "buffer_load_dwordx2 v[140:141], v220, s[40:43], 0 offen sc1\n" \
  ".LR_nf_%=:\n" \
  "v_mov_b32 v48, v0\n" \
  "v_mov_b32 v49, v4\n" \
  "v_mov_b32 v50, v1\n" \
  "v_mov_b32 v51, v5\n" \
  "v_mov_b32 v52, v2\n" \
  "v_mov_b32 v53, v6\n" \
  "v_mov_b32 v54, v3\n" \
  "v_mov_b32 v55, v7\n" \
  "ds_write_b128 v211, v[48:51] offset:0\n" \
  "ds_write_b128 v211, v[52:55] offset:1024\n" \
  "v_mov_b32 v56, v8\n" \
  "v_mov_b32 v57, v12\n" \
  "v_mov_b32 v58, v9\n" \
  "v_mov_b32 v59, v13\n" \
  "v_mov_b32 v60, v10\n" \
  "v_mov_b32 v61, v14\n" \
  "v_mov_b32 v62, v11\n" \
  "v_mov_b32 v63, v15\n" \
  "ds_write_b128 v211, v[56:59] offset:2048\n" \
  "ds_write_b128 v211, v[60:63] offset:3072\n" \
  "v_mov_b32 v64, v16\n" \
  "v_mov_b32 v65, v20\n" \
  "v_mov_b32 v66, v17\n" \
  "v_mov_b32 v67, v21\n" \
  "v_mov_b32 v68, v18\n" \
  "v_mov_b32 v69, v22\n" \
  "v_mov_b32 v70, v19\n" \
  "v_mov_b32 v71, v23\n" \
  "ds_write_b128 v211, v[64:67] offset:4096\n" \
  "ds_write_b128 v211, v[68:71] offset:5120\n" \
  "s_cmp_lg_u32 %19, 3\n" \
  "s_cbranch_scc1 .LR_h6_%=\n" \
  "v_mov_b32 v88, v40\n" \
  "v_mov_b32 v89, v44\n" \
  "v_mov_b32 v90, v41\n" \
  "v_mov_b32 v91, v45\n" \
  "v_mov_b32 v92, v42\n" \
  "v_mov_b32 v93, v46\n" \
  "v_mov_b32 v94, v43\n" \
  "v_mov_b32 v95, v47\n" \
  "ds_write_b128 v211, v[88:91] offset:10240\n" \
  "ds_write_b128 v211, v[92:95] offset:11264\n" \
  ".LR_h6_%=:\n" \
  "s_sub_u32 s88, 0, s88\n" \
  "s_xor_b32 s71, s71, s56\n" \
  "s_mov_b32 s61, s56\n" \
  "s_mov_b32 s56, s57\n" \
  "s_mov_b32 s57, s61\n" \
  "s_waitcnt lgkmcnt(0)\n" \
  "v_add_u32 v211, s88, v211\n" \
  "s_barrier\n" \
  "s_cmp_lt_u32 s48, 63\n" \
  "s_cbranch_scc1 .LR_hl_%=\n" \
  "s_barrier\n" \
  ".LR_end_%=:\n" \
  "s_waitcnt vmcnt(0)\n"
#define R_ASM_VOPS "+{v212}"(ri.sidx), "+{v213}"(ri.cidx), "+{v214}"(ri.l16), "+{v215}"(ri.ax), "+{v216}"(ri.am), "+{v217}"(ri.mo), "+{v218}"(ri.sel), "+{v219}"(ri.to), "+{v220}"(ri.fu), "+{v221}"(ri.p0), "+{v222}"(ri.p1), "+{v223}"(ri.p2), "+{v224}"(ri.p3), "+{v225}"(ri.d0), "+{v226}"(ri.d1), "+{v227}"(ri.d2), "+{v228}"(ri.d3), "+{v229}"(ri.d4)
#define R_ASM_SOPS "s"(ri.ws), "s"(ri.w), "s"(ri.go), "s"(ri.tag), "s"(ri.o_va), "s"(ri.o_vb), "s"(ri.lab), "s"(ri.o_u), "s"(ri.kind), "s"(ri.o_ctl), "s"(ri.pa), "s"(ri.pb), "s"(ri.pc), "s"(ri.f_d), "s"(ri.f_r), "s"(ri.ph)
#define R_ASM_CLOB "v0", "v1", "v2", "v3", "v4", "v5", "v6", "v7", "v8", "v9", "v10", "v11", "v12", "v13", "v14", "v15", "v16", "v17", "v18", "v19", "v20", "v21", "v22", "v23", "v24", "v25", "v26", "v27", "v28", "v29", "v30", "v31", "v32", "v33", "v34", "v35", "v36", "v37", "v38", "v39", "v40", "v41", "v42", "v43", "v44", "v45", "v46", "v47", "v48", "v49", "v50", "v51", "v52", "v53", "v54", "v55", "v56", "v57", "v58", "v59", "v60", "v61", "v62", "v63", "v64", "v65", "v66", "v67", "v68", "v69", "v70", "v71", "v72", "v73", "v74", "v75", "v76", "v77", "v78", "v79", "v80", "v81", "v82", "v83", "v84", "v85", "v86", "v87", "v88", "v89", "v90", "v91", "v92", "v93", "v94", "v95", "v96", "v97", "v98", "v99", "v100", "v101", "v102", "v103", "v104", "v105", "v106", "v107", "v108", "v109", "v110", "v111", "v112", "v113", "v114", "v115", "v116", "v117", "v118", "v119", "v120", "v121", "v122", "v123", "v124", "v125", "v126", "v127", "v128", "v129", "v130", "v131", "v132", "v133", "v134", "v135", "v136", "v137", "v138", "v139", "v140", "v141", "v142", "v143", "v144", "v145", "v146", "v147", "v148", "v149", "v150", "v151", "v152", "v153", "v154", "v155", "v156", "v157", "v158", "v159", "v160", "v161", "v162", "v163", "v164", "v165", "v166", "v167", "v168", "v169", "v170", "v171", "v172", "v173", "v174", "v175", "v176", "v177", "v178", "v179", "v180", "v181", "v182", "v183", "v184", "v185", "v186", "v187", "v188", "v189", "v190", "v191", "v192", "v193", "v194", "v195", "v196", "v197", "v198", "v199", "v200", "v201", "v202", "v203", "v204", "v205", "v206", "v207", "v208", "v209", "v210", "v211", "a0", "a1", "a2", "a3", "a4", "a5", "a6", "a7", "a8", "a9", "a10", "a11", "a12", "a13", "a14", "a15", "a16", "a17", "a18", "a19", "a20", "a21", "a22", "a23", "a24", "a25", "a26", "a27", "a28", "a29", "a30", "a31", "a32", "a33", "a34", "a35", "a36", "a37", "a38", "a39", "a40", "a41", "a42", "a43", "a44", "a45", "a46", "a47", "a48", "a49", "a50", "a51", "a52", "a53", "a54", "a55", "a56", "a57", "a58", "a59", "a60", "a61", "a62", "a63", "a64", "a65", "a66", "a67", "a68", "a69", "a70", "a71", "a72", "a73", "a74", "a75", "a76", "a77", "a78", "a79", "a80", "a81", "a82", "a83", "a84", "a85", "a86", "a87", "a88", "a89", "a90", "a91", "a92", "a93", "a94", "a95", "a96", "a97", "a98", "a99", "a100", "a101", "a102", "a103", "a104", "a105", "a106", "a107", "a108", "a109", "a110", "a111", "a112", "a113", "a114", "a115", "a116", "a117", "a118", "a119", "a120", "a121", "a122", "a123", "a124", "a125", "a126", "a127", "s40", "s41", "s42", "s43", "s44", "s45", "s46", "s47", "s48", "s49", "s50", "s51", "s52", "s53", "s54", "s55", "s56", "s57", "s58", "s59", "s60", "s61", "s62", "s63", "s64", "s65", "s66", "s67", "s68", "s69", "s70", "s71", "s72", "s73", "s74", "s75", "s76", "s77", "s78", "s79", "s80", "s81", "s82", "s83", "s84", "s85", "s86", "s87", "s88", "s89", "s90", "s91", "s92", "s93", "s94", "s95", "s96", "s97", "s98", "s99", "vcc", "scc", "memory"
#define K_ASM_TEXT \
  "s_mov_b64 s[40:41], %30\n" \
  "s_and_b32 s41, s41, 0xffff\n" \
  "s_mov_b32 s42, 255852544\n" \
  "s_mov_b32 s43, 0x00020000\n" \
  "s_mov_b32 s47, 0x00020000\n" \
  "s_lshl_b32 s72, %34, 12\n" \
  "buffer_load_dword v184, off, s[40:43], %42 sc1\n" \
  "s_mov_b64 s[44:45], %31\n" \
  "s_and_b32 s45, s45, 0xffff\n" \
  "s_mov_b32 s46, 4718592\n" \
  "s_add_u32 s60, %35, 0\n" \
  "s_add_u32 m0, s72, 16384\n" \
  "s_nop 0\n" \
  "buffer_load_dwordx4 v170, s[44:47], s60 offen lds\n" \
  "buffer_load_dwordx4 v171, s[44:47], s60 offen offset:1024 lds\n" \
  "buffer_load_dwordx4 v172, s[44:47], s60 offen offset:2048 lds\n" \
  "buffer_load_dwordx4 v173, s[44:47], s60 offen offset:3072 lds\n" \
  "s_add_u32 s60, %35, 524288\n" \
  "s_add_u32 m0, s72, 32768\n" \
  "s_nop 0\n" \
  "buffer_load_dwordx4 v170, s[44:47], s60 offen lds\n" \
  "buffer_load_dwordx4 v171, s[44:47], s60 offen offset:1024 lds\n" \
  "buffer_load_dwordx4 v172, s[44:47], s60 offen offset:2048 lds\n" \
  "buffer_load_dwordx4 v173, s[44:47], s60 offen offset:3072 lds\n" \
  "s_add_u32 s60, %35, 1048576\n" \
  "s_add_u32 m0, s72, 49152\n" \
  "s_nop 0\n" \
  "buffer_load_dwordx4 v170, s[44:47], s60 offen lds\n" \
  "buffer_load_dwordx4 v171, s[44:47], s60 offen offset:1024 lds\n" \
  "buffer_load_dwordx4 v172, s[44:47], s60 offen offset:2048 lds\n" \
  "buffer_load_dwordx4 v173, s[44:47], s60 offen offset:3072 lds\n" \
  "s_add_u32 s60, %35, 1572864\n" \
  "s_add_u32 m0, s72, 65536\n" \
  "s_nop 0\n" \
  "buffer_load_dwordx4 v170, s[44:47], s60 offen lds\n" \
  "buffer_load_dwordx4 v171, s[44:47], s60 offen offset:1024 lds\n" \
  "buffer_load_dwordx4 v172, s[44:47], s60 offen offset:2048 lds\n" \
  "buffer_load_dwordx4 v173, s[44:47], s60 offen offset:3072 lds\n" \
  "s_add_u32 s60, %35, 2097152\n" \
  "s_add_u32 m0, s72, 81920\n" \
  "s_nop 0\n" \
  "buffer_load_dwordx4 v170, s[44:47], s60 offen lds\n" \
  "buffer_load_dwordx4 v171, s[44:47], s60 offen offset:1024 lds\n" \
  "buffer_load_dwordx4 v172, s[44:47], s60 offen offset:2048 lds\n" \
  "buffer_load_dwordx4 v173, s[44:47], s60 offen offset:3072 lds\n" \
  "s_add_u32 s60, %35, 2621440\n" \
  "s_add_u32 m0, s72, 98304\n" \
  "s_nop 0\n" \
  "buffer_load_dwordx4 v170, s[44:47], s60 offen lds\n" \
  "buffer_load_dwordx4 v171, s[44:47], s60 offen offset:1024 lds\n" \
  "buffer_load_dwordx4 v172, s[44:47], s60 offen offset:2048 lds\n" \
  "buffer_load_dwordx4 v173, s[44:47], s60 offen offset:3072 lds\n" \
  "s_add_u32 s60, %35, 3145728\n" \
  "s_add_u32 m0, s72, 114688\n" \
  "s_nop 0\n" \
  "buffer_load_dwordx4 v170, s[44:47], s60 offen lds\n" \
  "buffer_load_dwordx4 v171, s[44:47], s60 offen offset:1024 lds\n" \
  "buffer_load_dwordx4 v172, s[44:47], s60 offen offset:2048 lds\n" \
  "buffer_load_dwordx4 v173, s[44:47], s60 offen offset:3072 lds\n" \
  "s_add_u32 s60, %35, 3670016\n" \
  "s_add_u32 m0, s72, 131072\n" \
  "s_nop 0\n" \
  "buffer_load_dwordx4 v170, s[44:47], s60 offen lds\n" \
  "buffer_load_dwordx4 v171, s[44:47], s60 offen offset:1024 lds\n" \
  "buffer_load_dwordx4 v172, s[44:47], s60 offen offset:2048 lds\n" \
  "buffer_load_dwordx4 v173, s[44:47], s60 offen offset:3072 lds\n" \
  "s_add_u32 s60, %35, 4194304\n" \
  "s_add_u32 m0, s72, 147456\n" \
  "s_nop 0\n" \
  "buffer_load_dwordx4 v170, s[44:47], s60 offen lds\n" \
  "buffer_load_dwordx4 v171, s[44:47], s60 offen offset:1024 lds\n" \
  "buffer_load_dwordx4 v172, s[44:47], s60 offen offset:2048 lds\n" \
  "buffer_load_dwordx4 v173, s[44:47], s60 offen offset:3072 lds\n" \
  "s_waitcnt vmcnt(24)\n" \
  "s_barrier\n" \
  "v_add_u32 v186, 16384, v182\n" \
  "ds_read_b64_tr_b16 a[0:1], v186\n" \
  "ds_read_b64_tr_b16 a[2:3], v186 offset:1024\n" \
  "ds_read_b64_tr_b16 a[4:5], v186 offset:8192\n" \
  "ds_read_b64_tr_b16 a[6:7], v186 offset:9216\n" \
  "v_add_u32 v187, 16384, v183\n" \
  "ds_read_b64_tr_b16 a[72:73], v187\n" \
  "ds_read_b64_tr_b16 a[74:75], v187 offset:1024\n" \
  "ds_read_b64_tr_b16 a[76:77], v187 offset:8192\n" \
  "ds_read_b64_tr_b16 a[78:79], v187 offset:9216\n" \
  "v_add_u32 v186, 32768, v182\n" \
  "ds_read_b64_tr_b16 a[8:9], v186\n" \
  "ds_read_b64_tr_b16 a[10:11], v186 offset:1024\n" \
  "ds_read_b64_tr_b16 a[12:13], v186 offset:8192\n" \
  "ds_read_b64_tr_b16 a[14:15], v186 offset:9216\n" \
  "v_add_u32 v187, 32768, v183\n" \
  "ds_read_b64_tr_b16 a[80:81], v187\n" \
  "ds_read_b64_tr_b16 a[82:83], v187 offset:1024\n" \
  "ds_read_b64_tr_b16 a[84:85], v187 offset:8192\n" \
  "ds_read_b64_tr_b16 a[86:87], v187 offset:9216\n" \
  "v_add_u32 v186, 49152, v182\n" \
  "ds_read_b64_tr_b16 a[16:17], v186\n" \
  "ds_read_b64_tr_b16 a[18:19], v186 offset:1024\n" \
  "ds_read_b64_tr_b16 a[20:21], v186 offset:8192\n" \
  "ds_read_b64_tr_b16 a[22:23], v186 offset:9216\n" \
  "v_add_u32 v187, 49152, v183\n" \
  "ds_read_b64_tr_b16 a[88:89], v187\n" \
  "ds_read_b64_tr_b16 a[90:91], v187 offset:1024\n" \
  "ds_read_b64_tr_b16 a[92:93], v187 offset:8192\n" \
  "ds_read_b64_tr_b16 a[94:95], v187 offset:9216\n" \
  "s_waitcnt lgkmcnt(0)\n" \
  "s_barrier\n" \
  "v_readfirstlane_b32 s55, v184\n" \
  "s_add_u32 s55, s55, 1\n" \
  "s_mov_b64 s[44:45], %33\n" \
  "s_and_b32 s45, s45, 0xffff\n" \
  "s_mov_b32 s46, 2359296\n" \
  "s_mov_b32 s51, 0\n" \
  "s_mul_i32 s52, %34, 9216\n" \
  "s_add_u32 s52, s52, 16384\n" \
  "s_add_u32 m0, s52, 0\n" \
  "s_nop 0\n" \
  "buffer_load_dwordx4 v196, s[44:47], s51 offen lds\n" \
  "s_add_u32 m0, s52, 1024\n" \
  "s_nop 0\n" \
  "buffer_load_dwordx4 v197, s[44:47], s51 offen lds\n" \
  "s_add_u32 m0, s52, 2048\n" \
  "s_nop 0\n" \
  "buffer_load_dwordx4 v198, s[44:47], s51 offen lds\n" \
  "s_add_u32 m0, s52, 3072\n" \
  "s_nop 0\n" \
  "buffer_load_dwordx4 v199, s[44:47], s51 offen lds\n" \
  "s_add_u32 m0, s52, 4096\n" \
  "s_nop 0\n" \
  "buffer_load_dwordx4 v200, s[44:47], s51 offen lds\n" \
  "s_add_u32 m0, s52, 5120\n" \
  "s_nop 0\n" \
  "buffer_load_dwordx4 v201, s[44:47], s51 offen lds\n" \
  "s_add_u32 m0, s52, 6144\n" \
  "s_nop 0\n" \
  "buffer_load_dwordx4 v202, s[44:47], s51 offen lds\n" \
  "s_add_u32 m0, s52, 7168\n" \
  "s_nop 0\n" \
  "buffer_load_dwordx4 v203, s[44:47], s51 offen lds\n" \
  "s_add_u32 m0, s52, 8192\n" \
  "s_nop 0\n" \
  "buffer_load_dwordx4 v204, s[44:47], s51 offen lds\n" \
  "s_add_u32 s51, s51, 36864\n" \
  "s_min_u32 s51, s51, 2322432\n" \
  "s_add_u32 s52, s52, 36864\n" \
  "s_cmp_ge_u32 s52, 163840\n" \
  "s_cselect_b32 s60, 147456, 0\n" \
  "s_sub_u32 s52, s52, s60\n" \
  "s_waitcnt vmcnt(21)\n" \
  "s_barrier\n" \
  "v_add_u32 v186, 65536, v182\n" \
  "ds_read_b64_tr_b16 a[24:25], v186\n" \
  "ds_read_b64_tr_b16 a[26:27], v186 offset:1024\n" \
  "ds_read_b64_tr_b16 a[28:29], v186 offset:8192\n" \
  "ds_read_b64_tr_b16 a[30:31], v186 offset:9216\n" \
  "v_add_u32 v187, 65536, v183\n" \
  "ds_read_b64_tr_b16 a[96:97], v187\n" \
  "ds_read_b64_tr_b16 a[98:99], v187 offset:1024\n" \
  "ds_read_b64_tr_b16 a[100:101], v187 offset:8192\n" \
  "ds_read_b64_tr_b16 a[102:103], v187 offset:9216\n" \
  "v_add_u32 v186, 81920, v182\n" \
  "ds_read_b64_tr_b16 a[32:33], v186\n" \
  "ds_read_b64_tr_b16 a[34:35], v186 offset:1024\n" \
  "ds_read_b64_tr_b16 a[36:37], v186 offset:8192\n" \
  "ds_read_b64_tr_b16 a[38:39], v186 offset:9216\n" \
  "v_add_u32 v187, 81920, v183\n" \
  "ds_read_b64_tr_b16 a[104:105], v187\n" \
  "ds_read_b64_tr_b16 a[106:107], v187 offset:1024\n" \
  "ds_read_b64_tr_b16 a[108:109], v187 offset:8192\n" \
  "ds_read_b64_tr_b16 a[110:111], v187 offset:9216\n" \
  "v_add_u32 v186, 98304, v182\n" \
  "ds_read_b64_tr_b16 a[40:41], v186\n" \
  "ds_read_b64_tr_b16 a[42:43], v186 offset:1024\n" \
  "ds_read_b64_tr_b16 a[44:45], v186 offset:8192\n" \
  "ds_read_b64_tr_b16 a[46:47], v186 offset:9216\n" \
  "v_add_u32 v187, 98304, v183\n" \
  "ds_read_b64_tr_b16 a[112:113], v187\n" \
  "ds_read_b64_tr_b16 a[114:115], v187 offset:1024\n" \
  "ds_read_b64_tr_b16 a[116:117], v187 offset:8192\n" \
  "ds_read_b64_tr_b16 a[118:119], v187 offset:9216\n" \
  "s_waitcnt lgkmcnt(0)\n" \
  "s_barrier\n" \
  "buffer_load_dwordx2 v[184:185], off, s[40:43], %41 sc1\n" \
  "s_add_u32 m0, s52, 0\n" \
  "s_nop 0\n" \
  "buffer_load_dwordx4 v196, s[44:47], s51 offen lds\n" \
  "s_add_u32 m0, s52, 1024\n" \
  "s_nop 0\n" \
  "buffer_load_dwordx4 v197, s[44:47], s51 offen lds\n" \
  "s_add_u32 m0, s52, 2048\n" \
  "s_nop 0\n" \
  "buffer_load_dwordx4 v198, s[44:47], s51 offen lds\n" \
  "s_add_u32 m0, s52, 3072\n" \
  "s_nop 0\n" \
  "buffer_load_dwordx4 v199, s[44:47], s51 offen lds\n" \
  "s_add_u32 m0, s52, 4096\n" \
  "s_nop 0\n" \
  "buffer_load_dwordx4 v200, s[44:47], s51 offen lds\n" \
  "s_add_u32 m0, s52, 5120\n" \
  "s_nop 0\n" \
  "buffer_load_dwordx4 v201, s[44:47], s51 offen lds\n" \
  "s_add_u32 m0, s52, 6144\n" \
  "s_nop 0\n" \
  "buffer_load_dwordx4 v202, s[44:47], s51 offen lds\n" \
  "s_add_u32 m0, s52, 7168\n" \
  "s_nop 0\n" \
  "buffer_load_dwordx4 v203, s[44:47], s51 offen lds\n" \
  "s_add_u32 m0, s52, 8192\n" \
  "s_nop 0\n" \
  "buffer_load_dwordx4 v204, s[44:47], s51 offen lds\n" \
  "s_add_u32 s51, s51, 36864\n" \
  "s_min_u32 s51, s51, 2322432\n" \
  "s_add_u32 s52, s52, 36864\n" \
  "s_cmp_ge_u32 s52, 163840\n" \
  "s_cselect_b32 s60, 147456, 0\n" \
  "s_sub_u32 s52, s52, s60\n" \
  "s_waitcnt vmcnt(19)\n" \
  "s_barrier\n" \
  "v_add_u32 v186, 114688, v182\n" \
  "ds_read_b64_tr_b16 a[48:49], v186\n" \
  "ds_read_b64_tr_b16 a[50:51], v186 offset:1024\n" \
  "ds_read_b64_tr_b16 a[52:53], v186 offset:8192\n" \
  "ds_read_b64_tr_b16 a[54:55], v186 offset:9216\n" \
  "v_add_u32 v187, 114688, v183\n" \
  "ds_read_b64_tr_b16 a[120:121], v187\n" \
  "ds_read_b64_tr_b16 a[122:123], v187 offset:1024\n" \
  "ds_read_b64_tr_b16 a[124:125], v187 offset:8192\n" \
  "ds_read_b64_tr_b16 a[126:127], v187 offset:9216\n" \
  "v_add_u32 v186, 131072, v182\n" \
  "ds_read_b64_tr_b16 a[56:57], v186\n" \
  "ds_read_b64_tr_b16 a[58:59], v186 offset:1024\n" \
  "ds_read_b64_tr_b16 a[60:61], v186 offset:8192\n" \
  "ds_read_b64_tr_b16 a[62:63], v186 offset:9216\n" \
  "v_add_u32 v187, 131072, v183\n" \
  "ds_read_b64_tr_b16 a[128:129], v187\n" \
  "ds_read_b64_tr_b16 a[130:131], v187 offset:1024\n" \
  "ds_read_b64_tr_b16 a[132:133], v187 offset:8192\n" \
  "ds_read_b64_tr_b16 a[134:135], v187 offset:9216\n" \
  "v_add_u32 v186, 147456, v182\n" \
  "ds_read_b64_tr_b16 a[64:65], v186\n" \
  "ds_read_b64_tr_b16 a[66:67], v186 offset:1024\n" \
  "ds_read_b64_tr_b16 a[68:69], v186 offset:8192\n" \
  "ds_read_b64_tr_b16 a[70:71], v186 offset:9216\n" \
  "v_add_u32 v187, 147456, v183\n" \
  "ds_read_b64_tr_b16 a[136:137], v187\n" \
  "ds_read_b64_tr_b16 a[138:139], v187 offset:1024\n" \
  "ds_read_b64_tr_b16 a[140:141], v187 offset:8192\n" \
  "ds_read_b64_tr_b16 a[142:143], v187 offset:9216\n" \
  "s_waitcnt lgkmcnt(0)\n" \
  "s_mov_b32 s69, 0\n" \
  "s_waitcnt vmcnt(9)\n" \
  "s_branch .LK_go1_%=\n" \
  ".LK_go_%=:\n" \
  "buffer_load_dwordx2 v[184:185], off, s[40:43], %41 sc1\n" \
  "s_waitcnt vmcnt(0)\n" \
  ".LK_go1_%=:\n" \
  "v_readfirstlane_b32 s96, v184\n" \
  "v_readfirstlane_b32 s63, v185\n" \
  "s_cmp_eq_u32 s63, s55\n" \
  "s_cselect_b32 s96, s96, 0\n" \
  "s_cmp_ge_u32 s96, 0x80000000\n" \
  "s_cbranch_scc1 .LK_go_x_%=\n" \
  "s_sleep 4\n" \
  "s_add_u32 s69, s69, 1\n" \
  "s_cmp_lt_u32 s69, 2097152\n" \
  "s_cbranch_scc1 .LK_go_%=\n" \
  "v_mov_b32 v184, 1\n" \
  "v_mov_b32 v185, s55\n" \
  "buffer_store_dwordx2 v[184:185], off, s[40:43], %42 offset:128 sc1\n" \
  ".LK_go_x_%=:\n" \
  "s_and_b32 s60, s96, 2\n" \
  "s_lshl_b32 s60, s60, 12\n" \
  "s_xor_b32 s53, %39, s60\n" \
  "s_mov_b32 s54, %40\n" \
  "s_andn2_b32 s60, 2, s96\n" \
  "s_lshl_b32 s60, s60, 12\n" \
  "s_add_u32 s49, %37, s60\n" \
  "s_add_u32 s50, %38, s60\n" \
  "s_xor_b32 s66, %38, s60\n" \
  "s_xor_b32 s59, %44, s60\n" \
  "s_or_b32 s50, s50, 0x40000000\n" \
  "s_mov_b32 s57, 2130706432\n" \
  "s_mov_b32 s48, 0\n" \
  "v_mov_b32 v169, s55\n" \
  "v_mov_b32 v167, 2130706432\n" \
  "v_mov_b32 v166, -1\n" \
  "s_mov_b64 exec, 3\n" \
  "v_mov_b32 v167, s54\n" \
  "s_mov_b64 exec, 1\n" \
  "v_mov_b32 v167, s53\n" \
  "v_mov_b32 v166, 0\n" \
  "s_mov_b64 exec, -1\n" \
  "s_barrier\n" \
  "ds_read_b128 v[32:35], v191\n" \
  "ds_read_b128 v[36:39], v191 offset:18432\n" \
  "ds_read_b128 v[40:43], v192\n" \
  "ds_read_b128 v[44:47], v192 offset:18432\n" \
  "ds_read_b128 v[48:51], v191 offset:128\n" \
  "ds_read_b128 v[52:55], v191 offset:18560\n" \
  "s_waitcnt lgkmcnt(0)\n" \
  "s_sub_u32 s49, s49, 524288\n" \
  "s_or_b32 s49, s49, 0x40000000\n" \
  "s_mov_b32 s56, -1\n" \
  "s_mov_b32 s57, 0x10000\n" \
  "s_mov_b32 s71, 36864\n" \
  ".LK_ma_%=:\n" \
  "v_mfma_f32_16x16x32_bf16 v[64:67], a[0:3], v[32:35], 0\n" \
  "ds_read_b128 v[56:59], v192 offset:128\n" \
  "v_mfma_f32_16x16x32_bf16 v[72:75], a[0:3], v[36:39], 0\n" \
  "ds_read_b128 v[60:63], v192 offset:18560\n" \
  "s_lshr_b32 s62, s49, 8\n" \
  "v_mfma_f32_16x16x32_bf16 v[68:71], a[72:75], v[32:35], 0\n" \
  "s_add_u32 m0, s52, 0\n" \
  "s_and_b32 s62, s62, 8192\n" \
  "s_xor_b32 s62, s62, s49\n" \
  "v_mfma_f32_16x16x32_bf16 v[76:79], a[72:75], v[36:39], 0\n" \
  "buffer_load_dwordx4 v196, s[44:47], s51 offen lds\n" \
  "v_mfma_f32_16x16x32_bf16 v[64:67], a[4:7], v[40:43], v[64:67]\n" \
  "ds_read_b128 v[32:35], v191 offset:256\n" \
  "v_cvt_pk_bf16_f32 v96, v80, v81\n" \
  "v_mfma_f32_16x16x32_bf16 v[72:75], a[4:7], v[44:47], v[72:75]\n" \
  "ds_read_b128 v[36:39], v191 offset:18688\n" \
  "v_cvt_pk_bf16_f32 v97, v82, v83\n" \
  "v_mfma_f32_16x16x32_bf16 v[68:71], a[76:79], v[40:43], v[68:71]\n" \
  "v_cvt_pk_bf16_f32 v98, v84, v85\n" \
  "v_cvt_pk_bf16_f32 v99, v86, v87\n" \
  "s_lshl_b32 s63, s57, 14\n" \
  "v_mfma_f32_16x16x32_bf16 v[76:79], a[76:79], v[44:47], v[76:79]\n" \
  "s_xor_b32 s63, s63, s59\n" \
  "v_mfma_f32_16x16x32_bf16 v[64:67], a[8:11], v[48:51], v[64:67]\n" \
  "ds_read_b128 v[40:43], v192 offset:256\n" \
  "v_mfma_f32_16x16x32_bf16 v[72:75], a[8:11], v[52:55], v[72:75]\n" \
  "ds_read_b128 v[44:47], v192 offset:18688\n" \
  "v_mfma_f32_16x16x32_bf16 v[68:71], a[80:83], v[48:51], v[68:71]\n" \
  "s_add_u32 m0, s52, 1024\n" \
  "v_mfma_f32_16x16x32_bf16 v[76:79], a[80:83], v[52:55], v[76:79]\n" \
  "buffer_load_dwordx4 v197, s[44:47], s51 offen lds\n" \
  "s_waitcnt lgkmcnt(5)\n" \
  "v_mfma_f32_16x16x32_bf16 v[64:67], a[12:15], v[56:59], v[64:67]\n" \
  "ds_read_b128 v[48:51], v191 offset:384\n" \
  "s_waitcnt lgkmcnt(5)\n" \
  "v_mfma_f32_16x16x32_bf16 v[72:75], a[12:15], v[60:63], v[72:75]\n" \
  "ds_read_b128 v[52:55], v191 offset:18816\n" \
  "v_mfma_f32_16x16x32_bf16 v[68:71], a[84:87], v[56:59], v[68:71]\n" \
  "buffer_store_dwordx4 v[96:99], v190, s[40:43], s62 offen sc1\n" \
  "v_mfma_f32_16x16x32_bf16 v[76:79], a[84:87], v[60:63], v[76:79]\n" \
  "buffer_store_dwordx4 v[96:99], v190, s[40:43], s63 offen sc1\n" \
  "s_waitcnt lgkmcnt(5)\n" \
  "v_mfma_f32_16x16x32_bf16 v[64:67], a[16:19], v[32:35], v[64:67]\n" \
  "ds_read_b128 v[56:59], v192 offset:384\n" \
  "s_waitcnt lgkmcnt(5)\n" \
  "v_cvt_pk_bf16_f32 v100, v88, v89\n" \
  "v_mfma_f32_16x16x32_bf16 v[72:75], a[16:19], v[36:39], v[72:75]\n" \
  "ds_read_b128 v[60:63], v192 offset:18816\n" \
  "v_cvt_pk_bf16_f32 v101, v90, v91\n" \
  "v_mfma_f32_16x16x32_bf16 v[68:71], a[88:91], v[32:35], v[68:71]\n" \
  "s_add_u32 m0, s52, 2048\n" \
  "v_cvt_pk_bf16_f32 v102, v92, v93\n" \
  "v_cvt_pk_bf16_f32 v103, v94, v95\n" \
  "v_mfma_f32_16x16x32_bf16 v[76:79], a[88:91], v[36:39], v[76:79]\n" \
  "buffer_load_dwordx4 v198, s[44:47], s51 offen lds\n" \
  "s_waitcnt lgkmcnt(5)\n" \
  "v_mfma_f32_16x16x32_bf16 v[64:67], a[20:23], v[40:43], v[64:67]\n" \
  "ds_read_b128 v[32:35], v191 offset:512\n" \
  "s_waitcnt lgkmcnt(5)\n" \
  "v_mfma_f32_16x16x32_bf16 v[72:75], a[20:23], v[44:47], v[72:75]\n" \
  "ds_read_b128 v[36:39], v191 offset:18944\n" \
  "v_mfma_f32_16x16x32_bf16 v[68:71], a[92:95], v[40:43], v[68:71]\n" \
  "buffer_store_dwordx4 v[100:103], v190, s[40:43], s62 offen offset:1024 sc1\n" \
  "v_mfma_f32_16x16x32_bf16 v[76:79], a[92:95], v[44:47], v[76:79]\n" \
  "buffer_store_dwordx4 v[100:103], v190, s[40:43], s63 offen offset:1024 sc1\n" \
  "s_waitcnt lgkmcnt(5)\n" \
  "v_mfma_f32_16x16x32_bf16 v[64:67], a[24:27], v[48:51], v[64:67]\n" \
  "ds_read_b128 v[40:43], v192 offset:512\n" \
  "s_add_u32 s49, s49, 524288\n" \
  "s_waitcnt lgkmcnt(5)\n" \
  "v_mfma_f32_16x16x32_bf16 v[72:75], a[24:27], v[52:55], v[72:75]\n" \
  "ds_read_b128 v[44:47], v192 offset:18944\n" \
  "s_bitset0_b32 s49, 30\n" \
  "v_mfma_f32_16x16x32_bf16 v[68:71], a[96:99], v[48:51], v[68:71]\n" \
  "s_add_u32 m0, s52, 3072\n" \
  "v_mfma_f32_16x16x32_bf16 v[76:79], a[96:99], v[52:55], v[76:79]\n" \
  "buffer_load_dwordx4 v199, s[44:47], s51 offen lds\n" \
  "s_waitcnt lgkmcnt(5)\n" \
  "v_mfma_f32_16x16x32_bf16 v[64:67], a[28:31], v[56:59], v[64:67]\n" \
  "ds_read_b128 v[48:51], v191 offset:640\n" \
  "s_waitcnt lgkmcnt(5)\n" \
  "v_mfma_f32_16x16x32_bf16 v[72:75], a[28:31], v[60:63], v[72:75]\n" \
  "ds_read_b128 v[52:55], v191 offset:19072\n" \
  "v_mfma_f32_16x16x32_bf16 v[68:71], a[100:103], v[56:59], v[68:71]\n" \
  "s_cmp_eq_u32 s48, 16\n" \
  "s_cselect_b32 s60, 22020096, 0\n" \
  "s_add_u32 s49, s49, s60\n" \
  "v_mfma_f32_16x16x32_bf16 v[76:79], a[100:103], v[60:63], v[76:79]\n" \
  "s_add_u32 s58, s48, 1\n" \
  "s_and_b32 s58, s58, 3\n" \
  "s_cselect_b32 s58, 0, 147456\n" \
  "s_waitcnt lgkmcnt(5)\n" \
  "v_mfma_f32_16x16x32_bf16 v[64:67], a[32:35], v[32:35], v[64:67]\n" \
  "ds_read_b128 v[56:59], v192 offset:640\n" \
  "s_sub_u32 s58, 36864, s58\n" \
  "s_waitcnt lgkmcnt(5)\n" \
  "v_mfma_f32_16x16x32_bf16 v[72:75], a[32:35], v[36:39], v[72:75]\n" \
  "ds_read_b128 v[60:63], v192 offset:19072\n" \
  "v_add_u32 v165, s58, v191\n" \
  "v_mfma_f32_16x16x32_bf16 v[68:71], a[104:107], v[32:35], v[68:71]\n" \
  "s_add_u32 m0, s52, 4096\n" \
  "v_add_u32 v164, s58, v192\n" \
  "v_mfma_f32_16x16x32_bf16 v[76:79], a[104:107], v[36:39], v[76:79]\n" \
  "buffer_load_dwordx4 v200, s[44:47], s51 offen lds\n" \
  "s_waitcnt lgkmcnt(5)\n" \
  "v_mfma_f32_16x16x32_bf16 v[64:67], a[36:39], v[40:43], v[64:67]\n" \
  "ds_read_b128 v[32:35], v191 offset:768\n" \
  "s_waitcnt lgkmcnt(5)\n" \
  "v_mfma_f32_16x16x32_bf16 v[72:75], a[36:39], v[44:47], v[72:75]\n" \
  "ds_read_b128 v[36:39], v191 offset:19200\n" \
  "v_mfma_f32_16x16x32_bf16 v[68:71], a[108:111], v[40:43], v[68:71]\n" \
  "v_mfma_f32_16x16x32_bf16 v[76:79], a[108:111], v[44:47], v[76:79]\n" \
  "s_waitcnt lgkmcnt(5)\n" \
  "v_mfma_f32_16x16x32_bf16 v[64:67], a[40:43], v[48:51], v[64:67]\n" \
  "ds_read_b128 v[40:43], v192 offset:768\n" \
  "s_waitcnt lgkmcnt(5)\n" \
  "v_mfma_f32_16x16x32_bf16 v[72:75], a[40:43], v[52:55], v[72:75]\n" \
  "ds_read_b128 v[44:47], v192 offset:19200\n" \
  "v_mfma_f32_16x16x32_bf16 v[68:71], a[112:115], v[48:51], v[68:71]\n" \
  "s_add_u32 m0, s52, 5120\n" \
  "v_mfma_f32_16x16x32_bf16 v[76:79], a[112:115], v[52:55], v[76:79]\n" \
  "buffer_load_dwordx4 v201, s[44:47], s51 offen lds\n" \
  "s_waitcnt lgkmcnt(5)\n" \
  "v_mfma_f32_16x16x32_bf16 v[64:67], a[44:47], v[56:59], v[64:67]\n" \
  "ds_read_b128 v[48:51], v191 offset:896\n" \
  "s_waitcnt lgkmcnt(5)\n" \
  "v_mfma_f32_16x16x32_bf16 v[72:75], a[44:47], v[60:63], v[72:75]\n" \
  "ds_read_b128 v[52:55], v191 offset:19328\n" \
  "v_mfma_f32_16x16x32_bf16 v[68:71], a[116:119], v[56:59], v[68:71]\n" \
  "v_mfma_f32_16x16x32_bf16 v[76:79], a[116:119], v[60:63], v[76:79]\n" \
  "s_waitcnt lgkmcnt(5)\n" \
  "v_mfma_f32_16x16x32_bf16 v[64:67], a[48:51], v[32:35], v[64:67]\n" \
  "ds_read_b128 v[56:59], v192 offset:896\n" \
  "s_waitcnt lgkmcnt(5)\n" \
  "v_mfma_f32_16x16x32_bf16 v[72:75], a[48:51], v[36:39], v[72:75]\n" \
  "ds_read_b128 v[60:63], v192 offset:19328\n" \
  "v_mfma_f32_16x16x32_bf16 v[68:71], a[120:123], v[32:35], v[68:71]\n" \
  "s_add_u32 m0, s52, 6144\n" \
  "v_mfma_f32_16x16x32_bf16 v[76:79], a[120:123], v[36:39], v[76:79]\n" \
  "buffer_load_dwordx4 v202, s[44:47], s51 offen lds\n" \
  "s_waitcnt lgkmcnt(5)\n" \
  "v_mfma_f32_16x16x32_bf16 v[64:67], a[52:55], v[40:43], v[64:67]\n" \
  "ds_read_b128 v[32:35], v191 offset:1024\n" \
  "s_waitcnt lgkmcnt(5)\n" \
  "v_mfma_f32_16x16x32_bf16 v[72:75], a[52:55], v[44:47], v[72:75]\n" \
  "ds_read_b128 v[36:39], v191 offset:19456\n" \
  "v_mfma_f32_16x16x32_bf16 v[68:71], a[124:127], v[40:43], v[68:71]\n" \
  "s_waitcnt vmcnt(11)\n" \
  "s_barrier\n" \
  "v_mfma_f32_16x16x32_bf16 v[76:79], a[124:127], v[44:47], v[76:79]\n" \
  "v_mov_b32 v168, s56\n" \
  "s_mov_b64 exec, 1\n" \
  "buffer_store_dwordx2 v[168:169], v167, s[40:43], 0 offen sc1\n" \
  "s_mov_b64 exec, -1\n" \
  "s_waitcnt lgkmcnt(5)\n" \
  "v_mfma_f32_16x16x32_bf16 v[64:67], a[56:59], v[48:51], v[64:67]\n" \
  "ds_read_b128 v[40:43], v192 offset:1024\n" \
  "s_waitcnt lgkmcnt(5)\n" \
  "v_mfma_f32_16x16x32_bf16 v[72:75], a[56:59], v[52:55], v[72:75]\n" \
  "ds_read_b128 v[44:47], v192 offset:19456\n" \
  "v_mfma_f32_16x16x32_bf16 v[68:71], a[128:131], v[48:51], v[68:71]\n" \
  "s_add_u32 m0, s52, 7168\n" \
  "v_mfma_f32_16x16x32_bf16 v[76:79], a[128:131], v[52:55], v[76:79]\n" \
  "buffer_load_dwordx4 v203, s[44:47], s51 offen lds\n" \
  "s_waitcnt lgkmcnt(5)\n" \
  "v_mfma_f32_16x16x32_bf16 v[64:67], a[60:63], v[56:59], v[64:67]\n" \
  "ds_read_b128 v[48:51], v165\n" \
  "s_waitcnt lgkmcnt(5)\n" \
  "v_mfma_f32_16x16x32_bf16 v[72:75], a[60:63], v[60:63], v[72:75]\n" \
  "ds_read_b128 v[52:55], v165 offset:18432\n" \
  "v_mfma_f32_16x16x32_bf16 v[68:71], a[132:135], v[56:59], v[68:71]\n" \
  "s_cmp_eq_u32 s51, 552960\n" \
  "s_cselect_b32 s70, 1585152, s71\n" \
  "v_mfma_f32_16x16x32_bf16 v[76:79], a[132:135], v[60:63], v[76:79]\n" \
  "s_waitcnt lgkmcnt(5)\n" \
  "v_mfma_f32_16x16x32_bf16 v[64:67], a[64:67], v[32:35], v[64:67]\n" \
  "ds_read_b128 v[56:59], v164\n" \
  "s_waitcnt lgkmcnt(5)\n" \
  "v_mfma_f32_16x16x32_bf16 v[72:75], a[64:67], v[36:39], v[72:75]\n" \
  "ds_read_b128 v[60:63], v164 offset:18432\n" \
  "v_mfma_f32_16x16x32_bf16 v[68:71], a[136:139], v[32:35], v[68:71]\n" \
  "s_add_u32 m0, s52, 8192\n" \
  "v_mfma_f32_16x16x32_bf16 v[76:79], a[136:139], v[36:39], v[76:79]\n" \
  "buffer_load_dwordx4 v204, s[44:47], s51 offen lds\n" \
  "s_waitcnt lgkmcnt(5)\n" \
  "v_mfma_f32_16x16x32_bf16 v[64:67], a[68:71], v[40:43], v[64:67]\n" \
  "ds_read_b128 v[32:35], v165 offset:128\n" \
  "s_add_u32 s51, s51, s70\n" \
  "s_waitcnt lgkmcnt(5)\n" \
  "v_mfma_f32_16x16x32_bf16 v[72:75], a[68:71], v[44:47], v[72:75]\n" \
  "ds_read_b128 v[36:39], v165 offset:18560\n" \
  "s_min_u32 s51, s51, 2322432\n" \
  "v_mfma_f32_16x16x32_bf16 v[68:71], a[140:143], v[40:43], v[68:71]\n" \
  "s_add_u32 s52, s52, 36864\n" \
  "v_mfma_f32_16x16x32_bf16 v[76:79], a[140:143], v[44:47], v[76:79]\n" \
  "s_cmp_ge_u32 s52, 163840\n" \
  "s_cselect_b32 s60, 147456, 0\n" \
  "s_sub_u32 s52, s52, s60\n" \
  "v_mov_b32 v191, v165\n" \
  "v_mov_b32 v192, v164\n" \
  "s_cmp_le_u32 s48, 3\n" \
  "s_cbranch_scc1 .LK_mea_%=\n" \
  ".LK_mea_b_%=:\n" \
  "s_min_u32 s56, s48, 16\n" \
  "s_cselect_b32 s57, 0, 42\n" \
  "s_add_u32 s57, s57, s48\n" \
  "s_add_u32 s48, s48, 1\n" \
  "s_waitcnt lgkmcnt(5)\n" \
  "v_mfma_f32_16x16x32_bf16 v[80:83], a[0:3], v[48:51], 0\n" \
  "ds_read_b128 v[40:43], v192 offset:128\n" \
  "s_waitcnt lgkmcnt(5)\n" \
  "v_mfma_f32_16x16x32_bf16 v[88:91], a[0:3], v[52:55], 0\n" \
  "ds_read_b128 v[44:47], v192 offset:18560\n" \
  "s_lshr_b32 s62, s49, 8\n" \
  "v_mfma_f32_16x16x32_bf16 v[84:87], a[72:75], v[48:51], 0\n" \
  "s_add_u32 m0, s52, 0\n" \
  "s_and_b32 s62, s62, 8192\n" \
  "s_xor_b32 s62, s62, s49\n" \
  "v_mfma_f32_16x16x32_bf16 v[92:95], a[72:75], v[52:55], 0\n" \
  "buffer_load_dwordx4 v196, s[44:47], s51 offen lds\n" \
  "s_waitcnt lgkmcnt(5)\n" \
  "v_mfma_f32_16x16x32_bf16 v[80:83], a[4:7], v[56:59], v[80:83]\n" \
  "ds_read_b128 v[48:51], v191 offset:256\n" \
  "s_waitcnt lgkmcnt(5)\n" \
  "v_cvt_pk_bf16_f32 v96, v64, v65\n" \
  "v_mfma_f32_16x16x32_bf16 v[88:91], a[4:7], v[60:63], v[88:91]\n" \
  "ds_read_b128 v[52:55], v191 offset:18688\n" \
  "v_cvt_pk_bf16_f32 v97, v66, v67\n" \
  "v_mfma_f32_16x16x32_bf16 v[84:87], a[76:79], v[56:59], v[84:87]\n" \
  "v_cvt_pk_bf16_f32 v98, v68, v69\n" \
  "v_cvt_pk_bf16_f32 v99, v70, v71\n" \
  "s_lshl_b32 s63, s57, 14\n" \
  "v_mfma_f32_16x16x32_bf16 v[92:95], a[76:79], v[60:63], v[92:95]\n" \
  "s_xor_b32 s63, s63, s59\n" \
  "s_waitcnt lgkmcnt(5)\n" \
  "v_mfma_f32_16x16x32_bf16 v[80:83], a[8:11], v[32:35], v[80:83]\n" \
  "ds_read_b128 v[56:59], v192 offset:256\n" \
  "s_waitcnt lgkmcnt(5)\n" \
  "v_mfma_f32_16x16x32_bf16 v[88:91], a[8:11], v[36:39], v[88:91]\n" \
  "ds_read_b128 v[60:63], v192 offset:18688\n" \
  "v_mfma_f32_16x16x32_bf16 v[84:87], a[80:83], v[32:35], v[84:87]\n" \
  "s_add_u32 m0, s52, 1024\n" \
  "v_mfma_f32_16x16x32_bf16 v[92:95], a[80:83], v[36:39], v[92:95]\n" \
  "buffer_load_dwordx4 v197, s[44:47], s51 offen lds\n" \
  "s_waitcnt lgkmcnt(5)\n" \
  "v_mfma_f32_16x16x32_bf16 v[80:83], a[12:15], v[40:43], v[80:83]\n" \
  "ds_read_b128 v[32:35], v191 offset:384\n" \
  "s_waitcnt lgkmcnt(5)\n" \
  "v_mfma_f32_16x16x32_bf16 v[88:91], a[12:15], v[44:47], v[88:91]\n" \
  "ds_read_b128 v[36:39], v191 offset:18816\n" \
  "v_mfma_f32_16x16x32_bf16 v[84:87], a[84:87], v[40:43], v[84:87]\n" \
  "buffer_store_dwordx4 v[96:99], v190, s[40:43], s62 offen sc1\n" \
  "v_mfma_f32_16x16x32_bf16 v[92:95], a[84:87], v[44:47], v[92:95]\n" \
  "buffer_store_dwordx4 v[96:99], v190, s[40:43], s63 offen sc1\n" \
  "s_waitcnt lgkmcnt(5)\n" \
  "v_mfma_f32_16x16x32_bf16 v[80:83], a[16:19], v[48:51], v[80:83]\n" \
  "ds_read_b128 v[40:43], v192 offset:384\n" \
  "s_waitcnt lgkmcnt(5)\n" \
  "v_cvt_pk_bf16_f32 v100, v72, v73\n" \
  "v_mfma_f32_16x16x32_bf16 v[88:91], a[16:19], v[52:55], v[88:91]\n" \
  "ds_read_b128 v[44:47], v192 offset:18816\n" \
  "v_cvt_pk_bf16_f32 v101, v74, v75\n" \
  "v_mfma_f32_16x16x32_bf16 v[84:87], a[88:91], v[48:51], v[84:87]\n" \
  "s_add_u32 m0, s52, 2048\n" \
  "v_cvt_pk_bf16_f32 v102, v76, v77\n" \
  "v_cvt_pk_bf16_f32 v103, v78, v79\n" \
  "v_mfma_f32_16x16x32_bf16 v[92:95], a[88:91], v[52:55], v[92:95]\n" \
  "buffer_load_dwordx4 v198, s[44:47], s51 offen lds\n" \
  "s_waitcnt lgkmcnt(5)\n" \
  "v_mfma_f32_16x16x32_bf16 v[80:83], a[20:23], v[56:59], v[80:83]\n" \
  "ds_read_b128 v[48:51], v191 offset:512\n" \
  "s_waitcnt lgkmcnt(5)\n" \
  "v_mfma_f32_16x16x32_bf16 v[88:91], a[20:23], v[60:63], v[88:91]\n" \
  "ds_read_b128 v[52:55], v191 offset:18944\n" \
  "v_mfma_f32_16x16x32_bf16 v[84:87], a[92:95], v[56:59], v[84:87]\n" \
  "buffer_store_dwordx4 v[100:103], v190, s[40:43], s62 offen offset:1024 sc1\n" \
  "v_mfma_f32_16x16x32_bf16 v[92:95], a[92:95], v[60:63], v[92:95]\n" \
  "buffer_store_dwordx4 v[100:103], v190, s[40:43], s63 offen offset:1024 sc1\n" \
  "s_waitcnt lgkmcnt(5)\n" \
  "v_mfma_f32_16x16x32_bf16 v[80:83], a[24:27], v[32:35], v[80:83]\n" \
  "ds_read_b128 v[56:59], v192 offset:512\n" \
  "s_add_u32 s49, s49, 524288\n" \
  "s_waitcnt lgkmcnt(5)\n" \
  "v_mfma_f32_16x16x32_bf16 v[88:91], a[24:27], v[36:39], v[88:91]\n" \
  "ds_read_b128 v[60:63], v192 offset:18944\n" \
  "s_bitset0_b32 s49, 30\n" \
  "v_mfma_f32_16x16x32_bf16 v[84:87], a[96:99], v[32:35], v[84:87]\n" \
  "s_add_u32 m0, s52, 3072\n" \
  "v_mfma_f32_16x16x32_bf16 v[92:95], a[96:99], v[36:39], v[92:95]\n" \
  "buffer_load_dwordx4 v199, s[44:47], s51 offen lds\n" \
  "s_waitcnt lgkmcnt(5)\n" \
  "v_mfma_f32_16x16x32_bf16 v[80:83], a[28:31], v[40:43], v[80:83]\n" \
  "ds_read_b128 v[32:35], v191 offset:640\n" \
  "s_waitcnt lgkmcnt(5)\n" \
  "v_mfma_f32_16x16x32_bf16 v[88:91], a[28:31], v[44:47], v[88:91]\n" \
  "ds_read_b128 v[36:39], v191 offset:19072\n" \
  "v_mfma_f32_16x16x32_bf16 v[84:87], a[100:103], v[40:43], v[84:87]\n" \
  "s_cmp_eq_u32 s48, 16\n" \
  "s_cselect_b32 s60, 22020096, 0\n" \
  "s_add_u32 s49, s49, s60\n" \
  "v_mfma_f32_16x16x32_bf16 v[92:95], a[100:103], v[44:47], v[92:95]\n" \
  "s_add_u32 s58, s48, 1\n" \
  "s_and_b32 s58, s58, 3\n" \
  "s_cselect_b32 s58, 0, 147456\n" \
  "s_waitcnt lgkmcnt(5)\n" \
  "v_mfma_f32_16x16x32_bf16 v[80:83], a[32:35], v[48:51], v[80:83]\n" \
  "ds_read_b128 v[40:43], v192 offset:640\n" \
  "s_sub_u32 s58, 36864, s58\n" \
  "s_waitcnt lgkmcnt(5)\n" \
  "v_mfma_f32_16x16x32_bf16 v[88:91], a[32:35], v[52:55], v[88:91]\n" \
  "ds_read_b128 v[44:47], v192 offset:19072\n" \
  "v_add_u32 v165, s58, v191\n" \
  "v_mfma_f32_16x16x32_bf16 v[84:87], a[104:107], v[48:51], v[84:87]\n" \
  "s_add_u32 m0, s52, 4096\n" \
  "v_add_u32 v164, s58, v192\n" \
  "v_mfma_f32_16x16x32_bf16 v[92:95], a[104:107], v[52:55], v[92:95]\n" \
  "buffer_load_dwordx4 v200, s[44:47], s51 offen lds\n" \
  "s_waitcnt lgkmcnt(5)\n" \
  "v_mfma_f32_16x16x32_bf16 v[80:83], a[36:39], v[56:59], v[80:83]\n" \
  "ds_read_b128 v[48:51], v191 offset:768\n" \
  "s_waitcnt lgkmcnt(5)\n" \
  "v_mfma_f32_16x16x32_bf16 v[88:91], a[36:39], v[60:63], v[88:91]\n" \
  "ds_read_b128 v[52:55], v191 offset:19200\n" \
  "v_mfma_f32_16x16x32_bf16 v[84:87], a[108:111], v[56:59], v[84:87]\n" \
  "v_mfma_f32_16x16x32_bf16 v[92:95], a[108:111], v[60:63], v[92:95]\n" \
  "s_waitcnt lgkmcnt(5)\n" \
  "v_mfma_f32_16x16x32_bf16 v[80:83], a[40:43], v[32:35], v[80:83]\n" \
  "ds_read_b128 v[56:59], v192 offset:768\n" \
  "s_waitcnt lgkmcnt(5)\n" \
  "v_mfma_f32_16x16x32_bf16 v[88:91], a[40:43], v[36:39], v[88:91]\n" \
  "ds_read_b128 v[60:63], v192 offset:19200\n" \
  "v_mfma_f32_16x16x32_bf16 v[84:87], a[112:115], v[32:35], v[84:87]\n" \
  "s_add_u32 m0, s52, 5120\n" \
  "v_mfma_f32_16x16x32_bf16 v[92:95], a[112:115], v[36:39], v[92:95]\n" \
  "buffer_load_dwordx4 v201, s[44:47], s51 offen lds\n" \
  "s_waitcnt lgkmcnt(5)\n" \
  "v_mfma_f32_16x16x32_bf16 v[80:83], a[44:47], v[40:43], v[80:83]\n" \
  "ds_read_b128 v[32:35], v191 offset:896\n" \
  "s_waitcnt lgkmcnt(5)\n" \
  "v_mfma_f32_16x16x32_bf16 v[88:91], a[44:47], v[44:47], v[88:91]\n" \
  "ds_read_b128 v[36:39], v191 offset:19328\n" \
  "v_mfma_f32_16x16x32_bf16 v[84:87], a[116:119], v[40:43], v[84:87]\n" \
  "v_mfma_f32_16x16x32_bf16 v[92:95], a[116:119], v[44:47], v[92:95]\n" \
  "s_waitcnt lgkmcnt(5)\n" \
  "v_mfma_f32_16x16x32_bf16 v[80:83], a[48:51], v[48:51], v[80:83]\n" \
  "ds_read_b128 v[40:43], v192 offset:896\n" \
  "s_waitcnt lgkmcnt(5)\n" \
  "v_mfma_f32_16x16x32_bf16 v[88:91], a[48:51], v[52:55], v[88:91]\n" \
  "ds_read_b128 v[44:47], v192 offset:19328\n" \
  "v_mfma_f32_16x16x32_bf16 v[84:87], a[120:123], v[48:51], v[84:87]\n" \
  "s_add_u32 m0, s52, 6144\n" \
  "v_mfma_f32_16x16x32_bf16 v[92:95], a[120:123], v[52:55], v[92:95]\n" \
  "buffer_load_dwordx4 v202, s[44:47], s51 offen lds\n" \
  "s_waitcnt lgkmcnt(5)\n" \
  "v_mfma_f32_16x16x32_bf16 v[80:83], a[52:55], v[56:59], v[80:83]\n" \
  "ds_read_b128 v[48:51], v191 offset:1024\n" \
  "s_waitcnt lgkmcnt(5)\n" \
  "v_mfma_f32_16x16x32_bf16 v[88:91], a[52:55], v[60:63], v[88:91]\n" \
  "ds_read_b128 v[52:55], v191 offset:19456\n" \
  "v_mfma_f32_16x16x32_bf16 v[84:87], a[124:127], v[56:59], v[84:87]\n" \
  "s_waitcnt vmcnt(11)\n" \
  "s_barrier\n" \
  "v_mfma_f32_16x16x32_bf16 v[92:95], a[124:127], v[60:63], v[92:95]\n" \
  "v_mov_b32 v168, s56\n" \
  "s_mov_b64 exec, 1\n" \
  "buffer_store_dwordx2 v[168:169], v167, s[40:43], 0 offen sc1\n" \
  "s_mov_b64 exec, -1\n" \
  "s_waitcnt lgkmcnt(5)\n" \
  "v_mfma_f32_16x16x32_bf16 v[80:83], a[56:59], v[32:35], v[80:83]\n" \
  "ds_read_b128 v[56:59], v192 offset:1024\n" \
  "s_waitcnt lgkmcnt(5)\n" \
  "v_mfma_f32_16x16x32_bf16 v[88:91], a[56:59], v[36:39], v[88:91]\n" \
  "ds_read_b128 v[60:63], v192 offset:19456\n" \
  "v_mfma_f32_16x16x32_bf16 v[84:87], a[128:131], v[32:35], v[84:87]\n" \
  "s_add_u32 m0, s52, 7168\n" \
  "v_mfma_f32_16x16x32_bf16 v[92:95], a[128:131], v[36:39], v[92:95]\n" \
  "buffer_load_dwordx4 v203, s[44:47], s51 offen lds\n" \
  "s_waitcnt lgkmcnt(5)\n" \
  "v_mfma_f32_16x16x32_bf16 v[80:83], a[60:63], v[40:43], v[80:83]\n" \
  "ds_read_b128 v[32:35], v165\n" \
  "s_waitcnt lgkmcnt(5)\n" \
  "v_mfma_f32_16x16x32_bf16 v[88:91], a[60:63], v[44:47], v[88:91]\n" \
  "ds_read_b128 v[36:39], v165 offset:18432\n" \
  "v_mfma_f32_16x16x32_bf16 v[84:87], a[132:135], v[40:43], v[84:87]\n" \
  "s_cmp_eq_u32 s51, 552960\n" \
  "s_cselect_b32 s70, 1585152, s71\n" \
  "v_mfma_f32_16x16x32_bf16 v[92:95], a[132:135], v[44:47], v[92:95]\n" \
  "s_waitcnt lgkmcnt(5)\n" \
  "v_mfma_f32_16x16x32_bf16 v[80:83], a[64:67], v[48:51], v[80:83]\n" \
  "ds_read_b128 v[40:43], v164\n" \
  "s_waitcnt lgkmcnt(5)\n" \
  "v_mfma_f32_16x16x32_bf16 v[88:91], a[64:67], v[52:55], v[88:91]\n" \
  "ds_read_b128 v[44:47], v164 offset:18432\n" \
  "v_mfma_f32_16x16x32_bf16 v[84:87], a[136:139], v[48:51], v[84:87]\n" \
  "s_add_u32 m0, s52, 8192\n" \
  "v_mfma_f32_16x16x32_bf16 v[92:95], a[136:139], v[52:55], v[92:95]\n" \
  "buffer_load_dwordx4 v204, s[44:47], s51 offen lds\n" \
  "s_waitcnt lgkmcnt(5)\n" \
  "v_mfma_f32_16x16x32_bf16 v[80:83], a[68:71], v[56:59], v[80:83]\n" \
  "ds_read_b128 v[48:51], v165 offset:128\n" \
  "s_add_u32 s51, s51, s70\n" \
  "s_waitcnt lgkmcnt(5)\n" \
  "v_mfma_f32_16x16x32_bf16 v[88:91], a[68:71], v[60:63], v[88:91]\n" \
  "ds_read_b128 v[52:55], v165 offset:18560\n" \
  "s_min_u32 s51, s51, 2322432\n" \
  "v_mfma_f32_16x16x32_bf16 v[84:87], a[140:143], v[56:59], v[84:87]\n" \
  "s_add_u32 s52, s52, 36864\n" \
  "v_mfma_f32_16x16x32_bf16 v[92:95], a[140:143], v[60:63], v[92:95]\n" \
  "s_cmp_ge_u32 s52, 163840\n" \
  "s_cselect_b32 s60, 147456, 0\n" \
  "s_sub_u32 s52, s52, s60\n" \
  "v_mov_b32 v191, v165\n" \
  "v_mov_b32 v192, v164\n" \
  "s_cmp_le_u32 s48, 3\n" \
  "s_cbranch_scc1 .LK_meb_%=\n" \
  ".LK_meb_b_%=:\n" \
  "s_min_u32 s56, s48, 16\n" \
  "s_cselect_b32 s57, 0, 42\n" \
  "s_add_u32 s57, s57, s48\n" \
  "s_add_u32 s48, s48, 1\n" \
  "s_cmp_lt_u32 s48, 22\n" \
  "s_waitcnt lgkmcnt(0)\n" \
  "s_cbranch_scc1 .LK_ma_%=\n" \
  "s_nop 7\n" \
  "s_lshr_b32 s62, s49, 8\n" \
  "s_and_b32 s62, s62, 8192\n" \
  "s_xor_b32 s62, s62, s49\n" \
  "v_cvt_pk_bf16_f32 v96, v80, v81\n" \
  "v_cvt_pk_bf16_f32 v97, v82, v83\n" \
  "v_cvt_pk_bf16_f32 v98, v84, v85\n" \
  "v_cvt_pk_bf16_f32 v99, v86, v87\n" \
  "s_lshl_b32 s63, s57, 14\n" \
  "s_xor_b32 s63, s63, s59\n" \
  "buffer_store_dwordx4 v[96:99], v190, s[40:43], s62 offen sc1\n" \
  "buffer_store_dwordx4 v[96:99], v190, s[40:43], s63 offen sc1\n" \
  "v_cvt_pk_bf16_f32 v100, v88, v89\n" \
  "v_cvt_pk_bf16_f32 v101, v90, v91\n" \
  "v_cvt_pk_bf16_f32 v102, v92, v93\n" \
  "v_cvt_pk_bf16_f32 v103, v94, v95\n" \
  "buffer_store_dwordx4 v[100:103], v190, s[40:43], s62 offen offset:1024 sc1\n" \
  "buffer_store_dwordx4 v[100:103], v190, s[40:43], s63 offen offset:1024 sc1\n" \
  "s_sub_u32 s49, s49, 24641536\n" \
  "s_mov_b32 s48, 16\n" \
  "s_waitcnt vmcnt(0)\n" \
  "s_barrier\n" \
  "v_mov_b32 v168, 1\n" \
  "s_mov_b64 exec, 2\n" \
  "buffer_store_dwordx2 v[168:169], v167, s[40:43], 0 offen sc1\n" \
  "s_mov_b64 exec, -1\n" \
  "v_subrev_u32 v191, 73728, v191\n" \
  "v_subrev_u32 v192, 73728, v192\n" \
  "s_waitcnt vmcnt(0)\n" \
  "s_cmp_eq_u32 %43, 77\n" \
  "s_cbranch_scc1 .LK_end_%=\n" \
  "s_mov_b64 s[44:45], %32\n" \
  "s_and_b32 s45, s45, 0xffff\n" \
  "s_mov_b32 s46, 4718592\n" \
  "s_add_u32 s60, %35, 0\n" \
  "s_add_u32 m0, s72, 16384\n" \
  "s_nop 0\n" \
  "buffer_load_dwordx4 v170, s[44:47], s60 offen lds\n" \
  "buffer_load_dwordx4 v171, s[44:47], s60 offen offset:1024 lds\n" \
  "buffer_load_dwordx4 v172, s[44:47], s60 offen offset:2048 lds\n" \
  "buffer_load_dwordx4 v173, s[44:47], s60 offen offset:3072 lds\n" \
  "s_add_u32 s60, %35, 524288\n" \
  "s_add_u32 m0, s72, 32768\n" \
  "s_nop 0\n" \
  "buffer_load_dwordx4 v170, s[44:47], s60 offen lds\n" \
  "buffer_load_dwordx4 v171, s[44:47], s60 offen offset:1024 lds\n" \
  "buffer_load_dwordx4 v172, s[44:47], s60 offen offset:2048 lds\n" \
  "buffer_load_dwordx4 v173, s[44:47], s60 offen offset:3072 lds\n" \
  "s_add_u32 s60, %35, 1048576\n" \
  "s_add_u32 m0, s72, 49152\n" \
  "s_nop 0\n" \
  "buffer_load_dwordx4 v170, s[44:47], s60 offen lds\n" \
  "buffer_load_dwordx4 v171, s[44:47], s60 offen offset:1024 lds\n" \
  "buffer_load_dwordx4 v172, s[44:47], s60 offen offset:2048 lds\n" \
  "buffer_load_dwordx4 v173, s[44:47], s60 offen offset:3072 lds\n" \
  "s_add_u32 s60, %35, 1572864\n" \
  "s_add_u32 m0, s72, 65536\n" \
  "s_nop 0\n" \
  "buffer_load_dwordx4 v170, s[44:47], s60 offen lds\n" \
  "buffer_load_dwordx4 v171, s[44:47], s60 offen offset:1024 lds\n" \
  "buffer_load_dwordx4 v172, s[44:47], s60 offen offset:2048 lds\n" \
  "buffer_load_dwordx4 v173, s[44:47], s60 offen offset:3072 lds\n" \
  "s_add_u32 s60, %35, 2097152\n" \
  "s_add_u32 m0, s72, 81920\n" \
  "s_nop 0\n" \
  "buffer_load_dwordx4 v170, s[44:47], s60 offen lds\n" \
  "buffer_load_dwordx4 v171, s[44:47], s60 offen offset:1024 lds\n" \
  "buffer_load_dwordx4 v172, s[44:47], s60 offen offset:2048 lds\n" \
  "buffer_load_dwordx4 v173, s[44:47], s60 offen offset:3072 lds\n" \
  "s_add_u32 s60, %35, 2621440\n" \
  "s_add_u32 m0, s72, 98304\n" \
  "s_nop 0\n" \
  "buffer_load_dwordx4 v170, s[44:47], s60 offen lds\n" \
  "buffer_load_dwordx4 v171, s[44:47], s60 offen offset:1024 lds\n" \
  "buffer_load_dwordx4 v172, s[44:47], s60 offen offset:2048 lds\n" \
  "buffer_load_dwordx4 v173, s[44:47], s60 offen offset:3072 lds\n" \
  "s_add_u32 s60, %35, 3145728\n" \
  "s_add_u32 m0, s72, 114688\n" \
  "s_nop 0\n" \
  "buffer_load_dwordx4 v170, s[44:47], s60 offen lds\n" \
  "buffer_load_dwordx4 v171, s[44:47], s60 offen offset:1024 lds\n" \
  "buffer_load_dwordx4 v172, s[44:47], s60 offen offset:2048 lds\n" \
  "buffer_load_dwordx4 v173, s[44:47], s60 offen offset:3072 lds\n" \
  "s_add_u32 s60, %35, 3670016\n" \
  "s_add_u32 m0, s72, 131072\n" \
  "s_nop 0\n" \
  "buffer_load_dwordx4 v170, s[44:47], s60 offen lds\n" \
  "buffer_load_dwordx4 v171, s[44:47], s60 offen offset:1024 lds\n" \
  "buffer_load_dwordx4 v172, s[44:47], s60 offen offset:2048 lds\n" \
  "buffer_load_dwordx4 v173, s[44:47], s60 offen offset:3072 lds\n" \
  "s_add_u32 s60, %35, 4194304\n" \
  "s_add_u32 m0, s72, 147456\n" \
  "s_nop 0\n" \
  "buffer_load_dwordx4 v170, s[44:47], s60 offen lds\n" \
  "buffer_load_dwordx4 v171, s[44:47], s60 offen offset:1024 lds\n" \
  "buffer_load_dwordx4 v172, s[44:47], s60 offen offset:2048 lds\n" \
  "buffer_load_dwordx4 v173, s[44:47], s60 offen offset:3072 lds\n" \
  "s_waitcnt vmcnt(24)\n" \
  "s_barrier\n" \
  "v_add_u32 v186, 16384, v182\n" \
  "ds_read_b64_tr_b16 a[144:145], v186\n" \
  "ds_read_b64_tr_b16 a[146:147], v186 offset:1024\n" \
  "ds_read_b64_tr_b16 a[148:149], v186 offset:8192\n" \
  "ds_read_b64_tr_b16 a[150:151], v186 offset:9216\n" \
  "v_add_u32 v187, 16384, v183\n" \
  "ds_read_b64_tr_b16 a[216:217], v187\n" \
  "ds_read_b64_tr_b16 a[218:219], v187 offset:1024\n" \
  "ds_read_b64_tr_b16 a[220:221], v187 offset:8192\n" \
  "ds_read_b64_tr_b16 a[222:223], v187 offset:9216\n" \
  "v_add_u32 v186, 32768, v182\n" \
  "ds_read_b64_tr_b16 a[152:153], v186\n" \
  "ds_read_b64_tr_b16 a[154:155], v186 offset:1024\n" \
  "ds_read_b64_tr_b16 a[156:157], v186 offset:8192\n" \
  "ds_read_b64_tr_b16 a[158:159], v186 offset:9216\n" \
  "v_add_u32 v187, 32768, v183\n" \
  "ds_read_b64_tr_b16 a[224:225], v187\n" \
  "ds_read_b64_tr_b16 a[226:227], v187 offset:1024\n" \
  "ds_read_b64_tr_b16 a[228:229], v187 offset:8192\n" \
  "ds_read_b64_tr_b16 a[230:231], v187 offset:9216\n" \
  "v_add_u32 v186, 49152, v182\n" \
  "ds_read_b64_tr_b16 a[160:161], v186\n" \
  "ds_read_b64_tr_b16 a[162:163], v186 offset:1024\n" \
  "ds_read_b64_tr_b16 a[164:165], v186 offset:8192\n" \
  "ds_read_b64_tr_b16 a[166:167], v186 offset:9216\n" \
  "v_add_u32 v187, 49152, v183\n" \
  "ds_read_b64_tr_b16 a[232:233], v187\n" \
  "ds_read_b64_tr_b16 a[234:235], v187 offset:1024\n" \
  "ds_read_b64_tr_b16 a[236:237], v187 offset:8192\n" \
  "ds_read_b64_tr_b16 a[238:239], v187 offset:9216\n" \
  "s_waitcnt lgkmcnt(0)\n" \
  "s_barrier\n" \
  "s_mov_b64 s[44:45], %33\n" \
  "s_and_b32 s45, s45, 0xffff\n" \
  "s_mov_b32 s46, 2359296\n" \
  "s_mov_b32 s51, 589824\n" \
  "s_mul_i32 s52, %34, 9216\n" \
  "s_add_u32 s52, s52, 16384\n" \
  "s_add_u32 m0, s52, 0\n" \
  "s_nop 0\n" \
  "buffer_load_dwordx4 v196, s[44:47], s51 offen lds\n" \
  "s_add_u32 m0, s52, 1024\n" \
  "s_nop 0\n" \
  "buffer_load_dwordx4 v197, s[44:47], s51 offen lds\n" \
  "s_add_u32 m0, s52, 2048\n" \
  "s_nop 0\n" \
  "buffer_load_dwordx4 v198, s[44:47], s51 offen lds\n" \
  "s_add_u32 m0, s52, 3072\n" \
  "s_nop 0\n" \
  "buffer_load_dwordx4 v199, s[44:47], s51 offen lds\n" \
  "s_add_u32 m0, s52, 4096\n" \
  "s_nop 0\n" \
  "buffer_load_dwordx4 v200, s[44:47], s51 offen lds\n" \
  "s_add_u32 m0, s52, 5120\n" \
  "s_nop 0\n" \
  "buffer_load_dwordx4 v201, s[44:47], s51 offen lds\n" \
  "s_add_u32 m0, s52, 6144\n" \
  "s_nop 0\n" \
  "buffer_load_dwordx4 v202, s[44:47], s51 offen lds\n" \
  "s_add_u32 m0, s52, 7168\n" \
  "s_nop 0\n" \
  "buffer_load_dwordx4 v203, s[44:47], s51 offen lds\n" \
  "s_add_u32 m0, s52, 8192\n" \
  "s_nop 0\n" \
  "buffer_load_dwordx4 v204, s[44:47], s51 offen lds\n" \
  "s_add_u32 s51, s51, 36864\n" \
  "s_min_u32 s51, s51, 2322432\n" \
  "s_add_u32 s52, s52, 36864\n" \
  "s_cmp_ge_u32 s52, 163840\n" \
  "s_cselect_b32 s60, 147456, 0\n" \
  "s_sub_u32 s52, s52, s60\n" \
  "s_waitcnt vmcnt(21)\n" \
  "s_barrier\n" \
  "v_add_u32 v186, 65536, v182\n" \
  "ds_read_b64_tr_b16 a[168:169], v186\n" \
  "ds_read_b64_tr_b16 a[170:171], v186 offset:1024\n" \
  "ds_read_b64_tr_b16 a[172:173], v186 offset:8192\n" \
  "ds_read_b64_tr_b16 a[174:175], v186 offset:9216\n" \
  "v_add_u32 v187, 65536, v183\n" \
  "ds_read_b64_tr_b16 a[240:241], v187\n" \
  "ds_read_b64_tr_b16 a[242:243], v187 offset:1024\n" \
  "ds_read_b64_tr_b16 a[244:245], v187 offset:8192\n" \
  "ds_read_b64_tr_b16 a[246:247], v187 offset:9216\n" \
  "v_add_u32 v186, 81920, v182\n" \
  "ds_read_b64_tr_b16 a[176:177], v186\n" \
  "ds_read_b64_tr_b16 a[178:179], v186 offset:1024\n" \
  "ds_read_b64_tr_b16 a[180:181], v186 offset:8192\n" \
  "ds_read_b64_tr_b16 a[182:183], v186 offset:9216\n" \
  "v_add_u32 v187, 81920, v183\n" \
  "ds_read_b64_tr_b16 a[248:249], v187\n" \
  "ds_read_b64_tr_b16 a[250:251], v187 offset:1024\n" \
  "ds_read_b64_tr_b16 a[252:253], v187 offset:8192\n" \
  "ds_read_b64_tr_b16 a[254:255], v187 offset:9216\n" \
  "v_add_u32 v186, 98304, v182\n" \
  "ds_read_b64_tr_b16 a[184:185], v186\n" \
  "ds_read_b64_tr_b16 a[186:187], v186 offset:1024\n" \
  "ds_read_b64_tr_b16 a[188:189], v186 offset:8192\n" \
  "ds_read_b64_tr_b16 a[190:191], v186 offset:9216\n" \
  "v_add_u32 v187, 98304, v183\n" \
  "ds_read_b64_tr_b16 v[0:1], v187\n" \
  "ds_read_b64_tr_b16 v[2:3], v187 offset:1024\n" \
  "ds_read_b64_tr_b16 v[4:5], v187 offset:8192\n" \
  "ds_read_b64_tr_b16 v[6:7], v187 offset:9216\n" \
  "s_waitcnt lgkmcnt(0)\n" \
  "s_barrier\n" \
  "s_add_u32 m0, s52, 0\n" \
  "s_nop 0\n" \
  "buffer_load_dwordx4 v196, s[44:47], s51 offen lds\n" \
  "s_add_u32 m0, s52, 1024\n" \
  "s_nop 0\n" \
  "buffer_load_dwordx4 v197, s[44:47], s51 offen lds\n" \
  "s_add_u32 m0, s52, 2048\n" \
  "s_nop 0\n" \
  "buffer_load_dwordx4 v198, s[44:47], s51 offen lds\n" \
  "s_add_u32 m0, s52, 3072\n" \
  "s_nop 0\n" \
  "buffer_load_dwordx4 v199, s[44:47], s51 offen lds\n" \
  "s_add_u32 m0, s52, 4096\n" \
  "s_nop 0\n" \
  "buffer_load_dwordx4 v200, s[44:47], s51 offen lds\n" \
  "s_add_u32 m0, s52, 5120\n" \
  "s_nop 0\n" \
  "buffer_load_dwordx4 v201, s[44:47], s51 offen lds\n" \
  "s_add_u32 m0, s52, 6144\n" \
  "s_nop 0\n" \
  "buffer_load_dwordx4 v202, s[44:47], s51 offen lds\n" \
  "s_add_u32 m0, s52, 7168\n" \
  "s_nop 0\n" \
  "buffer_load_dwordx4 v203, s[44:47], s51 offen lds\n" \
  "s_add_u32 m0, s52, 8192\n" \
  "s_nop 0\n" \
  "buffer_load_dwordx4 v204, s[44:47], s51 offen lds\n" \
  "s_add_u32 s51, s51, 36864\n" \
  "s_min_u32 s51, s51, 2322432\n" \
  "s_add_u32 s52, s52, 36864\n" \
  "s_cmp_ge_u32 s52, 163840\n" \
  "s_cselect_b32 s60, 147456, 0\n" \
  "s_sub_u32 s52, s52, s60\n" \
  "s_waitcnt vmcnt(18)\n" \
  "s_barrier\n" \
  "v_add_u32 v186, 114688, v182\n" \
  "ds_read_b64_tr_b16 a[192:193], v186\n" \
  "ds_read_b64_tr_b16 a[194:195], v186 offset:1024\n" \
  "ds_read_b64_tr_b16 a[196:197], v186 offset:8192\n" \
  "ds_read_b64_tr_b16 a[198:199], v186 offset:9216\n" \
  "v_add_u32 v187, 114688, v183\n" \
  "ds_read_b64_tr_b16 v[8:9], v187\n" \
  "ds_read_b64_tr_b16 v[10:11], v187 offset:1024\n" \
  "ds_read_b64_tr_b16 v[12:13], v187 offset:8192\n" \
  "ds_read_b64_tr_b16 v[14:15], v187 offset:9216\n" \
  "v_add_u32 v186, 131072, v182\n" \
  "ds_read_b64_tr_b16 a[200:201], v186\n" \
  "ds_read_b64_tr_b16 a[202:203], v186 offset:1024\n" \
  "ds_read_b64_tr_b16 a[204:205], v186 offset:8192\n" \
  "ds_read_b64_tr_b16 a[206:207], v186 offset:9216\n" \
  "v_add_u32 v187, 131072, v183\n" \
  "ds_read_b64_tr_b16 v[16:17], v187\n" \
  "ds_read_b64_tr_b16 v[18:19], v187 offset:1024\n" \
  "ds_read_b64_tr_b16 v[20:21], v187 offset:8192\n" \
  "ds_read_b64_tr_b16 v[22:23], v187 offset:9216\n" \
  "v_add_u32 v186, 147456, v182\n" \
  "ds_read_b64_tr_b16 a[208:209], v186\n" \
  "ds_read_b64_tr_b16 a[210:211], v186 offset:1024\n" \
  "ds_read_b64_tr_b16 a[212:213], v186 offset:8192\n" \
  "ds_read_b64_tr_b16 a[214:215], v186 offset:9216\n" \
  "v_add_u32 v187, 147456, v183\n" \
  "ds_read_b64_tr_b16 v[24:25], v187\n" \
  "ds_read_b64_tr_b16 v[26:27], v187 offset:1024\n" \
  "ds_read_b64_tr_b16 v[28:29], v187 offset:8192\n" \
  "ds_read_b64_tr_b16 v[30:31], v187 offset:9216\n" \
  "s_waitcnt lgkmcnt(0)\n" \
  "s_waitcnt vmcnt(9)\n" \
  "s_barrier\n" \
  "ds_read_b128 v[32:35], v191\n" \
  "ds_read_b128 v[36:39], v191 offset:18432\n" \
  "ds_read_b128 v[40:43], v192\n" \
  "ds_read_b128 v[44:47], v192 offset:18432\n" \
  "ds_read_b128 v[48:51], v191 offset:128\n" \
  "ds_read_b128 v[52:55], v191 offset:18560\n" \
  "s_waitcnt lgkmcnt(0)\n" \
  ".LK_loop_%=:\n" \
  "v_mfma_f32_16x16x32_bf16 v[64:67], a[0:3], v[32:35], 0\n" \
  "ds_read_b128 v[56:59], v192 offset:128\n" \
  "v_mfma_f32_16x16x32_bf16 v[72:75], a[0:3], v[36:39], 0\n" \
  "ds_read_b128 v[60:63], v192 offset:18560\n" \
  "s_lshr_b32 s62, s50, 8\n" \
  "v_mfma_f32_16x16x32_bf16 v[68:71], a[72:75], v[32:35], 0\n" \
  "s_and_b32 s62, s62, 8192\n" \
  "s_xor_b32 s62, s62, s50\n" \
  "v_cvt_pk_bf16_f32 v96, v80, v81\n" \
  "v_mfma_f32_16x16x32_bf16 v[76:79], a[72:75], v[36:39], 0\n" \
  "v_cvt_pk_bf16_f32 v97, v82, v83\n" \
  "v_cvt_pk_bf16_f32 v98, v84, v85\n" \
  "v_mfma_f32_16x16x32_bf16 v[64:67], a[4:7], v[40:43], v[64:67]\n" \
  "ds_read_b128 v[32:35], v191 offset:256\n" \
  "v_cvt_pk_bf16_f32 v99, v86, v87\n" \
  "v_mfma_f32_16x16x32_bf16 v[72:75], a[4:7], v[44:47], v[72:75]\n" \
  "ds_read_b128 v[36:39], v191 offset:18688\n" \
  "v_mfma_f32_16x16x32_bf16 v[68:71], a[76:79], v[40:43], v[68:71]\n" \
  "buffer_store_dwordx4 v[96:99], v190, s[40:43], s62 offen sc1\n" \
  "v_mfma_f32_16x16x32_bf16 v[76:79], a[76:79], v[44:47], v[76:79]\n" \
  "v_cvt_pk_bf16_f32 v100, v88, v89\n" \
  "v_cvt_pk_bf16_f32 v101, v90, v91\n" \
  "v_mfma_f32_16x16x32_bf16 v[64:67], a[8:11], v[48:51], v[64:67]\n" \
  "ds_read_b128 v[40:43], v192 offset:256\n" \
  "v_cvt_pk_bf16_f32 v102, v92, v93\n" \
  "v_mfma_f32_16x16x32_bf16 v[72:75], a[8:11], v[52:55], v[72:75]\n" \
  "ds_read_b128 v[44:47], v192 offset:18688\n" \
  "v_cvt_pk_bf16_f32 v103, v94, v95\n" \
  "v_mfma_f32_16x16x32_bf16 v[68:71], a[80:83], v[48:51], v[68:71]\n" \
  "buffer_store_dwordx4 v[100:103], v190, s[40:43], s62 offen offset:1024 sc1\n" \
  "v_mfma_f32_16x16x32_bf16 v[76:79], a[80:83], v[52:55], v[76:79]\n" \
  "s_add_u32 s50, s50, 524288\n" \
  "s_bitset0_b32 s50, 30\n" \
  "s_waitcnt lgkmcnt(5)\n" \
  "v_mfma_f32_16x16x32_bf16 v[64:67], a[12:15], v[56:59], v[64:67]\n" \
  "ds_read_b128 v[48:51], v191 offset:384\n" \
  "s_waitcnt lgkmcnt(5)\n" \
  "v_mfma_f32_16x16x32_bf16 v[72:75], a[12:15], v[60:63], v[72:75]\n" \
  "ds_read_b128 v[52:55], v191 offset:18816\n" \
  "v_mfma_f32_16x16x32_bf16 v[68:71], a[84:87], v[56:59], v[68:71]\n" \
  "v_mfma_f32_16x16x32_bf16 v[76:79], a[84:87], v[60:63], v[76:79]\n" \
  "s_waitcnt lgkmcnt(5)\n" \
  "v_mfma_f32_16x16x32_bf16 v[64:67], a[16:19], v[32:35], v[64:67]\n" \
  "ds_read_b128 v[56:59], v192 offset:384\n" \
  "s_waitcnt lgkmcnt(5)\n" \
  "v_mfma_f32_16x16x32_bf16 v[72:75], a[16:19], v[36:39], v[72:75]\n" \
  "ds_read_b128 v[60:63], v192 offset:18816\n" \
  "v_mfma_f32_16x16x32_bf16 v[68:71], a[88:91], v[32:35], v[68:71]\n" \
  "v_mfma_f32_16x16x32_bf16 v[76:79], a[88:91], v[36:39], v[76:79]\n" \
  "s_waitcnt lgkmcnt(5)\n" \
  "v_mfma_f32_16x16x32_bf16 v[64:67], a[20:23], v[40:43], v[64:67]\n" \
  "ds_read_b128 v[32:35], v191 offset:512\n" \
  "s_waitcnt lgkmcnt(5)\n" \
  "v_mfma_f32_16x16x32_bf16 v[72:75], a[20:23], v[44:47], v[72:75]\n" \
  "ds_read_b128 v[36:39], v191 offset:18944\n" \
  "v_mfma_f32_16x16x32_bf16 v[68:71], a[92:95], v[40:43], v[68:71]\n" \
  "v_mfma_f32_16x16x32_bf16 v[76:79], a[92:95], v[44:47], v[76:79]\n" \
  "s_waitcnt lgkmcnt(5)\n" \
  "v_mfma_f32_16x16x32_bf16 v[64:67], a[24:27], v[48:51], v[64:67]\n" \
  "ds_read_b128 v[40:43], v192 offset:512\n" \
  "s_waitcnt lgkmcnt(5)\n" \
  "v_mfma_f32_16x16x32_bf16 v[72:75], a[24:27], v[52:55], v[72:75]\n" \
  "ds_read_b128 v[44:47], v192 offset:18944\n" \
  "v_mfma_f32_16x16x32_bf16 v[68:71], a[96:99], v[48:51], v[68:71]\n" \
  "v_mfma_f32_16x16x32_bf16 v[76:79], a[96:99], v[52:55], v[76:79]\n" \
  "s_waitcnt lgkmcnt(5)\n" \
  "v_mfma_f32_16x16x32_bf16 v[64:67], a[28:31], v[56:59], v[64:67]\n" \
  "ds_read_b128 v[48:51], v191 offset:640\n" \
  "s_waitcnt lgkmcnt(5)\n" \
  "v_mfma_f32_16x16x32_bf16 v[72:75], a[28:31], v[60:63], v[72:75]\n" \
  "ds_read_b128 v[52:55], v191 offset:19072\n" \
  "v_mfma_f32_16x16x32_bf16 v[68:71], a[100:103], v[56:59], v[68:71]\n" \
  "s_add_u32 m0, s52, 0\n" \
  "v_mfma_f32_16x16x32_bf16 v[76:79], a[100:103], v[60:63], v[76:79]\n" \
  "buffer_load_dwordx4 v196, s[44:47], s51 offen lds\n" \
  "s_waitcnt lgkmcnt(5)\n" \
  "v_mfma_f32_16x16x32_bf16 v[64:67], a[32:35], v[32:35], v[64:67]\n" \
  "ds_read_b128 v[56:59], v192 offset:640\n" \
  "s_waitcnt lgkmcnt(5)\n" \
  "v_mfma_f32_16x16x32_bf16 v[72:75], a[32:35], v[36:39], v[72:75]\n" \
  "ds_read_b128 v[60:63], v192 offset:19072\n" \
  "v_mfma_f32_16x16x32_bf16 v[68:71], a[104:107], v[32:35], v[68:71]\n" \
  "v_mfma_f32_16x16x32_bf16 v[76:79], a[104:107], v[36:39], v[76:79]\n" \
  "s_waitcnt lgkmcnt(5)\n" \
  "v_mfma_f32_16x16x32_bf16 v[64:67], a[36:39], v[40:43], v[64:67]\n" \
  "ds_read_b128 v[32:35], v191 offset:768\n" \
  "s_waitcnt lgkmcnt(5)\n" \
  "v_mfma_f32_16x16x32_bf16 v[72:75], a[36:39], v[44:47], v[72:75]\n" \
  "ds_read_b128 v[36:39], v191 offset:19200\n" \
  "v_mfma_f32_16x16x32_bf16 v[68:71], a[108:111], v[40:43], v[68:71]\n" \
  "s_add_u32 m0, s52, 1024\n" \
  "v_mfma_f32_16x16x32_bf16 v[76:79], a[108:111], v[44:47], v[76:79]\n" \
  "buffer_load_dwordx4 v197, s[44:47], s51 offen lds\n" \
  "s_waitcnt lgkmcnt(5)\n" \
  "v_mfma_f32_16x16x32_bf16 v[64:67], a[40:43], v[48:51], v[64:67]\n" \
  "ds_read_b128 v[40:43], v192 offset:768\n" \
  "s_waitcnt lgkmcnt(5)\n" \
  "v_mfma_f32_16x16x32_bf16 v[72:75], a[40:43], v[52:55], v[72:75]\n" \
  "ds_read_b128 v[44:47], v192 offset:19200\n" \
  "v_mfma_f32_16x16x32_bf16 v[68:71], a[112:115], v[48:51], v[68:71]\n" \
  "v_mfma_f32_16x16x32_bf16 v[76:79], a[112:115], v[52:55], v[76:79]\n" \
  "s_waitcnt lgkmcnt(5)\n" \
  "v_mfma_f32_16x16x32_bf16 v[64:67], a[44:47], v[56:59], v[64:67]\n" \
  "ds_read_b128 v[48:51], v191 offset:896\n" \
  "s_waitcnt lgkmcnt(5)\n" \
  "v_mfma_f32_16x16x32_bf16 v[72:75], a[44:47], v[60:63], v[72:75]\n" \
  "ds_read_b128 v[52:55], v191 offset:19328\n" \
  "v_mfma_f32_16x16x32_bf16 v[68:71], a[116:119], v[56:59], v[68:71]\n" \
  "s_add_u32 m0, s52, 2048\n" \
  "v_add_u32 v168, s48, v166\n" \
  "v_mfma_f32_16x16x32_bf16 v[76:79], a[116:119], v[60:63], v[76:79]\n" \
  "buffer_load_dwordx4 v198, s[44:47], s51 offen lds\n" \
  "s_waitcnt lgkmcnt(5)\n" \
  "v_mfma_f32_16x16x32_bf16 v[64:67], a[48:51], v[32:35], v[64:67]\n" \
  "ds_read_b128 v[56:59], v192 offset:896\n" \
  "s_waitcnt lgkmcnt(5)\n" \
  "v_mfma_f32_16x16x32_bf16 v[72:75], a[48:51], v[36:39], v[72:75]\n" \
  "ds_read_b128 v[60:63], v192 offset:19328\n" \
  "v_mfma_f32_16x16x32_bf16 v[68:71], a[120:123], v[32:35], v[68:71]\n" \
  "s_waitcnt vmcnt(5)\n" \
  "s_barrier\n" \
  "v_mfma_f32_16x16x32_bf16 v[76:79], a[120:123], v[36:39], v[76:79]\n" \
  "buffer_store_dwordx2 v[168:169], v167, s[40:43], 0 offen sc1\n" \
  "s_waitcnt lgkmcnt(5)\n" \
  "v_mfma_f32_16x16x32_bf16 v[64:67], a[52:55], v[40:43], v[64:67]\n" \
  "ds_read_b128 v[32:35], v191 offset:1024\n" \
  "s_waitcnt lgkmcnt(5)\n" \
  "v_mfma_f32_16x16x32_bf16 v[72:75], a[52:55], v[44:47], v[72:75]\n" \
  "ds_read_b128 v[36:39], v191 offset:19456\n" \
  "v_mfma_f32_16x16x32_bf16 v[68:71], a[124:127], v[40:43], v[68:71]\n" \
  "s_add_u32 m0, s52, 3072\n" \
  "v_mfma_f32_16x16x32_bf16 v[76:79], a[124:127], v[44:47], v[76:79]\n" \
  "buffer_load_dwordx4 v199, s[44:47], s51 offen lds\n" \
  "s_waitcnt lgkmcnt(5)\n" \
  "v_mfma_f32_16x16x32_bf16 v[64:67], a[56:59], v[48:51], v[64:67]\n" \
  "ds_read_b128 v[40:43], v192 offset:1024\n" \
  "s_waitcnt lgkmcnt(5)\n" \
  "v_mfma_f32_16x16x32_bf16 v[72:75], a[56:59], v[52:55], v[72:75]\n" \
  "ds_read_b128 v[44:47], v192 offset:19456\n" \
  "v_mfma_f32_16x16x32_bf16 v[68:71], a[128:131], v[48:51], v[68:71]\n" \
  "v_mfma_f32_16x16x32_bf16 v[76:79], a[128:131], v[52:55], v[76:79]\n" \
  "s_waitcnt lgkmcnt(5)\n" \
  "v_mfma_f32_16x16x32_bf16 v[64:67], a[60:63], v[56:59], v[64:67]\n" \
  "ds_read_b128 v[48:51], v191\n" \
  "s_waitcnt lgkmcnt(5)\n" \
  "v_mfma_f32_16x16x32_bf16 v[72:75], a[60:63], v[60:63], v[72:75]\n" \
  "ds_read_b128 v[52:55], v191 offset:18432\n" \
  "v_mfma_f32_16x16x32_bf16 v[68:71], a[132:135], v[56:59], v[68:71]\n" \
  "s_add_u32 m0, s52, 4096\n" \
  "v_mfma_f32_16x16x32_bf16 v[76:79], a[132:135], v[60:63], v[76:79]\n" \
  "buffer_load_dwordx4 v200, s[44:47], s51 offen lds\n" \
  "s_waitcnt lgkmcnt(5)\n" \
  "v_mfma_f32_16x16x32_bf16 v[64:67], a[64:67], v[32:35], v[64:67]\n" \
  "ds_read_b128 v[56:59], v192\n" \
  "s_waitcnt lgkmcnt(5)\n" \
  "v_mfma_f32_16x16x32_bf16 v[72:75], a[64:67], v[36:39], v[72:75]\n" \
  "ds_read_b128 v[60:63], v192 offset:18432\n" \
  "v_mfma_f32_16x16x32_bf16 v[68:71], a[136:139], v[32:35], v[68:71]\n" \
  "v_mfma_f32_16x16x32_bf16 v[76:79], a[136:139], v[36:39], v[76:79]\n" \
  "s_waitcnt lgkmcnt(5)\n" \
  "v_mfma_f32_16x16x32_bf16 v[64:67], a[68:71], v[40:43], v[64:67]\n" \
  "ds_read_b128 v[32:35], v191 offset:128\n" \
  "s_waitcnt lgkmcnt(5)\n" \
  "v_mfma_f32_16x16x32_bf16 v[72:75], a[68:71], v[44:47], v[72:75]\n" \
  "ds_read_b128 v[36:39], v191 offset:18560\n" \
  "v_mfma_f32_16x16x32_bf16 v[68:71], a[140:143], v[40:43], v[68:71]\n" \
  "v_mfma_f32_16x16x32_bf16 v[76:79], a[140:143], v[44:47], v[76:79]\n" \
  "s_waitcnt lgkmcnt(5)\n" \
  "v_mfma_f32_16x16x32_bf16 v[80:83], a[144:147], v[48:51], 0\n" \
  "ds_read_b128 v[40:43], v192 offset:128\n" \
  "s_waitcnt lgkmcnt(5)\n" \
  "v_mfma_f32_16x16x32_bf16 v[88:91], a[144:147], v[52:55], 0\n" \
  "ds_read_b128 v[44:47], v192 offset:18560\n" \
  "s_lshr_b32 s62, s49, 8\n" \
  "v_mfma_f32_16x16x32_bf16 v[84:87], a[216:219], v[48:51], 0\n" \
  "s_and_b32 s62, s62, 8192\n" \
  "s_xor_b32 s62, s62, s49\n" \
  "v_cvt_pk_bf16_f32 v96, v64, v65\n" \
  "v_mfma_f32_16x16x32_bf16 v[92:95], a[216:219], v[52:55], 0\n" \
  "s_waitcnt lgkmcnt(5)\n" \
  "v_cvt_pk_bf16_f32 v97, v66, v67\n" \
  "v_cvt_pk_bf16_f32 v98, v68, v69\n" \
  "v_mfma_f32_16x16x32_bf16 v[80:83], a[148:151], v[56:59], v[80:83]\n" \
  "ds_read_b128 v[48:51], v191 offset:256\n" \
  "s_waitcnt lgkmcnt(5)\n" \
  "v_cvt_pk_bf16_f32 v99, v70, v71\n" \
  "v_mfma_f32_16x16x32_bf16 v[88:91], a[148:151], v[60:63], v[88:91]\n" \
  "ds_read_b128 v[52:55], v191 offset:18688\n" \
  "s_lshl_b32 s63, s48, 14\n" \
  "v_mfma_f32_16x16x32_bf16 v[84:87], a[220:223], v[56:59], v[84:87]\n" \
  "s_xor_b32 s63, s63, s59\n" \
  "v_mfma_f32_16x16x32_bf16 v[92:95], a[220:223], v[60:63], v[92:95]\n" \
  "buffer_store_dwordx4 v[96:99], v190, s[40:43], s62 offen sc1\n" \
  "s_waitcnt lgkmcnt(5)\n" \
  "v_mfma_f32_16x16x32_bf16 v[80:83], a[152:155], v[32:35], v[80:83]\n" \
  "ds_read_b128 v[56:59], v192 offset:256\n" \
  "s_waitcnt lgkmcnt(5)\n" \
  "v_mfma_f32_16x16x32_bf16 v[88:91], a[152:155], v[36:39], v[88:91]\n" \
  "ds_read_b128 v[60:63], v192 offset:18688\n" \
  "v_mfma_f32_16x16x32_bf16 v[84:87], a[224:227], v[32:35], v[84:87]\n" \
  "buffer_store_dwordx4 v[96:99], v190, s[40:43], s63 offen sc1\n" \
  "v_mfma_f32_16x16x32_bf16 v[92:95], a[224:227], v[36:39], v[92:95]\n" \
  "s_waitcnt lgkmcnt(5)\n" \
  "v_cvt_pk_bf16_f32 v100, v72, v73\n" \
  "v_cvt_pk_bf16_f32 v101, v74, v75\n" \
  "v_mfma_f32_16x16x32_bf16 v[80:83], a[156:159], v[40:43], v[80:83]\n" \
  "ds_read_b128 v[32:35], v191 offset:384\n" \
  "s_waitcnt lgkmcnt(5)\n" \
  "v_cvt_pk_bf16_f32 v102, v76, v77\n" \
  "v_mfma_f32_16x16x32_bf16 v[88:91], a[156:159], v[44:47], v[88:91]\n" \
  "ds_read_b128 v[36:39], v191 offset:18816\n" \
  "v_cvt_pk_bf16_f32 v103, v78, v79\n" \
  "v_mfma_f32_16x16x32_bf16 v[84:87], a[228:231], v[40:43], v[84:87]\n" \
  "buffer_store_dwordx4 v[100:103], v190, s[40:43], s62 offen offset:1024 sc1\n" \
  "v_mfma_f32_16x16x32_bf16 v[92:95], a[228:231], v[44:47], v[92:95]\n" \
  "buffer_store_dwordx4 v[100:103], v190, s[40:43], s63 offen offset:1024 sc1\n" \
  "s_waitcnt lgkmcnt(5)\n" \
  "v_mfma_f32_16x16x32_bf16 v[80:83], a[160:163], v[48:51], v[80:83]\n" \
  "ds_read_b128 v[40:43], v192 offset:384\n" \
  "s_add_u32 s49, s49, 524288\n" \
  "s_waitcnt lgkmcnt(5)\n" \
  "v_mfma_f32_16x16x32_bf16 v[88:91], a[160:163], v[52:55], v[88:91]\n" \
  "ds_read_b128 v[44:47], v192 offset:18816\n" \
  "s_add_u32 s58, s48, 1\n" \
  "v_mfma_f32_16x16x32_bf16 v[84:87], a[232:235], v[48:51], v[84:87]\n" \
  "s_and_b32 s58, s58, 3\n" \
  "s_cselect_b32 s58, 0, 147456\n" \
  "s_sub_u32 s58, 36864, s58\n" \
  "v_mfma_f32_16x16x32_bf16 v[92:95], a[232:235], v[52:55], v[92:95]\n" \
  "s_waitcnt lgkmcnt(5)\n" \
  "v_add_u32 v165, s58, v191\n" \
  "v_add_u32 v164, s58, v192\n" \
  "v_mfma_f32_16x16x32_bf16 v[80:83], a[164:167], v[56:59], v[80:83]\n" \
  "ds_read_b128 v[48:51], v191 offset:512\n" \
  "s_waitcnt lgkmcnt(5)\n" \
  "v_mfma_f32_16x16x32_bf16 v[88:91], a[164:167], v[60:63], v[88:91]\n" \
  "ds_read_b128 v[52:55], v191 offset:18944\n" \
  "v_mfma_f32_16x16x32_bf16 v[84:87], a[236:239], v[56:59], v[84:87]\n" \
  "v_mfma_f32_16x16x32_bf16 v[92:95], a[236:239], v[60:63], v[92:95]\n" \
  "s_waitcnt lgkmcnt(5)\n" \
  "v_mfma_f32_16x16x32_bf16 v[80:83], a[168:171], v[32:35], v[80:83]\n" \
  "ds_read_b128 v[56:59], v192 offset:512\n" \
  "s_waitcnt lgkmcnt(5)\n" \
  "v_mfma_f32_16x16x32_bf16 v[88:91], a[168:171], v[36:39], v[88:91]\n" \
  "ds_read_b128 v[60:63], v192 offset:18944\n" \
  "v_mfma_f32_16x16x32_bf16 v[84:87], a[240:243], v[32:35], v[84:87]\n" \
  "v_mfma_f32_16x16x32_bf16 v[92:95], a[240:243], v[36:39], v[92:95]\n" \
  "s_waitcnt lgkmcnt(5)\n" \
  "v_mfma_f32_16x16x32_bf16 v[80:83], a[172:175], v[40:43], v[80:83]\n" \
  "ds_read_b128 v[32:35], v191 offset:640\n" \
  "s_waitcnt lgkmcnt(5)\n" \
  "v_mfma_f32_16x16x32_bf16 v[88:91], a[172:175], v[44:47], v[88:91]\n" \
  "ds_read_b128 v[36:39], v191 offset:19072\n" \
  "v_mfma_f32_16x16x32_bf16 v[84:87], a[244:247], v[40:43], v[84:87]\n" \
  "v_mfma_f32_16x16x32_bf16 v[92:95], a[244:247], v[44:47], v[92:95]\n" \
  "s_waitcnt lgkmcnt(5)\n" \
  "v_mfma_f32_16x16x32_bf16 v[80:83], a[176:179], v[48:51], v[80:83]\n" \
  "ds_read_b128 v[40:43], v192 offset:640\n" \
  "s_waitcnt lgkmcnt(5)\n" \
  "v_mfma_f32_16x16x32_bf16 v[88:91], a[176:179], v[52:55], v[88:91]\n" \
  "ds_read_b128 v[44:47], v192 offset:19072\n" \
  "v_mfma_f32_16x16x32_bf16 v[84:87], a[248:251], v[48:51], v[84:87]\n" \
  "s_add_u32 m0, s52, 5120\n" \
  "v_mfma_f32_16x16x32_bf16 v[92:95], a[248:251], v[52:55], v[92:95]\n" \
  "buffer_load_dwordx4 v201, s[44:47], s51 offen lds\n" \
  "s_waitcnt lgkmcnt(5)\n" \
  "v_mfma_f32_16x16x32_bf16 v[80:83], a[180:183], v[56:59], v[80:83]\n" \
  "ds_read_b128 v[48:51], v191 offset:768\n" \
  "s_waitcnt lgkmcnt(5)\n" \
  "v_mfma_f32_16x16x32_bf16 v[88:91], a[180:183], v[60:63], v[88:91]\n" \
  "ds_read_b128 v[52:55], v191 offset:19200\n" \
  "v_mfma_f32_16x16x32_bf16 v[84:87], a[252:255], v[56:59], v[84:87]\n" \
  "v_mfma_f32_16x16x32_bf16 v[92:95], a[252:255], v[60:63], v[92:95]\n" \
  "s_waitcnt lgkmcnt(5)\n" \
  "v_mfma_f32_16x16x32_bf16 v[80:83], a[184:187], v[32:35], v[80:83]\n" \
  "ds_read_b128 v[56:59], v192 offset:768\n" \
  "s_waitcnt lgkmcnt(5)\n" \
  "v_mfma_f32_16x16x32_bf16 v[88:91], a[184:187], v[36:39], v[88:91]\n" \
  "ds_read_b128 v[60:63], v192 offset:19200\n" \
  "v_mfma_f32_16x16x32_bf16 v[84:87], v[0:3], v[32:35], v[84:87]\n" \
  "s_add_u32 m0, s52, 6144\n" \
  "v_mfma_f32_16x16x32_bf16 v[92:95], v[0:3], v[36:39], v[92:95]\n" \
  "buffer_load_dwordx4 v202, s[44:47], s51 offen lds\n" \
  "s_waitcnt lgkmcnt(5)\n" \
  "v_mfma_f32_16x16x32_bf16 v[80:83], a[188:191], v[40:43], v[80:83]\n" \
  "ds_read_b128 v[32:35], v191 offset:896\n" \
  "s_waitcnt lgkmcnt(5)\n" \
  "v_mfma_f32_16x16x32_bf16 v[88:91], a[188:191], v[44:47], v[88:91]\n" \
  "ds_read_b128 v[36:39], v191 offset:19328\n" \
  "v_mfma_f32_16x16x32_bf16 v[84:87], v[4:7], v[40:43], v[84:87]\n" \
  "v_mfma_f32_16x16x32_bf16 v[92:95], v[4:7], v[44:47], v[92:95]\n" \
  "s_waitcnt lgkmcnt(5)\n" \
  "v_mfma_f32_16x16x32_bf16 v[80:83], a[192:195], v[48:51], v[80:83]\n" \
  "ds_read_b128 v[40:43], v192 offset:896\n" \
  "s_waitcnt lgkmcnt(5)\n" \
  "v_mfma_f32_16x16x32_bf16 v[88:91], a[192:195], v[52:55], v[88:91]\n" \
  "ds_read_b128 v[44:47], v192 offset:19328\n" \
  "v_mfma_f32_16x16x32_bf16 v[84:87], v[8:11], v[48:51], v[84:87]\n" \
  "s_add_u32 m0, s52, 7168\n" \
  "v_mfma_f32_16x16x32_bf16 v[92:95], v[8:11], v[52:55], v[92:95]\n" \
  "buffer_load_dwordx4 v203, s[44:47], s51 offen lds\n" \
  "s_waitcnt lgkmcnt(5)\n" \
  "v_mfma_f32_16x16x32_bf16 v[80:83], a[196:199], v[56:59], v[80:83]\n" \
  "ds_read_b128 v[48:51], v191 offset:1024\n" \
  "s_waitcnt lgkmcnt(5)\n" \
  "v_mfma_f32_16x16x32_bf16 v[88:91], a[196:199], v[60:63], v[88:91]\n" \
  "ds_read_b128 v[52:55], v191 offset:19456\n" \
  "v_mfma_f32_16x16x32_bf16 v[84:87], v[12:15], v[56:59], v[84:87]\n" \
  "v_mfma_f32_16x16x32_bf16 v[92:95], v[12:15], v[60:63], v[92:95]\n" \
  "s_waitcnt lgkmcnt(5)\n" \
  "v_mfma_f32_16x16x32_bf16 v[80:83], a[200:203], v[32:35], v[80:83]\n" \
  "ds_read_b128 v[56:59], v192 offset:1024\n" \
  "s_waitcnt lgkmcnt(5)\n" \
  "v_mfma_f32_16x16x32_bf16 v[88:91], a[200:203], v[36:39], v[88:91]\n" \
  "ds_read_b128 v[60:63], v192 offset:19456\n" \
  "v_mfma_f32_16x16x32_bf16 v[84:87], v[16:19], v[32:35], v[84:87]\n" \
  "s_add_u32 m0, s52, 8192\n" \
  "v_mfma_f32_16x16x32_bf16 v[92:95], v[16:19], v[36:39], v[92:95]\n" \
  "buffer_load_dwordx4 v204, s[44:47], s51 offen lds\n" \
  "s_waitcnt lgkmcnt(5)\n" \
  "v_mfma_f32_16x16x32_bf16 v[80:83], a[204:207], v[40:43], v[80:83]\n" \
  "ds_read_b128 v[32:35], v165\n" \
  "s_add_u32 s51, s51, 36864\n" \
  "s_waitcnt lgkmcnt(5)\n" \
  "v_mfma_f32_16x16x32_bf16 v[88:91], a[204:207], v[44:47], v[88:91]\n" \
  "ds_read_b128 v[36:39], v165 offset:18432\n" \
  "s_min_u32 s51, s51, 2322432\n" \
  "v_mfma_f32_16x16x32_bf16 v[84:87], v[20:23], v[40:43], v[84:87]\n" \
  "s_add_u32 s52, s52, 36864\n" \
  "v_mfma_f32_16x16x32_bf16 v[92:95], v[20:23], v[44:47], v[92:95]\n" \
  "s_cmp_ge_u32 s52, 163840\n" \
  "s_cselect_b32 s60, 147456, 0\n" \
  "s_sub_u32 s52, s52, s60\n" \
  "s_waitcnt lgkmcnt(5)\n" \
  "v_mfma_f32_16x16x32_bf16 v[80:83], a[208:211], v[48:51], v[80:83]\n" \
  "ds_read_b128 v[40:43], v164\n" \
  "s_waitcnt lgkmcnt(5)\n" \
  "v_mfma_f32_16x16x32_bf16 v[88:91], a[208:211], v[52:55], v[88:91]\n" \
  "ds_read_b128 v[44:47], v164 offset:18432\n" \
  "v_mfma_f32_16x16x32_bf16 v[84:87], v[24:27], v[48:51], v[84:87]\n" \
  "v_mfma_f32_16x16x32_bf16 v[92:95], v[24:27], v[52:55], v[92:95]\n" \
  "s_waitcnt lgkmcnt(5)\n" \
  "v_mfma_f32_16x16x32_bf16 v[80:83], a[212:215], v[56:59], v[80:83]\n" \
  "ds_read_b128 v[48:51], v165 offset:128\n" \
  "s_waitcnt lgkmcnt(5)\n" \
  "v_mfma_f32_16x16x32_bf16 v[88:91], a[212:215], v[60:63], v[88:91]\n" \
  "ds_read_b128 v[52:55], v165 offset:18560\n" \
  "v_mfma_f32_16x16x32_bf16 v[84:87], v[28:31], v[56:59], v[84:87]\n" \
  "v_mov_b32 v191, v165\n" \
  "v_mov_b32 v192, v164\n" \
  "v_mfma_f32_16x16x32_bf16 v[92:95], v[28:31], v[60:63], v[92:95]\n" \
  "s_add_u32 s48, s48, 1\n" \
  "s_cmp_lt_u32 s48, 58\n" \
  "s_waitcnt lgkmcnt(0)\n" \
  "s_cbranch_scc1 .LK_loop_%=\n" \
  "s_nop 7\n" \
  "s_lshr_b32 s62, s50, 8\n" \
  "s_and_b32 s62, s62, 8192\n" \
  "s_xor_b32 s62, s62, s50\n" \
  "v_cvt_pk_bf16_f32 v96, v80, v81\n" \
  "v_cvt_pk_bf16_f32 v97, v82, v83\n" \
  "v_cvt_pk_bf16_f32 v98, v84, v85\n" \
  "v_cvt_pk_bf16_f32 v99, v86, v87\n" \
  "buffer_store_dwordx4 v[96:99], v190, s[40:43], s62 offen sc1\n" \
  "v_cvt_pk_bf16_f32 v100, v88, v89\n" \
  "v_cvt_pk_bf16_f32 v101, v90, v91\n" \
  "v_cvt_pk_bf16_f32 v102, v92, v93\n" \
  "v_cvt_pk_bf16_f32 v103, v94, v95\n" \
  "buffer_store_dwordx4 v[100:103], v190, s[40:43], s62 offen offset:1024 sc1\n" \
  "s_waitcnt vmcnt(0)\n" \
  "s_barrier\n" \
  "s_mov_b32 s48, 64\n" \
  "s_mov_b32 s69, 0\n" \
  ".LK_ie_%=:\n" \
  "s_cmp_ge_u32 s96, 0xc0000000\n" \
  "s_cbranch_scc1 .LK_ie_x_%=\n" \
  "buffer_load_dwordx2 v[184:185], off, s[40:43], %41 sc1\n" \
  "s_waitcnt vmcnt(0)\n" \
  "v_readfirstlane_b32 s96, v184\n" \
  "s_add_u32 s69, s69, 1\n" \
  "s_cmp_lt_u32 s69, 2097152\n" \
  "s_cbranch_scc1 .LK_ie_%=\n" \
  ".LK_ie_x_%=:\n" \
  "v_mov_b32 v168, s48\n" \
  "buffer_store_dwordx2 v[168:169], v167, s[40:43], 0 offen sc1\n" \
  "s_branch .LK_end_%=\n" \
  ".LK_mea_%=:\n" \
  "s_waitcnt vmcnt(7)\n" \
  "s_barrier\n" \
  "v_mov_b32 v168, s48\n" \
  "s_mov_b64 exec, 1\n" \
  "buffer_store_dwordx2 v[168:169], v167, s[40:43], 0 offen sc1\n" \
  "s_mov_b64 exec, -1\n" \
  "s_branch .LK_mea_b_%=\n" \
  ".LK_meb_%=:\n" \
  "s_waitcnt vmcnt(7)\n" \
  "s_barrier\n" \
  "v_mov_b32 v168, s48\n" \
  "s_mov_b64 exec, 1\n" \
  "buffer_store_dwordx2 v[168:169], v167, s[40:43], 0 offen sc1\n" \
  "s_mov_b64 exec, -1\n" \
  "s_cmp_lg_u32 s48, 3\n" \
  "s_cbranch_scc1 .LK_in_x_%=\n" \
  "s_mov_b32 s69, 0\n" \
  ".LK_in_%=:\n" \
  "s_cmp_ge_u32 s96, 0xc0000000\n" \
  "s_cbranch_scc1 .LK_in_x_%=\n" \
  "buffer_load_dwordx2 v[184:185], off, s[40:43], %41 sc1\n" \
  "s_waitcnt vmcnt(0)\n" \
  "v_readfirstlane_b32 s96, v184\n" \
  "s_add_u32 s69, s69, 1\n" \
  "s_cmp_lt_u32 s69, 2097152\n" \
  "s_cbranch_scc1 .LK_in_%=\n" \
  ".LK_in_x_%=:\n" \
  "s_branch .LK_meb_b_%=\n" \
  ".LK_end_%=:\n" \
  "s_mov_b32 %0, s55\n"
#define K_FIRST 16
#define U_HELP 8
static_assert(SDIR == 58, "generator");
#define U_ASM_TEXT \
  "s_mov_b64 s[40:41], %30\n" \
  "s_and_b32 s41, s41, 0xffff\n" \
  "s_mov_b32 s42, 255852544\n" \
  "s_mov_b32 s43, 0x00020000\n" \
  "s_mov_b32 s47, 0x00020000\n" \
  "s_lshl_b32 s72, %34, 12\n" \
  "buffer_load_dword v184, off, s[40:43], %42 sc1\n" \
  "s_and_b32 s73, %45, 255\n" \
  "s_lshr_b32 s74, %45, 8\n" \
  "s_mov_b32 s75, s73\n" \
  "s_mov_b32 s70, 2\n" \
  "s_mov_b64 s[44:45], %31\n" \
  "s_and_b32 s45, s45, 0xffff\n" \
  "s_mov_b32 s46, 4718592\n" \
  "s_add_u32 s60, %35, 0\n" \
  "s_add_u32 m0, s72, 16384\n" \
  "s_nop 0\n" \
  "buffer_load_dwordx4 v170, s[44:47], s60 offen lds\n" \
  "buffer_load_dwordx4 v171, s[44:47], s60 offen offset:1024 lds\n" \
  "buffer_load_dwordx4 v172, s[44:47], s60 offen offset:2048 lds\n" \
  "buffer_load_dwordx4 v173, s[44:47], s60 offen offset:3072 lds\n" \
  "s_add_u32 s60, %35, 524288\n" \
  "s_add_u32 m0, s72, 32768\n" \
  "s_nop 0\n" \
  "buffer_load_dwordx4 v170, s[44:47], s60 offen lds\n" \
  "buffer_load_dwordx4 v171, s[44:47], s60 offen offset:1024 lds\n" \
  "buffer_load_dwordx4 v172, s[44:47], s60 offen offset:2048 lds\n" \
  "buffer_load_dwordx4 v173, s[44:47], s60 offen offset:3072 lds\n" \
  "s_add_u32 s60, %35, 1048576\n" \
  "s_add_u32 m0, s72, 49152\n" \
  "s_nop 0\n" \
  "buffer_load_dwordx4 v170, s[44:47], s60 offen lds\n" \
  "buffer_load_dwordx4 v171, s[44:47], s60 offen offset:1024 lds\n" \
  "buffer_load_dwordx4 v172, s[44:47], s60 offen offset:2048 lds\n" \
  "buffer_load_dwordx4 v173, s[44:47], s60 offen offset:3072 lds\n" \
  "s_add_u32 s60, %35, 1572864\n" \
  "s_add_u32 m0, s72, 65536\n" \
  "s_nop 0\n" \
  "buffer_load_dwordx4 v170, s[44:47], s60 offen lds\n" \
  "buffer_load_dwordx4 v171, s[44:47], s60 offen offset:1024 lds\n" \
  "buffer_load_dwordx4 v172, s[44:47], s60 offen offset:2048 lds\n" \
  "buffer_load_dwordx4 v173, s[44:47], s60 offen offset:3072 lds\n" \
  "s_add_u32 s60, %35, 2097152\n" \
  "s_add_u32 m0, s72, 81920\n" \
  "s_nop 0\n" \
  "buffer_load_dwordx4 v170, s[44:47], s60 offen lds\n" \
  "buffer_load_dwordx4 v171, s[44:47], s60 offen offset:1024 lds\n" \
  "buffer_load_dwordx4 v172, s[44:47], s60 offen offset:2048 lds\n" \
  "buffer_load_dwordx4 v173, s[44:47], s60 offen offset:3072 lds\n" \
  "s_add_u32 s60, %35, 2621440\n" \
  "s_add_u32 m0, s72, 98304\n" \
  "s_nop 0\n" \
  "buffer_load_dwordx4 v170, s[44:47], s60 offen lds\n" \
  "buffer_load_dwordx4 v171, s[44:47], s60 offen offset:1024 lds\n" \
  "buffer_load_dwordx4 v172, s[44:47], s60 offen offset:2048 lds\n" \
  "buffer_load_dwordx4 v173, s[44:47], s60 offen offset:3072 lds\n" \
  "s_add_u32 s60, %35, 3145728\n" \
  "s_add_u32 m0, s72, 114688\n" \
  "s_nop 0\n" \
  "buffer_load_dwordx4 v170, s[44:47], s60 offen lds\n" \
  "buffer_load_dwordx4 v171, s[44:47], s60 offen offset:1024 lds\n" \
  "buffer_load_dwordx4 v172, s[44:47], s60 offen offset:2048 lds\n" \
  "buffer_load_dwordx4 v173, s[44:47], s60 offen offset:3072 lds\n" \
  "s_add_u32 s60, %35, 3670016\n" \
  "s_add_u32 m0, s72, 131072\n" \
  "s_nop 0\n" \
  "buffer_load_dwordx4 v170, s[44:47], s60 offen lds\n" \
  "buffer_load_dwordx4 v171, s[44:47], s60 offen offset:1024 lds\n" \
  "buffer_load_dwordx4 v172, s[44:47], s60 offen offset:2048 lds\n" \
  "buffer_load_dwordx4 v173, s[44:47], s60 offen offset:3072 lds\n" \
  "s_add_u32 s60, %35, 4194304\n" \
  "s_add_u32 m0, s72, 147456\n" \
  "s_nop 0\n" \
  "buffer_load_dwordx4 v170, s[44:47], s60 offen lds\n" \
  "buffer_load_dwordx4 v171, s[44:47], s60 offen offset:1024 lds\n" \
  "buffer_load_dwordx4 v172, s[44:47], s60 offen offset:2048 lds\n" \
  "buffer_load_dwordx4 v173, s[44:47], s60 offen offset:3072 lds\n" \
  "s_waitcnt vmcnt(24)\n" \
  "s_barrier\n" \
  "v_add_u32 v186, 16384, v182\n" \
  "ds_read_b64_tr_b16 a[0:1], v186\n" \
  "ds_read_b64_tr_b16 a[2:3], v186 offset:1024\n" \
  "ds_read_b64_tr_b16 a[4:5], v186 offset:8192\n" \
  "ds_read_b64_tr_b16 a[6:7], v186 offset:9216\n" \
  "v_add_u32 v187, 16384, v183\n" \
  "ds_read_b64_tr_b16 a[72:73], v187\n" \
  "ds_read_b64_tr_b16 a[74:75], v187 offset:1024\n" \
  "ds_read_b64_tr_b16 a[76:77], v187 offset:8192\n" \
  "ds_read_b64_tr_b16 a[78:79], v187 offset:9216\n" \
  "v_add_u32 v186, 32768, v182\n" \
  "ds_read_b64_tr_b16 a[8:9], v186\n" \
  "ds_read_b64_tr_b16 a[10:11], v186 offset:1024\n" \
  "ds_read_b64_tr_b16 a[12:13], v186 offset:8192\n" \
  "ds_read_b64_tr_b16 a[14:15], v186 offset:9216\n" \
  "v_add_u32 v187, 32768, v183\n" \
  "ds_read_b64_tr_b16 a[80:81], v187\n" \
  "ds_read_b64_tr_b16 a[82:83], v187 offset:1024\n" \
  "ds_read_b64_tr_b16 a[84:85], v187 offset:8192\n" \
  "ds_read_b64_tr_b16 a[86:87], v187 offset:9216\n" \
  "v_add_u32 v186, 49152, v182\n" \
  "ds_read_b64_tr_b16 a[16:17], v186\n" \
  "ds_read_b64_tr_b16 a[18:19], v186 offset:1024\n" \
  "ds_read_b64_tr_b16 a[20:21], v186 offset:8192\n" \
  "ds_read_b64_tr_b16 a[22:23], v186 offset:9216\n" \
  "v_add_u32 v187, 49152, v183\n" \
  "ds_read_b64_tr_b16 a[88:89], v187\n" \
  "ds_read_b64_tr_b16 a[90:91], v187 offset:1024\n" \
  "ds_read_b64_tr_b16 a[92:93], v187 offset:8192\n" \
  "ds_read_b64_tr_b16 a[94:95], v187 offset:9216\n" \
  "s_waitcnt lgkmcnt(0)\n" \
  "s_barrier\n" \
  "s_mov_b64 s[44:45], %32\n" \
  "s_and_b32 s45, s45, 0xffff\n" \
  "s_mov_b32 s46, 4718592\n" \
  "s_add_u32 s60, %36, 0\n" \
  "s_add_u32 m0, s72, 16384\n" \
  "s_nop 0\n" \
  "buffer_load_dwordx4 v174, s[44:47], s60 offen lds\n" \
  "buffer_load_dwordx4 v175, s[44:47], s60 offen offset:1024 lds\n" \
  "buffer_load_dwordx4 v176, s[44:47], s60 offen offset:2048 lds\n" \
  "buffer_load_dwordx4 v177, s[44:47], s60 offen offset:3072 lds\n" \
  "s_add_u32 s60, %36, 73728\n" \
  "s_add_u32 m0, s72, 32768\n" \
  "s_nop 0\n" \
  "buffer_load_dwordx4 v174, s[44:47], s60 offen lds\n" \
  "buffer_load_dwordx4 v175, s[44:47], s60 offen offset:1024 lds\n" \
  "buffer_load_dwordx4 v176, s[44:47], s60 offen offset:2048 lds\n" \
  "buffer_load_dwordx4 v177, s[44:47], s60 offen offset:3072 lds\n" \
  "s_add_u32 s60, %36, 256\n" \
  "s_add_u32 m0, s72, 49152\n" \
  "s_nop 0\n" \
  "buffer_load_dwordx4 v174, s[44:47], s60 offen lds\n" \
  "buffer_load_dwordx4 v175, s[44:47], s60 offen offset:1024 lds\n" \
  "buffer_load_dwordx4 v176, s[44:47], s60 offen offset:2048 lds\n" \
  "buffer_load_dwordx4 v177, s[44:47], s60 offen offset:3072 lds\n" \
  "v_readfirstlane_b32 s55, v184\n" \
  "s_add_u32 s55, s55, 1\n" \
  "s_waitcnt vmcnt(24)\n" \
  "s_barrier\n" \
  "v_add_u32 v186, 65536, v182\n" \
  "ds_read_b64_tr_b16 a[24:25], v186\n" \
  "ds_read_b64_tr_b16 a[26:27], v186 offset:1024\n" \
  "ds_read_b64_tr_b16 a[28:29], v186 offset:8192\n" \
  "ds_read_b64_tr_b16 a[30:31], v186 offset:9216\n" \
  "v_add_u32 v187, 65536, v183\n" \
  "ds_read_b64_tr_b16 a[96:97], v187\n" \
  "ds_read_b64_tr_b16 a[98:99], v187 offset:1024\n" \
  "ds_read_b64_tr_b16 a[100:101], v187 offset:8192\n" \
  "ds_read_b64_tr_b16 a[102:103], v187 offset:9216\n" \
  "v_add_u32 v186, 81920, v182\n" \
  "ds_read_b64_tr_b16 a[32:33], v186\n" \
  "ds_read_b64_tr_b16 a[34:35], v186 offset:1024\n" \
  "ds_read_b64_tr_b16 a[36:37], v186 offset:8192\n" \
  "ds_read_b64_tr_b16 a[38:39], v186 offset:9216\n" \
  "v_add_u32 v187, 81920, v183\n" \
  "ds_read_b64_tr_b16 a[104:105], v187\n" \
  "ds_read_b64_tr_b16 a[106:107], v187 offset:1024\n" \
  "ds_read_b64_tr_b16 a[108:109], v187 offset:8192\n" \
  "ds_read_b64_tr_b16 a[110:111], v187 offset:9216\n" \
  "v_add_u32 v186, 98304, v182\n" \
  "ds_read_b64_tr_b16 a[40:41], v186\n" \
  "ds_read_b64_tr_b16 a[42:43], v186 offset:1024\n" \
  "ds_read_b64_tr_b16 a[44:45], v186 offset:8192\n" \
  "ds_read_b64_tr_b16 a[46:47], v186 offset:9216\n" \
  "v_add_u32 v187, 98304, v183\n" \
  "ds_read_b64_tr_b16 a[112:113], v187\n" \
  "ds_read_b64_tr_b16 a[114:115], v187 offset:1024\n" \
  "ds_read_b64_tr_b16 a[116:117], v187 offset:8192\n" \
  "ds_read_b64_tr_b16 a[118:119], v187 offset:9216\n" \
  "s_waitcnt lgkmcnt(0)\n" \
  "s_barrier\n" \
  "s_add_u32 s60, %36, 73984\n" \
  "s_add_u32 m0, s72, 65536\n" \
  "s_nop 0\n" \
  "buffer_load_dwordx4 v174, s[44:47], s60 offen lds\n" \
  "buffer_load_dwordx4 v175, s[44:47], s60 offen offset:1024 lds\n" \
  "buffer_load_dwordx4 v176, s[44:47], s60 offen offset:2048 lds\n" \
  "buffer_load_dwordx4 v177, s[44:47], s60 offen offset:3072 lds\n" \
  "s_add_u32 s60, %36, 512\n" \
  "s_add_u32 m0, s72, 81920\n" \
  "s_nop 0\n" \
  "buffer_load_dwordx4 v174, s[44:47], s60 offen lds\n" \
  "buffer_load_dwordx4 v175, s[44:47], s60 offen offset:1024 lds\n" \
  "buffer_load_dwordx4 v176, s[44:47], s60 offen offset:2048 lds\n" \
  "buffer_load_dwordx4 v177, s[44:47], s60 offen offset:3072 lds\n" \
  "s_add_u32 s60, %36, 74240\n" \
  "s_add_u32 m0, s72, 98304\n" \
  "s_nop 0\n" \
  "buffer_load_dwordx4 v174, s[44:47], s60 offen lds\n" \
  "buffer_load_dwordx4 v175, s[44:47], s60 offen offset:1024 lds\n" \
  "buffer_load_dwordx4 v176, s[44:47], s60 offen offset:2048 lds\n" \
  "buffer_load_dwordx4 v177, s[44:47], s60 offen offset:3072 lds\n" \
  "s_waitcnt vmcnt(24)\n" \
  "s_barrier\n" \
  "v_add_u32 v186, 114688, v182\n" \
  "ds_read_b64_tr_b16 a[48:49], v186\n" \
  "ds_read_b64_tr_b16 a[50:51], v186 offset:1024\n" \
  "ds_read_b64_tr_b16 a[52:53], v186 offset:8192\n" \
  "ds_read_b64_tr_b16 a[54:55], v186 offset:9216\n" \
  "v_add_u32 v187, 114688, v183\n" \
  "ds_read_b64_tr_b16 a[120:121], v187\n" \
  "ds_read_b64_tr_b16 a[122:123], v187 offset:1024\n" \
  "ds_read_b64_tr_b16 a[124:125], v187 offset:8192\n" \
  "ds_read_b64_tr_b16 a[126:127], v187 offset:9216\n" \
  "v_add_u32 v186, 131072, v182\n" \
  "ds_read_b64_tr_b16 a[56:57], v186\n" \
  "ds_read_b64_tr_b16 a[58:59], v186 offset:1024\n" \
  "ds_read_b64_tr_b16 a[60:61], v186 offset:8192\n" \
  "ds_read_b64_tr_b16 a[62:63], v186 offset:9216\n" \
  "v_add_u32 v187, 131072, v183\n" \
  "ds_read_b64_tr_b16 a[128:129], v187\n" \
  "ds_read_b64_tr_b16 a[130:131], v187 offset:1024\n" \
  "ds_read_b64_tr_b16 a[132:133], v187 offset:8192\n" \
  "ds_read_b64_tr_b16 a[134:135], v187 offset:9216\n" \
  "v_add_u32 v186, 147456, v182\n" \
  "ds_read_b64_tr_b16 a[64:65], v186\n" \
  "ds_read_b64_tr_b16 a[66:67], v186 offset:1024\n" \
  "ds_read_b64_tr_b16 a[68:69], v186 offset:8192\n" \
  "ds_read_b64_tr_b16 a[70:71], v186 offset:9216\n" \
  "v_add_u32 v187, 147456, v183\n" \
  "ds_read_b64_tr_b16 a[136:137], v187\n" \
  "ds_read_b64_tr_b16 a[138:139], v187 offset:1024\n" \
  "ds_read_b64_tr_b16 a[140:141], v187 offset:8192\n" \
  "ds_read_b64_tr_b16 a[142:143], v187 offset:9216\n" \
  "s_waitcnt lgkmcnt(0)\n" \
  "s_barrier\n" \
  "s_add_u32 s60, %36, 768\n" \
  "s_add_u32 m0, s72, 114688\n" \
  "s_nop 0\n" \
  "buffer_load_dwordx4 v174, s[44:47], s60 offen lds\n" \
  "buffer_load_dwordx4 v175, s[44:47], s60 offen offset:1024 lds\n" \
  "buffer_load_dwordx4 v176, s[44:47], s60 offen offset:2048 lds\n" \
  "buffer_load_dwordx4 v177, s[44:47], s60 offen offset:3072 lds\n" \
  "s_add_u32 s60, %36, 74496\n" \
  "s_add_u32 m0, s72, 131072\n" \
  "s_nop 0\n" \
  "buffer_load_dwordx4 v174, s[44:47], s60 offen lds\n" \
  "buffer_load_dwordx4 v175, s[44:47], s60 offen offset:1024 lds\n" \
  "buffer_load_dwordx4 v176, s[44:47], s60 offen offset:2048 lds\n" \
  "buffer_load_dwordx4 v177, s[44:47], s60 offen offset:3072 lds\n" \
  "s_add_u32 s60, %36, 0\n" \
  "s_add_u32 m0, s72, 147456\n" \
  "s_nop 0\n" \
  "buffer_load_dwordx4 v178, s[44:47], s60 offen lds\n" \
  "buffer_load_dwordx4 v179, s[44:47], s60 offen offset:1024 lds\n" \
  "buffer_load_dwordx4 v180, s[44:47], s60 offen offset:2048 lds\n" \
  "buffer_load_dwordx4 v181, s[44:47], s60 offen offset:3072 lds\n" \
  "s_waitcnt vmcnt(24)\n" \
  "s_barrier\n" \
  "v_add_u32 v186, 16384, v182\n" \
  "ds_read_b64_tr_b16 a[144:145], v186\n" \
  "ds_read_b64_tr_b16 a[146:147], v186 offset:1024\n" \
  "ds_read_b64_tr_b16 a[148:149], v186 offset:8192\n" \
  "ds_read_b64_tr_b16 a[150:151], v186 offset:9216\n" \
  "v_add_u32 v187, 16384, v183\n" \
  "ds_read_b64_tr_b16 a[160:161], v187\n" \
  "ds_read_b64_tr_b16 a[162:163], v187 offset:1024\n" \
  "ds_read_b64_tr_b16 a[164:165], v187 offset:8192\n" \
  "ds_read_b64_tr_b16 a[166:167], v187 offset:9216\n" \
  "v_add_u32 v186, 32768, v182\n" \
  "ds_read_b64_tr_b16 a[152:153], v186\n" \
  "ds_read_b64_tr_b16 a[154:155], v186 offset:1024\n" \
  "ds_read_b64_tr_b16 a[156:157], v186 offset:8192\n" \
  "ds_read_b64_tr_b16 a[158:159], v186 offset:9216\n" \
  "v_add_u32 v187, 32768, v183\n" \
  "ds_read_b64_tr_b16 a[168:169], v187\n" \
  "ds_read_b64_tr_b16 a[170:171], v187 offset:1024\n" \
  "ds_read_b64_tr_b16 a[172:173], v187 offset:8192\n" \
  "ds_read_b64_tr_b16 a[174:175], v187 offset:9216\n" \
  "v_add_u32 v186, 49152, v182\n" \
  "ds_read_b64_tr_b16 a[176:177], v186\n" \
  "ds_read_b64_tr_b16 a[178:179], v186 offset:1024\n" \
  "ds_read_b64_tr_b16 a[180:181], v186 offset:8192\n" \
  "ds_read_b64_tr_b16 a[182:183], v186 offset:9216\n" \
  "v_add_u32 v187, 49152, v183\n" \
  "ds_read_b64_tr_b16 a[192:193], v187\n" \
  "ds_read_b64_tr_b16 a[194:195], v187 offset:1024\n" \
  "ds_read_b64_tr_b16 a[196:197], v187 offset:8192\n" \
  "ds_read_b64_tr_b16 a[198:199], v187 offset:9216\n" \
  "s_waitcnt lgkmcnt(0)\n" \
  "s_barrier\n" \
  "s_mov_b64 s[44:45], %33\n" \
  "s_and_b32 s45, s45, 0xffff\n" \
  "s_mov_b32 s46, 2359296\n" \
  "s_mul_i32 s51, s73, 36864\n" \
  "s_mul_i32 s52, %34, 9216\n" \
  "s_add_u32 s52, s52, 16384\n" \
  "s_add_u32 m0, s52, 0\n" \
  "s_nop 0\n" \
  "buffer_load_dwordx4 v196, s[44:47], s51 offen lds\n" \
  "s_add_u32 m0, s52, 1024\n" \
  "s_nop 0\n" \
  "buffer_load_dwordx4 v197, s[44:47], s51 offen lds\n" \
  "s_add_u32 m0, s52, 2048\n" \
  "s_nop 0\n" \
  "buffer_load_dwordx4 v198, s[44:47], s51 offen lds\n" \
  "s_add_u32 m0, s52, 3072\n" \
  "s_nop 0\n" \
  "buffer_load_dwordx4 v199, s[44:47], s51 offen lds\n" \
  "s_add_u32 m0, s52, 4096\n" \
  "s_nop 0\n" \
  "buffer_load_dwordx4 v200, s[44:47], s51 offen lds\n" \
  "s_add_u32 m0, s52, 5120\n" \
  "s_nop 0\n" \
  "buffer_load_dwordx4 v201, s[44:47], s51 offen lds\n" \
  "s_add_u32 m0, s52, 6144\n" \
  "s_nop 0\n" \
  "buffer_load_dwordx4 v202, s[44:47], s51 offen lds\n" \
  "s_add_u32 m0, s52, 7168\n" \
  "s_nop 0\n" \
  "buffer_load_dwordx4 v203, s[44:47], s51 offen lds\n" \
  "s_add_u32 m0, s52, 8192\n" \
  "s_nop 0\n" \
  "buffer_load_dwordx4 v204, s[44:47], s51 offen lds\n" \
  "s_cmp_lt_u32 s51, 589824\n" \
  "s_cselect_b32 s60, 36864, 0\n" \
  "s_add_u32 s51, s51, s60\n" \
  "s_add_u32 s51, s51, 36864\n" \
  "s_min_u32 s51, s51, 2322432\n" \
  "s_add_u32 s52, s52, 36864\n" \
  "s_cmp_ge_u32 s52, 163840\n" \
  "s_cselect_b32 s60, 147456, 0\n" \
  "s_sub_u32 s52, s52, s60\n" \
  "s_waitcnt vmcnt(21)\n" \
  "s_barrier\n" \
  "v_add_u32 v186, 65536, v182\n" \
  "ds_read_b64_tr_b16 a[184:185], v186\n" \
  "ds_read_b64_tr_b16 a[186:187], v186 offset:1024\n" \
  "ds_read_b64_tr_b16 a[188:189], v186 offset:8192\n" \
  "ds_read_b64_tr_b16 a[190:191], v186 offset:9216\n" \
  "v_add_u32 v187, 65536, v183\n" \
  "ds_read_b64_tr_b16 a[200:201], v187\n" \
  "ds_read_b64_tr_b16 a[202:203], v187 offset:1024\n" \
  "ds_read_b64_tr_b16 a[204:205], v187 offset:8192\n" \
  "ds_read_b64_tr_b16 a[206:207], v187 offset:9216\n" \
  "v_add_u32 v186, 81920, v182\n" \
  "ds_read_b64_tr_b16 a[208:209], v186\n" \
  "ds_read_b64_tr_b16 a[210:211], v186 offset:1024\n" \
  "ds_read_b64_tr_b16 a[212:213], v186 offset:8192\n" \
  "ds_read_b64_tr_b16 a[214:215], v186 offset:9216\n" \
  "v_add_u32 v187, 81920, v183\n" \
  "ds_read_b64_tr_b16 a[224:225], v187\n" \
  "ds_read_b64_tr_b16 a[226:227], v187 offset:1024\n" \
  "ds_read_b64_tr_b16 a[228:229], v187 offset:8192\n" \
  "ds_read_b64_tr_b16 a[230:231], v187 offset:9216\n" \
  "v_add_u32 v186, 98304, v182\n" \
  "ds_read_b64_tr_b16 a[216:217], v186\n" \
  "ds_read_b64_tr_b16 a[218:219], v186 offset:1024\n" \
  "ds_read_b64_tr_b16 a[220:221], v186 offset:8192\n" \
  "ds_read_b64_tr_b16 a[222:223], v186 offset:9216\n" \
  "v_add_u32 v187, 98304, v183\n" \
  "ds_read_b64_tr_b16 a[232:233], v187\n" \
  "ds_read_b64_tr_b16 a[234:235], v187 offset:1024\n" \
  "ds_read_b64_tr_b16 a[236:237], v187 offset:8192\n" \
  "ds_read_b64_tr_b16 a[238:239], v187 offset:9216\n" \
  "s_waitcnt lgkmcnt(0)\n" \
  "s_barrier\n" \
  "buffer_load_dwordx2 v[184:185], off, s[40:43], %41 sc1\n" \
  "s_add_u32 m0, s52, 0\n" \
  "s_nop 0\n" \
  "buffer_load_dwordx4 v196, s[44:47], s51 offen lds\n" \
  "s_add_u32 m0, s52, 1024\n" \
  "s_nop 0\n" \
  "buffer_load_dwordx4 v197, s[44:47], s51 offen lds\n" \
  "s_add_u32 m0, s52, 2048\n" \
  "s_nop 0\n" \
  "buffer_load_dwordx4 v198, s[44:47], s51 offen lds\n" \
  "s_add_u32 m0, s52, 3072\n" \
  "s_nop 0\n" \
  "buffer_load_dwordx4 v199, s[44:47], s51 offen lds\n" \
  "s_add_u32 m0, s52, 4096\n" \
  "s_nop 0\n" \
  "buffer_load_dwordx4 v200, s[44:47], s51 offen lds\n" \
  "s_add_u32 m0, s52, 5120\n" \
  "s_nop 0\n" \
  "buffer_load_dwordx4 v201, s[44:47], s51 offen lds\n" \
  "s_add_u32 m0, s52, 6144\n" \
  "s_nop 0\n" \
  "buffer_load_dwordx4 v202, s[44:47], s51 offen lds\n" \
  "s_add_u32 m0, s52, 7168\n" \
  "s_nop 0\n" \
  "buffer_load_dwordx4 v203, s[44:47], s51 offen lds\n" \
  "s_add_u32 m0, s52, 8192\n" \
  "s_nop 0\n" \
  "buffer_load_dwordx4 v204, s[44:47], s51 offen lds\n" \
  "s_cmp_lt_u32 s51, 589824\n" \
  "s_cselect_b32 s60, 36864, 0\n" \
  "s_add_u32 s51, s51, s60\n" \
  "s_add_u32 s51, s51, 36864\n" \
  "s_min_u32 s51, s51, 2322432\n" \
  "s_add_u32 s52, s52, 36864\n" \
  "s_cmp_ge_u32 s52, 163840\n" \
  "s_cselect_b32 s60, 147456, 0\n" \
  "s_sub_u32 s52, s52, s60\n" \
  "s_waitcnt vmcnt(19)\n" \
  "s_barrier\n" \
  "v_add_u32 v186, 114688, v182\n" \
  "ds_read_b64_tr_b16 a[240:241], v186\n" \
  "ds_read_b64_tr_b16 a[242:243], v186 offset:1024\n" \
  "ds_read_b64_tr_b16 a[244:245], v186 offset:8192\n" \
  "ds_read_b64_tr_b16 a[246:247], v186 offset:9216\n" \
  "v_add_u32 v187, 114688, v183\n" \
  "ds_read_b64_tr_b16 v[0:1], v187\n" \
  "ds_read_b64_tr_b16 v[2:3], v187 offset:1024\n" \
  "ds_read_b64_tr_b16 v[4:5], v187 offset:8192\n" \
  "ds_read_b64_tr_b16 v[6:7], v187 offset:9216\n" \
  "v_add_u32 v186, 131072, v182\n" \
  "ds_read_b64_tr_b16 a[248:249], v186\n" \
  "ds_read_b64_tr_b16 a[250:251], v186 offset:1024\n" \
  "ds_read_b64_tr_b16 a[252:253], v186 offset:8192\n" \
  "ds_read_b64_tr_b16 a[254:255], v186 offset:9216\n" \
  "v_add_u32 v187, 131072, v183\n" \
  "ds_read_b64_tr_b16 v[8:9], v187\n" \
  "ds_read_b64_tr_b16 v[10:11], v187 offset:1024\n" \
  "ds_read_b64_tr_b16 v[12:13], v187 offset:8192\n" \
  "ds_read_b64_tr_b16 v[14:15], v187 offset:9216\n" \
  "v_add_u32 v186, 147456, v182\n" \
  "ds_read_b64_tr_b16 v[16:17], v186\n" \
  "ds_read_b64_tr_b16 v[18:19], v186 offset:1024\n" \
  "ds_read_b64_tr_b16 v[20:21], v186 offset:8192\n" \
  "ds_read_b64_tr_b16 v[22:23], v186 offset:9216\n" \
  "v_add_u32 v187, 147456, v183\n" \
  "ds_read_b64_tr_b16 v[24:25], v187\n" \
  "ds_read_b64_tr_b16 v[26:27], v187 offset:1024\n" \
  "ds_read_b64_tr_b16 v[28:29], v187 offset:8192\n" \
  "ds_read_b64_tr_b16 v[30:31], v187 offset:9216\n" \
  "s_waitcnt lgkmcnt(0)\n" \
  "s_mov_b32 s69, 0\n" \
  "s_waitcnt vmcnt(9)\n" \
  "s_branch .LU_go1_%=\n" \
  ".LU_go_%=:\n" \
  "buffer_load_dwordx2 v[184:185], off, s[40:43], %41 sc1\n" \
  "s_waitcnt vmcnt(0)\n" \
  ".LU_go1_%=:\n" \
  "v_readfirstlane_b32 s96, v184\n" \
  "v_readfirstlane_b32 s63, v185\n" \
  "s_cmp_eq_u32 s63, s55\n" \
  "s_cselect_b32 s96, s96, 0\n" \
  "s_cmp_ge_u32 s96, 0x80000000\n" \
  "s_cbranch_scc1 .LU_go_x_%=\n" \
  "s_sleep 4\n" \
  "s_add_u32 s69, s69, 1\n" \
  "s_cmp_lt_u32 s69, 2097152\n" \
  "s_cbranch_scc1 .LU_go_%=\n" \
  "v_mov_b32 v184, 1\n" \
  "v_mov_b32 v185, s55\n" \
  "buffer_store_dwordx2 v[184:185], off, s[40:43], %42 offset:128 sc1\n" \
  ".LU_go_x_%=:\n" \
  "s_and_b32 s60, s96, 2\n" \
  "s_lshl_b32 s60, s60, 12\n" \
  "s_xor_b32 s53, %39, s60\n" \
  "s_xor_b32 s54, %40, s60\n" \
  "s_andn2_b32 s60, 2, s96\n" \
  "s_lshl_b32 s60, s60, 12\n" \
  "s_add_u32 s49, %37, s60\n" \
  "s_add_u32 s50, %38, s60\n" \
  "s_xor_b32 s66, %38, s60\n" \
  "s_xor_b32 s59, %44, s60\n" \
  "s_or_b32 s50, s50, 0x40000000\n" \
  "s_mov_b32 s57, 2130706432\n" \
  "s_mov_b32 s48, 0\n" \
  "v_mov_b32 v169, s55\n" \
  "v_mov_b32 v167, 2130706432\n" \
  "v_mov_b32 v166, 0\n" \
  "s_mov_b64 exec, 7\n" \
  "v_mov_b32 v167, %46\n" \
  "s_mov_b64 exec, 3\n" \
  "v_mov_b32 v167, s54\n" \
  "s_mov_b64 exec, 1\n" \
  "v_mov_b32 v167, s53\n" \
  "v_mov_b32 v166, 0\n" \
  "s_mov_b64 exec, -1\n" \
  "s_barrier\n" \
  "ds_read_b128 v[32:35], v191\n" \
  "ds_read_b128 v[36:39], v191 offset:18432\n" \
  "ds_read_b128 v[40:43], v192\n" \
  "ds_read_b128 v[44:47], v192 offset:18432\n" \
  "ds_read_b128 v[48:51], v191 offset:128\n" \
  "ds_read_b128 v[52:55], v191 offset:18560\n" \
  "s_waitcnt lgkmcnt(0)\n" \
  "v_mov_b64 v[164:165], 0\n" \
  ".LU_loop_%=:\n" \
  "v_mfma_f32_16x16x32_bf16 v[64:67], v[32:35], a[0:3], 0\n" \
  "ds_read_b128 v[56:59], v192 offset:128\n" \
  "v_mfma_f32_16x16x32_bf16 v[72:75], v[36:39], a[0:3], 0\n" \
  "ds_read_b128 v[60:63], v192 offset:18560\n" \
  "v_mfma_f32_16x16x32_bf16 v[68:71], v[32:35], a[72:75], 0\n" \
  "v_cvt_pk_bf16_f32 v104, v88, v89\n" \
  "v_cvt_pk_bf16_f32 v105, v90, v91\n" \
  "v_mfma_f32_16x16x32_bf16 v[76:79], v[36:39], a[72:75], 0\n" \
  "v_cvt_pk_bf16_f32 v106, v92, v93\n" \
  "v_cvt_pk_bf16_f32 v107, v94, v95\n" \
  "s_mov_b32 s63, s57\n" \
  "v_mfma_f32_16x16x32_bf16 v[64:67], v[40:43], a[4:7], v[64:67]\n" \
  "ds_read_b128 v[32:35], v191 offset:256\n" \
  "v_mfma_f32_16x16x32_bf16 v[72:75], v[44:47], a[4:7], v[72:75]\n" \
  "ds_read_b128 v[36:39], v191 offset:18688\n" \
  "v_mfma_f32_16x16x32_bf16 v[68:71], v[40:43], a[76:79], v[68:71]\n" \
  "buffer_store_dwordx4 v[104:107], v190, s[40:43], s63 offen sc1\n" \
  "v_mfma_f32_16x16x32_bf16 v[76:79], v[44:47], a[76:79], v[76:79]\n" \
  "v_mfma_f32_16x16x32_bf16 v[64:67], v[48:51], a[8:11], v[64:67]\n" \
  "ds_read_b128 v[40:43], v192 offset:256\n" \
  "v_mfma_f32_16x16x32_bf16 v[72:75], v[52:55], a[8:11], v[72:75]\n" \
  "ds_read_b128 v[44:47], v192 offset:18688\n" \
  "v_mfma_f32_16x16x32_bf16 v[68:71], v[48:51], a[80:83], v[68:71]\n" \
  "s_add_u32 m0, s52, 0\n" \
  "v_mfma_f32_16x16x32_bf16 v[76:79], v[52:55], a[80:83], v[76:79]\n" \
  "buffer_load_dwordx4 v196, s[44:47], s51 offen lds\n" \
  "s_waitcnt lgkmcnt(5)\n" \
  "v_mfma_f32_16x16x32_bf16 v[64:67], v[56:59], a[12:15], v[64:67]\n" \
  "ds_read_b128 v[48:51], v191 offset:384\n" \
  "s_mov_b32 s58, s75\n" \
  "s_waitcnt lgkmcnt(5)\n" \
  "v_mfma_f32_16x16x32_bf16 v[72:75], v[60:63], a[12:15], v[72:75]\n" \
  "ds_read_b128 v[52:55], v191 offset:18816\n" \
  "s_and_b32 s58, s58, 15\n" \
  "v_mfma_f32_16x16x32_bf16 v[68:71], v[56:59], a[84:87], v[68:71]\n" \
  "s_lshl_b32 s58, s58, 16\n" \
  "s_xor_b32 s50, s58, s66\n" \
  "s_xor_b32 s57, s58, s59\n" \
  "v_mfma_f32_16x16x32_bf16 v[76:79], v[60:63], a[84:87], v[76:79]\n" \
  "v_mul_i32_i24 v161, s70, v166\n" \
  "s_cmp_lt_u32 s73, 16\n" \
  "s_cselect_b32 s70, 2, 1\n" \
  "s_waitcnt lgkmcnt(5)\n" \
  "v_mfma_f32_16x16x32_bf16 v[64:67], v[32:35], a[16:19], v[64:67]\n" \
  "ds_read_b128 v[56:59], v192 offset:384\n" \
  "s_lshl_b32 s71, s70, 19\n" \
  "s_waitcnt lgkmcnt(5)\n" \
  "v_mfma_f32_16x16x32_bf16 v[72:75], v[36:39], a[16:19], v[72:75]\n" \
  "ds_read_b128 v[60:63], v192 offset:18816\n" \
  "s_mov_b32 s75, s73\n" \
  "v_mfma_f32_16x16x32_bf16 v[68:71], v[32:35], a[88:91], v[68:71]\n" \
  "s_add_u32 m0, s52, 1024\n" \
  "v_mfma_f32_16x16x32_bf16 v[76:79], v[36:39], a[88:91], v[76:79]\n" \
  "buffer_load_dwordx4 v197, s[44:47], s51 offen lds\n" \
  "s_waitcnt lgkmcnt(5)\n" \
  "v_mfma_f32_16x16x32_bf16 v[64:67], v[40:43], a[20:23], v[64:67]\n" \
  "ds_read_b128 v[32:35], v191 offset:512\n" \
  "s_waitcnt lgkmcnt(5)\n" \
  "v_mfma_f32_16x16x32_bf16 v[72:75], v[44:47], a[20:23], v[72:75]\n" \
  "ds_read_b128 v[36:39], v191 offset:18944\n" \
  "v_mfma_f32_16x16x32_bf16 v[68:71], v[40:43], a[92:95], v[68:71]\n" \
  "v_mfma_f32_16x16x32_bf16 v[76:79], v[44:47], a[92:95], v[76:79]\n" \
  "s_waitcnt lgkmcnt(5)\n" \
  "v_mfma_f32_16x16x32_bf16 v[64:67], v[48:51], a[24:27], v[64:67]\n" \
  "ds_read_b128 v[40:43], v192 offset:512\n" \
  "s_waitcnt lgkmcnt(5)\n" \
  "v_xor_b32 v160, 8, v194\n" \
  "v_mfma_f32_16x16x32_bf16 v[72:75], v[52:55], a[24:27], v[72:75]\n" \
  "ds_read_b128 v[44:47], v192 offset:18944\n" \
  "v_mfma_f32_16x16x32_bf16 v[68:71], v[48:51], a[96:99], v[68:71]\n" \
  "s_add_u32 m0, s52, 2048\n" \
  "v_add_u32 v168, s73, v161\n" \
  "v_mfma_f32_16x16x32_bf16 v[76:79], v[52:55], a[96:99], v[76:79]\n" \
  "buffer_load_dwordx4 v198, s[44:47], s51 offen lds\n" \
  "s_waitcnt lgkmcnt(5)\n" \
  "v_mfma_f32_16x16x32_bf16 v[64:67], v[56:59], a[28:31], v[64:67]\n" \
  "ds_read_b128 v[48:51], v191 offset:640\n" \
  "s_waitcnt lgkmcnt(5)\n" \
  "v_mfma_f32_16x16x32_bf16 v[72:75], v[60:63], a[28:31], v[72:75]\n" \
  "ds_read_b128 v[52:55], v191 offset:19072\n" \
  "v_mfma_f32_16x16x32_bf16 v[68:71], v[56:59], a[100:103], v[68:71]\n" \
  "s_waitcnt vmcnt(11)\n" \
  "s_barrier\n" \
  "v_mfma_f32_16x16x32_bf16 v[76:79], v[60:63], a[100:103], v[76:79]\n" \
  "ds_read_b64_tr_b16 v[104:105], v194\n" \
  "s_waitcnt lgkmcnt(6)\n" \
  "v_mfma_f32_16x16x32_bf16 v[64:67], v[32:35], a[32:35], v[64:67]\n" \
  "ds_read_b128 v[56:59], v192 offset:640\n" \
  "s_waitcnt lgkmcnt(6)\n" \
  "v_mfma_f32_16x16x32_bf16 v[72:75], v[36:39], a[32:35], v[72:75]\n" \
  "ds_read_b128 v[60:63], v192 offset:19072\n" \
  "v_mfma_f32_16x16x32_bf16 v[68:71], v[32:35], a[104:107], v[68:71]\n" \
  "s_add_u32 m0, s52, 3072\n" \
  "ds_read_b64_tr_b16 v[106:107], v194 offset:256\n" \
  "v_mfma_f32_16x16x32_bf16 v[76:79], v[36:39], a[104:107], v[76:79]\n" \
  "buffer_load_dwordx4 v199, s[44:47], s51 offen lds\n" \
  "s_waitcnt lgkmcnt(7)\n" \
  "v_mfma_f32_16x16x32_bf16 v[64:67], v[40:43], a[36:39], v[64:67]\n" \
  "ds_read_b128 v[32:35], v191 offset:768\n" \
  "s_waitcnt lgkmcnt(7)\n" \
  "v_mfma_f32_16x16x32_bf16 v[72:75], v[44:47], a[36:39], v[72:75]\n" \
  "ds_read_b128 v[36:39], v191 offset:19200\n" \
  "v_mfma_f32_16x16x32_bf16 v[68:71], v[40:43], a[108:111], v[68:71]\n" \
  "s_cmp_le_u32 s48, 7\n" \
  "s_cbranch_scc1 .LU_e2_%=\n" \
  ".LU_e2_b_%=:\n" \
  "v_mfma_f32_16x16x32_bf16 v[76:79], v[44:47], a[108:111], v[76:79]\n" \
  "ds_read_b64_tr_b16 v[108:109], v160\n" \
  "s_waitcnt lgkmcnt(8)\n" \
  "v_mfma_f32_16x16x32_bf16 v[64:67], v[48:51], a[40:43], v[64:67]\n" \
  "ds_read_b128 v[40:43], v192 offset:768\n" \
  "s_waitcnt lgkmcnt(8)\n" \
  "v_mfma_f32_16x16x32_bf16 v[72:75], v[52:55], a[40:43], v[72:75]\n" \
  "ds_read_b128 v[44:47], v192 offset:19200\n" \
  "v_mfma_f32_16x16x32_bf16 v[68:71], v[48:51], a[112:115], v[68:71]\n" \
  "s_add_u32 m0, s52, 4096\n" \
  "ds_read_b64_tr_b16 v[110:111], v160 offset:256\n" \
  "v_mfma_f32_16x16x32_bf16 v[76:79], v[52:55], a[112:115], v[76:79]\n" \
  "buffer_load_dwordx4 v200, s[44:47], s51 offen lds\n" \
  "s_waitcnt lgkmcnt(8)\n" \
  "v_mfma_f32_16x16x32_bf16 v[64:67], v[56:59], a[44:47], v[64:67]\n" \
  "ds_read_b128 v[48:51], v191 offset:896\n" \
  "s_waitcnt lgkmcnt(8)\n" \
  "v_mfma_f32_16x16x32_bf16 v[72:75], v[60:63], a[44:47], v[72:75]\n" \
  "ds_read_b128 v[52:55], v191 offset:19328\n" \
  "v_mfma_f32_16x16x32_bf16 v[68:71], v[56:59], a[116:119], v[68:71]\n" \
  "ds_read_b64_tr_b16 v[112:113], v194 offset:2048\n" \
  "v_mfma_f32_16x16x32_bf16 v[76:79], v[60:63], a[116:119], v[76:79]\n" \
  "ds_read_b64_tr_b16 v[114:115], v194 offset:2304\n" \
  "s_waitcnt lgkmcnt(9)\n" \
  "v_mfma_f32_16x16x32_bf16 v[64:67], v[32:35], a[48:51], v[64:67]\n" \
  "ds_read_b128 v[56:59], v192 offset:896\n" \
  "s_waitcnt lgkmcnt(9)\n" \
  "v_mfma_f32_16x16x32_bf16 v[72:75], v[36:39], a[48:51], v[72:75]\n" \
  "ds_read_b128 v[60:63], v192 offset:19328\n" \
  "v_mfma_f32_16x16x32_bf16 v[68:71], v[32:35], a[120:123], v[68:71]\n" \
  "s_add_u32 m0, s52, 5120\n" \
  "ds_read_b64_tr_b16 v[116:117], v160 offset:2048\n" \
  "v_mfma_f32_16x16x32_bf16 v[76:79], v[36:39], a[120:123], v[76:79]\n" \
  "buffer_load_dwordx4 v201, s[44:47], s51 offen lds\n" \
  "s_waitcnt lgkmcnt(9)\n" \
  "v_mfma_f32_16x16x32_bf16 v[64:67], v[40:43], a[52:55], v[64:67]\n" \
  "ds_read_b128 v[32:35], v191 offset:1024\n" \
  "s_waitcnt lgkmcnt(9)\n" \
  "v_mfma_f32_16x16x32_bf16 v[72:75], v[44:47], a[52:55], v[72:75]\n" \
  "ds_read_b128 v[36:39], v191 offset:19456\n" \
  "v_mfma_f32_16x16x32_bf16 v[68:71], v[40:43], a[124:127], v[68:71]\n" \
  "ds_read_b64_tr_b16 v[118:119], v160 offset:2304\n" \
  "v_mfma_f32_16x16x32_bf16 v[76:79], v[44:47], a[124:127], v[76:79]\n" \
  "ds_read_b64_tr_b16 v[120:121], v194 offset:4096\n" \
  "s_waitcnt lgkmcnt(10)\n" \
  "v_mfma_f32_16x16x32_bf16 v[64:67], v[48:51], a[56:59], v[64:67]\n" \
  "ds_read_b128 v[40:43], v192 offset:1024\n" \
  "s_waitcnt lgkmcnt(10)\n" \
  "v_mfma_f32_16x16x32_bf16 v[72:75], v[52:55], a[56:59], v[72:75]\n" \
  "ds_read_b128 v[44:47], v192 offset:19456\n" \
  "v_mfma_f32_16x16x32_bf16 v[68:71], v[48:51], a[128:131], v[68:71]\n" \
  "s_add_u32 m0, s52, 6144\n" \
  "ds_read_b64_tr_b16 v[122:123], v194 offset:4352\n" \
  "v_mfma_f32_16x16x32_bf16 v[76:79], v[52:55], a[128:131], v[76:79]\n" \
  "buffer_load_dwordx4 v202, s[44:47], s51 offen lds\n" \
  "s_waitcnt lgkmcnt(9)\n" \
  "v_mfma_f32_16x16x32_bf16 v[64:67], v[56:59], a[60:63], v[64:67]\n" \
  "ds_read_b64_tr_b16 v[124:125], v160 offset:4096\n" \
  "s_waitcnt lgkmcnt(9)\n" \
  "v_mfma_f32_16x16x32_bf16 v[72:75], v[60:63], a[60:63], v[72:75]\n" \
  "ds_read_b64_tr_b16 v[126:127], v160 offset:4352\n" \
  "v_mfma_f32_16x16x32_bf16 v[68:71], v[56:59], a[132:135], v[68:71]\n" \
  "s_add_u32 m0, s52, 7168\n" \
  "ds_read_b64_tr_b16 v[128:129], v194 offset:6144\n" \
  "v_mfma_f32_16x16x32_bf16 v[76:79], v[60:63], a[132:135], v[76:79]\n" \
  "buffer_load_dwordx4 v203, s[44:47], s51 offen lds\n" \
  "s_waitcnt lgkmcnt(9)\n" \
  "v_mfma_f32_16x16x32_bf16 v[64:67], v[32:35], a[64:67], v[64:67]\n" \
  "ds_read_b64_tr_b16 v[130:131], v194 offset:6400\n" \
  "s_waitcnt lgkmcnt(9)\n" \
  "v_mfma_f32_16x16x32_bf16 v[72:75], v[36:39], a[64:67], v[72:75]\n" \
  "ds_read_b64_tr_b16 v[132:133], v160 offset:6144\n" \
  "v_mfma_f32_16x16x32_bf16 v[68:71], v[32:35], a[136:139], v[68:71]\n" \
  "ds_read_b64_tr_b16 v[134:135], v160 offset:6400\n" \
  "v_mfma_f32_16x16x32_bf16 v[76:79], v[36:39], a[136:139], v[76:79]\n" \
  "s_waitcnt lgkmcnt(8)\n" \
  "v_mfma_f32_16x16x32_bf16 v[64:67], v[40:43], a[68:71], v[64:67]\n" \
  "s_waitcnt lgkmcnt(7)\n" \
  "v_mfma_f32_16x16x32_bf16 v[72:75], v[44:47], a[68:71], v[72:75]\n" \
  "s_waitcnt vmcnt(8)\n" \
  "buffer_store_dwordx2 v[168:169], v167, s[40:43], 0 offen sc1\n" \
  "v_mfma_f32_16x16x32_bf16 v[68:71], v[40:43], a[140:143], v[68:71]\n" \
  "s_add_u32 m0, s52, 8192\n" \
  "v_mfma_f32_16x16x32_bf16 v[76:79], v[44:47], a[140:143], v[76:79]\n" \
  "buffer_load_dwordx4 v204, s[44:47], s51 offen lds\n" \
  "s_cmp_le_u32 s48, 3\n" \
  "s_cbranch_scc1 .LU_first_%=\n" \
  ".LU_phb_%=:\n" \
  "v_mfma_f32_16x16x32_bf16 v[88:91], v[104:107], a[144:147], 0\n" \
  "v_mfma_f32_16x16x32_bf16 v[92:95], v[108:111], a[144:147], 0\n" \
  "s_lshr_b32 s62, s49, 8\n" \
  "s_and_b32 s62, s62, 8192\n" \
  "s_xor_b32 s62, s62, s49\n" \
  "v_mfma_f32_16x16x32_bf16 v[88:91], v[112:115], a[148:151], v[88:91]\n" \
  "v_cvt_pk_bf16_f32 v80, v64, v65\n" \
  "v_cvt_pk_bf16_f32 v81, v66, v67\n" \
  "v_mfma_f32_16x16x32_bf16 v[92:95], v[116:119], a[148:151], v[92:95]\n" \
  "s_waitcnt lgkmcnt(6)\n" \
  "v_cvt_pk_bf16_f32 v82, v72, v73\n" \
  "v_cvt_pk_bf16_f32 v83, v74, v75\n" \
  "v_mfma_f32_16x16x32_bf16 v[88:91], v[120:123], a[152:155], v[88:91]\n" \
  "buffer_store_dwordx4 v[80:83], v190, s[40:43], s62 offen sc1\n" \
  "s_waitcnt lgkmcnt(4)\n" \
  "v_mfma_f32_16x16x32_bf16 v[92:95], v[124:127], a[152:155], v[92:95]\n" \
  "ds_write_b128 v193, v[80:83]\n" \
  "s_waitcnt lgkmcnt(3)\n" \
  "v_mfma_f32_16x16x32_bf16 v[88:91], v[128:131], a[156:159], v[88:91]\n" \
  "s_waitcnt lgkmcnt(1)\n" \
  "v_cvt_pk_bf16_f32 v84, v76, v77\n" \
  "v_cvt_pk_bf16_f32 v85, v78, v79\n" \
  "v_mfma_f32_16x16x32_bf16 v[92:95], v[132:135], a[156:159], v[92:95]\n" \
  "v_cvt_pk_bf16_f32 v86, v68, v69\n" \
  "v_cvt_pk_bf16_f32 v87, v70, v71\n" \
  "v_mfma_f32_16x16x32_bf16 v[96:99], v[104:107], a[160:163], 0\n" \
  "buffer_store_dwordx4 v[84:87], v190, s[40:43], s62 offen offset:1024 sc1\n" \
  "v_mfma_f32_16x16x32_bf16 v[100:103], v[108:111], a[160:163], 0\n" \
  "ds_write_b128 v193, v[84:87] offset:1024\n" \
  "v_mfma_f32_16x16x32_bf16 v[96:99], v[112:115], a[164:167], v[96:99]\n" \
  "v_cvt_pk_bf16_f32 v136, v88, v89\n" \
  "v_cvt_pk_bf16_f32 v137, v90, v91\n" \
  "s_add_u32 s58, s48, 1\n" \
  "v_mfma_f32_16x16x32_bf16 v[100:103], v[116:119], a[164:167], v[100:103]\n" \
  "v_cvt_pk_bf16_f32 v138, v92, v93\n" \
  "v_cvt_pk_bf16_f32 v139, v94, v95\n" \
  "s_xor_b32 s63, s50, 0\n" \
  "v_mfma_f32_16x16x32_bf16 v[96:99], v[120:123], a[168:171], v[96:99]\n" \
  "s_add_u32 s63, s63, 0\n" \
  "s_and_b32 s58, s58, 3\n" \
  "s_cselect_b32 s58, 0, 147456\n" \
  "v_mfma_f32_16x16x32_bf16 v[100:103], v[124:127], a[168:171], v[100:103]\n" \
  "buffer_store_dwordx4 v[136:139], v190, s[40:43], s63 offen sc1\n" \
  "v_mfma_f32_16x16x32_bf16 v[96:99], v[128:131], a[172:175], v[96:99]\n" \
  "s_sub_u32 s58, 36864, s58\n" \
  "v_add_u32 v163, s58, v191\n" \
  "v_add_u32 v162, s58, v192\n" \
  "v_mfma_f32_16x16x32_bf16 v[100:103], v[132:135], a[172:175], v[100:103]\n" \
  "s_cmp_lt_u32 s51, 589824\n" \
  "s_cselect_b32 s60, 36864, 0\n" \
  "s_add_u32 s51, s51, s60\n" \
  "v_mfma_f32_16x16x32_bf16 v[88:91], v[104:107], a[176:179], 0\n" \
  "s_add_u32 s51, s51, 36864\n" \
  "s_min_u32 s51, s51, 2322432\n" \
  "s_add_u32 s52, s52, 36864\n" \
  "v_mfma_f32_16x16x32_bf16 v[92:95], v[108:111], a[176:179], 0\n" \
  "s_cmp_ge_u32 s52, 163840\n" \
  "s_cselect_b32 s60, 147456, 0\n" \
  "s_sub_u32 s52, s52, s60\n" \
  "v_mfma_f32_16x16x32_bf16 v[88:91], v[112:115], a[180:183], v[88:91]\n" \
  "v_cvt_pk_bf16_f32 v136, v96, v97\n" \
  "v_cvt_pk_bf16_f32 v137, v98, v99\n" \
  "v_mfma_f32_16x16x32_bf16 v[92:95], v[116:119], a[180:183], v[92:95]\n" \
  "v_cvt_pk_bf16_f32 v138, v100, v101\n" \
  "v_cvt_pk_bf16_f32 v139, v102, v103\n" \
  "s_xor_b32 s63, s50, 1024\n" \
  "v_mfma_f32_16x16x32_bf16 v[88:91], v[120:123], a[184:187], v[88:91]\n" \
  "s_add_u32 s63, s63, 1048576\n" \
  "v_mfma_f32_16x16x32_bf16 v[92:95], v[124:127], a[184:187], v[92:95]\n" \
  "buffer_store_dwordx4 v[136:139], v190, s[40:43], s63 offen sc1\n" \
  "v_mfma_f32_16x16x32_bf16 v[88:91], v[128:131], a[188:191], v[88:91]\n" \
  "s_nop 2\n" \
  "v_mfma_f32_16x16x32_bf16 v[92:95], v[132:135], a[188:191], v[92:95]\n" \
  "s_nop 0\n" \
  "s_nop 0\n" \
  "v_mfma_f32_16x16x32_bf16 v[96:99], v[104:107], a[192:195], 0\n" \
  "v_mfma_f32_16x16x32_bf16 v[100:103], v[108:111], a[192:195], 0\n" \
  "v_mfma_f32_16x16x32_bf16 v[96:99], v[112:115], a[196:199], v[96:99]\n" \
  "v_cvt_pk_bf16_f32 v136, v88, v89\n" \
  "v_cvt_pk_bf16_f32 v137, v90, v91\n" \
  "v_mfma_f32_16x16x32_bf16 v[100:103], v[116:119], a[196:199], v[100:103]\n" \
  "v_cvt_pk_bf16_f32 v138, v92, v93\n" \
  "v_cvt_pk_bf16_f32 v139, v94, v95\n" \
  "s_xor_b32 s63, s50, 278528\n" \
  "v_mfma_f32_16x16x32_bf16 v[96:99], v[120:123], a[200:203], v[96:99]\n" \
  "s_add_u32 s63, s63, 8388608\n" \
  "v_mfma_f32_16x16x32_bf16 v[100:103], v[124:127], a[200:203], v[100:103]\n" \
  "buffer_store_dwordx4 v[136:139], v190, s[40:43], s63 offen sc1\n" \
  "v_mfma_f32_16x16x32_bf16 v[96:99], v[128:131], a[204:207], v[96:99]\n" \
  "v_readfirstlane_b32 s58, v164\n" \
  "v_readfirstlane_b32 s61, v165\n" \
  "v_mfma_f32_16x16x32_bf16 v[100:103], v[132:135], a[204:207], v[100:103]\n" \
  "s_cmp_eq_u32 s61, s55\n" \
  "s_cselect_b32 s58, s58, 0\n" \
  "s_add_u32 s58, s58, 14\n" \
  "v_mfma_f32_16x16x32_bf16 v[88:91], v[104:107], a[208:211], 0\n" \
  "s_cmp_lt_u32 s58, s73\n" \
  "s_cbranch_scc1 .LU_fc_%=\n" \
  ".LU_fc_b_%=:\n" \
  "v_mfma_f32_16x16x32_bf16 v[92:95], v[108:111], a[208:211], 0\n" \
  "buffer_load_dwordx2 v[164:165], off, s[40:43], %43 sc1\n" \
  "v_mfma_f32_16x16x32_bf16 v[88:91], v[112:115], a[212:215], v[88:91]\n" \
  "v_cvt_pk_bf16_f32 v136, v96, v97\n" \
  "v_cvt_pk_bf16_f32 v137, v98, v99\n" \
  "v_mfma_f32_16x16x32_bf16 v[92:95], v[116:119], a[212:215], v[92:95]\n" \
  "v_cvt_pk_bf16_f32 v138, v100, v101\n" \
  "v_cvt_pk_bf16_f32 v139, v102, v103\n" \
  "s_xor_b32 s63, s50, 279552\n" \
  "v_mfma_f32_16x16x32_bf16 v[88:91], v[120:123], a[216:219], v[88:91]\n" \
  "s_add_u32 s63, s63, 9437184\n" \
  "v_mfma_f32_16x16x32_bf16 v[92:95], v[124:127], a[216:219], v[92:95]\n" \
  "buffer_store_dwordx4 v[136:139], v190, s[40:43], s63 offen sc1\n" \
  "v_mfma_f32_16x16x32_bf16 v[88:91], v[128:131], a[220:223], v[88:91]\n" \
  "s_nop 2\n" \
  "v_mfma_f32_16x16x32_bf16 v[92:95], v[132:135], a[220:223], v[92:95]\n" \
  "s_nop 0\n" \
  "s_nop 0\n" \
  "v_mfma_f32_16x16x32_bf16 v[96:99], v[104:107], a[224:227], 0\n" \
  "v_mfma_f32_16x16x32_bf16 v[100:103], v[108:111], a[224:227], 0\n" \
  "v_mfma_f32_16x16x32_bf16 v[96:99], v[112:115], a[228:231], v[96:99]\n" \
  "v_cvt_pk_bf16_f32 v136, v88, v89\n" \
  "v_cvt_pk_bf16_f32 v137, v90, v91\n" \
  "v_mfma_f32_16x16x32_bf16 v[100:103], v[116:119], a[228:231], v[100:103]\n" \
  "v_cvt_pk_bf16_f32 v138, v92, v93\n" \
  "v_cvt_pk_bf16_f32 v139, v94, v95\n" \
  "s_xor_b32 s63, s50, 557056\n" \
  "v_mfma_f32_16x16x32_bf16 v[96:99], v[120:123], a[232:235], v[96:99]\n" \
  "s_add_u32 s63, s63, 16777216\n" \
  "v_mfma_f32_16x16x32_bf16 v[100:103], v[124:127], a[232:235], v[100:103]\n" \
  "buffer_store_dwordx4 v[136:139], v190, s[40:43], s63 offen sc1\n" \
  "v_mfma_f32_16x16x32_bf16 v[96:99], v[128:131], a[236:239], v[96:99]\n" \
  "s_nop 2\n" \
  "v_mfma_f32_16x16x32_bf16 v[100:103], v[132:135], a[236:239], v[100:103]\n" \
  "s_nop 0\n" \
  "s_nop 0\n" \
  "v_mfma_f32_16x16x32_bf16 v[88:91], v[104:107], a[240:243], 0\n" \
  "v_mfma_f32_16x16x32_bf16 v[92:95], v[108:111], a[240:243], 0\n" \
  "v_mfma_f32_16x16x32_bf16 v[88:91], v[112:115], a[244:247], v[88:91]\n" \
  "v_cvt_pk_bf16_f32 v136, v96, v97\n" \
  "v_cvt_pk_bf16_f32 v137, v98, v99\n" \
  "v_mfma_f32_16x16x32_bf16 v[92:95], v[116:119], a[244:247], v[92:95]\n" \
  "v_cvt_pk_bf16_f32 v138, v100, v101\n" \
  "v_cvt_pk_bf16_f32 v139, v102, v103\n" \
  "s_xor_b32 s63, s50, 558080\n" \
  "v_mfma_f32_16x16x32_bf16 v[88:91], v[120:123], a[248:251], v[88:91]\n" \
  "s_add_u32 s63, s63, 17825792\n" \
  "v_mfma_f32_16x16x32_bf16 v[92:95], v[124:127], a[248:251], v[92:95]\n" \
  "buffer_store_dwordx4 v[136:139], v190, s[40:43], s63 offen sc1\n" \
  "v_mfma_f32_16x16x32_bf16 v[88:91], v[128:131], a[252:255], v[88:91]\n" \
  "v_mfma_f32_16x16x32_bf16 v[92:95], v[132:135], a[252:255], v[92:95]\n" \
  "v_mfma_f32_16x16x32_bf16 v[96:99], v[104:107], v[0:3], 0\n" \
  "ds_read_b128 v[32:35], v163\n" \
  "ds_read_b128 v[36:39], v163 offset:18432\n" \
  "v_mfma_f32_16x16x32_bf16 v[100:103], v[108:111], v[0:3], 0\n" \
  "ds_read_b128 v[40:43], v162\n" \
  "ds_read_b128 v[44:47], v162 offset:18432\n" \
  "v_mfma_f32_16x16x32_bf16 v[96:99], v[112:115], v[4:7], v[96:99]\n" \
  "v_cvt_pk_bf16_f32 v136, v88, v89\n" \
  "v_cvt_pk_bf16_f32 v137, v90, v91\n" \
  "v_mfma_f32_16x16x32_bf16 v[100:103], v[116:119], v[4:7], v[100:103]\n" \
  "v_cvt_pk_bf16_f32 v138, v92, v93\n" \
  "v_cvt_pk_bf16_f32 v139, v94, v95\n" \
  "s_xor_b32 s63, s50, 835584\n" \
  "v_mfma_f32_16x16x32_bf16 v[96:99], v[120:123], v[8:11], v[96:99]\n" \
  "s_add_u32 s63, s63, 25165824\n" \
  "ds_read_b128 v[48:51], v163 offset:128\n" \
  "v_mfma_f32_16x16x32_bf16 v[100:103], v[124:127], v[8:11], v[100:103]\n" \
  "buffer_store_dwordx4 v[136:139], v190, s[40:43], s63 offen sc1\n" \
  "v_mfma_f32_16x16x32_bf16 v[96:99], v[128:131], v[12:15], v[96:99]\n" \
  "ds_read_b128 v[52:55], v163 offset:18560\n" \
  "v_mfma_f32_16x16x32_bf16 v[100:103], v[132:135], v[12:15], v[100:103]\n" \
  "v_mov_b32 v191, v163\n" \
  "v_mov_b32 v192, v162\n" \
  "v_mfma_f32_16x16x32_bf16 v[88:91], v[104:107], v[16:19], 0\n" \
  "v_xor_b32 v193, 8192, v193\n" \
  "v_xor_b32 v194, 8192, v194\n" \
  "s_add_u32 s49, s49, s71\n" \
  "v_mfma_f32_16x16x32_bf16 v[92:95], v[108:111], v[16:19], 0\n" \
  "v_mfma_f32_16x16x32_bf16 v[88:91], v[112:115], v[20:23], v[88:91]\n" \
  "v_cvt_pk_bf16_f32 v104, v96, v97\n" \
  "v_cvt_pk_bf16_f32 v105, v98, v99\n" \
  "v_mfma_f32_16x16x32_bf16 v[92:95], v[116:119], v[20:23], v[92:95]\n" \
  "v_cvt_pk_bf16_f32 v106, v100, v101\n" \
  "v_cvt_pk_bf16_f32 v107, v102, v103\n" \
  "s_xor_b32 s63, s50, 836608\n" \
  "v_mfma_f32_16x16x32_bf16 v[88:91], v[120:123], v[24:27], v[88:91]\n" \
  "s_add_u32 s63, s63, 26214400\n" \
  "v_mfma_f32_16x16x32_bf16 v[92:95], v[124:127], v[24:27], v[92:95]\n" \
  "buffer_store_dwordx4 v[104:107], v190, s[40:43], s63 offen sc1\n" \
  "v_mfma_f32_16x16x32_bf16 v[88:91], v[128:131], v[28:31], v[88:91]\n" \
  "v_mfma_f32_16x16x32_bf16 v[92:95], v[132:135], v[28:31], v[92:95]\n" \
  "s_cmp_lt_u32 s48, 2\n" \
  "s_cbranch_scc1 .LU_now_%=\n" \
  ".LU_first_b_%=:\n" \
  "s_add_u32 s48, s48, 1\n" \
  "s_add_u32 s73, s73, s70\n" \
  "s_cmp_eq_u32 s73, s74\n" \
  "s_cselect_b32 s49, 2130706432, s49\n" \
  "s_cmp_le_u32 s73, s74\n" \
  "s_waitcnt lgkmcnt(0)\n" \
  "s_cbranch_scc1 .LU_loop_%=\n" \
  "s_nop 7\n" \
  "v_cvt_pk_bf16_f32 v104, v88, v89\n" \
  "v_cvt_pk_bf16_f32 v105, v90, v91\n" \
  "v_cvt_pk_bf16_f32 v106, v92, v93\n" \
  "v_cvt_pk_bf16_f32 v107, v94, v95\n" \
  "s_mov_b32 s63, s57\n" \
  "buffer_store_dwordx4 v[104:107], v190, s[40:43], s63 offen sc1\n" \
  "s_waitcnt vmcnt(0)\n" \
  "s_barrier\n" \
  "s_mov_b32 s48, s74\n" \
  "s_mov_b32 s69, 0\n" \
  ".LU_ie_%=:\n" \
  "s_cmp_ge_u32 s96, 0xc0000000\n" \
  "s_cbranch_scc1 .LU_ie_x_%=\n" \
  "buffer_load_dwordx2 v[184:185], off, s[40:43], %41 sc1\n" \
  "s_waitcnt vmcnt(0)\n" \
  "v_readfirstlane_b32 s96, v184\n" \
  "s_add_u32 s69, s69, 1\n" \
  "s_cmp_lt_u32 s69, 2097152\n" \
  "s_cbranch_scc1 .LU_ie_%=\n" \
  ".LU_ie_x_%=:\n" \
  "v_mov_b32 v168, s48\n" \
  "buffer_store_dwordx2 v[168:169], v167, s[40:43], 0 offen sc1\n" \
  "s_branch .LU_end_%=\n" \
  ".LU_e2_%=:\n" \
  "s_waitcnt vmcnt(4)\n" \
  "v_add_u32 v168, s73, v161\n" \
  "s_mov_b64 exec, 2\n" \
  "buffer_store_dwordx2 v[168:169], v167, s[40:43], 0 offen sc1\n" \
  "s_mov_b64 exec, -1\n" \
  "v_readfirstlane_b32 s96, v184\n" \
  "s_cmp_lg_u32 s48, 7\n" \
  "s_cbranch_scc1 .LU_in_x_%=\n" \
  "s_mov_b32 s69, 0\n" \
  ".LU_in_%=:\n" \
  "s_cmp_ge_u32 s96, 0xc0000000\n" \
  "s_cbranch_scc1 .LU_in_x_%=\n" \
  "buffer_load_dwordx2 v[184:185], off, s[40:43], %41 sc1\n" \
  "s_waitcnt vmcnt(0)\n" \
  "v_readfirstlane_b32 s96, v184\n" \
  "s_add_u32 s69, s69, 1\n" \
  "s_cmp_lt_u32 s69, 2097152\n" \
  "s_cbranch_scc1 .LU_in_%=\n" \
  ".LU_in_x_%=:\n" \
  "buffer_load_dwordx2 v[184:185], off, s[40:43], %41 sc1\n" \
  "s_branch .LU_e2_b_%=\n" \
  ".LU_fc_%=:\n" \
  "s_mov_b32 s69, 0\n" \
  ".LU_fc1_%=:\n" \
  "s_sleep 8\n" \
  "buffer_load_dwordx2 v[164:165], off, s[40:43], %43 sc1\n" \
  "s_waitcnt vmcnt(0)\n" \
  "v_readfirstlane_b32 s58, v164\n" \
  "v_readfirstlane_b32 s61, v165\n" \
  "s_cmp_eq_u32 s61, s55\n" \
  "s_cselect_b32 s58, s58, 0\n" \
  "s_add_u32 s58, s58, 14\n" \
  "s_cmp_lt_u32 s58, s73\n" \
  "s_cbranch_scc0 .LU_fc_b_%=\n" \
  "s_add_u32 s69, s69, 1\n" \
  "s_cmp_lt_u32 s69, 524288\n" \
  "s_cbranch_scc1 .LU_fc1_%=\n" \
  "v_mov_b32 v164, 1\n" \
  "v_mov_b32 v165, s55\n" \
  "buffer_store_dwordx2 v[164:165], off, s[40:43], %42 offset:128 sc1\n" \
  "s_waitcnt vmcnt(0)\n" \
  "v_mov_b32 v164, 100\n" \
  "s_branch .LU_fc_b_%=\n" \
  ".LU_first_%=:\n" \
  "s_waitcnt lgkmcnt(0)\n" \
  "s_nop 7\n" \
  "s_lshr_b32 s62, s49, 8\n" \
  "s_and_b32 s62, s62, 8192\n" \
  "s_xor_b32 s62, s62, s49\n" \
  "v_cvt_pk_bf16_f32 v80, v64, v65\n" \
  "v_cvt_pk_bf16_f32 v81, v66, v67\n" \
  "v_cvt_pk_bf16_f32 v82, v72, v73\n" \
  "v_cvt_pk_bf16_f32 v83, v74, v75\n" \
  "buffer_store_dwordx4 v[80:83], v190, s[40:43], s62 offen sc1\n" \
  "ds_write_b128 v193, v[80:83]\n" \
  "v_cvt_pk_bf16_f32 v84, v76, v77\n" \
  "v_cvt_pk_bf16_f32 v85, v78, v79\n" \
  "v_cvt_pk_bf16_f32 v86, v68, v69\n" \
  "v_cvt_pk_bf16_f32 v87, v70, v71\n" \
  "buffer_store_dwordx4 v[84:87], v190, s[40:43], s62 offen offset:1024 sc1\n" \
  "ds_write_b128 v193, v[84:87] offset:1024\n" \
  "s_cmp_eq_u32 s48, 3\n" \
  "s_cbranch_scc1 .LU_skip_%=\n" \
  "v_xor_b32 v160, 8, v194\n" \
  "s_waitcnt lgkmcnt(0)\n" \
  "s_barrier\n" \
  "v_xor_b32 v194, 8192, v194\n" \
  "v_xor_b32 v160, 8192, v160\n" \
  "ds_read_b64_tr_b16 v[104:105], v194\n" \
  "ds_read_b64_tr_b16 v[106:107], v194 offset:256\n" \
  "ds_read_b64_tr_b16 v[108:109], v160\n" \
  "ds_read_b64_tr_b16 v[110:111], v160 offset:256\n" \
  "ds_read_b64_tr_b16 v[112:113], v194 offset:2048\n" \
  "ds_read_b64_tr_b16 v[114:115], v194 offset:2304\n" \
  "ds_read_b64_tr_b16 v[116:117], v160 offset:2048\n" \
  "ds_read_b64_tr_b16 v[118:119], v160 offset:2304\n" \
  "ds_read_b64_tr_b16 v[120:121], v194 offset:4096\n" \
  "ds_read_b64_tr_b16 v[122:123], v194 offset:4352\n" \
  "ds_read_b64_tr_b16 v[124:125], v160 offset:4096\n" \
  "ds_read_b64_tr_b16 v[126:127], v160 offset:4352\n" \
  "ds_read_b64_tr_b16 v[128:129], v194 offset:6144\n" \
  "ds_read_b64_tr_b16 v[130:131], v194 offset:6400\n" \
  "ds_read_b64_tr_b16 v[132:133], v160 offset:6144\n" \
  "ds_read_b64_tr_b16 v[134:135], v160 offset:6400\n" \
  "v_xor_b32 v194, 8192, v194\n" \
  "s_mov_b32 s58, s73\n" \
  "s_and_b32 s58, s58, 15\n" \
  "s_lshl_b32 s58, s58, 16\n" \
  "s_xor_b32 s50, s58, s66\n" \
  "s_xor_b32 s57, s58, s59\n" \
  "s_waitcnt lgkmcnt(0)\n" \
  "s_branch .LU_phb_%=\n" \
  ".LU_skip_%=:\n" \
  "s_mov_b64 exec, 6\n" \
  "v_mov_b32 v166, -1\n" \
  "s_mov_b64 exec, -1\n" \
  "s_add_u32 s58, s48, 1\n" \
  "s_and_b32 s58, s58, 3\n" \
  "s_cselect_b32 s58, 0, 147456\n" \
  "s_sub_u32 s58, 36864, s58\n" \
  "v_add_u32 v163, s58, v191\n" \
  "v_add_u32 v162, s58, v192\n" \
  "s_cmp_lt_u32 s51, 589824\n" \
  "s_cselect_b32 s60, 36864, 0\n" \
  "s_add_u32 s51, s51, s60\n" \
  "s_add_u32 s51, s51, 36864\n" \
  "s_min_u32 s51, s51, 2322432\n" \
  "s_add_u32 s52, s52, 36864\n" \
  "s_cmp_ge_u32 s52, 163840\n" \
  "s_cselect_b32 s60, 147456, 0\n" \
  "s_sub_u32 s52, s52, s60\n" \
  "ds_read_b128 v[32:35], v163\n" \
  "ds_read_b128 v[36:39], v163 offset:18432\n" \
  "ds_read_b128 v[40:43], v162\n" \
  "ds_read_b128 v[44:47], v162 offset:18432\n" \
  "ds_read_b128 v[48:51], v163 offset:128\n" \
  "ds_read_b128 v[52:55], v163 offset:18560\n" \
  "v_mov_b32 v191, v163\n" \
  "v_mov_b32 v192, v162\n" \
  "v_xor_b32 v193, 8192, v193\n" \
  "v_xor_b32 v194, 8192, v194\n" \
  "s_add_u32 s49, s49, s71\n" \
  "s_mov_b32 s57, 2130706432\n" \
  "s_mov_b32 s69, 0\n" \
  ".LU_sk1_%=:\n" \
  "buffer_load_dwordx2 v[164:165], off, s[40:43], %43 sc1\n" \
  "s_waitcnt vmcnt(0)\n" \
  "v_readfirstlane_b32 s58, v164\n" \
  "v_readfirstlane_b32 s61, v165\n" \
  "s_cmp_eq_u32 s61, s55\n" \
  "s_cselect_b32 s58, s58, 0\n" \
  "s_add_u32 s58, s58, 14\n" \
  "s_cmp_lt_u32 s58, s73\n" \
  "s_cbranch_scc0 .LU_first_b_%=\n" \
  "v_mov_b32 v168, s73\n" \
  "s_mov_b64 exec, 2\n" \
  "buffer_store_dwordx2 v[168:169], v167, s[40:43], 0 offen sc1\n" \
  "s_mov_b64 exec, -1\n" \
  "s_sleep 16\n" \
  "s_add_u32 s69, s69, 1\n" \
  "s_cmp_lt_u32 s69, 524288\n" \
  "s_cbranch_scc1 .LU_sk1_%=\n" \
  "v_mov_b32 v164, 1\n" \
  "v_mov_b32 v165, s55\n" \
  "buffer_store_dwordx2 v[164:165], off, s[40:43], %42 offset:128 sc1\n" \
  "s_waitcnt vmcnt(0)\n" \
  "s_branch .LU_first_b_%=\n" \
  ".LU_now_%=:\n" \
  "s_nop 7\n" \
  "v_cvt_pk_bf16_f32 v104, v88, v89\n" \
  "v_cvt_pk_bf16_f32 v105, v90, v91\n" \
  "v_cvt_pk_bf16_f32 v106, v92, v93\n" \
  "v_cvt_pk_bf16_f32 v107, v94, v95\n" \
  "s_mov_b32 s63, s57\n" \
  "buffer_store_dwordx4 v[104:107], v190, s[40:43], s63 offen sc1\n" \
  "s_add_u32 s60, s73, 1\n" \
  "v_mov_b32 v168, s60\n" \
  "s_mov_b64 exec, 2\n" \
  "s_waitcnt vmcnt(0)\n" \
  "buffer_store_dwordx2 v[168:169], v167, s[40:43], 0 offen sc1\n" \
  "s_mov_b64 exec, -1\n" \
  "s_branch .LU_first_b_%=\n" \
  ".LU_end_%=:\n" \
  "s_mov_b32 %0, s55\n"
#define P_ASM_VOPS "=&s"(tag), "+{v190}"(pi.l16), "+{v191}"(pi.xr0), "+{v192}"(pi.xr1), "+{v193}"(pi.ew), "+{v194}"(pi.er), "+{v195}"(pi.uo), "+{v196}"(pi.xs0), "+{v197}"(pi.xs1), "+{v198}"(pi.xs2), "+{v199}"(pi.xs3), "+{v200}"(pi.xs4), "+{v201}"(pi.xs5), "+{v202}"(pi.xs6), "+{v203}"(pi.xs7), "+{v204}"(pi.xs8), "+{v170}"(pi.vq0), "+{v171}"(pi.vq1), "+{v172}"(pi.vq2), "+{v173}"(pi.vq3), "+{v174}"(pi.vo0), "+{v175}"(pi.vo1), "+{v176}"(pi.vo2), "+{v177}"(pi.vo3), "+{v178}"(pi.v50), "+{v179}"(pi.v51), "+{v180}"(pi.v52), "+{v181}"(pi.v53), "+{v182}"(pi.tro0), "+{v183}"(pi.tro1)
#define P_ASM_SOPS "s"(pi.ws), "s"(pi.Wa), "s"(pi.Wb), "s"(pi.x), "s"(pi.w), "s"(pi.o_a), "s"(pi.o_b), "s"(pi.o_p), "s"(pi.o_q), "s"(pi.f_a), "s"(pi.f_b), "s"(pi.f_go), "s"(pi.o_ctl), "s"(pi.f_c), "s"(pi.u8), "s"(pi.rng), "s"(pi.f_d)
#define P_ASM_CLOB "v0", "v1", "v2", "v3", "v4", "v5", "v6", "v7", "v8", "v9", "v10", "v11", "v12", "v13", "v14", "v15", "v16", "v17", "v18", "v19", "v20", "v21", "v22", "v23", "v24", "v25", "v26", "v27", "v28", "v29", "v30", "v31", "v32", "v33", "v34", "v35", "v36", "v37", "v38", "v39", "v40", "v41", "v42", "v43", "v44", "v45", "v46", "v47", "v48", "v49", "v50", "v51", "v52", "v53", "v54", "v55", "v56", "v57", "v58", "v59", "v60", "v61", "v62", "v63", "v64", "v65", "v66", "v67", "v68", "v69", "v70", "v71", "v72", "v73", "v74", "v75", "v76", "v77", "v78", "v79", "v80", "v81", "v82", "v83", "v84", "v85", "v86", "v87", "v88", "v89", "v90", "v91", "v92", "v93", "v94", "v95", "v96", "v97", "v98", "v99", "v100", "v101", "v102", "v103", "v104", "v105", "v106", "v107", "v108", "v109", "v110", "v111", "v112", "v113", "v114", "v115", "v116", "v117", "v118", "v119", "v120", "v121", "v122", "v123", "v124", "v125", "v126", "v127", "v128", "v129", "v130", "v131", "v132", "v133", "v134", "v135", "v136", "v137", "v138", "v139", "v140", "v141", "v142", "v143", "v144", "v145", "v146", "v147", "v148", "v149", "v150", "v151", "v152", "v153", "v154", "v155", "v156", "v157", "v158", "v159", "v160", "v161", "v162", "v163", "v164", "v165", "v166", "v167", "v168", "v169", "v184", "v185", "v186", "v187", "v188", "v189", "a0", "a1", "a2", "a3", "a4", "a5", "a6", "a7", "a8", "a9", "a10", "a11", "a12", "a13", "a14", "a15", "a16", "a17", "a18", "a19", "a20", "a21", "a22", "a23", "a24", "a25", "a26", "a27", "a28", "a29", "a30", "a31", "a32", "a33", "a34", "a35", "a36", "a37", "a38", "a39", "a40", "a41", "a42", "a43", "a44", "a45", "a46", "a47", "a48", "a49", "a50", "a51", "a52", "a53", "a54", "a55", "a56", "a57", "a58", "a59", "a60", "a61", "a62", "a63", "a64", "a65", "a66", "a67", "a68", "a69", "a70", "a71", "a72", "a73", "a74", "a75", "a76", "a77", "a78", "a79", "a80", "a81", "a82", "a83", "a84", "a85", "a86", "a87", "a88", "a89", "a90", "a91", "a92", "a93", "a94", "a95", "a96", "a97", "a98", "a99", "a100", "a101", "a102", "a103", "a104", "a105", "a106", "a107", "a108", "a109", "a110", "a111", "a112", "a113", "a114", "a115", "a116", "a117", "a118", "a119", "a120", "a121", "a122", "a123", "a124", "a125", "a126", "a127", "a128", "a129", "a130", "a131", "a132", "a133", "a134", "a135", "a136", "a137", "a138", "a139", "a140", "a141", "a142", "a143", "a144", "a145", "a146", "a147", "a148", "a149", "a150", "a151", "a152", "a153", "a154", "a155", "a156", "a157", "a158", "a159", "a160", "a161", "a162", "a163", "a164", "a165", "a166", "a167", "a168", "a169", "a170", "a171", "a172", "a173", "a174", "a175", "a176", "a177", "a178", "a179", "a180", "a181", "a182", "a183", "a184", "a185", "a186", "a187", "a188", "a189", "a190", "a191", "a192", "a193", "a194", "a195", "a196", "a197", "a198", "a199", "a200", "a201", "a202", "a203", "a204", "a205", "a206", "a207", "a208", "a209", "a210", "a211", "a212", "a213", "a214", "a215", "a216", "a217", "a218", "a219", "a220", "a221", "a222", "a223", "a224", "a225", "a226", "a227", "a228", "a229", "a230", "a231", "a232", "a233", "a234", "a235", "a236", "a237", "a238", "a239", "a240", "a241", "a242", "a243", "a244", "a245", "a246", "a247", "a248", "a249", "a250", "a251", "a252", "a253", "a254", "a255", "s40", "s41", "s42", "s43", "s44", "s45", "s46", "s47", "s48", "s49", "s50", "s51", "s52", "s53", "s54", "s55", "s56", "s57", "s58", "s59", "s60", "s61", "s62", "s63", "s64", "s65", "s66", "s67", "s68", "s69", "s70", "s71", "s72", "s73", "s74", "s75", "s76", "s77", "s78", "s79", "s80", "s81", "s82", "s83", "s84", "s85", "s86", "s87", "s88", "s89", "s90", "s91", "s92", "s93", "s94", "s95", "s96", "s97", "s98", "s99", "vcc", "scc", "memory"
#define O_ASM_TEXT \
  "s_mov_b64 s[40:41], %9\n" \
  "s_and_b32 s41, s41, 0xffff\n" \
  "s_mov_b32 s42, 255852544\n" \
  "s_mov_b32 s43, 0x00020000\n" \
  "s_mov_b64 s[44:45], %10\n" \
  "s_and_b32 s45, s45, 0xffff\n" \
  "s_mov_b32 s46, 4718592\n" \
  "s_mov_b32 s47, 0x00020000\n" \
  "s_mul_i32 s62, %11, 24576\n" \
  "s_add_u32 s60, %13, 0\n" \
  "s_add_u32 m0, s62, 0\n" \
  "s_nop 0\n" \
  "buffer_load_dwordx4 v194, s[44:47], s60 offen lds\n" \
  "s_add_u32 m0, s62, 1024\n" \
  "s_nop 0\n" \
  "buffer_load_dwordx4 v195, s[44:47], s60 offen lds\n" \
  "s_add_u32 m0, s62, 2048\n" \
  "s_nop 0\n" \
  "buffer_load_dwordx4 v196, s[44:47], s60 offen lds\n" \
  "s_add_u32 s60, %13, 18432\n" \
  "s_add_u32 m0, s62, 3072\n" \
  "s_nop 0\n" \
  "buffer_load_dwordx4 v194, s[44:47], s60 offen lds\n" \
  "s_add_u32 m0, s62, 4096\n" \
  "s_nop 0\n" \
  "buffer_load_dwordx4 v195, s[44:47], s60 offen lds\n" \
  "s_add_u32 m0, s62, 5120\n" \
  "s_nop 0\n" \
  "buffer_load_dwordx4 v196, s[44:47], s60 offen lds\n" \
  "s_add_u32 s60, %13, 36864\n" \
  "s_add_u32 m0, s62, 6144\n" \
  "s_nop 0\n" \
  "buffer_load_dwordx4 v194, s[44:47], s60 offen lds\n" \
  "s_add_u32 m0, s62, 7168\n" \
  "s_nop 0\n" \
  "buffer_load_dwordx4 v195, s[44:47], s60 offen lds\n" \
  "s_add_u32 m0, s62, 8192\n" \
  "s_nop 0\n" \
  "buffer_load_dwordx4 v196, s[44:47], s60 offen lds\n" \
  "s_add_u32 s60, %13, 55296\n" \
  "s_add_u32 m0, s62, 9216\n" \
  "s_nop 0\n" \
  "buffer_load_dwordx4 v194, s[44:47], s60 offen lds\n" \
  "s_add_u32 m0, s62, 10240\n" \
  "s_nop 0\n" \
  "buffer_load_dwordx4 v195, s[44:47], s60 offen lds\n" \
  "s_add_u32 m0, s62, 11264\n" \
  "s_nop 0\n" \
  "buffer_load_dwordx4 v196, s[44:47], s60 offen lds\n" \
  "s_add_u32 s60, %13, 73728\n" \
  "s_add_u32 m0, s62, 12288\n" \
  "s_nop 0\n" \
  "buffer_load_dwordx4 v194, s[44:47], s60 offen lds\n" \
  "s_add_u32 m0, s62, 13312\n" \
  "s_nop 0\n" \
  "buffer_load_dwordx4 v195, s[44:47], s60 offen lds\n" \
  "s_add_u32 m0, s62, 14336\n" \
  "s_nop 0\n" \
  "buffer_load_dwordx4 v196, s[44:47], s60 offen lds\n" \
  "s_add_u32 s60, %13, 92160\n" \
  "s_add_u32 m0, s62, 15360\n" \
  "s_nop 0\n" \
  "buffer_load_dwordx4 v194, s[44:47], s60 offen lds\n" \
  "s_add_u32 m0, s62, 16384\n" \
  "s_nop 0\n" \
  "buffer_load_dwordx4 v195, s[44:47], s60 offen lds\n" \
  "s_add_u32 m0, s62, 17408\n" \
  "s_nop 0\n" \
  "buffer_load_dwordx4 v196, s[44:47], s60 offen lds\n" \
  "s_add_u32 s60, %13, 110592\n" \
  "s_add_u32 m0, s62, 18432\n" \
  "s_nop 0\n" \
  "buffer_load_dwordx4 v194, s[44:47], s60 offen lds\n" \
  "s_add_u32 m0, s62, 19456\n" \
  "s_nop 0\n" \
  "buffer_load_dwordx4 v195, s[44:47], s60 offen lds\n" \
  "s_add_u32 m0, s62, 20480\n" \
  "s_nop 0\n" \
  "buffer_load_dwordx4 v196, s[44:47], s60 offen lds\n" \
  "s_add_u32 s60, %13, 129024\n" \
  "s_add_u32 m0, s62, 21504\n" \
  "s_nop 0\n" \
  "buffer_load_dwordx4 v194, s[44:47], s60 offen lds\n" \
  "s_add_u32 m0, s62, 22528\n" \
  "s_nop 0\n" \
  "buffer_load_dwordx4 v195, s[44:47], s60 offen lds\n" \
  "s_add_u32 m0, s62, 23552\n" \
  "s_nop 0\n" \
  "buffer_load_dwordx4 v196, s[44:47], s60 offen lds\n" \
  "s_waitcnt vmcnt(0)\n" \
  "ds_read_b64_tr_b16 a[0:1], v197\n" \
  "ds_read_b64_tr_b16 a[2:3], v197 offset:3072\n" \
  "ds_read_b64_tr_b16 a[4:5], v197 offset:32\n" \
  "ds_read_b64_tr_b16 a[6:7], v197 offset:3104\n" \
  "ds_read_b64_tr_b16 a[8:9], v197 offset:64\n" \
  "ds_read_b64_tr_b16 a[10:11], v197 offset:3136\n" \
  "ds_read_b64_tr_b16 a[12:13], v197 offset:96\n" \
  "ds_read_b64_tr_b16 a[14:15], v197 offset:3168\n" \
  "ds_read_b64_tr_b16 a[16:17], v197 offset:128\n" \
  "ds_read_b64_tr_b16 a[18:19], v197 offset:3200\n" \
  "ds_read_b64_tr_b16 a[20:21], v197 offset:160\n" \
  "ds_read_b64_tr_b16 a[22:23], v197 offset:3232\n" \
  "ds_read_b64_tr_b16 a[24:25], v197 offset:6144\n" \
  "ds_read_b64_tr_b16 a[26:27], v197 offset:9216\n" \
  "ds_read_b64_tr_b16 a[28:29], v197 offset:6176\n" \
  "ds_read_b64_tr_b16 a[30:31], v197 offset:9248\n" \
  "ds_read_b64_tr_b16 a[32:33], v197 offset:6208\n" \
  "ds_read_b64_tr_b16 a[34:35], v197 offset:9280\n" \
  "ds_read_b64_tr_b16 a[36:37], v197 offset:6240\n" \
  "ds_read_b64_tr_b16 a[38:39], v197 offset:9312\n" \
  "ds_read_b64_tr_b16 a[40:41], v197 offset:6272\n" \
  "ds_read_b64_tr_b16 a[42:43], v197 offset:9344\n" \
  "ds_read_b64_tr_b16 a[44:45], v197 offset:6304\n" \
  "ds_read_b64_tr_b16 a[46:47], v197 offset:9376\n" \
  "ds_read_b64_tr_b16 a[48:49], v197 offset:12288\n" \
  "ds_read_b64_tr_b16 a[50:51], v197 offset:15360\n" \
  "ds_read_b64_tr_b16 a[52:53], v197 offset:12320\n" \
  "ds_read_b64_tr_b16 a[54:55], v197 offset:15392\n" \
  "ds_read_b64_tr_b16 a[56:57], v197 offset:12352\n" \
  "ds_read_b64_tr_b16 a[58:59], v197 offset:15424\n" \
  "ds_read_b64_tr_b16 a[60:61], v197 offset:12384\n" \
  "ds_read_b64_tr_b16 a[62:63], v197 offset:15456\n" \
  "ds_read_b64_tr_b16 a[64:65], v197 offset:12416\n" \
  "ds_read_b64_tr_b16 a[66:67], v197 offset:15488\n" \
  "ds_read_b64_tr_b16 a[68:69], v197 offset:12448\n" \
  "ds_read_b64_tr_b16 a[70:71], v197 offset:15520\n" \
  "ds_read_b64_tr_b16 a[72:73], v197 offset:18432\n" \
  "ds_read_b64_tr_b16 a[74:75], v197 offset:21504\n" \
  "ds_read_b64_tr_b16 a[76:77], v197 offset:18464\n" \
  "ds_read_b64_tr_b16 a[78:79], v197 offset:21536\n" \
  "ds_read_b64_tr_b16 a[80:81], v197 offset:18496\n" \
  "ds_read_b64_tr_b16 a[82:83], v197 offset:21568\n" \
  "ds_read_b64_tr_b16 a[84:85], v197 offset:18528\n" \
  "ds_read_b64_tr_b16 a[86:87], v197 offset:21600\n" \
  "ds_read_b64_tr_b16 a[88:89], v197 offset:18560\n" \
  "ds_read_b64_tr_b16 a[90:91], v197 offset:21632\n" \
  "ds_read_b64_tr_b16 a[92:93], v197 offset:18592\n" \
  "ds_read_b64_tr_b16 a[94:95], v197 offset:21664\n" \
  "s_waitcnt lgkmcnt(0)\n" \
  "s_add_u32 s60, %13, 147456\n" \
  "s_add_u32 m0, s62, 0\n" \
  "s_nop 0\n" \
  "buffer_load_dwordx4 v194, s[44:47], s60 offen lds\n" \
  "s_add_u32 m0, s62, 1024\n" \
  "s_nop 0\n" \
  "buffer_load_dwordx4 v195, s[44:47], s60 offen lds\n" \
  "s_add_u32 m0, s62, 2048\n" \
  "s_nop 0\n" \
  "buffer_load_dwordx4 v196, s[44:47], s60 offen lds\n" \
  "s_add_u32 s60, %13, 165888\n" \
  "s_add_u32 m0, s62, 3072\n" \
  "s_nop 0\n" \
  "buffer_load_dwordx4 v194, s[44:47], s60 offen lds\n" \
  "s_add_u32 m0, s62, 4096\n" \
  "s_nop 0\n" \
  "buffer_load_dwordx4 v195, s[44:47], s60 offen lds\n" \
  "s_add_u32 m0, s62, 5120\n" \
  "s_nop 0\n" \
  "buffer_load_dwordx4 v196, s[44:47], s60 offen lds\n" \
  "s_add_u32 s60, %13, 184320\n" \
  "s_add_u32 m0, s62, 6144\n" \
  "s_nop 0\n" \
  "buffer_load_dwordx4 v194, s[44:47], s60 offen lds\n" \
  "s_add_u32 m0, s62, 7168\n" \
  "s_nop 0\n" \
  "buffer_load_dwordx4 v195, s[44:47], s60 offen lds\n" \
  "s_add_u32 m0, s62, 8192\n" \
  "s_nop 0\n" \
  "buffer_load_dwordx4 v196, s[44:47], s60 offen lds\n" \
  "s_add_u32 s60, %13, 202752\n" \
  "s_add_u32 m0, s62, 9216\n" \
  "s_nop 0\n" \
  "buffer_load_dwordx4 v194, s[44:47], s60 offen lds\n" \
  "s_add_u32 m0, s62, 10240\n" \
  "s_nop 0\n" \
  "buffer_load_dwordx4 v195, s[44:47], s60 offen lds\n" \
  "s_add_u32 m0, s62, 11264\n" \
  "s_nop 0\n" \
  "buffer_load_dwordx4 v196, s[44:47], s60 offen lds\n" \
  "s_add_u32 s60, %13, 221184\n" \
  "s_add_u32 m0, s62, 12288\n" \
  "s_nop 0\n" \
  "buffer_load_dwordx4 v194, s[44:47], s60 offen lds\n" \
  "s_add_u32 m0, s62, 13312\n" \
  "s_nop 0\n" \
  "buffer_load_dwordx4 v195, s[44:47], s60 offen lds\n" \
  "s_add_u32 m0, s62, 14336\n" \
  "s_nop 0\n" \
  "buffer_load_dwordx4 v196, s[44:47], s60 offen lds\n" \
  "s_add_u32 s60, %13, 239616\n" \
  "s_add_u32 m0, s62, 15360\n" \
  "s_nop 0\n" \
  "buffer_load_dwordx4 v194, s[44:47], s60 offen lds\n" \
  "s_add_u32 m0, s62, 16384\n" \
  "s_nop 0\n" \
  "buffer_load_dwordx4 v195, s[44:47], s60 offen lds\n" \
  "s_add_u32 m0, s62, 17408\n" \
  "s_nop 0\n" \
  "buffer_load_dwordx4 v196, s[44:47], s60 offen lds\n" \
  "s_add_u32 s60, %13, 258048\n" \
  "s_add_u32 m0, s62, 18432\n" \
  "s_nop 0\n" \
  "buffer_load_dwordx4 v194, s[44:47], s60 offen lds\n" \
  "s_add_u32 m0, s62, 19456\n" \
  "s_nop 0\n" \
  "buffer_load_dwordx4 v195, s[44:47], s60 offen lds\n" \
  "s_add_u32 m0, s62, 20480\n" \
  "s_nop 0\n" \
  "buffer_load_dwordx4 v196, s[44:47], s60 offen lds\n" \
  "s_add_u32 s60, %13, 276480\n" \
  "s_add_u32 m0, s62, 21504\n" \
  "s_nop 0\n" \
  "buffer_load_dwordx4 v194, s[44:47], s60 offen lds\n" \
  "s_add_u32 m0, s62, 22528\n" \
  "s_nop 0\n" \
  "buffer_load_dwordx4 v195, s[44:47], s60 offen lds\n" \
  "s_add_u32 m0, s62, 23552\n" \
  "s_nop 0\n" \
  "buffer_load_dwordx4 v196, s[44:47], s60 offen lds\n" \
  "s_waitcnt vmcnt(0)\n" \
  "ds_read_b64_tr_b16 a[96:97], v197\n" \
  "ds_read_b64_tr_b16 a[98:99], v197 offset:3072\n" \
  "ds_read_b64_tr_b16 a[100:101], v197 offset:32\n" \
  "ds_read_b64_tr_b16 a[102:103], v197 offset:3104\n" \
  "ds_read_b64_tr_b16 a[104:105], v197 offset:64\n" \
  "ds_read_b64_tr_b16 a[106:107], v197 offset:3136\n" \
  "ds_read_b64_tr_b16 a[108:109], v197 offset:96\n" \
  "ds_read_b64_tr_b16 a[110:111], v197 offset:3168\n" \
  "ds_read_b64_tr_b16 a[112:113], v197 offset:128\n" \
  "ds_read_b64_tr_b16 a[114:115], v197 offset:3200\n" \
  "ds_read_b64_tr_b16 a[116:117], v197 offset:160\n" \
  "ds_read_b64_tr_b16 a[118:119], v197 offset:3232\n" \
  "ds_read_b64_tr_b16 a[120:121], v197 offset:6144\n" \
  "ds_read_b64_tr_b16 a[122:123], v197 offset:9216\n" \
  "ds_read_b64_tr_b16 a[124:125], v197 offset:6176\n" \
  "ds_read_b64_tr_b16 a[126:127], v197 offset:9248\n" \
  "ds_read_b64_tr_b16 a[128:129], v197 offset:6208\n" \
  "ds_read_b64_tr_b16 a[130:131], v197 offset:9280\n" \
  "ds_read_b64_tr_b16 a[132:133], v197 offset:6240\n" \
  "ds_read_b64_tr_b16 a[134:135], v197 offset:9312\n" \
  "ds_read_b64_tr_b16 a[136:137], v197 offset:6272\n" \
  "ds_read_b64_tr_b16 a[138:139], v197 offset:9344\n" \
  "ds_read_b64_tr_b16 a[140:141], v197 offset:6304\n" \
  "ds_read_b64_tr_b16 a[142:143], v197 offset:9376\n" \
  "ds_read_b64_tr_b16 a[144:145], v197 offset:12288\n" \
  "ds_read_b64_tr_b16 a[146:147], v197 offset:15360\n" \
  "ds_read_b64_tr_b16 a[148:149], v197 offset:12320\n" \
  "ds_read_b64_tr_b16 a[150:151], v197 offset:15392\n" \
  "ds_read_b64_tr_b16 a[152:153], v197 offset:12352\n" \
  "ds_read_b64_tr_b16 a[154:155], v197 offset:15424\n" \
  "ds_read_b64_tr_b16 a[156:157], v197 offset:12384\n" \
  "ds_read_b64_tr_b16 a[158:159], v197 offset:15456\n" \
  "ds_read_b64_tr_b16 a[160:161], v197 offset:12416\n" \
  "ds_read_b64_tr_b16 a[162:163], v197 offset:15488\n" \
  "ds_read_b64_tr_b16 a[164:165], v197 offset:12448\n" \
  "ds_read_b64_tr_b16 a[166:167], v197 offset:15520\n" \
  "ds_read_b64_tr_b16 a[168:169], v197 offset:18432\n" \
  "ds_read_b64_tr_b16 a[170:171], v197 offset:21504\n" \
  "ds_read_b64_tr_b16 a[172:173], v197 offset:18464\n" \
  "ds_read_b64_tr_b16 a[174:175], v197 offset:21536\n" \
  "ds_read_b64_tr_b16 a[176:177], v197 offset:18496\n" \
  "ds_read_b64_tr_b16 a[178:179], v197 offset:21568\n" \
  "ds_read_b64_tr_b16 a[180:181], v197 offset:18528\n" \
  "ds_read_b64_tr_b16 a[182:183], v197 offset:21600\n" \
  "ds_read_b64_tr_b16 a[184:185], v197 offset:18560\n" \
  "ds_read_b64_tr_b16 a[186:187], v197 offset:21632\n" \
  "ds_read_b64_tr_b16 a[188:189], v197 offset:18592\n" \
  "ds_read_b64_tr_b16 a[190:191], v197 offset:21664\n" \
  "s_waitcnt lgkmcnt(0)\n" \
  "s_barrier\n" \
  "s_mov_b32 s49, %14\n" \
  "s_mov_b32 s50, %15\n" \
  "s_mov_b32 s48, 0\n" \
  "s_mov_b32 s53, 2130706432\n" \
  "s_mov_b32 s94, 0\n" \
  "v_mov_b32 v187, %12\n" \
  "s_branch .LO_fl_%=\n" \
  ".LO_fl_b_%=:\n" \
  "s_mov_b32 s69, 0\n" \
  ".LO_p0_%=:\n" \
  "buffer_load_dwordx2 v[188:189], v193, s[40:43], 0 offen sc1\n" \
  "s_waitcnt vmcnt(0)\n" \
  "v_cmp_ne_u32 vcc, %12, v189\n" \
  "s_nop 1\n" \
  "v_cndmask_b32 v188, v188, 0, vcc\n" \
  "v_cmp_ge_i32 vcc, s94, v188\n" \
  "s_cbranch_vccz .LO_p0_x_%=\n" \
  "s_sleep 2\n" \
  "s_add_u32 s69, s69, 1\n" \
  "s_and_b32 s63, s69, 1023\n" \
  "s_cbranch_scc1 .LO_p0_%=\n" \
  "buffer_load_dwordx2 v[188:189], off, s[40:43], %17 offset:128 sc1\n" \
  "s_waitcnt vmcnt(0)\n" \
  "v_readfirstlane_b32 s63, v189\n" \
  "s_cmp_eq_u32 s63, %12\n" \
  "s_cbranch_scc1 .LO_p0_x_%=\n" \
  "s_cmp_le_u32 s69, 2097152\n" \
  "s_cbranch_scc1 .LO_p0_%=\n" \
  "v_mov_b32 v188, 1\n" \
  "v_mov_b32 v189, %12\n" \
  "buffer_store_dwordx2 v[188:189], off, s[40:43], %17 offset:128 sc1\n" \
  "s_waitcnt vmcnt(0)\n" \
  ".LO_p0_x_%=:\n" \
  "s_lshr_b32 s76, s49, 8\n" \
  "s_and_b32 s76, s76, 8192\n" \
  "s_xor_b32 s76, s76, s49\n" \
  "s_add_u32 s77, s76, 4096\n" \
  "s_add_u32 s78, s76, 16384\n" \
  "s_add_u32 s79, s76, 20480\n" \
  "s_add_u32 s49, s49, 524288\n" \
  "buffer_load_dwordx4 v[0:3], v190, s[40:43], s76 offen sc1\n" \
  "buffer_load_dwordx4 v[8:11], v190, s[40:43], s76 offen offset:2048 sc1\n" \
  "buffer_load_dwordx4 v[16:19], v190, s[40:43], s77 offen sc1\n" \
  "buffer_load_dwordx4 v[24:27], v190, s[40:43], s77 offen offset:2048 sc1\n" \
  "buffer_load_dwordx4 v[32:35], v190, s[40:43], s78 offen sc1\n" \
  "buffer_load_dwordx4 v[40:43], v190, s[40:43], s78 offen offset:2048 sc1\n" \
  "buffer_load_dwordx4 v[48:51], v190, s[40:43], s79 offen sc1\n" \
  "buffer_load_dwordx4 v[56:59], v190, s[40:43], s79 offen offset:2048 sc1\n" \
  "buffer_load_dwordx4 v[4:7], v190, s[40:43], s76 offen offset:1024 sc1\n" \
  "buffer_load_dwordx4 v[12:15], v190, s[40:43], s76 offen offset:3072 sc1\n" \
  "buffer_load_dwordx4 v[20:23], v190, s[40:43], s77 offen offset:1024 sc1\n" \
  "buffer_load_dwordx4 v[28:31], v190, s[40:43], s77 offen offset:3072 sc1\n" \
  "buffer_load_dwordx4 v[36:39], v190, s[40:43], s78 offen offset:1024 sc1\n" \
  "buffer_load_dwordx4 v[44:47], v190, s[40:43], s78 offen offset:3072 sc1\n" \
  "buffer_load_dwordx4 v[52:55], v190, s[40:43], s79 offen offset:1024 sc1\n" \
  "buffer_load_dwordx4 v[60:63], v190, s[40:43], s79 offen offset:3072 sc1\n" \
  "s_lshr_b32 s76, s49, 8\n" \
  "s_and_b32 s76, s76, 8192\n" \
  "s_xor_b32 s76, s76, s49\n" \
  "s_add_u32 s77, s76, 4096\n" \
  "s_add_u32 s78, s76, 16384\n" \
  "s_add_u32 s79, s76, 20480\n" \
  "s_add_u32 s49, s49, 524288\n" \
  "s_mov_b32 s94, 1\n" \
  "s_waitcnt vmcnt(0)\n" \
  ".LO_loop_%=:\n" \
  "s_waitcnt vmcnt(19)\n" \
  "v_cmp_ne_u32 vcc, %12, v189\n" \
  "s_nop 1\n" \
  "v_cndmask_b32 v188, v188, 0, vcc\n" \
  "v_cmp_ge_i32 vcc, s94, v188\n" \
  "s_mov_b32 s54, 0\n" \
  "s_cbranch_vccnz .LO_pl_%=\n" \
  ".LO_pl_b_%=:\n" \
  "s_waitcnt vmcnt(18)\n" \
  "v_mfma_f32_16x16x32_bf16 v[64:67], a[0:3], v[0:3], 0\n" \
  "buffer_load_dwordx2 v[188:189], v193, s[40:43], 0 offen sc1\n" \
  "v_mfma_f32_16x16x32_bf16 v[68:71], a[4:7], v[0:3], 0\n" \
  "s_waitcnt lgkmcnt(3)\n" \
  "v_mfma_f32_16x16x32_bf16 v[72:75], a[8:11], v[0:3], 0\n" \
  "ds_write_b128 v191, v[88:91] offset:6144\n" \
  "s_waitcnt lgkmcnt(3)\n" \
  "v_mfma_f32_16x16x32_bf16 v[76:79], a[12:15], v[0:3], 0\n" \
  "s_waitcnt lgkmcnt(2)\n" \
  "v_mfma_f32_16x16x32_bf16 v[80:83], a[16:19], v[0:3], 0\n" \
  "s_waitcnt lgkmcnt(1)\n" \
  "v_mfma_f32_16x16x32_bf16 v[84:87], a[20:23], v[0:3], 0\n" \
  "buffer_load_dwordx4 v[0:3], v190, s[40:43], s76 offen sc1\n" \
  "s_waitcnt vmcnt(19)\n" \
  "v_mfma_f32_16x16x32_bf16 v[64:67], a[24:27], v[8:11], v[64:67]\n" \
  "v_mfma_f32_16x16x32_bf16 v[68:71], a[28:31], v[8:11], v[68:71]\n" \
  "v_mfma_f32_16x16x32_bf16 v[72:75], a[32:35], v[8:11], v[72:75]\n" \
  "ds_write_b128 v191, v[92:95] offset:7168\n" \
  "v_mfma_f32_16x16x32_bf16 v[76:79], a[36:39], v[8:11], v[76:79]\n" \
  "v_mfma_f32_16x16x32_bf16 v[80:83], a[40:43], v[8:11], v[80:83]\n" \
  "v_mfma_f32_16x16x32_bf16 v[84:87], a[44:47], v[8:11], v[84:87]\n" \
  "buffer_load_dwordx4 v[8:11], v190, s[40:43], s76 offen offset:2048 sc1\n" \
  "s_waitcnt vmcnt(19)\n" \
  "v_mfma_f32_16x16x32_bf16 v[64:67], a[48:51], v[16:19], v[64:67]\n" \
  "v_mfma_f32_16x16x32_bf16 v[68:71], a[52:55], v[16:19], v[68:71]\n" \
  "v_mfma_f32_16x16x32_bf16 v[72:75], a[56:59], v[16:19], v[72:75]\n" \
  "ds_write_b128 v191, v[96:99] offset:8192\n" \
  "v_mfma_f32_16x16x32_bf16 v[76:79], a[60:63], v[16:19], v[76:79]\n" \
  "v_mfma_f32_16x16x32_bf16 v[80:83], a[64:67], v[16:19], v[80:83]\n" \
  "v_mfma_f32_16x16x32_bf16 v[84:87], a[68:71], v[16:19], v[84:87]\n" \
  "buffer_load_dwordx4 v[16:19], v190, s[40:43], s77 offen sc1\n" \
  "s_waitcnt vmcnt(19)\n" \
  "v_mfma_f32_16x16x32_bf16 v[64:67], a[72:75], v[24:27], v[64:67]\n" \
  "v_mfma_f32_16x16x32_bf16 v[68:71], a[76:79], v[24:27], v[68:71]\n" \
  "v_mfma_f32_16x16x32_bf16 v[72:75], a[80:83], v[24:27], v[72:75]\n" \
  "ds_write_b128 v191, v[100:103] offset:9216\n" \
  "v_mfma_f32_16x16x32_bf16 v[76:79], a[84:87], v[24:27], v[76:79]\n" \
  "v_mfma_f32_16x16x32_bf16 v[80:83], a[88:91], v[24:27], v[80:83]\n" \
  "v_mfma_f32_16x16x32_bf16 v[84:87], a[92:95], v[24:27], v[84:87]\n" \
  "buffer_load_dwordx4 v[24:27], v190, s[40:43], s77 offen offset:2048 sc1\n" \
  "s_waitcnt vmcnt(19)\n" \
  "v_mfma_f32_16x16x32_bf16 v[64:67], a[96:99], v[32:35], v[64:67]\n" \
  "v_mfma_f32_16x16x32_bf16 v[68:71], a[100:103], v[32:35], v[68:71]\n" \
  "v_mfma_f32_16x16x32_bf16 v[72:75], a[104:107], v[32:35], v[72:75]\n" \
  "ds_write_b128 v191, v[104:107] offset:10240\n" \
  "v_mfma_f32_16x16x32_bf16 v[76:79], a[108:111], v[32:35], v[76:79]\n" \
  "v_mfma_f32_16x16x32_bf16 v[80:83], a[112:115], v[32:35], v[80:83]\n" \
  "v_mfma_f32_16x16x32_bf16 v[84:87], a[116:119], v[32:35], v[84:87]\n" \
  "buffer_load_dwordx4 v[32:35], v190, s[40:43], s78 offen sc1\n" \
  "s_waitcnt vmcnt(19)\n" \
  "v_mfma_f32_16x16x32_bf16 v[64:67], a[120:123], v[40:43], v[64:67]\n" \
  "v_mfma_f32_16x16x32_bf16 v[68:71], a[124:127], v[40:43], v[68:71]\n" \
  "v_mfma_f32_16x16x32_bf16 v[72:75], a[128:131], v[40:43], v[72:75]\n" \
  "ds_write_b128 v191, v[108:111] offset:11264\n" \
  "v_mfma_f32_16x16x32_bf16 v[76:79], a[132:135], v[40:43], v[76:79]\n" \
  "v_xor_b32 v191, 65536, v191\n" \
  "v_mfma_f32_16x16x32_bf16 v[80:83], a[136:139], v[40:43], v[80:83]\n" \
  "v_mfma_f32_16x16x32_bf16 v[84:87], a[140:143], v[40:43], v[84:87]\n" \
  "buffer_load_dwordx4 v[40:43], v190, s[40:43], s78 offen offset:2048 sc1\n" \
  "s_waitcnt vmcnt(19)\n" \
  "v_mfma_f32_16x16x32_bf16 v[64:67], a[144:147], v[48:51], v[64:67]\n" \
  "v_mfma_f32_16x16x32_bf16 v[68:71], a[148:151], v[48:51], v[68:71]\n" \
  "v_mfma_f32_16x16x32_bf16 v[72:75], a[152:155], v[48:51], v[72:75]\n" \
  "v_mfma_f32_16x16x32_bf16 v[76:79], a[156:159], v[48:51], v[76:79]\n" \
  "s_waitcnt lgkmcnt(0)\n" \
  "s_waitcnt vmcnt(8)\n" \
  "s_barrier\n" \
  "v_mfma_f32_16x16x32_bf16 v[80:83], a[160:163], v[48:51], v[80:83]\n" \
  "ds_read_b128 v[120:123], v192\n" \
  "v_mfma_f32_16x16x32_bf16 v[84:87], a[164:167], v[48:51], v[84:87]\n" \
  "buffer_load_dwordx4 v[48:51], v190, s[40:43], s79 offen sc1\n" \
  "v_mfma_f32_16x16x32_bf16 v[64:67], a[168:171], v[56:59], v[64:67]\n" \
  "ds_read_b128 v[112:115], v192 offset:12288\n" \
  "v_mfma_f32_16x16x32_bf16 v[68:71], a[172:175], v[56:59], v[68:71]\n" \
  "s_add_i32 s60, s48, -1\n" \
  "v_mov_b32 v186, s60\n" \
  "s_mov_b64 exec, 1\n" \
  "buffer_store_dwordx2 v[186:187], off, s[40:43], %16 sc1\n" \
  "s_mov_b64 exec, -1\n" \
  "v_mfma_f32_16x16x32_bf16 v[72:75], a[176:179], v[56:59], v[72:75]\n" \
  "ds_read_b128 v[116:119], v192 offset:24576\n" \
  "v_mfma_f32_16x16x32_bf16 v[76:79], a[180:183], v[56:59], v[76:79]\n" \
  "ds_read_b128 v[124:127], v192 offset:36864\n" \
  "v_mfma_f32_16x16x32_bf16 v[80:83], a[184:187], v[56:59], v[80:83]\n" \
  "ds_read_b128 v[128:131], v192 offset:1024\n" \
  "v_mfma_f32_16x16x32_bf16 v[84:87], a[188:191], v[56:59], v[84:87]\n" \
  "buffer_load_dwordx4 v[56:59], v190, s[40:43], s79 offen offset:2048 sc1\n" \
  "v_mfma_f32_16x16x32_bf16 v[88:91], a[0:3], v[4:7], 0\n" \
  "ds_read_b128 v[132:135], v192 offset:13312\n" \
  "v_mfma_f32_16x16x32_bf16 v[92:95], a[4:7], v[4:7], 0\n" \
  "ds_read_b128 v[136:139], v192 offset:25600\n" \
  "v_mfma_f32_16x16x32_bf16 v[96:99], a[8:11], v[4:7], 0\n" \
  "ds_write_b128 v191, v[64:67]\n" \
  "v_mfma_f32_16x16x32_bf16 v[100:103], a[12:15], v[4:7], 0\n" \
  "ds_read_b128 v[140:143], v192 offset:37888\n" \
  "v_mfma_f32_16x16x32_bf16 v[104:107], a[16:19], v[4:7], 0\n" \
  "ds_read_b128 v[144:147], v192 offset:2048\n" \
  "v_mfma_f32_16x16x32_bf16 v[108:111], a[20:23], v[4:7], 0\n" \
  "buffer_load_dwordx4 v[4:7], v190, s[40:43], s76 offen offset:1024 sc1\n" \
  "v_mfma_f32_16x16x32_bf16 v[88:91], a[24:27], v[12:15], v[88:91]\n" \
  "ds_read_b128 v[148:151], v192 offset:14336\n" \
  "v_mfma_f32_16x16x32_bf16 v[92:95], a[28:31], v[12:15], v[92:95]\n" \
  "ds_read_b128 v[152:155], v192 offset:26624\n" \
  "v_mfma_f32_16x16x32_bf16 v[96:99], a[32:35], v[12:15], v[96:99]\n" \
  "ds_write_b128 v191, v[68:71] offset:1024\n" \
  "v_mfma_f32_16x16x32_bf16 v[100:103], a[36:39], v[12:15], v[100:103]\n" \
  "ds_read_b128 v[156:159], v192 offset:38912\n" \
  "v_mfma_f32_16x16x32_bf16 v[104:107], a[40:43], v[12:15], v[104:107]\n" \
  "s_waitcnt lgkmcnt(12)\n" \
  "v_add_f32 v120, v120, v112\n" \
  "v_add_f32 v121, v121, v113\n" \
  "v_mfma_f32_16x16x32_bf16 v[108:111], a[44:47], v[12:15], v[108:111]\n" \
  "buffer_load_dwordx4 v[12:15], v190, s[40:43], s76 offen offset:3072 sc1\n" \
  "v_mfma_f32_16x16x32_bf16 v[88:91], a[48:51], v[20:23], v[88:91]\n" \
  "v_add_f32 v122, v122, v114\n" \
  "v_add_f32 v123, v123, v115\n" \
  "v_mfma_f32_16x16x32_bf16 v[92:95], a[52:55], v[20:23], v[92:95]\n" \
  "s_waitcnt lgkmcnt(10)\n" \
  "v_add_f32 v116, v116, v124\n" \
  "v_add_f32 v117, v117, v125\n" \
  "v_mfma_f32_16x16x32_bf16 v[96:99], a[56:59], v[20:23], v[96:99]\n" \
  "ds_write_b128 v191, v[72:75] offset:2048\n" \
  "v_mfma_f32_16x16x32_bf16 v[100:103], a[60:63], v[20:23], v[100:103]\n" \
  "v_add_f32 v118, v118, v126\n" \
  "v_add_f32 v119, v119, v127\n" \
  "v_mfma_f32_16x16x32_bf16 v[104:107], a[64:67], v[20:23], v[104:107]\n" \
  "v_add_f32 v120, v120, v116\n" \
  "v_add_f32 v121, v121, v117\n" \
  "v_mfma_f32_16x16x32_bf16 v[108:111], a[68:71], v[20:23], v[108:111]\n" \
  "buffer_load_dwordx4 v[20:23], v190, s[40:43], s77 offen offset:1024 sc1\n" \
  "v_mfma_f32_16x16x32_bf16 v[88:91], a[72:75], v[28:31], v[88:91]\n" \
  "v_add_f32 v122, v122, v118\n" \
  "v_add_f32 v123, v123, v119\n" \
  "v_mfma_f32_16x16x32_bf16 v[92:95], a[76:79], v[28:31], v[92:95]\n" \
  "v_cvt_pk_bf16_f32 v112, v120, v121\n" \
  "v_cvt_pk_bf16_f32 v113, v122, v123\n" \
  "v_mfma_f32_16x16x32_bf16 v[96:99], a[80:83], v[28:31], v[96:99]\n" \
  "ds_write_b128 v191, v[76:79] offset:3072\n" \
  "v_mfma_f32_16x16x32_bf16 v[100:103], a[84:87], v[28:31], v[100:103]\n" \
  "s_waitcnt lgkmcnt(10)\n" \
  "v_add_f32 v128, v128, v132\n" \
  "v_add_f32 v129, v129, v133\n" \
  "v_mfma_f32_16x16x32_bf16 v[104:107], a[88:91], v[28:31], v[104:107]\n" \
  "v_add_f32 v130, v130, v134\n" \
  "v_add_f32 v131, v131, v135\n" \
  "v_mfma_f32_16x16x32_bf16 v[108:111], a[92:95], v[28:31], v[108:111]\n" \
  "buffer_load_dwordx4 v[28:31], v190, s[40:43], s77 offen offset:3072 sc1\n" \
  "v_mfma_f32_16x16x32_bf16 v[88:91], a[96:99], v[36:39], v[88:91]\n" \
  "s_waitcnt lgkmcnt(7)\n" \
  "v_add_f32 v136, v136, v140\n" \
  "v_add_f32 v137, v137, v141\n" \
  "v_mfma_f32_16x16x32_bf16 v[92:95], a[100:103], v[36:39], v[92:95]\n" \
  "v_add_f32 v138, v138, v142\n" \
  "v_add_f32 v139, v139, v143\n" \
  "v_mfma_f32_16x16x32_bf16 v[96:99], a[104:107], v[36:39], v[96:99]\n" \
  "ds_write_b128 v191, v[80:83] offset:4096\n" \
  "v_mfma_f32_16x16x32_bf16 v[100:103], a[108:111], v[36:39], v[100:103]\n" \
  "v_add_f32 v128, v128, v136\n" \
  "v_add_f32 v129, v129, v137\n" \
  "v_mfma_f32_16x16x32_bf16 v[104:107], a[112:115], v[36:39], v[104:107]\n" \
  "v_add_f32 v130, v130, v138\n" \
  "v_add_f32 v131, v131, v139\n" \
  "v_mfma_f32_16x16x32_bf16 v[108:111], a[116:119], v[36:39], v[108:111]\n" \
  "buffer_load_dwordx4 v[36:39], v190, s[40:43], s78 offen offset:1024 sc1\n" \
  "v_mfma_f32_16x16x32_bf16 v[88:91], a[120:123], v[44:47], v[88:91]\n" \
  "v_cvt_pk_bf16_f32 v114, v128, v129\n" \
  "v_cvt_pk_bf16_f32 v115, v130, v131\n" \
  "v_mfma_f32_16x16x32_bf16 v[92:95], a[124:127], v[44:47], v[92:95]\n" \
  "buffer_store_dwordx4 v[112:115], v190, s[40:43], s53 offen sc1\n" \
  "v_mfma_f32_16x16x32_bf16 v[96:99], a[128:131], v[44:47], v[96:99]\n" \
  "ds_write_b128 v191, v[84:87] offset:5120\n" \
  "v_mfma_f32_16x16x32_bf16 v[100:103], a[132:135], v[44:47], v[100:103]\n" \
  "s_waitcnt lgkmcnt(7)\n" \
  "v_add_f32 v144, v144, v148\n" \
  "v_add_f32 v145, v145, v149\n" \
  "v_mfma_f32_16x16x32_bf16 v[104:107], a[136:139], v[44:47], v[104:107]\n" \
  "v_add_f32 v146, v146, v150\n" \
  "v_add_f32 v147, v147, v151\n" \
  "v_mfma_f32_16x16x32_bf16 v[108:111], a[140:143], v[44:47], v[108:111]\n" \
  "buffer_load_dwordx4 v[44:47], v190, s[40:43], s78 offen offset:3072 sc1\n" \
  "v_mfma_f32_16x16x32_bf16 v[88:91], a[144:147], v[52:55], v[88:91]\n" \
  "s_waitcnt lgkmcnt(4)\n" \
  "v_add_f32 v152, v152, v156\n" \
  "v_add_f32 v153, v153, v157\n" \
  "v_mfma_f32_16x16x32_bf16 v[92:95], a[148:151], v[52:55], v[92:95]\n" \
  "v_add_f32 v154, v154, v158\n" \
  "v_add_f32 v155, v155, v159\n" \
  "v_mfma_f32_16x16x32_bf16 v[96:99], a[152:155], v[52:55], v[96:99]\n" \
  "v_mfma_f32_16x16x32_bf16 v[100:103], a[156:159], v[52:55], v[100:103]\n" \
  "v_add_f32 v144, v144, v152\n" \
  "v_add_f32 v145, v145, v153\n" \
  "v_mfma_f32_16x16x32_bf16 v[104:107], a[160:163], v[52:55], v[104:107]\n" \
  "v_add_f32 v146, v146, v154\n" \
  "v_add_f32 v147, v147, v155\n" \
  "v_mfma_f32_16x16x32_bf16 v[108:111], a[164:167], v[52:55], v[108:111]\n" \
  "buffer_load_dwordx4 v[52:55], v190, s[40:43], s79 offen offset:1024 sc1\n" \
  "s_waitcnt vmcnt(18)\n" \
  "v_mfma_f32_16x16x32_bf16 v[88:91], a[168:171], v[60:63], v[88:91]\n" \
  "v_cvt_pk_bf16_f32 v116, v144, v145\n" \
  "v_cvt_pk_bf16_f32 v117, v146, v147\n" \
  "v_mfma_f32_16x16x32_bf16 v[92:95], a[172:175], v[60:63], v[92:95]\n" \
  "buffer_store_dwordx2 v[116:117], v198, s[40:43], s53 offen offset:1024 sc1\n" \
  "v_mfma_f32_16x16x32_bf16 v[96:99], a[176:179], v[60:63], v[96:99]\n" \
  "s_lshr_b32 s53, s50, 8\n" \
  "s_and_b32 s53, s53, 8192\n" \
  "s_xor_b32 s53, s53, s50\n" \
  "s_add_u32 s50, s50, 393216\n" \
  "v_xor_b32 v192, 65536, v192\n" \
  "v_mfma_f32_16x16x32_bf16 v[100:103], a[180:183], v[60:63], v[100:103]\n" \
  "v_mfma_f32_16x16x32_bf16 v[104:107], a[184:187], v[60:63], v[104:107]\n" \
  "v_mfma_f32_16x16x32_bf16 v[108:111], a[188:191], v[60:63], v[108:111]\n" \
  "buffer_load_dwordx4 v[60:63], v190, s[40:43], s79 offen offset:3072 sc1\n" \
  "s_cmp_lg_u32 s54, 0\n" \
  "s_cbranch_scc1 .LO_ft_%=\n" \
  ".LO_ft_b_%=:\n" \
  "s_lshr_b32 s76, s49, 8\n" \
  "s_and_b32 s76, s76, 8192\n" \
  "s_xor_b32 s76, s76, s49\n" \
  "s_add_u32 s77, s76, 4096\n" \
  "s_add_u32 s78, s76, 16384\n" \
  "s_add_u32 s79, s76, 20480\n" \
  "s_add_u32 s49, s49, 524288\n" \
  "s_add_u32 s48, s48, 1\n" \
  "s_add_u32 s94, s48, 1\n" \
  "s_cmp_lt_u32 s94, %18\n" \
  "s_cselect_b32 s94, s94, -1\n" \
  "s_cmp_lt_u32 s48, %18\n" \
  "s_cbranch_scc1 .LO_loop_%=\n" \
  ".LO_fl_%=:\n" \
  "s_nop 7\n" \
  "ds_write_b128 v191, v[88:91] offset:6144\n" \
  "ds_write_b128 v191, v[92:95] offset:7168\n" \
  "ds_write_b128 v191, v[96:99] offset:8192\n" \
  "ds_write_b128 v191, v[100:103] offset:9216\n" \
  "ds_write_b128 v191, v[104:107] offset:10240\n" \
  "ds_write_b128 v191, v[108:111] offset:11264\n" \
  "s_waitcnt lgkmcnt(0)\n" \
  "s_barrier\n" \
  "ds_read_b128 v[120:123], v192\n" \
  "ds_read_b128 v[112:115], v192 offset:12288\n" \
  "ds_read_b128 v[116:119], v192 offset:24576\n" \
  "ds_read_b128 v[124:127], v192 offset:36864\n" \
  "ds_read_b128 v[128:131], v192 offset:1024\n" \
  "ds_read_b128 v[132:135], v192 offset:13312\n" \
  "ds_read_b128 v[136:139], v192 offset:25600\n" \
  "ds_read_b128 v[140:143], v192 offset:37888\n" \
  "ds_read_b128 v[144:147], v192 offset:2048\n" \
  "ds_read_b128 v[148:151], v192 offset:14336\n" \
  "ds_read_b128 v[152:155], v192 offset:26624\n" \
  "ds_read_b128 v[156:159], v192 offset:38912\n" \
  "s_waitcnt lgkmcnt(10)\n" \
  "v_add_f32 v120, v120, v112\n" \
  "v_add_f32 v121, v121, v113\n" \
  "v_add_f32 v122, v122, v114\n" \
  "v_add_f32 v123, v123, v115\n" \
  "s_waitcnt lgkmcnt(8)\n" \
  "v_add_f32 v116, v116, v124\n" \
  "v_add_f32 v117, v117, v125\n" \
  "v_add_f32 v118, v118, v126\n" \
  "v_add_f32 v119, v119, v127\n" \
  "v_add_f32 v120, v120, v116\n" \
  "v_add_f32 v121, v121, v117\n" \
  "v_add_f32 v122, v122, v118\n" \
  "v_add_f32 v123, v123, v119\n" \
  "v_cvt_pk_bf16_f32 v112, v120, v121\n" \
  "v_cvt_pk_bf16_f32 v113, v122, v123\n" \
  "s_waitcnt lgkmcnt(6)\n" \
  "v_add_f32 v128, v128, v132\n" \
  "v_add_f32 v129, v129, v133\n" \
  "v_add_f32 v130, v130, v134\n" \
  "v_add_f32 v131, v131, v135\n" \
  "s_waitcnt lgkmcnt(4)\n" \
  "v_add_f32 v136, v136, v140\n" \
  "v_add_f32 v137, v137, v141\n" \
  "v_add_f32 v138, v138, v142\n" \
  "v_add_f32 v139, v139, v143\n" \
  "v_add_f32 v128, v128, v136\n" \
  "v_add_f32 v129, v129, v137\n" \
  "v_add_f32 v130, v130, v138\n" \
  "v_add_f32 v131, v131, v139\n" \
  "v_cvt_pk_bf16_f32 v114, v128, v129\n" \
  "v_cvt_pk_bf16_f32 v115, v130, v131\n" \
  "buffer_store_dwordx4 v[112:115], v190, s[40:43], s53 offen sc1\n" \
  "s_waitcnt lgkmcnt(2)\n" \
  "v_add_f32 v144, v144, v148\n" \
  "v_add_f32 v145, v145, v149\n" \
  "v_add_f32 v146, v146, v150\n" \
  "v_add_f32 v147, v147, v151\n" \
  "s_waitcnt lgkmcnt(0)\n" \
  "v_add_f32 v152, v152, v156\n" \
  "v_add_f32 v153, v153, v157\n" \
  "v_add_f32 v154, v154, v158\n" \
  "v_add_f32 v155, v155, v159\n" \
  "v_add_f32 v144, v144, v152\n" \
  "v_add_f32 v145, v145, v153\n" \
  "v_add_f32 v146, v146, v154\n" \
  "v_add_f32 v147, v147, v155\n" \
  "v_cvt_pk_bf16_f32 v116, v144, v145\n" \
  "v_cvt_pk_bf16_f32 v117, v146, v147\n" \
  "buffer_store_dwordx2 v[116:117], v198, s[40:43], s53 offen offset:1024 sc1\n" \
  "s_waitcnt vmcnt(0)\n" \
  "s_barrier\n" \
  "s_add_i32 s60, s48, 0\n" \
  "v_mov_b32 v186, s60\n" \
  "s_mov_b64 exec, 1\n" \
  "buffer_store_dwordx2 v[186:187], off, s[40:43], %16 sc1\n" \
  "s_mov_b64 exec, -1\n" \
  "s_cmp_eq_u32 s48, 0\n" \
  "s_cbranch_scc1 .LO_fl_b_%=\n" \
  "s_branch .LO_end_%=\n" \
  ".LO_pl_%=:\n" \
  "s_add_u32 s60, s48, 2\n" \
  "s_cmp_lg_u32 s60, %18\n" \
  "s_cbranch_scc1 .LO_ft_%=\n" \
  "s_mov_b32 s54, s76\n" \
  "s_mov_b32 s76, 2130706432\n" \
  "s_mov_b32 s77, 2130706432\n" \
  "s_mov_b32 s78, 2130706432\n" \
  "s_mov_b32 s79, 2130706432\n" \
  "s_branch .LO_pl_b_%=\n" \
  ".LO_ft_%=:\n" \
  "s_mov_b32 s69, 0\n" \
  ".LO_p1_%=:\n" \
  "buffer_load_dwordx2 v[188:189], v193, s[40:43], 0 offen sc1\n" \
  "s_waitcnt vmcnt(0)\n" \
  "v_cmp_ne_u32 vcc, %12, v189\n" \
  "s_nop 1\n" \
  "v_cndmask_b32 v188, v188, 0, vcc\n" \
  "v_cmp_ge_i32 vcc, s94, v188\n" \
  "s_cbranch_vccz .LO_p1_x_%=\n" \
  "s_sleep 2\n" \
  "s_add_u32 s69, s69, 1\n" \
  "s_and_b32 s63, s69, 1023\n" \
  "s_cbranch_scc1 .LO_p1_%=\n" \
  "buffer_load_dwordx2 v[188:189], off, s[40:43], %17 offset:128 sc1\n" \
  "s_waitcnt vmcnt(0)\n" \
  "v_readfirstlane_b32 s63, v189\n" \
  "s_cmp_eq_u32 s63, %12\n" \
  "s_cbranch_scc1 .LO_p1_x_%=\n" \
  "s_cmp_le_u32 s69, 2097152\n" \
  "s_cbranch_scc1 .LO_p1_%=\n" \
  "v_mov_b32 v188, 1\n" \
  "v_mov_b32 v189, %12\n" \
  "buffer_store_dwordx2 v[188:189], off, s[40:43], %17 offset:128 sc1\n" \
  "s_waitcnt vmcnt(0)\n" \
  ".LO_p1_x_%=:\n" \
  "s_cmp_eq_u32 s54, 0\n" \
  "s_cbranch_scc1 .LO_pl_b_%=\n" \
  "s_mov_b32 s76, s54\n" \
  "s_add_u32 s77, s76, 4096\n" \
  "s_add_u32 s78, s76, 16384\n" \
  "s_add_u32 s79, s76, 20480\n" \
  "buffer_load_dwordx4 v[0:3], v190, s[40:43], s76 offen sc1\n" \
  "buffer_load_dwordx4 v[8:11], v190, s[40:43], s76 offen offset:2048 sc1\n" \
  "buffer_load_dwordx4 v[16:19], v190, s[40:43], s77 offen sc1\n" \
  "buffer_load_dwordx4 v[24:27], v190, s[40:43], s77 offen offset:2048 sc1\n" \
  "buffer_load_dwordx4 v[32:35], v190, s[40:43], s78 offen sc1\n" \
  "buffer_load_dwordx4 v[40:43], v190, s[40:43], s78 offen offset:2048 sc1\n" \
  "buffer_load_dwordx4 v[48:51], v190, s[40:43], s79 offen sc1\n" \
  "buffer_load_dwordx4 v[56:59], v190, s[40:43], s79 offen offset:2048 sc1\n" \
  "buffer_load_dwordx4 v[4:7], v190, s[40:43], s76 offen offset:1024 sc1\n" \
  "buffer_load_dwordx4 v[12:15], v190, s[40:43], s76 offen offset:3072 sc1\n" \
  "buffer_load_dwordx4 v[20:23], v190, s[40:43], s77 offen offset:1024 sc1\n" \
  "buffer_load_dwordx4 v[28:31], v190, s[40:43], s77 offen offset:3072 sc1\n" \
  "buffer_load_dwordx4 v[36:39], v190, s[40:43], s78 offen offset:1024 sc1\n" \
  "buffer_load_dwordx4 v[44:47], v190, s[40:43], s78 offen offset:3072 sc1\n" \
  "buffer_load_dwordx4 v[52:55], v190, s[40:43], s79 offen offset:1024 sc1\n" \
  "buffer_load_dwordx4 v[60:63], v190, s[40:43], s79 offen offset:3072 sc1\n" \
  "s_waitcnt vmcnt(8)\n" \
  "s_branch .LO_ft_b_%=\n" \
  ".LO_end_%=:\n"
#define O_ASM_VOPS "+{v190}"(oi.l16), "+{v191}"(oi.rw), "+{v192}"(oi.rr), "+{v193}"(oi.fo), "+{v194}"(oi.wv0), "+{v195}"(oi.wv1), "+{v196}"(oi.wv2), "+{v197}"(oi.tro), "+{v198}"(oi.l8)
#define O_ASM_SOPS "s"(oi.ws), "s"(oi.Wo), "s"(oi.w), "s"(oi.tag), "s"(oi.o_w), "s"(oi.o_o), "s"(oi.o_a), "s"(oi.f_a), "s"(oi.o_ctl), "s"(oi.ns)
#define O_ASM_CLOB "v0", "v1", "v2", "v3", "v4", "v5", "v6", "v7", "v8", "v9", "v10", "v11", "v12", "v13", "v14", "v15", "v16", "v17", "v18", "v19", "v20", "v21", "v22", "v23", "v24", "v25", "v26", "v27", "v28", "v29", "v30", "v31", "v32", "v33", "v34", "v35", "v36", "v37", "v38", "v39", "v40", "v41", "v42", "v43", "v44", "v45", "v46", "v47", "v48", "v49", "v50", "v51", "v52", "v53", "v54", "v55", "v56", "v57", "v58", "v59", "v60", "v61", "v62", "v63", "v64", "v65", "v66", "v67", "v68", "v69", "v70", "v71", "v72", "v73", "v74", "v75", "v76", "v77", "v78", "v79", "v80", "v81", "v82", "v83", "v84", "v85", "v86", "v87", "v88", "v89", "v90", "v91", "v92", "v93", "v94", "v95", "v96", "v97", "v98", "v99", "v100", "v101", "v102", "v103", "v104", "v105", "v106", "v107", "v108", "v109", "v110", "v111", "v112", "v113", "v114", "v115", "v116", "v117", "v118", "v119", "v120", "v121", "v122", "v123", "v124", "v125", "v126", "v127", "v128", "v129", "v130", "v131", "v132", "v133", "v134", "v135", "v136", "v137", "v138", "v139", "v140", "v141", "v142", "v143", "v144", "v145", "v146", "v147", "v148", "v149", "v150", "v151", "v152", "v153", "v154", "v155", "v156", "v157", "v158", "v159", "v160", "v161", "v162", "v163", "v164", "v165", "v166", "v167", "v168", "v169", "v170", "v171", "v172", "v173", "v174", "v175", "v176", "v177", "v178", "v179", "v180", "v181", "v182", "v183", "v184", "v185", "v186", "v187", "v188", "v189", "a0", "a1", "a2", "a3", "a4", "a5", "a6", "a7", "a8", "a9", "a10", "a11", "a12", "a13", "a14", "a15", "a16", "a17", "a18", "a19", "a20", "a21", "a22", "a23", "a24", "a25", "a26", "a27", "a28", "a29", "a30", "a31", "a32", "a33", "a34", "a35", "a36", "a37", "a38", "a39", "a40", "a41", "a42", "a43", "a44", "a45", "a46", "a47", "a48", "a49", "a50", "a51", "a52", "a53", "a54", "a55", "a56", "a57", "a58", "a59", "a60", "a61", "a62", "a63", "a64", "a65", "a66", "a67", "a68", "a69", "a70", "a71", "a72", "a73", "a74", "a75", "a76", "a77", "a78", "a79", "a80", "a81", "a82", "a83", "a84", "a85", "a86", "a87", "a88", "a89", "a90", "a91", "a92", "a93", "a94", "a95", "a96", "a97", "a98", "a99", "a100", "a101", "a102", "a103", "a104", "a105", "a106", "a107", "a108", "a109", "a110", "a111", "a112", "a113", "a114", "a115", "a116", "a117", "a118", "a119", "a120", "a121", "a122", "a123", "a124", "a125", "a126", "a127", "a128", "a129", "a130", "a131", "a132", "a133", "a134", "a135", "a136", "a137", "a138", "a139", "a140", "a141", "a142", "a143", "a144", "a145", "a146", "a147", "a148", "a149", "a150", "a151", "a152", "a153", "a154", "a155", "a156", "a157", "a158", "a159", "a160", "a161", "a162", "a163", "a164", "a165", "a166", "a167", "a168", "a169", "a170", "a171", "a172", "a173", "a174", "a175", "a176", "a177", "a178", "a179", "a180", "a181", "a182", "a183", "a184", "a185", "a186", "a187", "a188", "a189", "a190", "a191", "s40", "s41", "s42", "s43", "s44", "s45", "s46", "s47", "s48", "s49", "s50", "s51", "s52", "s53", "s54", "s55", "s56", "s57", "s58", "s59", "s60", "s61", "s62", "s63", "s64", "s65", "s66", "s67", "s68", "s69", "s70", "s71", "s72", "s73", "s74", "s75", "s76", "s77", "s78", "s79", "s80", "s81", "s82", "s83", "s84", "s85", "s86", "s87", "s88", "s89", "s90", "s91", "s92", "s93", "s94", "s95", "s96", "s97", "s98", "s99", "vcc", "scc", "memory"
#define F_ASM_TEXT \
  "s_mov_b64 s[40:41], %30\n" \
  "s_and_b32 s41, s41, 0xffff\n" \
  "s_mov_b32 s42, 255852544\n" \
  "s_mov_b32 s43, 0x00020000\n" \
  "s_mov_b64 s[44:45], %31\n" \
  "s_and_b32 s45, s45, 0xffff\n" \
  "s_mov_b32 s46, 2359296\n" \
  "s_mov_b32 s47, 0x00020000\n" \
  "s_mov_b32 s48, 0\n" \
  "s_mov_b32 s58, 0\n" \
  "s_mov_b32 s59, 0\n" \
  "s_mov_b32 s49, %36\n" \
  "s_mov_b32 s50, %37\n" \
  "s_mov_b32 s51, %35\n" \
  "s_mov_b32 s56, %38\n" \
  "s_mov_b32 s57, %39\n" \
  "s_mov_b32 s64, %43\n" \
  "v_not_b32 v197, %32\n" \
  "v_mov_b32 v62, -1\n" \
  "s_lshl_b32 s62, s58, 17\n" \
  "s_add_u32 s62, s62, %34\n" \
  "buffer_load_dwordx4 v[64:67], v203, s[40:43], s62 offen sc1\n" \
  "buffer_load_dwordx4 v[68:71], v203, s[40:43], s62 offen offset:1024 sc1\n" \
  "buffer_load_dwordx4 v[72:75], v203, s[40:43], s62 offen offset:2048 sc1\n" \
  "buffer_load_dwordx4 v[76:79], v203, s[40:43], s62 offen offset:3072 sc1\n" \
  "buffer_load_dwordx4 v[80:83], v204, s[40:43], s62 offen sc1\n" \
  "buffer_load_dwordx4 v[84:87], v204, s[40:43], s62 offen offset:1024 sc1\n" \
  "buffer_load_dwordx4 v[88:91], v204, s[40:43], s62 offen offset:2048 sc1\n" \
  "buffer_load_dwordx4 v[92:95], v204, s[40:43], s62 offen offset:3072 sc1\n" \
  "buffer_load_dwordx4 v[96:99], v205, s[40:43], s62 offen sc1\n" \
  "buffer_load_dwordx4 v[100:103], v205, s[40:43], s62 offen offset:1024 sc1\n" \
  "buffer_load_dwordx4 v[104:107], v205, s[40:43], s62 offen offset:2048 sc1\n" \
  "buffer_load_dwordx4 v[108:111], v205, s[40:43], s62 offen offset:3072 sc1\n" \
  "buffer_load_dwordx4 v[112:115], v206, s[40:43], s62 offen sc1\n" \
  "buffer_load_dwordx4 v[116:119], v206, s[40:43], s62 offen offset:1024 sc1\n" \
  "buffer_load_dwordx4 v[120:123], v206, s[40:43], s62 offen offset:2048 sc1\n" \
  "buffer_load_dwordx4 v[124:127], v206, s[40:43], s62 offen offset:3072 sc1\n" \
  ".LF_pre_%=:\n" \
  "v_readfirstlane_b32 s60, v197\n" \
  "s_cmp_eq_u32 s60, %32\n" \
  "s_cbranch_scc1 .LF_fu_x_%=\n" \
  ".LF_fu_%=:\n" \
  "buffer_load_dwordx2 v[196:197], off, s[40:43], s64 sc1\n" \
  "buffer_load_dwordx2 v[62:63], off, s[40:43], s50 offset:256 sc1\n" \
  "s_memrealtime s[76:77]\n" \
  "s_waitcnt vmcnt(0) lgkmcnt(0)\n" \
  "v_readfirstlane_b32 s60, v197\n" \
  "s_cmp_eq_u32 s60, %32\n" \
  "s_cbranch_scc1 .LF_fu_x_%=\n" \
  "v_readfirstlane_b32 s60, v62\n" \
  "s_sub_u32 s60, s60, 58\n" \
  "s_sub_u32 s60, s60, s48\n" \
  "s_cmp_lt_u32 s48, s58\n" \
  "s_cselect_b32 s60, s60, -1\n" \
  "v_readfirstlane_b32 s61, v63\n" \
  "s_sub_u32 s61, s61, s76\n" \
  "s_cmp_lt_i32 s61, -60\n" \
  "s_cselect_b32 s60, s60, -1\n" \
  "s_cmp_lt_u32 s60, 8\n" \
  "s_cbranch_scc1 .LF_loop_%=\n" \
  "s_sleep 6\n" \
  "s_add_u32 s59, s59, 1\n" \
  "s_cmp_lt_u32 s59, 131072\n" \
  "s_cbranch_scc1 .LF_fu_%=\n" \
  "v_mov_b32 v32, 1\n" \
  "v_mov_b32 v33, %32\n" \
  "buffer_store_dwordx2 v[32:33], off, s[40:43], %40 offset:128 sc1\n" \
  "s_waitcnt vmcnt(0)\n" \
  ".LF_fu_x_%=:\n" \
  "s_add_u32 s61, s58, 58\n" \
  "s_and_b32 s61, s61, 15\n" \
  "s_lshl_b32 s61, s61, 16\n" \
  "s_xor_b32 s61, s61, %33\n" \
  "s_xor_b32 s60, s61, 0\n" \
  "buffer_load_dwordx4 v[32:35], v202, s[40:43], s60 offen sc1\n" \
  "s_xor_b32 s60, s61, 1024\n" \
  "buffer_load_dwordx4 v[36:39], v202, s[40:43], s60 offen sc1\n" \
  "s_xor_b32 s60, s61, 2048\n" \
  "buffer_load_dwordx4 v[40:43], v202, s[40:43], s60 offen sc1\n" \
  "s_xor_b32 s60, s61, 3072\n" \
  "buffer_load_dwordx4 v[44:47], v202, s[40:43], s60 offen sc1\n" \
  "s_xor_b32 s60, s61, 4096\n" \
  "buffer_load_dwordx4 v[48:51], v202, s[40:43], s60 offen sc1\n" \
  "s_xor_b32 s60, s61, 5120\n" \
  "buffer_load_dwordx4 v[52:55], v202, s[40:43], s60 offen sc1\n" \
  "s_xor_b32 s60, s61, 6144\n" \
  "buffer_load_dwordx4 v[56:59], v202, s[40:43], s60 offen sc1\n" \
  "s_xor_b32 s60, s61, 7168\n" \
  "buffer_load_dwordx4 v[60:63], v202, s[40:43], s60 offen sc1\n" \
  "s_add_u32 s64, s64, 128\n" \
  "v_mov_b64 v[128:129], 0\n" \
  "v_mov_b64 v[130:131], 0\n" \
  "v_mov_b64 v[132:133], 0\n" \
  "v_mov_b64 v[134:135], 0\n" \
  "v_mov_b64 v[136:137], 0\n" \
  "v_mov_b64 v[138:139], 0\n" \
  "v_mov_b64 v[140:141], 0\n" \
  "v_mov_b64 v[142:143], 0\n" \
  "v_mov_b64 v[144:145], 0\n" \
  "v_mov_b64 v[146:147], 0\n" \
  "v_mov_b64 v[148:149], 0\n" \
  "v_mov_b64 v[150:151], 0\n" \
  "v_mov_b64 v[152:153], 0\n" \
  "v_mov_b64 v[154:155], 0\n" \
  "v_mov_b64 v[156:157], 0\n" \
  "v_mov_b64 v[158:159], 0\n" \
  "v_mov_b64 v[160:161], 0\n" \
  "v_mov_b64 v[162:163], 0\n" \
  "v_mov_b64 v[164:165], 0\n" \
  "v_mov_b64 v[166:167], 0\n" \
  "v_mov_b64 v[168:169], 0\n" \
  "v_mov_b64 v[170:171], 0\n" \
  "v_mov_b64 v[172:173], 0\n" \
  "v_mov_b64 v[174:175], 0\n" \
  "v_mov_b64 v[176:177], 0\n" \
  "v_mov_b64 v[178:179], 0\n" \
  "v_mov_b64 v[180:181], 0\n" \
  "v_mov_b64 v[182:183], 0\n" \
  "v_mov_b64 v[184:185], 0\n" \
  "v_mov_b64 v[186:187], 0\n" \
  "v_mov_b64 v[188:189], 0\n" \
  "v_mov_b64 v[190:191], 0\n" \
  "s_waitcnt vmcnt(0)\n" \
  "v_mov_b32 v0, v32\n" \
  "v_mov_b32 v1, v36\n" \
  "v_mov_b32 v2, v33\n" \
  "v_mov_b32 v3, v37\n" \
  "v_mov_b32 v4, v34\n" \
  "v_mov_b32 v5, v38\n" \
  "v_mov_b32 v6, v35\n" \
  "v_mov_b32 v7, v39\n" \
  "v_mov_b32 v8, v40\n" \
  "v_mov_b32 v9, v44\n" \
  "v_mov_b32 v10, v41\n" \
  "v_mov_b32 v11, v45\n" \
  "v_mov_b32 v12, v42\n" \
  "v_mov_b32 v13, v46\n" \
  "v_mov_b32 v14, v43\n" \
  "v_mov_b32 v15, v47\n" \
  "v_mov_b32 v16, v48\n" \
  "v_mov_b32 v17, v52\n" \
  "v_mov_b32 v18, v49\n" \
  "v_mov_b32 v19, v53\n" \
  "v_mov_b32 v20, v50\n" \
  "v_mov_b32 v21, v54\n" \
  "v_mov_b32 v22, v51\n" \
  "v_mov_b32 v23, v55\n" \
  "v_mov_b32 v24, v56\n" \
  "v_mov_b32 v25, v60\n" \
  "v_mov_b32 v26, v57\n" \
  "v_mov_b32 v27, v61\n" \
  "v_mov_b32 v28, v58\n" \
  "v_mov_b32 v29, v62\n" \
  "v_mov_b32 v30, v59\n" \
  "v_mov_b32 v31, v63\n" \
  "s_nop 1\n" \
  "v_smfmac_f32_16x16x64_bf16 v[128:131], v[64:67], v[0:7], v200\n" \
  "v_smfmac_f32_16x16x64_bf16 v[132:135], v[68:71], v[8:15], v200\n" \
  "v_smfmac_f32_16x16x64_bf16 v[136:139], v[72:75], v[16:23], v200\n" \
  "v_smfmac_f32_16x16x64_bf16 v[140:143], v[76:79], v[24:31], v200\n" \
  "v_smfmac_f32_16x16x64_bf16 v[144:147], v[80:83], v[0:7], v200\n" \
  "v_smfmac_f32_16x16x64_bf16 v[148:151], v[84:87], v[8:15], v200\n" \
  "v_smfmac_f32_16x16x64_bf16 v[152:155], v[88:91], v[16:23], v200\n" \
  "v_smfmac_f32_16x16x64_bf16 v[156:159], v[92:95], v[24:31], v200\n" \
  "v_smfmac_f32_16x16x64_bf16 v[160:163], v[96:99], v[0:7], v200\n" \
  "v_smfmac_f32_16x16x64_bf16 v[164:167], v[100:103], v[8:15], v200\n" \
  "v_smfmac_f32_16x16x64_bf16 v[168:171], v[104:107], v[16:23], v200\n" \
  "v_smfmac_f32_16x16x64_bf16 v[172:175], v[108:111], v[24:31], v200\n" \
  "v_smfmac_f32_16x16x64_bf16 v[176:179], v[112:115], v[0:7], v200\n" \
  "v_smfmac_f32_16x16x64_bf16 v[180:183], v[116:119], v[8:15], v200\n" \
  "v_smfmac_f32_16x16x64_bf16 v[184:187], v[120:123], v[16:23], v200\n" \
  "v_smfmac_f32_16x16x64_bf16 v[188:191], v[124:127], v[24:31], v200\n" \
  "s_add_u32 s58, s58, 1\n" \
  "buffer_load_dwordx2 v[62:63], off, s[40:43], s50 offset:256 sc1\n" \
  "buffer_load_dwordx2 v[196:197], off, s[40:43], s64 sc1\n" \
  "s_cmp_eq_u32 s58, 6\n" \
  "s_cbranch_scc1 .LF_ne_%=\n" \
  "s_lshl_b32 s62, s58, 17\n" \
  "s_add_u32 s62, s62, %34\n" \
  "buffer_load_dwordx4 v[64:67], v203, s[40:43], s62 offen sc1\n" \
  "buffer_load_dwordx4 v[68:71], v203, s[40:43], s62 offen offset:1024 sc1\n" \
  "buffer_load_dwordx4 v[72:75], v203, s[40:43], s62 offen offset:2048 sc1\n" \
  "buffer_load_dwordx4 v[76:79], v203, s[40:43], s62 offen offset:3072 sc1\n" \
  "buffer_load_dwordx4 v[80:83], v204, s[40:43], s62 offen sc1\n" \
  "buffer_load_dwordx4 v[84:87], v204, s[40:43], s62 offen offset:1024 sc1\n" \
  "buffer_load_dwordx4 v[88:91], v204, s[40:43], s62 offen offset:2048 sc1\n" \
  "buffer_load_dwordx4 v[92:95], v204, s[40:43], s62 offen offset:3072 sc1\n" \
  "buffer_load_dwordx4 v[96:99], v205, s[40:43], s62 offen sc1\n" \
  "buffer_load_dwordx4 v[100:103], v205, s[40:43], s62 offen offset:1024 sc1\n" \
  "buffer_load_dwordx4 v[104:107], v205, s[40:43], s62 offen offset:2048 sc1\n" \
  "buffer_load_dwordx4 v[108:111], v205, s[40:43], s62 offen offset:3072 sc1\n" \
  "buffer_load_dwordx4 v[112:115], v206, s[40:43], s62 offen sc1\n" \
  "buffer_load_dwordx4 v[116:119], v206, s[40:43], s62 offen offset:1024 sc1\n" \
  "buffer_load_dwordx4 v[120:123], v206, s[40:43], s62 offen offset:2048 sc1\n" \
  "buffer_load_dwordx4 v[124:127], v206, s[40:43], s62 offen offset:3072 sc1\n" \
  ".LF_ne_%=:\n" \
  "s_nop 7\n" \
  "v_cvt_pk_bf16_f32 v128, v128, v129\n" \
  "v_cvt_pk_bf16_f32 v129, v130, v131\n" \
  "v_cvt_pk_bf16_f32 v130, v132, v133\n" \
  "v_cvt_pk_bf16_f32 v131, v134, v135\n" \
  "v_cvt_pk_bf16_f32 v132, v136, v137\n" \
  "v_cvt_pk_bf16_f32 v133, v138, v139\n" \
  "v_cvt_pk_bf16_f32 v134, v140, v141\n" \
  "v_cvt_pk_bf16_f32 v135, v142, v143\n" \
  "v_cvt_pk_bf16_f32 v136, v144, v145\n" \
  "v_cvt_pk_bf16_f32 v137, v146, v147\n" \
  "v_cvt_pk_bf16_f32 v138, v148, v149\n" \
  "v_cvt_pk_bf16_f32 v139, v150, v151\n" \
  "v_cvt_pk_bf16_f32 v140, v152, v153\n" \
  "v_cvt_pk_bf16_f32 v141, v154, v155\n" \
  "v_cvt_pk_bf16_f32 v142, v156, v157\n" \
  "v_cvt_pk_bf16_f32 v143, v158, v159\n" \
  "v_cvt_pk_bf16_f32 v144, v160, v161\n" \
  "v_cvt_pk_bf16_f32 v145, v162, v163\n" \
  "v_cvt_pk_bf16_f32 v146, v164, v165\n" \
  "v_cvt_pk_bf16_f32 v147, v166, v167\n" \
  "v_cvt_pk_bf16_f32 v148, v168, v169\n" \
  "v_cvt_pk_bf16_f32 v149, v170, v171\n" \
  "v_cvt_pk_bf16_f32 v150, v172, v173\n" \
  "v_cvt_pk_bf16_f32 v151, v174, v175\n" \
  "v_cvt_pk_bf16_f32 v152, v176, v177\n" \
  "v_cvt_pk_bf16_f32 v153, v178, v179\n" \
  "v_cvt_pk_bf16_f32 v154, v180, v181\n" \
  "v_cvt_pk_bf16_f32 v155, v182, v183\n" \
  "v_cvt_pk_bf16_f32 v156, v184, v185\n" \
  "v_cvt_pk_bf16_f32 v157, v186, v187\n" \
  "v_cvt_pk_bf16_f32 v158, v188, v189\n" \
  "v_cvt_pk_bf16_f32 v159, v190, v191\n" \
  "s_cmp_eq_u32 s58, 1\n" \
  "s_cbranch_scc1 .LF_w1_%=\n" \
  "s_cmp_eq_u32 s58, 2\n" \
  "s_cbranch_scc1 .LF_w2_%=\n" \
  "s_cmp_eq_u32 s58, 3\n" \
  "s_cbranch_scc1 .LF_w3_%=\n" \
  "s_cmp_eq_u32 s58, 4\n" \
  "s_cbranch_scc1 .LF_w4_%=\n" \
  "s_cmp_eq_u32 s58, 5\n" \
  "s_cbranch_scc1 .LF_w5_%=\n" \
  "v_accvgpr_write_b32 a160, v128\n" \
  "v_accvgpr_write_b32 a161, v129\n" \
  "v_accvgpr_write_b32 a162, v130\n" \
  "v_accvgpr_write_b32 a163, v131\n" \
  "v_accvgpr_write_b32 a164, v132\n" \
  "v_accvgpr_write_b32 a165, v133\n" \
  "v_accvgpr_write_b32 a166, v134\n" \
  "v_accvgpr_write_b32 a167, v135\n" \
  "v_accvgpr_write_b32 a168, v136\n" \
  "v_accvgpr_write_b32 a169, v137\n" \
  "v_accvgpr_write_b32 a170, v138\n" \
  "v_accvgpr_write_b32 a171, v139\n" \
  "v_accvgpr_write_b32 a172, v140\n" \
  "v_accvgpr_write_b32 a173, v141\n" \
  "v_accvgpr_write_b32 a174, v142\n" \
  "v_accvgpr_write_b32 a175, v143\n" \
  "v_accvgpr_write_b32 a176, v144\n" \
  "v_accvgpr_write_b32 a177, v145\n" \
  "v_accvgpr_write_b32 a178, v146\n" \
  "v_accvgpr_write_b32 a179, v147\n" \
  "v_accvgpr_write_b32 a180, v148\n" \
  "v_accvgpr_write_b32 a181, v149\n" \
  "v_accvgpr_write_b32 a182, v150\n" \
  "v_accvgpr_write_b32 a183, v151\n" \
  "v_accvgpr_write_b32 a184, v152\n" \
  "v_accvgpr_write_b32 a185, v153\n" \
  "v_accvgpr_write_b32 a186, v154\n" \
  "v_accvgpr_write_b32 a187, v155\n" \
  "v_accvgpr_write_b32 a188, v156\n" \
  "v_accvgpr_write_b32 a189, v157\n" \
  "v_accvgpr_write_b32 a190, v158\n" \
  "v_accvgpr_write_b32 a191, v159\n" \
  ".LF_w_b_%=:\n" \
  "s_cmp_lt_u32 s58, 6\n" \
  "s_waitcnt vmcnt(16)\n" \
  "s_cbranch_scc1 .LF_pre_%=\n" \
  "s_waitcnt vmcnt(0)\n" \
  ".LF_loop_%=:\n" \
  "v_mov_b64 v[40:41], 0\n" \
  "v_mov_b64 v[42:43], 0\n" \
  "v_mov_b64 v[44:45], 0\n" \
  "v_mov_b64 v[46:47], 0\n" \
  "v_mov_b64 v[48:49], 0\n" \
  "v_mov_b64 v[50:51], 0\n" \
  "v_mov_b64 v[52:53], 0\n" \
  "v_mov_b64 v[54:55], 0\n" \
  "s_cmp_lg_u32 %42, 0\n" \
  "s_cbranch_scc1 .LF_go_%=\n" \
  "s_mov_b32 s69, 0\n" \
  "s_add_u32 s53, s48, 58\n" \
  "s_cmp_eq_u32 s48, 5\n" \
  "s_cselect_b32 s54, 16, 0\n" \
  "s_lshr_b32 s54, %41, s54\n" \
  "s_and_b32 s54, s54, 0x7fff\n" \
  "s_branch .LF_tw1_%=\n" \
  ".LF_tw_%=:\n" \
  "buffer_load_dwordx2 v[62:63], off, s[40:43], s50 offset:256 sc1\n" \
  "s_waitcnt vmcnt(0)\n" \
  ".LF_tw1_%=:\n" \
  "s_memrealtime s[76:77]\n" \
  "v_readfirstlane_b32 s62, v62\n" \
  "v_readfirstlane_b32 s61, v63\n" \
  "s_add_u32 s55, s61, s54\n" \
  "s_waitcnt lgkmcnt(0)\n" \
  "s_sub_u32 s60, s55, s76\n" \
  "s_cmp_gt_i32 s62, s53\n" \
  "s_cbranch_scc1 .LF_go_%=\n" \
  "s_cmp_eq_u32 s62, s53\n" \
  "s_cselect_b32 s60, s60, 999\n" \
  "s_cmp_lt_i32 s60, 400\n" \
  "s_cbranch_scc1 .LF_clk_%=\n" \
  "s_sleep 8\n" \
  "s_add_u32 s69, s69, 1\n" \
  "s_cmp_lt_u32 s69, 20000\n" \
  "s_cbranch_scc1 .LF_tw_%=\n" \
  "s_branch .LF_go_%=\n" \
  ".LF_clk_%=:\n" \
  "s_cmp_le_i32 s60, 3\n" \
  "s_cbranch_scc1 .LF_go_%=\n" \
  "s_sleep 1\n" \
  "s_cmp_le_i32 s60, 30\n" \
  "s_cbranch_scc1 .LF_cl2_%=\n" \
  "s_sleep 6\n" \
  ".LF_cl2_%=:\n" \
  "s_memrealtime s[76:77]\n" \
  "s_waitcnt lgkmcnt(0)\n" \
  "s_sub_u32 s60, s55, s76\n" \
  "s_branch .LF_clk_%=\n" \
  ".LF_go_%=:\n" \
  "s_barrier\n" \
  "s_mov_b32 s69, 0\n" \
  "s_lshr_b32 s52, s49, 8\n" \
  "s_and_b32 s52, s52, 8192\n" \
  "s_xor_b32 s52, s52, s49\n" \
  "s_xor_b32 s60, s50, s56\n" \
  "buffer_load_dwordx2 v[62:63], off, s[40:43], s60 offset:256 sc1\n" \
  ".LF_rd_%=:\n" \
  "buffer_load_dwordx4 v[0:3], v202, s[40:43], s52 offen sc1\n" \
  "buffer_load_dwordx4 v[4:7], v202, s[40:43], s52 offen offset:1024 sc1\n" \
  "s_mov_b32 s60, s51\n" \
  "buffer_load_dwordx4 v[8:11], v207, s[40:43], s60 offen sc1\n" \
  "s_add_u32 s60, s60, 4096\n" \
  "buffer_load_dwordx4 v[24:27], v207, s[40:43], s60 offen sc1\n" \
  "s_add_u32 s60, s60, 12288\n" \
  "buffer_load_dwordx4 v[12:15], v207, s[40:43], s60 offen sc1\n" \
  "s_add_u32 s60, s60, 4096\n" \
  "buffer_load_dwordx4 v[28:31], v207, s[40:43], s60 offen sc1\n" \
  "s_add_u32 s60, s60, 12288\n" \
  "buffer_load_dwordx4 v[16:19], v207, s[40:43], s60 offen sc1\n" \
  "s_add_u32 s60, s60, 4096\n" \
  "buffer_load_dwordx4 v[32:35], v207, s[40:43], s60 offen sc1\n" \
  "s_add_u32 s60, s60, 12288\n" \
  "buffer_load_dwordx4 v[20:23], v207, s[40:43], s60 offen sc1\n" \
  "s_add_u32 s60, s60, 4096\n" \
  "buffer_load_dwordx4 v[36:39], v207, s[40:43], s60 offen sc1\n" \
  "s_waitcnt vmcnt(8)\n" \
  "v_permlane16_swap_b32 v0, v1\n" \
  "v_permlane16_swap_b32 v2, v3\n" \
  "v_permlane16_swap_b32 v4, v5\n" \
  "v_permlane16_swap_b32 v6, v7\n" \
  "s_mov_b64 s[78:79], 0\n" \
  "s_cmp_eq_u32 s48, 1\n" \
  "s_cbranch_scc1 .LF_r1_%=\n" \
  "s_cmp_eq_u32 s48, 2\n" \
  "s_cbranch_scc1 .LF_r2_%=\n" \
  "s_cmp_eq_u32 s48, 3\n" \
  "s_cbranch_scc1 .LF_r3_%=\n" \
  "s_cmp_eq_u32 s48, 4\n" \
  "s_cbranch_scc1 .LF_r4_%=\n" \
  "s_cmp_eq_u32 s48, 5\n" \
  "s_cbranch_scc1 .LF_r5_%=\n" \
  "v_mov_b32 v192, v212\n" \
  "v_mov_b32 v193, v213\n" \
  "v_mov_b32 v194, v214\n" \
  "s_waitcnt vmcnt(7)\n" \
  "v_smfmac_f32_16x16x64_bf16 v[40:43], v[8:11], v[0:7], v200\n" \
  "s_waitcnt vmcnt(6)\n" \
  "v_smfmac_f32_16x16x64_bf16 v[40:43], v[24:27], a[0:7], v201\n" \
  "s_waitcnt vmcnt(5)\n" \
  "v_smfmac_f32_16x16x64_bf16 v[44:47], v[12:15], v[0:7], v200\n" \
  "s_waitcnt vmcnt(4)\n" \
  "v_smfmac_f32_16x16x64_bf16 v[44:47], v[28:31], a[8:15], v201\n" \
  "s_waitcnt vmcnt(3)\n" \
  "v_smfmac_f32_16x16x64_bf16 v[48:51], v[16:19], v[0:7], v200\n" \
  "s_waitcnt vmcnt(2)\n" \
  "v_smfmac_f32_16x16x64_bf16 v[48:51], v[32:35], a[16:23], v201\n" \
  "s_waitcnt vmcnt(1)\n" \
  "v_smfmac_f32_16x16x64_bf16 v[52:55], v[20:23], v[0:7], v200\n" \
  "s_waitcnt vmcnt(0)\n" \
  "v_smfmac_f32_16x16x64_bf16 v[52:55], v[36:39], a[24:31], v201\n" \
  ".LF_r_b_%=:\n" \
  "s_nop 1\n" \
  "v_permlane32_swap_b32 v40, v42\n" \
  "v_permlane32_swap_b32 v41, v43\n" \
  "v_add_f32 v40, v40, v42\n" \
  "v_add_f32 v41, v41, v43\n" \
  "v_cmp_u_f32 vcc, v40, v41\n" \
  "ds_write2st64_b32 v208, v40, v41 offset0:0 offset1:4\n" \
  "s_or_b64 s[78:79], s[78:79], vcc\n" \
  "v_permlane32_swap_b32 v44, v46\n" \
  "v_permlane32_swap_b32 v45, v47\n" \
  "v_add_f32 v44, v44, v46\n" \
  "v_add_f32 v45, v45, v47\n" \
  "v_cmp_u_f32 vcc, v44, v45\n" \
  "ds_write2st64_b32 v208, v44, v45 offset0:8 offset1:12\n" \
  "s_or_b64 s[78:79], s[78:79], vcc\n" \
  "v_permlane32_swap_b32 v48, v50\n" \
  "v_permlane32_swap_b32 v49, v51\n" \
  "v_add_f32 v48, v48, v50\n" \
  "v_add_f32 v49, v49, v51\n" \
  "v_cmp_u_f32 vcc, v48, v49\n" \
  "ds_write2st64_b32 v208, v48, v49 offset0:16 offset1:20\n" \
  "s_or_b64 s[78:79], s[78:79], vcc\n" \
  "v_permlane32_swap_b32 v52, v54\n" \
  "v_permlane32_swap_b32 v53, v55\n" \
  "v_add_f32 v52, v52, v54\n" \
  "v_add_f32 v53, v53, v55\n" \
  "v_cmp_u_f32 vcc, v52, v53\n" \
  "ds_write2st64_b32 v208, v52, v53 offset0:24 offset1:28\n" \
  "s_or_b64 s[78:79], s[78:79], vcc\n" \
  "s_cbranch_scc1 .LF_bad_%=\n" \
  ".LF_ok_%=:\n" \
  "s_waitcnt lgkmcnt(0)\n" \
  "s_barrier\n" \
  "ds_read_b128 v[40:43], v209\n" \
  "ds_read_b128 v[44:47], v209 offset:512\n" \
  "s_waitcnt lgkmcnt(1)\n" \
  "v_add_f32 v40, v40, v42\n" \
  "v_add_f32 v41, v41, v43\n" \
  "s_waitcnt lgkmcnt(0)\n" \
  "v_add_f32 v44, v44, v46\n" \
  "v_add_f32 v45, v45, v47\n" \
  "v_add_f32 v40, v40, v44\n" \
  "v_add_f32 v41, v41, v45\n" \
  "v_cvt_pk_bf16_f32 v40, v40, v41\n" \
  "v_and_b32 v41, 0xffff0000, v40\n" \
  "v_lshlrev_b32 v40, 16, v40\n" \
  "v_mul_f32 v40, v192, v40\n" \
  "v_mul_f32 v41, v192, v41\n" \
  "v_cvt_pk_bf16_f32 v40, v40, v41\n" \
  "v_and_b32 v41, 0xffff0000, v40\n" \
  "v_lshlrev_b32 v40, 16, v40\n" \
  "v_add_f32 v40, v40, v193\n" \
  "v_add_f32 v41, v41, v194\n" \
  "v_cvt_pk_bf16_f32 v237, v40, v41\n" \
  "s_cmp_eq_u32 s48, 5\n" \
  "s_cbranch_scc1 .LF_out_%=\n" \
  "v_mov_b32 v232, v233\n" \
  "v_mov_b32 v233, v234\n" \
  "v_mov_b32 v234, v235\n" \
  "v_mov_b32 v235, v236\n" \
  "v_mov_b32 v236, v237\n" \
  "v_xor_b32 v208, 8192, v208\n" \
  "v_xor_b32 v209, 8192, v209\n" \
  "s_add_u32 s49, s49, 1048576\n" \
  "s_xor_b32 s50, s50, s56\n" \
  "s_mov_b32 s60, s56\n" \
  "s_mov_b32 s56, s57\n" \
  "s_mov_b32 s57, s60\n" \
  "s_add_u32 s51, s51, 65536\n" \
  "s_add_u32 s48, s48, 1\n" \
  "s_cmp_lt_u32 s58, 6\n" \
  "s_cbranch_scc1 .LF_fu_%=\n" \
  "s_branch .LF_loop_%=\n" \
  ".LF_out_%=:\n" \
  "s_mov_b32 s60, 184320\n" \
  "buffer_store_dword v237, v210, s[44:47], s60 offen\n" \
  "s_mov_b32 s60, 147456\n" \
  "buffer_store_dword v236, v210, s[44:47], s60 offen\n" \
  "s_mov_b32 s60, 110592\n" \
  "buffer_store_dword v235, v210, s[44:47], s60 offen\n" \
  "s_mov_b32 s60, 73728\n" \
  "buffer_store_dword v234, v210, s[44:47], s60 offen\n" \
  "s_mov_b32 s60, 36864\n" \
  "buffer_store_dword v233, v210, s[44:47], s60 offen\n" \
  "s_mov_b32 s60, 0\n" \
  "buffer_store_dword v232, v210, s[44:47], s60 offen\n" \
  "s_branch .LF_end_%=\n" \
  ".LF_w1_%=:\n" \
  "v_accvgpr_write_b32 a0, v128\n" \
  "v_accvgpr_write_b32 a1, v129\n" \
  "v_accvgpr_write_b32 a2, v130\n" \
  "v_accvgpr_write_b32 a3, v131\n" \
  "v_accvgpr_write_b32 a4, v132\n" \
  "v_accvgpr_write_b32 a5, v133\n" \
  "v_accvgpr_write_b32 a6, v134\n" \
  "v_accvgpr_write_b32 a7, v135\n" \
  "v_accvgpr_write_b32 a8, v136\n" \
  "v_accvgpr_write_b32 a9, v137\n" \
  "v_accvgpr_write_b32 a10, v138\n" \
  "v_accvgpr_write_b32 a11, v139\n" \
  "v_accvgpr_write_b32 a12, v140\n" \
  "v_accvgpr_write_b32 a13, v141\n" \
  "v_accvgpr_write_b32 a14, v142\n" \
  "v_accvgpr_write_b32 a15, v143\n" \
  "v_accvgpr_write_b32 a16, v144\n" \
  "v_accvgpr_write_b32 a17, v145\n" \
  "v_accvgpr_write_b32 a18, v146\n" \
  "v_accvgpr_write_b32 a19, v147\n" \
  "v_accvgpr_write_b32 a20, v148\n" \
  "v_accvgpr_write_b32 a21, v149\n" \
  "v_accvgpr_write_b32 a22, v150\n" \
  "v_accvgpr_write_b32 a23, v151\n" \
  "v_accvgpr_write_b32 a24, v152\n" \
  "v_accvgpr_write_b32 a25, v153\n" \
  "v_accvgpr_write_b32 a26, v154\n" \
  "v_accvgpr_write_b32 a27, v155\n" \
  "v_accvgpr_write_b32 a28, v156\n" \
  "v_accvgpr_write_b32 a29, v157\n" \
  "v_accvgpr_write_b32 a30, v158\n" \
  "v_accvgpr_write_b32 a31, v159\n" \
  "s_branch .LF_w_b_%=\n" \
  ".LF_w2_%=:\n" \
  "v_accvgpr_write_b32 a32, v128\n" \
  "v_accvgpr_write_b32 a33, v129\n" \
  "v_accvgpr_write_b32 a34, v130\n" \
  "v_accvgpr_write_b32 a35, v131\n" \
  "v_accvgpr_write_b32 a36, v132\n" \
  "v_accvgpr_write_b32 a37, v133\n" \
  "v_accvgpr_write_b32 a38, v134\n" \
  "v_accvgpr_write_b32 a39, v135\n" \
  "v_accvgpr_write_b32 a40, v136\n" \
  "v_accvgpr_write_b32 a41, v137\n" \
  "v_accvgpr_write_b32 a42, v138\n" \
  "v_accvgpr_write_b32 a43, v139\n" \
  "v_accvgpr_write_b32 a44, v140\n" \
  "v_accvgpr_write_b32 a45, v141\n" \
  "v_accvgpr_write_b32 a46, v142\n" \
  "v_accvgpr_write_b32 a47, v143\n" \
  "v_accvgpr_write_b32 a48, v144\n" \
  "v_accvgpr_write_b32 a49, v145\n" \
  "v_accvgpr_write_b32 a50, v146\n" \
  "v_accvgpr_write_b32 a51, v147\n" \
  "v_accvgpr_write_b32 a52, v148\n" \
  "v_accvgpr_write_b32 a53, v149\n" \
  "v_accvgpr_write_b32 a54, v150\n" \
  "v_accvgpr_write_b32 a55, v151\n" \
  "v_accvgpr_write_b32 a56, v152\n" \
  "v_accvgpr_write_b32 a57, v153\n" \
  "v_accvgpr_write_b32 a58, v154\n" \
  "v_accvgpr_write_b32 a59, v155\n" \
  "v_accvgpr_write_b32 a60, v156\n" \
  "v_accvgpr_write_b32 a61, v157\n" \
  "v_accvgpr_write_b32 a62, v158\n" \
  "v_accvgpr_write_b32 a63, v159\n" \
  "s_branch .LF_w_b_%=\n" \
  ".LF_w3_%=:\n" \
  "v_accvgpr_write_b32 a64, v128\n" \
  "v_accvgpr_write_b32 a65, v129\n" \
  "v_accvgpr_write_b32 a66, v130\n" \
  "v_accvgpr_write_b32 a67, v131\n" \
  "v_accvgpr_write_b32 a68, v132\n" \
  "v_accvgpr_write_b32 a69, v133\n" \
  "v_accvgpr_write_b32 a70, v134\n" \
  "v_accvgpr_write_b32 a71, v135\n" \
  "v_accvgpr_write_b32 a72, v136\n" \
  "v_accvgpr_write_b32 a73, v137\n" \
  "v_accvgpr_write_b32 a74, v138\n" \
  "v_accvgpr_write_b32 a75, v139\n" \
  "v_accvgpr_write_b32 a76, v140\n" \
  "v_accvgpr_write_b32 a77, v141\n" \
  "v_accvgpr_write_b32 a78, v142\n" \
  "v_accvgpr_write_b32 a79, v143\n" \
  "v_accvgpr_write_b32 a80, v144\n" \
  "v_accvgpr_write_b32 a81, v145\n" \
  "v_accvgpr_write_b32 a82, v146\n" \
  "v_accvgpr_write_b32 a83, v147\n" \
  "v_accvgpr_write_b32 a84, v148\n" \
  "v_accvgpr_write_b32 a85, v149\n" \
  "v_accvgpr_write_b32 a86, v150\n" \
  "v_accvgpr_write_b32 a87, v151\n" \
  "v_accvgpr_write_b32 a88, v152\n" \
  "v_accvgpr_write_b32 a89, v153\n" \
  "v_accvgpr_write_b32 a90, v154\n" \
  "v_accvgpr_write_b32 a91, v155\n" \
  "v_accvgpr_write_b32 a92, v156\n" \
  "v_accvgpr_write_b32 a93, v157\n" \
  "v_accvgpr_write_b32 a94, v158\n" \
  "v_accvgpr_write_b32 a95, v159\n" \
  "s_branch .LF_w_b_%=\n" \
  ".LF_w4_%=:\n" \
  "v_accvgpr_write_b32 a96, v128\n" \
  "v_accvgpr_write_b32 a97, v129\n" \
  "v_accvgpr_write_b32 a98, v130\n" \
  "v_accvgpr_write_b32 a99, v131\n" \
  "v_accvgpr_write_b32 a100, v132\n" \
  "v_accvgpr_write_b32 a101, v133\n" \
  "v_accvgpr_write_b32 a102, v134\n" \
  "v_accvgpr_write_b32 a103, v135\n" \
  "v_accvgpr_write_b32 a104, v136\n" \
  "v_accvgpr_write_b32 a105, v137\n" \
  "v_accvgpr_write_b32 a106, v138\n" \
  "v_accvgpr_write_b32 a107, v139\n" \
  "v_accvgpr_write_b32 a108, v140\n" \
  "v_accvgpr_write_b32 a109, v141\n" \
  "v_accvgpr_write_b32 a110, v142\n" \
  "v_accvgpr_write_b32 a111, v143\n" \
  "v_accvgpr_write_b32 a112, v144\n" \
  "v_accvgpr_write_b32 a113, v145\n" \
  "v_accvgpr_write_b32 a114, v146\n" \
  "v_accvgpr_write_b32 a115, v147\n" \
  "v_accvgpr_write_b32 a116, v148\n" \
  "v_accvgpr_write_b32 a117, v149\n" \
  "v_accvgpr_write_b32 a118, v150\n" \
  "v_accvgpr_write_b32 a119, v151\n" \
  "v_accvgpr_write_b32 a120, v152\n" \
  "v_accvgpr_write_b32 a121, v153\n" \
  "v_accvgpr_write_b32 a122, v154\n" \
  "v_accvgpr_write_b32 a123, v155\n" \
  "v_accvgpr_write_b32 a124, v156\n" \
  "v_accvgpr_write_b32 a125, v157\n" \
  "v_accvgpr_write_b32 a126, v158\n" \
  "v_accvgpr_write_b32 a127, v159\n" \
  "s_branch .LF_w_b_%=\n" \
  ".LF_w5_%=:\n" \
  "v_accvgpr_write_b32 a128, v128\n" \
  "v_accvgpr_write_b32 a129, v129\n" \
  "v_accvgpr_write_b32 a130, v130\n" \
  "v_accvgpr_write_b32 a131, v131\n" \
  "v_accvgpr_write_b32 a132, v132\n" \
  "v_accvgpr_write_b32 a133, v133\n" \
  "v_accvgpr_write_b32 a134, v134\n" \
  "v_accvgpr_write_b32 a135, v135\n" \
  "v_accvgpr_write_b32 a136, v136\n" \
  "v_accvgpr_write_b32 a137, v137\n" \
  "v_accvgpr_write_b32 a138, v138\n" \
  "v_accvgpr_write_b32 a139, v139\n" \
  "v_accvgpr_write_b32 a140, v140\n" \
  "v_accvgpr_write_b32 a141, v141\n" \
  "v_accvgpr_write_b32 a142, v142\n" \
  "v_accvgpr_write_b32 a143, v143\n" \
  "v_accvgpr_write_b32 a144, v144\n" \
  "v_accvgpr_write_b32 a145, v145\n" \
  "v_accvgpr_write_b32 a146, v146\n" \
  "v_accvgpr_write_b32 a147, v147\n" \
  "v_accvgpr_write_b32 a148, v148\n" \
  "v_accvgpr_write_b32 a149, v149\n" \
  "v_accvgpr_write_b32 a150, v150\n" \
  "v_accvgpr_write_b32 a151, v151\n" \
  "v_accvgpr_write_b32 a152, v152\n" \
  "v_accvgpr_write_b32 a153, v153\n" \
  "v_accvgpr_write_b32 a154, v154\n" \
  "v_accvgpr_write_b32 a155, v155\n" \
  "v_accvgpr_write_b32 a156, v156\n" \
  "v_accvgpr_write_b32 a157, v157\n" \
  "v_accvgpr_write_b32 a158, v158\n" \
  "v_accvgpr_write_b32 a159, v159\n" \
  "s_branch .LF_w_b_%=\n" \
  ".LF_r1_%=:\n" \
  "v_mov_b32 v192, v215\n" \
  "v_mov_b32 v193, v216\n" \
  "v_mov_b32 v194, v217\n" \
  "s_waitcnt vmcnt(7)\n" \
  "v_smfmac_f32_16x16x64_bf16 v[40:43], v[8:11], v[0:7], v200\n" \
  "s_waitcnt vmcnt(6)\n" \
  "v_smfmac_f32_16x16x64_bf16 v[40:43], v[24:27], a[32:39], v201\n" \
  "s_waitcnt vmcnt(5)\n" \
  "v_smfmac_f32_16x16x64_bf16 v[44:47], v[12:15], v[0:7], v200\n" \
  "s_waitcnt vmcnt(4)\n" \
  "v_smfmac_f32_16x16x64_bf16 v[44:47], v[28:31], a[40:47], v201\n" \
  "s_waitcnt vmcnt(3)\n" \
  "v_smfmac_f32_16x16x64_bf16 v[48:51], v[16:19], v[0:7], v200\n" \
  "s_waitcnt vmcnt(2)\n" \
  "v_smfmac_f32_16x16x64_bf16 v[48:51], v[32:35], a[48:55], v201\n" \
  "s_waitcnt vmcnt(1)\n" \
  "v_smfmac_f32_16x16x64_bf16 v[52:55], v[20:23], v[0:7], v200\n" \
  "s_waitcnt vmcnt(0)\n" \
  "v_smfmac_f32_16x16x64_bf16 v[52:55], v[36:39], a[56:63], v201\n" \
  "s_branch .LF_r_b_%=\n" \
  ".LF_r2_%=:\n" \
  "v_mov_b32 v192, v218\n" \
  "v_mov_b32 v193, v219\n" \
  "v_mov_b32 v194, v220\n" \
  "s_waitcnt vmcnt(7)\n" \
  "v_smfmac_f32_16x16x64_bf16 v[40:43], v[8:11], v[0:7], v200\n" \
  "s_waitcnt vmcnt(6)\n" \
  "v_smfmac_f32_16x16x64_bf16 v[40:43], v[24:27], a[64:71], v201\n" \
  "s_waitcnt vmcnt(5)\n" \
  "v_smfmac_f32_16x16x64_bf16 v[44:47], v[12:15], v[0:7], v200\n" \
  "s_waitcnt vmcnt(4)\n" \
  "v_smfmac_f32_16x16x64_bf16 v[44:47], v[28:31], a[72:79], v201\n" \
  "s_waitcnt vmcnt(3)\n" \
  "v_smfmac_f32_16x16x64_bf16 v[48:51], v[16:19], v[0:7], v200\n" \
  "s_waitcnt vmcnt(2)\n" \
  "v_smfmac_f32_16x16x64_bf16 v[48:51], v[32:35], a[80:87], v201\n" \
  "s_waitcnt vmcnt(1)\n" \
  "v_smfmac_f32_16x16x64_bf16 v[52:55], v[20:23], v[0:7], v200\n" \
  "s_waitcnt vmcnt(0)\n" \
  "v_smfmac_f32_16x16x64_bf16 v[52:55], v[36:39], a[88:95], v201\n" \
  "s_branch .LF_r_b_%=\n" \
  ".LF_r3_%=:\n" \
  "v_mov_b32 v192, v221\n" \
  "v_mov_b32 v193, v222\n" \
  "v_mov_b32 v194, v223\n" \
  "s_waitcnt vmcnt(7)\n" \
  "v_smfmac_f32_16x16x64_bf16 v[40:43], v[8:11], v[0:7], v200\n" \
  "s_waitcnt vmcnt(6)\n" \
  "v_smfmac_f32_16x16x64_bf16 v[40:43], v[24:27], a[96:103], v201\n" \
  "s_waitcnt vmcnt(5)\n" \
  "v_smfmac_f32_16x16x64_bf16 v[44:47], v[12:15], v[0:7], v200\n" \
  "s_waitcnt vmcnt(4)\n" \
  "v_smfmac_f32_16x16x64_bf16 v[44:47], v[28:31], a[104:111], v201\n" \
  "s_waitcnt vmcnt(3)\n" \
  "v_smfmac_f32_16x16x64_bf16 v[48:51], v[16:19], v[0:7], v200\n" \
  "s_waitcnt vmcnt(2)\n" \
  "v_smfmac_f32_16x16x64_bf16 v[48:51], v[32:35], a[112:119], v201\n" \
  "s_waitcnt vmcnt(1)\n" \
  "v_smfmac_f32_16x16x64_bf16 v[52:55], v[20:23], v[0:7], v200\n" \
  "s_waitcnt vmcnt(0)\n" \
  "v_smfmac_f32_16x16x64_bf16 v[52:55], v[36:39], a[120:127], v201\n" \
  "s_branch .LF_r_b_%=\n" \
  ".LF_r4_%=:\n" \
  "v_mov_b32 v192, v224\n" \
  "v_mov_b32 v193, v225\n" \
  "v_mov_b32 v194, v226\n" \
  "s_waitcnt vmcnt(7)\n" \
  "v_smfmac_f32_16x16x64_bf16 v[40:43], v[8:11], v[0:7], v200\n" \
  "s_waitcnt vmcnt(6)\n" \
  "v_smfmac_f32_16x16x64_bf16 v[40:43], v[24:27], a[128:135], v201\n" \
  "s_waitcnt vmcnt(5)\n" \
  "v_smfmac_f32_16x16x64_bf16 v[44:47], v[12:15], v[0:7], v200\n" \
  "s_waitcnt vmcnt(4)\n" \
  "v_smfmac_f32_16x16x64_bf16 v[44:47], v[28:31], a[136:143], v201\n" \
  "s_waitcnt vmcnt(3)\n" \
  "v_smfmac_f32_16x16x64_bf16 v[48:51], v[16:19], v[0:7], v200\n" \
  "s_waitcnt vmcnt(2)\n" \
  "v_smfmac_f32_16x16x64_bf16 v[48:51], v[32:35], a[144:151], v201\n" \
  "s_waitcnt vmcnt(1)\n" \
  "v_smfmac_f32_16x16x64_bf16 v[52:55], v[20:23], v[0:7], v200\n" \
  "s_waitcnt vmcnt(0)\n" \
  "v_smfmac_f32_16x16x64_bf16 v[52:55], v[36:39], a[152:159], v201\n" \
  "s_branch .LF_r_b_%=\n" \
  ".LF_r5_%=:\n" \
  "v_mov_b32 v192, v227\n" \
  "v_mov_b32 v193, v228\n" \
  "v_mov_b32 v194, v229\n" \
  "s_waitcnt vmcnt(7)\n" \
  "v_smfmac_f32_16x16x64_bf16 v[40:43], v[8:11], v[0:7], v200\n" \
  "s_waitcnt vmcnt(6)\n" \
  "v_smfmac_f32_16x16x64_bf16 v[40:43], v[24:27], a[160:167], v201\n" \
  "s_waitcnt vmcnt(5)\n" \
  "v_smfmac_f32_16x16x64_bf16 v[44:47], v[12:15], v[0:7], v200\n" \
  "s_waitcnt vmcnt(4)\n" \
  "v_smfmac_f32_16x16x64_bf16 v[44:47], v[28:31], a[168:175], v201\n" \
  "s_waitcnt vmcnt(3)\n" \
  "v_smfmac_f32_16x16x64_bf16 v[48:51], v[16:19], v[0:7], v200\n" \
  "s_waitcnt vmcnt(2)\n" \
  "v_smfmac_f32_16x16x64_bf16 v[48:51], v[32:35], a[176:183], v201\n" \
  "s_waitcnt vmcnt(1)\n" \
  "v_smfmac_f32_16x16x64_bf16 v[52:55], v[20:23], v[0:7], v200\n" \
  "s_waitcnt vmcnt(0)\n" \
  "v_smfmac_f32_16x16x64_bf16 v[52:55], v[36:39], a[184:191], v201\n" \
  "s_branch .LF_r_b_%=\n" \
  ".LF_bad_%=:\n" \
  "v_max_u32 v60, v0, v1\n" \
  "v_max3_u32 v60, v60, v2, v3\n" \
  "v_max3_u32 v60, v60, v4, v5\n" \
  "v_max3_u32 v60, v60, v6, v7\n" \
  "v_max3_u32 v60, v60, v8, v9\n" \
  "v_max3_u32 v60, v60, v10, v11\n" \
  "v_max3_u32 v60, v60, v12, v13\n" \
  "v_max3_u32 v60, v60, v14, v15\n" \
  "v_max3_u32 v60, v60, v16, v17\n" \
  "v_max3_u32 v60, v60, v18, v19\n" \
  "v_max3_u32 v60, v60, v20, v21\n" \
  "v_max3_u32 v60, v60, v22, v23\n" \
  "v_max3_u32 v60, v60, v24, v25\n" \
  "v_max3_u32 v60, v60, v26, v27\n" \
  "v_max3_u32 v60, v60, v28, v29\n" \
  "v_max3_u32 v60, v60, v30, v31\n" \
  "v_max3_u32 v60, v60, v32, v33\n" \
  "v_max3_u32 v60, v60, v34, v35\n" \
  "v_max3_u32 v60, v60, v36, v37\n" \
  "v_max3_u32 v60, v60, v38, v39\n" \
  "v_cmp_eq_u32 vcc, -1, v60\n" \
  "s_cbranch_vccz .LF_ok_%=\n" \
  "v_mov_b64 v[40:41], 0\n" \
  "v_mov_b64 v[42:43], 0\n" \
  "v_mov_b64 v[44:45], 0\n" \
  "v_mov_b64 v[46:47], 0\n" \
  "v_mov_b64 v[48:49], 0\n" \
  "v_mov_b64 v[50:51], 0\n" \
  "v_mov_b64 v[52:53], 0\n" \
  "v_mov_b64 v[54:55], 0\n" \
  "s_sleep 2\n" \
  "s_cmp_eq_u32 s48, 5\n" \
  "s_cbranch_scc1 .LF_bd1_%=\n" \
  "s_sleep 20\n" \
  ".LF_bd1_%=:\n" \
  "s_add_u32 s69, s69, 1\n" \
  "s_and_b32 s63, s69, 1023\n" \
  "s_cbranch_scc1 .LF_rd_%=\n" \
  "buffer_load_dwordx2 v[60:61], off, s[40:43], %40 offset:128 sc1\n" \
  "s_waitcnt vmcnt(0)\n" \
  "v_readfirstlane_b32 s63, v61\n" \
  "s_cmp_eq_u32 s63, %32\n" \
  "s_cbranch_scc1 .LF_ok_%=\n" \
  "s_cmp_le_u32 s69, 2097152\n" \
  "s_cbranch_scc1 .LF_rd_%=\n" \
  "v_mov_b32 v60, 1\n" \
  "v_mov_b32 v61, %32\n" \
  "buffer_store_dwordx2 v[60:61], off, s[40:43], %40 offset:128 sc1\n" \
  "s_branch .LF_ok_%=\n" \
  ".LF_end_%=:\n"
#define F_ASM_VOPS "+{v200}"(fi.sidx), "+{v201}"(fi.cidx), "+{v202}"(fi.l16), "+{v203}"(fi.ae0), "+{v204}"(fi.ae1), "+{v205}"(fi.ae2), "+{v206}"(fi.ae3), "+{v207}"(fi.am), "+{v208}"(fi.lw), "+{v209}"(fi.lr), "+{v210}"(fi.oo), "+{v211}"(fi.d0), "+{v212}"(fi.g[0]), "+{v213}"(fi.b[0]), "+{v214}"(fi.c[0]), "+{v215}"(fi.g[1]), "+{v216}"(fi.b[1]), "+{v217}"(fi.c[1]), "+{v218}"(fi.g[2]), "+{v219}"(fi.b[2]), "+{v220}"(fi.c[2]), "+{v221}"(fi.g[3]), "+{v222}"(fi.b[3]), "+{v223}"(fi.c[3]), "+{v224}"(fi.g[4]), "+{v225}"(fi.b[4]), "+{v226}"(fi.c[4]), "+{v227}"(fi.g[5]), "+{v228}"(fi.b[5]), "+{v229}"(fi.c[5])
#define F_ASM_SOPS "s"(fi.ws), "s"(fi.out), "s"(fi.tag), "s"(fi.o_u), "s"(fi.o_e), "s"(fi.o_x), "s"(fi.o_va), "s"(fi.o_tw), "s"(fi.xa), "s"(fi.xb), "s"(fi.o_ctl), "s"(fi.pa), "s"(fi.w), "s"(fi.f_w)
#define F_ASM_CLOB "v0", "v1", "v2", "v3", "v4", "v5", "v6", "v7", "v8", "v9", "v10", "v11", "v12", "v13", "v14", "v15", "v16", "v17", "v18", "v19", "v20", "v21", "v22", "v23", "v24", "v25", "v26", "v27", "v28", "v29", "v30", "v31", "v32", "v33", "v34", "v35", "v36", "v37", "v38", "v39", "v40", "v41", "v42", "v43", "v44", "v45", "v46", "v47", "v48", "v49", "v50", "v51", "v52", "v53", "v54", "v55", "v56", "v57", "v58", "v59", "v60", "v61", "v62", "v63", "v64", "v65", "v66", "v67", "v68", "v69", "v70", "v71", "v72", "v73", "v74", "v75", "v76", "v77", "v78", "v79", "v80", "v81", "v82", "v83", "v84", "v85", "v86", "v87", "v88", "v89", "v90", "v91", "v92", "v93", "v94", "v95", "v96", "v97", "v98", "v99", "v100", "v101", "v102", "v103", "v104", "v105", "v106", "v107", "v108", "v109", "v110", "v111", "v112", "v113", "v114", "v115", "v116", "v117", "v118", "v119", "v120", "v121", "v122", "v123", "v124", "v125", "v126", "v127", "v128", "v129", "v130", "v131", "v132", "v133", "v134", "v135", "v136", "v137", "v138", "v139", "v140", "v141", "v142", "v143", "v144", "v145", "v146", "v147", "v148", "v149", "v150", "v151", "v152", "v153", "v154", "v155", "v156", "v157", "v158", "v159", "v160", "v161", "v162", "v163", "v164", "v165", "v166", "v167", "v168", "v169", "v170", "v171", "v172", "v173", "v174", "v175", "v176", "v177", "v178", "v179", "v180", "v181", "v182", "v183", "v184", "v185", "v186", "v187", "v188", "v189", "v190", "v191", "v192", "v193", "v194", "v195", "v196", "v197", "v232", "v233", "v234", "v235", "v236", "v237", "a0", "a1", "a2", "a3", "a4", "a5", "a6", "a7", "a8", "a9", "a10", "a11", "a12", "a13", "a14", "a15", "a16", "a17", "a18", "a19", "a20", "a21", "a22", "a23", "a24", "a25", "a26", "a27", "a28", "a29", "a30", "a31", "a32", "a33", "a34", "a35", "a36", "a37", "a38", "a39", "a40", "a41", "a42", "a43", "a44", "a45", "a46", "a47", "a48", "a49", "a50", "a51", "a52", "a53", "a54", "a55", "a56", "a57", "a58", "a59", "a60", "a61", "a62", "a63", "a64", "a65", "a66", "a67", "a68", "a69", "a70", "a71", "a72", "a73", "a74", "a75", "a76", "a77", "a78", "a79", "a80", "a81", "a82", "a83", "a84", "a85", "a86", "a87", "a88", "a89", "a90", "a91", "a92", "a93", "a94", "a95", "a96", "a97", "a98", "a99", "a100", "a101", "a102", "a103", "a104", "a105", "a106", "a107", "a108", "a109", "a110", "a111", "a112", "a113", "a114", "a115", "a116", "a117", "a118", "a119", "a120", "a121", "a122", "a123", "a124", "a125", "a126", "a127", "a128", "a129", "a130", "a131", "a132", "a133", "a134", "a135", "a136", "a137", "a138", "a139", "a140", "a141", "a142", "a143", "a144", "a145", "a146", "a147", "a148", "a149", "a150", "a151", "a152", "a153", "a154", "a155", "a156", "a157", "a158", "a159", "a160", "a161", "a162", "a163", "a164", "a165", "a166", "a167", "a168", "a169", "a170", "a171", "a172", "a173", "a174", "a175", "a176", "a177", "a178", "a179", "a180", "a181", "a182", "a183", "a184", "a185", "a186", "a187", "a188", "a189", "a190", "a191", "s40", "s41", "s42", "s43", "s44", "s45", "s46", "s47", "s48", "s49", "s50", "s51", "s52", "s53", "s54", "s55", "s56", "s57", "s58", "s59", "s60", "s61", "s62", "s63", "s64", "s65", "s66", "s67", "s68", "s69", "s70", "s71", "s72", "s73", "s74", "s75", "s76", "s77", "s78", "s79", "s80", "s81", "s82", "s83", "s84", "s85", "s86", "s87", "s88", "s89", "s90", "s91", "s92", "s93", "s94", "s95", "s96", "s97", "s98", "s99", "vcc", "scc", "memory"

struct RedIn {
  unsigned sidx, cidx, l16, ax, am, mo, sel, to, fu, p0, p1, p2, p3, d0, d1, d2, d3, d4;
  const void* ws;
  unsigned w, go, tag, o_va, o_vb, lab, o_u, kind, o_ctl, pa, pb, pc, f_d, f_r, ph;
};
__device__ __forceinline__ void red_role(const Params& p, int w, int T, char* smem) {
  const int grp = T & 3;
  CODE_PAD(PAD_RED);
  char* const ws = p.ws;
  Ctx cx = {ws, 0u};
  const int l = lane_id(), n = l & 15, g = l >> 4;
  comm_init(cx, grp * 9 + (T >> 2), w, smem);
  Bulk bk;
  bk.init(cx, grp);
  bk.pl.load(cx, grp);
  const Place& pl = bk.pl;
  char* const base = comm_base(ws) - CO;
  const unsigned dl = (unsigned)(ws - base);
  RedIn ri;
  ri.sidx = (n & 8) ? 0xEEEEu : 0x4444u;
  ri.cidx = (n & 3) * 0x5555u;
  ri.l16 = l * 16;
  ri.ax = W_PX * 32768 + (n >> 3) * 512 + g * 128 + (n & 7) * 16;
  ri.am = W_PM * 32768 + l * 16;
  const unsigned mt = (W_MEM + (T >> 4)) * 32768 + (T & 15) * 256;
  ri.mo = mt + (2 * (g & 1) + (g >> 1)) * 64 + (n >> 3) * 32 + (n & 1) * 16 + ((n >> 1) & 3) * 4;
  ri.sel = (n & 1) ? 0x03020706u : 0x05040100u;
  ri.to = l < 4 ? CO + TW_OFF + (16384u ^ pl.dyn(l)) + 256 : 0x7F000000u;
  ri.fu = CO + pl.near(N_U + min(10 * w - 10 + min(n, 11), w == 3 ? 31 : 10 * w - 1)) + (T < 32 ? (T >> 1) & 3 : T & 3) * 8 + (l & 32);
  ri.d0 = 0; ri.d1 = 0; ri.d2 = 0; ri.d3 = 0; ri.d4 = 0;
  ri.ws = base; ri.w = w; ri.go = pl.go; ri.tag = cx.tag; ri.lab = grp; ri.o_ctl = dl;
  ri.o_va = __builtin_amdgcn_readfirstlane(CO + (win_static(W_VO + 2 * (T >> 2)) ^ pl.dyn(grp)));
  ri.o_vb = __builtin_amdgcn_readfirstlane(CO + (win_static(W_VO + 2 * (T >> 2) + 1) ^ pl.dyn(grp)));
  auto dead = [&](int i) { return i < 0 ? CO + (16384u ^ pl.dyn(g)) + mt + n * 16 : (i < 4 ? ri.o_va : ri.o_vb) + (i & 3) * 1024 + l * 16; };
  ri.p0 = w ? dead(3 * w - 4) : T < 8 && l < 4 ? ri.to - 116 : 0x7F000000u;
  ri.p1 = w ? dead(3 * w - 3) : l < 4 ? 0u : 0x7F000000u; ri.p2 = dead(3 * w - 2);
  const int pz = (T >> 2) * 3 + w - 1;
  ri.p3 = pz < 24 ? CO + (16384u ^ pl.dyn(grp)) + (W_PX + (pz >> 2)) * 32768 + (pz & 3) * 1024 + l * 16 : 0x7F000000u;
  ri.o_u = __builtin_amdgcn_readfirstlane(u_tile(T, 0, 0) ^ (~bk.w13 & 8192u));
  ri.kind = __builtin_amdgcn_readfirstlane(T == 35 ? CO + TW_OFF + (16384u ^ pl.dyn(0)) + 512 : 0u);
  ri.pa = __builtin_amdgcn_readfirstlane(0) + (p.slow & 255) | (p.vlead << 16) | (p.push3 << 24);
  ri.pb = p.dret | (p.late_r << 16);
  ri.pc = p.t_first | (p.look << 16);
  ri.ph = p.hdel | (p.hdl2 << 8) | 0x10000;
  ri.f_d = __builtin_amdgcn_readfirstlane(T < 32 ? CO + pl.near(N_RD + T) : 0x7F000000u);
  ri.f_r = dl + F_R * 128 + T * 8;
  asm volatile(R_ASM_TEXT : R_ASM_VOPS : R_ASM_SOPS : R_ASM_CLOB);
}

__device__ __forceinline__ int c_line(int h) { return (h >> 2) * 512 + (h & 1) * 256 + (h & 2) * 64; }
struct ChainIn {
  unsigned sidx, l16, qw, kw, fq, fk, kxo, pst, ps2, lx, sid2, qxo, sto, plx, mk, ck0, ck2, hx, viw, fva, pk3, fvs, uo, po0, po1, po2, co0, co1, co2;
  unsigned vq0, vq1, vq2, vq3, vo0, vo1, vo2, vo3, v50, v51, v52, v53, tro0, tro1, tro2, tro3, tro4, m0o;
  const void *ws, *Wa, *Wb, *mem0;
  unsigned w, o_a, o_b, o_nx, o_exp, o_u, lab, f_p, f_m, f_u, f_dur, f_go, o_ctl, o_dbg, push, dret, u5, o_qx, f_s, f_r;
};

#define CODE_END ".Lcode_end:\n"
template <bool SC>
__device__ __forceinline__ void chain_role(const Params& p, int w, int h, int grp) {
  constexpr unsigned QT = 9216, KT = 13312, SX = 16384, VI = 9216, VX = 16384;
  const int l = lane_id(), n = l & 15, g = l >> 4, q4 = (l >> 2) & 3, p4 = l & 3;
  const int ty = (n >> 1) & 1, row = 2 * (n >> 2) + (n & 1), cid = h * 2 + (SC ? 0 : 1);
  char* const base = comm_base(p.ws) - CO;
  const unsigned dl = (unsigned)(p.ws - base);
  ChainIn ci;
  ci.sidx = (n & 2) ? 0xEEEEu : 0x4444u;
  ci.l16 = l * 16;
  ci.m0o = row * (DM * 2) + g * 32 + ty * 16;
  if (SC) {
    ci.qw = QT + (32 * w + n) * 16 + g * 4;
    ci.kw = KT + (32 * w + n) * 16 + (((g & 1) << 1) | (g >> 1)) * 4;
    ci.fq = QT + (4 * g + q4) * 16 + (p4 & 1) * 8;
    ci.fk = KT + (4 * g + q4) * 16 + (p4 >> 1) * 8;
    ci.kxo = (g * 16 + 8 * (q4 & 1) + 4 * (q4 >> 1) + p4) * 16;
    const unsigned ex = CO + win_static(W_PX + (h >> 3)) + (h & 7) * 512 + (g & 1) * 128 + (n & 7) * 16 + (g >> 1) * 8 + (n >> 3);
    if (w < 2) ci.pst = ex + w * 256;
    else if (g & 1) ci.pst = CO + win_static(W_PC) + c_line(h) + (n & 7) * 16 + (((n >> 2) ^ (g >> 1)) & 1) * 8 + 2 * (w & 1) + (n >> 3);
    else ci.pst = CO + win_static(W_PM) + (h >> 1) * 256 + (g >> 1) * 128 + (n & 7) * 16 + (h & 1) * 8 + 2 * (w & 1) + (n >> 3);
    ci.ps2 = w < 2 ? ci.pst + 2 : ex + 2 * (w & 1);
    ci.mk = (g & 1) ? 0u : 0x3F800000u;
    ci.ck0 = g == 1 && !(h & 2) ? 0x3F800000u : 0u;
    ci.ck2 = g == 1 && (h & 2) ? 0x3F800000u : 0u;
    ci.hx = SX + 2048 + (w & 1) * 512 + l * 8;
    ci.lx = SX + l * 8 + (w == 1 ? 4 : 0);
    ci.qxo = (w >> 1) * 1024 + (g * 16 + 8 * (w & 1) + (n & 7)) * 16;
    ci.sto = 0x80000 + h * 256 + (8 * w + (n & 7)) * 8;
    ci.plx = n >= 8 ? 0x7F000000u : w * 16384 + n * 16 + ((g & 1) ? 4096 + c_line(h) + (((n >> 2) ^ (g >> 1)) & 1) * 8 : h * 128 + (g >> 1) * 8);
  } else {
    ci.viw = VI + (32 * w + n) * 16 + g * 4;
    ci.fva = VI + (4 * g + q4) * 16 + (p4 & 1) * 8;
    ci.pk3 = VI + n * 16 + g * 4; ci.fvs = w * 1024 + l * 16;
    ci.uo = w == 1 ? l * 16 : 0x7F000000u;
    auto vo_at = [&](int T0) {
      const int T = T0 + 2 * w + (g >> 1);
      return CO + win_static(W_VO + 2 * (T >> 2) + (h >> 4)) + (h & 15) * 256 + n * 16 + (T & 3);
    };
    ci.po0 = vo_at(8 * (g & 1)); ci.po1 = vo_at(16 + 8 * (g & 1));
    auto co_at = [&](int T0) { return (T0 + 2 * w + (g >> 1)) * 16384 + h * 256 + n * 16; };
    ci.co0 = co_at(8 * (g & 1)); ci.co1 = co_at(16 + 8 * (g & 1)); ci.co2 = co_at(w == 0 ? 16 : w == 1 ? 22 : 28) + (g & 1) * 8;
    if (w == 0) ci.po1 = h < 4 && l < 4 ? CO + TW_OFF + 16384 + 384 + l : 0x7F000000u;
    ci.po2 = vo_at(w == 0 ? 16 : w == 1 ? 22 : 28) + (g & 1) * 8;
  }
  ci.sid2 = (n & 8) ? 0xEEEEu : 0x4444u;
  unsigned vq[4], vo[4], v5[4];
#pragma unroll
  for (int i = 0; i < 4; ++i) {
    const int ch = n ^ (2 * ((g & 1) | (i << 1))), tp = ch >> 1;
    vq[i] = (g + 4 * i) * (HD * 2) + ch * 16 - 1024 * i;
    vo[i] = (g + 4 * i) * (DM * 2) + ch * 16 - 1024 * i;
    v5[i] = (g + 4 * i + 16 - 16 * (tp >> 2)) * (DM * 2) + (32 + (tp & 3)) * 32 + (ch & 1) * 16 - 1024 * i;
  }
  ci.vq0 = vq[0]; ci.vq1 = vq[1]; ci.vq2 = vq[2]; ci.vq3 = vq[3];
  ci.vo0 = vo[0]; ci.vo1 = vo[1]; ci.vo2 = vo[2]; ci.vo3 = vo[3];
  ci.v50 = v5[0]; ci.v51 = v5[1]; ci.v52 = v5[2]; ci.v53 = v5[3];
  {
    const int rowl = 16 * (g >> 1) + 8 * (q4 >> 1) + 4 * (g & 1) + (q4 & 1), rho = (rowl & 1) | (((rowl >> 2) & 3) << 1);
    ci.tro0 = rowl * 256 + (((4 * w + (p4 >> 1)) ^ (2 * rho)) * 16) + (p4 & 1) * 8;
    ci.tro1 = rowl * 256 + (((4 * w + 2 + (p4 >> 1)) ^ (2 * rho)) * 16) + (p4 & 1) * 8;
    const int row2 = 16 * (g & 1) + 4 * (g >> 1) + (q4 & 1), rho2 = (row2 & 1) | (((row2 >> 2) & 3) << 1);
    ci.tro2 = row2 * 256 + (((4 * w + 2 * (q4 >> 1) + (p4 >> 1)) ^ (2 * rho2)) * 16) + (p4 & 1) * 8;
    ci.tro3 = w < 2 ? (ci.tro2 ^ (w * 64)) + 7 * 16384 : ci.tro2 + 9 * 16384;
    ci.tro4 = w < 2 ? ci.tro3 + 16384 : ci.tro3 ^ 128u;
  }
  ci.ws = base; ci.Wa = SC ? p.Wq : p.Wv; ci.Wb = SC ? p.Wk : p.Wo; ci.mem0 = p.mem0;
  ci.w = w;
  ci.o_a = 16 * w * (HD * 2) + h * 256;
  ci.o_b = (h * DH + 32 * w) * (DM * 2);
  ci.o_nx = near_head(B_K, h) + (w & 1) * 4096;
  ci.o_exp = near_head(B_K, h) + (SC ? 0 : 4096);
  ci.o_u = 0;
  ci.u5 = w == 1 ? 26 * 65536 + 3 * 16384 : 36 * 65536 + 8192;
  ci.o_qx = PB + 2 * B_O + (SC ? h * 16384 : 0); ci.f_s = __builtin_amdgcn_readfirstlane(dl + (F_S + h) * 128);
  ci.f_r = dl + F_R * 128;
  ci.lab = grp;
  ci.f_p = __builtin_amdgcn_readfirstlane(CO + near_static(N_K + h) + (SC ? 0 : 8));
  ci.f_m = SC ? dl + (F_M + cid) * 128 + (w & 1) * 8 : w == 1 ? dl + (F_M + cid) * 128 : 0x7F000000u; ci.f_u = 0; ci.f_dur = __builtin_amdgcn_readfirstlane(CO + near_static(N_DUR + h));
  ci.f_go = dl + (F_GO + grp) * 128; ci.o_ctl = dl;
  ci.o_dbg = dl + (unsigned)WS_END + (cid * 4 + w) * 4096;
  ci.push = __builtin_amdgcn_readfirstlane((w == 0 && (SC ? h < 4 : h >= 24)) + (p.gate << 8));
  ci.dret = p.dret | (p.late_s << 16);
  if (SC) asm volatile(X_ASM_TEXT M_ASM_TEXT CODE_END : X_ASM_VOPS : X_ASM_SOPS : X_ASM_CLOB);
  else asm volatile(W_ASM_TEXT V_ASM_TEXT : V_ASM_VOPS : V_ASM_SOPS : V_ASM_CLOB);
}

struct ProjIn {
  unsigned l16, xr0, xr1, ew, er, uo, xs0, xs1, xs2, xs3, xs4, xs5, xs6, xs7, xs8;
  unsigned vq0, vq1, vq2, vq3, vo0, vo1, vo2, vo3, v50, v51, v52, v53, tro0, tro1;
  const void *ws, *Wa, *Wb, *x;
  unsigned w, o_a, o_b, o_p, o_q, f_a, f_b, f_go, o_ctl, f_c, u8, rng, f_d;
};
template <bool VU, bool QO = false>
__device__ __forceinline__ unsigned proj_role(const Params& p, int w, int h) {
  const int l = lane_id(), n = l & 15, g = l >> 4, q4 = (l >> 2) & 3, p4 = l & 3;
  char* const base = comm_base(p.ws) - CO;
  const unsigned dl = (unsigned)(p.ws - base);
  ProjIn pi;
  pi.l16 = l * 16; pi.uo = 0;
  unsigned xs[9];
#pragma unroll
  for (int i = 0; i < 9; ++i) {
    const int ci = (9 * w + i) * 64 + l, r = ci / 72, cc = ci - r * 72;
    xs[i] = (r * 72 + (cc ^ ((r >> 1) & 7))) * 16;
  }
  pi.xs0 = xs[0]; pi.xs1 = xs[1]; pi.xs2 = xs[2]; pi.xs3 = xs[3]; pi.xs4 = xs[4]; pi.xs5 = xs[5]; pi.xs6 = xs[6]; pi.xs7 = xs[7]; pi.xs8 = xs[8];
  pi.xr0 = 16384 + n * 1152 + ((g ^ ((n >> 1) & 7)) * 16); pi.xr1 = 16384 + n * 1152 + (((4 + g) ^ ((n >> 1) & 7)) * 16);
  pi.ew = w * 2048 + n * 64 + ((g ^ ((n >> 1) & 1) ^ ((n >> 2) & 2)) * 16);
  pi.er = 8192 + (g >> 1) * 1032 + (8 * (g & 1) + q4) * 64 + ((p4 ^ (q4 >> 1) ^ (2 * (g & 1))) * 16);
  unsigned vq[4], vo[4], v5[4];
#pragma unroll
  for (int i = 0; i < 4; ++i) {
    const int ch = n ^ (2 * (g | ((i >> 1) << 2))), tp = ch >> 1;
    vq[i] = (g + 4 * i) * (HD * 2) + ch * 16 - 1024 * i;
    vo[i] = (g + 4 * i) * (DM * 2) + ch * 16 - 1024 * i;
    v5[i] = (g + 4 * i + 64 * (tp & 1)) * (DM * 2) + (32 + (tp >> 1)) * 32 + (ch & 1) * 16 - 1024 * i;
  }
  pi.vq0 = vq[0]; pi.vq1 = vq[1]; pi.vq2 = vq[2]; pi.vq3 = vq[3];
  pi.vo0 = vo[0]; pi.vo1 = vo[1]; pi.vo2 = vo[2]; pi.vo3 = vo[3];
  pi.v50 = v5[0]; pi.v51 = v5[1]; pi.v52 = v5[2]; pi.v53 = v5[3];
  const int rho = q4 | ((g & 1) << 2);
  pi.tro0 = (8 * g + q4) * 256 + (((4 * w + (p4 >> 1)) ^ (2 * rho)) * 16) + (p4 & 1) * 8;
  pi.tro1 = (8 * g + q4) * 256 + (((4 * w + 2 + (p4 >> 1)) ^ (2 * rho)) * 16) + (p4 & 1) * 8;
  pi.ws = base; pi.Wa = VU ? p.Wv : QO ? p.Wq : p.Wk; pi.Wb = VU ? p.Wo : p.Wq; pi.x = p.x;
  pi.w = w;
  pi.o_a = 16 * w * (HD * 2) + h * 256;
  pi.o_b = (h * DH + 16 * w) * (DM * 2);
  pi.o_p = PB + 2 * (VU ? B_V : QO ? B_Q : B_K) + h * 16384 + w * 2048 + (VU && QO ? 524288 : 0);
  pi.o_q = VU ? u_tile(2 * w, h >> 3, h & 7) ^ 8192u : PB + 2 * B_Q + h * 16384 + w * 2048 + (K_FIRST - 1) * 524288;
  pi.f_a = __builtin_amdgcn_readfirstlane(w || (QO && !VU) ? 0x7F000000u : CO + near_static(N_K + h) + (VU ? 8 + 8 * QO : 0));
  pi.f_b = __builtin_amdgcn_readfirstlane(VU ? CO + near_static(N_U + h) + w * 8 + 32 * QO : w || QO ? 0x7F000000u : dl + (F_Q + h) * 128);
  pi.f_go = dl + (F_GO + 4 + (h & 3)) * 128; pi.o_ctl = dl;
  pi.f_c = VU ? dl + (F_M + 2 * h + 1) * 128 : QO ? 77 : 78;
  pi.f_d = __builtin_amdgcn_readfirstlane(QO ? 0x7F000000u : dl + (F_U + h) * 128 + w * 8);
  pi.rng = QO ? 1 | (2 * U_HELP + 1) << 8 : 64 << 8;
  pi.u8 = QO && !VU ? 0x7F000000u : (VU ? u_tile(32 + w, h >> 3, h & 7) : near_head(B_K, h) + w * 2048) ^ 8192u;
  unsigned tag;
  if (!VU) sleep_n(QO ? p.q0 : p.pj0);
  if (VU) asm volatile(U_ASM_TEXT : P_ASM_VOPS : P_ASM_SOPS : P_ASM_CLOB);
  else asm volatile(K_ASM_TEXT : P_ASM_VOPS : P_ASM_SOPS : P_ASM_CLOB);
  return tag;
}

constexpr int NS4 = 67;
__device__ __forceinline__ unsigned y_at(const Bulk& bk, int quad, unsigned o) { return bk.at(B_YT + quad * 16384 + o) ^ (quad >= Y_NEAR && quad < Y_FAR ? 8192u : 0u); }
__device__ __forceinline__ void mix_role(const Params& p, unsigned tag, int w, int ct, int s0, int ds, int s1) {
  const int l = lane_id(), n = l & 15, g = l >> 4;
  const Ctx cx = {p.ws, tag};
  Bulk bk;
  bk.init(cx, 4 + (ct & 3));
  const int tr = (w >> 1) * 16 + n, dc = ct * 96 + (w & 1) * 48 + 4 * g;
  const rsrc_t rx = mk_rsrc(p.x, (size_t)SL * DM * 2), rout = mk_rsrc(p.out, (size_t)SL * DM * 2);
  u32x2 dv[3];
#pragma unroll
  for (int i = 0; i < 3; ++i) dv[i] = *(const u32x2*)(p.dsk + dc + 16 * i);
  struct In { u32x4 a[4]; u32x2 b[4]; u32x2 x[3], y[3]; float g; bool ok; };
  auto ldb = [&](In& in, int sg) {
    const int t = sg * 32 + tr;
    in.g = __uint_as_float(__builtin_amdgcn_raw_buffer_load_b32(bk.rs, t * 4, bk.at(B_G), 16));
#pragma unroll
    for (int i = 0; i < 3; ++i) {
      in.y[i] = __builtin_amdgcn_raw_buffer_load_b64(bk.rs, y_at(bk, (dc + 16 * i) >> 2, t * 8), 0, 16);
      in.x[i] = __builtin_amdgcn_raw_buffer_load_b64(rx, (t * DM + dc) * 2, 32 * i, 0);
    }
  };
  int seen = 0;
  auto lda = [&](In& in, int sg) {
    in.ok = seen > sg;
    const unsigned l8 = in.ok ? l * 8 : 0x3F000000u;
#pragma unroll
    for (int kq = 0; kq < 4; ++kq) {
      const unsigned o = bk.at(B_AP + ((sg * 6 + ct) * 4 + kq) * 8192 + w * 2048);
      in.a[kq] = ld128(bk.rs, 2 * l8, o);
      in.b[kq] = __builtin_amdgcn_raw_buffer_load_b64(bk.rs, l8, o + 1024, 16);
    }
  };
  auto step = [&](In& in, In& n2, int sg) {
    if (!in.ok) {
      wait_ge_n<2>(cx, F_A + ct * 4, 1, 4, sg + 1, seen);
      lda(in, sg);
#pragma unroll
      for (int i = 0; i < 4; ++i) asm volatile("" : "+v"(in.a[i]), "+v"(in.b[i]));
    }
    ldb(n2, min(sg + 2 * ds, NSEG - 1));
    lda(n2, sg + 2 * ds);
    SB();
#pragma unroll
    for (int i = 0; i < 3; ++i) {
      float o[4];
#pragma unroll
      for (int j = 0; j < 4; ++j) {
        auto part = [&](int kq) { const unsigned u = i < 2 ? in.a[kq][2 * (i & 1) + (j >> 1)] : in.b[kq][j >> 1]; return (j & 1) ? hi16(u) : lo16(u); };
        const float av = (part(0) + part(1)) + (part(2) + part(3));
        const float y = (j & 1) ? hi16(in.y[i][j >> 1]) : lo16(in.y[i][j >> 1]);
        const float xf = (j & 1) ? hi16(in.x[i][j >> 1]) : lo16(in.x[i][j >> 1]), df = (j & 1) ? hi16(dv[i][j >> 1]) : lo16(dv[i][j >> 1]);
        o[j] = rbf(in.g * rbf(av)) + rbf(rbf(1.0f - in.g) * rbf(y + rbf(xf * df)));
      }
      __builtin_amdgcn_raw_buffer_store_b64((u32x2){pk2(o[0], o[1]), pk2(o[2], o[3])}, rout, ((sg * 32 + tr) * DM + dc) * 2, 32 * i, OUT_AUX);
    }
    return sg + ds < s1;
  };
  wait_all<30>(cx, F_W, 1);
  In a, b, c;
  wait_ge_n<2>(cx, F_A + ct * 4, 1, 4, 1, seen);
  ldb(a, s0); lda(a, s0);
  ldb(b, s0 + ds); lda(b, s0 + ds);
  for (int sg = s0; step(a, c, sg) && step(b, a, sg + ds) && step(c, b, sg + 2 * ds); sg += 3 * ds) {}
}

__device__ __forceinline__ void att_role(const Params& p, unsigned tag, int w, int h) {
  CODE_PAD(PAD_ATT);
  char* const ws = p.ws;
  const int l = lane_id(), n = l & 15, g = l >> 4;
  const Ctx cx = {ws, tag};
  Bulk bk;
  bk.init(cx, 4 + (h & 3));
  const unsigned l16 = l * 16, vmo = (n + 16 * (((g & 1) << 1) | (g >> 1))) * 16;
  const float scale = 0.08838834764831845f;
  auto rnd2 = [&](float a, float b, float& ra, float& rb) {
    unsigned u = pk2(a, b);
    u = pk2(lo16(u) * scale, hi16(u) * scale);
    ra = lo16(u); rb = hi16(u);
  };
  auto soft = [&](const f32x4* st, int qi, u32x4& px, u32x2& pm) {
    const float neg = rbf(-1000000000.0f);
    float v12[12];
    rnd2((g & 1) ? st[0][2] : st[0][0], (g & 1) ? st[0][3] : st[0][1], v12[0], v12[1]);
    v12[2] = v12[0]; v12[3] = v12[1];
#pragma unroll
    for (int j = 0; j < 4; j += 2) { rnd2(st[1][j], st[1][j + 1], v12[4 + j], v12[5 + j]); rnd2(st[2][j], st[2][j + 1], v12[8 + j], v12[9 + j]); }
#pragma unroll
    for (int j = 0; j < 4; ++j) {
      v12[4 + j] = (4 * g + j <= qi) ? v12[4 + j] : neg;
      v12[8 + j] = (16 + 4 * g + j <= qi) ? v12[8 + j] : neg;
    }
    float mx = v12[0];
#pragma unroll
    for (int j = 1; j < 12; ++j) mx = fmaxf(mx, v12[j]);
    mx = red_max_g(mx);
#pragma unroll
    for (int j = 0; j < 12; ++j) v12[j] = __builtin_amdgcn_exp2f((v12[j] - mx) * 1.4426950408889634f);
    float sum = v12[0] + v12[1];
#pragma unroll
    for (int j = 4; j < 12; ++j) sum += v12[j];
    sum = red_sum_g(sum);
    const float inv = __builtin_amdgcn_rcpf(sum);
    px = (u32x4){pk2(v12[4] * inv, v12[5] * inv), pk2(v12[6] * inv, v12[7] * inv), pk2(v12[8] * inv, v12[9] * inv), pk2(v12[10] * inv, v12[11] * inv)};
    pm = (u32x2){pk2(v12[0] * inv, v12[1] * inv), 0u};
  };
  int seenp = 0, seenm = 0;
  auto wait_proj = [&](int target) {
    unsigned spins = 0;
    while (seenp < target) {
      const int v = (int)flag_get(cx, bk.proj_flag(cx, min(l, 3), h)), v2 = __builtin_amdgcn_readlane(v, 3);
      seenp = min(min(max(__builtin_amdgcn_readlane(v, 0), K_FIRST), __builtin_amdgcn_readlane(v, 1)), min(__builtin_amdgcn_readlane(v, 2), v2 < 2 * U_HELP ? v2 : NSEG));
      if (seenp < target) { __builtin_amdgcn_s_sleep(4); if (spin_fail(spins, cx)) break; }
    }
    __builtin_amdgcn_fence(__ATOMIC_ACQUIRE, "wavefront");
  };
  GAS u64* const fm = flag_ptr(ws, F_M + 2 * h + ((l >> 1) & 1)) + (l & 1 & ~(l >> 1));
  auto min8 = [&](u64 f) {
    int v = (unsigned)(f >> 32) == cx.tag ? (int)(unsigned)f : 0;
#pragma unroll
    for (int m = 1; m < 4; m <<= 1) v = min(v, __shfl_xor(v, m));
    return __builtin_amdgcn_readfirstlane(v);
  };
  auto wait_m = [&](int target) {
    unsigned spins = 0;
    while (seenm < target) {
      seenm = min8(__hip_atomic_load(fm, RLX_AGENT));
      if (seenm < target) { __builtin_amdgcn_s_sleep(2); if (spin_fail(spins, cx)) break; }
    }
    __builtin_amdgcn_fence(__ATOMIC_ACQUIRE, "wavefront");
  };
  bf16x8 qn[4][2], kx[4][2], vn[8];
  auto ahead = [&](int s) {
    const unsigned so = (s * NH + h) * 8192, oq = bk.at(B_Q + so), ok = bk.at(B_K + so), ov = bk.at(B_V + so);
#pragma unroll
    for (int k = 0; k < 8; ++k) {
      qn[k >> 1][k & 1] = ldfrag(bk.rs, l16, oq + k * 1024);
      kx[k >> 1][k & 1] = ldfrag(bk.rs, l16, ok + k * 1024);
    }
#pragma unroll
    for (int et = 0; et < 8; ++et) vn[et] = ldfrag(bk.rs, l16, ov + et * 1024);
  };
  bf16x8 km[4];
  u32x2 vm[4];
  auto ldm = [&](int s) {
    const unsigned om = near_head(B_K, h) ^ (~bk.w13 & 8192u) ^ ((s ? s - 1 : 8192) << 14);
#pragma unroll
    for (int ks = 0; ks < 4; ++ks) km[ks] = ldfrag(bk.rs, l16, om + ks * 1024);
#pragma unroll
    for (int k = 0; k < 4; ++k) vm[k] = __builtin_amdgcn_raw_buffer_load_b64(bk.rs, vmo, om + 4096 + k * 1024, 16);
  };
  if (w && l == 0) flag_put(cx, flag_ptr(ws, F_O + h) + w, w);
  wait_proj(w + 1);
  ahead(w);
  wait_m(w + 1);
  ldm(w);
  bool pend = false, fq = false;
  u64 fw = 0;
  for (int s = w; s < SDIR; s += 4) {
    bf16x8 vx[8];
    u32x2 vc[4];
    f32x4 st[2][3] = {};
#pragma unroll
    for (int ks = 0; ks < 4; ++ks) { st[0][1] = MF(kx[ks][0], qn[ks][0], st[0][1]); st[1][1] = MF(kx[ks][0], qn[ks][1], st[1][1]); st[1][2] = MF(kx[ks][1], qn[ks][1], st[1][2]); }
#pragma unroll
    for (int ks = 0; ks < 4; ++ks) { st[0][0] = MF(km[ks], qn[ks][0], st[0][0]); st[1][0] = MF(km[ks], qn[ks][1], st[1][0]); vc[ks] = vm[ks]; vx[ks] = vn[ks]; vx[ks + 4] = vn[ks + 4]; }
    SB();
    if (fq) {
      seenm = max(seenm, min8(fw));
      __builtin_amdgcn_fence(__ATOMIC_ACQUIRE, "wavefront");
    }
    const bool more = s + 4 < SDIR;
    bool have = more && seenm >= s + 5 && seenp >= s + 5;
    fq = have;
    if (have) {
      ldm(s + 4);
      fw = __hip_atomic_load(fm, RLX_AGENT);
      ahead(s + 4);
    }
    SB();
    const unsigned oo = bk.at(B_O + (s * NH + h) * 8192);
#pragma unroll
    for (int qt = 0; qt < 2; ++qt) {
      u32x4 px;
      u32x2 pm;
      soft(st[qt], qt * 16 + n, px, pm);
      const u32x4 pxs = {px[2], px[3], px[0], px[1]};
      f32x4 o[8];
#pragma unroll
      for (int et = 0; et < 8; ++et) {
        const f32x4 z = {};
        o[et] = MF(vx[et], __builtin_bit_cast(bf16x8, (et & 1) ? pxs : px), z);
        o[et] = MF(zext8((u32x2){vc[et >> 1][et & 1], 0u}), zext8(pm), o[et]);
      }
      if (qt == 0 && pend) {
        if (have) WAIT_VM(24); else WAIT_VM(0);
        if (l == 0) flag_put(cx, flag_ptr(ws, F_O + h) + w, s);
      }
#pragma unroll
      for (int k = 0; k < 4; ++k) st128(pack8(o[2 * k], o[2 * k + 1]), bk.rs, l16, oo + qt * 1024 + k * 2048);
    }
    SB();
    pend = true;
    if (!have) {
      pend = false;
      WAIT_VM(0);
      if (l == 0) flag_put(cx, flag_ptr(ws, F_O + h) + w, s + 4);
      if (more) { wait_proj(s + 5); ahead(s + 4); wait_m(s + 5); ldm(s + 4); }
    }
  }
}

__device__ __forceinline__ void xprep_role(const Params& p, unsigned tag, int w, int h0) {
  const int l = lane_id(), n = l & 15, g = l >> 4, qt = w & 1, par = w >> 1;
  const Ctx cx = {p.ws, tag};
  Bulk bk;
  bk.init(cx, 4 + (h0 & 3));
  const unsigned l16 = l * 16;
  const unsigned xo = X_AT + ((bk.w13 ^ 8192u) & 8192u);
  for (int i = 0; i < NDIR * 9; ++i)
    st128((u32x4){~0u, ~0u, ~0u, ~0u}, bk.rs, g * 16384 + (h0 + 8 * w) * 256 + n * 16, (bk.at(B_O + (i / 9) * 524288) ^ 8192u) + (i % 9) * 65536);
  for (unsigned spins = 0; __any(flag_get(cx, (l & 4) ? flag_ptr(p.ws, F_Q + h0 + 8 * (l & 3)) : flag_ptr(p.ws, F_Y) + 35 + h0 + 8 * (l & 3)) == 0u) && !spin_fail(spins, cx);) __builtin_amdgcn_s_sleep(8);
  __builtin_amdgcn_fence(__ATOMIC_ACQUIRE, "wavefront");
#pragma unroll 1
  for (int h = h0; h < NH; h += 8) {
  bf16x8 qq[NDIR / 2][4], kk[NDIR / 2][8];
#pragma unroll
  for (int i2 = 0; i2 < NDIR; i2 += 2) {
    const unsigned so = ((SDIR + i2 + par) * NH + h) * 8192, oq = bk.at(B_Q + so) + qt * 1024, ok = bk.at(B_K + so);
#pragma unroll
    for (int ks = 0; ks < 4; ++ks) {
      qq[i2 >> 1][ks] = ldfrag(bk.rs, l16, oq + ks * 2048);
      kk[i2 >> 1][2 * ks] = ldfrag(bk.rs, l16, ok + ks * 2048); kk[i2 >> 1][2 * ks + 1] = ldfrag(bk.rs, l16, ok + ks * 2048 + 1024);
    }
  }
  SB();
#pragma unroll
  for (int i2 = 0; i2 < NDIR; i2 += 2) {
    const int i = i2 + par;
    const unsigned oc = (bk.at(B_O + ((SDIR + i) * NH + h) * 8192) ^ 8192u) + qt * 1024;
    f32x4 a = {}, b = {};
#pragma unroll
    for (int ks = 0; ks < 4; ++ks) {
      const bf16x8 q = qq[i2 >> 1][ks];
      st128(__builtin_bit_cast(u32x4, q), bk.rs, l16, oc + ks * 2048);
      a = MF(kk[i2 >> 1][2 * ks], q, a); b = MF(kk[i2 >> 1][2 * ks + 1], q, b);
    }
    float mx = -__builtin_inff(), sum = 0.f;
#pragma unroll
    for (int j = 0; j < 4; ++j) {
      a[j] = 4 * g + j <= 16 * qt + n ? a[j] : -__builtin_inff(); b[j] = 16 + 4 * g + j <= 16 * qt + n ? b[j] : -__builtin_inff();
      mx = fmaxf(mx, fmaxf(a[j], b[j]));
    }
    const float c = 0.08838834764831845f * 1.4426950408889634f, nx = -red_max_g(mx) * c;
#pragma unroll
    for (int j = 0; j < 4; ++j) { a[j] = __builtin_amdgcn_exp2f(__builtin_fmaf(a[j], c, nx)); b[j] = __builtin_amdgcn_exp2f(__builtin_fmaf(b[j], c, nx)); sum += a[j] + b[j]; }
    sum = red_sum_g(sum);
    const unsigned eo = xo + 0x100000 + (i * 4 + 2 * qt + (n >> 3)) * 32768 + (h >> 4) * 16384 + (h & 15) * 512 + (g >> 1) * 128 + (n & 7) * 16 + (g & 1) * 8;
    __builtin_amdgcn_raw_buffer_store_b64((u32x2){pk2(a[0], a[1]), pk2(a[2], a[3])}, bk.rs, eo, 0, 16);
    __builtin_amdgcn_raw_buffer_store_b64((u32x2){pk2(b[0], b[1]), pk2(b[2], b[3])}, bk.rs, eo, 256, 16);
    if (g == 0) __builtin_amdgcn_raw_buffer_store_b64((u32x2){__float_as_uint(nx), __float_as_uint(sum)}, bk.rs, h * 256 + (16 * qt + n) * 8, xo + 0x80000 + i * 65536, 16);
    __builtin_amdgcn_raw_buffer_store_b64((u32x2){0xFFFFFFFFu, 0xFFFFFFFFu}, bk.rs, (2 * qt + (g >> 1)) * 16384 + ((g & 1) ? 4096 + c_line(h) : h * 128) + n * 8, xo + i * 65536, 16);
  }
  }
  WAIT_VM(0);
  BARRIER();
  if (l == 0) flag_put(cx, flag_ptr(p.ws, F_S + h0 + 8 * w), 1u);
}

struct OutIn {
  unsigned l16, rw, rr, fo, wv0, wv1, wv2, tro, l8;
  const void *ws, *Wo;
  unsigned w, tag, o_w, o_o, o_a, f_a, o_ctl, ns;
};
__device__ __forceinline__ void op_role(const Params& p, unsigned tag, int w, int ct, int kq, int grp) {
  const Ctx cx = {p.ws, tag};
  Bulk bk;
  bk.init(cx, grp);
  const int l = lane_id(), g = l >> 4, q4 = (l >> 2) & 3, p4 = l & 3, h0 = kq * 8 + w * 2;
  char* const base = comm_base(p.ws) - CO;
  const unsigned dl = (unsigned)(p.ws - base);
  OutIn oi;
  oi.l16 = l * 16; oi.l8 = l * 8;
  oi.rw = 65536 + w * 12288 + l * 16;
  oi.rr = 65536 + w * 3072 + l * 16;
  oi.fo = dl + (F_O + h0 + ((l >> 2) & 1)) * 128 + (l & 3) * 8;
  auto piece = [&](int i) { const int idx = i * 64 + l, row = idx / 12; return row * (DM * 2) + (idx - row * 12) * 16; };
  oi.wv0 = piece(0); oi.wv1 = piece(1); oi.wv2 = piece(2);
  oi.tro = w * 24576 + (4 * g + q4) * 192 + p4 * 8;
  oi.ws = base; oi.Wo = p.Wo; oi.w = w; oi.tag = tag; oi.o_ctl = dl; oi.ns = SDIR;
  oi.o_w = (kq * 1024 + w * 256) * (DM * 2) + ct * 192;
  oi.o_o = __builtin_amdgcn_readfirstlane(bk.w13 + 2 * B_O + h0 * 16384);
  oi.o_a = __builtin_amdgcn_readfirstlane(bk.w13 + 2 * B_AP + (ct * 4 + kq) * 16384 + w * 2048);
  oi.f_a = __builtin_amdgcn_readfirstlane(w ? 0x7F000000u : dl + (F_A + ct * 4 + kq) * 128);
  asm volatile(O_ASM_TEXT : O_ASM_VOPS : O_ASM_SOPS : O_ASM_CLOB);
}

typedef __attribute__((ext_vector_type(16))) float f32x16;
constexpr int S4_PRM = 0;
constexpr int S4_KD = S4_PRM + 2560;
constexpr int S4_KH = S4_KD + 2624;
constexpr int S4_U = S4_KH + 5200;
constexpr int S4_USTR = 4352 + 64;
constexpr int S4_WSZ = S4_U + 4 * S4_USTR;
constexpr int S4_RAW = 4 * S4_WSZ;

#define S4M(i) do {} while (0)
struct S4In { u32x2 u[8]; unsigned la, ai, cr, ci, dt; };
__device__ __forceinline__ float s4_params(LAS char* my, const S4In& in) {
  const int l = lane_id();
  const u16 pl_[2] = {(u16)in.la, (u16)(in.la >> 16)}, pa_[2] = {(u16)in.ai, (u16)(in.ai >> 16)}, pr_[2] = {(u16)in.cr, (u16)(in.cr >> 16)}, pi_[2] = {(u16)in.ci, (u16)(in.ci >> 16)};
  const float dt = rbf(expf(bf2f((u16)in.dt)));
  f32x4 pr[2];
#pragma unroll
  for (int k = 0; k < 2; ++k) {
    const float a = -rbf(expf(bf2f(pl_[k]))) * dt;
    const float b = bf2f(pa_[k]) * dt;
    const float c_hi = 0.15915494f, c_lo = (float)(0.15915494309189535 - (double)0.15915494f);
    const float bh = b * c_hi;
    const float bl = __builtin_fmaf(b, c_hi, -bh) + b * c_lo;
    const float cr = bf2f(pr_[k]), ci = bf2f(pi_[k]);
    pr[k] = (f32x4){a * 1.4426950408889634f, bh, bl, log2f(hypotf(cr, ci))};
    *(LAS float*)(my + S4_PRM + 2048 + (l + 64 * k) * 4) = -atan2f(ci, cr) * c_hi;
  }
  float top = fmaxf(fmaxf(pr[0][3], pr[1][3]), -100.f);
#pragma unroll
  for (int m = 1; m < 64; m <<= 1) top = fmaxf(top, __shfl_xor(top, m));
  top = ceilf(top);
#pragma unroll
  for (int k = 0; k < 2; ++k) { pr[k][3] += 14.f - top; *(LAS f32x4*)(my + S4_PRM + (l + 64 * k) * 16) = pr[k]; }
  return exp2f(top - 27.f) * dt;
}
typedef __attribute__((ext_vector_type(2))) _Float16 f16x2;
typedef __attribute__((ext_vector_type(8))) _Float16 f16x8;
__device__ __forceinline__ void s4_modes(LAS char* my, int q0, int q1, f32x4 (&D)[3]) {
  const int l = lane_id(), i = l & 15, kk = l >> 4;
  const f32x2 pe[2] = {{(float)(8 * i), (float)(128 * (i >> 3) + (i & 7))},
                       {(float)(i < 8 ? 256 + 128 * (i >> 2) + 2 * (i & 3) : 2048), (float)(i < 8 ? 512 + 128 * (i >> 1) + 4 * (i & 1) : 128 * i)}};
  const f32x2 pp[2] = {{-pe[0][0], pe[0][1]}, pe[1]};
#pragma unroll 1
  for (int q = q0; q < q1; ++q) {
    u32x4 op[4];
    const f32x4 pc = *(LAS f32x4*)(my + S4_PRM + 2048 + (4 * q + kk) * 16);
#pragma unroll
    for (int j = 0; j < 4; ++j) {
      const f32x4 pr = *(LAS f32x4*)(my + S4_PRM + (16 * q + 4 * kk + j) * 16);
#pragma unroll
      for (int m = 0; m < 2; ++m) {
        const f32x2 ea = __builtin_elementwise_fma((f32x2){pr[0], pr[0]}, pe[m], (f32x2){m ? 14.f : pr[3], 14.f});
        const f32x2 t = pp[m] * pr[1];
        f32x2 fr = __builtin_elementwise_fma((f32x2){pr[1], pr[1]}, pp[m], -(f32x2){__builtin_rintf(t[0]), __builtin_rintf(t[1])});
        fr = __builtin_elementwise_fma((f32x2){pr[2], pr[2]}, pp[m], fr);
        if (!m) fr[0] += pc[j];
#pragma unroll
        for (int h = 0; h < 2; ++h) {
          const f32x2 v = (f32x2){__builtin_amdgcn_cosf(fr[h]), __builtin_amdgcn_sinf(fr[h])} * __builtin_amdgcn_exp2f(ea[h]);
          op[2 * m + h][j] = __builtin_bit_cast(unsigned, __builtin_convertvector(v, f16x2));
        }
      }
    }
#pragma unroll
    for (int m = 0; m < 3; ++m) D[m] = __builtin_amdgcn_mfma_f32_16x16x32_f16(__builtin_bit_cast(f16x8, op[m + 1]), __builtin_bit_cast(f16x8, op[0]), D[m], 0, 0, 0);
  }
}
__device__ __forceinline__ void s4d_task(const Ctx& cx, const Bulk& bk, unsigned yflip, int gentle, int w, int task, char* smem, const S4In& in, char* ws, int q0, f32x4 (&D)[3], float ksc) {
  const int l = lane_id(), tid = w * 64 + l;
  LAS char* const lds = (LAS char*)smem;
  LAS char* const my = lds + w * S4_WSZ;
  S4M(2);
  s4_modes(my, q0, 8, D);
  {
    const int i = l & 15, kk = l >> 4;
#pragma unroll
    for (int r = 0; r < 4; ++r) {
      const int row = 4 * kk + r;
      *(LAS float*)(my + S4_KD + (128 * (row >> 3) + (row & 7) + 8 * i) * 4) = ksc * D[0][r];
      *(LAS float*)(my + S4_KD + (row < 8 ? 256 + 64 * kk + r + 4 * i : 640 + i) * 4) = ksc * D[1][r];
      *(LAS float*)(my + S4_KD + (row < 8 ? 384 + 32 * (row >> 1) + (row & 1) + 2 * i : 384 + 16 * row + i) * 4) = ksc * D[2][r];
    }
  }
  S4M(3);
#pragma unroll
  for (int k = 0; k < 2; ++k)
#pragma unroll
    for (int j = 0; j < 8; j += 4) *(LAS u32x4*)(lds + S4_RAW + k * 8208 + tid * 32 + j * 4) = (u32x4){in.u[j][k], in.u[j + 1][k], in.u[j + 2][k], in.u[j + 3][k]};
  BARRIER();
  u32x2 r[11];
#pragma unroll
  for (int k = 0; k < 2; ++k)
#pragma unroll
    for (int j = 0; j < 12; j += 4) {
      const u32x4 v = *(LAS u32x4*)(lds + S4_RAW + k * 8208 + tid * 32 + j * 4);
#pragma unroll
      for (int i = 0; i < 4; ++i) if (j + i < 11) r[j + i][k] = v[i];
    }
#pragma unroll
  for (int ch = 0; ch < 4; ++ch) {
    unsigned pr[10];
#pragma unroll
    for (int j = 0; j < 10; ++j) pr[j] = __builtin_amdgcn_perm(r[j + 1][ch >> 1], r[j][ch >> 1], (ch & 1) ? 0x07060302u : 0x05040100u);
    if (tid == 255) { pr[7] &= 0xFFFFu; pr[8] = 0u; pr[9] = 0u; }
    LAS char* ub = lds + ch * S4_WSZ + S4_U + 64 + tid * 16;
#pragma unroll
    for (int sft = 0; sft < 4; ++sft) *(LAS u32x4*)(ub + sft * S4_USTR) = (u32x4){pr[sft], pr[sft + 2], pr[sft + 4], pr[sft + 6]};
    if (tid < 4) {
#pragma unroll
      for (int sft = 0; sft < 4; ++sft) {
        const u32x4 v = {0u, 0u, sft == 3 ? pr[0] << 16 : 0u, sft == 0 ? 0u : sft == 1 ? pr[0] << 16 : pr[sft - 2]};
        *(LAS u32x4*)(lds + ch * S4_WSZ + S4_U + sft * S4_USTR + (3 - tid) * 16) = tid == 0 ? v : (u32x4){0u, 0u, 0u, 0u};
      }
    }
  }
  {
    if (l < 5) {
      *(LAS u32x4*)(my + S4_KH + l * 16) = (u32x4){0u, 0u, 0u, 0u};
    }
#pragma unroll
    for (int k = 0; k < 4; ++k) {
      const int grp = l + 64 * k;
      float v[8];
#pragma unroll
      for (int j = 0; j < 8; j += 2) {
        const unsigned c = pk2((float)(8 * grp + j), (float)(8 * grp + j + 1)) - 0x42804280u;
        const bool low = k == 0 && grp < 16;
        v[j] = *(LAS float*)(my + S4_KD + (low ? 8 * grp + j : c & 0xFFFFu) * 4);
        v[j + 1] = *(LAS float*)(my + S4_KD + (low ? 8 * grp + j + 1 : c >> 16) * 4);
      }
      *(LAS u32x4*)(my + S4_KH + 80 + (grp >> 2) * 80 + (grp & 3) * 16) = (u32x4){pk2(v[7], v[6]), pk2(v[5], v[4]), pk2(v[3], v[2]), pk2(v[1], v[0])};
    }
  }
  __syncthreads();
  S4M(4);
  {
    const int r = l & 31, hh = l >> 5, sft = (r + 1) & 3;
    unsigned ua = w * S4_WSZ + S4_U + sft * S4_USTR + (r + 25 - sft - 8 * hh - 16) * 2;
    const unsigned kz = w * S4_WSZ + S4_KH + hh * 16;
    unsigned kb1 = kz + (33 + r) * 80;
    unsigned kb0 = kz + (1 + r) * 80;
    f32x16 acc0 = {}, acc1 = {};
    unsigned ub = ua + 72;
    asm volatile("" : "+v"(ub));
    struct Ops { bf16x8 a0, a1, b0, b1; };
    auto ld = [&](int ks) {
      Ops o;
      const unsigned uo = ua + (1 - ks) * 32;
      o.a0 = cat2(*(LAS bf16x4*)(lds + uo), *(LAS bf16x4*)(lds + uo + 8));
      o.a1 = cat2(*(LAS bf16x4*)(lds + uo + 2048), *(LAS bf16x4*)(lds + uo + 2056));
      o.b0 = *(LAS bf16x8*)(lds + kb0 + ks * 32);
      o.b1 = *(LAS bf16x8*)(lds + kb1 + ks * 32);
      return o;
    };
    auto step = [&](const Ops& o, Ops& nx, int ks) {
      const unsigned uo = ua + (1 - ks) * 32, up = ub + (1 - ks) * 32;
      acc1 = __builtin_amdgcn_mfma_f32_32x32x16_bf16(o.a0, o.b1, acc1, 0, 0, 0); SB();
      nx.a0 = cat2(*(LAS bf16x4*)(lds + uo), *(LAS bf16x4*)(lds + up)); nx.b1 = *(LAS bf16x8*)(lds + kb1 + ks * 32); SB();
      acc0 = __builtin_amdgcn_mfma_f32_32x32x16_bf16(o.a0, o.b0, acc0, 0, 0, 0); SB();
      nx.b0 = *(LAS bf16x8*)(lds + kb0 + ks * 32);
      nx.a1 = cat2(*(LAS bf16x4*)(lds + uo + 2048), *(LAS bf16x4*)(lds + up + 2048));
      if (ks) {
        ub += 64;
        ua += 64;
        kb1 -= 80;
        kb0 = max(kb0 - 80, kz);
      }
      SB();
      acc1 = __builtin_amdgcn_mfma_f32_32x32x16_bf16(o.a1, o.b0, acc1, 0, 0, 0); SB();
    };
    Ops e0 = ld(0), e1 = ld(1), o0, o1;
    ua += 64; kb1 -= 80; kb0 = max(kb0 - 80, kz);
#pragma unroll 1
    for (int D = 0; D < 32; D += 2) {
      step(e0, o0, 0);
      step(e1, o1, 1);
      step(o0, e0, 0);
      step(o1, e1, 1);
      sleep_n(gentle);
    }
    S4M(5);
#pragma unroll
    for (int j = 0; j < 4; ++j) {
      const unsigned off = (32 * r + 8 * j + 4 * hh) * 2;
      *(LAS u32x2*)(my + off) = (u32x2){pk2(acc0[4 * j], acc0[4 * j + 1]), pk2(acc0[4 * j + 2], acc0[4 * j + 3])};
      *(LAS u32x2*)(my + off + 2048) = (u32x2){pk2(acc1[4 * j], acc1[4 * j + 1]), pk2(acc1[4 * j + 2], acc1[4 * j + 3])};
    }
    if (yflip && w == 0)
      for (unsigned spins = 0; __builtin_amdgcn_readfirstlane(flag_get(cx, flag_ptr(ws, F_Z) + (task - Y_NEAR))) == 0u && !spin_fail(spins, cx);) __builtin_amdgcn_s_sleep(30);
    __syncthreads();
#pragma unroll
    for (int j = 0; j < 2; ++j) {
      u32x2 c[4];
#pragma unroll
      for (int k = 0; k < 4; ++k) c[k] = *(LAS u32x2*)(lds + k * S4_WSZ + (j * 256 + tid) * 8);
      const unsigned o = bk.at(B_YT + task * 16384 + j * 8192) ^ yflip;
#pragma unroll
      for (int k = 0; k < 2; ++k)
        st128((u32x4){__builtin_amdgcn_perm(c[1][k], c[0][k], 0x05040100u), __builtin_amdgcn_perm(c[3][k], c[2][k], 0x05040100u),
                      __builtin_amdgcn_perm(c[1][k], c[0][k], 0x07060302u), __builtin_amdgcn_perm(c[3][k], c[2][k], 0x07060302u)}, bk.rs, tid * 32 + k * 16, o);
    }
  }
  __syncthreads();
  S4M(6);
}

__device__ __forceinline__ void warm_l2(const u16* W, unsigned off, bool rows256, int w) {
  const rsrc_t rw = mk_rsrc(W, (size_t)DM * HD * 2);
  const int l = lane_id();
  const unsigned vo = rows256 ? (l >> 4) * (HD * 2) + (l & 15) * 16 : l * 16, st = rows256 ? 4 * HD * 2 : 1024;
  u32x4 v[36];
#pragma unroll
  for (int i = 0; i < 36; ++i) v[i] = __builtin_amdgcn_raw_buffer_load_b128(rw, vo, off + (i * 4 + w) * st, 0);
#pragma unroll
  for (int i = 0; i < 36; ++i) asm volatile("" :: "v"(v[i]));
}

__device__ __forceinline__ void x_ahead(const Params& p, int w, int idx) {
  const rsrc_t rx = mk_rsrc(p.x, (size_t)SL * DM * 2);
  for (int i = 0; i < p.xpre; ++i) dma16(rx, lane_id() * 16, ((((idx - 35) >> 2) * 4 + w) + i * 32) * 1024, LDS_BYTES - 1024);
}
__device__ __forceinline__ unsigned s4_role(const Params& p, int w, int idx, char* smem) {
  CODE_PAD(PAD_S4);
  char* const ws = p.ws;
  const int l = lane_id(), tid = w * 64 + l;
  const bool near = idx >= 8 && idx < 35;
  const int r_ = idx - 8, t0 = near ? Y_NEAR + 5 * r_ : idx + (idx > 0);
  const int t1 = near ? t0 + 5 - p.s4skip : idx + 2;
  const rsrc_t rx = mk_rsrc(p.x, (size_t)SL * DM * 2);
  S4In in[5];
#pragma unroll
  for (int k = 0; k < 5; ++k) {
    const int d = min(t0 + k, t1 - 1) * 4 + w, o = d * (NST / 2) + l;
    in[k].la = ((const unsigned*)p.lar)[o]; in[k].ai = ((const unsigned*)p.aim)[o]; in[k].cr = ((const unsigned*)p.cre)[o]; in[k].ci = ((const unsigned*)p.cim)[o];
    in[k].dt = __builtin_amdgcn_raw_buffer_load_b16(mk_rsrc(p.ldt, DM * 2), d * 2, 0, 0);
  }
  const unsigned gen = gen_load(ws);
  LAS char* const my = (LAS char*)smem + w * S4_WSZ;
  float ksc = s4_params(my, in[0]);
  f32x4 D[3] = {};
  const int qs = near ? p.s4q : p.s4f;
  s4_modes(my, 0, qs, D);
#pragma unroll
  for (int m = 0; m < 5; ++m)
#pragma unroll
    for (int j = 0; j < 8; ++j) in[m].u[j] = __builtin_amdgcn_raw_buffer_load_b64(rx, t0 + m < t1 ? (tid * 8 + j) * (DM * 2) : 0x7F000000u, (t0 + m) * 8, 0);
  if (near) sleep_n((r_ % 7) * p.stag);
  s4_modes(my, qs, 8, D);
  const Ctx cx = {ws, __builtin_amdgcn_readfirstlane(gen) + 1u};
  Bulk bk;
  bk.init(cx, idx & 7);
#pragma unroll 1
  for (int k = t0; k < t1; ++k) {
    s4d_task(cx, bk, near ? 8192u : 0u, near ? p.gentle : 0, w, k, smem, in[0], ws, k == t0 ? 8 : 0, D, ksc);
    in[0] = in[1]; in[1] = in[2]; in[2] = in[3]; in[3] = in[4];
    ksc = s4_params(my, in[0]);
    D[0] = D[1] = D[2] = (f32x4){0.f, 0.f, 0.f, 0.f};
  }
  WAIT_VM(0);
  BARRIER();
  if (tid == 0) flag_put(cx, flag_ptr(ws, F_Y) + idx, 1u);
  return cx.tag;
}
__device__ __forceinline__ void gate_role(const Params& p, int w, int idx) {
  char* const ws = p.ws;
  const int l = lane_id(), tid = w * 64 + l, n = l & 15, g = l >> 4;
  const unsigned gen = gen_load(ws);
  x_ahead(p, w, idx);
  sleep_n(p.gate0);
  const rsrc_t rx = mk_rsrc(p.x, (size_t)SL * DM * 2), rg = mk_rsrc(p.gw, DM * 2);
  const int t = ((idx - 35) * 4 + w) * 16 + g;
  u32x4 gw[4], xv[4][4];
  u32x2 gt, xt[4];
#pragma unroll
  for (int k = 0; k < 4; ++k) gw[k] = __builtin_amdgcn_raw_buffer_load_b128(rg, n * 16, k * 256, 0);
  gt = __builtin_amdgcn_raw_buffer_load_b64(rg, n * 8, 1024, 0);
#pragma unroll
  for (int r = 0; r < 4; ++r) {
#pragma unroll
    for (int k = 0; k < 4; ++k) xv[r][k] = __builtin_amdgcn_raw_buffer_load_b128(rx, t * (DM * 2) + n * 16, r * 4 * (DM * 2) + k * 256, 0);
    xt[r] = __builtin_amdgcn_raw_buffer_load_b64(rx, t * (DM * 2) + n * 8, r * 4 * (DM * 2) + 1024, 0);
  }
  SB();
  const Ctx cx = {ws, __builtin_amdgcn_readfirstlane(gen) + 1u};
  Bulk bk;
  bk.init(cx, idx & 7);
  for (int z = idx - 35; z < Y_FAR - Y_NEAR; z += 32)
#pragma unroll
    for (int m = 0; m < 4; ++m) st128((u32x4){0u, 0u, 0u, 0u}, bk.rs, tid * 16, (bk.at(B_YT + (Y_NEAR + z) * 16384 + m * 4096) ^ 8192u));
#pragma unroll
  for (int r = 0; r < 4; ++r) {
    float acc = 0.f;
#pragma unroll
    for (int k = 0; k < 4; ++k)
#pragma unroll
      for (int j = 0; j < 4; ++j) acc += rbf(lo16(xv[r][k][j]) * lo16(gw[k][j])) + rbf(hi16(xv[r][k][j]) * hi16(gw[k][j]));
#pragma unroll
    for (int j = 0; j < 2; ++j) acc += rbf(lo16(xt[r][j]) * lo16(gt[j])) + rbf(hi16(xt[r][j]) * hi16(gt[j]));
#pragma unroll
    for (int m = 8; m >= 1; m >>= 1) acc += __shfl_xor(acc, m);
    const float gg = rbf(1.0f / (1.0f + expf(-rbf(acc))));
    if (n == 0) __builtin_amdgcn_raw_buffer_store_b32(__float_as_uint(gg), bk.rs, (t + 4 * r) * 4, bk.at(B_G), 16);
  }
  WAIT_VM(0);
  BARRIER();
  if (tid < 5 && idx - 35 + 32 * tid < Y_FAR - Y_NEAR) flag_put(cx, flag_ptr(ws, F_Z) + (idx - 35 + 32 * tid), 1u);
}

struct FinIn {
  unsigned sidx, cidx, l16, ae0, ae1, ae2, ae3, am, lw, lr, oo, d0;
  float g[NDIR], b[NDIR], c[NDIR];
  const void *ws, *out;
  unsigned tag, o_u, o_e, o_x, o_va, o_tw, xa, xb, o_ctl, pa, w, f_w;
};
__device__ __forceinline__ void watch_role(const Params& p, unsigned tag) {
  const int l = lane_id();
  const Ctx cx = {p.ws, tag};
  wait_all<30>(cx, F_Y, 64);
  wait_all<30>(cx, F_Y + 4, NS4 - 64);
  unsigned spins = 0;
  while (__any(flag_get(cx, flag_ptr(p.ws, F_S + (l & 31))) == 0u) && !spin_fail(spins, cx)) __builtin_amdgcn_s_sleep(30);
  publish(cx, 0, F_W, 1);
  for (int i = 0; i < NDIR; ++i) {
    GAS u64* const f = flag_ptr(p.ws, F_U + (l & 31)) + (l >> 5) * 2;
    while (__any((int)min(flag_get(cx, f), flag_get(cx, f + 1)) <= SDIR + i) && !spin_fail(spins, cx)) __builtin_amdgcn_s_sleep(8);
    publish(cx, 0, F_W + 1 + i, 1);
  }
}
struct FinX { unsigned x[NDIR], d; };
__device__ __forceinline__ int fin_col(int T) { const int l = lane_id(); return T * 16 + 8 * ((l >> 3) & 1) + 2 * (l & 3); }
__device__ __forceinline__ int fin_row(int w) { const int l = lane_id(); return SDIR * 32 + 8 * w + 2 * (l >> 4) + ((l >> 2) & 1); }
__device__ __forceinline__ FinX fin_x(const Params& p, int w, int T) {
  FinX f;
#pragma unroll
  for (int i = 0; i < NDIR; ++i) f.x[i] = *(const unsigned*)(p.x + (size_t)(fin_row(w) + 32 * i) * DM + fin_col(T));
  f.d = *(const unsigned*)(p.dsk + fin_col(T));
  return f;
}
__device__ __forceinline__ void fin_role(const Params& p, unsigned tag, int w, int T, const FinX& fx) {
  char* const ws = p.ws;
  const int l = lane_id(), n = l & 15, g = l >> 4;
  const Ctx cx = {ws, tag};
  Bulk bk;
  bk.init(cx, T & 7);
  bk.pl.load(cx, T & 7);
  const Place& pl = bk.pl;
  FinIn fi;
  if (w == 0) wait_all<30>(cx, F_W, 1);
  BARRIER();
  const int d = fin_col(T), r0 = fin_row(w);
  float (&gg)[NDIR] = fi.g, (&b0)[NDIR] = fi.b, (&b1)[NDIR] = fi.c;
#pragma unroll
  for (int i = 0; i < NDIR; ++i) {
    const int t = r0 + 32 * i;
    gg[i] = __uint_as_float(__builtin_amdgcn_raw_buffer_load_b32(bk.rs, t * 4, bk.at(B_G), 16));
    const unsigned yv = __builtin_amdgcn_raw_buffer_load_b32(bk.rs, y_at(bk, d >> 2, t * 8 + (d & 2) * 2), 0, 16);
    const unsigned xv = fx.x[i], dv = fx.d;
    b0[i] = rbf(rbf(1.0f - gg[i]) * rbf(lo16(yv) + rbf(lo16(xv) * lo16(dv)))); b1[i] = rbf(rbf(1.0f - gg[i]) * rbf(hi16(yv) + rbf(hi16(xv) * hi16(dv))));
  }
#pragma unroll
  for (int i = 0; i < NDIR; ++i) asm volatile("" : "+v"(b0[i]), "+v"(b1[i]));
  char* const base = comm_base(ws) - CO;
  fi.sidx = (n & 8) ? 0xEEEEu : 0x4444u;
  fi.l16 = l * 16; fi.d0 = 0;
  fi.cidx = (n & 3) * 0x5555u;
  auto ae = [&](int j) { return g <= j ? j * 32768 + (n >> 3) * 512 + g * 128 + (n & 7) * 16 : 0x7F000000u; };
  fi.ae0 = ae(0); fi.ae1 = ae(1); fi.ae2 = ae(2); fi.ae3 = ae(3);
  fi.am = w * 1024 + l * 16;
  fi.lw = (w >> 1) * 512 + (2 * (g & 1) + (g >> 1)) * 128 + (n >> 1) * 16 + (w & 1) * 8 + (n & 1) * 4;
  fi.lr = w * 2048 + ((l >> 2) & 1) * 1024 + g * 128 + (((l >> 3) & 1) * 4 + (l & 3)) * 16;
  fi.oo = (r0 * DM + d) * 2;
  fi.f_w = (unsigned)(ws - base) + (F_W + 1) * 128;
  fi.ws = base; fi.out = p.out; fi.tag = __builtin_amdgcn_readfirstlane(tag); fi.o_ctl = (unsigned)(ws - base); fi.xa = pl.slot(SDIR - 1) ^ pl.slot(SDIR); fi.xb = pl.slot(SDIR) ^ pl.slot(SDIR + 1);
  fi.o_u = __builtin_amdgcn_readfirstlane(u_tile(T, w, 0) ^ (~bk.w13 & 8192u));
  fi.o_x = X_AT + ((bk.w13 ^ 8192u) & 8192u);
  fi.o_e = __builtin_amdgcn_readfirstlane(fi.o_x + 0x100000 + (w >> 1) * 16384 + (w & 1) * 4096);
  fi.o_va = __builtin_amdgcn_readfirstlane((bk.w13 ^ 8192u) + 2 * B_O + T * 16384 + w * 2048);
  fi.o_tw = __builtin_amdgcn_readfirstlane(CO + TW_OFF + (16384u ^ pl.dyn(T & 3) ^ pl.slot(SDIR - 1)));
  fi.pa = p.xt0 | (p.xt1 << 16); fi.w = w;
  asm volatile(F_ASM_TEXT : F_ASM_VOPS : F_ASM_SOPS : F_ASM_CLOB);
  if (T == 0 && w == 0 && lane_id() == 0) __hip_atomic_store((GAS unsigned*)flag_ptr(ws, F_GEN), tag, RLX_AGENT);
}

__device__ __forceinline__ void code_ahead(int i, int n) {
  const char* pc;
  unsigned back, len;
  asm volatile("s_getpc_b64 %0\n.Lcp_%=:\n\ts_mov_b32 %1, .Lcp_%=-_Z6k_mega6Params\n\ts_mov_b32 %2, .Lcode_end-_Z6k_mega6Params" : "=s"(pc), "=s"(back), "=s"(len));
  const rsrc_t rc = mk_rsrc(pc - back, len);
  for (unsigned o = i * 1024; o < len; o += n * 1024) dma16(rc, lane_id() * 16, o, LDS_BYTES - 1024);
  WAIT_VM(0);
}

__global__ void __launch_bounds__(256, 1) k_mega(Params p) {
  extern __shared__ __attribute__((aligned(16))) char smem[];
  const int x = blockIdx.x & 7, j = blockIdx.x >> 3;
  const int w = __builtin_amdgcn_readfirstlane(threadIdx.x >> 6);
  if (j >= p.jhi) return;
  if (w == 1 && blockIdx.x != 128 && ((p.tlb >> (x < 4 ? (j < 16 ? 0 : j < 25 ? 1 : 2) : (j & 24) == 16 ? 4 : 3)) & 1)) touch_pages(p.ws, j);
  if (x < 4 && (j < 16 || j > 24)) sleep_n(x ? p.v0 : p.s0);
  if (x < 4 && j < 8) { chain_role<true>(p, w, x + 4 * j, x); return; }
  else if (x < 4 && j < 16) { chain_role<false>(p, w, x + 4 * (j - 8), x); return; }
  else if (x < 4 && j < 25) red_role(p, w, x + 4 * (j - 16), smem);
  else if (x == 0 && j == 25) {
    if (w == 0) keep_role(p);
    else if (p.cpf & 4) code_ahead(w - 1, 3);
  }
  else {
    unsigned tag;
    int T = -1, mix = -1, ms0 = 0, mds = 1, ms1 = p.mixs;
    FinX fx;
    if (x >= 4 && j < 16) {
      const int h = (x & 3) + 4 * (j & 7);
      tag = j >= 8 ? proj_role<false>(p, w, h) : proj_role<true>(p, w, h);
      if (j >= 8 && h < 30) mix = h % 6, ms0 = p.mixs + h / 6, mds = 5, ms1 = SDIR;
      if (j >= 8 && h >= 30) { T = (h - 30) * 35; fx = fin_x(p, w, T); }
    } else {
      if (x >= 4 && j < 24) {
        const int h = (x & 3) + 4 * (j & 7);
        if (p.cpf & 1) code_ahead((j & 7) * 4 + w, 32);
        if (p.wl2 & 1) warm_l2(p.Wo, h * DH * DM * 2, false, w);
        gate_role(p, w, 35 + h);
        tag = proj_role<false, true>(p, w, h);
        if (w == 0 && lane_id() == 0) flag_put({p.ws, tag}, flag_ptr(p.ws, F_Y) + 35 + h, 1u);
        att_role(p, tag, w, h);
      } else {
        if (x >= 4) {
          tag = proj_role<true, true>(p, w, (x & 3) + 4 * (j & 7));
          if (j >= 30) xprep_role(p, tag, w, (x & 3) + 4 * (j & 1));
        }
        if (x >= 4 && j < 30) op_role(p, tag, w, j - 24, x & 3, x);
        else {
          if (x < 4 && j >= 26 && (p.cpf & 2)) code_ahead((j - 26) * 4 + w, 24);
          T = x < 4 ? (j - 25) * 4 + x : (j - 23) * 4 + (x & 3);
          fx = fin_x(p, w, T);
#pragma unroll
          for (int i = 0; i < NDIR; ++i) asm volatile("" : "+v"(fx.x[i]), "+v"(fx.d));
          tag = s4_role(p, w, x < 4 ? 7 + x * 7 + (j - 25) : (x & 3) * 2 + j - 30, smem);
          if (T == 35) { if (w == 0) watch_role(p, tag); T = -1; }
          if (T > 28 && p.mixs) mix = T - 29;
        }
      }
    }
    if (mix >= 0) mix_role(p, tag, w, mix, ms0, mds, ms1);
    if (T >= 0) fin_role(p, tag, w, T, fx);
  }
}

extern "C" void kernel_launch(void* const* d_in, const int* in_sizes, int n_in, void* d_out, int out_size, void* d_ws, size_t ws_size,
                              hipStream_t stream) {
  Params p;
  p.x = (const u16*)d_in[0]; p.mem0 = (const u16*)d_in[1];
  p.Wq = (const u16*)d_in[2]; p.Wk = (const u16*)d_in[3]; p.Wv = (const u16*)d_in[4]; p.Wo = (const u16*)d_in[5];
  p.lar = (const u16*)d_in[8]; p.aim = (const u16*)d_in[9]; p.cre = (const u16*)d_in[10]; p.cim = (const u16*)d_in[11];
  p.ldt = (const u16*)d_in[12]; p.dsk = (const u16*)d_in[13]; p.gw = (const u16*)d_in[14];
  p.out = (u16*)d_out; p.ws = (char*)d_ws;
  (void)hipFuncSetAttribute((const void*)k_mega, hipFuncAttributeMaxDynamicSharedMemorySize, LDS_BYTES);
  {
    k_mega<<<256, 256, LDS_BYTES, stream>>>(p);
  }
}
```

```cpp
#include <hip/hip_runtime.h>
#include <hip/hip_bf16.h>
#include <stdint.h>
#include <type_traits>

typedef __attribute__((ext_vector_type(2))) __bf16 bf16x2;
typedef __attribute__((ext_vector_type(4))) __bf16 bf16x4;
typedef __attribute__((ext_vector_type(8))) __bf16 bf16x8;
typedef __attribute__((ext_vector_type(2))) float f32x2;
typedef __attribute__((ext_vector_type(4))) float f32x4;
typedef __attribute__((ext_vector_type(2))) unsigned u32x2;
typedef __attribute__((ext_vector_type(4))) unsigned u32x4;
typedef unsigned short u16;
typedef __amdgpu_buffer_rsrc_t rsrc_t;
#define LAS __attribute__((address_space(3)))
#define GAS __attribute__((address_space(1)))

constexpr int SL = 2048, DM = 576, NH = 32, DH = 128, HD = 4096, NSEG = 64, NST = 128;

constexpr size_t WS_CTL = 0;
constexpr int CTL_BYTES = 65536;
constexpr int F_GEN = 0, F_TMO = 1, F_GO = 2, F_XCC = 10, F_PRB = 14, F_INITW = 16, F_Y = 20;
constexpr int F_U = 320;
constexpr int F_W = 360;
constexpr int F_R = 300;
constexpr int F_M = 65, F_O = 129, F_A = 200, F_Q = 256;
constexpr int W_VO = 1, W_PX = 19, W_PM = 23, W_PC = 24, W_MEM = 25;
constexpr int N_DUR = 0, N_K = 32, N_U = 64, N_RD = 96;
constexpr size_t COMM_BYTES = (size_t)8 << 20;
constexpr unsigned FR_BYTES = SL * HD * 2;
constexpr unsigned B_K = 0;
constexpr unsigned B_V = FR_BYTES;
constexpr unsigned B_U = FR_BYTES;
constexpr unsigned B_O = 3 * FR_BYTES;
constexpr unsigned B_Q = 4 * FR_BYTES;
constexpr unsigned B_AP = 5 * FR_BYTES + NH * 8192;
constexpr unsigned B_YT = B_AP + NSEG * 24 * 16384;
constexpr unsigned B_G = B_YT + DM * SL * 2;
constexpr int SDIR = 58, NDIR = NSEG - SDIR;
constexpr unsigned X_AT = 8u << 20;
constexpr int Y_NEAR = 0, Y_FAR = 144, F_G = 310;
constexpr int F_S = 26;
#define RG_U 3
#define UNEAR 8192u
#define NCOPY 4
#define RG_QKV 63
#define RG_M 63
#define RG_O 63
#define RG_AP 63
#define PB_MB 12u
constexpr unsigned CO = 4u << 20, PB = PB_MB << 20, BASE_BYTES = 244u << 20;
constexpr unsigned U_AT = (128u << 20) - PB;
constexpr size_t WS_END = (size_t)240 << 20;
constexpr int LDS_BYTES = 163840;
constexpr unsigned SPIN_CAP = 1u << 21;

struct Params {
  const u16 *x, *mem0, *Wq, *Wk, *Wv, *Wo, *lar, *aim, *cre, *cim, *ldt, *dsk, *gw;
  u16* out;
  char* ws;
#define TUNE_LIST(X) X(keep_at, "KD", 20) X(t_a, "TA", -6) X(t_first, "TY", 0) X(t_d, "TD", 12) X(rd0, "RD0", 52) X(gentle, "GENTLE", 0) X(dur_dec, "DDEC", 8) X(dur_inc, "DINC", 8) X(dur0, "DP0", 75) X(boost, "BOOST", 3) X(stag, "STAG", 40) X(dret, "DRET", 76) X(cpf, "CPF", 3) X(sa, "SA", 8) X(sb, "SB", 53) X(sa2, "SA2", 18) X(x2, "X2", 6) X(g2, "G2", 3) X(xg0, "XG0", 7) X(xg1, "XG1", 6) X(xtra, "XTRA", 9) X(s4q, "S4Q", 0) X(s4f, "S4F", 8) X(wl2, "WL2", 3) X(s0, "S0", 30) X(v0, "V0", 0) X(xl0, "XL0", 56) X(tlb, "TLB", 18) X(xt0, "XT0", 110) X(xt1, "XT1", 80) X(vlead, "VLEAD", 23) X(xpre, "XPRE", 7) X(pj0, "PJ0", 0) X(xlast, "XLAST", 8) X(hdel, "HDEL", 0) X(hdl2, "HDL2", 0) X(q0, "Q0", 30) X(s4skip, "S4SKIP", 0) X(mixs, "MIXS", 0) X(qa, "QA", 6) X(qb, "QB", 24) X(ob0, "OB0", 30) X(ob1, "OB1", 38) X(late_r, "LR", 8) X(late_s, "LS", 8)
#define TUNE_DECL(n, e, v) static constexpr int n = v;
  TUNE_LIST(TUNE_DECL)
};

__device__ __forceinline__ int lane_id() {
  int z = 0;
  asm volatile("" : "+v"(z));
  return __builtin_amdgcn_mbcnt_hi(~0u, __builtin_amdgcn_mbcnt_lo(~0u, z));
}
__device__ __forceinline__ float bf2f(u16 x) { return __uint_as_float((unsigned)x << 16); }
__device__ __forceinline__ float rbf(float x) { return (float)(__bf16)x; }
__device__ __forceinline__ unsigned pk2(float a, float b) {
  f32x2 v = {a, b};
  return __builtin_bit_cast(unsigned, __builtin_convertvector(v, bf16x2));
}
__device__ __forceinline__ float lo16(unsigned p) { return __uint_as_float(p << 16); }
__device__ __forceinline__ float hi16(unsigned p) { return __uint_as_float(p & 0xFFFF0000u); }
__device__ __forceinline__ bf16x8 zext8(u32x2 v) {
  u32x4 z = {v[0], v[1], 0u, 0u};
  return __builtin_bit_cast(bf16x8, z);
}
__device__ __forceinline__ bf16x4 tr_read(const char* smem_base, unsigned off) {
  return __builtin_amdgcn_ds_read_tr16_b64_v4bf16((LAS bf16x4*)((LAS char*)smem_base + off));
}
__device__ __forceinline__ float red_max_g(float x) {
  auto r = __builtin_amdgcn_permlane16_swap(__float_as_uint(x), __float_as_uint(x), false, false);
  x = fmaxf(__uint_as_float(r[0]), __uint_as_float(r[1]));
  auto q = __builtin_amdgcn_permlane32_swap(__float_as_uint(x), __float_as_uint(x), false, false);
  return fmaxf(__uint_as_float(q[0]), __uint_as_float(q[1]));
}
__device__ __forceinline__ float red_sum_g(float x) {
  auto r = __builtin_amdgcn_permlane16_swap(__float_as_uint(x), __float_as_uint(x), false, false);
  x = __uint_as_float(r[0]) + __uint_as_float(r[1]);
  auto q = __builtin_amdgcn_permlane32_swap(__float_as_uint(x), __float_as_uint(x), false, false);
  return __uint_as_float(q[0]) + __uint_as_float(q[1]);
}
__device__ __forceinline__ rsrc_t mk_rsrc(const void* p, size_t bytes) {
  return __builtin_amdgcn_make_buffer_rsrc((void*)p, 0, (int)bytes, 0x00020000);
}

#define STR2(x) #x
#define STR(x) STR2(x)
#define CODE_PAD(N) asm volatile("s_branch .Lpad_%=\n\t.fill " STR(N) ", 4, 0xBF800000\n\t.Lpad_%=:" ::: "memory")
#define PAD_RED 0
#define PAD_ATT 0
#define PAD_OP 0
#define PAD_S4 0
#define PAD_CH 0
#define WAIT_VM(n) asm volatile("s_waitcnt vmcnt(" #n ")" ::: "memory")
#define WAIT_LGKM0() asm volatile("s_waitcnt lgkmcnt(0)" ::: "memory")
#define BARRIER() do { WAIT_LGKM0(); __builtin_amdgcn_s_barrier(); asm volatile("" ::: "memory"); } while (0)
#define SB() __builtin_amdgcn_sched_barrier(0)
#define MF(a, b, c) __builtin_amdgcn_mfma_f32_16x16x32_bf16(a, b, c, 0, 0, 0)
#define RLX_AGENT __ATOMIC_RELAXED, __HIP_MEMORY_SCOPE_AGENT
#define DEV_COUNT(i, cond) do {} while (0)
#define DEV_COUNT_DECL do {} while (0)
#define DEV_COUNT_FLUSH() do {} while (0)
__device__ __forceinline__ bf16x8 cat2(bf16x4 a, bf16x4 b) { return __builtin_shufflevector(a, b, 0, 1, 2, 3, 4, 5, 6, 7); }
__device__ __forceinline__ u32x4 pack8(const f32x4& a, const f32x4& b) {
  return (u32x4){pk2(a[0], a[1]), pk2(a[2], a[3]), pk2(b[0], b[1]), pk2(b[2], b[3])};
}
__device__ __forceinline__ void dma16(rsrc_t rs, unsigned voff, unsigned soff, unsigned lds_addr) {
  unsigned keep;
  asm volatile("s_mov_b32 %0, m0\n\ts_mov_b32 m0, %3\n\ts_nop 0\n\tbuffer_load_dwordx4 %1, %2, %4 offen lds\n\ts_mov_b32 m0, %0"
               : "=&s"(keep) : "v"(voff), "s"(rs), "s"(lds_addr), "s"(soff) : "memory");
}
__device__ __forceinline__ void st128(u32x4 v, rsrc_t rs, unsigned voff, unsigned soff) {
  __builtin_amdgcn_raw_buffer_store_b128(v, rs, voff, soff, 16);
  asm volatile("s_nop 1" ::"v"(v) : "memory");
}
__device__ __forceinline__ u32x4 ld128(rsrc_t rs, unsigned voff, unsigned soff) { return __builtin_amdgcn_raw_buffer_load_b128(rs, voff, soff, 16); }
__device__ __forceinline__ bf16x8 ldfrag(rsrc_t rs, unsigned voff, unsigned soff) { return __builtin_bit_cast(bf16x8, ld128(rs, voff, soff)); }
__device__ __forceinline__ void sleep_n(int n) { for (int i = 0; i < n; ++i) __builtin_amdgcn_s_sleep(1); }

__device__ __forceinline__ char* comm_base(char* ws) {
  return (char*)(((uintptr_t)ws + CTL_BYTES + ((2u << 20) - 1)) & ~(uintptr_t)((2u << 20) - 1));
}
__device__ __forceinline__ unsigned rt_now() {
  unsigned long long t;
  asm volatile("s_memrealtime %0\n\ts_waitcnt lgkmcnt(0)" : "=s"(t) :: "memory");
  return (unsigned)t;
}
__device__ __forceinline__ int wait_rt(unsigned t) {
  int d = (int)(t - rt_now());
  if (d <= 0 || d > 2000) return -d;
  for (d -= 14; d > 0; d -= 3) __builtin_amdgcn_s_sleep(1);
  do { d = (int)(t - rt_now()); } while (d > 0);
  return -d;
}
__device__ __forceinline__ unsigned xcc_id() { return (unsigned)__builtin_amdgcn_s_getreg(20 | (3 << 11)) & 7u; }
typedef unsigned long long u64;
struct Ctx { char* ws; unsigned tag; };
__device__ __forceinline__ GAS u64* flag_ptr(char* ws, int idx) { return (GAS u64*)(ws + WS_CTL + idx * 128); }
__device__ __forceinline__ unsigned gen_load(char* ws) { return __hip_atomic_load((GAS unsigned*)flag_ptr(ws, F_GEN), RLX_AGENT); }
__device__ __forceinline__ unsigned flag_get(const Ctx& cx, GAS u64* p) {
  const u64 v = __hip_atomic_load(p, RLX_AGENT);
  return (unsigned)(v >> 32) == cx.tag ? (unsigned)v : 0u;
}
__device__ __forceinline__ void flag_put(const Ctx& cx, GAS u64* p, unsigned v) { __hip_atomic_store(p, ((u64)cx.tag << 32) | v, RLX_AGENT); }
__device__ __forceinline__ bool spin_fail(unsigned& spins, const Ctx& cx) {
  ++spins;
  if (spins > SPIN_CAP) { flag_put(cx, flag_ptr(cx.ws, F_TMO), 1u); return true; }
  return (spins & 1023u) == 0 && __builtin_amdgcn_readfirstlane(flag_get(cx, flag_ptr(cx.ws, F_TMO)));
}
template <int SLEEP = 1>
__device__ __forceinline__ void wait_ge_n(const Ctx& cx, int idx, int stride, int cnt, int target, int& seen) {
  unsigned spins = 0;
  const int k = lane_id() & 3;
  while (seen < target) {
    int v = (int)flag_get(cx, flag_ptr(cx.ws, idx + (k < cnt ? k : 0) * stride));
    seen = min(min(__builtin_amdgcn_readlane(v, 0), __builtin_amdgcn_readlane(v, 1)), min(__builtin_amdgcn_readlane(v, 2), __builtin_amdgcn_readlane(v, 3)));
    if (seen < target) {
      __builtin_amdgcn_s_sleep(SLEEP);
      if (spin_fail(spins, cx)) break;
    }
  }
  __builtin_amdgcn_fence(__ATOMIC_ACQUIRE, "wavefront");
}
template <int SLEEP>
__device__ __forceinline__ void wait_all(const Ctx& cx, int idx, int cnt) {
  unsigned spins = 0;
  const int k = lane_id();
  while (__any(flag_get(cx, flag_ptr(cx.ws, idx) + (k < cnt ? k : 0)) == 0u)) {
    __builtin_amdgcn_s_sleep(SLEEP);
    if (spin_fail(spins, cx)) break;
  }
  __builtin_amdgcn_fence(__ATOMIC_ACQUIRE, "wavefront");
}
__device__ __forceinline__ void publish(const Ctx& cx, int w, int idx, int value) {
  if (w == 0 && lane_id() == 0) flag_put(cx, flag_ptr(cx.ws, idx), (unsigned)value);
}

constexpr unsigned X1 = 0x101000u, TW_OFF = 27 * 32768 + 1024;
__device__ __forceinline__ unsigned win_static(int win) { return win * 32768 + (win >> 4) * 16384; }
__device__ __forceinline__ unsigned near_static(int i) { return (win_static(28 + (i >> 5)) ^ ((i & 32) << 7) ^ ((i & 64) << 8)) + (i & 31) * 128; }
struct Place {
  unsigned go;
  __device__ __forceinline__ void load(const Ctx& cx, int grp, unsigned need = 0xC0000000u) {
    unsigned spins = 0;
    while ((go = __builtin_amdgcn_readfirstlane(flag_get(cx, flag_ptr(cx.ws, F_GO + grp)))) < need && !spin_fail(spins, cx)) __builtin_amdgcn_s_sleep(8);
    __builtin_amdgcn_fence(__ATOMIC_ACQUIRE, "wavefront");
  }
  __device__ __forceinline__ unsigned dyn(int x) const { return ((go >> (4 * x)) & 7u) << 12; }
  __device__ __forceinline__ unsigned tog() const { return (((go >> 16) & 7u) << 12) | (2u << 20); }
  __device__ __forceinline__ unsigned slot(int s) const { return (((s ^ (s >> 1)) & 1) ? tog() : 0u) ^ ((s & 2) ? X1 : 0u); }
  __device__ __forceinline__ unsigned near(int i) const { return near_static(i) ^ (dyn(0) & 8192u); }
};
#define BULK_FLIP 0u
#define OUT_AUX 0
#define PROBE_N 6
struct Bulk {
  rsrc_t rs;
  unsigned w13;
  Place pl;
  __device__ __forceinline__ GAS u64* proj_flag(const Ctx& cx, int m, int h) const {
    return m == 0 ? flag_ptr(cx.ws, F_Q + h) : (GAS u64*)(comm_base(cx.ws) + pl.near(N_K + h) + (m - 1) * 8);
  }
  __device__ __forceinline__ void init(const Ctx& cx, int grp, u64 early = 0) {
    pl.go = __builtin_amdgcn_readfirstlane((unsigned)(early >> 32) == cx.tag ? (unsigned)early : 0u);
    if (pl.go < 0x80000000u) pl.load(cx, grp, 0x80000000u);
    rs = mk_rsrc(comm_base(cx.ws) - CO, BASE_BYTES);
    w13 = PB + ((~pl.go ^ BULK_FLIP) & 2u) * 4096;
  }
  __device__ __forceinline__ unsigned at(unsigned o) const {
    const unsigned q = w13 + ((o >> 13) << 14) + (o & 8191u);
    return q ^ ((q >> 8) & 8192u);
  }
};
__device__ __forceinline__ unsigned near_head(unsigned b, int h) { return PB + 2 * b + h * (1u << 20) + ((h >> 1) & 1) * 8192 + ((2 * h) & 63) * 16384; }
__device__ __forceinline__ unsigned u_tile(int T, int hb, int h7) {
  const int wv = T < 32 ? (T >> 1) & 3 : T & 3;
  return PB + 2 * B_U + T * (1u << 20) + ((T >> 1) & 1) * 8192 + (((T >> 3) & 3) * 4 + wv) * 65536 + (hb ^ ((T >> 3) & 3)) * 16384 + (h7 ^ (2 * wv + (T < 32 ? T & 1 : 0))) * 1024;
}
__device__ __forceinline__ unsigned xcc_page(unsigned xc) { return ((xc >> 1) & 1u) | ((xc >> 2) << 1) | ((xc & 1u) << 2); }

__device__ __forceinline__ void touch_pages(char* ws, int i) {
  const rsrc_t rs = mk_rsrc(comm_base(ws) - CO, BASE_BYTES);
  const unsigned o = lane_id() * (2u << 20) + (1u << 20) + (i & 63) * 128;
  const unsigned a = __builtin_amdgcn_raw_buffer_load_b32(rs, o, 4u << 20, 0), b = __builtin_amdgcn_raw_buffer_load_b32(rs, o, 132u << 20, 0);
  asm volatile("" :: "v"(a), "v"(b));
}

__device__ __forceinline__ void comm_init(Ctx& cx, int rid, int w, char* smem) {
  const int l = lane_id(), tid = w * 64 + l;
  const rsrc_t rs = mk_rsrc(comm_base(cx.ws), COMM_BYTES);
  const unsigned gen = gen_load(cx.ws);
  if (rid == 0) {
    LAS unsigned* const best = (LAS unsigned*)smem + 16;
    const unsigned base = w * 1024, r0 = rt_now(), c0 = (unsigned)__builtin_amdgcn_s_memtime();
    best[32] = 0u;
#pragma unroll
    for (int k = 0; k < 8; ++k) __builtin_amdgcn_raw_buffer_store_b128((u32x4){0u, 0u, 0u, 0u}, rs, base + k * 4096 + l * 16, 0, 16);
    WAIT_VM(0);
    BARRIER();
    unsigned mine = 0xFFFFu;
    auto sample = [&](int pg, int k) {
      unsigned long long t0, t1;
      asm volatile("s_memtime %0\n\ts_waitcnt lgkmcnt(0)" : "=s"(t0) :: "memory");
      const unsigned v = __builtin_amdgcn_raw_buffer_load_b32(rs, base + pg * 4096 + k * 128, 0, 16);
      asm volatile("s_waitcnt vmcnt(0)\n\ts_memtime %0\n\ts_waitcnt lgkmcnt(0)" : "=s"(t1) : "v"(v) : "memory");
      if (l == pg) mine = min(mine, (unsigned)(t1 - t0));
      return (unsigned)(t1 - t0);
    };
    auto all = [&]() {
      if (l < 8) best[w * 8 + l] = mine;
      __syncthreads();
      const int k8 = l & 7;
      const unsigned b = min(min(best[k8], best[8 + k8]), min(best[16 + k8], best[24 + k8]));
      __syncthreads();
      return b;
    };
    auto page = [](int j) { return (j & 1) | ((j & 2) << 1); };
    const unsigned lim = 25u * ((unsigned)__builtin_amdgcn_s_memtime() - c0) / max(rt_now() - r0, 1u);
    cx.tag = __builtin_amdgcn_readfirstlane(gen) + 1u;
    volatile LAS unsigned* const found = best + 32;
    for (int k = 0; k < 40 && !__builtin_amdgcn_readfirstlane(*found); ++k) {
      const int pg = page(w + (k >> 1)) | ((k & 1) << 1);
      if (sample(pg, k >> 3) < lim && l == 0) atomicCAS((unsigned*)found, 0u, 1u | (pg & 2));
    }
    unsigned b = all();
    b = min(b, (unsigned)__shfl_xor((int)b, 1)); b = min(b, (unsigned)__shfl_xor((int)b, 4));
    const unsigned fd = __builtin_amdgcn_readfirstlane(*found);
    const unsigned half = fd ? fd & 2u : __builtin_amdgcn_readlane(b, 2) < __builtin_amdgcn_readlane(b, 0) ? 2u : 0u;
    if (w == 3 && l < 8) flag_put(cx, flag_ptr(cx.ws, F_GO + l), 0x80000000u | half);
    unsigned xc = 0;
    if (w < 3) {
#pragma unroll
      for (int k = 0; k < PROBE_N; ++k) sample(page(w + k) | half, 5 + (k >> 2));
    } else {
      for (unsigned spins = 0; __any((xc = flag_get(cx, flag_ptr(cx.ws, F_INITW) + min(l, 35))) == 0u) && !spin_fail(spins, cx);) __builtin_amdgcn_s_sleep(2);
    }
    b = (all() << 3) | (l & 7);
    b = min(b, (unsigned)__shfl_xor((int)b, 1)); b = min(b, (unsigned)__shfl_xor((int)b, 4));
    if (w == 3) {
      const unsigned v = l ? __shfl((int)xc, min(l, 3) * 9 - 1) - 1u : xcc_id();
      const unsigned p0 = (__builtin_amdgcn_readlane(b, half) & 7u) ^ xcc_page(xcc_id());
      unsigned seen = 0, go = 0xC0020000u;
#pragma unroll
      for (int x = 0; x < 4; ++x) seen |= 1u << (__builtin_amdgcn_readlane(v, x) & 7u);
#pragma unroll
      for (int x = 0; x < 4; ++x)
        go |= (seen == 15u || seen == 240u ? (xcc_page(__builtin_amdgcn_readlane(v, x)) ^ p0) & 7u : page(x) | half) << (4 * x);
      if (l < 8) flag_put(cx, flag_ptr(cx.ws, F_GO + l), go);
    }
    return;
  }
  for (int j = (rid - 1) * 256 + tid; j < 34 * 2048; j += 35 * 256) {
    const int wi = 33 - (j >> 11), ch = j & 2047;
    const int win = wi < 24 ? wi + 1 : wi < 27 ? wi + 65 : wi == 27 ? 59 : wi == 28 ? 123 : wi - 2;
    const unsigned f = wi < 30 ? 0xFFFFFFFFu : 0u;
    __builtin_amdgcn_raw_buffer_store_b128((u32x4){f, f, f, f}, rs, win * 32768 + ch * 16, 0, 16);
  }
  cx.tag = __builtin_amdgcn_readfirstlane(gen) + 1u;
  WAIT_VM(0);
  BARRIER();
  if (tid == 0) flag_put(cx, flag_ptr(cx.ws, F_INITW) + rid - 1, xcc_id() + 1u);
}

__device__ __forceinline__ void keep_role(const Params& p) {
  char* const ws = p.ws;
  const int l = lane_id();
  const Ctx cx = {ws, __builtin_amdgcn_readfirstlane(gen_load(ws)) + 1u};
  if (l == 0) flag_put(cx, flag_ptr(ws, F_INITW) + 35, 1u);
  const rsrc_t crs = mk_rsrc(comm_base(ws), COMM_BYTES);
  Place pl;
  pl.load(cx, 0);
  int dp[2] = {4 * p.dur0, 4 * p.rd0}, boost = 0;
  for (unsigned spins = 0; p.t_first && __any(min(flag_get(cx, flag_ptr(ws, F_U + (l & 31)) + (l >> 5)), flag_get(cx, flag_ptr(ws, F_U + (l & 31)) + 2 + (l >> 5))) == 0u) && !spin_fail(spins, cx);)
    __builtin_amdgcn_s_sleep(1);
  unsigned t = rt_now() + p.t_first - 2 * (p.dur0 + p.t_a + p.xtra) - (p.rd0 + p.t_d + p.xg0), ta = 0, shift = 0;
  auto plan = [&](int b) {
    u32x4 wd = {0u, 0u, 0u, 0u};
#pragma unroll
    for (int k = 0; k < 4; ++k) {
      const int s = 4 * b + k;
      const unsigned t1 = t + ((dp[0] + k) >> 2) + p.t_a + boost + (s < p.sa ? p.xtra : s < p.sa2 ? p.x2 : s > p.xl0 ? p.xlast : 0);
      t = t1 + ((dp[1] + 3 - k) >> 2) + p.t_d + boost + (s < p.sa ? p.xg0 : s < p.sa2 ? p.g2 : s >= p.sb ? p.xg1 : 0);
      if ((l & 7) == k) wd = (u32x4){(unsigned)s, t1, t, 0u};
      if (k == 2) ta = t;
    }
    st128(wd, crs, l < 32 ? (16384u ^ pl.dyn(l >> 3)) + (l & 7) * 16 : 0x7F000000u, TW_OFF + 1024 + b * 128);
  };
  plan(0);
  for (int b = 1; b < NSEG / 4; ++b) {
    u32x2 dw;
    const unsigned before = shift;
    for (unsigned spins = 0; !spin_fail(spins, cx); shift += p.dret) {
      const int d = (int)(ta + shift + p.keep_at - rt_now());
      if (d < -20 && d > -30000) continue;
      if (d > 60 && d < 2000) sleep_n((d - 40) >> 2);
      while ((unsigned)(ta + shift + p.keep_at - rt_now() - 1u) < 2000u) __builtin_amdgcn_s_sleep(2);
      const unsigned f = flag_get(cx, (GAS u64*)(comm_base(ws) + TW_OFF + (16384u ^ pl.dyn(0)) + 512));
      dw = __builtin_amdgcn_raw_buffer_load_b64(crs, pl.near((l < 32 ? N_DUR : N_RD - 32) + l), 0, 16);
      if ((__builtin_amdgcn_readfirstlane(f) & 255u) >= (unsigned)(4 * b - 2)) { shift = __builtin_amdgcn_readfirstlane(f) >> 8; break; }
    }
    if (b > 2) boost = shift - before > (unsigned)p.dret ? min(boost + p.boost, 12) : shift == before ? max(boost - 1, 0) : boost;
    unsigned dur = dw[1] == cx.tag && (dw[0] & 1023u) ? (unsigned)((float)(dw[0] >> 10) * 64.f * __builtin_amdgcn_rcpf((float)(dw[0] & 1023u))) : 0u;
    const unsigned long long none = __ballot(dur == 0u);
#pragma unroll
    for (int m = 1; m < 32; m <<= 1) dur = max(dur, (unsigned)__shfl_xor((int)dur, m));
#pragma unroll
    for (int k = 0; k < 2; ++k) {
      const int dm = __builtin_amdgcn_readlane(dur, 32 * k), d = dp[k];
      if ((unsigned)(none >> (32 * k)) == 0u) dp[k] = max(min(dm, d + p.dur_inc), d - p.dur_dec);
    }
    plan(b);
  }
}

#define X_ASM_TEXT \
  "s_cmp_gt_u32 %28, 1\n" \
  "s_cbranch_scc1 .LX_other_%=\n" \
  "s_mov_b64 s[40:41], %24\n" \
  "s_and_b32 s41, s41, 0xffff\n" \
  "s_mov_b32 s42, 255852544\n" \
  "s_mov_b32 s43, 0x00020000\n" \
  "s_mov_b32 s46, 4718592\n" \
  "s_mov_b32 s47, 0x00020000\n" \
  "s_lshl_b32 s72, %28, 11\n" \
  "buffer_load_dword v196, off, s[40:43], %38 sc1\n" \
  "s_mov_b64 s[44:45], %25\n" \
  "s_and_b32 s45, s45, 0xffff\n" \
  "s_add_u32 s60, %29, 0\n" \
  "s_lshl_b32 s61, s72, 1\n" \
  "s_add_u32 m0, s61, 16384\n" \
  "s_nop 0\n" \
  "buffer_load_dwordx4 v200, s[44:47], s60 offen lds\n" \
  "buffer_load_dwordx4 v201, s[44:47], s60 offen offset:1024 lds\n" \
  "buffer_load_dwordx4 v202, s[44:47], s60 offen offset:2048 lds\n" \
  "buffer_load_dwordx4 v203, s[44:47], s60 offen offset:3072 lds\n" \
  "s_add_u32 s60, %29, 524288\n" \
  "s_lshl_b32 s61, s72, 1\n" \
  "s_add_u32 m0, s61, 32768\n" \
  "s_nop 0\n" \
  "buffer_load_dwordx4 v200, s[44:47], s60 offen lds\n" \
  "buffer_load_dwordx4 v201, s[44:47], s60 offen offset:1024 lds\n" \
  "buffer_load_dwordx4 v202, s[44:47], s60 offen offset:2048 lds\n" \
  "buffer_load_dwordx4 v203, s[44:47], s60 offen offset:3072 lds\n" \
  "s_add_u32 s60, %29, 1048576\n" \
  "s_lshl_b32 s61, s72, 1\n" \
  "s_add_u32 m0, s61, 49152\n" \
  "s_nop 0\n" \
  "buffer_load_dwordx4 v200, s[44:47], s60 offen lds\n" \
  "buffer_load_dwordx4 v201, s[44:47], s60 offen offset:1024 lds\n" \
  "buffer_load_dwordx4 v202, s[44:47], s60 offen offset:2048 lds\n" \
  "buffer_load_dwordx4 v203, s[44:47], s60 offen offset:3072 lds\n" \
  "s_add_u32 s60, %29, 1572864\n" \
  "s_lshl_b32 s61, s72, 1\n" \
  "s_add_u32 m0, s61, 65536\n" \
  "s_nop 0\n" \
  "buffer_load_dwordx4 v200, s[44:47], s60 offen lds\n" \
  "buffer_load_dwordx4 v201, s[44:47], s60 offen offset:1024 lds\n" \
  "buffer_load_dwordx4 v202, s[44:47], s60 offen offset:2048 lds\n" \
  "buffer_load_dwordx4 v203, s[44:47], s60 offen offset:3072 lds\n" \
  "s_add_u32 s60, %29, 2097152\n" \
  "s_lshl_b32 s61, s72, 1\n" \
  "s_add_u32 m0, s61, 81920\n" \
  "s_nop 0\n" \
  "buffer_load_dwordx4 v200, s[44:47], s60 offen lds\n" \
  "buffer_load_dwordx4 v201, s[44:47], s60 offen offset:1024 lds\n" \
  "buffer_load_dwordx4 v202, s[44:47], s60 offen offset:2048 lds\n" \
  "buffer_load_dwordx4 v203, s[44:47], s60 offen offset:3072 lds\n" \
  "s_add_u32 s60, %29, 2621440\n" \
  "s_lshl_b32 s61, s72, 1\n" \
  "s_add_u32 m0, s61, 98304\n" \
  "s_nop 0\n" \
  "buffer_load_dwordx4 v200, s[44:47], s60 offen lds\n" \
  "buffer_load_dwordx4 v201, s[44:47], s60 offen offset:1024 lds\n" \
  "buffer_load_dwordx4 v202, s[44:47], s60 offen offset:2048 lds\n" \
  "buffer_load_dwordx4 v203, s[44:47], s60 offen offset:3072 lds\n" \
  "s_add_u32 s60, %29, 3145728\n" \
  "s_lshl_b32 s61, s72, 1\n" \
  "s_add_u32 m0, s61, 114688\n" \
  "s_nop 0\n" \
  "buffer_load_dwordx4 v200, s[44:47], s60 offen lds\n" \
  "buffer_load_dwordx4 v201, s[44:47], s60 offen offset:1024 lds\n" \
  "buffer_load_dwordx4 v202, s[44:47], s60 offen offset:2048 lds\n" \
  "buffer_load_dwordx4 v203, s[44:47], s60 offen offset:3072 lds\n" \
  "s_add_u32 s60, %29, 3670016\n" \
  "s_lshl_b32 s61, s72, 1\n" \
  "s_add_u32 m0, s61, 131072\n" \
  "s_nop 0\n" \
  "buffer_load_dwordx4 v200, s[44:47], s60 offen lds\n" \
  "buffer_load_dwordx4 v201, s[44:47], s60 offen offset:1024 lds\n" \
  "buffer_load_dwordx4 v202, s[44:47], s60 offen offset:2048 lds\n" \
  "buffer_load_dwordx4 v203, s[44:47], s60 offen offset:3072 lds\n" \
  "s_add_u32 s60, %29, 4194304\n" \
  "s_lshl_b32 s61, s72, 1\n" \
  "s_add_u32 m0, s61, 147456\n" \
  "s_nop 0\n" \
  "buffer_load_dwordx4 v200, s[44:47], s60 offen lds\n" \
  "buffer_load_dwordx4 v201, s[44:47], s60 offen offset:1024 lds\n" \
  "buffer_load_dwordx4 v202, s[44:47], s60 offen offset:2048 lds\n" \
  "buffer_load_dwordx4 v203, s[44:47], s60 offen offset:3072 lds\n" \
  "s_mov_b64 s[44:45], %27\n" \
  "s_and_b32 s45, s45, 0xffff\n" \
  "s_lshr_b32 s60, s72, 3\n" \
  "s_mov_b32 m0, s72\n" \
  "s_nop 0\n" \
  "buffer_load_dwordx4 v206, s[44:47], s60 offen lds\n" \
  "s_add_u32 s60, s60, 128\n" \
  "s_add_u32 m0, s72, 1024\n" \
  "s_nop 0\n" \
  "buffer_load_dwordx4 v206, s[44:47], s60 offen lds\n" \
  "s_movk_i32 s60, 1024\n" \
  "s_movk_i32 m0, 8192\n" \
  "s_nop 0\n" \
  "buffer_load_dwordx4 v206, s[44:47], s60 offen lds\n" \
  "s_waitcnt vmcnt(27)\n" \
  "s_barrier\n" \
  "v_add_u32 v194, 16384, v204\n" \
  "ds_read_b64_tr_b16 a[0:1], v194\n" \
  "ds_read_b64_tr_b16 a[2:3], v194 offset:512\n" \
  "ds_read_b64_tr_b16 a[4:5], v194 offset:8192\n" \
  "ds_read_b64_tr_b16 a[6:7], v194 offset:8704\n" \
  "v_add_u32 v195, 16384, v205\n" \
  "ds_read_b64_tr_b16 a[72:73], v195\n" \
  "ds_read_b64_tr_b16 a[74:75], v195 offset:512\n" \
  "ds_read_b64_tr_b16 a[76:77], v195 offset:8192\n" \
  "ds_read_b64_tr_b16 a[78:79], v195 offset:8704\n" \
  "v_add_u32 v194, 32768, v204\n" \
  "ds_read_b64_tr_b16 a[8:9], v194\n" \
  "ds_read_b64_tr_b16 a[10:11], v194 offset:512\n" \
  "ds_read_b64_tr_b16 a[12:13], v194 offset:8192\n" \
  "ds_read_b64_tr_b16 a[14:15], v194 offset:8704\n" \
  "v_add_u32 v195, 32768, v205\n" \
  "ds_read_b64_tr_b16 a[80:81], v195\n" \
  "ds_read_b64_tr_b16 a[82:83], v195 offset:512\n" \
  "ds_read_b64_tr_b16 a[84:85], v195 offset:8192\n" \
  "ds_read_b64_tr_b16 a[86:87], v195 offset:8704\n" \
  "v_add_u32 v194, 49152, v204\n" \
  "ds_read_b64_tr_b16 a[16:17], v194\n" \
  "ds_read_b64_tr_b16 a[18:19], v194 offset:512\n" \
  "ds_read_b64_tr_b16 a[20:21], v194 offset:8192\n" \
  "ds_read_b64_tr_b16 a[22:23], v194 offset:8704\n" \
  "v_add_u32 v195, 49152, v205\n" \
  "ds_read_b64_tr_b16 a[88:89], v195\n" \
  "ds_read_b64_tr_b16 a[90:91], v195 offset:512\n" \
  "ds_read_b64_tr_b16 a[92:93], v195 offset:8192\n" \
  "ds_read_b64_tr_b16 a[94:95], v195 offset:8704\n" \
  "s_waitcnt lgkmcnt(0)\n" \
  "s_barrier\n" \
  "s_mov_b64 s[44:45], %26\n" \
  "s_and_b32 s45, s45, 0xffff\n" \
  "s_add_u32 s60, %29, 0\n" \
  "s_lshl_b32 s61, s72, 1\n" \
  "s_add_u32 m0, s61, 16384\n" \
  "s_nop 0\n" \
  "buffer_load_dwordx4 v200, s[44:47], s60 offen lds\n" \
  "buffer_load_dwordx4 v201, s[44:47], s60 offen offset:1024 lds\n" \
  "buffer_load_dwordx4 v202, s[44:47], s60 offen offset:2048 lds\n" \
  "buffer_load_dwordx4 v203, s[44:47], s60 offen offset:3072 lds\n" \
  "s_add_u32 s60, %29, 524288\n" \
  "s_lshl_b32 s61, s72, 1\n" \
  "s_add_u32 m0, s61, 32768\n" \
  "s_nop 0\n" \
  "buffer_load_dwordx4 v200, s[44:47], s60 offen lds\n" \
  "buffer_load_dwordx4 v201, s[44:47], s60 offen offset:1024 lds\n" \
  "buffer_load_dwordx4 v202, s[44:47], s60 offen offset:2048 lds\n" \
  "buffer_load_dwordx4 v203, s[44:47], s60 offen offset:3072 lds\n" \
  "s_add_u32 s60, %29, 1048576\n" \
  "s_lshl_b32 s61, s72, 1\n" \
  "s_add_u32 m0, s61, 49152\n" \
  "s_nop 0\n" \
  "buffer_load_dwordx4 v200, s[44:47], s60 offen lds\n" \
  "buffer_load_dwordx4 v201, s[44:47], s60 offen offset:1024 lds\n" \
  "buffer_load_dwordx4 v202, s[44:47], s60 offen offset:2048 lds\n" \
  "buffer_load_dwordx4 v203, s[44:47], s60 offen offset:3072 lds\n" \
  "v_readfirstlane_b32 s55, v196\n" \
  "s_add_u32 s55, s55, 1\n" \
  "s_waitcnt vmcnt(27)\n" \
  "s_barrier\n" \
  "v_add_u32 v194, 65536, v204\n" \
  "ds_read_b64_tr_b16 a[24:25], v194\n" \
  "ds_read_b64_tr_b16 a[26:27], v194 offset:512\n" \
  "ds_read_b64_tr_b16 a[28:29], v194 offset:8192\n" \
  "ds_read_b64_tr_b16 a[30:31], v194 offset:8704\n" \
  "v_add_u32 v195, 65536, v205\n" \
  "ds_read_b64_tr_b16 a[96:97], v195\n" \
  "ds_read_b64_tr_b16 a[98:99], v195 offset:512\n" \
  "ds_read_b64_tr_b16 a[100:101], v195 offset:8192\n" \
  "ds_read_b64_tr_b16 a[102:103], v195 offset:8704\n" \
  "v_add_u32 v194, 81920, v204\n" \
  "ds_read_b64_tr_b16 a[32:33], v194\n" \
  "ds_read_b64_tr_b16 a[34:35], v194 offset:512\n" \
  "ds_read_b64_tr_b16 a[36:37], v194 offset:8192\n" \
  "ds_read_b64_tr_b16 a[38:39], v194 offset:8704\n" \
  "v_add_u32 v195, 81920, v205\n" \
  "ds_read_b64_tr_b16 a[104:105], v195\n" \
  "ds_read_b64_tr_b16 a[106:107], v195 offset:512\n" \
  "ds_read_b64_tr_b16 a[108:109], v195 offset:8192\n" \
  "ds_read_b64_tr_b16 a[110:111], v195 offset:8704\n" \
  "v_add_u32 v194, 98304, v204\n" \
  "ds_read_b64_tr_b16 a[40:41], v194\n" \
  "ds_read_b64_tr_b16 a[42:43], v194 offset:512\n" \
  "ds_read_b64_tr_b16 a[44:45], v194 offset:8192\n" \
  "ds_read_b64_tr_b16 a[46:47], v194 offset:8704\n" \
  "v_add_u32 v195, 98304, v205\n" \
  "ds_read_b64_tr_b16 a[112:113], v195\n" \
  "ds_read_b64_tr_b16 a[114:115], v195 offset:512\n" \
  "ds_read_b64_tr_b16 a[116:117], v195 offset:8192\n" \
  "ds_read_b64_tr_b16 a[118:119], v195 offset:8704\n" \
  "s_waitcnt lgkmcnt(0)\n" \
  "s_barrier\n" \
  "s_add_u32 s60, %29, 1572864\n" \
  "s_lshl_b32 s61, s72, 1\n" \
  "s_add_u32 m0, s61, 65536\n" \
  "s_nop 0\n" \
  "buffer_load_dwordx4 v200, s[44:47], s60 offen lds\n" \
  "buffer_load_dwordx4 v201, s[44:47], s60 offen offset:1024 lds\n" \
  "buffer_load_dwordx4 v202, s[44:47], s60 offen offset:2048 lds\n" \
  "buffer_load_dwordx4 v203, s[44:47], s60 offen offset:3072 lds\n" \
  "s_add_u32 s60, %29, 2097152\n" \
  "s_lshl_b32 s61, s72, 1\n" \
  "s_add_u32 m0, s61, 81920\n" \
  "s_nop 0\n" \
  "buffer_load_dwordx4 v200, s[44:47], s60 offen lds\n" \
  "buffer_load_dwordx4 v201, s[44:47], s60 offen offset:1024 lds\n" \
  "buffer_load_dwordx4 v202, s[44:47], s60 offen offset:2048 lds\n" \
  "buffer_load_dwordx4 v203, s[44:47], s60 offen offset:3072 lds\n" \
  "s_add_u32 s60, %29, 2621440\n" \
  "s_lshl_b32 s61, s72, 1\n" \
  "s_add_u32 m0, s61, 98304\n" \
  "s_nop 0\n" \
  "buffer_load_dwordx4 v200, s[44:47], s60 offen lds\n" \
  "buffer_load_dwordx4 v201, s[44:47], s60 offen offset:1024 lds\n" \
  "buffer_load_dwordx4 v202, s[44:47], s60 offen offset:2048 lds\n" \
  "buffer_load_dwordx4 v203, s[44:47], s60 offen offset:3072 lds\n" \
  "s_waitcnt vmcnt(27)\n" \
  "s_barrier\n" \
  "v_add_u32 v194, 114688, v204\n" \
  "ds_read_b64_tr_b16 a[48:49], v194\n" \
  "ds_read_b64_tr_b16 a[50:51], v194 offset:512\n" \
  "ds_read_b64_tr_b16 a[52:53], v194 offset:8192\n" \
  "ds_read_b64_tr_b16 a[54:55], v194 offset:8704\n" \
  "v_add_u32 v195, 114688, v205\n" \
  "ds_read_b64_tr_b16 a[120:121], v195\n" \
  "ds_read_b64_tr_b16 a[122:123], v195 offset:512\n" \
  "ds_read_b64_tr_b16 a[124:125], v195 offset:8192\n" \
  "ds_read_b64_tr_b16 a[126:127], v195 offset:8704\n" \
  "v_add_u32 v194, 131072, v204\n" \
  "ds_read_b64_tr_b16 a[56:57], v194\n" \
  "ds_read_b64_tr_b16 a[58:59], v194 offset:512\n" \
  "ds_read_b64_tr_b16 a[60:61], v194 offset:8192\n" \
  "ds_read_b64_tr_b16 a[62:63], v194 offset:8704\n" \
  "v_add_u32 v195, 131072, v205\n" \
  "ds_read_b64_tr_b16 a[128:129], v195\n" \
  "ds_read_b64_tr_b16 a[130:131], v195 offset:512\n" \
  "ds_read_b64_tr_b16 a[132:133], v195 offset:8192\n" \
  "ds_read_b64_tr_b16 a[134:135], v195 offset:8704\n" \
  "v_add_u32 v194, 147456, v204\n" \
  "ds_read_b64_tr_b16 a[64:65], v194\n" \
  "ds_read_b64_tr_b16 a[66:67], v194 offset:512\n" \
  "ds_read_b64_tr_b16 a[68:69], v194 offset:8192\n" \
  "ds_read_b64_tr_b16 a[70:71], v194 offset:8704\n" \
  "v_add_u32 v195, 147456, v205\n" \
  "ds_read_b64_tr_b16 a[136:137], v195\n" \
  "ds_read_b64_tr_b16 a[138:139], v195 offset:512\n" \
  "ds_read_b64_tr_b16 a[140:141], v195 offset:8192\n" \
  "ds_read_b64_tr_b16 a[142:143], v195 offset:8704\n" \
  "s_waitcnt lgkmcnt(0)\n" \
  "s_barrier\n" \
  "s_add_u32 s60, %29, 3145728\n" \
  "s_lshl_b32 s61, s72, 1\n" \
  "s_add_u32 m0, s61, 114688\n" \
  "s_nop 0\n" \
  "buffer_load_dwordx4 v200, s[44:47], s60 offen lds\n" \
  "buffer_load_dwordx4 v201, s[44:47], s60 offen offset:1024 lds\n" \
  "buffer_load_dwordx4 v202, s[44:47], s60 offen offset:2048 lds\n" \
  "buffer_load_dwordx4 v203, s[44:47], s60 offen offset:3072 lds\n" \
  "s_add_u32 s60, %29, 3670016\n" \
  "s_lshl_b32 s61, s72, 1\n" \
  "s_add_u32 m0, s61, 131072\n" \
  "s_nop 0\n" \
  "buffer_load_dwordx4 v200, s[44:47], s60 offen lds\n" \
  "buffer_load_dwordx4 v201, s[44:47], s60 offen offset:1024 lds\n" \
  "buffer_load_dwordx4 v202, s[44:47], s60 offen offset:2048 lds\n" \
  "buffer_load_dwordx4 v203, s[44:47], s60 offen offset:3072 lds\n" \
  "s_add_u32 s60, %29, 4194304\n" \
  "s_lshl_b32 s61, s72, 1\n" \
  "s_add_u32 m0, s61, 147456\n" \
  "s_nop 0\n" \
  "buffer_load_dwordx4 v200, s[44:47], s60 offen lds\n" \
  "buffer_load_dwordx4 v201, s[44:47], s60 offen offset:1024 lds\n" \
  "buffer_load_dwordx4 v202, s[44:47], s60 offen offset:2048 lds\n" \
  "buffer_load_dwordx4 v203, s[44:47], s60 offen offset:3072 lds\n" \
  "buffer_load_dwordx2 v[198:199], off, s[40:43], %37 sc1\n" \
  "s_waitcnt vmcnt(25)\n" \
  "s_barrier\n" \
  "v_add_u32 v195, 16384, v205\n" \
  "ds_read_b64_tr_b16 a[216:217], v195\n" \
  "ds_read_b64_tr_b16 a[218:219], v195 offset:512\n" \
  "ds_read_b64_tr_b16 a[220:221], v195 offset:8192\n" \
  "ds_read_b64_tr_b16 a[222:223], v195 offset:8704\n" \
  "v_add_u32 v195, 32768, v205\n" \
  "ds_read_b64_tr_b16 a[224:225], v195\n" \
  "ds_read_b64_tr_b16 a[226:227], v195 offset:512\n" \
  "ds_read_b64_tr_b16 a[228:229], v195 offset:8192\n" \
  "ds_read_b64_tr_b16 a[230:231], v195 offset:8704\n" \
  "v_add_u32 v195, 49152, v205\n" \
  "ds_read_b64_tr_b16 a[232:233], v195\n" \
  "ds_read_b64_tr_b16 a[234:235], v195 offset:512\n" \
  "ds_read_b64_tr_b16 a[236:237], v195 offset:8192\n" \
  "ds_read_b64_tr_b16 a[238:239], v195 offset:8704\n" \
  "s_waitcnt lgkmcnt(0)\n" \
  "s_waitcnt vmcnt(13)\n" \
  "s_barrier\n" \
  "v_add_u32 v195, 65536, v205\n" \
  "ds_read_b64_tr_b16 a[240:241], v195\n" \
  "ds_read_b64_tr_b16 a[242:243], v195 offset:512\n" \
  "ds_read_b64_tr_b16 a[244:245], v195 offset:8192\n" \
  "ds_read_b64_tr_b16 a[246:247], v195 offset:8704\n" \
  "v_add_u32 v194, 81920, v204\n" \
  "ds_read_b64_tr_b16 a[176:177], v194\n" \
  "ds_read_b64_tr_b16 a[178:179], v194 offset:512\n" \
  "ds_read_b64_tr_b16 a[180:181], v194 offset:8192\n" \
  "ds_read_b64_tr_b16 a[182:183], v194 offset:8704\n" \
  "v_add_u32 v195, 81920, v205\n" \
  "ds_read_b64_tr_b16 a[248:249], v195\n" \
  "ds_read_b64_tr_b16 a[250:251], v195 offset:512\n" \
  "ds_read_b64_tr_b16 a[252:253], v195 offset:8192\n" \
  "ds_read_b64_tr_b16 a[254:255], v195 offset:8704\n" \
  "v_add_u32 v194, 98304, v204\n" \
  "ds_read_b64_tr_b16 a[184:185], v194\n" \
  "ds_read_b64_tr_b16 a[186:187], v194 offset:512\n" \
  "ds_read_b64_tr_b16 a[188:189], v194 offset:8192\n" \
  "ds_read_b64_tr_b16 a[190:191], v194 offset:8704\n" \
  "v_add_u32 v195, 98304, v205\n" \
  "ds_read_b64_tr_b16 v[0:1], v195\n" \
  "ds_read_b64_tr_b16 v[2:3], v195 offset:512\n" \
  "ds_read_b64_tr_b16 v[4:5], v195 offset:8192\n" \
  "ds_read_b64_tr_b16 v[6:7], v195 offset:8704\n" \
  "s_waitcnt lgkmcnt(0)\n" \
  "s_waitcnt vmcnt(1)\n" \
  "s_barrier\n" \
  "v_add_u32 v194, 114688, v204\n" \
  "ds_read_b64_tr_b16 a[192:193], v194\n" \
  "ds_read_b64_tr_b16 a[194:195], v194 offset:512\n" \
  "ds_read_b64_tr_b16 a[196:197], v194 offset:8192\n" \
  "ds_read_b64_tr_b16 a[198:199], v194 offset:8704\n" \
  "v_add_u32 v195, 114688, v205\n" \
  "ds_read_b64_tr_b16 v[8:9], v195\n" \
  "ds_read_b64_tr_b16 v[10:11], v195 offset:512\n" \
  "ds_read_b64_tr_b16 v[12:13], v195 offset:8192\n" \
  "ds_read_b64_tr_b16 v[14:15], v195 offset:8704\n" \
  "v_add_u32 v194, 131072, v204\n" \
  "ds_read_b64_tr_b16 a[200:201], v194\n" \
  "ds_read_b64_tr_b16 a[202:203], v194 offset:512\n" \
  "ds_read_b64_tr_b16 a[204:205], v194 offset:8192\n" \
  "ds_read_b64_tr_b16 a[206:207], v194 offset:8704\n" \
  "v_add_u32 v195, 131072, v205\n" \
  "ds_read_b64_tr_b16 v[16:17], v195\n" \
  "ds_read_b64_tr_b16 v[18:19], v195 offset:512\n" \
  "ds_read_b64_tr_b16 v[20:21], v195 offset:8192\n" \
  "ds_read_b64_tr_b16 v[22:23], v195 offset:8704\n" \
  "v_add_u32 v194, 147456, v204\n" \
  "ds_read_b64_tr_b16 a[208:209], v194\n" \
  "ds_read_b64_tr_b16 a[210:211], v194 offset:512\n" \
  "ds_read_b64_tr_b16 a[212:213], v194 offset:8192\n" \
  "ds_read_b64_tr_b16 a[214:215], v194 offset:8704\n" \
  "v_add_u32 v195, 147456, v205\n" \
  "ds_read_b64_tr_b16 v[24:25], v195\n" \
  "ds_read_b64_tr_b16 v[26:27], v195 offset:512\n" \
  "ds_read_b64_tr_b16 v[28:29], v195 offset:8192\n" \
  "ds_read_b64_tr_b16 v[30:31], v195 offset:8704\n" \
  "s_waitcnt lgkmcnt(0)\n" \
  "s_waitcnt vmcnt(0)\n" \
  "v_readfirstlane_b32 s96, v198\n" \
  "v_readfirstlane_b32 s63, v199\n" \
  "s_cmp_eq_u32 s63, s55\n" \
  "s_cselect_b32 s96, s96, 0\n" \
  "s_cmp_ge_u32 s96, 0xc0000000\n" \
  "s_cbranch_scc1 .LX_go_x_%=\n" \
  "s_mov_b32 s69, 0\n" \
  ".LX_go_%=:\n" \
  "buffer_load_dwordx2 v[196:197], off, s[40:43], %37 sc1\n" \
  "s_waitcnt vmcnt(0)\n" \
  "v_readfirstlane_b32 s96, v196\n" \
  "v_readfirstlane_b32 s63, v197\n" \
  "s_cmp_eq_u32 s63, s55\n" \
  "s_cselect_b32 s96, s96, 0\n" \
  "s_cmp_ge_u32 s96, 0xc0000000\n" \
  "s_cbranch_scc1 .LX_go_x_%=\n" \
  "s_sleep 8\n" \
  "s_add_u32 s69, s69, 1\n" \
  "s_and_b32 s63, s69, 1023\n" \
  "s_cbranch_scc1 .LX_go_%=\n" \
  "s_cmp_gt_u32 s69, 2097152\n" \
  "s_cbranch_scc1 .LX_go_set_%=\n" \
  "buffer_load_dwordx2 v[196:197], off, s[40:43], %38 offset:128 sc1\n" \
  "s_waitcnt vmcnt(0)\n" \
  "v_readfirstlane_b32 s63, v197\n" \
  "s_cmp_eq_u32 s63, s55\n" \
  "s_cbranch_scc0 .LX_go_%=\n" \
  "s_branch .LX_go_x_%=\n" \
  ".LX_go_set_%=:\n" \
  "v_mov_b32 v196, 1\n" \
  "v_mov_b32 v197, s55\n" \
  "buffer_store_dwordx2 v[196:197], off, s[40:43], %38 offset:128 sc1\n" \
  "s_waitcnt vmcnt(0)\n" \
  ".LX_go_x_%=:\n" \
  "s_and_b32 s60, s96, 2\n" \
  "s_lshl_b32 s60, s60, 12\n" \
  "s_xor_b32 s74, %34, s60\n" \
  "s_xor_b32 s75, %36, s60\n" \
  "s_bfe_u32 s56, s96, 0x30010\n" \
  "s_lshl_b32 s56, s56, 12\n" \
  "s_or_b32 s56, s56, 0x200000\n" \
  "s_mov_b32 s57, 1052672\n" \
  "s_lshl_b32 s61, %33, 2\n" \
  "s_lshr_b32 s51, s96, s61\n" \
  "s_and_b32 s51, s51, 7\n" \
  "s_lshl_b32 s51, s51, 12\n" \
  "s_xor_b32 s51, s51, 4210688\n" \
  "s_mov_b32 s71, 0\n" \
  "s_add_u32 s45, s51, 886784\n" \
  "s_mov_b32 s46, -1\n" \
  "v_mov_b64 v[32:33], -1\n" \
  "v_mov_b64 v[34:35], -1\n" \
  "v_and_b32 v194, 3, v246\n" \
  "v_lshlrev_b32 v194, 2, v194\n" \
  "v_lshrrev_b32 v194, v194, s96\n" \
  "v_and_b32 v194, 7, v194\n" \
  "v_lshlrev_b32 v194, 12, v194\n" \
  "v_and_b32 v246, -4, v246\n" \
  "v_xor_b32 v246, v246, v194\n" \
  "v_and_b32 v194, 3, v247\n" \
  "v_lshlrev_b32 v194, 2, v194\n" \
  "v_lshrrev_b32 v194, v194, s96\n" \
  "v_and_b32 v194, 7, v194\n" \
  "v_lshlrev_b32 v194, 12, v194\n" \
  "v_and_b32 v247, -4, v247\n" \
  "v_xor_b32 v247, v247, v194\n" \
  "s_xor_b32 s62, s56, s57\n" \
  "s_nop 1\n" \
  "s_andn2_b32 s60, 2, s96\n" \
  "s_lshl_b32 s60, s60, 12\n" \
  "s_xor_b32 s60, s60, 8192\n" \
  "s_xor_b32 s58, %31, s60\n" \
  "s_mov_b32 s49, 0\n" \
  "s_add_u32 s47, %41, s60\n" \
  "s_add_u32 s97, s60, 8388608\n" \
  "s_xor_b32 s50, %32, s60\n" \
  "s_mov_b32 s48, 0\n" \
  "s_mov_b32 s54, 0\n" \
  "s_cmp_eq_u32 %28, 0\n" \
  "s_cselect_b64 s[88:89], -1, 0\n" \
  "s_mov_b32 s68, 0\n" \
  "s_mov_b32 s70, 1\n" \
  "s_mov_b32 s73, 0\n" \
  "s_mov_b32 s44, 0\n" \
  "s_mov_b32 s53, 0\n" \
  "s_memrealtime s[86:87]\n" \
  "s_memtime s[76:77]\n" \
  "s_waitcnt lgkmcnt(0)\n" \
  "s_mov_b32 s90, s76\n" \
  "s_mov_b32 s87, 340\n" \
  "s_mov_b32 s64, 0x3e0293ee\n" \
  "s_mov_b32 s65, 0xbe0293ee\n" \
  "v_mov_b64 v[44:45], 0\n" \
  "v_mov_b64 v[46:47], 0\n" \
  "v_mov_b64 v[48:49], 0\n" \
  "v_mov_b64 v[50:51], 0\n" \
  "v_mov_b64 v[52:53], 0\n" \
  "v_mov_b64 v[54:55], 0\n" \
  "v_mov_b64 v[56:57], 0\n" \
  "v_mov_b64 v[58:59], 0\n" \
  "v_mov_b32 v40, 0\n" \
  "v_mov_b32 v41, s55\n" \
  "v_mov_b64 v[42:43], v[40:41]\n" \
  "s_mov_b64 s[92:93], 0\n" \
  "s_mov_b32 s92, 0xffff0000\n" \
  "s_lshr_b32 s60, s72, 1\n" \
  "s_add_u32 s60, s60, 32768\n" \
  "v_add_u32 v178, s60, v240\n" \
  "v_xor_b32 v246, 1073741824, v246\n" \
  "v_xor_b32 v247, 1073741824, v247\n" \
  "v_mov_b32 v182, 1065369472\n" \
  "v_mov_b32 v183, 1065369472\n" \
  "s_and_b32 s60, s72, 2048\n" \
  "s_add_u32 s50, s50, s60\n" \
  "s_lshr_b32 s60, s60, 1\n" \
  "v_add_u32 v179, s60, v244\n" \
  "s_lshr_b32 s60, s72, 9\n" \
  "s_add_u32 s60, s60, 17408\n" \
  "v_mov_b32 v184, s60\n" \
  "s_mov_b32 s52, 0\n" \
  "s_barrier\n" \
  "ds_write_b32 v184, v40\n" \
  "s_branch .LX_xr_%=\n" \
  ".LX_dry_b_%=:\n" \
  "s_waitcnt vmcnt(0)\n" \
  "s_barrier\n" \
  "ds_read_b64 v[64:65], v178\n" \
  "ds_read_b64 v[66:67], v178 offset:2048\n" \
  "ds_read_b64 v[68:69], v178 offset:4096\n" \
  "ds_read_b64 v[70:71], v178 offset:6144\n" \
  "ds_read_b64 v[72:73], v178 offset:8\n" \
  "ds_read_b64 v[74:75], v178 offset:2056\n" \
  "ds_read_b64 v[76:77], v178 offset:4104\n" \
  "ds_read_b64 v[78:79], v178 offset:6152\n" \
  "v_xor_b32 v178, 8192, v178\n" \
  "s_mov_b32 s67, 2130706432\n" \
  "s_waitcnt vmcnt(0) lgkmcnt(0)\n" \
  "s_barrier\n" \
  "s_branch .LX_p_%=\n" \
  "s_nop 7\n" \
  ".p2align 6\n" \
  ".fill (((40 - (.LX_p_%= - .LX_rot_%=)) & 63) >> 2), 4, 0xBF800000\n" \
  ".LX_rot_%=:\n" \
  "s_xor_b32 s51, s51, s56\n" \
  "s_xor_b32 s71, s71, s56\n" \
  "v_xor_b32 v246, s56, v246\n" \
  "v_xor_b32 v247, s56, v247\n" \
  "s_mov_b32 s60, s56\n" \
  "s_mov_b32 s56, s57\n" \
  "s_mov_b32 s57, s60\n" \
  "s_add_u32 s59, s51, 819200\n" \
  "s_mov_b32 m0, 0\n" \
  "s_mov_b32 s70, 0\n" \
  "s_mov_b32 s68, 0\n" \
  "s_waitcnt lgkmcnt(0)\n" \
  "s_cmp_lg_u32 %28, 0\n" \
  "s_cbranch_scc1 .LX_g3_%=\n" \
  "s_mov_b32 s69, 0\n" \
  ".LX_tw_%=:\n" \
  "v_readfirstlane_b32 s63, v36\n" \
  "s_cmp_lg_u32 s63, s48\n" \
  "s_cbranch_scc1 .LX_tw_re_%=\n" \
  "v_readfirstlane_b32 s53, v38\n" \
  ".LX_rt_%=:\n" \
  "s_memrealtime s[60:61]\n" \
  "s_add_u32 s73, s53, s44\n" \
  "s_waitcnt lgkmcnt(0)\n" \
  "s_sub_u32 s62, s73, s60\n" \
  "s_cmp_ge_u32 s62, 2000\n" \
  "s_cbranch_scc1 .LX_lat_%=\n" \
  "s_sub_u32 s62, s62, 30\n" \
  ".LX_slp_%=:\n" \
  "s_cmp_le_i32 s62, 0\n" \
  "s_cbranch_scc1 .LX_clk_%=\n" \
  "s_sleep 1\n" \
  "s_sub_u32 s62, s62, 3\n" \
  "s_branch .LX_slp_%=\n" \
  ".LX_clk_%=:\n" \
  "s_memrealtime s[60:61]\n" \
  "s_waitcnt lgkmcnt(0)\n" \
  "s_sub_u32 s62, s73, s60\n" \
  "s_cmp_gt_i32 s62, 21\n" \
  "s_cbranch_scc1 .LX_clk_%=\n" \
  "s_cmp_le_i32 s62, 11\n" \
  "s_cbranch_scc1 .LX_clk2_%=\n" \
  "s_memrealtime s[76:77]\n" \
  "s_nop 4\n" \
  "s_memrealtime s[78:79]\n" \
  "s_nop 4\n" \
  "s_memrealtime s[80:81]\n" \
  "s_nop 4\n" \
  "s_memrealtime s[82:83]\n" \
  "s_waitcnt lgkmcnt(0)\n" \
  "s_mov_b32 s77, 1\n" \
  "s_cmp_eq_u32 s78, s76\n" \
  "s_addc_u32 s77, s77, 0\n" \
  "s_cmp_eq_u32 s80, s76\n" \
  "s_addc_u32 s77, s77, 0\n" \
  "s_cmp_eq_u32 s82, s76\n" \
  "s_addc_u32 s77, s77, 0\n" \
  "s_mul_i32 s77, s77, 24\n" \
  "s_lshr_b32 s78, s87, 2\n" \
  "s_sub_u32 s77, s78, s77\n" \
  "s_max_i32 s77, s77, 0\n" \
  "s_sub_u32 s76, s73, s76\n" \
  "s_mul_i32 s76, s76, s87\n" \
  "s_ashr_i32 s76, s76, 4\n" \
  "s_sub_u32 s76, s76, s77\n" \
  "s_sub_u32 s76, s76, 200\n" \
  "s_max_i32 s76, s76, 0\n" \
  "s_min_i32 s76, s76, 320\n" \
  "s_lshr_b32 s76, s76, 3\n" \
  "s_lshl_b32 s76, s76, 2\n" \
  "s_mov_b32 s68, 1\n" \
  "s_getpc_b64 s[78:79]\n" \
  "s_sub_u32 s76, 180, s76\n" \
  "s_add_u32 s78, s78, s76\n" \
  "s_addc_u32 s79, s79, 0\n" \
  "s_setpc_b64 s[78:79]\n" \
  "s_nop 1\n" \
  "s_nop 1\n" \
  "s_nop 1\n" \
  "s_nop 1\n" \
  "s_nop 1\n" \
  "s_nop 1\n" \
  "s_nop 1\n" \
  "s_nop 1\n" \
  "s_nop 1\n" \
  "s_nop 1\n" \
  "s_nop 1\n" \
  "s_nop 1\n" \
  "s_nop 1\n" \
  "s_nop 1\n" \
  "s_nop 1\n" \
  "s_nop 1\n" \
  "s_nop 1\n" \
  "s_nop 1\n" \
  "s_nop 1\n" \
  "s_nop 1\n" \
  "s_nop 1\n" \
  "s_nop 1\n" \
  "s_nop 1\n" \
  "s_nop 1\n" \
  "s_nop 1\n" \
  "s_nop 1\n" \
  "s_nop 1\n" \
  "s_nop 1\n" \
  "s_nop 1\n" \
  "s_nop 1\n" \
  "s_nop 1\n" \
  "s_nop 1\n" \
  "s_nop 1\n" \
  "s_nop 1\n" \
  "s_nop 1\n" \
  "s_nop 1\n" \
  "s_nop 1\n" \
  "s_nop 1\n" \
  "s_nop 1\n" \
  "s_nop 1\n" \
  ".LX_noclk_%=:\n" \
  "buffer_load_dwordx4 v240, s[40:43], s59 offen sc1 lds\n" \
  "s_memtime s[66:67]\n" \
  "buffer_load_dwordx4 v240, s[40:43], s59 offen offset:1024 sc1 lds\n" \
  "buffer_load_dwordx4 v240, s[40:43], s59 offen offset:2048 sc1 lds\n" \
  "buffer_load_dwordx4 v240, s[40:43], s59 offen offset:3072 sc1 lds\n" \
  "s_add_u32 s60, s59, 32768\n" \
  "s_movk_i32 m0, 4096\n" \
  "s_nop 0\n" \
  "buffer_load_dwordx4 v240, s[40:43], s60 offen sc1 lds\n" \
  "buffer_load_dwordx4 v240, s[40:43], s60 offen offset:1024 sc1 lds\n" \
  "buffer_load_dwordx4 v240, s[40:43], s60 offen offset:2048 sc1 lds\n" \
  "buffer_load_dwordx4 v240, s[40:43], s60 offen offset:3072 sc1 lds\n" \
  "s_add_u32 s60, s59, 65536\n" \
  "s_movk_i32 m0, 8192\n" \
  "s_nop 0\n" \
  "buffer_load_dwordx4 v240, s[40:43], s60 offen sc1 lds\n" \
  ".LX_g3_%=:\n" \
  "s_waitcnt vmcnt(6)\n" \
  "s_barrier\n" \
  ".LX_p_%=:\n" \
  "ds_read_b128 v[100:103], v240\n" \
  "ds_read_b128 v[104:107], v240 offset:1024\n" \
  "ds_read_b128 v[108:111], v240 offset:2048\n" \
  "s_waitcnt lgkmcnt(2)\n" \
  "v_smfmac_f32_16x16x64_bf16 v[44:47], v[100:103], a[0:7], v239\n" \
  "s_waitcnt vmcnt(0)\n" \
  "s_barrier\n" \
  "s_waitcnt lgkmcnt(1)\n" \
  "v_smfmac_f32_16x16x64_bf16 v[44:47], v[104:107], a[8:15], v239\n" \
  "ds_read_b128 v[112:115], v240 offset:3072\n" \
  "v_smfmac_f32_16x16x64_bf16 v[48:51], v[100:103], a[72:79], v239\n" \
  "ds_read_b128 v[116:119], v240 offset:6144\n" \
  "s_waitcnt lgkmcnt(2)\n" \
  "v_smfmac_f32_16x16x64_bf16 v[44:47], v[108:111], a[16:23], v239\n" \
  "ds_read_b128 v[120:123], v240 offset:4096\n" \
  "v_smfmac_f32_16x16x64_bf16 v[48:51], v[104:107], a[80:87], v239\n" \
  "ds_read_b128 v[124:127], v240 offset:5120\n" \
  "v_smfmac_f32_16x16x64_bf16 v[48:51], v[108:111], a[88:95], v239\n" \
  "ds_read_b128 v[128:131], v240 offset:7168\n" \
  "s_waitcnt lgkmcnt(4)\n" \
  "v_smfmac_f32_16x16x64_bf16 v[44:47], v[112:115], a[24:31], v239\n" \
  "ds_read_b128 v[132:135], v240 offset:8192\n" \
  "v_smfmac_f32_16x16x64_bf16 v[48:51], v[112:115], a[96:103], v239\n" \
  "s_waitcnt lgkmcnt(3)\n" \
  "v_smfmac_f32_16x16x64_bf16 v[44:47], v[120:123], a[32:39], v239\n" \
  "s_waitcnt lgkmcnt(2)\n" \
  "v_smfmac_f32_16x16x64_bf16 v[44:47], v[124:127], a[40:47], v239\n" \
  "v_smfmac_f32_16x16x64_bf16 v[44:47], v[116:119], a[48:55], v239\n" \
  "s_waitcnt lgkmcnt(1)\n" \
  "v_smfmac_f32_16x16x64_bf16 v[44:47], v[128:131], a[56:63], v239\n" \
  "s_waitcnt lgkmcnt(0)\n" \
  "v_smfmac_f32_16x16x64_bf16 v[44:47], v[132:135], a[64:71], v239\n" \
  "s_nop 2\n" \
  "v_smfmac_f32_16x16x64_bf16 v[48:51], v[120:123], a[104:111], v239\n" \
  "s_nop 2\n" \
  "v_smfmac_f32_16x16x64_bf16 v[48:51], v[124:127], a[112:119], v239\n" \
  "v_add_f32 v45, v45, v47\n" \
  "v_smfmac_f32_16x16x64_bf16 v[48:51], v[116:119], a[120:127], v239\n" \
  "v_add_f32 v44, v44, v46\n" \
  "v_cmp_u_f32 vcc, v44, v45\n" \
  "v_smfmac_f32_16x16x64_bf16 v[48:51], v[128:131], a[128:135], v239\n" \
  "v_mul_f32 v45, s64, v45\n" \
  "v_smfmac_f32_16x16x64_bf16 v[48:51], v[132:135], a[136:143], v239\n" \
  "s_cbranch_vccnz .LX_slow_%=\n" \
  ".LX_back_%=:\n" \
  "s_cmp_eq_u32 s48, 63\n" \
  "v_mul_f32 v44, s64, v44\n" \
  "v_cvt_pk_bf16_f32 v44, v44, v45\n" \
  "v_smfmac_f32_16x16x64_bf16 v[52:55], v[120:123], a[176:183], v239\n" \
  "ds_write_b32 v241, v44\n" \
  "s_nop 1\n" \
  "v_add_f32 v48, v48, v50\n" \
  "v_add_f32 v49, v49, v51\n" \
  "v_mul_f32 v49, s64, v49\n" \
  "v_mul_f32 v48, s64, v48\n" \
  "v_cvt_pk_bf16_f32 v48, v48, v49\n" \
  "v_smfmac_f32_16x16x64_bf16 v[52:55], v[124:127], a[184:191], v239\n" \
  "ds_write_b32 v241, v48 offset:256\n" \
  "v_smfmac_f32_16x16x64_bf16 v[52:55], v[116:119], a[192:199], v239\n" \
  "v_smfmac_f32_16x16x64_bf16 v[56:59], v[100:103], a[216:223], v239\n" \
  "s_waitcnt lgkmcnt(1)\n" \
  "s_barrier\n" \
  "v_smfmac_f32_16x16x64_bf16 v[52:55], v[128:131], a[200:207], v239\n" \
  "ds_read_b64_tr_b16 v[102:103], v243 offset:1536\n" \
  "ds_read_b64_tr_b16 v[100:101], v243 offset:1024\n" \
  "v_smfmac_f32_16x16x64_bf16 v[52:55], v[132:135], a[208:215], v239\n" \
  "ds_read_b64_tr_b16 v[138:139], v243 offset:512\n" \
  "ds_read_b64_tr_b16 v[136:137], v243\n" \
  "v_smfmac_f32_16x16x64_bf16 v[56:59], v[104:107], a[224:231], v239\n" \
  "s_waitcnt lgkmcnt(4)\n" \
  "s_barrier\n" \
  "v_smfmac_f32_16x16x64_bf16 v[56:59], v[108:111], a[232:239], v239\n" \
  "ds_read_b64_tr_b16 v[104:105], v243 offset:256\n" \
  "ds_read_b64_tr_b16 v[106:107], v243 offset:768\n" \
  "ds_read_b64_tr_b16 v[110:111], v243 offset:1792\n" \
  "v_smfmac_f32_16x16x64_bf16 v[56:59], v[112:115], a[240:247], v239\n" \
  "ds_read_b64_tr_b16 v[108:109], v243 offset:1280\n" \
  "s_waitcnt lgkmcnt(4)\n" \
  "v_mfma_f32_16x16x32_bf16 v[112:115], v[64:67], v[136:139], 0\n" \
  "v_add_f32 v52, v52, v54\n" \
  "v_mfma_f32_16x16x32_bf16 v[112:115], v[68:71], v[100:103], v[112:115]\n" \
  "s_waitcnt lgkmcnt(2)\n" \
  "v_mfma_f32_16x16x32_bf16 v[112:115], v[72:75], v[104:107], v[112:115]\n" \
  "s_waitcnt lgkmcnt(0)\n" \
  "v_mfma_f32_16x16x32_bf16 v[112:115], v[76:79], v[108:111], v[112:115]\n" \
  "s_nop 1\n" \
  "v_add_f32 v53, v53, v55\n" \
  "v_smfmac_f32_16x16x64_bf16 v[56:59], v[120:123], a[248:255], v239\n" \
  "s_barrier\n" \
  "ds_read_b64 v[188:189], v255\n" \
  "s_nop 1\n" \
  "v_exp_f32 v100, v112\n" \
  "v_exp_f32 v101, v113\n" \
  "v_exp_f32 v103, v115\n" \
  "v_exp_f32 v102, v114\n" \
  "v_cvt_pk_bf16_f32 v186, v100, v101\n" \
  "v_cvt_pk_bf16_f32 v187, v102, v103\n" \
  "v_smfmac_f32_16x16x64_bf16 v[56:59], v[124:127], v[0:7], v239\n" \
  "buffer_store_dwordx2 v[186:187], v246, s[40:43], 0 offen sc1\n" \
  "buffer_store_dwordx2 v[186:187], v247, s[40:43], 0 offen sc1\n" \
  "v_mfma_f32_16x16x16_bf16 v[100:103], v[182:183], v[186:187], 0\n" \
  "s_nop 0\n" \
  "s_waitcnt lgkmcnt(0)\n" \
  "v_add_f32 v53, v53, v189\n" \
  "v_smfmac_f32_16x16x64_bf16 v[56:59], v[116:119], v[8:15], v239\n" \
  "v_add_f32 v52, v52, v188\n" \
  "v_smfmac_f32_16x16x64_bf16 v[56:59], v[128:131], v[16:23], v239\n" \
  "v_cvt_pk_bf16_f32 v52, v52, v53\n" \
  "v_smfmac_f32_16x16x64_bf16 v[56:59], v[132:135], v[24:31], v239\n" \
  "ds_write_b32 v248, v100\n" \
  "ds_write_b32 v242, v52\n" \
  "s_nop 5\n" \
  "v_add_f32 v57, v57, v59\n" \
  "v_add_f32 v56, v56, v58\n" \
  "v_cvt_pk_bf16_f32 v56, v56, v57\n" \
  "ds_write_b32 v242, v56 offset:256\n" \
  "s_waitcnt lgkmcnt(1)\n" \
  "s_barrier\n" \
  "s_waitcnt lgkmcnt(0)\n" \
  "s_barrier\n" \
  "s_cbranch_scc1 .LX_xr_%=\n" \
  "s_memrealtime s[98:99]\n" \
  "s_memtime s[84:85]\n" \
  ".LX_loop_%=:\n" \
  "s_cmp_ge_u32 s48, 55\n" \
  "s_cbranch_scc1 .LX_xl_%=\n" \
  ".LX_xr_b_%=:\n" \
  "s_add_u32 s95, s48, 1\n" \
  "s_and_b32 s63, s95, 3\n" \
  "s_lshl_b32 s95, s95, 5\n" \
  "s_lshl_b32 s63, s63, 4\n" \
  "s_sub_u32 s95, s95, s63\n" \
  "s_add_u32 s95, s95, s45\n" \
  "s_mov_b64 exec, s[88:89]\n" \
  "buffer_load_dwordx4 v[36:39], off, s[40:43], s95 sc1\n" \
  "s_mov_b64 exec, -1\n" \
  "v_and_b32 v246, 3221225471, v246\n" \
  "v_and_b32 v247, 3221225471, v247\n" \
  "s_waitcnt lgkmcnt(0)\n" \
  "s_lshl_b32 s60, s66, 10\n" \
  "s_or_b32 s60, s60, s87\n" \
  "s_cmp_lg_u32 s68, 0\n" \
  "s_cselect_b32 s60, s60, 0\n" \
  "v_mov_b32 v187, s60\n" \
  "s_sub_u32 s76, s84, s90\n" \
  "s_sub_u32 s77, s98, s86\n" \
  "v_cvt_f32_u32 v188, s76\n" \
  "v_cvt_f32_u32 v189, s77\n" \
  "v_rcp_f32 v189, v189\n" \
  "v_mul_f32 v188, 16.0, v188\n" \
  "v_fma_f32 v188, v188, v189, 0.5\n" \
  "v_cvt_u32_f32 v188, v188\n" \
  "s_nop 0\n" \
  "v_readfirstlane_b32 s76, v188\n" \
  "s_max_u32 s76, s76, 160\n" \
  "s_min_u32 s76, s76, 400\n" \
  "s_add_u32 s77, s48, 0\n" \
  "s_and_b32 s77, s77, 3\n" \
  "s_cselect_b32 s87, s87, s76\n" \
  "s_cselect_b32 s86, s86, s98\n" \
  "s_cselect_b32 s90, s90, s84\n" \
  "s_add_u32 s48, s48, 1\n" \
  "ds_read_b64 v[64:65], v178\n" \
  "ds_read_b64 v[66:67], v178 offset:2048\n" \
  "ds_read_b64 v[68:69], v178 offset:4096\n" \
  "ds_read_b64 v[70:71], v178 offset:6144\n" \
  "ds_read_b64 v[72:73], v178 offset:8\n" \
  "ds_read_b64 v[74:75], v178 offset:2056\n" \
  "ds_read_b64 v[76:77], v178 offset:4104\n" \
  "ds_read_b64 v[78:79], v178 offset:6152\n" \
  "v_xor_b32 v178, 8192, v178\n" \
  "v_mov_b64 v[44:45], 0\n" \
  "v_mov_b64 v[46:47], 0\n" \
  "v_mov_b64 v[48:49], 0\n" \
  "v_mov_b64 v[50:51], 0\n" \
  "v_mov_b64 v[52:53], 0\n" \
  "v_mov_b64 v[54:55], 0\n" \
  "v_mov_b64 v[56:57], 0\n" \
  "v_mov_b64 v[58:59], 0\n" \
  "v_mov_b32 v40, s48\n" \
  "s_waitcnt vmcnt(0)\n" \
  "ds_write2_b32 v184, v40, v187 offset1:2\n" \
  "s_cmp_eq_u32 s48, 58\n" \
  "s_cbranch_scc1 .LX_ef_%=\n" \
  ".LX_ef_b_%=:\n" \
  "s_branch .LX_rot_%=\n" \
  "s_nop 7\n" \
  ".LX_slow_%=:\n" \
  "s_bitcmp1_b32 s70, 0\n" \
  "s_cbranch_scc1 .LX_back_%=\n" \
  "s_waitcnt lgkmcnt(0)\n" \
  "s_cmp_lt_u32 s70, 10\n" \
  "s_nop 3\n" \
  "s_cbranch_scc1 .LX_nol_%=\n" \
  "v_max_u32 v189, v100, v101\n" \
  "v_max3_u32 v189, v189, v102, v103\n" \
  "v_max3_u32 v189, v189, v104, v105\n" \
  "v_max3_u32 v189, v189, v106, v107\n" \
  "v_max3_u32 v189, v189, v108, v109\n" \
  "v_max3_u32 v189, v189, v110, v111\n" \
  "v_max3_u32 v189, v189, v112, v113\n" \
  "v_max3_u32 v189, v189, v114, v115\n" \
  "v_max3_u32 v189, v189, v120, v121\n" \
  "v_max3_u32 v189, v189, v122, v123\n" \
  "v_max3_u32 v189, v189, v124, v125\n" \
  "v_max3_u32 v189, v189, v126, v127\n" \
  "v_max3_u32 v189, v189, v116, v117\n" \
  "v_max3_u32 v189, v189, v118, v119\n" \
  "v_max3_u32 v189, v189, v128, v129\n" \
  "v_max3_u32 v189, v189, v130, v131\n" \
  "v_max3_u32 v189, v189, v132, v133\n" \
  "v_max3_u32 v189, v189, v134, v135\n" \
  "v_cmp_eq_u32 vcc, -1, v189\n" \
  "s_cbranch_vccz .LX_back_%=\n" \
  ".LX_nol_%=:\n" \
  "s_add_u32 s70, s70, 2\n" \
  "s_cmp_gt_u32 s70, 65536\n" \
  "s_cbranch_scc1 .LX_dset_%=\n" \
  "s_barrier\n" \
  "v_mov_b64 v[44:45], 0\n" \
  "v_mov_b64 v[46:47], 0\n" \
  "v_mov_b64 v[48:49], 0\n" \
  "v_mov_b64 v[50:51], 0\n" \
  "v_mov_b64 v[52:53], 0\n" \
  "v_mov_b64 v[54:55], 0\n" \
  "v_mov_b64 v[56:57], 0\n" \
  "v_mov_b64 v[58:59], 0\n" \
  "s_cmp_lg_u32 %28, 0\n" \
  "s_cbranch_scc1 .LX_g3_%=\n" \
  "s_and_b32 s62, %40, 0xffff\n" \
  "s_add_u32 s44, s44, s62\n" \
  "s_xor_b32 s59, s59, 0\n" \
  "s_mov_b32 s68, 0\n" \
  "s_mov_b32 m0, 0\n" \
  "s_branch .LX_rt_%=\n" \
  ".LX_dset_%=:\n" \
  "v_mov_b32 v124, 1\n" \
  "v_mov_b32 v125, s55\n" \
  "buffer_store_dwordx2 v[124:125], off, s[40:43], %38 offset:128 sc1\n" \
  "s_mov_b32 s70, 1\n" \
  "v_mov_b64 v[44:45], 0\n" \
  "v_mov_b64 v[46:47], 0\n" \
  "v_mov_b64 v[48:49], 0\n" \
  "v_mov_b64 v[50:51], 0\n" \
  "v_mov_b64 v[52:53], 0\n" \
  "v_mov_b64 v[54:55], 0\n" \
  "v_mov_b64 v[56:57], 0\n" \
  "v_mov_b64 v[58:59], 0\n" \
  "s_waitcnt vmcnt(0) lgkmcnt(0)\n" \
  "s_barrier\n" \
  "s_branch .LX_p_%=\n" \
  ".LX_xr_%=:\n" \
  "s_waitcnt lgkmcnt(0)\n" \
  "s_nop 7\n" \
  "ds_read_b64_tr_b16 v[132:133], v244\n" \
  "ds_read_b64_tr_b16 v[134:135], v244 offset:256\n" \
  "ds_read_b64_tr_b16 v[136:137], v244 offset:512\n" \
  "ds_read_b64_tr_b16 v[138:139], v244 offset:768\n" \
  "ds_read_b64_tr_b16 v[140:141], v244 offset:1024\n" \
  "ds_read_b64_tr_b16 v[142:143], v244 offset:1280\n" \
  "ds_read_b64_tr_b16 v[144:145], v244 offset:1536\n" \
  "ds_read_b64_tr_b16 v[146:147], v244 offset:1792\n" \
  "s_waitcnt lgkmcnt(6)\n" \
  "v_mfma_f32_16x16x32_bf16 v[148:151], v[132:135], v[84:87], 0\n" \
  "s_waitcnt lgkmcnt(4)\n" \
  "v_mfma_f32_16x16x32_bf16 v[148:151], v[136:139], v[88:91], v[148:151]\n" \
  "s_waitcnt lgkmcnt(2)\n" \
  "v_mfma_f32_16x16x32_bf16 v[148:151], v[140:143], v[92:95], v[148:151]\n" \
  "s_waitcnt lgkmcnt(0)\n" \
  "v_mfma_f32_16x16x32_bf16 v[148:151], v[144:147], v[96:99], v[148:151]\n" \
  "s_sub_u32 s60, s48, 58\n" \
  "s_lshl_b32 s60, s60, 16\n" \
  "s_add_u32 s60, s60, s97\n" \
  "s_cmp_eq_u32 s48, 0\n" \
  "s_cselect_b32 s60, 2130706432, s60\n" \
  "s_nop 2\n" \
  "v_max_f32 v158, v148, v149\n" \
  "v_max3_f32 v158, v150, v151, v158\n" \
  "v_mov_b32 v159, v158\n" \
  "s_nop 1\n" \
  "v_permlane32_swap_b32 v158, v159\n" \
  "v_max_f32 v158, v158, v159\n" \
  "v_mul_f32 v152, s65, v158\n" \
  "v_min_f32 v152, v152, v180\n" \
  "v_fma_f32 v148, v148, s64, v152\n" \
  "v_fma_f32 v149, v149, s64, v152\n" \
  "v_fma_f32 v150, v150, s64, v152\n" \
  "v_fma_f32 v151, v151, s64, v152\n" \
  "v_sub_f32 v160, v152, v180\n" \
  "v_exp_f32 v148, v148\n" \
  "v_exp_f32 v149, v149\n" \
  "v_exp_f32 v150, v150\n" \
  "v_exp_f32 v151, v151\n" \
  "v_exp_f32 v160, v160\n" \
  "v_add_f32 v153, v148, v149\n" \
  "v_add_f32 v158, v150, v151\n" \
  "v_add_f32 v153, v153, v158\n" \
  "v_mov_b32 v159, v153\n" \
  "s_nop 1\n" \
  "v_permlane32_swap_b32 v153, v159\n" \
  "v_add_f32 v153, v153, v159\n" \
  "v_fmac_f32 v153, v160, v181\n" \
  "v_rcp_f32 v154, v153\n" \
  "s_nop 0\n" \
  "v_mul_f32 v160, v160, v154\n" \
  "v_mul_f32 v154, v154, v252\n" \
  "v_mul_f32 v148, v148, v154\n" \
  "v_mul_f32 v149, v149, v154\n" \
  "v_mul_f32 v150, v150, v154\n" \
  "v_mul_f32 v151, v151, v154\n" \
  "v_fmac_f32 v148, v160, v253\n" \
  "v_fmac_f32 v150, v160, v254\n" \
  "v_cvt_pk_bf16_f32 v156, v148, v149\n" \
  "v_cvt_pk_bf16_f32 v157, v150, v151\n" \
  "buffer_store_dwordx2 v[156:157], v251, s[40:43], s60 offen sc1\n" \
  "s_cmp_eq_u32 s48, 63\n" \
  "s_cbranch_scc1 .LX_end_%=\n" \
  ".LX_xq_%=:\n" \
  "s_add_u32 s61, s48, 1\n" \
  "s_lshl_b32 s60, s61, 19\n" \
  "s_add_u32 s60, s60, s47\n" \
  "s_lshr_b32 s62, s60, 8\n" \
  "s_and_b32 s62, s62, 8192\n" \
  "s_xor_b32 s62, s62, s60\n" \
  "s_cmp_eq_u32 s48, 0\n" \
  "s_cselect_b32 s62, 2130706432, s62\n" \
  "s_add_u32 s63, s62, 4096\n" \
  "buffer_load_dwordx4 v[84:87], v249, s[40:43], s62 offen sc1\n" \
  "buffer_load_dwordx4 v[88:91], v249, s[40:43], s62 offen offset:2048 sc1\n" \
  "buffer_load_dwordx4 v[92:95], v249, s[40:43], s63 offen sc1\n" \
  "buffer_load_dwordx4 v[96:99], v249, s[40:43], s63 offen offset:2048 sc1\n" \
  "s_sub_u32 s60, s61, 58\n" \
  "s_lshl_b32 s60, s60, 16\n" \
  "s_add_u32 s60, s60, s97\n" \
  "s_cmp_eq_u32 s48, 0\n" \
  "s_cselect_b32 s60, 2130706432, s60\n" \
  "buffer_load_dwordx2 v[180:181], v250, s[40:43], s60 offen sc1\n" \
  "s_cbranch_scc1 .LX_dry_b_%=\n" \
  "s_branch .LX_xr_b_%=\n" \
  ".LX_xl_%=:\n" \
  "s_cmp_eq_u32 s48, 57\n" \
  "s_cbranch_scc1 .LX_xq_%=\n" \
  "s_cmp_eq_u32 s48, 56\n" \
  "s_cbranch_scc1 .LX_xc_%=\n" \
  "s_cmp_lg_u32 s48, 55\n" \
  "s_cbranch_scc1 .LX_xr_%=\n" \
  "buffer_load_dwordx2 v[180:181], off, s[40:43], %42 sc1\n" \
  "s_branch .LX_xr_b_%=\n" \
  ".LX_xc_%=:\n" \
  "v_readfirstlane_b32 s63, v181\n" \
  "s_cmp_eq_u32 s63, s55\n" \
  "s_cbranch_scc1 .LX_xr_b_%=\n" \
  "s_mov_b32 s69, 0\n" \
  ".LX_xs_%=:\n" \
  "buffer_load_dwordx2 v[180:181], off, s[40:43], %42 sc1\n" \
  "s_waitcnt vmcnt(0)\n" \
  "v_readfirstlane_b32 s62, v180\n" \
  "v_readfirstlane_b32 s63, v181\n" \
  "s_cmp_eq_u32 s63, s55\n" \
  "s_cselect_b32 s62, s62, 0\n" \
  "s_cmp_lg_u32 s62, 0\n" \
  "s_cbranch_scc1 .LX_xs_x_%=\n" \
  "s_sleep 2\n" \
  "s_add_u32 s69, s69, 1\n" \
  "s_and_b32 s63, s69, 1023\n" \
  "s_cbranch_scc1 .LX_xs_%=\n" \
  "s_cmp_gt_u32 s69, 2097152\n" \
  "s_cbranch_scc1 .LX_xs_set_%=\n" \
  "buffer_load_dwordx2 v[180:181], off, s[40:43], %38 offset:128 sc1\n" \
  "s_waitcnt vmcnt(0)\n" \
  "v_readfirstlane_b32 s63, v181\n" \
  "s_cmp_eq_u32 s63, s55\n" \
  "s_cbranch_scc0 .LX_xs_%=\n" \
  "s_branch .LX_xs_x_%=\n" \
  ".LX_xs_set_%=:\n" \
  "v_mov_b32 v180, 1\n" \
  "v_mov_b32 v181, s55\n" \
  "buffer_store_dwordx2 v[180:181], off, s[40:43], %38 offset:128 sc1\n" \
  "s_waitcnt vmcnt(0)\n" \
  ".LX_xs_x_%=:\n" \
  "s_branch .LX_xr_b_%=\n" \
  ".LX_ef_%=:\n" \
  "s_waitcnt vmcnt(0) lgkmcnt(0)\n" \
  "s_branch .LX_ef_b_%=\n" \
  ".LX_tw_re_%=:\n" \
  "s_add_u32 s69, s69, 1\n" \
  "s_cmp_gt_u32 s69, 200\n" \
  "s_cbranch_scc1 .LX_noclk_%=\n" \
  "s_sleep 2\n" \
  "buffer_load_dwordx4 v[36:39], off, s[40:43], s95 sc1\n" \
  "s_waitcnt vmcnt(0)\n" \
  "s_branch .LX_tw_%=\n" \
  ".LX_lat_%=:\n" \
  "s_lshr_b32 s63, %40, 16\n" \
  "s_add_u32 s62, s62, s63\n" \
  "s_cmp_lt_u32 s62, 4294937296\n" \
  "s_cbranch_scc1 .LX_noclk_%=\n" \
  "s_and_b32 s63, %40, 0xffff\n" \
  ".LX_lat1_%=:\n" \
  "s_add_u32 s44, s44, s63\n" \
  "s_add_u32 s62, s62, s63\n" \
  "s_cbranch_scc0 .LX_lat1_%=\n" \
  "s_branch .LX_rt_%=\n" \
  ".LX_clk2_%=:\n" \
  "s_cmp_le_i32 s62, 3\n" \
  "s_cbranch_scc1 .LX_clk_done_%=\n" \
  "s_memrealtime s[60:61]\n" \
  "s_waitcnt lgkmcnt(0)\n" \
  "s_sub_u32 s62, s73, s60\n" \
  "s_branch .LX_clk2_%=\n" \
  ".LX_clk_done_%=:\n" \
  "s_branch .LX_noclk_%=\n" \
  ".LX_end_%=:\n" \
  "s_waitcnt vmcnt(0)\n" \
  "s_waitcnt vmcnt(0)\n" \
  "s_nop 7\n" \
  "s_endpgm\n" \
  ".LX_other_%=:\n"
#define X_ASM_VOPS "+{v239}"(ci.sidx), "+{v240}"(ci.l16), "+{v241}"(ci.qw), "+{v242}"(ci.kw), "+{v243}"(ci.fq), "+{v244}"(ci.fk), "+{v245}"(ci.kxo), "+{v246}"(ci.pst), "+{v247}"(ci.ps2), "+{v248}"(ci.lx), "+{v249}"(ci.qxo), "+{v250}"(ci.sto), "+{v251}"(ci.plx), "+{v252}"(ci.mk), "+{v253}"(ci.ck0), "+{v254}"(ci.ck2), "+{v255}"(ci.hx), "+{v200}"(ci.vq0), "+{v201}"(ci.vq1), "+{v202}"(ci.vq2), "+{v203}"(ci.vq3), "+{v204}"(ci.tro0), "+{v205}"(ci.tro1), "+{v206}"(ci.m0o)
#define X_ASM_SOPS "s"(ci.ws), "s"(ci.Wa), "s"(ci.Wb), "s"(ci.mem0), "s"(ci.w), "s"(ci.o_a), "s"(ci.o_b), "s"(ci.o_nx), "s"(ci.o_exp), "s"(ci.lab), "s"(ci.f_p), "s"(ci.f_m), "s"(ci.f_dur), "s"(ci.f_go), "s"(ci.o_ctl), "s"(ci.o_dbg), "s"(ci.dret), "s"(ci.o_qx), "s"(ci.f_s), "s"(ci.f_r)
#define X_ASM_CLOB "v0", "v1", "v2", "v3", "v4", "v5", "v6", "v7", "v8", "v9", "v10", "v11", "v12", "v13", "v14", "v15", "v16", "v17", "v18", "v19", "v20", "v21", "v22", "v23", "v24", "v25", "v26", "v27", "v28", "v29", "v30", "v31", "v32", "v33", "v34", "v35", "v36", "v37", "v38", "v39", "v40", "v41", "v42", "v43", "v44", "v45", "v46", "v47", "v48", "v49", "v50", "v51", "v52", "v53", "v54", "v55", "v56", "v57", "v58", "v59", "v60", "v61", "v62", "v63", "v64", "v65", "v66", "v67", "v68", "v69", "v70", "v71", "v72", "v73", "v74", "v75", "v76", "v77", "v78", "v79", "v80", "v81", "v82", "v83", "v84", "v85", "v86", "v87", "v88", "v89", "v90", "v91", "v92", "v93", "v94", "v95", "v96", "v97", "v98", "v99", "v100", "v101", "v102", "v103", "v104", "v105", "v106", "v107", "v108", "v109", "v110", "v111", "v112", "v113", "v114", "v115", "v116", "v117", "v118", "v119", "v120", "v121", "v122", "v123", "v124", "v125", "v126", "v127", "v128", "v129", "v130", "v131", "v132", "v133", "v134", "v135", "v136", "v137", "v138", "v139", "v140", "v141", "v142", "v143", "v144", "v145", "v146", "v147", "v148", "v149", "v150", "v151", "v152", "v153", "v154", "v155", "v156", "v157", "v158", "v159", "v160", "v161", "v162", "v163", "v164", "v165", "v166", "v167", "v168", "v169", "v170", "v171", "v172", "v173", "v174", "v175", "v176", "v177", "v178", "v179", "v180", "v181", "v182", "v183", "v184", "v185", "v186", "v187", "v188", "v189", "v190", "v191", "v192", "v193", "v194", "v195", "v196", "v197", "v198", "v199", "a0", "a1", "a2", "a3", "a4", "a5", "a6", "a7", "a8", "a9", "a10", "a11", "a12", "a13", "a14", "a15", "a16", "a17", "a18", "a19", "a20", "a21", "a22", "a23", "a24", "a25", "a26", "a27", "a28", "a29", "a30", "a31", "a32", "a33", "a34", "a35", "a36", "a37", "a38", "a39", "a40", "a41", "a42", "a43", "a44", "a45", "a46", "a47", "a48", "a49", "a50", "a51", "a52", "a53", "a54", "a55", "a56", "a57", "a58", "a59", "a60", "a61", "a62", "a63", "a64", "a65", "a66", "a67", "a68", "a69", "a70", "a71", "a72", "a73", "a74", "a75", "a76", "a77", "a78", "a79", "a80", "a81", "a82", "a83", "a84", "a85", "a86", "a87", "a88", "a89", "a90", "a91", "a92", "a93", "a94", "a95", "a96", "a97", "a98", "a99", "a100", "a101", "a102", "a103", "a104", "a105", "a106", "a107", "a108", "a109", "a110", "a111", "a112", "a113", "a114", "a115", "a116", "a117", "a118", "a119", "a120", "a121", "a122", "a123", "a124", "a125", "a126", "a127", "a128", "a129", "a130", "a131", "a132", "a133", "a134", "a135", "a136", "a137", "a138", "a139", "a140", "a141", "a142", "a143", "a144", "a145", "a146", "a147", "a148", "a149", "a150", "a151", "a152", "a153", "a154", "a155", "a156", "a157", "a158", "a159", "a160", "a161", "a162", "a163", "a164", "a165", "a166", "a167", "a168", "a169", "a170", "a171", "a172", "a173", "a174", "a175", "a176", "a177", "a178", "a179", "a180", "a181", "a182", "a183", "a184", "a185", "a186", "a187", "a188", "a189", "a190", "a191", "a192", "a193", "a194", "a195", "a196", "a197", "a198", "a199", "a200", "a201", "a202", "a203", "a204", "a205", "a206", "a207", "a208", "a209", "a210", "a211", "a212", "a213", "a214", "a215", "a216", "a217", "a218", "a219", "a220", "a221", "a222", "a223", "a224", "a225", "a226", "a227", "a228", "a229", "a230", "a231", "a232", "a233", "a234", "a235", "a236", "a237", "a238", "a239", "a240", "a241", "a242", "a243", "a244", "a245", "a246", "a247", "a248", "a249", "a250", "a251", "a252", "a253", "a254", "a255", "s40", "s41", "s42", "s43", "s44", "s45", "s46", "s47", "s48", "s49", "s50", "s51", "s52", "s53", "s54", "s55", "s56", "s57", "s58", "s59", "s60", "s61", "s62", "s63", "s64", "s65", "s66", "s67", "s68", "s69", "s70", "s71", "s72", "s73", "s74", "s75", "s76", "s77", "s78", "s79", "s80", "s81", "s82", "s83", "s84", "s85", "s86", "s87", "s88", "s89", "s90", "s91", "s92", "s93", "s94", "s95", "s96", "s97", "s98", "s99", "vcc", "scc", "memory"
#define M_ASM_TEXT \
  "s_mov_b64 s[40:41], %24\n" \
  "s_and_b32 s41, s41, 0xffff\n" \
  "s_mov_b32 s42, 255852544\n" \
  "s_mov_b32 s43, 0x00020000\n" \
  "s_mov_b32 s46, 4718592\n" \
  "s_mov_b32 s47, 0x00020000\n" \
  "s_lshl_b32 s72, %28, 11\n" \
  "buffer_load_dword v196, off, s[40:43], %38 sc1\n" \
  "s_mov_b64 s[44:45], %25\n" \
  "s_and_b32 s45, s45, 0xffff\n" \
  "s_add_u32 s60, %29, 0\n" \
  "s_lshl_b32 s61, s72, 1\n" \
  "s_add_u32 m0, s61, 16384\n" \
  "s_nop 0\n" \
  "buffer_load_dwordx4 v200, s[44:47], s60 offen lds\n" \
  "buffer_load_dwordx4 v201, s[44:47], s60 offen offset:1024 lds\n" \
  "buffer_load_dwordx4 v202, s[44:47], s60 offen offset:2048 lds\n" \
  "buffer_load_dwordx4 v203, s[44:47], s60 offen offset:3072 lds\n" \
  "s_add_u32 s60, %29, 524288\n" \
  "s_lshl_b32 s61, s72, 1\n" \
  "s_add_u32 m0, s61, 32768\n" \
  "s_nop 0\n" \
  "buffer_load_dwordx4 v200, s[44:47], s60 offen lds\n" \
  "buffer_load_dwordx4 v201, s[44:47], s60 offen offset:1024 lds\n" \
  "buffer_load_dwordx4 v202, s[44:47], s60 offen offset:2048 lds\n" \
  "buffer_load_dwordx4 v203, s[44:47], s60 offen offset:3072 lds\n" \
  "s_add_u32 s60, %29, 1048576\n" \
  "s_lshl_b32 s61, s72, 1\n" \
  "s_add_u32 m0, s61, 49152\n" \
  "s_nop 0\n" \
  "buffer_load_dwordx4 v200, s[44:47], s60 offen lds\n" \
  "buffer_load_dwordx4 v201, s[44:47], s60 offen offset:1024 lds\n" \
  "buffer_load_dwordx4 v202, s[44:47], s60 offen offset:2048 lds\n" \
  "buffer_load_dwordx4 v203, s[44:47], s60 offen offset:3072 lds\n" \
  "s_add_u32 s60, %29, 1572864\n" \
  "s_lshl_b32 s61, s72, 1\n" \
  "s_add_u32 m0, s61, 65536\n" \
  "s_nop 0\n" \
  "buffer_load_dwordx4 v200, s[44:47], s60 offen lds\n" \
  "buffer_load_dwordx4 v201, s[44:47], s60 offen offset:1024 lds\n" \
  "buffer_load_dwordx4 v202, s[44:47], s60 offen offset:2048 lds\n" \
  "buffer_load_dwordx4 v203, s[44:47], s60 offen offset:3072 lds\n" \
  "s_add_u32 s60, %29, 2097152\n" \
  "s_lshl_b32 s61, s72, 1\n" \
  "s_add_u32 m0, s61, 81920\n" \
  "s_nop 0\n" \
  "buffer_load_dwordx4 v200, s[44:47], s60 offen lds\n" \
  "buffer_load_dwordx4 v201, s[44:47], s60 offen offset:1024 lds\n" \
  "buffer_load_dwordx4 v202, s[44:47], s60 offen offset:2048 lds\n" \
  "buffer_load_dwordx4 v203, s[44:47], s60 offen offset:3072 lds\n" \
  "s_add_u32 s60, %29, 2621440\n" \
  "s_lshl_b32 s61, s72, 1\n" \
  "s_add_u32 m0, s61, 98304\n" \
  "s_nop 0\n" \
  "buffer_load_dwordx4 v200, s[44:47], s60 offen lds\n" \
  "buffer_load_dwordx4 v201, s[44:47], s60 offen offset:1024 lds\n" \
  "buffer_load_dwordx4 v202, s[44:47], s60 offen offset:2048 lds\n" \
  "buffer_load_dwordx4 v203, s[44:47], s60 offen offset:3072 lds\n" \
  "s_add_u32 s60, %29, 3145728\n" \
  "s_lshl_b32 s61, s72, 1\n" \
  "s_add_u32 m0, s61, 114688\n" \
  "s_nop 0\n" \
  "buffer_load_dwordx4 v200, s[44:47], s60 offen lds\n" \
  "buffer_load_dwordx4 v201, s[44:47], s60 offen offset:1024 lds\n" \
  "buffer_load_dwordx4 v202, s[44:47], s60 offen offset:2048 lds\n" \
  "buffer_load_dwordx4 v203, s[44:47], s60 offen offset:3072 lds\n" \
  "s_add_u32 s60, %29, 3670016\n" \
  "s_lshl_b32 s61, s72, 1\n" \
  "s_add_u32 m0, s61, 131072\n" \
  "s_nop 0\n" \
  "buffer_load_dwordx4 v200, s[44:47], s60 offen lds\n" \
  "buffer_load_dwordx4 v201, s[44:47], s60 offen offset:1024 lds\n" \
  "buffer_load_dwordx4 v202, s[44:47], s60 offen offset:2048 lds\n" \
  "buffer_load_dwordx4 v203, s[44:47], s60 offen offset:3072 lds\n" \
  "s_add_u32 s60, %29, 4194304\n" \
  "s_lshl_b32 s61, s72, 1\n" \
  "s_add_u32 m0, s61, 147456\n" \
  "s_nop 0\n" \
  "buffer_load_dwordx4 v200, s[44:47], s60 offen lds\n" \
  "buffer_load_dwordx4 v201, s[44:47], s60 offen offset:1024 lds\n" \
  "buffer_load_dwordx4 v202, s[44:47], s60 offen offset:2048 lds\n" \
  "buffer_load_dwordx4 v203, s[44:47], s60 offen offset:3072 lds\n" \
  "s_mov_b64 s[44:45], %27\n" \
  "s_and_b32 s45, s45, 0xffff\n" \
  "s_lshr_b32 s60, s72, 3\n" \
  "s_mov_b32 m0, s72\n" \
  "s_nop 0\n" \
  "buffer_load_dwordx4 v206, s[44:47], s60 offen lds\n" \
  "s_add_u32 s60, s60, 128\n" \
  "s_add_u32 m0, s72, 1024\n" \
  "s_nop 0\n" \
  "buffer_load_dwordx4 v206, s[44:47], s60 offen lds\n" \
  "s_movk_i32 s60, 1024\n" \
  "s_movk_i32 m0, 8192\n" \
  "s_nop 0\n" \
  "buffer_load_dwordx4 v206, s[44:47], s60 offen lds\n" \
  "s_waitcnt vmcnt(27)\n" \
  "s_barrier\n" \
  "v_add_u32 v194, 16384, v204\n" \
  "ds_read_b64_tr_b16 a[0:1], v194\n" \
  "ds_read_b64_tr_b16 a[2:3], v194 offset:512\n" \
  "ds_read_b64_tr_b16 a[4:5], v194 offset:8192\n" \
  "ds_read_b64_tr_b16 a[6:7], v194 offset:8704\n" \
  "v_add_u32 v195, 16384, v205\n" \
  "ds_read_b64_tr_b16 a[72:73], v195\n" \
  "ds_read_b64_tr_b16 a[74:75], v195 offset:512\n" \
  "ds_read_b64_tr_b16 a[76:77], v195 offset:8192\n" \
  "ds_read_b64_tr_b16 a[78:79], v195 offset:8704\n" \
  "v_add_u32 v194, 32768, v204\n" \
  "ds_read_b64_tr_b16 a[8:9], v194\n" \
  "ds_read_b64_tr_b16 a[10:11], v194 offset:512\n" \
  "ds_read_b64_tr_b16 a[12:13], v194 offset:8192\n" \
  "ds_read_b64_tr_b16 a[14:15], v194 offset:8704\n" \
  "v_add_u32 v195, 32768, v205\n" \
  "ds_read_b64_tr_b16 a[80:81], v195\n" \
  "ds_read_b64_tr_b16 a[82:83], v195 offset:512\n" \
  "ds_read_b64_tr_b16 a[84:85], v195 offset:8192\n" \
  "ds_read_b64_tr_b16 a[86:87], v195 offset:8704\n" \
  "v_add_u32 v194, 49152, v204\n" \
  "ds_read_b64_tr_b16 a[16:17], v194\n" \
  "ds_read_b64_tr_b16 a[18:19], v194 offset:512\n" \
  "ds_read_b64_tr_b16 a[20:21], v194 offset:8192\n" \
  "ds_read_b64_tr_b16 a[22:23], v194 offset:8704\n" \
  "v_add_u32 v195, 49152, v205\n" \
  "ds_read_b64_tr_b16 a[88:89], v195\n" \
  "ds_read_b64_tr_b16 a[90:91], v195 offset:512\n" \
  "ds_read_b64_tr_b16 a[92:93], v195 offset:8192\n" \
  "ds_read_b64_tr_b16 a[94:95], v195 offset:8704\n" \
  "s_waitcnt lgkmcnt(0)\n" \
  "s_barrier\n" \
  "s_mov_b64 s[44:45], %26\n" \
  "s_and_b32 s45, s45, 0xffff\n" \
  "s_add_u32 s60, %29, 0\n" \
  "s_lshl_b32 s61, s72, 1\n" \
  "s_add_u32 m0, s61, 16384\n" \
  "s_nop 0\n" \
  "buffer_load_dwordx4 v200, s[44:47], s60 offen lds\n" \
  "buffer_load_dwordx4 v201, s[44:47], s60 offen offset:1024 lds\n" \
  "buffer_load_dwordx4 v202, s[44:47], s60 offen offset:2048 lds\n" \
  "buffer_load_dwordx4 v203, s[44:47], s60 offen offset:3072 lds\n" \
  "s_add_u32 s60, %29, 524288\n" \
  "s_lshl_b32 s61, s72, 1\n" \
  "s_add_u32 m0, s61, 32768\n" \
  "s_nop 0\n" \
  "buffer_load_dwordx4 v200, s[44:47], s60 offen lds\n" \
  "buffer_load_dwordx4 v201, s[44:47], s60 offen offset:1024 lds\n" \
  "buffer_load_dwordx4 v202, s[44:47], s60 offen offset:2048 lds\n" \
  "buffer_load_dwordx4 v203, s[44:47], s60 offen offset:3072 lds\n" \
  "s_add_u32 s60, %29, 1048576\n" \
  "s_lshl_b32 s61, s72, 1\n" \
  "s_add_u32 m0, s61, 49152\n" \
  "s_nop 0\n" \
  "buffer_load_dwordx4 v200, s[44:47], s60 offen lds\n" \
  "buffer_load_dwordx4 v201, s[44:47], s60 offen offset:1024 lds\n" \
  "buffer_load_dwordx4 v202, s[44:47], s60 offen offset:2048 lds\n" \
  "buffer_load_dwordx4 v203, s[44:47], s60 offen offset:3072 lds\n" \
  "v_readfirstlane_b32 s55, v196\n" \
  "s_add_u32 s55, s55, 1\n" \
  "s_waitcnt vmcnt(27)\n" \
  "s_barrier\n" \
  "v_add_u32 v194, 65536, v204\n" \
  "ds_read_b64_tr_b16 a[24:25], v194\n" \
  "ds_read_b64_tr_b16 a[26:27], v194 offset:512\n" \
  "ds_read_b64_tr_b16 a[28:29], v194 offset:8192\n" \
  "ds_read_b64_tr_b16 a[30:31], v194 offset:8704\n" \
  "v_add_u32 v195, 65536, v205\n" \
  "ds_read_b64_tr_b16 a[96:97], v195\n" \
  "ds_read_b64_tr_b16 a[98:99], v195 offset:512\n" \
  "ds_read_b64_tr_b16 a[100:101], v195 offset:8192\n" \
  "ds_read_b64_tr_b16 a[102:103], v195 offset:8704\n" \
  "v_add_u32 v194, 81920, v204\n" \
  "ds_read_b64_tr_b16 a[32:33], v194\n" \
  "ds_read_b64_tr_b16 a[34:35], v194 offset:512\n" \
  "ds_read_b64_tr_b16 a[36:37], v194 offset:8192\n" \
  "ds_read_b64_tr_b16 a[38:39], v194 offset:8704\n" \
  "v_add_u32 v195, 81920, v205\n" \
  "ds_read_b64_tr_b16 a[104:105], v195\n" \
  "ds_read_b64_tr_b16 a[106:107], v195 offset:512\n" \
  "ds_read_b64_tr_b16 a[108:109], v195 offset:8192\n" \
  "ds_read_b64_tr_b16 a[110:111], v195 offset:8704\n" \
  "v_add_u32 v194, 98304, v204\n" \
  "ds_read_b64_tr_b16 a[40:41], v194\n" \
  "ds_read_b64_tr_b16 a[42:43], v194 offset:512\n" \
  "ds_read_b64_tr_b16 a[44:45], v194 offset:8192\n" \
  "ds_read_b64_tr_b16 a[46:47], v194 offset:8704\n" \
  "v_add_u32 v195, 98304, v205\n" \
  "ds_read_b64_tr_b16 a[112:113], v195\n" \
  "ds_read_b64_tr_b16 a[114:115], v195 offset:512\n" \
  "ds_read_b64_tr_b16 a[116:117], v195 offset:8192\n" \
  "ds_read_b64_tr_b16 a[118:119], v195 offset:8704\n" \
  "s_waitcnt lgkmcnt(0)\n" \
  "s_barrier\n" \
  "s_add_u32 s60, %29, 1572864\n" \
  "s_lshl_b32 s61, s72, 1\n" \
  "s_add_u32 m0, s61, 65536\n" \
  "s_nop 0\n" \
  "buffer_load_dwordx4 v200, s[44:47], s60 offen lds\n" \
  "buffer_load_dwordx4 v201, s[44:47], s60 offen offset:1024 lds\n" \
  "buffer_load_dwordx4 v202, s[44:47], s60 offen offset:2048 lds\n" \
  "buffer_load_dwordx4 v203, s[44:47], s60 offen offset:3072 lds\n" \
  "s_add_u32 s60, %29, 2097152\n" \
  "s_lshl_b32 s61, s72, 1\n" \
  "s_add_u32 m0, s61, 81920\n" \
  "s_nop 0\n" \
  "buffer_load_dwordx4 v200, s[44:47], s60 offen lds\n" \
  "buffer_load_dwordx4 v201, s[44:47], s60 offen offset:1024 lds\n" \
  "buffer_load_dwordx4 v202, s[44:47], s60 offen offset:2048 lds\n" \
  "buffer_load_dwordx4 v203, s[44:47], s60 offen offset:3072 lds\n" \
  "s_add_u32 s60, %29, 2621440\n" \
  "s_lshl_b32 s61, s72, 1\n" \
  "s_add_u32 m0, s61, 98304\n" \
  "s_nop 0\n" \
  "buffer_load_dwordx4 v200, s[44:47], s60 offen lds\n" \
  "buffer_load_dwordx4 v201, s[44:47], s60 offen offset:1024 lds\n" \
  "buffer_load_dwordx4 v202, s[44:47], s60 offen offset:2048 lds\n" \
  "buffer_load_dwordx4 v203, s[44:47], s60 offen offset:3072 lds\n" \
  "s_waitcnt vmcnt(27)\n" \
  "s_barrier\n" \
  "v_add_u32 v194, 114688, v204\n" \
  "ds_read_b64_tr_b16 a[48:49], v194\n" \
  "ds_read_b64_tr_b16 a[50:51], v194 offset:512\n" \
  "ds_read_b64_tr_b16 a[52:53], v194 offset:8192\n" \
  "ds_read_b64_tr_b16 a[54:55], v194 offset:8704\n" \
  "v_add_u32 v195, 114688, v205\n" \
  "ds_read_b64_tr_b16 a[120:121], v195\n" \
  "ds_read_b64_tr_b16 a[122:123], v195 offset:512\n" \
  "ds_read_b64_tr_b16 a[124:125], v195 offset:8192\n" \
  "ds_read_b64_tr_b16 a[126:127], v195 offset:8704\n" \
  "v_add_u32 v194, 131072, v204\n" \
  "ds_read_b64_tr_b16 a[56:57], v194\n" \
  "ds_read_b64_tr_b16 a[58:59], v194 offset:512\n" \
  "ds_read_b64_tr_b16 a[60:61], v194 offset:8192\n" \
  "ds_read_b64_tr_b16 a[62:63], v194 offset:8704\n" \
  "v_add_u32 v195, 131072, v205\n" \
  "ds_read_b64_tr_b16 a[128:129], v195\n" \
  "ds_read_b64_tr_b16 a[130:131], v195 offset:512\n" \
  "ds_read_b64_tr_b16 a[132:133], v195 offset:8192\n" \
  "ds_read_b64_tr_b16 a[134:135], v195 offset:8704\n" \
  "v_add_u32 v194, 147456, v204\n" \
  "ds_read_b64_tr_b16 a[64:65], v194\n" \
  "ds_read_b64_tr_b16 a[66:67], v194 offset:512\n" \
  "ds_read_b64_tr_b16 a[68:69], v194 offset:8192\n" \
  "ds_read_b64_tr_b16 a[70:71], v194 offset:8704\n" \
  "v_add_u32 v195, 147456, v205\n" \
  "ds_read_b64_tr_b16 a[136:137], v195\n" \
  "ds_read_b64_tr_b16 a[138:139], v195 offset:512\n" \
  "ds_read_b64_tr_b16 a[140:141], v195 offset:8192\n" \
  "ds_read_b64_tr_b16 a[142:143], v195 offset:8704\n" \
  "s_waitcnt lgkmcnt(0)\n" \
  "s_barrier\n" \
  "s_add_u32 s60, %29, 3145728\n" \
  "s_lshl_b32 s61, s72, 1\n" \
  "s_add_u32 m0, s61, 114688\n" \
  "s_nop 0\n" \
  "buffer_load_dwordx4 v200, s[44:47], s60 offen lds\n" \
  "buffer_load_dwordx4 v201, s[44:47], s60 offen offset:1024 lds\n" \
  "buffer_load_dwordx4 v202, s[44:47], s60 offen offset:2048 lds\n" \
  "buffer_load_dwordx4 v203, s[44:47], s60 offen offset:3072 lds\n" \
  "s_add_u32 s60, %29, 3670016\n" \
  "s_lshl_b32 s61, s72, 1\n" \
  "s_add_u32 m0, s61, 131072\n" \
  "s_nop 0\n" \
  "buffer_load_dwordx4 v200, s[44:47], s60 offen lds\n" \
  "buffer_load_dwordx4 v201, s[44:47], s60 offen offset:1024 lds\n" \
  "buffer_load_dwordx4 v202, s[44:47], s60 offen offset:2048 lds\n" \
  "buffer_load_dwordx4 v203, s[44:47], s60 offen offset:3072 lds\n" \
  "s_add_u32 s60, %29, 4194304\n" \
  "s_lshl_b32 s61, s72, 1\n" \
  "s_add_u32 m0, s61, 147456\n" \
  "s_nop 0\n" \
  "buffer_load_dwordx4 v200, s[44:47], s60 offen lds\n" \
  "buffer_load_dwordx4 v201, s[44:47], s60 offen offset:1024 lds\n" \
  "buffer_load_dwordx4 v202, s[44:47], s60 offen offset:2048 lds\n" \
  "buffer_load_dwordx4 v203, s[44:47], s60 offen offset:3072 lds\n" \
  "buffer_load_dwordx2 v[198:199], off, s[40:43], %37 sc1\n" \
  "s_waitcnt vmcnt(25)\n" \
  "s_barrier\n" \
  "v_add_u32 v194, 16384, v204\n" \
  "ds_read_b64_tr_b16 a[144:145], v194\n" \
  "ds_read_b64_tr_b16 a[146:147], v194 offset:512\n" \
  "ds_read_b64_tr_b16 a[148:149], v194 offset:8192\n" \
  "ds_read_b64_tr_b16 a[150:151], v194 offset:8704\n" \
  "v_add_u32 v195, 16384, v205\n" \
  "ds_read_b64_tr_b16 a[216:217], v195\n" \
  "ds_read_b64_tr_b16 a[218:219], v195 offset:512\n" \
  "ds_read_b64_tr_b16 a[220:221], v195 offset:8192\n" \
  "ds_read_b64_tr_b16 a[222:223], v195 offset:8704\n" \
  "v_xor_b32 v195, 128, v194\n" \
  "ds_read_b64_tr_b16 v[32:33], v195\n" \
  "ds_read_b64_tr_b16 v[34:35], v195 offset:512\n" \
  "ds_read_b64_tr_b16 v[36:37], v195 offset:8192\n" \
  "ds_read_b64_tr_b16 v[38:39], v195 offset:8704\n" \
  "v_add_u32 v194, 32768, v204\n" \
  "ds_read_b64_tr_b16 a[152:153], v194\n" \
  "ds_read_b64_tr_b16 a[154:155], v194 offset:512\n" \
  "ds_read_b64_tr_b16 a[156:157], v194 offset:8192\n" \
  "ds_read_b64_tr_b16 a[158:159], v194 offset:8704\n" \
  "v_add_u32 v195, 32768, v205\n" \
  "ds_read_b64_tr_b16 a[224:225], v195\n" \
  "ds_read_b64_tr_b16 a[226:227], v195 offset:512\n" \
  "ds_read_b64_tr_b16 a[228:229], v195 offset:8192\n" \
  "ds_read_b64_tr_b16 a[230:231], v195 offset:8704\n" \
  "v_xor_b32 v195, 128, v194\n" \
  "ds_read_b64_tr_b16 v[40:41], v195\n" \
  "ds_read_b64_tr_b16 v[42:43], v195 offset:512\n" \
  "ds_read_b64_tr_b16 v[44:45], v195 offset:8192\n" \
  "ds_read_b64_tr_b16 v[46:47], v195 offset:8704\n" \
  "v_add_u32 v194, 49152, v204\n" \
  "ds_read_b64_tr_b16 a[160:161], v194\n" \
  "ds_read_b64_tr_b16 a[162:163], v194 offset:512\n" \
  "ds_read_b64_tr_b16 a[164:165], v194 offset:8192\n" \
  "ds_read_b64_tr_b16 a[166:167], v194 offset:8704\n" \
  "v_add_u32 v195, 49152, v205\n" \
  "ds_read_b64_tr_b16 a[232:233], v195\n" \
  "ds_read_b64_tr_b16 a[234:235], v195 offset:512\n" \
  "ds_read_b64_tr_b16 a[236:237], v195 offset:8192\n" \
  "ds_read_b64_tr_b16 a[238:239], v195 offset:8704\n" \
  "v_xor_b32 v195, 128, v194\n" \
  "ds_read_b64_tr_b16 v[48:49], v195\n" \
  "ds_read_b64_tr_b16 v[50:51], v195 offset:512\n" \
  "ds_read_b64_tr_b16 v[52:53], v195 offset:8192\n" \
  "ds_read_b64_tr_b16 v[54:55], v195 offset:8704\n" \
  "s_waitcnt lgkmcnt(0)\n" \
  "s_waitcnt vmcnt(13)\n" \
  "s_barrier\n" \
  "v_add_u32 v194, 65536, v204\n" \
  "ds_read_b64_tr_b16 a[168:169], v194\n" \
  "ds_read_b64_tr_b16 a[170:171], v194 offset:512\n" \
  "ds_read_b64_tr_b16 a[172:173], v194 offset:8192\n" \
  "ds_read_b64_tr_b16 a[174:175], v194 offset:8704\n" \
  "v_add_u32 v195, 65536, v205\n" \
  "ds_read_b64_tr_b16 a[240:241], v195\n" \
  "ds_read_b64_tr_b16 a[242:243], v195 offset:512\n" \
  "ds_read_b64_tr_b16 a[244:245], v195 offset:8192\n" \
  "ds_read_b64_tr_b16 a[246:247], v195 offset:8704\n" \
  "v_xor_b32 v195, 128, v194\n" \
  "ds_read_b64_tr_b16 v[56:57], v195\n" \
  "ds_read_b64_tr_b16 v[58:59], v195 offset:512\n" \
  "ds_read_b64_tr_b16 v[60:61], v195 offset:8192\n" \
  "ds_read_b64_tr_b16 v[62:63], v195 offset:8704\n" \
  "v_add_u32 v194, 81920, v204\n" \
  "ds_read_b64_tr_b16 a[176:177], v194\n" \
  "ds_read_b64_tr_b16 a[178:179], v194 offset:512\n" \
  "ds_read_b64_tr_b16 a[180:181], v194 offset:8192\n" \
  "ds_read_b64_tr_b16 a[182:183], v194 offset:8704\n" \
  "v_add_u32 v195, 81920, v205\n" \
  "ds_read_b64_tr_b16 a[248:249], v195\n" \
  "ds_read_b64_tr_b16 a[250:251], v195 offset:512\n" \
  "ds_read_b64_tr_b16 a[252:253], v195 offset:8192\n" \
  "ds_read_b64_tr_b16 a[254:255], v195 offset:8704\n" \
  "v_add_u32 v194, 98304, v204\n" \
  "ds_read_b64_tr_b16 a[184:185], v194\n" \
  "ds_read_b64_tr_b16 a[186:187], v194 offset:512\n" \
  "ds_read_b64_tr_b16 a[188:189], v194 offset:8192\n" \
  "ds_read_b64_tr_b16 a[190:191], v194 offset:8704\n" \
  "v_add_u32 v195, 98304, v205\n" \
  "ds_read_b64_tr_b16 v[0:1], v195\n" \
  "ds_read_b64_tr_b16 v[2:3], v195 offset:512\n" \
  "ds_read_b64_tr_b16 v[4:5], v195 offset:8192\n" \
  "ds_read_b64_tr_b16 v[6:7], v195 offset:8704\n" \
  "s_waitcnt lgkmcnt(0)\n" \
  "s_waitcnt vmcnt(1)\n" \
  "s_barrier\n" \
  "v_add_u32 v194, 114688, v204\n" \
  "ds_read_b64_tr_b16 a[192:193], v194\n" \
  "ds_read_b64_tr_b16 a[194:195], v194 offset:512\n" \
  "ds_read_b64_tr_b16 a[196:197], v194 offset:8192\n" \
  "ds_read_b64_tr_b16 a[198:199], v194 offset:8704\n" \
  "v_add_u32 v195, 114688, v205\n" \
  "ds_read_b64_tr_b16 v[8:9], v195\n" \
  "ds_read_b64_tr_b16 v[10:11], v195 offset:512\n" \
  "ds_read_b64_tr_b16 v[12:13], v195 offset:8192\n" \
  "ds_read_b64_tr_b16 v[14:15], v195 offset:8704\n" \
  "v_add_u32 v194, 131072, v204\n" \
  "ds_read_b64_tr_b16 a[200:201], v194\n" \
  "ds_read_b64_tr_b16 a[202:203], v194 offset:512\n" \
  "ds_read_b64_tr_b16 a[204:205], v194 offset:8192\n" \
  "ds_read_b64_tr_b16 a[206:207], v194 offset:8704\n" \
  "v_add_u32 v195, 131072, v205\n" \
  "ds_read_b64_tr_b16 v[16:17], v195\n" \
  "ds_read_b64_tr_b16 v[18:19], v195 offset:512\n" \
  "ds_read_b64_tr_b16 v[20:21], v195 offset:8192\n" \
  "ds_read_b64_tr_b16 v[22:23], v195 offset:8704\n" \
  "v_add_u32 v194, 147456, v204\n" \
  "ds_read_b64_tr_b16 a[208:209], v194\n" \
  "ds_read_b64_tr_b16 a[210:211], v194 offset:512\n" \
  "ds_read_b64_tr_b16 a[212:213], v194 offset:8192\n" \
  "ds_read_b64_tr_b16 a[214:215], v194 offset:8704\n" \
  "v_add_u32 v195, 147456, v205\n" \
  "ds_read_b64_tr_b16 v[24:25], v195\n" \
  "ds_read_b64_tr_b16 v[26:27], v195 offset:512\n" \
  "ds_read_b64_tr_b16 v[28:29], v195 offset:8192\n" \
  "ds_read_b64_tr_b16 v[30:31], v195 offset:8704\n" \
  "s_waitcnt lgkmcnt(0)\n" \
  "s_waitcnt vmcnt(0)\n" \
  "v_readfirstlane_b32 s96, v198\n" \
  "v_readfirstlane_b32 s63, v199\n" \
  "s_cmp_eq_u32 s63, s55\n" \
  "s_cselect_b32 s96, s96, 0\n" \
  "s_cmp_ge_u32 s96, 0xc0000000\n" \
  "s_cbranch_scc1 .LM_go_x_%=\n" \
  "s_mov_b32 s69, 0\n" \
  ".LM_go_%=:\n" \
  "buffer_load_dwordx2 v[196:197], off, s[40:43], %37 sc1\n" \
  "s_waitcnt vmcnt(0)\n" \
  "v_readfirstlane_b32 s96, v196\n" \
  "v_readfirstlane_b32 s63, v197\n" \
  "s_cmp_eq_u32 s63, s55\n" \
  "s_cselect_b32 s96, s96, 0\n" \
  "s_cmp_ge_u32 s96, 0xc0000000\n" \
  "s_cbranch_scc1 .LM_go_x_%=\n" \
  "s_sleep 8\n" \
  "s_add_u32 s69, s69, 1\n" \
  "s_and_b32 s63, s69, 1023\n" \
  "s_cbranch_scc1 .LM_go_%=\n" \
  "s_cmp_gt_u32 s69, 2097152\n" \
  "s_cbranch_scc1 .LM_go_set_%=\n" \
  "buffer_load_dwordx2 v[196:197], off, s[40:43], %38 offset:128 sc1\n" \
  "s_waitcnt vmcnt(0)\n" \
  "v_readfirstlane_b32 s63, v197\n" \
  "s_cmp_eq_u32 s63, s55\n" \
  "s_cbranch_scc0 .LM_go_%=\n" \
  "s_branch .LM_go_x_%=\n" \
  ".LM_go_set_%=:\n" \
  "v_mov_b32 v196, 1\n" \
  "v_mov_b32 v197, s55\n" \
  "buffer_store_dwordx2 v[196:197], off, s[40:43], %38 offset:128 sc1\n" \
  "s_waitcnt vmcnt(0)\n" \
  ".LM_go_x_%=:\n" \
  "s_and_b32 s60, s96, 2\n" \
  "s_lshl_b32 s60, s60, 12\n" \
  "s_xor_b32 s74, %34, s60\n" \
  "s_xor_b32 s75, %36, s60\n" \
  "s_bfe_u32 s56, s96, 0x30010\n" \
  "s_lshl_b32 s56, s56, 12\n" \
  "s_or_b32 s56, s56, 0x200000\n" \
  "s_mov_b32 s57, 1052672\n" \
  "s_lshl_b32 s61, %33, 2\n" \
  "s_lshr_b32 s51, s96, s61\n" \
  "s_and_b32 s51, s51, 7\n" \
  "s_lshl_b32 s51, s51, 12\n" \
  "s_xor_b32 s51, s51, 4210688\n" \
  "s_mov_b32 s71, 0\n" \
  "s_add_u32 s45, s51, 886784\n" \
  "s_mov_b32 s46, -1\n" \
  "v_mov_b64 v[64:65], -1\n" \
  "v_mov_b64 v[66:67], -1\n" \
  "v_and_b32 v194, 3, v246\n" \
  "v_lshlrev_b32 v194, 2, v194\n" \
  "v_lshrrev_b32 v194, v194, s96\n" \
  "v_and_b32 v194, 7, v194\n" \
  "v_lshlrev_b32 v194, 12, v194\n" \
  "v_and_b32 v246, -4, v246\n" \
  "v_xor_b32 v246, v246, v194\n" \
  "v_and_b32 v194, 3, v247\n" \
  "v_lshlrev_b32 v194, 2, v194\n" \
  "v_lshrrev_b32 v194, v194, s96\n" \
  "v_and_b32 v194, 7, v194\n" \
  "v_lshlrev_b32 v194, 12, v194\n" \
  "v_and_b32 v247, -4, v247\n" \
  "v_xor_b32 v247, v247, v194\n" \
  "v_xor_b32 v190, s56, v246\n" \
  "buffer_store_dwordx2 v[64:65], v190, s[40:43], 0 offen sc1\n" \
  "v_xor_b32 v191, s56, v247\n" \
  "buffer_store_dwordx2 v[64:65], v191, s[40:43], 0 offen sc1\n" \
  "buffer_store_dwordx2 v[64:65], v191, s[40:43], 0 offen offset:256 sc1\n" \
  "s_xor_b32 s62, s56, s57\n" \
  "s_nop 1\n" \
  "v_xor_b32 v190, s62, v246\n" \
  "buffer_store_dwordx2 v[64:65], v190, s[40:43], 0 offen sc1\n" \
  "v_xor_b32 v191, s62, v247\n" \
  "buffer_store_dwordx2 v[64:65], v191, s[40:43], 0 offen sc1\n" \
  "buffer_store_dwordx2 v[64:65], v191, s[40:43], 0 offen offset:256 sc1\n" \
  "s_andn2_b32 s60, 2, s96\n" \
  "s_lshl_b32 s60, s60, 12\n" \
  "s_xor_b32 s60, s60, 8192\n" \
  "s_xor_b32 s58, %31, s60\n" \
  "s_mov_b32 s49, 0\n" \
  "s_add_u32 s47, %41, s60\n" \
  "s_add_u32 s97, s60, 8388608\n" \
  "s_xor_b32 s50, %32, s60\n" \
  "s_mov_b32 s48, 0\n" \
  "s_mov_b32 s54, 0\n" \
  "s_cmp_eq_u32 %28, 0\n" \
  "s_cselect_b64 s[88:89], -1, 0\n" \
  "s_mov_b32 s68, 0\n" \
  "s_mov_b32 s70, 1\n" \
  "s_mov_b32 s73, 0\n" \
  "s_mov_b32 s44, 0\n" \
  "s_mov_b32 s53, 0\n" \
  "s_memrealtime s[86:87]\n" \
  "s_memtime s[76:77]\n" \
  "s_waitcnt lgkmcnt(0)\n" \
  "s_mov_b32 s90, s76\n" \
  "s_mov_b32 s87, 340\n" \
  "s_mov_b32 s64, 0x3e0293ee\n" \
  "s_mov_b32 s65, 0xbe0293ee\n" \
  "v_mov_b64 v[76:77], 0\n" \
  "v_mov_b64 v[78:79], 0\n" \
  "v_mov_b64 v[80:81], 0\n" \
  "v_mov_b64 v[82:83], 0\n" \
  "v_mov_b64 v[84:85], 0\n" \
  "v_mov_b64 v[86:87], 0\n" \
  "v_mov_b64 v[88:89], 0\n" \
  "v_mov_b64 v[90:91], 0\n" \
  "v_mov_b64 v[92:93], 0\n" \
  "v_mov_b64 v[94:95], 0\n" \
  "v_mov_b32 v72, 0\n" \
  "v_mov_b32 v73, s55\n" \
  "v_mov_b64 v[74:75], v[72:73]\n" \
  "s_mov_b64 s[92:93], 0\n" \
  "s_mov_b32 s92, 0xffff0000\n" \
  "s_mov_b32 s60, 8192\n" \
  "s_add_u32 s60, s60, 32768\n" \
  "v_add_u32 v176, s60, v240\n" \
  "v_mov_b32 v180, v252\n" \
  "v_mov_b64 v[96:97], 0\n" \
  "v_mov_b64 v[98:99], 0\n" \
  "v_mov_b32 v96, v253\n" \
  "v_mov_b32 v98, v254\n" \
  "s_and_b32 s60, s72, 2048\n" \
  "s_add_u32 s50, s50, s60\n" \
  "s_lshr_b32 s60, s60, 1\n" \
  "v_add_u32 v177, s60, v244\n" \
  "s_lshr_b32 s60, s72, 9\n" \
  "s_add_u32 s60, s60, 17408\n" \
  "v_mov_b32 v184, s60\n" \
  "s_mov_b32 s52, 0\n" \
  "s_barrier\n" \
  "ds_write_b32 v184, v72\n" \
  "s_branch .LM_xr_%=\n" \
  ".LM_dry_b_%=:\n" \
  "s_and_b32 s60, s72, 2048\n" \
  "s_lshl_b32 s91, s60, 1\n" \
  "s_add_u32 s91, s91, 40960\n" \
  "v_mov_b64 v[182:183], 0\n" \
  "s_mov_b32 s69, 0\n" \
  ".LM_k0_%=:\n" \
  "buffer_load_dwordx2 v[196:197], off, s[40:43], s74 sc1\n" \
  "s_waitcnt vmcnt(0)\n" \
  "v_readfirstlane_b32 s54, v196\n" \
  "v_readfirstlane_b32 s63, v197\n" \
  "s_cmp_eq_u32 s63, s55\n" \
  "s_cselect_b32 s54, s54, 0\n" \
  "s_cmp_gt_i32 s54, 1\n" \
  "s_cbranch_scc1 .LM_k0_x_%=\n" \
  "s_sleep 2\n" \
  "s_add_u32 s69, s69, 1\n" \
  "s_and_b32 s63, s69, 1023\n" \
  "s_cbranch_scc1 .LM_k0_%=\n" \
  "s_cmp_gt_u32 s69, 2097152\n" \
  "s_cbranch_scc1 .LM_k0_set_%=\n" \
  "buffer_load_dwordx2 v[196:197], off, s[40:43], %38 offset:128 sc1\n" \
  "s_waitcnt vmcnt(0)\n" \
  "v_readfirstlane_b32 s63, v197\n" \
  "s_cmp_eq_u32 s63, s55\n" \
  "s_cbranch_scc0 .LM_k0_%=\n" \
  "s_branch .LM_k0_x_%=\n" \
  ".LM_k0_set_%=:\n" \
  "v_mov_b32 v196, 1\n" \
  "v_mov_b32 v197, s55\n" \
  "buffer_store_dwordx2 v[196:197], off, s[40:43], %38 offset:128 sc1\n" \
  "s_waitcnt vmcnt(0)\n" \
  ".LM_k0_x_%=:\n" \
  "s_xor_b32 s60, s49, s58\n" \
  "s_add_u32 s49, s49, 16384\n" \
  "s_xor_b32 s91, s91, 8192\n" \
  "s_mov_b32 m0, s91\n" \
  "s_nop 0\n" \
  "buffer_load_dwordx4 v245, s[40:43], s60 offen sc1 lds\n" \
  "buffer_load_dwordx4 v245, s[40:43], s60 offen offset:1024 sc1 lds\n" \
  "buffer_load_dwordx4 v245, s[40:43], s60 offen offset:2048 sc1 lds\n" \
  "buffer_load_dwordx4 v245, s[40:43], s60 offen offset:3072 sc1 lds\n" \
  "s_xor_b32 s60, s49, s58\n" \
  "s_add_u32 s49, s49, 16384\n" \
  "s_xor_b32 s91, s91, 8192\n" \
  "s_mov_b32 m0, s91\n" \
  "s_nop 0\n" \
  "buffer_load_dwordx4 v245, s[40:43], s60 offen sc1 lds\n" \
  "buffer_load_dwordx4 v245, s[40:43], s60 offen offset:1024 sc1 lds\n" \
  "buffer_load_dwordx4 v245, s[40:43], s60 offen offset:2048 sc1 lds\n" \
  "buffer_load_dwordx4 v245, s[40:43], s60 offen offset:3072 sc1 lds\n" \
  "s_waitcnt vmcnt(0)\n" \
  "s_barrier\n" \
  "v_xor_b32 v176, 8192, v176\n" \
  "s_mov_b32 s67, 2130706432\n" \
  "s_waitcnt vmcnt(0) lgkmcnt(0)\n" \
  "s_barrier\n" \
  "s_branch .LM_p_%=\n" \
  "s_nop 7\n" \
  ".p2align 6\n" \
  ".fill (((24 - (.LM_p_%= - .LM_rot_%=)) & 63) >> 2), 4, 0xBF800000\n" \
  ".LM_rot_%=:\n" \
  "s_xor_b32 s51, s51, s56\n" \
  "s_xor_b32 s71, s71, s56\n" \
  "v_xor_b32 v246, s56, v246\n" \
  "v_xor_b32 v247, s56, v247\n" \
  "s_mov_b32 s60, s56\n" \
  "s_mov_b32 s56, s57\n" \
  "s_mov_b32 s57, s60\n" \
  "s_add_u32 s59, s51, 819200\n" \
  "s_mov_b32 m0, 0\n" \
  "s_mov_b32 s70, 0\n" \
  "s_waitcnt lgkmcnt(0)\n" \
  ".LM_rt_%=:\n" \
  "s_barrier\n" \
  ".LM_p_%=:\n" \
  "ds_read_b128 v[116:119], v240\n" \
  "ds_read_b128 v[120:123], v240 offset:1024\n" \
  "ds_read_b128 v[124:127], v240 offset:2048\n" \
  "s_waitcnt lgkmcnt(2)\n" \
  "v_smfmac_f32_16x16x64_bf16 v[80:83], v[116:119], a[72:79], v239\n" \
  "s_barrier\n" \
  "v_smfmac_f32_16x16x64_bf16 v[76:79], v[116:119], a[0:7], v239\n" \
  "ds_read_b128 v[128:131], v240 offset:3072\n" \
  "s_waitcnt lgkmcnt(2)\n" \
  "v_smfmac_f32_16x16x64_bf16 v[80:83], v[120:123], a[80:87], v239\n" \
  "ds_read_b128 v[132:135], v240 offset:4096\n" \
  "v_smfmac_f32_16x16x64_bf16 v[76:79], v[120:123], a[8:15], v239\n" \
  "ds_read_b128 v[136:139], v240 offset:5120\n" \
  "s_waitcnt lgkmcnt(3)\n" \
  "v_smfmac_f32_16x16x64_bf16 v[76:79], v[124:127], a[16:23], v239\n" \
  "ds_read_b128 v[140:143], v240 offset:6144\n" \
  "ds_read_b128 v[144:147], v240 offset:7168\n" \
  "v_smfmac_f32_16x16x64_bf16 v[80:83], v[124:127], a[88:95], v239\n" \
  "s_waitcnt lgkmcnt(4)\n" \
  "v_smfmac_f32_16x16x64_bf16 v[76:79], v[128:131], a[24:31], v239\n" \
  "ds_read_b128 v[148:151], v240 offset:8192\n" \
  "s_waitcnt lgkmcnt(4)\n" \
  "v_smfmac_f32_16x16x64_bf16 v[76:79], v[132:135], a[32:39], v239\n" \
  "s_waitcnt lgkmcnt(3)\n" \
  "v_smfmac_f32_16x16x64_bf16 v[76:79], v[136:139], a[40:47], v239\n" \
  "s_waitcnt lgkmcnt(2)\n" \
  "v_smfmac_f32_16x16x64_bf16 v[76:79], v[140:143], a[48:55], v239\n" \
  "s_waitcnt lgkmcnt(1)\n" \
  "v_smfmac_f32_16x16x64_bf16 v[76:79], v[144:147], a[56:63], v239\n" \
  "s_waitcnt lgkmcnt(0)\n" \
  "v_smfmac_f32_16x16x64_bf16 v[76:79], v[148:151], a[64:71], v239\n" \
  "s_nop 2\n" \
  "v_smfmac_f32_16x16x64_bf16 v[80:83], v[128:131], a[96:103], v239\n" \
  "s_nop 2\n" \
  "v_smfmac_f32_16x16x64_bf16 v[80:83], v[132:135], a[104:111], v239\n" \
  "v_add_f32 v76, v76, v78\n" \
  "v_add_f32 v77, v77, v79\n" \
  "v_smfmac_f32_16x16x64_bf16 v[80:83], v[136:139], a[112:119], v239\n" \
  "v_cmp_u_f32 vcc, v76, v77\n" \
  "v_smfmac_f32_16x16x64_bf16 v[80:83], v[140:143], a[120:127], v239\n" \
  "v_mul_f32 v77, s64, v77\n" \
  "v_smfmac_f32_16x16x64_bf16 v[80:83], v[144:147], a[128:135], v239\n" \
  "v_mul_f32 v76, s64, v76\n" \
  "v_smfmac_f32_16x16x64_bf16 v[80:83], v[148:151], a[136:143], v239\n" \
  "s_cbranch_vccnz .LM_slow_%=\n" \
  ".LM_back_%=:\n" \
  "s_cmp_eq_u32 s48, 63\n" \
  "v_cvt_pk_bf16_f32 v76, v76, v77\n" \
  "v_smfmac_f32_16x16x64_bf16 v[92:95], v[116:119], v[32:39], v239\n" \
  "ds_write_b32 v241, v76\n" \
  "s_nop 2\n" \
  "v_add_f32 v80, v80, v82\n" \
  "v_mul_f32 v80, s64, v80\n" \
  "v_add_f32 v81, v81, v83\n" \
  "v_mul_f32 v81, s64, v81\n" \
  "v_cvt_pk_bf16_f32 v80, v80, v81\n" \
  "v_smfmac_f32_16x16x64_bf16 v[92:95], v[120:123], v[40:47], v239\n" \
  "ds_write_b32 v241, v80 offset:256\n" \
  "v_smfmac_f32_16x16x64_bf16 v[92:95], v[124:127], v[48:55], v239\n" \
  "v_smfmac_f32_16x16x64_bf16 v[92:95], v[128:131], v[56:63], v239\n" \
  "s_waitcnt lgkmcnt(1)\n" \
  "s_barrier\n" \
  "v_smfmac_f32_16x16x64_bf16 v[84:87], v[116:119], a[144:151], v239\n" \
  "s_nop 0\n" \
  "ds_read_b64_tr_b16 v[152:153], v243 offset:1024\n" \
  "v_smfmac_f32_16x16x64_bf16 v[88:91], v[116:119], a[216:223], v239\n" \
  "s_nop 1\n" \
  "v_add_f32 v93, v93, v95\n" \
  "s_waitcnt lgkmcnt(1)\n" \
  "s_barrier\n" \
  "v_add_f32 v92, v92, v94\n" \
  "v_smfmac_f32_16x16x64_bf16 v[84:87], v[120:123], a[152:159], v239\n" \
  "ds_read_b64_tr_b16 v[116:117], v243\n" \
  "v_smfmac_f32_16x16x64_bf16 v[84:87], v[124:127], a[160:167], v239\n" \
  "ds_write_b64 v255, v[92:93]\n" \
  "v_smfmac_f32_16x16x64_bf16 v[84:87], v[128:131], a[168:175], v239\n" \
  "v_smfmac_f32_16x16x64_bf16 v[84:87], v[132:135], a[176:183], v239\n" \
  "s_waitcnt vmcnt(0) \n" \
  "buffer_store_dwordx2 v[72:73], off, s[40:43], %35 sc1\n" \
  "v_smfmac_f32_16x16x64_bf16 v[84:87], v[136:139], a[184:191], v239\n" \
  "v_smfmac_f32_16x16x64_bf16 v[84:87], v[140:143], a[192:199], v239\n" \
  "s_waitcnt lgkmcnt(0)\n" \
  "s_barrier\n" \
  "v_smfmac_f32_16x16x64_bf16 v[84:87], v[144:147], a[200:207], v239\n" \
  "v_smfmac_f32_16x16x64_bf16 v[84:87], v[148:151], a[208:215], v239\n" \
  "ds_read_b64_tr_b16 v[154:155], v243 offset:1536\n" \
  "ds_read_b64_tr_b16 v[158:159], v243 offset:1792\n" \
  "v_smfmac_f32_16x16x64_bf16 v[88:91], v[120:123], a[224:231], v239\n" \
  "s_nop 2\n" \
  "v_smfmac_f32_16x16x64_bf16 v[88:91], v[124:127], a[232:239], v239\n" \
  "s_nop 0\n" \
  "v_add_f32 v84, v84, v86\n" \
  "v_smfmac_f32_16x16x64_bf16 v[88:91], v[128:131], a[240:247], v239\n" \
  "ds_read_b64_tr_b16 v[120:121], v243 offset:256\n" \
  "v_add_f32 v85, v85, v87\n" \
  "v_smfmac_f32_16x16x64_bf16 v[88:91], v[132:135], a[248:255], v239\n" \
  "ds_read_b64_tr_b16 v[122:123], v243 offset:768\n" \
  "v_cvt_pk_bf16_f32 v84, v84, v85\n" \
  "v_smfmac_f32_16x16x64_bf16 v[88:91], v[136:139], v[0:7], v239\n" \
  "v_smfmac_f32_16x16x64_bf16 v[88:91], v[140:143], v[8:15], v239\n" \
  "ds_write_b32 v242, v84\n" \
  "v_smfmac_f32_16x16x64_bf16 v[88:91], v[144:147], v[16:23], v239\n" \
  "v_smfmac_f32_16x16x64_bf16 v[88:91], v[148:151], v[24:31], v239\n" \
  "ds_read_b64_tr_b16 v[118:119], v243 offset:512\n" \
  "s_nop 6\n" \
  "v_add_f32 v88, v88, v90\n" \
  "v_add_f32 v89, v89, v91\n" \
  "v_cvt_pk_bf16_f32 v88, v88, v89\n" \
  "ds_write_b32 v242, v88 offset:256\n" \
  "s_waitcnt lgkmcnt(2)\n" \
  "s_barrier\n" \
  "ds_read_b64_tr_b16 v[124:125], v244 offset:1024\n" \
  "ds_read_b64_tr_b16 v[126:127], v244 offset:1536\n" \
  "ds_read_b64_tr_b16 v[130:131], v244 offset:512\n" \
  "ds_read_b64_tr_b16 v[128:129], v244\n" \
  "s_waitcnt lgkmcnt(4)\n" \
  "s_barrier\n" \
  "s_cbranch_scc1 .LM_xr_%=\n" \
  "ds_read_b64_tr_b16 v[134:135], v244 offset:768\n" \
  "ds_read_b64_tr_b16 v[132:133], v244 offset:256\n" \
  "ds_read_b64_tr_b16 v[156:157], v243 offset:1280\n" \
  "ds_read_b64_tr_b16 v[136:137], v244 offset:1280\n" \
  "ds_read_b64 v[188:189], v248\n" \
  "ds_read_b64_tr_b16 v[138:139], v244 offset:1792\n" \
  "s_waitcnt lgkmcnt(6)\n" \
  "v_mfma_f32_16x16x32_bf16 v[140:143], v[128:131], v[116:119], 0\n" \
  "v_mfma_f32_16x16x32_bf16 v[140:143], v[124:127], v[152:155], v[140:143]\n" \
  "s_waitcnt lgkmcnt(4)\n" \
  "v_mfma_f32_16x16x32_bf16 v[140:143], v[132:135], v[120:123], v[140:143]\n" \
  "s_waitcnt lgkmcnt(0)\n" \
  "v_mfma_f32_16x16x32_bf16 v[140:143], v[136:139], v[156:159], v[140:143]\n" \
  "s_nop 7\n" \
  "v_exp_f32 v119, v143\n" \
  "v_exp_f32 v117, v141\n" \
  "v_exp_f32 v118, v142\n" \
  "v_exp_f32 v116, v140\n" \
  "v_add_f32 v188, v188, v189\n" \
  "v_pk_add_f32 v[186:187], v[116:117], v[118:119]\n" \
  "s_nop 0\n" \
  "v_pk_add_f32 v[174:175], v[186:187], v[186:187] op_sel:[0,1] op_sel_hi:[1,0]\n" \
  "v_pk_fma_f32 v[116:117], v[116:117], v[180:181], v[96:97] op_sel_hi:[1,0,1]\n" \
  "v_pk_fma_f32 v[118:119], v[118:119], v[180:181], v[98:99] op_sel_hi:[1,0,1]\n" \
  "v_permlane32_swap_b32 v174, v175\n" \
  "v_add_f32 v174, v174, v175\n" \
  "v_add_f32 v174, v174, v188\n" \
  "v_rcp_f32 v188, v174\n" \
  "v_add_u32 v187, 3766484992, v174\n" \
  "v_pk_mul_f32 v[116:117], v[116:117], v[188:189] op_sel_hi:[1,0]\n" \
  "v_pk_mul_f32 v[118:119], v[118:119], v[188:189] op_sel_hi:[1,0]\n" \
  "v_cvt_pk_bf16_f32 v188, v116, v117\n" \
  "v_cvt_pk_bf16_f32 v189, v118, v119\n" \
  "v_cmp_le_u32 vcc, s52, v187\n" \
  "s_cbranch_vccnz .LM_ex_%=\n" \
  ".LM_ex_b_%=:\n" \
  "buffer_store_dwordx2 v[188:189], v246, s[40:43], 0 offen sc1\n" \
  "s_memrealtime s[98:99]\n" \
  "s_memtime s[84:85]\n" \
  ".LM_loop_%=:\n" \
  "s_cmp_ge_u32 s48, 55\n" \
  "s_cbranch_scc1 .LM_xl_%=\n" \
  ".LM_xr_b_%=:\n" \
  "ds_read_b64_tr_b16 v[116:117], v177\n" \
  "ds_read_b64_tr_b16 v[118:119], v177 offset:256\n" \
  "ds_read_b64_tr_b16 v[120:121], v177 offset:512\n" \
  "ds_read_b64_tr_b16 v[122:123], v177 offset:768\n" \
  "ds_read_b32 v188, v184\n" \
  "v_readfirstlane_b32 s63, v183\n" \
  "v_readfirstlane_b32 s62, v182\n" \
  "s_cmp_eq_u32 s63, s55\n" \
  "s_cselect_b32 s62, s62, 0\n" \
  "s_max_i32 s54, s54, s62\n" \
  "s_add_u32 s48, s48, 1\n" \
  "s_add_u32 s62, s48, 1\n" \
  "s_min_u32 s62, s62, 63\n" \
  "s_cmp_le_i32 s54, s62\n" \
  "s_cbranch_scc1 .LM_pk_go_%=\n" \
  ".LM_pk_ok_%=:\n" \
  "s_xor_b32 s60, s49, s58\n" \
  "s_add_u32 s49, s49, 16384\n" \
  "s_xor_b32 s91, s91, 8192\n" \
  "s_mov_b32 m0, s91\n" \
  "s_nop 0\n" \
  "buffer_load_dwordx4 v245, s[40:43], s60 offen sc1 lds\n" \
  "buffer_load_dwordx4 v245, s[40:43], s60 offen offset:1024 sc1 lds\n" \
  "buffer_load_dwordx4 v245, s[40:43], s60 offen offset:2048 sc1 lds\n" \
  "buffer_load_dwordx4 v245, s[40:43], s60 offen offset:3072 sc1 lds\n" \
  "s_add_u32 s62, s48, 2\n" \
  "s_cmp_le_i32 s54, s62\n" \
  "s_cselect_b32 s62, s74, 2130706432\n" \
  "buffer_load_dwordx2 v[182:183], off, s[40:43], s62 sc1\n" \
  "v_xor_b32 v176, 8192, v176\n" \
  "v_mov_b64 v[76:77], 0\n" \
  "v_mov_b64 v[78:79], 0\n" \
  "v_mov_b64 v[80:81], 0\n" \
  "v_mov_b64 v[82:83], 0\n" \
  "v_mov_b64 v[84:85], 0\n" \
  "v_mov_b64 v[86:87], 0\n" \
  "v_mov_b64 v[88:89], 0\n" \
  "v_mov_b64 v[90:91], 0\n" \
  "v_mov_b64 v[92:93], 0\n" \
  "v_mov_b64 v[94:95], 0\n" \
  "v_mov_b32 v72, s48\n" \
  "s_sub_u32 s60, s48, 2\n" \
  "s_cselect_b32 s60, 8192, s60\n" \
  "s_lshl_b32 s60, s60, 14\n" \
  "s_xor_b32 s60, s60, s50\n" \
  "s_cmp_le_u32 s48, 58\n" \
  "s_cselect_b32 s60, s60, 2130706432\n" \
  "s_waitcnt lgkmcnt(3)\n" \
  "buffer_store_dwordx4 v[116:119], v240, s[40:43], s60 offen sc1\n" \
  "s_waitcnt lgkmcnt(1)\n" \
  "buffer_store_dwordx4 v[120:123], v240, s[40:43], s60 offen offset:1024 sc1\n" \
  "s_waitcnt lgkmcnt(0)\n" \
  "s_waitcnt lgkmcnt(0)\n" \
  "v_readfirstlane_b32 s60, v188\n" \
  "v_mov_b32 v189, s55\n" \
  "s_lshl_b32 s66, s66, 10\n" \
  "s_and_b32 s61, s60, 1023\n" \
  "s_cselect_b32 s62, 4096000, 0\n" \
  "s_andn2_b32 s60, s60, 1023\n" \
  "s_sub_u32 s60, s66, s60\n" \
  "s_mov_b32 s66, s84\n" \
  "s_or_b32 s60, s60, s61\n" \
  "s_cmp_lt_u32 s60, s62\n" \
  "s_cselect_b32 s60, s60, -1\n" \
  "s_min_u32 s46, s46, s60\n" \
  "v_mov_b32 v188, s46\n" \
  "s_cmp_lg_u32 s46, -1\n" \
  "s_cselect_b32 s61, s75, 2130706432\n" \
  "s_and_b32 s60, s48, 3\n" \
  "s_cmp_eq_u32 s60, 2\n" \
  "s_cselect_b32 s61, s61, 2130706432\n" \
  "s_cselect_b32 s46, -1, s46\n" \
  "buffer_store_dwordx2 v[188:189], off, s[40:43], s61 sc1\n" \
  "s_cmp_eq_u32 s48, 58\n" \
  "s_cbranch_scc1 .LM_ef_%=\n" \
  ".LM_ef_b_%=:\n" \
  "s_branch .LM_rot_%=\n" \
  "s_nop 7\n" \
  ".LM_slow_%=:\n" \
  "s_bitcmp1_b32 s70, 0\n" \
  "s_cbranch_scc1 .LM_back_%=\n" \
  "s_waitcnt lgkmcnt(0)\n" \
  "s_cmp_lt_u32 s70, 10\n" \
  "s_nop 3\n" \
  "s_cbranch_scc1 .LM_nol_%=\n" \
  "v_max_u32 v189, v116, v117\n" \
  "v_max3_u32 v189, v189, v118, v119\n" \
  "v_max3_u32 v189, v189, v120, v121\n" \
  "v_max3_u32 v189, v189, v122, v123\n" \
  "v_max3_u32 v189, v189, v124, v125\n" \
  "v_max3_u32 v189, v189, v126, v127\n" \
  "v_max3_u32 v189, v189, v128, v129\n" \
  "v_max3_u32 v189, v189, v130, v131\n" \
  "v_max3_u32 v189, v189, v132, v133\n" \
  "v_max3_u32 v189, v189, v134, v135\n" \
  "v_max3_u32 v189, v189, v136, v137\n" \
  "v_max3_u32 v189, v189, v138, v139\n" \
  "v_max3_u32 v189, v189, v140, v141\n" \
  "v_max3_u32 v189, v189, v142, v143\n" \
  "v_max3_u32 v189, v189, v144, v145\n" \
  "v_max3_u32 v189, v189, v146, v147\n" \
  "v_max3_u32 v189, v189, v148, v149\n" \
  "v_max3_u32 v189, v189, v150, v151\n" \
  "v_cmp_eq_u32 vcc, -1, v189\n" \
  "s_cbranch_vccz .LM_back_%=\n" \
  ".LM_nol_%=:\n" \
  "s_add_u32 s70, s70, 2\n" \
  "s_cmp_gt_u32 s70, 65536\n" \
  "s_cbranch_scc1 .LM_dset_%=\n" \
  "s_barrier\n" \
  "v_mov_b64 v[76:77], 0\n" \
  "v_mov_b64 v[78:79], 0\n" \
  "v_mov_b64 v[80:81], 0\n" \
  "v_mov_b64 v[82:83], 0\n" \
  "v_mov_b64 v[84:85], 0\n" \
  "v_mov_b64 v[86:87], 0\n" \
  "v_mov_b64 v[88:89], 0\n" \
  "v_mov_b64 v[90:91], 0\n" \
  "v_mov_b64 v[92:93], 0\n" \
  "v_mov_b64 v[94:95], 0\n" \
  "s_branch .LM_rt_%=\n" \
  ".LM_dset_%=:\n" \
  "v_mov_b32 v136, 1\n" \
  "v_mov_b32 v137, s55\n" \
  "buffer_store_dwordx2 v[136:137], off, s[40:43], %38 offset:128 sc1\n" \
  "s_mov_b32 s70, 1\n" \
  "v_mov_b64 v[76:77], 0\n" \
  "v_mov_b64 v[78:79], 0\n" \
  "v_mov_b64 v[80:81], 0\n" \
  "v_mov_b64 v[82:83], 0\n" \
  "v_mov_b64 v[84:85], 0\n" \
  "v_mov_b64 v[86:87], 0\n" \
  "v_mov_b64 v[88:89], 0\n" \
  "v_mov_b64 v[90:91], 0\n" \
  "v_mov_b64 v[92:93], 0\n" \
  "v_mov_b64 v[94:95], 0\n" \
  "s_waitcnt vmcnt(0) lgkmcnt(0)\n" \
  "s_barrier\n" \
  "s_branch .LM_p_%=\n" \
  ".LM_xr_%=:\n" \
  "s_waitcnt lgkmcnt(0)\n" \
  "s_nop 7\n" \
  "ds_read_b64_tr_b16 v[132:133], v244\n" \
  "ds_read_b64_tr_b16 v[134:135], v244 offset:256\n" \
  "ds_read_b64_tr_b16 v[136:137], v244 offset:512\n" \
  "ds_read_b64_tr_b16 v[138:139], v244 offset:768\n" \
  "ds_read_b64_tr_b16 v[140:141], v244 offset:1024\n" \
  "ds_read_b64_tr_b16 v[142:143], v244 offset:1280\n" \
  "ds_read_b64_tr_b16 v[144:145], v244 offset:1536\n" \
  "ds_read_b64_tr_b16 v[146:147], v244 offset:1792\n" \
  "s_waitcnt lgkmcnt(6)\n" \
  "v_mfma_f32_16x16x32_bf16 v[148:151], v[132:135], v[100:103], 0\n" \
  "s_waitcnt lgkmcnt(4)\n" \
  "v_mfma_f32_16x16x32_bf16 v[148:151], v[136:139], v[104:107], v[148:151]\n" \
  "s_waitcnt lgkmcnt(2)\n" \
  "v_mfma_f32_16x16x32_bf16 v[148:151], v[140:143], v[108:111], v[148:151]\n" \
  "s_waitcnt lgkmcnt(0)\n" \
  "v_mfma_f32_16x16x32_bf16 v[148:151], v[144:147], v[112:115], v[148:151]\n" \
  "s_sub_u32 s60, s48, 58\n" \
  "s_lshl_b32 s60, s60, 16\n" \
  "s_add_u32 s60, s60, s97\n" \
  "s_cmp_eq_u32 s48, 0\n" \
  "s_cselect_b32 s60, 2130706432, s60\n" \
  "s_nop 2\n" \
  "v_max_f32 v158, v148, v149\n" \
  "v_max3_f32 v158, v150, v151, v158\n" \
  "v_mov_b32 v159, v158\n" \
  "s_nop 1\n" \
  "v_permlane32_swap_b32 v158, v159\n" \
  "v_max_f32 v158, v158, v159\n" \
  "v_mul_f32 v152, s65, v158\n" \
  "v_min_f32 v152, v152, v178\n" \
  "v_fma_f32 v148, v148, s64, v152\n" \
  "v_fma_f32 v149, v149, s64, v152\n" \
  "v_fma_f32 v150, v150, s64, v152\n" \
  "v_fma_f32 v151, v151, s64, v152\n" \
  "v_sub_f32 v160, v152, v178\n" \
  "v_exp_f32 v148, v148\n" \
  "v_exp_f32 v149, v149\n" \
  "v_exp_f32 v150, v150\n" \
  "v_exp_f32 v151, v151\n" \
  "v_exp_f32 v160, v160\n" \
  "v_add_f32 v153, v148, v149\n" \
  "v_add_f32 v158, v150, v151\n" \
  "v_add_f32 v153, v153, v158\n" \
  "v_mov_b32 v159, v153\n" \
  "s_nop 1\n" \
  "v_permlane32_swap_b32 v153, v159\n" \
  "v_add_f32 v153, v153, v159\n" \
  "v_fmac_f32 v153, v160, v179\n" \
  "v_rcp_f32 v154, v153\n" \
  "s_nop 0\n" \
  "v_mul_f32 v160, v160, v154\n" \
  "v_mul_f32 v154, v154, v252\n" \
  "v_mul_f32 v148, v148, v154\n" \
  "v_mul_f32 v149, v149, v154\n" \
  "v_mul_f32 v150, v150, v154\n" \
  "v_mul_f32 v151, v151, v154\n" \
  "v_fmac_f32 v148, v160, v253\n" \
  "v_fmac_f32 v150, v160, v254\n" \
  "v_cvt_pk_bf16_f32 v156, v148, v149\n" \
  "v_cvt_pk_bf16_f32 v157, v150, v151\n" \
  "buffer_store_dwordx2 v[156:157], v251, s[40:43], s60 offen sc1\n" \
  "s_cmp_eq_u32 s48, 63\n" \
  "s_cbranch_scc1 .LM_end_%=\n" \
  ".LM_xq_%=:\n" \
  "s_add_u32 s61, s48, 1\n" \
  "s_lshl_b32 s60, s61, 19\n" \
  "s_add_u32 s60, s60, s47\n" \
  "s_lshr_b32 s62, s60, 8\n" \
  "s_and_b32 s62, s62, 8192\n" \
  "s_xor_b32 s62, s62, s60\n" \
  "s_cmp_eq_u32 s48, 0\n" \
  "s_cselect_b32 s62, 2130706432, s62\n" \
  "s_add_u32 s63, s62, 4096\n" \
  "buffer_load_dwordx4 v[100:103], v249, s[40:43], s62 offen sc1\n" \
  "buffer_load_dwordx4 v[104:107], v249, s[40:43], s62 offen offset:2048 sc1\n" \
  "buffer_load_dwordx4 v[108:111], v249, s[40:43], s63 offen sc1\n" \
  "buffer_load_dwordx4 v[112:115], v249, s[40:43], s63 offen offset:2048 sc1\n" \
  "s_sub_u32 s60, s61, 58\n" \
  "s_lshl_b32 s60, s60, 16\n" \
  "s_add_u32 s60, s60, s97\n" \
  "s_cmp_eq_u32 s48, 0\n" \
  "s_cselect_b32 s60, 2130706432, s60\n" \
  "buffer_load_dwordx2 v[178:179], v250, s[40:43], s60 offen sc1\n" \
  "s_cbranch_scc1 .LM_dry_b_%=\n" \
  "s_branch .LM_xr_b_%=\n" \
  ".LM_xl_%=:\n" \
  "s_cmp_eq_u32 s48, 57\n" \
  "s_cbranch_scc1 .LM_xq_%=\n" \
  "s_cmp_eq_u32 s48, 56\n" \
  "s_cbranch_scc1 .LM_xc_%=\n" \
  "s_cmp_lg_u32 s48, 55\n" \
  "s_cbranch_scc1 .LM_xr_%=\n" \
  "buffer_load_dwordx2 v[178:179], off, s[40:43], %42 sc1\n" \
  "s_branch .LM_xr_b_%=\n" \
  ".LM_xc_%=:\n" \
  "v_readfirstlane_b32 s63, v179\n" \
  "s_cmp_eq_u32 s63, s55\n" \
  "s_cbranch_scc1 .LM_xr_b_%=\n" \
  "s_mov_b32 s69, 0\n" \
  ".LM_xs_%=:\n" \
  "buffer_load_dwordx2 v[178:179], off, s[40:43], %42 sc1\n" \
  "s_waitcnt vmcnt(0)\n" \
  "v_readfirstlane_b32 s62, v178\n" \
  "v_readfirstlane_b32 s63, v179\n" \
  "s_cmp_eq_u32 s63, s55\n" \
  "s_cselect_b32 s62, s62, 0\n" \
  "s_cmp_lg_u32 s62, 0\n" \
  "s_cbranch_scc1 .LM_xs_x_%=\n" \
  "s_sleep 2\n" \
  "s_add_u32 s69, s69, 1\n" \
  "s_and_b32 s63, s69, 1023\n" \
  "s_cbranch_scc1 .LM_xs_%=\n" \
  "s_cmp_gt_u32 s69, 2097152\n" \
  "s_cbranch_scc1 .LM_xs_set_%=\n" \
  "buffer_load_dwordx2 v[178:179], off, s[40:43], %38 offset:128 sc1\n" \
  "s_waitcnt vmcnt(0)\n" \
  "v_readfirstlane_b32 s63, v179\n" \
  "s_cmp_eq_u32 s63, s55\n" \
  "s_cbranch_scc0 .LM_xs_%=\n" \
  "s_branch .LM_xs_x_%=\n" \
  ".LM_xs_set_%=:\n" \
  "v_mov_b32 v178, 1\n" \
  "v_mov_b32 v179, s55\n" \
  "buffer_store_dwordx2 v[178:179], off, s[40:43], %38 offset:128 sc1\n" \
  "s_waitcnt vmcnt(0)\n" \
  ".LM_xs_x_%=:\n" \
  "s_branch .LM_xr_b_%=\n" \
  ".LM_pk_go_%=:\n" \
  "s_mov_b32 s94, s62\n" \
  "s_mov_b32 s69, 0\n" \
  ".LM_pk_%=:\n" \
  "buffer_load_dwordx2 v[186:187], off, s[40:43], s74 sc1\n" \
  "s_waitcnt vmcnt(0)\n" \
  "v_readfirstlane_b32 s54, v186\n" \
  "v_readfirstlane_b32 s63, v187\n" \
  "s_cmp_eq_u32 s63, s55\n" \
  "s_cselect_b32 s54, s54, 0\n" \
  "s_cmp_gt_i32 s54, s94\n" \
  "s_cbranch_scc1 .LM_pk_x_%=\n" \
  "s_sleep 2\n" \
  "s_add_u32 s69, s69, 1\n" \
  "s_and_b32 s63, s69, 1023\n" \
  "s_cbranch_scc1 .LM_pk_%=\n" \
  "s_cmp_gt_u32 s69, 2097152\n" \
  "s_cbranch_scc1 .LM_pk_set_%=\n" \
  "buffer_load_dwordx2 v[186:187], off, s[40:43], %38 offset:128 sc1\n" \
  "s_waitcnt vmcnt(0)\n" \
  "v_readfirstlane_b32 s63, v187\n" \
  "s_cmp_eq_u32 s63, s55\n" \
  "s_cbranch_scc0 .LM_pk_%=\n" \
  "s_branch .LM_pk_x_%=\n" \
  ".LM_pk_set_%=:\n" \
  "v_mov_b32 v186, 1\n" \
  "v_mov_b32 v187, s55\n" \
  "buffer_store_dwordx2 v[186:187], off, s[40:43], %38 offset:128 sc1\n" \
  "s_waitcnt vmcnt(0)\n" \
  ".LM_pk_x_%=:\n" \
  "s_branch .LM_pk_ok_%=\n" \
  ".LM_ef_%=:\n" \
  "s_waitcnt vmcnt(0) lgkmcnt(0)\n" \
  "buffer_store_dwordx2 v[72:73], off, s[40:43], %35 sc1\n" \
  "s_branch .LM_ef_b_%=\n" \
  ".LM_ex_%=:\n" \
  "s_waitcnt vmcnt(0) lgkmcnt(0)\n" \
  "s_nop 7\n" \
  "ds_read_b64_tr_b16 v[120:121], v243\n" \
  "ds_read_b64_tr_b16 v[122:123], v243 offset:256\n" \
  "ds_read_b64_tr_b16 v[124:125], v243 offset:512\n" \
  "ds_read_b64_tr_b16 v[126:127], v243 offset:768\n" \
  "ds_read_b64_tr_b16 v[128:129], v243 offset:1024\n" \
  "ds_read_b64_tr_b16 v[130:131], v243 offset:1280\n" \
  "ds_read_b64_tr_b16 v[132:133], v243 offset:1536\n" \
  "ds_read_b64_tr_b16 v[134:135], v243 offset:1792\n" \
  "ds_read_b128 v[136:139], v176\n" \
  "ds_read_b128 v[144:147], v176 offset:2048\n" \
  "ds_read_b128 v[148:151], v176 offset:4096\n" \
  "ds_read_b128 v[152:155], v176 offset:6144\n" \
  "s_waitcnt lgkmcnt(3)\n" \
  "v_mfma_f32_16x16x32_bf16 v[156:159], v[136:139], v[120:123], 0\n" \
  "s_waitcnt lgkmcnt(2)\n" \
  "v_mfma_f32_16x16x32_bf16 v[156:159], v[144:147], v[124:127], v[156:159]\n" \
  "s_waitcnt lgkmcnt(1)\n" \
  "v_mfma_f32_16x16x32_bf16 v[156:159], v[148:151], v[128:131], v[156:159]\n" \
  "s_waitcnt lgkmcnt(0)\n" \
  "v_mfma_f32_16x16x32_bf16 v[156:159], v[152:155], v[132:135], v[156:159]\n" \
  "s_nop 7\n" \
  "ds_read_b128 v[136:139], v176 offset:1024\n" \
  "ds_read_b128 v[144:147], v176 offset:3072\n" \
  "ds_read_b128 v[148:151], v176 offset:5120\n" \
  "ds_read_b128 v[152:155], v176 offset:7168\n" \
  "s_waitcnt lgkmcnt(3)\n" \
  "v_mfma_f32_16x16x32_bf16 v[160:163], v[136:139], v[120:123], 0\n" \
  "s_waitcnt lgkmcnt(2)\n" \
  "v_mfma_f32_16x16x32_bf16 v[160:163], v[144:147], v[124:127], v[160:163]\n" \
  "s_waitcnt lgkmcnt(1)\n" \
  "v_mfma_f32_16x16x32_bf16 v[160:163], v[148:151], v[128:131], v[160:163]\n" \
  "s_waitcnt lgkmcnt(0)\n" \
  "v_mfma_f32_16x16x32_bf16 v[160:163], v[152:155], v[132:135], v[160:163]\n" \
  "s_nop 7\n" \
  "v_max_f32 v164, v140, v141\n" \
  "v_max3_f32 v164, v164, v142, v143\n" \
  "v_max3_f32 v164, v164, v156, v157\n" \
  "v_max3_f32 v164, v164, v158, v159\n" \
  "s_nop 2\n" \
  "v_max3_f32 v164, v164, v160, v161\n" \
  "v_max3_f32 v164, v164, v162, v163\n" \
  "v_mov_b32 v165, v164\n" \
  "s_nop 1\n" \
  "v_permlane32_swap_b32 v164, v165\n" \
  "v_max_f32 v164, v164, v165\n" \
  "v_mov_b32 v165, v164\n" \
  "s_nop 1\n" \
  "v_permlane16_swap_b32 v164, v165\n" \
  "v_max_f32 v164, v164, v165\n" \
  "v_sub_f32 v116, v140, v164\n" \
  "v_sub_f32 v117, v141, v164\n" \
  "v_sub_f32 v118, v142, v164\n" \
  "v_sub_f32 v119, v143, v164\n" \
  "v_sub_f32 v156, v156, v164\n" \
  "v_sub_f32 v157, v157, v164\n" \
  "v_sub_f32 v158, v158, v164\n" \
  "v_sub_f32 v159, v159, v164\n" \
  "v_sub_f32 v160, v160, v164\n" \
  "v_sub_f32 v161, v161, v164\n" \
  "v_sub_f32 v162, v162, v164\n" \
  "v_sub_f32 v163, v163, v164\n" \
  "v_exp_f32 v116, v116\n" \
  "v_exp_f32 v117, v117\n" \
  "v_exp_f32 v118, v118\n" \
  "v_exp_f32 v119, v119\n" \
  "v_exp_f32 v156, v156\n" \
  "v_exp_f32 v157, v157\n" \
  "v_exp_f32 v158, v158\n" \
  "v_exp_f32 v159, v159\n" \
  "v_exp_f32 v160, v160\n" \
  "v_exp_f32 v161, v161\n" \
  "v_exp_f32 v162, v162\n" \
  "v_exp_f32 v163, v163\n" \
  "v_add_f32 v164, v116, v117\n" \
  "v_add_f32 v164, v164, v118\n" \
  "v_add_f32 v164, v164, v119\n" \
  "v_mul_f32 v164, 0.5, v164\n" \
  "v_add_f32 v164, v164, v156\n" \
  "v_add_f32 v164, v164, v157\n" \
  "v_add_f32 v164, v164, v158\n" \
  "v_add_f32 v164, v164, v159\n" \
  "v_add_f32 v164, v164, v160\n" \
  "v_add_f32 v164, v164, v161\n" \
  "v_add_f32 v164, v164, v162\n" \
  "v_add_f32 v164, v164, v163\n" \
  "v_mov_b32 v165, v164\n" \
  "s_nop 1\n" \
  "v_permlane32_swap_b32 v164, v165\n" \
  "v_add_f32 v164, v164, v165\n" \
  "v_mov_b32 v165, v164\n" \
  "s_nop 1\n" \
  "v_permlane16_swap_b32 v164, v165\n" \
  "v_add_f32 v164, v164, v165\n" \
  "v_rcp_f32 v188, v164\n" \
  "v_cvt_pk_bf16_f32 v156, v156, v157\n" \
  "v_cvt_pk_bf16_f32 v157, v158, v159\n" \
  "v_cvt_pk_bf16_f32 v160, v160, v161\n" \
  "v_cvt_pk_bf16_f32 v161, v162, v163\n" \
  "v_mov_b32 v165, 17408\n" \
  "s_cmp_eq_u32 s48, 0\n" \
  "s_cbranch_scc1 .LM_ex2_%=\n" \
  ".LM_ex1_%=:\n" \
  "ds_read_b64 v[166:167], v165\n" \
  "s_waitcnt lgkmcnt(0)\n" \
  "v_min_u32 v166, v166, v167\n" \
  "v_cmp_ge_u32 vcc, s48, v166\n" \
  "s_cbranch_vccnz .LM_ex1_%=\n" \
  ".LM_ex2_%=:\n" \
  "buffer_store_dwordx2 v[156:157], v247, s[40:43], 0 offen sc1\n" \
  "buffer_store_dwordx2 v[160:161], v247, s[40:43], 0 offen offset:256 sc1\n" \
  "v_pk_fma_f32 v[116:117], v[116:117], v[180:181], v[96:97] op_sel_hi:[1,0,1]\n" \
  "v_pk_fma_f32 v[118:119], v[118:119], v[180:181], v[98:99] op_sel_hi:[1,0,1]\n" \
  "v_pk_mul_f32 v[116:117], v[116:117], v[188:189] op_sel_hi:[1,0]\n" \
  "v_pk_mul_f32 v[118:119], v[118:119], v[188:189] op_sel_hi:[1,0]\n" \
  "v_cvt_pk_bf16_f32 v188, v116, v117\n" \
  "v_cvt_pk_bf16_f32 v189, v118, v119\n" \
  "s_waitcnt vmcnt(0)\n" \
  "s_mov_b32 s52, 2.0\n" \
  "s_cmp_eq_u32 s48, 0\n" \
  "s_cbranch_scc1 .LM_ex_b_%=\n" \
  "v_lshrrev_b32 v165, 1, v240\n" \
  "v_min_u32 v165, 280, v165\n" \
  "buffer_load_dwordx2 v[166:167], v165, s[40:43], %43 offen sc1\n" \
  "s_waitcnt vmcnt(0)\n" \
  "v_cmp_eq_u32 vcc, s55, v167\n" \
  "s_nop 1\n" \
  "v_cndmask_b32 v166, 0, v166, vcc\n" \
  "v_add_u32 v164, 2, v166\n" \
  "s_mov_b32 s69, 0\n" \
  ".LM_ex3_%=:\n" \
  "s_sleep 8\n" \
  "buffer_load_dwordx2 v[166:167], v165, s[40:43], %43 offen sc1\n" \
  "s_waitcnt vmcnt(0)\n" \
  "v_cmp_eq_u32 vcc, s55, v167\n" \
  "s_nop 1\n" \
  "v_cndmask_b32 v166, 0, v166, vcc\n" \
  "v_cmp_gt_u32 vcc, v164, v166\n" \
  "s_cbranch_vccz .LM_ex_b_%=\n" \
  "s_add_u32 s69, s69, 1\n" \
  "s_and_b32 s63, s69, 1023\n" \
  "s_cbranch_scc1 .LM_ex3_%=\n" \
  "s_cmp_gt_u32 s69, 2097152\n" \
  "s_cbranch_scc1 .LM_ex3_set_%=\n" \
  "buffer_load_dwordx2 v[166:167], off, s[40:43], %38 offset:128 sc1\n" \
  "s_waitcnt vmcnt(0)\n" \
  "v_readfirstlane_b32 s63, v167\n" \
  "s_cmp_eq_u32 s63, s55\n" \
  "s_cbranch_scc0 .LM_ex3_%=\n" \
  "s_branch .LM_ex_b_%=\n" \
  ".LM_ex3_set_%=:\n" \
  "v_mov_b32 v166, 1\n" \
  "v_mov_b32 v167, s55\n" \
  "buffer_store_dwordx2 v[166:167], off, s[40:43], %38 offset:128 sc1\n" \
  "s_waitcnt vmcnt(0)\n" \
  "s_branch .LM_ex_b_%=\n" \
  ".LM_end_%=:\n" \
  "s_waitcnt vmcnt(0)\n" \
  "s_waitcnt vmcnt(0)\n" \
  "s_nop 7\n" \
  "s_endpgm\n"
#define M_ASM_VOPS "+{v239}"(ci.sidx), "+{v240}"(ci.l16), "+{v241}"(ci.qw), "+{v242}"(ci.kw), "+{v243}"(ci.fq), "+{v244}"(ci.fk), "+{v245}"(ci.kxo), "+{v246}"(ci.pst), "+{v247}"(ci.ps2), "+{v248}"(ci.lx), "+{v249}"(ci.qxo), "+{v250}"(ci.sto), "+{v251}"(ci.plx), "+{v252}"(ci.mk), "+{v253}"(ci.ck0), "+{v254}"(ci.ck2), "+{v255}"(ci.hx), "+{v200}"(ci.vq0), "+{v201}"(ci.vq1), "+{v202}"(ci.vq2), "+{v203}"(ci.vq3), "+{v204}"(ci.tro0), "+{v205}"(ci.tro1), "+{v206}"(ci.m0o)
#define M_ASM_SOPS "s"(ci.ws), "s"(ci.Wa), "s"(ci.Wb), "s"(ci.mem0), "s"(ci.w), "s"(ci.o_a), "s"(ci.o_b), "s"(ci.o_nx), "s"(ci.o_exp), "s"(ci.lab), "s"(ci.f_p), "s"(ci.f_m), "s"(ci.f_dur), "s"(ci.f_go), "s"(ci.o_ctl), "s"(ci.o_dbg), "s"(ci.dret), "s"(ci.o_qx), "s"(ci.f_s), "s"(ci.f_r)
#define M_ASM_CLOB "v0", "v1", "v2", "v3", "v4", "v5", "v6", "v7", "v8", "v9", "v10", "v11", "v12", "v13", "v14", "v15", "v16", "v17", "v18", "v19", "v20", "v21", "v22", "v23", "v24", "v25", "v26", "v27", "v28", "v29", "v30", "v31", "v32", "v33", "v34", "v35", "v36", "v37", "v38", "v39", "v40", "v41", "v42", "v43", "v44", "v45", "v46", "v47", "v48", "v49", "v50", "v51", "v52", "v53", "v54", "v55", "v56", "v57", "v58", "v59", "v60", "v61", "v62", "v63", "v64", "v65", "v66", "v67", "v68", "v69", "v70", "v71", "v72", "v73", "v74", "v75", "v76", "v77", "v78", "v79", "v80", "v81", "v82", "v83", "v84", "v85", "v86", "v87", "v88", "v89", "v90", "v91", "v92", "v93", "v94", "v95", "v96", "v97", "v98", "v99", "v100", "v101", "v102", "v103", "v104", "v105", "v106", "v107", "v108", "v109", "v110", "v111", "v112", "v113", "v114", "v115", "v116", "v117", "v118", "v119", "v120", "v121", "v122", "v123", "v124", "v125", "v126", "v127", "v128", "v129", "v130", "v131", "v132", "v133", "v134", "v135", "v136", "v137", "v138", "v139", "v140", "v141", "v142", "v143", "v144", "v145", "v146", "v147", "v148", "v149", "v150", "v151", "v152", "v153", "v154", "v155", "v156", "v157", "v158", "v159", "v160", "v161", "v162", "v163", "v164", "v165", "v166", "v167", "v168", "v169", "v170", "v171", "v172", "v173", "v174", "v175", "v176", "v177", "v178", "v179", "v180", "v181", "v182", "v183", "v184", "v185", "v186", "v187", "v188", "v189", "v190", "v191", "v192", "v193", "v194", "v195", "v196", "v197", "v198", "v199", "a0", "a1", "a2", "a3", "a4", "a5", "a6", "a7", "a8", "a9", "a10", "a11", "a12", "a13", "a14", "a15", "a16", "a17", "a18", "a19", "a20", "a21", "a22", "a23", "a24", "a25", "a26", "a27", "a28", "a29", "a30", "a31", "a32", "a33", "a34", "a35", "a36", "a37", "a38", "a39", "a40", "a41", "a42", "a43", "a44", "a45", "a46", "a47", "a48", "a49", "a50", "a51", "a52", "a53", "a54", "a55", "a56", "a57", "a58", "a59", "a60", "a61", "a62", "a63", "a64", "a65", "a66", "a67", "a68", "a69", "a70", "a71", "a72", "a73", "a74", "a75", "a76", "a77", "a78", "a79", "a80", "a81", "a82", "a83", "a84", "a85", "a86", "a87", "a88", "a89", "a90", "a91", "a92", "a93", "a94", "a95", "a96", "a97", "a98", "a99", "a100", "a101", "a102", "a103", "a104", "a105", "a106", "a107", "a108", "a109", "a110", "a111", "a112", "a113", "a114", "a115", "a116", "a117", "a118", "a119", "a120", "a121", "a122", "a123", "a124", "a125", "a126", "a127", "a128", "a129", "a130", "a131", "a132", "a133", "a134", "a135", "a136", "a137", "a138", "a139", "a140", "a141", "a142", "a143", "a144", "a145", "a146", "a147", "a148", "a149", "a150", "a151", "a152", "a153", "a154", "a155", "a156", "a157", "a158", "a159", "a160", "a161", "a162", "a163", "a164", "a165", "a166", "a167", "a168", "a169", "a170", "a171", "a172", "a173", "a174", "a175", "a176", "a177", "a178", "a179", "a180", "a181", "a182", "a183", "a184", "a185", "a186", "a187", "a188", "a189", "a190", "a191", "a192", "a193", "a194", "a195", "a196", "a197", "a198", "a199", "a200", "a201", "a202", "a203", "a204", "a205", "a206", "a207", "a208", "a209", "a210", "a211", "a212", "a213", "a214", "a215", "a216", "a217", "a218", "a219", "a220", "a221", "a222", "a223", "a224", "a225", "a226", "a227", "a228", "a229", "a230", "a231", "a232", "a233", "a234", "a235", "a236", "a237", "a238", "a239", "a240", "a241", "a242", "a243", "a244", "a245", "a246", "a247", "a248", "a249", "a250", "a251", "a252", "a253", "a254", "a255", "s40", "s41", "s42", "s43", "s44", "s45", "s46", "s47", "s48", "s49", "s50", "s51", "s52", "s53", "s54", "s55", "s56", "s57", "s58", "s59", "s60", "s61", "s62", "s63", "s64", "s65", "s66", "s67", "s68", "s69", "s70", "s71", "s72", "s73", "s74", "s75", "s76", "s77", "s78", "s79", "s80", "s81", "s82", "s83", "s84", "s85", "s86", "s87", "s88", "s89", "s90", "s91", "s92", "s93", "s94", "s95", "s96", "s97", "s98", "s99", "vcc", "scc", "memory"
#define V_ASM_TEXT \
  "s_mov_b64 s[40:41], %32\n" \
  "s_and_b32 s41, s41, 0xffff\n" \
  "s_mov_b32 s42, 255852544\n" \
  "s_mov_b32 s43, 0x00020000\n" \
  "s_mov_b32 s46, 4718592\n" \
  "s_mov_b32 s47, 0x00020000\n" \
  "s_lshl_b32 s72, %36, 11\n" \
  "buffer_load_dword v196, off, s[40:43], %46 sc1\n" \
  "s_mov_b64 s[44:45], %34\n" \
  "s_and_b32 s45, s45, 0xffff\n" \
  "s_add_u32 s60, %38, 0\n" \
  "s_lshl_b32 s61, s72, 1\n" \
  "s_add_u32 m0, s61, 16384\n" \
  "s_nop 0\n" \
  "buffer_load_dwordx4 v204, s[44:47], s60 offen lds\n" \
  "buffer_load_dwordx4 v205, s[44:47], s60 offen offset:1024 lds\n" \
  "buffer_load_dwordx4 v206, s[44:47], s60 offen offset:2048 lds\n" \
  "buffer_load_dwordx4 v207, s[44:47], s60 offen offset:3072 lds\n" \
  "s_add_u32 s60, %38, 18432\n" \
  "s_lshl_b32 s61, s72, 1\n" \
  "s_add_u32 m0, s61, 32768\n" \
  "s_nop 0\n" \
  "buffer_load_dwordx4 v204, s[44:47], s60 offen lds\n" \
  "buffer_load_dwordx4 v205, s[44:47], s60 offen offset:1024 lds\n" \
  "buffer_load_dwordx4 v206, s[44:47], s60 offen offset:2048 lds\n" \
  "buffer_load_dwordx4 v207, s[44:47], s60 offen offset:3072 lds\n" \
  "s_add_u32 s60, %38, 256\n" \
  "s_lshl_b32 s61, s72, 1\n" \
  "s_add_u32 m0, s61, 49152\n" \
  "s_nop 0\n" \
  "buffer_load_dwordx4 v204, s[44:47], s60 offen lds\n" \
  "buffer_load_dwordx4 v205, s[44:47], s60 offen offset:1024 lds\n" \
  "buffer_load_dwordx4 v206, s[44:47], s60 offen offset:2048 lds\n" \
  "buffer_load_dwordx4 v207, s[44:47], s60 offen offset:3072 lds\n" \
  "s_add_u32 s60, %38, 18688\n" \
  "s_lshl_b32 s61, s72, 1\n" \
  "s_add_u32 m0, s61, 65536\n" \
  "s_nop 0\n" \
  "buffer_load_dwordx4 v204, s[44:47], s60 offen lds\n" \
  "buffer_load_dwordx4 v205, s[44:47], s60 offen offset:1024 lds\n" \
  "buffer_load_dwordx4 v206, s[44:47], s60 offen offset:2048 lds\n" \
  "buffer_load_dwordx4 v207, s[44:47], s60 offen offset:3072 lds\n" \
  "s_add_u32 s60, %38, 512\n" \
  "s_lshl_b32 s61, s72, 1\n" \
  "s_add_u32 m0, s61, 81920\n" \
  "s_nop 0\n" \
  "buffer_load_dwordx4 v204, s[44:47], s60 offen lds\n" \
  "buffer_load_dwordx4 v205, s[44:47], s60 offen offset:1024 lds\n" \
  "buffer_load_dwordx4 v206, s[44:47], s60 offen offset:2048 lds\n" \
  "buffer_load_dwordx4 v207, s[44:47], s60 offen offset:3072 lds\n" \
  "s_add_u32 s60, %38, 18944\n" \
  "s_lshl_b32 s61, s72, 1\n" \
  "s_add_u32 m0, s61, 98304\n" \
  "s_nop 0\n" \
  "buffer_load_dwordx4 v204, s[44:47], s60 offen lds\n" \
  "buffer_load_dwordx4 v205, s[44:47], s60 offen offset:1024 lds\n" \
  "buffer_load_dwordx4 v206, s[44:47], s60 offen offset:2048 lds\n" \
  "buffer_load_dwordx4 v207, s[44:47], s60 offen offset:3072 lds\n" \
  "s_add_u32 s60, %38, 768\n" \
  "s_lshl_b32 s61, s72, 1\n" \
  "s_add_u32 m0, s61, 114688\n" \
  "s_nop 0\n" \
  "buffer_load_dwordx4 v204, s[44:47], s60 offen lds\n" \
  "buffer_load_dwordx4 v205, s[44:47], s60 offen offset:1024 lds\n" \
  "buffer_load_dwordx4 v206, s[44:47], s60 offen offset:2048 lds\n" \
  "buffer_load_dwordx4 v207, s[44:47], s60 offen offset:3072 lds\n" \
  "s_add_u32 s60, %38, 19200\n" \
  "s_lshl_b32 s61, s72, 1\n" \
  "s_add_u32 m0, s61, 131072\n" \
  "s_nop 0\n" \
  "buffer_load_dwordx4 v204, s[44:47], s60 offen lds\n" \
  "buffer_load_dwordx4 v205, s[44:47], s60 offen offset:1024 lds\n" \
  "buffer_load_dwordx4 v206, s[44:47], s60 offen offset:2048 lds\n" \
  "buffer_load_dwordx4 v207, s[44:47], s60 offen offset:3072 lds\n" \
  "s_mov_b32 s60, %38\n" \
  "s_lshl_b32 s61, s72, 1\n" \
  "s_add_u32 m0, s61, 147456\n" \
  "s_nop 0\n" \
  "buffer_load_dwordx4 v208, s[44:47], s60 offen lds\n" \
  "buffer_load_dwordx4 v209, s[44:47], s60 offen offset:1024 lds\n" \
  "buffer_load_dwordx4 v210, s[44:47], s60 offen offset:2048 lds\n" \
  "buffer_load_dwordx4 v211, s[44:47], s60 offen offset:3072 lds\n" \
  "s_mov_b64 s[44:45], %35\n" \
  "s_and_b32 s45, s45, 0xffff\n" \
  "s_lshr_b32 s60, s72, 3\n" \
  "s_mov_b32 m0, s72\n" \
  "s_nop 0\n" \
  "buffer_load_dwordx4 v214, s[44:47], s60 offen lds\n" \
  "s_add_u32 s60, s60, 128\n" \
  "s_add_u32 m0, s72, 1024\n" \
  "s_nop 0\n" \
  "buffer_load_dwordx4 v214, s[44:47], s60 offen lds\n" \
  "s_movk_i32 s60, 1024\n" \
  "s_movk_i32 m0, 8192\n" \
  "s_nop 0\n" \
  "buffer_load_dwordx4 v214, s[44:47], s60 offen lds\n" \
  "s_waitcnt vmcnt(27)\n" \
  "s_barrier\n" \
  "v_add_u32 v194, 16384, v215\n" \
  "v_xor_b32 v195, 2176, v194\n" \
  "ds_read_b64_tr_b16 a[144:145], v194\n" \
  "ds_read_b64_tr_b16 a[146:147], v194 offset:512\n" \
  "ds_read_b64_tr_b16 a[148:149], v195\n" \
  "ds_read_b64_tr_b16 a[150:151], v195 offset:512\n" \
  "ds_read_b64_tr_b16 a[152:153], v194 offset:8192\n" \
  "ds_read_b64_tr_b16 a[154:155], v194 offset:8704\n" \
  "ds_read_b64_tr_b16 a[156:157], v195 offset:8192\n" \
  "ds_read_b64_tr_b16 a[158:159], v195 offset:8704\n" \
  "v_add_u32 v194, 32768, v215\n" \
  "v_xor_b32 v195, 2176, v194\n" \
  "ds_read_b64_tr_b16 a[160:161], v194\n" \
  "ds_read_b64_tr_b16 a[162:163], v194 offset:512\n" \
  "ds_read_b64_tr_b16 a[164:165], v195\n" \
  "ds_read_b64_tr_b16 a[166:167], v195 offset:512\n" \
  "ds_read_b64_tr_b16 a[168:169], v194 offset:8192\n" \
  "ds_read_b64_tr_b16 a[170:171], v194 offset:8704\n" \
  "ds_read_b64_tr_b16 a[172:173], v195 offset:8192\n" \
  "ds_read_b64_tr_b16 a[174:175], v195 offset:8704\n" \
  "v_add_u32 v194, 49152, v215\n" \
  "v_xor_b32 v195, 2176, v194\n" \
  "ds_read_b64_tr_b16 a[176:177], v194\n" \
  "ds_read_b64_tr_b16 a[178:179], v194 offset:512\n" \
  "ds_read_b64_tr_b16 a[180:181], v195\n" \
  "ds_read_b64_tr_b16 a[182:183], v195 offset:512\n" \
  "ds_read_b64_tr_b16 a[184:185], v194 offset:8192\n" \
  "ds_read_b64_tr_b16 a[186:187], v194 offset:8704\n" \
  "ds_read_b64_tr_b16 a[188:189], v195 offset:8192\n" \
  "ds_read_b64_tr_b16 a[190:191], v195 offset:8704\n" \
  "s_waitcnt lgkmcnt(0)\n" \
  "s_barrier\n" \
  "s_mov_b64 s[44:45], %33\n" \
  "s_and_b32 s45, s45, 0xffff\n" \
  "s_add_u32 s60, %37, 0\n" \
  "s_lshl_b32 s61, s72, 1\n" \
  "s_add_u32 m0, s61, 16384\n" \
  "s_nop 0\n" \
  "buffer_load_dwordx4 v200, s[44:47], s60 offen lds\n" \
  "buffer_load_dwordx4 v201, s[44:47], s60 offen offset:1024 lds\n" \
  "buffer_load_dwordx4 v202, s[44:47], s60 offen offset:2048 lds\n" \
  "buffer_load_dwordx4 v203, s[44:47], s60 offen offset:3072 lds\n" \
  "s_add_u32 s60, %37, 524288\n" \
  "s_lshl_b32 s61, s72, 1\n" \
  "s_add_u32 m0, s61, 32768\n" \
  "s_nop 0\n" \
  "buffer_load_dwordx4 v200, s[44:47], s60 offen lds\n" \
  "buffer_load_dwordx4 v201, s[44:47], s60 offen offset:1024 lds\n" \
  "buffer_load_dwordx4 v202, s[44:47], s60 offen offset:2048 lds\n" \
  "buffer_load_dwordx4 v203, s[44:47], s60 offen offset:3072 lds\n" \
  "s_add_u32 s60, %37, 1048576\n" \
  "s_lshl_b32 s61, s72, 1\n" \
  "s_add_u32 m0, s61, 49152\n" \
  "s_nop 0\n" \
  "buffer_load_dwordx4 v200, s[44:47], s60 offen lds\n" \
  "buffer_load_dwordx4 v201, s[44:47], s60 offen offset:1024 lds\n" \
  "buffer_load_dwordx4 v202, s[44:47], s60 offen offset:2048 lds\n" \
  "buffer_load_dwordx4 v203, s[44:47], s60 offen offset:3072 lds\n" \
  "v_readfirstlane_b32 s55, v196\n" \
  "s_add_u32 s55, s55, 1\n" \
  "s_waitcnt vmcnt(27)\n" \
  "s_barrier\n" \
  "v_add_u32 v194, 65536, v215\n" \
  "v_xor_b32 v195, 2176, v194\n" \
  "ds_read_b64_tr_b16 a[192:193], v194\n" \
  "ds_read_b64_tr_b16 a[194:195], v194 offset:512\n" \
  "ds_read_b64_tr_b16 a[196:197], v195\n" \
  "ds_read_b64_tr_b16 a[198:199], v195 offset:512\n" \
  "ds_read_b64_tr_b16 a[200:201], v194 offset:8192\n" \
  "ds_read_b64_tr_b16 a[202:203], v194 offset:8704\n" \
  "ds_read_b64_tr_b16 a[204:205], v195 offset:8192\n" \
  "ds_read_b64_tr_b16 a[206:207], v195 offset:8704\n" \
  "v_add_u32 v194, 81920, v215\n" \
  "v_xor_b32 v195, 2176, v194\n" \
  "ds_read_b64_tr_b16 a[208:209], v194\n" \
  "ds_read_b64_tr_b16 a[210:211], v194 offset:512\n" \
  "ds_read_b64_tr_b16 a[212:213], v195\n" \
  "ds_read_b64_tr_b16 a[214:215], v195 offset:512\n" \
  "ds_read_b64_tr_b16 a[216:217], v194 offset:8192\n" \
  "ds_read_b64_tr_b16 a[218:219], v194 offset:8704\n" \
  "ds_read_b64_tr_b16 a[220:221], v195 offset:8192\n" \
  "ds_read_b64_tr_b16 a[222:223], v195 offset:8704\n" \
  "v_add_u32 v194, 98304, v215\n" \
  "v_xor_b32 v195, 2176, v194\n" \
  "ds_read_b64_tr_b16 a[224:225], v194\n" \
  "ds_read_b64_tr_b16 a[226:227], v194 offset:512\n" \
  "ds_read_b64_tr_b16 a[228:229], v195\n" \
  "ds_read_b64_tr_b16 a[230:231], v195 offset:512\n" \
  "ds_read_b64_tr_b16 a[232:233], v194 offset:8192\n" \
  "ds_read_b64_tr_b16 a[234:235], v194 offset:8704\n" \
  "ds_read_b64_tr_b16 a[236:237], v195 offset:8192\n" \
  "ds_read_b64_tr_b16 a[238:239], v195 offset:8704\n" \
  "s_waitcnt lgkmcnt(0)\n" \
  "s_barrier\n" \
  "s_add_u32 s60, %37, 1572864\n" \
  "s_lshl_b32 s61, s72, 1\n" \
  "s_add_u32 m0, s61, 65536\n" \
  "s_nop 0\n" \
  "buffer_load_dwordx4 v200, s[44:47], s60 offen lds\n" \
  "buffer_load_dwordx4 v201, s[44:47], s60 offen offset:1024 lds\n" \
  "buffer_load_dwordx4 v202, s[44:47], s60 offen offset:2048 lds\n" \
  "buffer_load_dwordx4 v203, s[44:47], s60 offen offset:3072 lds\n" \
  "s_add_u32 s60, %37, 2097152\n" \
  "s_lshl_b32 s61, s72, 1\n" \
  "s_add_u32 m0, s61, 81920\n" \
  "s_nop 0\n" \
  "buffer_load_dwordx4 v200, s[44:47], s60 offen lds\n" \
  "buffer_load_dwordx4 v201, s[44:47], s60 offen offset:1024 lds\n" \
  "buffer_load_dwordx4 v202, s[44:47], s60 offen offset:2048 lds\n" \
  "buffer_load_dwordx4 v203, s[44:47], s60 offen offset:3072 lds\n" \
  "s_add_u32 s60, %37, 2621440\n" \
  "s_lshl_b32 s61, s72, 1\n" \
  "s_add_u32 m0, s61, 98304\n" \
  "s_nop 0\n" \
  "buffer_load_dwordx4 v200, s[44:47], s60 offen lds\n" \
  "buffer_load_dwordx4 v201, s[44:47], s60 offen offset:1024 lds\n" \
  "buffer_load_dwordx4 v202, s[44:47], s60 offen offset:2048 lds\n" \
  "buffer_load_dwordx4 v203, s[44:47], s60 offen offset:3072 lds\n" \
  "s_waitcnt vmcnt(27)\n" \
  "s_barrier\n" \
  "v_add_u32 v194, 114688, v215\n" \
  "v_xor_b32 v195, 2176, v194\n" \
  "ds_read_b64_tr_b16 a[240:241], v194\n" \
  "ds_read_b64_tr_b16 a[242:243], v194 offset:512\n" \
  "ds_read_b64_tr_b16 a[244:245], v195\n" \
  "ds_read_b64_tr_b16 a[246:247], v195 offset:512\n" \
  "ds_read_b64_tr_b16 a[248:249], v194 offset:8192\n" \
  "ds_read_b64_tr_b16 a[250:251], v194 offset:8704\n" \
  "ds_read_b64_tr_b16 a[252:253], v195 offset:8192\n" \
  "ds_read_b64_tr_b16 a[254:255], v195 offset:8704\n" \
  "v_add_u32 v194, 131072, v215\n" \
  "v_xor_b32 v195, 2176, v194\n" \
  "ds_read_b64_tr_b16 v[0:1], v194\n" \
  "ds_read_b64_tr_b16 v[2:3], v194 offset:512\n" \
  "ds_read_b64_tr_b16 v[4:5], v195\n" \
  "ds_read_b64_tr_b16 v[6:7], v195 offset:512\n" \
  "ds_read_b64_tr_b16 v[8:9], v194 offset:8192\n" \
  "ds_read_b64_tr_b16 v[10:11], v194 offset:8704\n" \
  "ds_read_b64_tr_b16 v[12:13], v195 offset:8192\n" \
  "ds_read_b64_tr_b16 v[14:15], v195 offset:8704\n" \
  "v_mov_b32 v194, v216\n" \
  "v_xor_b32 v195, 2176, v194\n" \
  "ds_read_b64_tr_b16 v[16:17], v194\n" \
  "ds_read_b64_tr_b16 v[18:19], v194 offset:512\n" \
  "ds_read_b64_tr_b16 v[20:21], v195\n" \
  "ds_read_b64_tr_b16 v[22:23], v195 offset:512\n" \
  "ds_read_b64_tr_b16 v[24:25], v194 offset:8192\n" \
  "ds_read_b64_tr_b16 v[26:27], v194 offset:8704\n" \
  "ds_read_b64_tr_b16 v[28:29], v195 offset:8192\n" \
  "ds_read_b64_tr_b16 v[30:31], v195 offset:8704\n" \
  "v_mov_b32 v194, v217\n" \
  "v_xor_b32 v195, 2176, v194\n" \
  "ds_read_b64_tr_b16 v[32:33], v194\n" \
  "ds_read_b64_tr_b16 v[34:35], v194 offset:512\n" \
  "ds_read_b64_tr_b16 v[36:37], v195\n" \
  "ds_read_b64_tr_b16 v[38:39], v195 offset:512\n" \
  "ds_read_b64_tr_b16 v[40:41], v194 offset:8192\n" \
  "ds_read_b64_tr_b16 v[42:43], v194 offset:8704\n" \
  "ds_read_b64_tr_b16 v[44:45], v195 offset:8192\n" \
  "ds_read_b64_tr_b16 v[46:47], v195 offset:8704\n" \
  "s_waitcnt lgkmcnt(0)\n" \
  "s_barrier\n" \
  "s_add_u32 s60, %37, 3145728\n" \
  "s_lshl_b32 s61, s72, 1\n" \
  "s_add_u32 m0, s61, 114688\n" \
  "s_nop 0\n" \
  "buffer_load_dwordx4 v200, s[44:47], s60 offen lds\n" \
  "buffer_load_dwordx4 v201, s[44:47], s60 offen offset:1024 lds\n" \
  "buffer_load_dwordx4 v202, s[44:47], s60 offen offset:2048 lds\n" \
  "buffer_load_dwordx4 v203, s[44:47], s60 offen offset:3072 lds\n" \
  "s_add_u32 s60, %37, 3670016\n" \
  "s_lshl_b32 s61, s72, 1\n" \
  "s_add_u32 m0, s61, 131072\n" \
  "s_nop 0\n" \
  "buffer_load_dwordx4 v200, s[44:47], s60 offen lds\n" \
  "buffer_load_dwordx4 v201, s[44:47], s60 offen offset:1024 lds\n" \
  "buffer_load_dwordx4 v202, s[44:47], s60 offen offset:2048 lds\n" \
  "buffer_load_dwordx4 v203, s[44:47], s60 offen offset:3072 lds\n" \
  "s_add_u32 s60, %37, 4194304\n" \
  "s_lshl_b32 s61, s72, 1\n" \
  "s_add_u32 m0, s61, 147456\n" \
  "s_nop 0\n" \
  "buffer_load_dwordx4 v200, s[44:47], s60 offen lds\n" \
  "buffer_load_dwordx4 v201, s[44:47], s60 offen offset:1024 lds\n" \
  "buffer_load_dwordx4 v202, s[44:47], s60 offen offset:2048 lds\n" \
  "buffer_load_dwordx4 v203, s[44:47], s60 offen offset:3072 lds\n" \
  "buffer_load_dwordx2 v[198:199], off, s[40:43], %45 sc1\n" \
  "s_waitcnt vmcnt(25)\n" \
  "s_barrier\n" \
  "v_add_u32 v194, 16384, v212\n" \
  "ds_read_b64_tr_b16 a[0:1], v194\n" \
  "ds_read_b64_tr_b16 a[2:3], v194 offset:512\n" \
  "ds_read_b64_tr_b16 a[4:5], v194 offset:8192\n" \
  "ds_read_b64_tr_b16 a[6:7], v194 offset:8704\n" \
  "v_add_u32 v195, 16384, v213\n" \
  "ds_read_b64_tr_b16 a[72:73], v195\n" \
  "ds_read_b64_tr_b16 a[74:75], v195 offset:512\n" \
  "ds_read_b64_tr_b16 a[76:77], v195 offset:8192\n" \
  "ds_read_b64_tr_b16 a[78:79], v195 offset:8704\n" \
  "v_add_u32 v194, 32768, v212\n" \
  "ds_read_b64_tr_b16 a[8:9], v194\n" \
  "ds_read_b64_tr_b16 a[10:11], v194 offset:512\n" \
  "ds_read_b64_tr_b16 a[12:13], v194 offset:8192\n" \
  "ds_read_b64_tr_b16 a[14:15], v194 offset:8704\n" \
  "v_add_u32 v195, 32768, v213\n" \
  "ds_read_b64_tr_b16 a[80:81], v195\n" \
  "ds_read_b64_tr_b16 a[82:83], v195 offset:512\n" \
  "ds_read_b64_tr_b16 a[84:85], v195 offset:8192\n" \
  "ds_read_b64_tr_b16 a[86:87], v195 offset:8704\n" \
  "v_add_u32 v194, 49152, v212\n" \
  "ds_read_b64_tr_b16 a[16:17], v194\n" \
  "ds_read_b64_tr_b16 a[18:19], v194 offset:512\n" \
  "ds_read_b64_tr_b16 a[20:21], v194 offset:8192\n" \
  "ds_read_b64_tr_b16 a[22:23], v194 offset:8704\n" \
  "v_add_u32 v195, 49152, v213\n" \
  "ds_read_b64_tr_b16 a[88:89], v195\n" \
  "ds_read_b64_tr_b16 a[90:91], v195 offset:512\n" \
  "ds_read_b64_tr_b16 a[92:93], v195 offset:8192\n" \
  "ds_read_b64_tr_b16 a[94:95], v195 offset:8704\n" \
  "s_waitcnt lgkmcnt(0)\n" \
  "s_waitcnt vmcnt(13)\n" \
  "s_barrier\n" \
  "v_add_u32 v194, 65536, v212\n" \
  "ds_read_b64_tr_b16 a[24:25], v194\n" \
  "ds_read_b64_tr_b16 a[26:27], v194 offset:512\n" \
  "ds_read_b64_tr_b16 a[28:29], v194 offset:8192\n" \
  "ds_read_b64_tr_b16 a[30:31], v194 offset:8704\n" \
  "v_add_u32 v195, 65536, v213\n" \
  "ds_read_b64_tr_b16 a[96:97], v195\n" \
  "ds_read_b64_tr_b16 a[98:99], v195 offset:512\n" \
  "ds_read_b64_tr_b16 a[100:101], v195 offset:8192\n" \
  "ds_read_b64_tr_b16 a[102:103], v195 offset:8704\n" \
  "v_add_u32 v194, 81920, v212\n" \
  "ds_read_b64_tr_b16 a[32:33], v194\n" \
  "ds_read_b64_tr_b16 a[34:35], v194 offset:512\n" \
  "ds_read_b64_tr_b16 a[36:37], v194 offset:8192\n" \
  "ds_read_b64_tr_b16 a[38:39], v194 offset:8704\n" \
  "v_add_u32 v195, 81920, v213\n" \
  "ds_read_b64_tr_b16 a[104:105], v195\n" \
  "ds_read_b64_tr_b16 a[106:107], v195 offset:512\n" \
  "ds_read_b64_tr_b16 a[108:109], v195 offset:8192\n" \
  "ds_read_b64_tr_b16 a[110:111], v195 offset:8704\n" \
  "v_add_u32 v194, 98304, v212\n" \
  "ds_read_b64_tr_b16 a[40:41], v194\n" \
  "ds_read_b64_tr_b16 a[42:43], v194 offset:512\n" \
  "ds_read_b64_tr_b16 a[44:45], v194 offset:8192\n" \
  "ds_read_b64_tr_b16 a[46:47], v194 offset:8704\n" \
  "v_add_u32 v195, 98304, v213\n" \
  "ds_read_b64_tr_b16 a[112:113], v195\n" \
  "ds_read_b64_tr_b16 a[114:115], v195 offset:512\n" \
  "ds_read_b64_tr_b16 a[116:117], v195 offset:8192\n" \
  "ds_read_b64_tr_b16 a[118:119], v195 offset:8704\n" \
  "s_waitcnt lgkmcnt(0)\n" \
  "s_waitcnt vmcnt(1)\n" \
  "s_barrier\n" \
  "v_add_u32 v194, 114688, v212\n" \
  "ds_read_b64_tr_b16 a[48:49], v194\n" \
  "ds_read_b64_tr_b16 a[50:51], v194 offset:512\n" \
  "ds_read_b64_tr_b16 a[52:53], v194 offset:8192\n" \
  "ds_read_b64_tr_b16 a[54:55], v194 offset:8704\n" \
  "v_add_u32 v195, 114688, v213\n" \
  "ds_read_b64_tr_b16 a[120:121], v195\n" \
  "ds_read_b64_tr_b16 a[122:123], v195 offset:512\n" \
  "ds_read_b64_tr_b16 a[124:125], v195 offset:8192\n" \
  "ds_read_b64_tr_b16 a[126:127], v195 offset:8704\n" \
  "v_add_u32 v194, 131072, v212\n" \
  "ds_read_b64_tr_b16 a[56:57], v194\n" \
  "ds_read_b64_tr_b16 a[58:59], v194 offset:512\n" \
  "ds_read_b64_tr_b16 a[60:61], v194 offset:8192\n" \
  "ds_read_b64_tr_b16 a[62:63], v194 offset:8704\n" \
  "v_add_u32 v195, 131072, v213\n" \
  "ds_read_b64_tr_b16 a[128:129], v195\n" \
  "ds_read_b64_tr_b16 a[130:131], v195 offset:512\n" \
  "ds_read_b64_tr_b16 a[132:133], v195 offset:8192\n" \
  "ds_read_b64_tr_b16 a[134:135], v195 offset:8704\n" \
  "v_add_u32 v194, 147456, v212\n" \
  "ds_read_b64_tr_b16 a[64:65], v194\n" \
  "ds_read_b64_tr_b16 a[66:67], v194 offset:512\n" \
  "ds_read_b64_tr_b16 a[68:69], v194 offset:8192\n" \
  "ds_read_b64_tr_b16 a[70:71], v194 offset:8704\n" \
  "v_add_u32 v195, 147456, v213\n" \
  "ds_read_b64_tr_b16 a[136:137], v195\n" \
  "ds_read_b64_tr_b16 a[138:139], v195 offset:512\n" \
  "ds_read_b64_tr_b16 a[140:141], v195 offset:8192\n" \
  "ds_read_b64_tr_b16 a[142:143], v195 offset:8704\n" \
  "s_waitcnt lgkmcnt(0)\n" \
  "s_waitcnt vmcnt(0)\n" \
  "v_readfirstlane_b32 s96, v198\n" \
  "v_readfirstlane_b32 s63, v199\n" \
  "s_cmp_eq_u32 s63, s55\n" \
  "s_cselect_b32 s96, s96, 0\n" \
  "s_cmp_ge_u32 s96, 0xc0000000\n" \
  "s_cbranch_scc1 .LV_go_x_%=\n" \
  "s_mov_b32 s69, 0\n" \
  ".LV_go_%=:\n" \
  "buffer_load_dwordx2 v[196:197], off, s[40:43], %45 sc1\n" \
  "s_waitcnt vmcnt(0)\n" \
  "v_readfirstlane_b32 s96, v196\n" \
  "v_readfirstlane_b32 s63, v197\n" \
  "s_cmp_eq_u32 s63, s55\n" \
  "s_cselect_b32 s96, s96, 0\n" \
  "s_cmp_ge_u32 s96, 0xc0000000\n" \
  "s_cbranch_scc1 .LV_go_x_%=\n" \
  "s_sleep 8\n" \
  "s_add_u32 s69, s69, 1\n" \
  "s_and_b32 s63, s69, 1023\n" \
  "s_cbranch_scc1 .LV_go_%=\n" \
  "s_cmp_gt_u32 s69, 2097152\n" \
  "s_cbranch_scc1 .LV_go_set_%=\n" \
  "buffer_load_dwordx2 v[196:197], off, s[40:43], %46 offset:128 sc1\n" \
  "s_waitcnt vmcnt(0)\n" \
  "v_readfirstlane_b32 s63, v197\n" \
  "s_cmp_eq_u32 s63, s55\n" \
  "s_cbranch_scc0 .LV_go_%=\n" \
  "s_branch .LV_go_x_%=\n" \
  ".LV_go_set_%=:\n" \
  "v_mov_b32 v196, 1\n" \
  "v_mov_b32 v197, s55\n" \
  "buffer_store_dwordx2 v[196:197], off, s[40:43], %46 offset:128 sc1\n" \
  "s_waitcnt vmcnt(0)\n" \
  ".LV_go_x_%=:\n" \
  "s_and_b32 s60, s96, 2\n" \
  "s_lshl_b32 s60, s60, 12\n" \
  "s_xor_b32 s74, %42, s60\n" \
  "s_xor_b32 s75, %44, s60\n" \
  "s_bfe_u32 s56, s96, 0x30010\n" \
  "s_lshl_b32 s56, s56, 12\n" \
  "s_or_b32 s56, s56, 0x200000\n" \
  "s_mov_b32 s57, 1052672\n" \
  "s_lshl_b32 s61, %41, 2\n" \
  "s_lshr_b32 s51, s96, s61\n" \
  "s_and_b32 s51, s51, 7\n" \
  "s_lshl_b32 s51, s51, 12\n" \
  "s_xor_b32 s51, s51, 4210688\n" \
  "s_mov_b32 s71, 0\n" \
  "s_add_u32 s45, s51, 886784\n" \
  "s_mov_b32 s46, -1\n" \
  "v_mov_b64 v[48:49], -1\n" \
  "v_mov_b64 v[50:51], -1\n" \
  "v_and_b32 v194, 3, v250\n" \
  "v_lshlrev_b32 v194, 2, v194\n" \
  "v_lshrrev_b32 v194, v194, s96\n" \
  "v_and_b32 v194, 7, v194\n" \
  "v_lshlrev_b32 v194, 12, v194\n" \
  "v_and_b32 v250, -4, v250\n" \
  "v_xor_b32 v250, v250, v194\n" \
  "v_and_b32 v194, 3, v251\n" \
  "v_lshlrev_b32 v194, 2, v194\n" \
  "v_lshrrev_b32 v194, v194, s96\n" \
  "v_and_b32 v194, 7, v194\n" \
  "v_lshlrev_b32 v194, 12, v194\n" \
  "v_and_b32 v251, -4, v251\n" \
  "v_xor_b32 v251, v251, v194\n" \
  "v_and_b32 v194, 3, v252\n" \
  "v_lshlrev_b32 v194, 2, v194\n" \
  "v_lshrrev_b32 v194, v194, s96\n" \
  "v_and_b32 v194, 7, v194\n" \
  "v_lshlrev_b32 v194, 12, v194\n" \
  "v_and_b32 v252, -4, v252\n" \
  "v_xor_b32 v252, v252, v194\n" \
  "v_xor_b32 v185, s56, v250\n" \
  "buffer_store_dwordx4 v[48:51], v185, s[40:43], 0 offen sc1\n" \
  "v_xor_b32 v186, s56, v251\n" \
  "buffer_store_dwordx4 v[48:51], v186, s[40:43], 0 offen sc1\n" \
  "v_xor_b32 v187, s56, v252\n" \
  "buffer_store_dwordx2 v[48:49], v187, s[40:43], 0 offen sc1\n" \
  "s_xor_b32 s62, s56, s57\n" \
  "s_nop 1\n" \
  "v_xor_b32 v185, s62, v250\n" \
  "buffer_store_dwordx4 v[48:51], v185, s[40:43], 0 offen sc1\n" \
  "v_xor_b32 v186, s62, v251\n" \
  "buffer_store_dwordx4 v[48:51], v186, s[40:43], 0 offen sc1\n" \
  "v_xor_b32 v187, s62, v252\n" \
  "buffer_store_dwordx2 v[48:49], v187, s[40:43], 0 offen sc1\n" \
  "s_andn2_b32 s60, 2, s96\n" \
  "s_lshl_b32 s60, s60, 12\n" \
  "s_xor_b32 s60, s60, 8192\n" \
  "s_xor_b32 s58, %39, s60\n" \
  "s_mov_b32 s49, 0\n" \
  "s_add_u32 s47, %49, s60\n" \
  "s_add_u32 s97, s60, 8388608\n" \
  "s_xor_b32 s50, %40, s60\n" \
  "s_mov_b32 s48, 0\n" \
  "s_mov_b32 s54, 0\n" \
  "s_cmp_eq_u32 %36, 0\n" \
  "s_cselect_b64 s[88:89], -1, 0\n" \
  "s_mov_b32 s68, 0\n" \
  "s_mov_b32 s70, 1\n" \
  "s_mov_b32 s73, 0\n" \
  "s_mov_b32 s44, 0\n" \
  "s_mov_b32 s53, 0\n" \
  "s_memrealtime s[86:87]\n" \
  "s_memtime s[76:77]\n" \
  "s_waitcnt lgkmcnt(0)\n" \
  "s_mov_b32 s90, s76\n" \
  "s_mov_b32 s87, 340\n" \
  "s_mov_b32 s64, 0x3e0293ee\n" \
  "s_mov_b32 s65, 0xbe0293ee\n" \
  "v_mov_b64 v[60:61], 0\n" \
  "v_mov_b64 v[62:63], 0\n" \
  "v_mov_b64 v[64:65], 0\n" \
  "v_mov_b64 v[66:67], 0\n" \
  "v_mov_b64 v[68:69], 0\n" \
  "v_mov_b64 v[70:71], 0\n" \
  "v_mov_b64 v[72:73], 0\n" \
  "v_mov_b64 v[74:75], 0\n" \
  "v_mov_b64 v[76:77], 0\n" \
  "v_mov_b64 v[78:79], 0\n" \
  "v_mov_b64 v[80:81], 0\n" \
  "v_mov_b64 v[82:83], 0\n" \
  "v_mov_b64 v[84:85], 0\n" \
  "v_mov_b64 v[86:87], 0\n" \
  "v_mov_b32 v56, 0\n" \
  "v_mov_b32 v57, s55\n" \
  "v_mov_b64 v[58:59], v[56:57]\n" \
  "v_mov_b64 v[90:91], 0\n" \
  "s_mov_b32 s67, 2130706432\n" \
  "s_waitcnt vmcnt(0) lgkmcnt(0)\n" \
  "s_barrier\n" \
  "s_branch .LV_p_%=\n" \
  "s_nop 7\n" \
  ".p2align 6\n" \
  ".fill (((20 - (.LV_p_%= - .LV_rot_%=)) & 63) >> 2), 4, 0xBF800000\n" \
  ".LV_rot_%=:\n" \
  "s_xor_b32 s51, s51, s56\n" \
  "s_xor_b32 s71, s71, s56\n" \
  "v_xor_b32 v250, s56, v250\n" \
  "v_xor_b32 v251, s56, v251\n" \
  "v_xor_b32 v252, s56, v252\n" \
  "s_mov_b32 s60, s56\n" \
  "s_mov_b32 s56, s57\n" \
  "s_mov_b32 s57, s60\n" \
  "s_add_u32 s59, s51, 819200\n" \
  "s_mov_b32 m0, 0\n" \
  "s_mov_b32 s70, 0\n" \
  "s_waitcnt lgkmcnt(0)\n" \
  ".LV_rt_%=:\n" \
  "s_barrier\n" \
  ".LV_p_%=:\n" \
  "ds_read_b128 v[92:95], v243\n" \
  "ds_read_b128 v[96:99], v243 offset:3072\n" \
  "ds_read_b128 v[100:103], v243 offset:1024\n" \
  "ds_read_b128 v[104:107], v243 offset:2048\n" \
  "s_waitcnt lgkmcnt(3)\n" \
  "v_smfmac_f32_16x16x64_bf16 v[60:63], v[92:95], a[0:7], v242\n" \
  "s_barrier\n" \
  "ds_read_b128 v[108:111], v243 offset:4096\n" \
  "v_smfmac_f32_16x16x64_bf16 v[64:67], v[92:95], a[72:79], v242\n" \
  "ds_read_b128 v[112:115], v243 offset:5120\n" \
  "s_waitcnt lgkmcnt(3)\n" \
  "v_smfmac_f32_16x16x64_bf16 v[60:63], v[100:103], a[8:15], v242\n" \
  "ds_read_b128 v[116:119], v243 offset:6144\n" \
  "s_waitcnt lgkmcnt(3)\n" \
  "v_smfmac_f32_16x16x64_bf16 v[60:63], v[104:107], a[16:23], v242\n" \
  "ds_read_b128 v[120:123], v243 offset:7168\n" \
  "v_smfmac_f32_16x16x64_bf16 v[60:63], v[96:99], a[24:31], v242\n" \
  "ds_read_b128 v[124:127], v243 offset:8192\n" \
  "s_waitcnt lgkmcnt(4)\n" \
  "v_smfmac_f32_16x16x64_bf16 v[60:63], v[108:111], a[32:39], v242\n" \
  "s_waitcnt lgkmcnt(3)\n" \
  "v_smfmac_f32_16x16x64_bf16 v[60:63], v[112:115], a[40:47], v242\n" \
  "s_waitcnt lgkmcnt(2)\n" \
  "v_smfmac_f32_16x16x64_bf16 v[60:63], v[116:119], a[48:55], v242\n" \
  "s_waitcnt lgkmcnt(1)\n" \
  "v_smfmac_f32_16x16x64_bf16 v[60:63], v[120:123], a[56:63], v242\n" \
  "s_waitcnt lgkmcnt(0)\n" \
  "v_smfmac_f32_16x16x64_bf16 v[60:63], v[124:127], a[64:71], v242\n" \
  "s_nop 2\n" \
  "v_smfmac_f32_16x16x64_bf16 v[64:67], v[100:103], a[80:87], v242\n" \
  "s_nop 2\n" \
  "v_smfmac_f32_16x16x64_bf16 v[64:67], v[104:107], a[88:95], v242\n" \
  "v_add_f32 v60, v60, v62\n" \
  "v_add_f32 v61, v61, v63\n" \
  "v_cvt_pk_bf16_f32 v88, v60, v61\n" \
  "v_cmp_u_f32 vcc, v60, v61\n" \
  "v_smfmac_f32_16x16x64_bf16 v[64:67], v[96:99], a[96:103], v242\n" \
  "s_cbranch_vccnz .LV_slow_%=\n" \
  ".LV_back_%=:\n" \
  "ds_write_b32 v244, v88\n" \
  "v_smfmac_f32_16x16x64_bf16 v[64:67], v[108:111], a[104:111], v242\n" \
  "v_smfmac_f32_16x16x64_bf16 v[64:67], v[112:115], a[112:119], v242\n" \
  "v_smfmac_f32_16x16x64_bf16 v[64:67], v[116:119], a[120:127], v242\n" \
  "v_smfmac_f32_16x16x64_bf16 v[64:67], v[120:123], a[128:135], v242\n" \
  "s_waitcnt lgkmcnt(0)\n" \
  "s_waitcnt vmcnt(0) \n" \
  "s_barrier\n" \
  "ds_read_b64_tr_b16 v[92:93], v245\n" \
  "ds_read_b64_tr_b16 v[94:95], v245 offset:512\n" \
  "v_smfmac_f32_16x16x64_bf16 v[64:67], v[124:127], a[136:143], v242\n" \
  "ds_read_b64_tr_b16 v[96:97], v245 offset:1024\n" \
  "ds_read_b64_tr_b16 v[98:99], v245 offset:1536\n" \
  "s_nop 5\n" \
  "v_add_f32 v64, v64, v66\n" \
  "v_add_f32 v65, v65, v67\n" \
  "v_cvt_pk_bf16_f32 v89, v64, v65\n" \
  "s_waitcnt lgkmcnt(2)\n" \
  "v_smfmac_f32_16x16x64_bf16 v[76:79], v[92:95], a[208:215], v248\n" \
  "ds_write_b32 v244, v89 offset:256\n" \
  "s_waitcnt lgkmcnt(1)\n" \
  "v_smfmac_f32_16x16x64_bf16 v[76:79], v[96:99], a[216:223], v248\n" \
  "v_smfmac_f32_16x16x64_bf16 v[68:71], v[92:95], a[144:151], v248\n" \
  "v_smfmac_f32_16x16x64_bf16 v[72:75], v[92:95], a[176:183], v248\n" \
  "buffer_store_dwordx2 v[56:57], off, s[40:43], %43 sc1\n" \
  "v_smfmac_f32_16x16x64_bf16 v[68:71], v[96:99], a[152:159], v248\n" \
  "s_waitcnt lgkmcnt(0)\n" \
  "s_barrier\n" \
  "v_smfmac_f32_16x16x64_bf16 v[80:83], v[92:95], a[240:247], v248\n" \
  "ds_read_b64_tr_b16 v[102:103], v245 offset:768\n" \
  "ds_read_b64_tr_b16 v[100:101], v245 offset:256\n" \
  "v_smfmac_f32_16x16x64_bf16 v[84:87], v[92:95], v[16:23], v248\n" \
  "ds_read_b64_tr_b16 v[94:95], v245 offset:1792\n" \
  "ds_read_b64_tr_b16 v[92:93], v245 offset:1280\n" \
  "v_smfmac_f32_16x16x64_bf16 v[80:83], v[96:99], a[248:255], v248\n" \
  "v_smfmac_f32_16x16x64_bf16 v[72:75], v[96:99], a[184:191], v248\n" \
  "s_waitcnt lgkmcnt(2)\n" \
  "v_smfmac_f32_16x16x64_bf16 v[68:71], v[100:103], a[160:167], v248\n" \
  "s_waitcnt lgkmcnt(0)\n" \
  "v_smfmac_f32_16x16x64_bf16 v[68:71], v[92:95], a[168:175], v248\n" \
  "s_nop 2\n" \
  "v_smfmac_f32_16x16x64_bf16 v[72:75], v[100:103], a[192:199], v248\n" \
  "s_nop 2\n" \
  "v_smfmac_f32_16x16x64_bf16 v[72:75], v[92:95], a[200:207], v248\n" \
  "s_nop 1\n" \
  "v_cvt_pk_bf16_f32 v68, v68, v69\n" \
  "v_smfmac_f32_16x16x64_bf16 v[84:87], v[96:99], v[24:31], v248\n" \
  "s_nop 0\n" \
  "v_cvt_pk_bf16_f32 v69, v70, v71\n" \
  "v_smfmac_f32_16x16x64_bf16 v[76:79], v[100:103], a[224:231], v248\n" \
  "v_smfmac_f32_16x16x64_bf16 v[80:83], v[100:103], v[0:7], v248\n" \
  "v_cvt_pk_bf16_f32 v71, v74, v75\n" \
  "v_smfmac_f32_16x16x64_bf16 v[84:87], v[100:103], v[32:39], v248\n" \
  "v_cvt_pk_bf16_f32 v70, v72, v73\n" \
  "v_smfmac_f32_16x16x64_bf16 v[76:79], v[92:95], a[232:239], v248\n" \
  "s_nop 1\n" \
  "v_permlane16_swap_b32 v69, v71\n" \
  "v_smfmac_f32_16x16x64_bf16 v[80:83], v[92:95], v[8:15], v248\n" \
  "s_nop 0\n" \
  "s_nop 0\n" \
  "v_permlane16_swap_b32 v68, v70\n" \
  "v_smfmac_f32_16x16x64_bf16 v[84:87], v[92:95], v[40:47], v248\n" \
  "buffer_store_dwordx4 v[68:71], v250, s[40:43], 0 offen sc1\n" \
  "v_cvt_pk_bf16_f32 v76, v76, v77\n" \
  "v_cvt_pk_bf16_f32 v77, v78, v79\n" \
  "s_nop 0\n" \
  "v_cvt_pk_bf16_f32 v78, v80, v81\n" \
  "v_cvt_pk_bf16_f32 v79, v82, v83\n" \
  "s_nop 0\n" \
  "v_permlane16_swap_b32 v76, v78\n" \
  "v_cvt_pk_bf16_f32 v84, v84, v85\n" \
  "v_cvt_pk_bf16_f32 v85, v86, v87\n" \
  "v_permlane16_swap_b32 v77, v79\n" \
  "buffer_store_dwordx4 v[76:79], v251, s[40:43], 0 offen sc1\n" \
  "buffer_store_dwordx2 v[84:85], v252, s[40:43], 0 offen sc1\n" \
  ".LV_loop_%=:\n" \
  "s_cmp_ge_u32 s48, 58\n" \
  "s_cbranch_scc1 .LV_cp_%=\n" \
  ".LV_cp_b_%=:\n" \
  "v_mov_b64 v[60:61], 0\n" \
  "v_mov_b64 v[62:63], 0\n" \
  "v_mov_b64 v[64:65], 0\n" \
  "v_mov_b64 v[66:67], 0\n" \
  "v_mov_b64 v[68:69], 0\n" \
  "v_mov_b64 v[70:71], 0\n" \
  "v_mov_b64 v[72:73], 0\n" \
  "v_mov_b64 v[74:75], 0\n" \
  "v_mov_b64 v[76:77], 0\n" \
  "v_mov_b64 v[78:79], 0\n" \
  "v_mov_b64 v[80:81], 0\n" \
  "v_mov_b64 v[82:83], 0\n" \
  "v_mov_b64 v[84:85], 0\n" \
  "v_mov_b64 v[86:87], 0\n" \
  "ds_read_b32 v92, v246\n" \
  "ds_read_b32 v93, v246 offset:256\n" \
  "v_mov_b64 v[94:95], 0\n" \
  "s_sub_u32 s60, s48, 1\n" \
  "s_cselect_b32 s60, 8192, s60\n" \
  "s_lshl_b32 s60, s60, 14\n" \
  "s_xor_b32 s60, s60, s50\n" \
  "buffer_store_dwordx4 v[88:91], v247, s[40:43], s60 offen sc1\n" \
  "s_waitcnt lgkmcnt(0)\n" \
  "buffer_store_dwordx4 v[92:95], v249, s[40:43], s60 offen sc1\n" \
  "s_add_u32 s48, s48, 1\n" \
  "v_mov_b32 v56, s48\n" \
  "s_waitcnt lgkmcnt(0)\n" \
  "s_cmp_gt_u32 s48, 57\n" \
  "s_cbranch_scc1 .LV_ef_%=\n" \
  ".LV_ef_b_%=:\n" \
  "s_branch .LV_rot_%=\n" \
  "s_nop 7\n" \
  ".LV_slow_%=:\n" \
  "s_bitcmp1_b32 s70, 0\n" \
  "s_cbranch_scc1 .LV_back_%=\n" \
  "s_waitcnt lgkmcnt(0)\n" \
  "s_cmp_lt_u32 s70, 10\n" \
  "s_nop 3\n" \
  "s_cbranch_scc1 .LV_nol_%=\n" \
  "v_max_u32 v217, v92, v93\n" \
  "v_max3_u32 v217, v217, v94, v95\n" \
  "v_max3_u32 v217, v217, v100, v101\n" \
  "v_max3_u32 v217, v217, v102, v103\n" \
  "v_max3_u32 v217, v217, v104, v105\n" \
  "v_max3_u32 v217, v217, v106, v107\n" \
  "v_max3_u32 v217, v217, v96, v97\n" \
  "v_max3_u32 v217, v217, v98, v99\n" \
  "v_max3_u32 v217, v217, v108, v109\n" \
  "v_max3_u32 v217, v217, v110, v111\n" \
  "v_max3_u32 v217, v217, v112, v113\n" \
  "v_max3_u32 v217, v217, v114, v115\n" \
  "v_max3_u32 v217, v217, v116, v117\n" \
  "v_max3_u32 v217, v217, v118, v119\n" \
  "v_max3_u32 v217, v217, v120, v121\n" \
  "v_max3_u32 v217, v217, v122, v123\n" \
  "v_max3_u32 v217, v217, v124, v125\n" \
  "v_max3_u32 v217, v217, v126, v127\n" \
  "v_cmp_eq_u32 vcc, -1, v217\n" \
  "s_cbranch_vccz .LV_back_%=\n" \
  ".LV_nol_%=:\n" \
  "s_add_u32 s70, s70, 2\n" \
  "s_cmp_gt_u32 s70, 65536\n" \
  "s_cbranch_scc1 .LV_dset_%=\n" \
  "s_barrier\n" \
  "v_mov_b64 v[60:61], 0\n" \
  "v_mov_b64 v[62:63], 0\n" \
  "v_mov_b64 v[64:65], 0\n" \
  "v_mov_b64 v[66:67], 0\n" \
  "v_mov_b64 v[68:69], 0\n" \
  "v_mov_b64 v[70:71], 0\n" \
  "v_mov_b64 v[72:73], 0\n" \
  "v_mov_b64 v[74:75], 0\n" \
  "v_mov_b64 v[76:77], 0\n" \
  "v_mov_b64 v[78:79], 0\n" \
  "v_mov_b64 v[80:81], 0\n" \
  "v_mov_b64 v[82:83], 0\n" \
  "v_mov_b64 v[84:85], 0\n" \
  "v_mov_b64 v[86:87], 0\n" \
  "s_branch .LV_rt_%=\n" \
  ".LV_dset_%=:\n" \
  "v_mov_b32 v112, 1\n" \
  "v_mov_b32 v113, s55\n" \
  "buffer_store_dwordx2 v[112:113], off, s[40:43], %46 offset:128 sc1\n" \
  "s_mov_b32 s70, 1\n" \
  "v_mov_b64 v[60:61], 0\n" \
  "v_mov_b64 v[62:63], 0\n" \
  "v_mov_b64 v[64:65], 0\n" \
  "v_mov_b64 v[66:67], 0\n" \
  "v_mov_b64 v[68:69], 0\n" \
  "v_mov_b64 v[70:71], 0\n" \
  "v_mov_b64 v[72:73], 0\n" \
  "v_mov_b64 v[74:75], 0\n" \
  "v_mov_b64 v[76:77], 0\n" \
  "v_mov_b64 v[78:79], 0\n" \
  "v_mov_b64 v[80:81], 0\n" \
  "v_mov_b64 v[82:83], 0\n" \
  "v_mov_b64 v[84:85], 0\n" \
  "v_mov_b64 v[86:87], 0\n" \
  "s_waitcnt vmcnt(0) lgkmcnt(0)\n" \
  "s_barrier\n" \
  "s_branch .LV_p_%=\n" \
  ".LV_cp_%=:\n" \
  "s_sub_u32 s60, s48, 58\n" \
  "s_lshl_b32 s60, s60, 20\n" \
  "s_add_u32 s60, s60, s47\n" \
  "s_lshr_b32 s62, s60, 8\n" \
  "s_and_b32 s62, s62, 8192\n" \
  "s_xor_b32 s62, s62, s60\n" \
  "buffer_store_dwordx4 v[68:71], v253, s[40:43], s62 offen sc1\n" \
  "buffer_store_dwordx4 v[76:79], v254, s[40:43], s62 offen sc1\n" \
  "buffer_store_dwordx2 v[84:85], v255, s[40:43], s62 offen sc1\n" \
  "s_nop 1\n" \
  "s_branch .LV_cp_b_%=\n" \
  ".LV_ef_%=:\n" \
  "s_cmp_eq_u32 s48, 64\n" \
  "s_cbranch_scc1 .LV_end_%=\n" \
  "s_waitcnt vmcnt(0)\n" \
  "s_barrier\n" \
  "buffer_store_dwordx2 v[56:57], off, s[40:43], %43 sc1\n" \
  "s_branch .LV_ef_b_%=\n" \
  ".LV_end_%=:\n" \
  "s_waitcnt vmcnt(0)\n" \
  "s_waitcnt vmcnt(0)\n" \
  "s_nop 7\n" \
  "s_endpgm\n"
#define V_ASM_VOPS "+{v242}"(ci.sidx), "+{v243}"(ci.l16), "+{v244}"(ci.viw), "+{v245}"(ci.fva), "+{v246}"(ci.pk3), "+{v247}"(ci.fvs), "+{v248}"(ci.sid2), "+{v249}"(ci.uo), "+{v250}"(ci.po0), "+{v251}"(ci.po1), "+{v252}"(ci.po2), "+{v253}"(ci.co0), "+{v254}"(ci.co1), "+{v255}"(ci.co2), "+{v200}"(ci.vq0), "+{v201}"(ci.vq1), "+{v202}"(ci.vq2), "+{v203}"(ci.vq3), "+{v204}"(ci.vo0), "+{v205}"(ci.vo1), "+{v206}"(ci.vo2), "+{v207}"(ci.vo3), "+{v208}"(ci.v50), "+{v209}"(ci.v51), "+{v210}"(ci.v52), "+{v211}"(ci.v53), "+{v212}"(ci.tro0), "+{v213}"(ci.tro1), "+{v214}"(ci.m0o), "+{v215}"(ci.tro2), "+{v216}"(ci.tro3), "+{v217}"(ci.tro4)
#define V_ASM_SOPS "s"(ci.ws), "s"(ci.Wa), "s"(ci.Wb), "s"(ci.mem0), "s"(ci.w), "s"(ci.o_a), "s"(ci.o_b), "s"(ci.o_nx), "s"(ci.o_exp), "s"(ci.lab), "s"(ci.f_p), "s"(ci.f_m), "s"(ci.f_dur), "s"(ci.f_go), "s"(ci.o_ctl), "s"(ci.o_dbg), "s"(ci.dret), "s"(ci.o_qx), "s"(ci.f_s), "s"(ci.f_r)
#define V_ASM_CLOB "v0", "v1", "v2", "v3", "v4", "v5", "v6", "v7", "v8", "v9", "v10", "v11", "v12", "v13", "v14", "v15", "v16", "v17", "v18", "v19", "v20", "v21", "v22", "v23", "v24", "v25", "v26", "v27", "v28", "v29", "v30", "v31", "v32", "v33", "v34", "v35", "v36", "v37", "v38", "v39", "v40", "v41", "v42", "v43", "v44", "v45", "v46", "v47", "v48", "v49", "v50", "v51", "v52", "v53", "v54", "v55", "v56", "v57", "v58", "v59", "v60", "v61", "v62", "v63", "v64", "v65", "v66", "v67", "v68", "v69", "v70", "v71", "v72", "v73", "v74", "v75", "v76", "v77", "v78", "v79", "v80", "v81", "v82", "v83", "v84", "v85", "v86", "v87", "v88", "v89", "v90", "v91", "v92", "v93", "v94", "v95", "v96", "v97", "v98", "v99", "v100", "v101", "v102", "v103", "v104", "v105", "v106", "v107", "v108", "v109", "v110", "v111", "v112", "v113", "v114", "v115", "v116", "v117", "v118", "v119", "v120", "v121", "v122", "v123", "v124", "v125", "v126", "v127", "v128", "v129", "v130", "v131", "v132", "v133", "v134", "v135", "v136", "v137", "v138", "v139", "v140", "v141", "v142", "v143", "v144", "v145", "v146", "v147", "v148", "v149", "v150", "v151", "v152", "v153", "v154", "v155", "v156", "v157", "v158", "v159", "v160", "v161", "v162", "v163", "v164", "v165", "v166", "v167", "v168", "v169", "v170", "v171", "v172", "v173", "v174", "v175", "v176", "v177", "v178", "v179", "v180", "v181", "v182", "v183", "v184", "v185", "v186", "v187", "v188", "v189", "v190", "v191", "v192", "v193", "v194", "v195", "v196", "v197", "v198", "v199", "a0", "a1", "a2", "a3", "a4", "a5", "a6", "a7", "a8", "a9", "a10", "a11", "a12", "a13", "a14", "a15", "a16", "a17", "a18", "a19", "a20", "a21", "a22", "a23", "a24", "a25", "a26", "a27", "a28", "a29", "a30", "a31", "a32", "a33", "a34", "a35", "a36", "a37", "a38", "a39", "a40", "a41", "a42", "a43", "a44", "a45", "a46", "a47", "a48", "a49", "a50", "a51", "a52", "a53", "a54", "a55", "a56", "a57", "a58", "a59", "a60", "a61", "a62", "a63", "a64", "a65", "a66", "a67", "a68", "a69", "a70", "a71", "a72", "a73", "a74", "a75", "a76", "a77", "a78", "a79", "a80", "a81", "a82", "a83", "a84", "a85", "a86", "a87", "a88", "a89", "a90", "a91", "a92", "a93", "a94", "a95", "a96", "a97", "a98", "a99", "a100", "a101", "a102", "a103", "a104", "a105", "a106", "a107", "a108", "a109", "a110", "a111", "a112", "a113", "a114", "a115", "a116", "a117", "a118", "a119", "a120", "a121", "a122", "a123", "a124", "a125", "a126", "a127", "a128", "a129", "a130", "a131", "a132", "a133", "a134", "a135", "a136", "a137", "a138", "a139", "a140", "a141", "a142", "a143", "a144", "a145", "a146", "a147", "a148", "a149", "a150", "a151", "a152", "a153", "a154", "a155", "a156", "a157", "a158", "a159", "a160", "a161", "a162", "a163", "a164", "a165", "a166", "a167", "a168", "a169", "a170", "a171", "a172", "a173", "a174", "a175", "a176", "a177", "a178", "a179", "a180", "a181", "a182", "a183", "a184", "a185", "a186", "a187", "a188", "a189", "a190", "a191", "a192", "a193", "a194", "a195", "a196", "a197", "a198", "a199", "a200", "a201", "a202", "a203", "a204", "a205", "a206", "a207", "a208", "a209", "a210", "a211", "a212", "a213", "a214", "a215", "a216", "a217", "a218", "a219", "a220", "a221", "a222", "a223", "a224", "a225", "a226", "a227", "a228", "a229", "a230", "a231", "a232", "a233", "a234", "a235", "a236", "a237", "a238", "a239", "a240", "a241", "a242", "a243", "a244", "a245", "a246", "a247", "a248", "a249", "a250", "a251", "a252", "a253", "a254", "a255", "s40", "s41", "s42", "s43", "s44", "s45", "s46", "s47", "s48", "s49", "s50", "s51", "s52", "s53", "s54", "s55", "s56", "s57", "s58", "s59", "s60", "s61", "s62", "s63", "s64", "s65", "s66", "s67", "s68", "s69", "s70", "s71", "s72", "s73", "s74", "s75", "s76", "s77", "s78", "s79", "s80", "s81", "s82", "s83", "s84", "s85", "s86", "s87", "s88", "s89", "s90", "s91", "s92", "s93", "s94", "s95", "s96", "s97", "s98", "s99", "vcc", "scc", "memory"
#define W_ASM_TEXT \
  "s_cmp_gt_u32 %36, 0\n" \
  "s_cbranch_scc1 .LW_other_%=\n" \
  "s_mov_b64 s[40:41], %32\n" \
  "s_and_b32 s41, s41, 0xffff\n" \
  "s_mov_b32 s42, 255852544\n" \
  "s_mov_b32 s43, 0x00020000\n" \
  "s_mov_b32 s46, 4718592\n" \
  "s_mov_b32 s47, 0x00020000\n" \
  "s_lshl_b32 s72, %36, 11\n" \
  "buffer_load_dword v196, off, s[40:43], %46 sc1\n" \
  "s_mov_b64 s[44:45], %34\n" \
  "s_and_b32 s45, s45, 0xffff\n" \
  "s_add_u32 s60, %38, 0\n" \
  "s_lshl_b32 s61, s72, 1\n" \
  "s_add_u32 m0, s61, 16384\n" \
  "s_nop 0\n" \
  "buffer_load_dwordx4 v204, s[44:47], s60 offen lds\n" \
  "buffer_load_dwordx4 v205, s[44:47], s60 offen offset:1024 lds\n" \
  "buffer_load_dwordx4 v206, s[44:47], s60 offen offset:2048 lds\n" \
  "buffer_load_dwordx4 v207, s[44:47], s60 offen offset:3072 lds\n" \
  "s_add_u32 s60, %38, 18432\n" \
  "s_lshl_b32 s61, s72, 1\n" \
  "s_add_u32 m0, s61, 32768\n" \
  "s_nop 0\n" \
  "buffer_load_dwordx4 v204, s[44:47], s60 offen lds\n" \
  "buffer_load_dwordx4 v205, s[44:47], s60 offen offset:1024 lds\n" \
  "buffer_load_dwordx4 v206, s[44:47], s60 offen offset:2048 lds\n" \
  "buffer_load_dwordx4 v207, s[44:47], s60 offen offset:3072 lds\n" \
  "s_add_u32 s60, %38, 256\n" \
  "s_lshl_b32 s61, s72, 1\n" \
  "s_add_u32 m0, s61, 49152\n" \
  "s_nop 0\n" \
  "buffer_load_dwordx4 v204, s[44:47], s60 offen lds\n" \
  "buffer_load_dwordx4 v205, s[44:47], s60 offen offset:1024 lds\n" \
  "buffer_load_dwordx4 v206, s[44:47], s60 offen offset:2048 lds\n" \
  "buffer_load_dwordx4 v207, s[44:47], s60 offen offset:3072 lds\n" \
  "s_add_u32 s60, %38, 18688\n" \
  "s_lshl_b32 s61, s72, 1\n" \
  "s_add_u32 m0, s61, 65536\n" \
  "s_nop 0\n" \
  "buffer_load_dwordx4 v204, s[44:47], s60 offen lds\n" \
  "buffer_load_dwordx4 v205, s[44:47], s60 offen offset:1024 lds\n" \
  "buffer_load_dwordx4 v206, s[44:47], s60 offen offset:2048 lds\n" \
  "buffer_load_dwordx4 v207, s[44:47], s60 offen offset:3072 lds\n" \
  "s_add_u32 s60, %38, 512\n" \
  "s_lshl_b32 s61, s72, 1\n" \
  "s_add_u32 m0, s61, 81920\n" \
  "s_nop 0\n" \
  "buffer_load_dwordx4 v204, s[44:47], s60 offen lds\n" \
  "buffer_load_dwordx4 v205, s[44:47], s60 offen offset:1024 lds\n" \
  "buffer_load_dwordx4 v206, s[44:47], s60 offen offset:2048 lds\n" \
  "buffer_load_dwordx4 v207, s[44:47], s60 offen offset:3072 lds\n" \
  "s_add_u32 s60, %38, 18944\n" \
  "s_lshl_b32 s61, s72, 1\n" \
  "s_add_u32 m0, s61, 98304\n" \
  "s_nop 0\n" \
  "buffer_load_dwordx4 v204, s[44:47], s60 offen lds\n" \
  "buffer_load_dwordx4 v205, s[44:47], s60 offen offset:1024 lds\n" \
  "buffer_load_dwordx4 v206, s[44:47], s60 offen offset:2048 lds\n" \
  "buffer_load_dwordx4 v207, s[44:47], s60 offen offset:3072 lds\n" \
  "s_add_u32 s60, %38, 768\n" \
  "s_lshl_b32 s61, s72, 1\n" \
  "s_add_u32 m0, s61, 114688\n" \
  "s_nop 0\n" \
  "buffer_load_dwordx4 v204, s[44:47], s60 offen lds\n" \
  "buffer_load_dwordx4 v205, s[44:47], s60 offen offset:1024 lds\n" \
  "buffer_load_dwordx4 v206, s[44:47], s60 offen offset:2048 lds\n" \
  "buffer_load_dwordx4 v207, s[44:47], s60 offen offset:3072 lds\n" \
  "s_add_u32 s60, %38, 19200\n" \
  "s_lshl_b32 s61, s72, 1\n" \
  "s_add_u32 m0, s61, 131072\n" \
  "s_nop 0\n" \
  "buffer_load_dwordx4 v204, s[44:47], s60 offen lds\n" \
  "buffer_load_dwordx4 v205, s[44:47], s60 offen offset:1024 lds\n" \
  "buffer_load_dwordx4 v206, s[44:47], s60 offen offset:2048 lds\n" \
  "buffer_load_dwordx4 v207, s[44:47], s60 offen offset:3072 lds\n" \
  "s_mov_b32 s60, %38\n" \
  "s_lshl_b32 s61, s72, 1\n" \
  "s_add_u32 m0, s61, 147456\n" \
  "s_nop 0\n" \
  "buffer_load_dwordx4 v208, s[44:47], s60 offen lds\n" \
  "buffer_load_dwordx4 v209, s[44:47], s60 offen offset:1024 lds\n" \
  "buffer_load_dwordx4 v210, s[44:47], s60 offen offset:2048 lds\n" \
  "buffer_load_dwordx4 v211, s[44:47], s60 offen offset:3072 lds\n" \
  "s_mov_b64 s[44:45], %35\n" \
  "s_and_b32 s45, s45, 0xffff\n" \
  "s_lshr_b32 s60, s72, 3\n" \
  "s_mov_b32 m0, s72\n" \
  "s_nop 0\n" \
  "buffer_load_dwordx4 v214, s[44:47], s60 offen lds\n" \
  "s_add_u32 s60, s60, 128\n" \
  "s_add_u32 m0, s72, 1024\n" \
  "s_nop 0\n" \
  "buffer_load_dwordx4 v214, s[44:47], s60 offen lds\n" \
  "s_movk_i32 s60, 1024\n" \
  "s_movk_i32 m0, 8192\n" \
  "s_nop 0\n" \
  "buffer_load_dwordx4 v214, s[44:47], s60 offen lds\n" \
  "s_waitcnt vmcnt(27)\n" \
  "s_barrier\n" \
  "v_add_u32 v194, 16384, v215\n" \
  "v_xor_b32 v195, 2176, v194\n" \
  "ds_read_b64_tr_b16 a[144:145], v194\n" \
  "ds_read_b64_tr_b16 a[146:147], v194 offset:512\n" \
  "ds_read_b64_tr_b16 a[148:149], v195\n" \
  "ds_read_b64_tr_b16 a[150:151], v195 offset:512\n" \
  "ds_read_b64_tr_b16 a[152:153], v194 offset:8192\n" \
  "ds_read_b64_tr_b16 a[154:155], v194 offset:8704\n" \
  "ds_read_b64_tr_b16 a[156:157], v195 offset:8192\n" \
  "ds_read_b64_tr_b16 a[158:159], v195 offset:8704\n" \
  "v_add_u32 v194, 32768, v215\n" \
  "v_xor_b32 v195, 2176, v194\n" \
  "ds_read_b64_tr_b16 a[160:161], v194\n" \
  "ds_read_b64_tr_b16 a[162:163], v194 offset:512\n" \
  "ds_read_b64_tr_b16 a[164:165], v195\n" \
  "ds_read_b64_tr_b16 a[166:167], v195 offset:512\n" \
  "ds_read_b64_tr_b16 a[168:169], v194 offset:8192\n" \
  "ds_read_b64_tr_b16 a[170:171], v194 offset:8704\n" \
  "ds_read_b64_tr_b16 a[172:173], v195 offset:8192\n" \
  "ds_read_b64_tr_b16 a[174:175], v195 offset:8704\n" \
  "v_add_u32 v194, 49152, v215\n" \
  "v_xor_b32 v195, 2176, v194\n" \
  "ds_read_b64_tr_b16 a[176:177], v194\n" \
  "ds_read_b64_tr_b16 a[178:179], v194 offset:512\n" \
  "ds_read_b64_tr_b16 a[180:181], v195\n" \
  "ds_read_b64_tr_b16 a[182:183], v195 offset:512\n" \
  "ds_read_b64_tr_b16 a[184:185], v194 offset:8192\n" \
  "ds_read_b64_tr_b16 a[186:187], v194 offset:8704\n" \
  "ds_read_b64_tr_b16 a[188:189], v195 offset:8192\n" \
  "ds_read_b64_tr_b16 a[190:191], v195 offset:8704\n" \
  "s_waitcnt lgkmcnt(0)\n" \
  "s_barrier\n" \
  "s_mov_b64 s[44:45], %33\n" \
  "s_and_b32 s45, s45, 0xffff\n" \
  "s_add_u32 s60, %37, 0\n" \
  "s_lshl_b32 s61, s72, 1\n" \
  "s_add_u32 m0, s61, 16384\n" \
  "s_nop 0\n" \
  "buffer_load_dwordx4 v200, s[44:47], s60 offen lds\n" \
  "buffer_load_dwordx4 v201, s[44:47], s60 offen offset:1024 lds\n" \
  "buffer_load_dwordx4 v202, s[44:47], s60 offen offset:2048 lds\n" \
  "buffer_load_dwordx4 v203, s[44:47], s60 offen offset:3072 lds\n" \
  "s_add_u32 s60, %37, 524288\n" \
  "s_lshl_b32 s61, s72, 1\n" \
  "s_add_u32 m0, s61, 32768\n" \
  "s_nop 0\n" \
  "buffer_load_dwordx4 v200, s[44:47], s60 offen lds\n" \
  "buffer_load_dwordx4 v201, s[44:47], s60 offen offset:1024 lds\n" \
  "buffer_load_dwordx4 v202, s[44:47], s60 offen offset:2048 lds\n" \
  "buffer_load_dwordx4 v203, s[44:47], s60 offen offset:3072 lds\n" \
  "s_add_u32 s60, %37, 1048576\n" \
  "s_lshl_b32 s61, s72, 1\n" \
  "s_add_u32 m0, s61, 49152\n" \
  "s_nop 0\n" \
  "buffer_load_dwordx4 v200, s[44:47], s60 offen lds\n" \
  "buffer_load_dwordx4 v201, s[44:47], s60 offen offset:1024 lds\n" \
  "buffer_load_dwordx4 v202, s[44:47], s60 offen offset:2048 lds\n" \
  "buffer_load_dwordx4 v203, s[44:47], s60 offen offset:3072 lds\n" \
  "v_readfirstlane_b32 s55, v196\n" \
  "s_add_u32 s55, s55, 1\n" \
  "s_waitcnt vmcnt(27)\n" \
  "s_barrier\n" \
  "v_add_u32 v194, 65536, v215\n" \
  "v_xor_b32 v195, 2176, v194\n" \
  "ds_read_b64_tr_b16 a[192:193], v194\n" \
  "ds_read_b64_tr_b16 a[194:195], v194 offset:512\n" \
  "ds_read_b64_tr_b16 a[196:197], v195\n" \
  "ds_read_b64_tr_b16 a[198:199], v195 offset:512\n" \
  "ds_read_b64_tr_b16 a[200:201], v194 offset:8192\n" \
  "ds_read_b64_tr_b16 a[202:203], v194 offset:8704\n" \
  "ds_read_b64_tr_b16 a[204:205], v195 offset:8192\n" \
  "ds_read_b64_tr_b16 a[206:207], v195 offset:8704\n" \
  "v_add_u32 v194, 81920, v215\n" \
  "v_xor_b32 v195, 2176, v194\n" \
  "ds_read_b64_tr_b16 a[208:209], v194\n" \
  "ds_read_b64_tr_b16 a[210:211], v194 offset:512\n" \
  "ds_read_b64_tr_b16 a[212:213], v195\n" \
  "ds_read_b64_tr_b16 a[214:215], v195 offset:512\n" \
  "ds_read_b64_tr_b16 a[216:217], v194 offset:8192\n" \
  "ds_read_b64_tr_b16 a[218:219], v194 offset:8704\n" \
  "ds_read_b64_tr_b16 a[220:221], v195 offset:8192\n" \
  "ds_read_b64_tr_b16 a[222:223], v195 offset:8704\n" \
  "v_add_u32 v194, 98304, v215\n" \
  "v_xor_b32 v195, 2176, v194\n" \
  "ds_read_b64_tr_b16 a[224:225], v194\n" \
  "ds_read_b64_tr_b16 a[226:227], v194 offset:512\n" \
  "ds_read_b64_tr_b16 a[228:229], v195\n" \
  "ds_read_b64_tr_b16 a[230:231], v195 offset:512\n" \
  "ds_read_b64_tr_b16 a[232:233], v194 offset:8192\n" \
  "ds_read_b64_tr_b16 a[234:235], v194 offset:8704\n" \
  "ds_read_b64_tr_b16 a[236:237], v195 offset:8192\n" \
  "ds_read_b64_tr_b16 a[238:239], v195 offset:8704\n" \
  "s_waitcnt lgkmcnt(0)\n" \
  "s_barrier\n" \
  "s_add_u32 s60, %37, 1572864\n" \
  "s_lshl_b32 s61, s72, 1\n" \
  "s_add_u32 m0, s61, 65536\n" \
  "s_nop 0\n" \
  "buffer_load_dwordx4 v200, s[44:47], s60 offen lds\n" \
  "buffer_load_dwordx4 v201, s[44:47], s60 offen offset:1024 lds\n" \
  "buffer_load_dwordx4 v202, s[44:47], s60 offen offset:2048 lds\n" \
  "buffer_load_dwordx4 v203, s[44:47], s60 offen offset:3072 lds\n" \
  "s_add_u32 s60, %37, 2097152\n" \
  "s_lshl_b32 s61, s72, 1\n" \
  "s_add_u32 m0, s61, 81920\n" \
  "s_nop 0\n" \
  "buffer_load_dwordx4 v200, s[44:47], s60 offen lds\n" \
  "buffer_load_dwordx4 v201, s[44:47], s60 offen offset:1024 lds\n" \
  "buffer_load_dwordx4 v202, s[44:47], s60 offen offset:2048 lds\n" \
  "buffer_load_dwordx4 v203, s[44:47], s60 offen offset:3072 lds\n" \
  "s_add_u32 s60, %37, 2621440\n" \
  "s_lshl_b32 s61, s72, 1\n" \
  "s_add_u32 m0, s61, 98304\n" \
  "s_nop 0\n" \
  "buffer_load_dwordx4 v200, s[44:47], s60 offen lds\n" \
  "buffer_load_dwordx4 v201, s[44:47], s60 offen offset:1024 lds\n" \
  "buffer_load_dwordx4 v202, s[44:47], s60 offen offset:2048 lds\n" \
  "buffer_load_dwordx4 v203, s[44:47], s60 offen offset:3072 lds\n" \
  "s_waitcnt vmcnt(27)\n" \
  "s_barrier\n" \
  "s_waitcnt lgkmcnt(0)\n" \
  "s_barrier\n" \
  "s_add_u32 s60, %37, 3145728\n" \
  "s_lshl_b32 s61, s72, 1\n" \
  "s_add_u32 m0, s61, 114688\n" \
  "s_nop 0\n" \
  "buffer_load_dwordx4 v200, s[44:47], s60 offen lds\n" \
  "buffer_load_dwordx4 v201, s[44:47], s60 offen offset:1024 lds\n" \
  "buffer_load_dwordx4 v202, s[44:47], s60 offen offset:2048 lds\n" \
  "buffer_load_dwordx4 v203, s[44:47], s60 offen offset:3072 lds\n" \
  "s_add_u32 s60, %37, 3670016\n" \
  "s_lshl_b32 s61, s72, 1\n" \
  "s_add_u32 m0, s61, 131072\n" \
  "s_nop 0\n" \
  "buffer_load_dwordx4 v200, s[44:47], s60 offen lds\n" \
  "buffer_load_dwordx4 v201, s[44:47], s60 offen offset:1024 lds\n" \
  "buffer_load_dwordx4 v202, s[44:47], s60 offen offset:2048 lds\n" \
  "buffer_load_dwordx4 v203, s[44:47], s60 offen offset:3072 lds\n" \
  "s_add_u32 s60, %37, 4194304\n" \
  "s_lshl_b32 s61, s72, 1\n" \
  "s_add_u32 m0, s61, 147456\n" \
  "s_nop 0\n" \
  "buffer_load_dwordx4 v200, s[44:47], s60 offen lds\n" \
  "buffer_load_dwordx4 v201, s[44:47], s60 offen offset:1024 lds\n" \
  "buffer_load_dwordx4 v202, s[44:47], s60 offen offset:2048 lds\n" \
  "buffer_load_dwordx4 v203, s[44:47], s60 offen offset:3072 lds\n" \
  "buffer_load_dwordx2 v[198:199], off, s[40:43], %45 sc1\n" \
  "s_waitcnt vmcnt(25)\n" \
  "s_barrier\n" \
  "v_add_u32 v194, 16384, v212\n" \
  "ds_read_b64_tr_b16 a[0:1], v194\n" \
  "ds_read_b64_tr_b16 a[2:3], v194 offset:512\n" \
  "ds_read_b64_tr_b16 a[4:5], v194 offset:8192\n" \
  "ds_read_b64_tr_b16 a[6:7], v194 offset:8704\n" \
  "v_add_u32 v195, 16384, v213\n" \
  "ds_read_b64_tr_b16 a[72:73], v195\n" \
  "ds_read_b64_tr_b16 a[74:75], v195 offset:512\n" \
  "ds_read_b64_tr_b16 a[76:77], v195 offset:8192\n" \
  "ds_read_b64_tr_b16 a[78:79], v195 offset:8704\n" \
  "v_add_u32 v194, 32768, v212\n" \
  "ds_read_b64_tr_b16 a[8:9], v194\n" \
  "ds_read_b64_tr_b16 a[10:11], v194 offset:512\n" \
  "ds_read_b64_tr_b16 a[12:13], v194 offset:8192\n" \
  "ds_read_b64_tr_b16 a[14:15], v194 offset:8704\n" \
  "v_add_u32 v195, 32768, v213\n" \
  "ds_read_b64_tr_b16 a[80:81], v195\n" \
  "ds_read_b64_tr_b16 a[82:83], v195 offset:512\n" \
  "ds_read_b64_tr_b16 a[84:85], v195 offset:8192\n" \
  "ds_read_b64_tr_b16 a[86:87], v195 offset:8704\n" \
  "v_add_u32 v194, 49152, v212\n" \
  "ds_read_b64_tr_b16 a[16:17], v194\n" \
  "ds_read_b64_tr_b16 a[18:19], v194 offset:512\n" \
  "ds_read_b64_tr_b16 a[20:21], v194 offset:8192\n" \
  "ds_read_b64_tr_b16 a[22:23], v194 offset:8704\n" \
  "v_add_u32 v195, 49152, v213\n" \
  "ds_read_b64_tr_b16 a[88:89], v195\n" \
  "ds_read_b64_tr_b16 a[90:91], v195 offset:512\n" \
  "ds_read_b64_tr_b16 a[92:93], v195 offset:8192\n" \
  "ds_read_b64_tr_b16 a[94:95], v195 offset:8704\n" \
  "s_waitcnt lgkmcnt(0)\n" \
  "s_waitcnt vmcnt(13)\n" \
  "s_barrier\n" \
  "v_add_u32 v194, 65536, v212\n" \
  "ds_read_b64_tr_b16 a[24:25], v194\n" \
  "ds_read_b64_tr_b16 a[26:27], v194 offset:512\n" \
  "ds_read_b64_tr_b16 a[28:29], v194 offset:8192\n" \
  "ds_read_b64_tr_b16 a[30:31], v194 offset:8704\n" \
  "v_add_u32 v195, 65536, v213\n" \
  "ds_read_b64_tr_b16 a[96:97], v195\n" \
  "ds_read_b64_tr_b16 a[98:99], v195 offset:512\n" \
  "ds_read_b64_tr_b16 a[100:101], v195 offset:8192\n" \
  "ds_read_b64_tr_b16 a[102:103], v195 offset:8704\n" \
  "v_add_u32 v194, 81920, v212\n" \
  "ds_read_b64_tr_b16 a[32:33], v194\n" \
  "ds_read_b64_tr_b16 a[34:35], v194 offset:512\n" \
  "ds_read_b64_tr_b16 a[36:37], v194 offset:8192\n" \
  "ds_read_b64_tr_b16 a[38:39], v194 offset:8704\n" \
  "v_add_u32 v195, 81920, v213\n" \
  "ds_read_b64_tr_b16 a[104:105], v195\n" \
  "ds_read_b64_tr_b16 a[106:107], v195 offset:512\n" \
  "ds_read_b64_tr_b16 a[108:109], v195 offset:8192\n" \
  "ds_read_b64_tr_b16 a[110:111], v195 offset:8704\n" \
  "v_add_u32 v194, 98304, v212\n" \
  "ds_read_b64_tr_b16 a[40:41], v194\n" \
  "ds_read_b64_tr_b16 a[42:43], v194 offset:512\n" \
  "ds_read_b64_tr_b16 a[44:45], v194 offset:8192\n" \
  "ds_read_b64_tr_b16 a[46:47], v194 offset:8704\n" \
  "v_add_u32 v195, 98304, v213\n" \
  "ds_read_b64_tr_b16 a[112:113], v195\n" \
  "ds_read_b64_tr_b16 a[114:115], v195 offset:512\n" \
  "ds_read_b64_tr_b16 a[116:117], v195 offset:8192\n" \
  "ds_read_b64_tr_b16 a[118:119], v195 offset:8704\n" \
  "s_waitcnt lgkmcnt(0)\n" \
  "s_waitcnt vmcnt(1)\n" \
  "s_barrier\n" \
  "v_add_u32 v194, 114688, v212\n" \
  "ds_read_b64_tr_b16 a[48:49], v194\n" \
  "ds_read_b64_tr_b16 a[50:51], v194 offset:512\n" \
  "ds_read_b64_tr_b16 a[52:53], v194 offset:8192\n" \
  "ds_read_b64_tr_b16 a[54:55], v194 offset:8704\n" \
  "v_add_u32 v195, 114688, v213\n" \
  "ds_read_b64_tr_b16 a[120:121], v195\n" \
  "ds_read_b64_tr_b16 a[122:123], v195 offset:512\n" \
  "ds_read_b64_tr_b16 a[124:125], v195 offset:8192\n" \
  "ds_read_b64_tr_b16 a[126:127], v195 offset:8704\n" \
  "v_add_u32 v194, 131072, v212\n" \
  "ds_read_b64_tr_b16 a[56:57], v194\n" \
  "ds_read_b64_tr_b16 a[58:59], v194 offset:512\n" \
  "ds_read_b64_tr_b16 a[60:61], v194 offset:8192\n" \
  "ds_read_b64_tr_b16 a[62:63], v194 offset:8704\n" \
  "v_add_u32 v195, 131072, v213\n" \
  "ds_read_b64_tr_b16 a[128:129], v195\n" \
  "ds_read_b64_tr_b16 a[130:131], v195 offset:512\n" \
  "ds_read_b64_tr_b16 a[132:133], v195 offset:8192\n" \
  "ds_read_b64_tr_b16 a[134:135], v195 offset:8704\n" \
  "v_add_u32 v194, 147456, v212\n" \
  "ds_read_b64_tr_b16 a[64:65], v194\n" \
  "ds_read_b64_tr_b16 a[66:67], v194 offset:512\n" \
  "ds_read_b64_tr_b16 a[68:69], v194 offset:8192\n" \
  "ds_read_b64_tr_b16 a[70:71], v194 offset:8704\n" \
  "v_add_u32 v195, 147456, v213\n" \
  "ds_read_b64_tr_b16 a[136:137], v195\n" \
  "ds_read_b64_tr_b16 a[138:139], v195 offset:512\n" \
  "ds_read_b64_tr_b16 a[140:141], v195 offset:8192\n" \
  "ds_read_b64_tr_b16 a[142:143], v195 offset:8704\n" \
  "s_waitcnt lgkmcnt(0)\n" \
  "s_waitcnt vmcnt(0)\n" \
  "v_readfirstlane_b32 s96, v198\n" \
  "v_readfirstlane_b32 s63, v199\n" \
  "s_cmp_eq_u32 s63, s55\n" \
  "s_cselect_b32 s96, s96, 0\n" \
  "s_cmp_ge_u32 s96, 0xc0000000\n" \
  "s_cbranch_scc1 .LW_go_x_%=\n" \
  "s_mov_b32 s69, 0\n" \
  ".LW_go_%=:\n" \
  "buffer_load_dwordx2 v[196:197], off, s[40:43], %45 sc1\n" \
  "s_waitcnt vmcnt(0)\n" \
  "v_readfirstlane_b32 s96, v196\n" \
  "v_readfirstlane_b32 s63, v197\n" \
  "s_cmp_eq_u32 s63, s55\n" \
  "s_cselect_b32 s96, s96, 0\n" \
  "s_cmp_ge_u32 s96, 0xc0000000\n" \
  "s_cbranch_scc1 .LW_go_x_%=\n" \
  "s_sleep 8\n" \
  "s_add_u32 s69, s69, 1\n" \
  "s_and_b32 s63, s69, 1023\n" \
  "s_cbranch_scc1 .LW_go_%=\n" \
  "s_cmp_gt_u32 s69, 2097152\n" \
  "s_cbranch_scc1 .LW_go_set_%=\n" \
  "buffer_load_dwordx2 v[196:197], off, s[40:43], %46 offset:128 sc1\n" \
  "s_waitcnt vmcnt(0)\n" \
  "v_readfirstlane_b32 s63, v197\n" \
  "s_cmp_eq_u32 s63, s55\n" \
  "s_cbranch_scc0 .LW_go_%=\n" \
  "s_branch .LW_go_x_%=\n" \
  ".LW_go_set_%=:\n" \
  "v_mov_b32 v196, 1\n" \
  "v_mov_b32 v197, s55\n" \
  "buffer_store_dwordx2 v[196:197], off, s[40:43], %46 offset:128 sc1\n" \
  "s_waitcnt vmcnt(0)\n" \
  ".LW_go_x_%=:\n" \
  "s_and_b32 s60, s96, 2\n" \
  "s_lshl_b32 s60, s60, 12\n" \
  "s_xor_b32 s74, %42, s60\n" \
  "s_xor_b32 s75, %44, s60\n" \
  "s_bfe_u32 s56, s96, 0x30010\n" \
  "s_lshl_b32 s56, s56, 12\n" \
  "s_or_b32 s56, s56, 0x200000\n" \
  "s_mov_b32 s57, 1052672\n" \
  "s_lshl_b32 s61, %41, 2\n" \
  "s_lshr_b32 s51, s96, s61\n" \
  "s_and_b32 s51, s51, 7\n" \
  "s_lshl_b32 s51, s51, 12\n" \
  "s_xor_b32 s51, s51, 4210688\n" \
  "s_mov_b32 s71, 0\n" \
  "s_add_u32 s45, s51, 886784\n" \
  "s_mov_b32 s46, -1\n" \
  "v_mov_b64 v[48:49], -1\n" \
  "v_mov_b64 v[50:51], -1\n" \
  "v_and_b32 v194, 3, v250\n" \
  "v_lshlrev_b32 v194, 2, v194\n" \
  "v_lshrrev_b32 v194, v194, s96\n" \
  "v_and_b32 v194, 7, v194\n" \
  "v_lshlrev_b32 v194, 12, v194\n" \
  "v_and_b32 v250, -4, v250\n" \
  "v_xor_b32 v250, v250, v194\n" \
  "v_and_b32 v194, 3, v252\n" \
  "v_lshlrev_b32 v194, 2, v194\n" \
  "v_lshrrev_b32 v194, v194, s96\n" \
  "v_and_b32 v194, 7, v194\n" \
  "v_lshlrev_b32 v194, 12, v194\n" \
  "v_and_b32 v252, -4, v252\n" \
  "v_xor_b32 v252, v252, v194\n" \
  "v_and_b32 v194, 3, v251\n" \
  "v_lshlrev_b32 v194, 2, v194\n" \
  "v_lshrrev_b32 v194, v194, s96\n" \
  "v_and_b32 v194, 7, v194\n" \
  "v_lshlrev_b32 v194, 12, v194\n" \
  "v_and_b32 v251, -4, v251\n" \
  "v_xor_b32 v251, v251, v194\n" \
  "v_xor_b32 v185, s56, v250\n" \
  "buffer_store_dwordx4 v[48:51], v185, s[40:43], 0 offen sc1\n" \
  "v_xor_b32 v186, s56, v252\n" \
  "buffer_store_dwordx2 v[48:49], v186, s[40:43], 0 offen sc1\n" \
  "s_xor_b32 s62, s56, s57\n" \
  "s_nop 1\n" \
  "v_xor_b32 v185, s62, v250\n" \
  "buffer_store_dwordx4 v[48:51], v185, s[40:43], 0 offen sc1\n" \
  "v_xor_b32 v186, s62, v252\n" \
  "buffer_store_dwordx2 v[48:49], v186, s[40:43], 0 offen sc1\n" \
  "s_andn2_b32 s60, 2, s96\n" \
  "s_lshl_b32 s60, s60, 12\n" \
  "s_xor_b32 s60, s60, 8192\n" \
  "s_xor_b32 s58, %39, s60\n" \
  "s_mov_b32 s49, 0\n" \
  "s_add_u32 s47, %49, s60\n" \
  "s_add_u32 s97, s60, 8388608\n" \
  "s_xor_b32 s50, %40, s60\n" \
  "s_mov_b32 s48, 0\n" \
  "s_mov_b32 s54, 0\n" \
  "s_cmp_eq_u32 %36, 0\n" \
  "s_cselect_b64 s[88:89], -1, 0\n" \
  "s_mov_b32 s68, 0\n" \
  "s_mov_b32 s70, 1\n" \
  "s_mov_b32 s73, 0\n" \
  "s_mov_b32 s44, 0\n" \
  "s_mov_b32 s53, 0\n" \
  "s_memrealtime s[86:87]\n" \
  "s_memtime s[76:77]\n" \
  "s_waitcnt lgkmcnt(0)\n" \
  "s_mov_b32 s90, s76\n" \
  "s_mov_b32 s87, 340\n" \
  "s_mov_b32 s64, 0x3e0293ee\n" \
  "s_mov_b32 s65, 0xbe0293ee\n" \
  "v_mov_b64 v[60:61], 0\n" \
  "v_mov_b64 v[62:63], 0\n" \
  "v_mov_b64 v[64:65], 0\n" \
  "v_mov_b64 v[66:67], 0\n" \
  "v_mov_b64 v[68:69], 0\n" \
  "v_mov_b64 v[70:71], 0\n" \
  "v_mov_b64 v[72:73], 0\n" \
  "v_mov_b64 v[74:75], 0\n" \
  "v_mov_b64 v[76:77], 0\n" \
  "v_mov_b64 v[78:79], 0\n" \
  "v_mov_b32 v56, 0\n" \
  "v_mov_b32 v57, s55\n" \
  "v_mov_b64 v[58:59], v[56:57]\n" \
  "v_mov_b64 v[82:83], 0\n" \
  "s_mov_b32 s67, 2130706432\n" \
  "s_waitcnt vmcnt(0) lgkmcnt(0)\n" \
  "s_barrier\n" \
  "s_branch .LW_p_%=\n" \
  "s_nop 7\n" \
  ".p2align 6\n" \
  ".fill (((40 - (.LW_p_%= - .LW_rot_%=)) & 63) >> 2), 4, 0xBF800000\n" \
  ".LW_rot_%=:\n" \
  "s_xor_b32 s51, s51, s56\n" \
  "s_xor_b32 s71, s71, s56\n" \
  "v_xor_b32 v250, s56, v250\n" \
  "v_xor_b32 v252, s56, v252\n" \
  "s_mov_b32 s60, s56\n" \
  "s_mov_b32 s56, s57\n" \
  "s_mov_b32 s57, s60\n" \
  "s_add_u32 s59, s51, 819200\n" \
  "s_mov_b32 m0, 0\n" \
  "s_mov_b32 s70, 0\n" \
  "s_mov_b32 s68, 0\n" \
  "s_waitcnt lgkmcnt(0)\n" \
  "s_mov_b32 s69, 0\n" \
  ".LW_tw_%=:\n" \
  "v_readfirstlane_b32 s63, v52\n" \
  "s_cmp_lg_u32 s63, s48\n" \
  "s_cbranch_scc1 .LW_tw_re_%=\n" \
  "v_readfirstlane_b32 s53, v54\n" \
  ".LW_rt_%=:\n" \
  "s_memrealtime s[60:61]\n" \
  "s_add_u32 s73, s53, s44\n" \
  "s_waitcnt lgkmcnt(0)\n" \
  "s_sub_u32 s62, s73, s60\n" \
  "s_cmp_ge_u32 s62, 2000\n" \
  "s_cbranch_scc1 .LW_lat_%=\n" \
  "s_sub_u32 s62, s62, 30\n" \
  ".LW_slp_%=:\n" \
  "s_cmp_le_i32 s62, 0\n" \
  "s_cbranch_scc1 .LW_clk_%=\n" \
  "s_sleep 1\n" \
  "s_sub_u32 s62, s62, 3\n" \
  "s_branch .LW_slp_%=\n" \
  ".LW_clk_%=:\n" \
  "s_memrealtime s[60:61]\n" \
  "s_waitcnt lgkmcnt(0)\n" \
  "s_sub_u32 s62, s73, s60\n" \
  "s_cmp_gt_i32 s62, 21\n" \
  "s_cbranch_scc1 .LW_clk_%=\n" \
  "s_cmp_le_i32 s62, 11\n" \
  "s_cbranch_scc1 .LW_clk2_%=\n" \
  "s_memrealtime s[76:77]\n" \
  "s_nop 4\n" \
  "s_memrealtime s[78:79]\n" \
  "s_nop 4\n" \
  "s_memrealtime s[80:81]\n" \
  "s_nop 4\n" \
  "s_memrealtime s[82:83]\n" \
  "s_waitcnt lgkmcnt(0)\n" \
  "s_mov_b32 s77, 1\n" \
  "s_cmp_eq_u32 s78, s76\n" \
  "s_addc_u32 s77, s77, 0\n" \
  "s_cmp_eq_u32 s80, s76\n" \
  "s_addc_u32 s77, s77, 0\n" \
  "s_cmp_eq_u32 s82, s76\n" \
  "s_addc_u32 s77, s77, 0\n" \
  "s_mul_i32 s77, s77, 24\n" \
  "s_lshr_b32 s78, s87, 2\n" \
  "s_sub_u32 s77, s78, s77\n" \
  "s_max_i32 s77, s77, 0\n" \
  "s_sub_u32 s76, s73, s76\n" \
  "s_mul_i32 s76, s76, s87\n" \
  "s_ashr_i32 s76, s76, 4\n" \
  "s_sub_u32 s76, s76, s77\n" \
  "s_sub_u32 s76, s76, 200\n" \
  "s_max_i32 s76, s76, 0\n" \
  "s_min_i32 s76, s76, 320\n" \
  "s_lshr_b32 s76, s76, 3\n" \
  "s_lshl_b32 s76, s76, 2\n" \
  "s_mov_b32 s68, 1\n" \
  "s_getpc_b64 s[78:79]\n" \
  "s_sub_u32 s76, 180, s76\n" \
  "s_add_u32 s78, s78, s76\n" \
  "s_addc_u32 s79, s79, 0\n" \
  "s_setpc_b64 s[78:79]\n" \
  "s_nop 1\n" \
  "s_nop 1\n" \
  "s_nop 1\n" \
  "s_nop 1\n" \
  "s_nop 1\n" \
  "s_nop 1\n" \
  "s_nop 1\n" \
  "s_nop 1\n" \
  "s_nop 1\n" \
  "s_nop 1\n" \
  "s_nop 1\n" \
  "s_nop 1\n" \
  "s_nop 1\n" \
  "s_nop 1\n" \
  "s_nop 1\n" \
  "s_nop 1\n" \
  "s_nop 1\n" \
  "s_nop 1\n" \
  "s_nop 1\n" \
  "s_nop 1\n" \
  "s_nop 1\n" \
  "s_nop 1\n" \
  "s_nop 1\n" \
  "s_nop 1\n" \
  "s_nop 1\n" \
  "s_nop 1\n" \
  "s_nop 1\n" \
  "s_nop 1\n" \
  "s_nop 1\n" \
  "s_nop 1\n" \
  "s_nop 1\n" \
  "s_nop 1\n" \
  "s_nop 1\n" \
  "s_nop 1\n" \
  "s_nop 1\n" \
  "s_nop 1\n" \
  "s_nop 1\n" \
  "s_nop 1\n" \
  "s_nop 1\n" \
  "s_nop 1\n" \
  ".LW_noclk_%=:\n" \
  "buffer_load_dwordx4 v243, s[40:43], s59 offen sc1 lds\n" \
  "buffer_load_dwordx4 v243, s[40:43], s59 offen offset:1024 sc1 lds\n" \
  "buffer_load_dwordx4 v243, s[40:43], s59 offen offset:2048 sc1 lds\n" \
  "buffer_load_dwordx4 v243, s[40:43], s59 offen offset:3072 sc1 lds\n" \
  "s_add_u32 s60, s59, 32768\n" \
  "s_movk_i32 m0, 4096\n" \
  "s_nop 0\n" \
  "buffer_load_dwordx4 v243, s[40:43], s60 offen sc1 lds\n" \
  "buffer_load_dwordx4 v243, s[40:43], s60 offen offset:1024 sc1 lds\n" \
  "buffer_load_dwordx4 v243, s[40:43], s60 offen offset:2048 sc1 lds\n" \
  "buffer_load_dwordx4 v243, s[40:43], s60 offen offset:3072 sc1 lds\n" \
  "s_add_u32 s60, s59, 65536\n" \
  "s_movk_i32 m0, 8192\n" \
  "s_nop 0\n" \
  "buffer_load_dwordx4 v243, s[40:43], s60 offen sc1 lds\n" \
  ".LW_g3_%=:\n" \
  "s_waitcnt vmcnt(5)\n" \
  "s_barrier\n" \
  ".LW_p_%=:\n" \
  "ds_read_b128 v[84:87], v243\n" \
  "ds_read_b128 v[88:91], v243 offset:1024\n" \
  "ds_read_b128 v[92:95], v243 offset:2048\n" \
  "ds_read_b128 v[96:99], v243 offset:3072\n" \
  "s_waitcnt lgkmcnt(3)\n" \
  "v_smfmac_f32_16x16x64_bf16 v[60:63], v[84:87], a[0:7], v242\n" \
  "s_waitcnt vmcnt(0)\n" \
  "s_barrier\n" \
  "ds_read_b128 v[100:103], v243 offset:5120\n" \
  "ds_read_b128 v[104:107], v243 offset:4096\n" \
  "s_waitcnt lgkmcnt(4)\n" \
  "v_smfmac_f32_16x16x64_bf16 v[60:63], v[88:91], a[8:15], v242\n" \
  "ds_read_b128 v[108:111], v243 offset:7168\n" \
  "s_waitcnt lgkmcnt(4)\n" \
  "v_smfmac_f32_16x16x64_bf16 v[60:63], v[92:95], a[16:23], v242\n" \
  "ds_read_b128 v[112:115], v243 offset:6144\n" \
  "v_smfmac_f32_16x16x64_bf16 v[64:67], v[84:87], a[72:79], v242\n" \
  "ds_read_b128 v[116:119], v243 offset:8192\n" \
  "s_waitcnt lgkmcnt(5)\n" \
  "v_smfmac_f32_16x16x64_bf16 v[60:63], v[96:99], a[24:31], v242\n" \
  "s_waitcnt lgkmcnt(3)\n" \
  "v_smfmac_f32_16x16x64_bf16 v[60:63], v[104:107], a[32:39], v242\n" \
  "v_smfmac_f32_16x16x64_bf16 v[60:63], v[100:103], a[40:47], v242\n" \
  "s_waitcnt lgkmcnt(1)\n" \
  "v_smfmac_f32_16x16x64_bf16 v[60:63], v[112:115], a[48:55], v242\n" \
  "v_smfmac_f32_16x16x64_bf16 v[60:63], v[108:111], a[56:63], v242\n" \
  "s_waitcnt lgkmcnt(0)\n" \
  "v_smfmac_f32_16x16x64_bf16 v[60:63], v[116:119], a[64:71], v242\n" \
  "s_nop 2\n" \
  "v_smfmac_f32_16x16x64_bf16 v[64:67], v[88:91], a[80:87], v242\n" \
  "s_nop 3\n" \
  "v_add_f32 v60, v60, v62\n" \
  "v_add_f32 v61, v61, v63\n" \
  "v_cmp_u_f32 vcc, v60, v61\n" \
  "v_cvt_pk_bf16_f32 v80, v60, v61\n" \
  "v_smfmac_f32_16x16x64_bf16 v[64:67], v[92:95], a[88:95], v242\n" \
  "s_cbranch_vccnz .LW_slow_%=\n" \
  ".LW_back_%=:\n" \
  "ds_write_b32 v244, v80\n" \
  "v_smfmac_f32_16x16x64_bf16 v[64:67], v[96:99], a[96:103], v242\n" \
  "v_smfmac_f32_16x16x64_bf16 v[64:67], v[104:107], a[104:111], v242\n" \
  "v_smfmac_f32_16x16x64_bf16 v[64:67], v[100:103], a[112:119], v242\n" \
  "v_smfmac_f32_16x16x64_bf16 v[64:67], v[112:115], a[120:127], v242\n" \
  "s_waitcnt lgkmcnt(0)\n" \
  "s_barrier\n" \
  "v_smfmac_f32_16x16x64_bf16 v[64:67], v[108:111], a[128:135], v242\n" \
  "ds_read_b64_tr_b16 v[86:87], v245 offset:1536\n" \
  "ds_read_b64_tr_b16 v[90:91], v245 offset:512\n" \
  "v_smfmac_f32_16x16x64_bf16 v[64:67], v[116:119], a[136:143], v242\n" \
  "ds_read_b64_tr_b16 v[88:89], v245\n" \
  "s_nop 6\n" \
  "v_add_f32 v65, v65, v67\n" \
  "v_add_f32 v64, v64, v66\n" \
  "v_cvt_pk_bf16_f32 v81, v64, v65\n" \
  "ds_write_b32 v244, v81 offset:256\n" \
  "s_waitcnt lgkmcnt(1)\n" \
  "v_smfmac_f32_16x16x64_bf16 v[76:79], v[88:91], a[208:215], v248\n" \
  "v_smfmac_f32_16x16x64_bf16 v[72:75], v[88:91], a[176:183], v248\n" \
  "ds_read_b64_tr_b16 v[84:85], v245 offset:1024\n" \
  "v_smfmac_f32_16x16x64_bf16 v[68:71], v[88:91], a[144:151], v248\n" \
  "s_waitcnt lgkmcnt(1)\n" \
  "s_barrier\n" \
  "ds_read_b64_tr_b16 v[88:89], v245 offset:256\n" \
  "ds_read_b64_tr_b16 v[90:91], v245 offset:768\n" \
  "ds_read_b64_tr_b16 v[94:95], v245 offset:1792\n" \
  "s_waitcnt lgkmcnt(3)\n" \
  "v_smfmac_f32_16x16x64_bf16 v[68:71], v[84:87], a[152:159], v248\n" \
  "ds_read_b64_tr_b16 v[92:93], v245 offset:1280\n" \
  "v_smfmac_f32_16x16x64_bf16 v[72:75], v[84:87], a[184:191], v248\n" \
  "v_smfmac_f32_16x16x64_bf16 v[76:79], v[84:87], a[216:223], v248\n" \
  "s_waitcnt lgkmcnt(2)\n" \
  "v_smfmac_f32_16x16x64_bf16 v[68:71], v[88:91], a[160:167], v248\n" \
  "s_waitcnt lgkmcnt(0)\n" \
  "v_smfmac_f32_16x16x64_bf16 v[68:71], v[92:95], a[168:175], v248\n" \
  "s_nop 2\n" \
  "v_smfmac_f32_16x16x64_bf16 v[72:75], v[88:91], a[192:199], v248\n" \
  "s_nop 0\n" \
  "v_smfmac_f32_16x16x64_bf16 v[72:75], v[92:95], a[200:207], v248\n" \
  "s_nop 2\n" \
  "v_smfmac_f32_16x16x64_bf16 v[76:79], v[88:91], a[224:231], v248\n" \
  "s_nop 0\n" \
  "v_smfmac_f32_16x16x64_bf16 v[76:79], v[92:95], a[232:239], v248\n" \
  "v_cvt_pk_bf16_f32 v68, v68, v69\n" \
  "v_cvt_pk_bf16_f32 v69, v70, v71\n" \
  "v_cvt_pk_bf16_f32 v71, v74, v75\n" \
  "v_cvt_pk_bf16_f32 v70, v72, v73\n" \
  "s_nop 1\n" \
  "v_permlane16_swap_b32 v68, v70\n" \
  "v_permlane16_swap_b32 v69, v71\n" \
  "v_cvt_pk_bf16_f32 v76, v76, v77\n" \
  "v_cvt_pk_bf16_f32 v77, v78, v79\n" \
  "buffer_store_dwordx4 v[68:71], v250, s[40:43], 0 offen sc1\n" \
  "buffer_store_dwordx2 v[76:77], v252, s[40:43], 0 offen sc1\n" \
  ".LW_loop_%=:\n" \
  "s_memrealtime s[98:99]\n" \
  "s_memtime s[84:85]\n" \
  "s_cmp_ge_u32 s48, 58\n" \
  "s_cbranch_scc1 .LW_cp_%=\n" \
  ".LW_cp_b_%=:\n" \
  "v_mov_b64 v[60:61], 0\n" \
  "v_mov_b64 v[62:63], 0\n" \
  "v_mov_b64 v[64:65], 0\n" \
  "v_mov_b64 v[66:67], 0\n" \
  "v_mov_b64 v[68:69], 0\n" \
  "v_mov_b64 v[70:71], 0\n" \
  "v_mov_b64 v[72:73], 0\n" \
  "v_mov_b64 v[74:75], 0\n" \
  "v_mov_b64 v[76:77], 0\n" \
  "v_mov_b64 v[78:79], 0\n" \
  "s_add_u32 s95, s48, 1\n" \
  "s_and_b32 s63, s95, 3\n" \
  "s_lshl_b32 s95, s95, 5\n" \
  "s_lshl_b32 s63, s63, 4\n" \
  "s_sub_u32 s95, s95, s63\n" \
  "s_add_u32 s95, s95, s45\n" \
  "buffer_load_dwordx4 v[52:55], off, s[40:43], s95 sc1\n" \
  "s_add_u32 s48, s48, 1\n" \
  "v_mov_b32 v56, s48\n" \
  "s_waitcnt lgkmcnt(0)\n" \
  "s_cmp_gt_u32 s48, 57\n" \
  "s_cbranch_scc1 .LW_ef_%=\n" \
  ".LW_ef_b_%=:\n" \
  "s_sub_u32 s76, s84, s90\n" \
  "s_sub_u32 s77, s98, s86\n" \
  "v_cvt_f32_u32 v216, s76\n" \
  "v_cvt_f32_u32 v217, s77\n" \
  "v_rcp_f32 v217, v217\n" \
  "v_mul_f32 v216, 16.0, v216\n" \
  "v_fma_f32 v216, v216, v217, 0.5\n" \
  "v_cvt_u32_f32 v216, v216\n" \
  "s_nop 0\n" \
  "v_readfirstlane_b32 s76, v216\n" \
  "s_max_u32 s76, s76, 160\n" \
  "s_min_u32 s76, s76, 400\n" \
  "s_add_u32 s77, s48, 3\n" \
  "s_and_b32 s77, s77, 3\n" \
  "s_cselect_b32 s87, s87, s76\n" \
  "s_cselect_b32 s86, s86, s98\n" \
  "s_cselect_b32 s90, s90, s84\n" \
  "s_waitcnt vmcnt(0)\n" \
  "s_branch .LW_rot_%=\n" \
  "s_nop 7\n" \
  ".LW_slow_%=:\n" \
  "s_bitcmp1_b32 s70, 0\n" \
  "s_cbranch_scc1 .LW_back_%=\n" \
  "s_waitcnt lgkmcnt(0)\n" \
  "s_cmp_lt_u32 s70, 10\n" \
  "s_nop 3\n" \
  "s_cbranch_scc1 .LW_nol_%=\n" \
  "v_max_u32 v217, v84, v85\n" \
  "v_max3_u32 v217, v217, v86, v87\n" \
  "v_max3_u32 v217, v217, v88, v89\n" \
  "v_max3_u32 v217, v217, v90, v91\n" \
  "v_max3_u32 v217, v217, v92, v93\n" \
  "v_max3_u32 v217, v217, v94, v95\n" \
  "v_max3_u32 v217, v217, v96, v97\n" \
  "v_max3_u32 v217, v217, v98, v99\n" \
  "v_max3_u32 v217, v217, v104, v105\n" \
  "v_max3_u32 v217, v217, v106, v107\n" \
  "v_max3_u32 v217, v217, v100, v101\n" \
  "v_max3_u32 v217, v217, v102, v103\n" \
  "v_max3_u32 v217, v217, v112, v113\n" \
  "v_max3_u32 v217, v217, v114, v115\n" \
  "v_max3_u32 v217, v217, v108, v109\n" \
  "v_max3_u32 v217, v217, v110, v111\n" \
  "v_max3_u32 v217, v217, v116, v117\n" \
  "v_max3_u32 v217, v217, v118, v119\n" \
  "v_cmp_eq_u32 vcc, -1, v217\n" \
  "s_cbranch_vccz .LW_back_%=\n" \
  ".LW_nol_%=:\n" \
  "s_add_u32 s70, s70, 2\n" \
  "s_cmp_gt_u32 s70, 65536\n" \
  "s_cbranch_scc1 .LW_dset_%=\n" \
  "s_barrier\n" \
  "v_mov_b64 v[60:61], 0\n" \
  "v_mov_b64 v[62:63], 0\n" \
  "v_mov_b64 v[64:65], 0\n" \
  "v_mov_b64 v[66:67], 0\n" \
  "v_mov_b64 v[68:69], 0\n" \
  "v_mov_b64 v[70:71], 0\n" \
  "v_mov_b64 v[72:73], 0\n" \
  "v_mov_b64 v[74:75], 0\n" \
  "v_mov_b64 v[76:77], 0\n" \
  "v_mov_b64 v[78:79], 0\n" \
  "s_and_b32 s62, %48, 0xffff\n" \
  "s_add_u32 s44, s44, s62\n" \
  "s_xor_b32 s59, s59, 0\n" \
  "s_mov_b32 s68, 0\n" \
  "s_mov_b32 m0, 0\n" \
  "s_branch .LW_rt_%=\n" \
  ".LW_dset_%=:\n" \
  "v_mov_b32 v100, 1\n" \
  "v_mov_b32 v101, s55\n" \
  "buffer_store_dwordx2 v[100:101], off, s[40:43], %46 offset:128 sc1\n" \
  "s_mov_b32 s70, 1\n" \
  "v_mov_b64 v[60:61], 0\n" \
  "v_mov_b64 v[62:63], 0\n" \
  "v_mov_b64 v[64:65], 0\n" \
  "v_mov_b64 v[66:67], 0\n" \
  "v_mov_b64 v[68:69], 0\n" \
  "v_mov_b64 v[70:71], 0\n" \
  "v_mov_b64 v[72:73], 0\n" \
  "v_mov_b64 v[74:75], 0\n" \
  "v_mov_b64 v[76:77], 0\n" \
  "v_mov_b64 v[78:79], 0\n" \
  "s_waitcnt vmcnt(0) lgkmcnt(0)\n" \
  "s_barrier\n" \
  "s_branch .LW_p_%=\n" \
  ".LW_cp_%=:\n" \
  "s_sub_u32 s60, s48, 58\n" \
  "s_lshl_b32 s60, s60, 20\n" \
  "s_add_u32 s60, s60, s47\n" \
  "s_lshr_b32 s62, s60, 8\n" \
  "s_and_b32 s62, s62, 8192\n" \
  "s_xor_b32 s62, s62, s60\n" \
  "buffer_store_dwordx4 v[68:71], v253, s[40:43], s62 offen sc1\n" \
  "buffer_store_dwordx2 v[76:77], v255, s[40:43], s62 offen sc1\n" \
  "s_nop 1\n" \
  "s_branch .LW_cp_b_%=\n" \
  ".LW_ef_%=:\n" \
  "s_cmp_eq_u32 s48, 64\n" \
  "s_cbranch_scc1 .LW_end_%=\n" \
  "s_waitcnt vmcnt(0)\n" \
  "s_barrier\n" \
  "s_branch .LW_ef_b_%=\n" \
  ".LW_tw_re_%=:\n" \
  "s_add_u32 s69, s69, 1\n" \
  "s_cmp_gt_u32 s69, 200\n" \
  "s_cbranch_scc1 .LW_noclk_%=\n" \
  "s_sleep 2\n" \
  "buffer_load_dwordx4 v[52:55], off, s[40:43], s95 sc1\n" \
  "s_waitcnt vmcnt(0)\n" \
  "s_branch .LW_tw_%=\n" \
  ".LW_lat_%=:\n" \
  "s_lshr_b32 s63, %48, 16\n" \
  "s_add_u32 s62, s62, s63\n" \
  "s_cmp_lt_u32 s62, 4294937296\n" \
  "s_cbranch_scc1 .LW_noclk_%=\n" \
  "s_and_b32 s63, %48, 0xffff\n" \
  ".LW_lat1_%=:\n" \
  "s_add_u32 s44, s44, s63\n" \
  "s_add_u32 s62, s62, s63\n" \
  "s_cbranch_scc0 .LW_lat1_%=\n" \
  "s_branch .LW_rt_%=\n" \
  ".LW_clk2_%=:\n" \
  "s_cmp_le_i32 s62, 3\n" \
  "s_cbranch_scc1 .LW_clk_done_%=\n" \
  "s_memrealtime s[60:61]\n" \
  "s_waitcnt lgkmcnt(0)\n" \
  "s_sub_u32 s62, s73, s60\n" \
  "s_branch .LW_clk2_%=\n" \
  ".LW_clk_done_%=:\n" \
  "s_branch .LW_noclk_%=\n" \
  ".LW_end_%=:\n" \
  "s_waitcnt vmcnt(0)\n" \
  "s_waitcnt vmcnt(0)\n" \
  "s_nop 7\n" \
  "s_endpgm\n" \
  ".LW_other_%=:\n"
#define W_ASM_VOPS "+{v242}"(ci.sidx), "+{v243}"(ci.l16), "+{v244}"(ci.viw), "+{v245}"(ci.fva), "+{v246}"(ci.pk3), "+{v247}"(ci.fvs), "+{v248}"(ci.sid2), "+{v249}"(ci.uo), "+{v250}"(ci.po0), "+{v251}"(ci.po1), "+{v252}"(ci.po2), "+{v253}"(ci.co0), "+{v254}"(ci.co1), "+{v255}"(ci.co2), "+{v200}"(ci.vq0), "+{v201}"(ci.vq1), "+{v202}"(ci.vq2), "+{v203}"(ci.vq3), "+{v204}"(ci.vo0), "+{v205}"(ci.vo1), "+{v206}"(ci.vo2), "+{v207}"(ci.vo3), "+{v208}"(ci.v50), "+{v209}"(ci.v51), "+{v210}"(ci.v52), "+{v211}"(ci.v53), "+{v212}"(ci.tro0), "+{v213}"(ci.tro1), "+{v214}"(ci.m0o), "+{v215}"(ci.tro2), "+{v216}"(ci.tro3), "+{v217}"(ci.tro4)
#define W_ASM_SOPS "s"(ci.ws), "s"(ci.Wa), "s"(ci.Wb), "s"(ci.mem0), "s"(ci.w), "s"(ci.o_a), "s"(ci.o_b), "s"(ci.o_nx), "s"(ci.o_exp), "s"(ci.lab), "s"(ci.f_p), "s"(ci.f_m), "s"(ci.f_dur), "s"(ci.f_go), "s"(ci.o_ctl), "s"(ci.o_dbg), "s"(ci.dret), "s"(ci.o_qx), "s"(ci.f_s), "s"(ci.f_r)
#define W_ASM_CLOB "v0", "v1", "v2", "v3", "v4", "v5", "v6", "v7", "v8", "v9", "v10", "v11", "v12", "v13", "v14", "v15", "v16", "v17", "v18", "v19", "v20", "v21", "v22", "v23", "v24", "v25", "v26", "v27", "v28", "v29", "v30", "v31", "v32", "v33", "v34", "v35", "v36", "v37", "v38", "v39", "v40", "v41", "v42", "v43", "v44", "v45", "v46", "v47", "v48", "v49", "v50", "v51", "v52", "v53", "v54", "v55", "v56", "v57", "v58", "v59", "v60", "v61", "v62", "v63", "v64", "v65", "v66", "v67", "v68", "v69", "v70", "v71", "v72", "v73", "v74", "v75", "v76", "v77", "v78", "v79", "v80", "v81", "v82", "v83", "v84", "v85", "v86", "v87", "v88", "v89", "v90", "v91", "v92", "v93", "v94", "v95", "v96", "v97", "v98", "v99", "v100", "v101", "v102", "v103", "v104", "v105", "v106", "v107", "v108", "v109", "v110", "v111", "v112", "v113", "v114", "v115", "v116", "v117", "v118", "v119", "v120", "v121", "v122", "v123", "v124", "v125", "v126", "v127", "v128", "v129", "v130", "v131", "v132", "v133", "v134", "v135", "v136", "v137", "v138", "v139", "v140", "v141", "v142", "v143", "v144", "v145", "v146", "v147", "v148", "v149", "v150", "v151", "v152", "v153", "v154", "v155", "v156", "v157", "v158", "v159", "v160", "v161", "v162", "v163", "v164", "v165", "v166", "v167", "v168", "v169", "v170", "v171", "v172", "v173", "v174", "v175", "v176", "v177", "v178", "v179", "v180", "v181", "v182", "v183", "v184", "v185", "v186", "v187", "v188", "v189", "v190", "v191", "v192", "v193", "v194", "v195", "v196", "v197", "v198", "v199", "a0", "a1", "a2", "a3", "a4", "a5", "a6", "a7", "a8", "a9", "a10", "a11", "a12", "a13", "a14", "a15", "a16", "a17", "a18", "a19", "a20", "a21", "a22", "a23", "a24", "a25", "a26", "a27", "a28", "a29", "a30", "a31", "a32", "a33", "a34", "a35", "a36", "a37", "a38", "a39", "a40", "a41", "a42", "a43", "a44", "a45", "a46", "a47", "a48", "a49", "a50", "a51", "a52", "a53", "a54", "a55", "a56", "a57", "a58", "a59", "a60", "a61", "a62", "a63", "a64", "a65", "a66", "a67", "a68", "a69", "a70", "a71", "a72", "a73", "a74", "a75", "a76", "a77", "a78", "a79", "a80", "a81", "a82", "a83", "a84", "a85", "a86", "a87", "a88", "a89", "a90", "a91", "a92", "a93", "a94", "a95", "a96", "a97", "a98", "a99", "a100", "a101", "a102", "a103", "a104", "a105", "a106", "a107", "a108", "a109", "a110", "a111", "a112", "a113", "a114", "a115", "a116", "a117", "a118", "a119", "a120", "a121", "a122", "a123", "a124", "a125", "a126", "a127", "a128", "a129", "a130", "a131", "a132", "a133", "a134", "a135", "a136", "a137", "a138", "a139", "a140", "a141", "a142", "a143", "a144", "a145", "a146", "a147", "a148", "a149", "a150", "a151", "a152", "a153", "a154", "a155", "a156", "a157", "a158", "a159", "a160", "a161", "a162", "a163", "a164", "a165", "a166", "a167", "a168", "a169", "a170", "a171", "a172", "a173", "a174", "a175", "a176", "a177", "a178", "a179", "a180", "a181", "a182", "a183", "a184", "a185", "a186", "a187", "a188", "a189", "a190", "a191", "a192", "a193", "a194", "a195", "a196", "a197", "a198", "a199", "a200", "a201", "a202", "a203", "a204", "a205", "a206", "a207", "a208", "a209", "a210", "a211", "a212", "a213", "a214", "a215", "a216", "a217", "a218", "a219", "a220", "a221", "a222", "a223", "a224", "a225", "a226", "a227", "a228", "a229", "a230", "a231", "a232", "a233", "a234", "a235", "a236", "a237", "a238", "a239", "a240", "a241", "a242", "a243", "a244", "a245", "a246", "a247", "a248", "a249", "a250", "a251", "a252", "a253", "a254", "a255", "s40", "s41", "s42", "s43", "s44", "s45", "s46", "s47", "s48", "s49", "s50", "s51", "s52", "s53", "s54", "s55", "s56", "s57", "s58", "s59", "s60", "s61", "s62", "s63", "s64", "s65", "s66", "s67", "s68", "s69", "s70", "s71", "s72", "s73", "s74", "s75", "s76", "s77", "s78", "s79", "s80", "s81", "s82", "s83", "s84", "s85", "s86", "s87", "s88", "s89", "s90", "s91", "s92", "s93", "s94", "s95", "s96", "s97", "s98", "s99", "vcc", "scc", "memory"
#define R_ASM_TEXT \
  "s_mov_b64 s[40:41], %18\n" \
  "s_and_b32 s41, s41, 0xffff\n" \
  "s_mov_b32 s42, 255852544\n" \
  "s_mov_b32 s43, 0x00020000\n" \
  "s_bfe_u32 s56, %20, 0x30010\n" \
  "s_lshl_b32 s56, s56, 12\n" \
  "s_or_b32 s56, s56, 0x200000\n" \
  "s_mov_b32 s57, 1052672\n" \
  "s_mov_b32 s48, 0\n" \
  "s_cmp_lg_u32 %19, 0\n" \
  "s_cbranch_scc1 .LR_help_%=\n" \
  "s_lshl_b32 s61, %24, 2\n" \
  "s_lshr_b32 s51, %20, s61\n" \
  "s_and_b32 s51, s51, 7\n" \
  "s_lshl_b32 s51, s51, 12\n" \
  "s_xor_b32 s51, s51, 4210688\n" \
  "s_mov_b32 s71, s56\n" \
  "s_add_u32 s47, s51, 886784\n" \
  "s_lshr_b32 s52, %20, 0\n" \
  "s_and_b32 s52, s52, 7\n" \
  "s_lshl_b32 s52, s52, 12\n" \
  "s_xor_b32 s52, s52, 4210688\n" \
  "s_xor_b32 s52, s52, s56\n" \
  "s_lshr_b32 s64, %20, 4\n" \
  "s_and_b32 s64, s64, 7\n" \
  "s_lshl_b32 s64, s64, 12\n" \
  "s_xor_b32 s64, s64, 4210688\n" \
  "s_xor_b32 s64, s64, s56\n" \
  "s_lshr_b32 s65, %20, 8\n" \
  "s_and_b32 s65, s65, 7\n" \
  "s_lshl_b32 s65, s65, 12\n" \
  "s_xor_b32 s65, s65, 4210688\n" \
  "s_xor_b32 s65, s65, s56\n" \
  "s_lshr_b32 s67, %20, 12\n" \
  "s_and_b32 s67, s67, 7\n" \
  "s_lshl_b32 s67, s67, 12\n" \
  "s_xor_b32 s67, s67, 4210688\n" \
  "s_xor_b32 s67, s67, s56\n" \
  "v_add_u32 v208, 32768, v215\n" \
  "v_add_u32 v209, 65536, v215\n" \
  "v_add_u32 v210, 98304, v215\n" \
  "v_add_u32 v211, 40960, v214\n" \
  "s_mov_b32 s49, %22\n" \
  "s_mov_b32 s58, %23\n" \
  "s_memrealtime s[86:87]\n" \
  "s_memtime s[90:91]\n" \
  "s_waitcnt lgkmcnt(0)\n" \
  "s_mov_b32 s87, 900\n" \
  "s_mov_b32 s50, 0\n" \
  "s_mov_b32 s44, 0\n" \
  "s_mov_b32 s88, 0\n" \
  "s_mov_b32 s45, 0\n" \
  "s_mov_b32 s72, 2130706432\n" \
  "s_mov_b32 s46, 4096\n" \
  "v_mov_b32 v199, %21\n" \
  "s_mov_b32 s89, 2130706432\n" \
  "s_xor_b32 s51, s51, 0x40000000\n" \
  "s_xor_b32 s49, s49, 0x40000000\n" \
  "s_xor_b32 s58, s58, 0x40000000\n" \
  "s_xor_b32 s47, s47, 0x40000000\n" \
  "s_xor_b32 s52, s52, 0x40000000\n" \
  "s_xor_b32 s64, s64, 0x40000000\n" \
  "s_xor_b32 s65, s65, 0x40000000\n" \
  "s_xor_b32 s67, s67, 0x40000000\n" \
  ".LR_loop_%=:\n" \
  "s_add_u32 s95, s48, 0\n" \
  "s_and_b32 s63, s95, 3\n" \
  "s_lshl_b32 s95, s95, 5\n" \
  "s_lshl_b32 s63, s63, 4\n" \
  "s_sub_u32 s95, s95, s63\n" \
  "s_add_u32 s95, s95, s47\n" \
  "s_mov_b32 s54, s51\n" \
  "s_add_u32 s94, s51, 32768\n" \
  "s_mov_b32 s70, 0\n" \
  "s_mov_b32 s96, 0\n" \
  "buffer_load_dwordx4 v[200:203], off, s[40:43], s95 sc1\n" \
  "s_sub_u32 s76, s84, s90\n" \
  "s_sub_u32 s77, s98, s86\n" \
  "v_cvt_f32_u32 v196, s76\n" \
  "v_cvt_f32_u32 v197, s77\n" \
  "v_rcp_f32 v197, v197\n" \
  "v_mul_f32 v196, 16.0, v196\n" \
  "v_fma_f32 v196, v196, v197, 0.5\n" \
  "v_cvt_u32_f32 v196, v196\n" \
  "s_nop 0\n" \
  "v_readfirstlane_b32 s76, v196\n" \
  "s_max_u32 s76, s76, 160\n" \
  "s_min_u32 s76, s76, 400\n" \
  "s_add_u32 s77, s48, 2\n" \
  "s_and_b32 s77, s77, 3\n" \
  "s_cselect_b32 s87, s87, s76\n" \
  "s_cselect_b32 s86, s86, s98\n" \
  "s_cselect_b32 s90, s90, s84\n" \
  "v_mov_b64 v[64:65], 0\n" \
  "v_mov_b64 v[66:67], 0\n" \
  "v_mov_b64 v[68:69], 0\n" \
  "v_mov_b64 v[70:71], 0\n" \
  "v_mov_b64 v[72:73], 0\n" \
  "v_mov_b64 v[74:75], 0\n" \
  "v_mov_b64 v[76:77], 0\n" \
  "v_mov_b64 v[78:79], 0\n" \
  "v_mov_b64 v[80:81], 0\n" \
  "v_mov_b64 v[82:83], 0\n" \
  "v_mov_b64 v[84:85], 0\n" \
  "v_mov_b64 v[86:87], 0\n" \
  "v_mov_b64 v[88:89], 0\n" \
  "v_mov_b64 v[90:91], 0\n" \
  "v_mov_b64 v[92:93], 0\n" \
  "v_mov_b64 v[94:95], 0\n" \
  "v_mov_b64 v[96:97], 0\n" \
  "v_mov_b64 v[98:99], 0\n" \
  "v_mov_b64 v[100:101], 0\n" \
  "v_mov_b64 v[102:103], 0\n" \
  "v_mov_b64 v[104:105], 0\n" \
  "v_mov_b64 v[106:107], 0\n" \
  "v_mov_b64 v[108:109], 0\n" \
  "v_mov_b64 v[110:111], 0\n" \
  "v_mov_b64 v[112:113], 0\n" \
  "v_mov_b64 v[114:115], 0\n" \
  "v_mov_b64 v[116:117], 0\n" \
  "v_mov_b64 v[118:119], 0\n" \
  "v_mov_b64 v[120:121], 0\n" \
  "v_mov_b64 v[122:123], 0\n" \
  "v_mov_b64 v[124:125], 0\n" \
  "v_mov_b64 v[126:127], 0\n" \
  "v_mov_b64 v[192:193], 0\n" \
  "v_mov_b64 v[194:195], 0\n" \
  "buffer_store_dwordx2 v[198:199], off, s[40:43], s89 sc1\n" \
  "s_barrier\n" \
  "ds_read_b128 a[0:3], v211 offset:0\n" \
  "ds_read_b128 a[4:7], v211 offset:1024\n" \
  "ds_read_b128 a[8:11], v211 offset:2048\n" \
  "ds_read_b128 a[12:15], v211 offset:3072\n" \
  "ds_read_b128 a[16:19], v211 offset:4096\n" \
  "ds_read_b128 a[20:23], v211 offset:5120\n" \
  "ds_read_b128 a[24:27], v211 offset:6144\n" \
  "ds_read_b128 a[28:31], v211 offset:7168\n" \
  "ds_read_b128 a[32:35], v211 offset:8192\n" \
  "ds_read_b128 a[36:39], v211 offset:9216\n" \
  "ds_read_b128 a[40:43], v211 offset:10240\n" \
  "ds_read_b128 a[44:47], v211 offset:11264\n" \
  "ds_read_b128 a[48:51], v211 offset:12288\n" \
  "ds_read_b128 a[52:55], v211 offset:13312\n" \
  "ds_read_b128 a[56:59], v211 offset:14336\n" \
  "ds_read_b128 a[60:63], v211 offset:15360\n" \
  "ds_read_b128 a[64:67], v211 offset:16384\n" \
  "ds_read_b128 a[68:71], v211 offset:17408\n" \
  "ds_read_b128 a[72:75], v211 offset:18432\n" \
  "ds_read_b128 a[76:79], v211 offset:19456\n" \
  "ds_read_b128 a[80:83], v211 offset:20480\n" \
  "ds_read_b128 a[84:87], v211 offset:21504\n" \
  "ds_read_b128 a[88:91], v211 offset:22528\n" \
  "ds_read_b128 a[92:95], v211 offset:23552\n" \
  "ds_read_b128 a[96:99], v211 offset:24576\n" \
  "ds_read_b128 a[100:103], v211 offset:25600\n" \
  "ds_read_b128 a[104:107], v211 offset:26624\n" \
  "ds_read_b128 a[108:111], v211 offset:27648\n" \
  "ds_read_b128 a[112:115], v211 offset:28672\n" \
  "ds_read_b128 a[116:119], v211 offset:29696\n" \
  "ds_read_b128 a[120:123], v211 offset:30720\n" \
  "ds_read_b128 a[124:127], v211 offset:31744\n" \
  "v_xor_b32 v211, 32768, v211\n" \
  "s_mov_b32 s69, 0\n" \
  "s_waitcnt vmcnt(1)\n" \
  ".LR_tw_%=:\n" \
  "v_readfirstlane_b32 s60, v200\n" \
  "s_cmp_lg_u32 s60, s48\n" \
  "s_cbranch_scc1 .LR_tw_re_%=\n" \
  "v_readfirstlane_b32 s50, v201\n" \
  "s_cmp_eq_u32 s48, 0\n" \
  "s_cselect_b32 s50, s86, s50\n" \
  "s_cmp_lg_u32 s44, s45\n" \
  "s_cselect_b32 s60, s46, 0\n" \
  "s_mov_b32 s45, s44\n" \
  "s_xor_b32 s54, s54, s60\n" \
  "s_xor_b32 s94, s94, s60\n" \
  ".LR_again_%=:\n" \
  "s_memrealtime s[60:61]\n" \
  "s_add_u32 s53, s50, s44\n" \
  "s_bfe_u32 s63, %28, 0x80010\n" \
  "s_waitcnt lgkmcnt(0)\n" \
  "s_sub_u32 s62, s53, s60\n" \
  "s_sub_u32 s61, s62, 14\n" \
  "s_cmp_ge_u32 s61, 2000\n" \
  "s_cbranch_scc1 .LR_near_%=\n" \
  "s_sub_u32 s62, s62, s63\n" \
  "s_cmp_le_i32 s62, 0\n" \
  "s_cbranch_scc1 .LR_fin_%=\n" \
  "s_sub_u32 s62, s62, 9\n" \
  ".LR_slp_%=:\n" \
  "s_cmp_le_i32 s62, 0\n" \
  "s_cbranch_scc1 .LR_clk_%=\n" \
  "s_sleep 1\n" \
  "s_sub_u32 s62, s62, 3\n" \
  "s_branch .LR_slp_%=\n" \
  ".LR_clk_%=:\n" \
  "s_memrealtime s[60:61]\n" \
  "s_waitcnt lgkmcnt(0)\n" \
  "s_sub_u32 s62, s53, s60\n" \
  "s_cmp_gt_i32 s62, s63\n" \
  "s_cbranch_scc1 .LR_clk_%=\n" \
  ".LR_fin_%=:\n" \
  "s_memrealtime s[76:77]\n" \
  "s_nop 4\n" \
  "s_memrealtime s[78:79]\n" \
  "s_nop 4\n" \
  "s_memrealtime s[80:81]\n" \
  "s_nop 4\n" \
  "s_memrealtime s[82:83]\n" \
  "s_waitcnt lgkmcnt(0)\n" \
  "s_mov_b32 s77, 1\n" \
  "s_cmp_eq_u32 s78, s76\n" \
  "s_addc_u32 s77, s77, 0\n" \
  "s_cmp_eq_u32 s80, s76\n" \
  "s_addc_u32 s77, s77, 0\n" \
  "s_cmp_eq_u32 s82, s76\n" \
  "s_addc_u32 s77, s77, 0\n" \
  "s_mul_i32 s77, s77, 24\n" \
  "s_lshr_b32 s78, s87, 2\n" \
  "s_sub_u32 s77, s78, s77\n" \
  "s_max_i32 s77, s77, 0\n" \
  "s_sub_u32 s76, s53, s76\n" \
  "s_mul_i32 s76, s76, s87\n" \
  "s_ashr_i32 s76, s76, 4\n" \
  "s_sub_u32 s76, s76, s77\n" \
  "s_sub_u32 s76, s76, 220\n" \
  "s_max_i32 s76, s76, 0\n" \
  "s_min_i32 s76, s76, 400\n" \
  "s_lshr_b32 s76, s76, 3\n" \
  "s_lshl_b32 s76, s76, 2\n" \
  "s_getpc_b64 s[78:79]\n" \
  "s_sub_u32 s76, 220, s76\n" \
  "s_add_u32 s78, s78, s76\n" \
  "s_addc_u32 s79, s79, 0\n" \
  "s_setpc_b64 s[78:79]\n" \
  "s_nop 1\n" \
  "s_nop 1\n" \
  "s_nop 1\n" \
  "s_nop 1\n" \
  "s_nop 1\n" \
  "s_nop 1\n" \
  "s_nop 1\n" \
  "s_nop 1\n" \
  "s_nop 1\n" \
  "s_nop 1\n" \
  "s_nop 1\n" \
  "s_nop 1\n" \
  "s_nop 1\n" \
  "s_nop 1\n" \
  "s_nop 1\n" \
  "s_nop 1\n" \
  "s_nop 1\n" \
  "s_nop 1\n" \
  "s_nop 1\n" \
  "s_nop 1\n" \
  "s_nop 1\n" \
  "s_nop 1\n" \
  "s_nop 1\n" \
  "s_nop 1\n" \
  "s_nop 1\n" \
  "s_nop 1\n" \
  "s_nop 1\n" \
  "s_nop 1\n" \
  "s_nop 1\n" \
  "s_nop 1\n" \
  "s_nop 1\n" \
  "s_nop 1\n" \
  "s_nop 1\n" \
  "s_nop 1\n" \
  "s_nop 1\n" \
  "s_nop 1\n" \
  "s_nop 1\n" \
  "s_nop 1\n" \
  "s_nop 1\n" \
  "s_nop 1\n" \
  "s_nop 1\n" \
  "s_nop 1\n" \
  "s_nop 1\n" \
  "s_nop 1\n" \
  "s_nop 1\n" \
  "s_nop 1\n" \
  "s_nop 1\n" \
  "s_nop 1\n" \
  "s_nop 1\n" \
  "s_nop 1\n" \
  ".LR_b2_%=:\n" \
  "buffer_load_dwordx4 v[0:3], v215, s[40:43], s54 offen sc1\n" \
  "s_memtime s[92:93]\n" \
  "s_cmp_lg_u32 s96, 0\n" \
  "buffer_load_dwordx4 v[4:7], v215, s[40:43], s54 offen offset:1024 sc1\n" \
  "buffer_load_dwordx4 v[8:11], v215, s[40:43], s54 offen offset:2048 sc1\n" \
  "buffer_load_dwordx4 v[12:15], v215, s[40:43], s54 offen offset:3072 sc1\n" \
  "buffer_load_dwordx4 v[16:19], v208, s[40:43], s54 offen sc1\n" \
  "buffer_load_dwordx4 v[20:23], v208, s[40:43], s54 offen offset:1024 sc1\n" \
  "buffer_load_dwordx4 v[24:27], v208, s[40:43], s54 offen offset:2048 sc1\n" \
  "buffer_load_dwordx4 v[28:31], v208, s[40:43], s54 offen offset:3072 sc1\n" \
  "buffer_load_dwordx4 v[32:35], v209, s[40:43], s54 offen sc1\n" \
  "buffer_load_dwordx4 v[36:39], v209, s[40:43], s54 offen offset:1024 sc1\n" \
  "buffer_load_dwordx4 v[40:43], v209, s[40:43], s54 offen offset:2048 sc1\n" \
  "buffer_load_dwordx4 v[44:47], v209, s[40:43], s54 offen offset:3072 sc1\n" \
  "buffer_load_dwordx4 v[48:51], v210, s[40:43], s54 offen sc1\n" \
  "buffer_load_dwordx4 v[52:55], v210, s[40:43], s54 offen offset:1024 sc1\n" \
  "buffer_load_dwordx4 v[56:59], v210, s[40:43], s54 offen offset:2048 sc1\n" \
  "buffer_load_dwordx4 v[60:63], v210, s[40:43], s54 offen offset:3072 sc1\n" \
  "buffer_load_dwordx4 v[128:131], v214, s[40:43], s49 offen sc1\n" \
  "buffer_load_dwordx4 v[132:135], v214, s[40:43], s49 offen offset:1024 sc1\n" \
  "buffer_load_dwordx4 v[136:139], v214, s[40:43], s49 offen offset:2048 sc1\n" \
  "buffer_load_dwordx4 v[140:143], v214, s[40:43], s49 offen offset:3072 sc1\n" \
  "buffer_load_dwordx4 v[144:147], v214, s[40:43], s58 offen sc1\n" \
  "buffer_load_dwordx4 v[148:151], v214, s[40:43], s58 offen offset:1024 sc1\n" \
  "buffer_load_dwordx4 v[152:155], v214, s[40:43], s58 offen offset:2048 sc1\n" \
  "buffer_load_dwordx4 v[156:159], v214, s[40:43], s58 offen offset:3072 sc1\n" \
  "buffer_load_dwordx4 v[204:207], v215, s[40:43], s54 offen sc1\n" \
  "buffer_load_dwordx4 v[204:207], v215, s[40:43], s54 offen sc1\n" \
  "buffer_load_dwordx4 v[204:207], v215, s[40:43], s54 offen sc1\n" \
  "buffer_load_dwordx4 v[160:163], v216, s[40:43], s54 offen sc1\n" \
  "buffer_load_dwordx4 v[164:167], v216, s[40:43], s54 offen offset:1024 sc1\n" \
  "buffer_load_dwordx4 v[168:171], v216, s[40:43], s54 offen offset:2048 sc1\n" \
  "buffer_load_dwordx4 v[172:175], v216, s[40:43], s54 offen offset:3072 sc1\n" \
  "buffer_load_dwordx4 v[176:179], v216, s[40:43], s94 offen sc1\n" \
  "buffer_load_dwordx4 v[180:183], v216, s[40:43], s94 offen offset:1024 sc1\n" \
  "buffer_load_dwordx4 v[184:187], v216, s[40:43], s94 offen offset:2048 sc1\n" \
  "buffer_load_dwordx4 v[188:191], v216, s[40:43], s94 offen offset:3072 sc1\n" \
  "s_cbranch_scc1 .LR_nb_%=\n" \
  "s_barrier\n" \
  ".LR_nb_%=:\n" \
  "s_waitcnt vmcnt(34)\n" \
  "v_smfmac_f32_16x16x64_bf16 v[64:67], v[0:3], a[0:7], v212\n" \
  "s_waitcnt vmcnt(33)\n" \
  "s_nop 1\n" \
  "v_smfmac_f32_16x16x64_bf16 v[68:71], v[4:7], a[8:15], v212\n" \
  "s_waitcnt vmcnt(32)\n" \
  "s_nop 1\n" \
  "v_smfmac_f32_16x16x64_bf16 v[72:75], v[8:11], a[16:23], v212\n" \
  "s_waitcnt vmcnt(31)\n" \
  "v_cvt_pk_bf16_f32 v64, v64, v65\n" \
  "v_cvt_pk_bf16_f32 v65, v66, v67\n" \
  "v_smfmac_f32_16x16x64_bf16 v[76:79], v[12:15], a[24:31], v212\n" \
  "s_waitcnt vmcnt(30)\n" \
  "v_cvt_pk_bf16_f32 v66, v68, v69\n" \
  "v_cvt_pk_bf16_f32 v67, v70, v71\n" \
  "v_smfmac_f32_16x16x64_bf16 v[80:83], v[16:19], a[32:39], v212\n" \
  "s_waitcnt vmcnt(29)\n" \
  "v_cvt_pk_bf16_f32 v68, v72, v73\n" \
  "v_cvt_pk_bf16_f32 v69, v74, v75\n" \
  "v_smfmac_f32_16x16x64_bf16 v[84:87], v[20:23], a[40:47], v212\n" \
  "s_waitcnt vmcnt(28)\n" \
  "v_cvt_pk_bf16_f32 v70, v76, v77\n" \
  "v_cvt_pk_bf16_f32 v71, v78, v79\n" \
  "v_smfmac_f32_16x16x64_bf16 v[88:91], v[24:27], a[48:55], v212\n" \
  "s_waitcnt vmcnt(27)\n" \
  "v_cvt_pk_bf16_f32 v80, v80, v81\n" \
  "v_cvt_pk_bf16_f32 v81, v82, v83\n" \
  "v_smfmac_f32_16x16x64_bf16 v[92:95], v[28:31], a[56:63], v212\n" \
  "s_waitcnt vmcnt(26)\n" \
  "v_cvt_pk_bf16_f32 v82, v84, v85\n" \
  "v_cvt_pk_bf16_f32 v83, v86, v87\n" \
  "v_smfmac_f32_16x16x64_bf16 v[96:99], v[32:35], a[64:71], v212\n" \
  "s_waitcnt vmcnt(25)\n" \
  "v_cvt_pk_bf16_f32 v84, v88, v89\n" \
  "v_cvt_pk_bf16_f32 v85, v90, v91\n" \
  "v_smfmac_f32_16x16x64_bf16 v[100:103], v[36:39], a[72:79], v212\n" \
  "s_waitcnt vmcnt(24)\n" \
  "v_cvt_pk_bf16_f32 v86, v92, v93\n" \
  "v_cvt_pk_bf16_f32 v87, v94, v95\n" \
  "v_smfmac_f32_16x16x64_bf16 v[104:107], v[40:43], a[80:87], v212\n" \
  "s_waitcnt vmcnt(23)\n" \
  "v_cvt_pk_bf16_f32 v96, v96, v97\n" \
  "v_cvt_pk_bf16_f32 v97, v98, v99\n" \
  "v_smfmac_f32_16x16x64_bf16 v[108:111], v[44:47], a[88:95], v212\n" \
  "s_waitcnt vmcnt(22)\n" \
  "v_cvt_pk_bf16_f32 v98, v100, v101\n" \
  "v_cvt_pk_bf16_f32 v99, v102, v103\n" \
  "v_smfmac_f32_16x16x64_bf16 v[112:115], v[48:51], a[96:103], v212\n" \
  "s_waitcnt vmcnt(21)\n" \
  "v_cvt_pk_bf16_f32 v100, v104, v105\n" \
  "v_cvt_pk_bf16_f32 v101, v106, v107\n" \
  "v_smfmac_f32_16x16x64_bf16 v[116:119], v[52:55], a[104:111], v212\n" \
  "s_waitcnt vmcnt(20)\n" \
  "v_cvt_pk_bf16_f32 v102, v108, v109\n" \
  "v_cvt_pk_bf16_f32 v103, v110, v111\n" \
  "v_smfmac_f32_16x16x64_bf16 v[120:123], v[56:59], a[112:119], v212\n" \
  "s_waitcnt vmcnt(19)\n" \
  "v_cvt_pk_bf16_f32 v112, v112, v113\n" \
  "v_cvt_pk_bf16_f32 v113, v114, v115\n" \
  "v_smfmac_f32_16x16x64_bf16 v[124:127], v[60:63], a[120:127], v212\n" \
  "s_mov_b32 s96, 1\n" \
  "v_cvt_pk_bf16_f32 v114, v116, v117\n" \
  "v_cvt_pk_bf16_f32 v115, v118, v119\n" \
  "s_nop 0\n" \
  "v_cvt_pk_bf16_f32 v116, v120, v121\n" \
  "v_cvt_pk_bf16_f32 v117, v122, v123\n" \
  "s_nop 1\n" \
  "v_cvt_pk_bf16_f32 v118, v124, v125\n" \
  "v_cvt_pk_bf16_f32 v119, v126, v127\n" \
  "s_waitcnt vmcnt(18)\n" \
  "v_permlane16_swap_b32 v128, v129\n" \
  "v_permlane16_swap_b32 v130, v131\n" \
  "s_waitcnt vmcnt(17)\n" \
  "v_permlane16_swap_b32 v132, v133\n" \
  "v_permlane16_swap_b32 v134, v135\n" \
  "s_waitcnt vmcnt(16)\n" \
  "v_permlane16_swap_b32 v136, v137\n" \
  "v_permlane16_swap_b32 v138, v139\n" \
  "s_waitcnt vmcnt(15)\n" \
  "v_permlane16_swap_b32 v140, v141\n" \
  "v_permlane16_swap_b32 v142, v143\n" \
  "s_waitcnt vmcnt(14)\n" \
  "v_permlane16_swap_b32 v144, v145\n" \
  "v_permlane16_swap_b32 v146, v147\n" \
  "s_waitcnt vmcnt(13)\n" \
  "v_permlane16_swap_b32 v148, v149\n" \
  "v_permlane16_swap_b32 v150, v151\n" \
  "s_waitcnt vmcnt(12)\n" \
  "v_permlane16_swap_b32 v152, v153\n" \
  "v_permlane16_swap_b32 v154, v155\n" \
  "s_waitcnt vmcnt(11)\n" \
  "v_permlane16_swap_b32 v156, v157\n" \
  "v_permlane16_swap_b32 v158, v159\n" \
  "s_nop 0\n" \
  "s_waitcnt vmcnt(7)\n" \
  "v_smfmac_f32_16x16x64_bf16 v[192:195], v[160:163], v[128:135], v212\n" \
  "s_waitcnt vmcnt(6)\n" \
  "v_smfmac_f32_16x16x64_bf16 v[192:195], v[164:167], v[136:143], v212\n" \
  "s_waitcnt vmcnt(5)\n" \
  "v_smfmac_f32_16x16x64_bf16 v[192:195], v[168:171], v[144:151], v212\n" \
  "s_waitcnt vmcnt(4)\n" \
  "v_smfmac_f32_16x16x64_bf16 v[192:195], v[172:175], v[152:159], v212\n" \
  "s_waitcnt vmcnt(3)\n" \
  "v_smfmac_f32_16x16x64_bf16 v[192:195], v[176:179], v[64:71], v213\n" \
  "s_waitcnt vmcnt(2)\n" \
  "v_smfmac_f32_16x16x64_bf16 v[192:195], v[180:183], v[80:87], v213\n" \
  "s_waitcnt vmcnt(1)\n" \
  "v_smfmac_f32_16x16x64_bf16 v[192:195], v[184:187], v[96:103], v213\n" \
  "s_waitcnt vmcnt(0)\n" \
  "v_smfmac_f32_16x16x64_bf16 v[192:195], v[188:191], v[112:119], v213\n" \
  "s_nop 7\n" \
  "v_permlane32_swap_b32 v192, v194\n" \
  "v_permlane32_swap_b32 v193, v195\n" \
  "v_add_f32 v192, v192, v194\n" \
  "v_add_f32 v193, v193, v195\n" \
  "v_cvt_pk_bf16_f32 v194, v192, v193\n" \
  "v_cmp_u_f32 vcc, v192, v193\n" \
  "s_nop 0\n" \
  "v_mov_b32_dpp v195, v194 quad_perm:[1,0,3,2] row_mask:0xf bank_mask:0xf\n" \
  "v_perm_b32 v194, v195, v194, v218\n" \
  "s_cbranch_vccnz .LR_bad_%=\n" \
  ".LR_good_%=:\n" \
  "buffer_store_dword v194, v217, s[40:43], s52 offen sc1\n" \
  "buffer_store_dword v194, v217, s[40:43], s64 offen sc1\n" \
  "buffer_store_dword v194, v217, s[40:43], s65 offen sc1\n" \
  "buffer_store_dword v194, v217, s[40:43], s67 offen sc1\n" \
  "s_memrealtime s[98:99]\n" \
  "s_memtime s[84:85]\n" \
  "s_waitcnt lgkmcnt(0)\n" \
  "s_sub_u32 s60, s84, s92\n" \
  "s_cmp_lt_u32 s60, 4000\n" \
  "s_cselect_b32 s60, s60, 4194304\n" \
  "s_cmp_eq_u32 s70, 0\n" \
  "s_cselect_b32 s60, s60, 4194304\n" \
  "s_min_u32 s66, s66, s60\n" \
  "s_lshl_b32 s60, s66, 10\n" \
  "s_cselect_b32 s89, %30, 2130706432\n" \
  "s_or_b32 s60, s60, s87\n" \
  "v_mov_b32 v198, s60\n" \
  "s_and_b32 s61, s48, 3\n" \
  "s_cmp_eq_u32 s61, 1\n" \
  "s_cselect_b32 s89, s89, 2130706432\n" \
  "s_cselect_b32 s66, 4194304, s66\n" \
  "s_cmp_eq_u32 s48, 0\n" \
  "s_cbranch_scc1 .LR_go0_%=\n" \
  "s_cmp_lg_u32 %26, 0\n" \
  "s_cbranch_scc1 .LR_extra_%=\n" \
  ".LR_extra_b_%=:\n" \
  "s_xor_b32 s51, s51, s56\n" \
  "s_xor_b32 s49, s49, s56\n" \
  "s_xor_b32 s58, s58, s56\n" \
  "s_xor_b32 s52, s52, s57\n" \
  "s_xor_b32 s64, s64, s57\n" \
  "s_xor_b32 s65, s65, s57\n" \
  "s_xor_b32 s67, s67, s57\n" \
  "s_xor_b32 s71, s71, s57\n" \
  "s_mov_b32 s60, s56\n" \
  "s_mov_b32 s56, s57\n" \
  "s_mov_b32 s57, s60\n" \
  ".LR_go_%=:\n" \
  "s_add_u32 s48, s48, 1\n" \
  "s_cmp_lt_u32 s48, 64\n" \
  "s_cbranch_scc1 .LR_loop_%=\n" \
  "s_branch .LR_end_%=\n" \
  ".LR_tw_re_%=:\n" \
  "s_add_u32 s69, s69, 1\n" \
  "s_cmp_gt_u32 s69, 64\n" \
  "s_cbranch_scc1 .LR_now_%=\n" \
  "buffer_load_dwordx4 v[200:203], off, s[40:43], s95 sc1\n" \
  "s_waitcnt vmcnt(0)\n" \
  "s_branch .LR_tw_%=\n" \
  ".LR_now_%=:\n" \
  "s_waitcnt lgkmcnt(0)\n" \
  "s_branch .LR_b2_%=\n" \
  ".LR_near_%=:\n" \
  "s_cmp_ge_u32 s62, 14\n" \
  "s_cbranch_scc1 .LR_lat_%=\n" \
  "s_cmp_le_u32 s62, 4\n" \
  "s_cbranch_scc1 .LR_b2_%=\n" \
  "s_memrealtime s[60:61]\n" \
  "s_waitcnt lgkmcnt(0)\n" \
  "s_sub_u32 s62, s53, s60\n" \
  "s_branch .LR_near_%=\n" \
  ".LR_lat_%=:\n" \
  "s_lshr_b32 s63, %29, 16\n" \
  "s_add_u32 s62, s62, s63\n" \
  "s_cmp_lt_u32 s62, 4294937296\n" \
  "s_cbranch_scc1 .LR_b2_%=\n" \
  "s_and_b32 s63, %29, 0xffff\n" \
  ".LR_lat1_%=:\n" \
  "s_add_u32 s44, s44, s63\n" \
  "s_add_u32 s62, s62, s63\n" \
  "s_cbranch_scc0 .LR_lat1_%=\n" \
  "s_branch .LR_again_%=\n" \
  ".LR_mv_%=:\n" \
  "s_sub_u32 s76, s77, s44\n" \
  "s_cmp_gt_u32 s76, 2000\n" \
  "s_cbranch_scc1 .LR_b2_%=\n" \
  "s_mov_b32 s44, s77\n" \
  "s_branch .LR_again_%=\n" \
  ".LR_go0_%=:\n" \
  "s_xor_b32 s51, s51, 0x40000000\n" \
  "s_xor_b32 s49, s49, 0x40000000\n" \
  "s_xor_b32 s58, s58, 0x40000000\n" \
  "s_xor_b32 s47, s47, 0x40000000\n" \
  "s_xor_b32 s52, s52, 0x40000000\n" \
  "s_xor_b32 s64, s64, 0x40000000\n" \
  "s_xor_b32 s65, s65, 0x40000000\n" \
  "s_xor_b32 s67, s67, 0x40000000\n" \
  "s_mov_b32 s44, 0\n" \
  "s_mov_b32 s89, 2130706432\n" \
  "s_mov_b32 s87, 340\n" \
  "s_mov_b32 s66, 4194304\n" \
  "s_add_u32 s95, s47, 16\n" \
  "s_mov_b32 s69, 0\n" \
  ".LR_go1_%=:\n" \
  "buffer_load_dwordx4 v[200:203], off, s[40:43], s95 sc1\n" \
  "s_add_u32 s69, s69, 1\n" \
  "s_waitcnt vmcnt(0)\n" \
  "v_readfirstlane_b32 s60, v200\n" \
  "s_sleep 1\n" \
  "s_cmp_gt_u32 s69, 200\n" \
  "s_cselect_b32 s60, 1, s60\n" \
  "s_cmp_lg_u32 s60, 1\n" \
  "s_cbranch_scc1 .LR_go1_%=\n" \
  "s_branch .LR_go_%=\n" \
  ".LR_extra_%=:\n" \
  "s_sub_u32 s62, s48, 42\n" \
  "s_cmp_lt_u32 s62, 21\n" \
  "s_cselect_b32 s62, s71, 2130706432\n" \
  "s_cmp_eq_u32 s61, 2\n" \
  "s_cselect_b32 s61, %26, 2130706432\n" \
  "v_readfirstlane_b32 s63, v202\n" \
  "s_sub_u32 s60, s63, s68\n" \
  "s_mov_b32 s68, s63\n" \
  "s_add_u32 s63, s63, s60\n" \
  "s_add_u32 s63, s63, s44\n" \
  "s_lshl_b32 s60, s44, 8\n" \
  "s_or_b32 s60, s60, s48\n" \
  "v_add_u32_e64 v196, 1, s48\n" \
  "v_mov_b32 v197, s63\n" \
  "v_xor_b32 v204, s62, v219\n" \
  "v_or_b32 v204, v204, v222\n" \
  "v_writelane_b32 v196, s60, 4\n" \
  "v_writelane_b32 v197, %21, 4\n" \
  "v_writelane_b32 v204, s61, 4\n" \
  "s_nop 1\n" \
  "buffer_store_dwordx2 v[196:197], v204, s[40:43], 0 offen sc1\n" \
  "s_branch .LR_extra_b_%=\n" \
  ".LR_bad_%=:\n" \
  "s_cmp_eq_u32 s48, 0\n" \
  "s_cbranch_scc1 .LR_good_%=\n" \
  "s_add_u32 s70, s70, 1\n" \
  "s_cmp_gt_u32 s70, 5\n" \
  "s_cbranch_scc1 .LR_bad1_%=\n" \
  ".LR_redo_%=:\n" \
  "s_and_b32 s62, %29, 0xffff\n" \
  "s_add_u32 s44, s44, s62\n" \
  "v_mov_b32 v196, s44\n" \
  "v_xor_b32 v197, s71, v221\n" \
  "s_xor_b32 s54, s54, s46\n" \
  "s_xor_b32 s94, s94, s46\n" \
  "v_mov_b64 v[64:65], 0\n" \
  "v_mov_b64 v[66:67], 0\n" \
  "v_mov_b64 v[68:69], 0\n" \
  "v_mov_b64 v[70:71], 0\n" \
  "v_mov_b64 v[72:73], 0\n" \
  "v_mov_b64 v[74:75], 0\n" \
  "v_mov_b64 v[76:77], 0\n" \
  "v_mov_b64 v[78:79], 0\n" \
  "v_mov_b64 v[80:81], 0\n" \
  "v_mov_b64 v[82:83], 0\n" \
  "v_mov_b64 v[84:85], 0\n" \
  "v_mov_b64 v[86:87], 0\n" \
  "v_mov_b64 v[88:89], 0\n" \
  "v_mov_b64 v[90:91], 0\n" \
  "v_mov_b64 v[92:93], 0\n" \
  "v_mov_b64 v[94:95], 0\n" \
  "v_mov_b64 v[96:97], 0\n" \
  "v_mov_b64 v[98:99], 0\n" \
  "v_mov_b64 v[100:101], 0\n" \
  "v_mov_b64 v[102:103], 0\n" \
  "v_mov_b64 v[104:105], 0\n" \
  "v_mov_b64 v[106:107], 0\n" \
  "v_mov_b64 v[108:109], 0\n" \
  "v_mov_b64 v[110:111], 0\n" \
  "v_mov_b64 v[112:113], 0\n" \
  "v_mov_b64 v[114:115], 0\n" \
  "v_mov_b64 v[116:117], 0\n" \
  "v_mov_b64 v[118:119], 0\n" \
  "v_mov_b64 v[120:121], 0\n" \
  "v_mov_b64 v[122:123], 0\n" \
  "v_mov_b64 v[124:125], 0\n" \
  "v_mov_b64 v[126:127], 0\n" \
  "v_mov_b64 v[192:193], 0\n" \
  "v_mov_b64 v[194:195], 0\n" \
  "s_branch .LR_again_%=\n" \
  ".LR_bad1_%=:\n" \
  "v_max_u32 v196, v0, v1\n" \
  "v_max3_u32 v196, v196, v2, v3\n" \
  "v_max3_u32 v196, v196, v4, v5\n" \
  "v_max3_u32 v196, v196, v6, v7\n" \
  "v_max3_u32 v196, v196, v8, v9\n" \
  "v_max3_u32 v196, v196, v10, v11\n" \
  "v_max3_u32 v196, v196, v12, v13\n" \
  "v_max3_u32 v196, v196, v14, v15\n" \
  "v_max3_u32 v196, v196, v16, v17\n" \
  "v_max3_u32 v196, v196, v18, v19\n" \
  "v_max3_u32 v196, v196, v20, v21\n" \
  "v_max3_u32 v196, v196, v22, v23\n" \
  "v_max3_u32 v196, v196, v24, v25\n" \
  "v_max3_u32 v196, v196, v26, v27\n" \
  "v_max3_u32 v196, v196, v28, v29\n" \
  "v_max3_u32 v196, v196, v30, v31\n" \
  "v_max3_u32 v196, v196, v32, v33\n" \
  "v_max3_u32 v196, v196, v34, v35\n" \
  "v_max3_u32 v196, v196, v36, v37\n" \
  "v_max3_u32 v196, v196, v38, v39\n" \
  "v_max3_u32 v196, v196, v40, v41\n" \
  "v_max3_u32 v196, v196, v42, v43\n" \
  "v_max3_u32 v196, v196, v44, v45\n" \
  "v_max3_u32 v196, v196, v46, v47\n" \
  "v_max3_u32 v196, v196, v48, v49\n" \
  "v_max3_u32 v196, v196, v50, v51\n" \
  "v_max3_u32 v196, v196, v52, v53\n" \
  "v_max3_u32 v196, v196, v54, v55\n" \
  "v_max3_u32 v196, v196, v56, v57\n" \
  "v_max3_u32 v196, v196, v58, v59\n" \
  "v_max3_u32 v196, v196, v60, v61\n" \
  "v_max3_u32 v196, v196, v62, v63\n" \
  "v_max_u32 v197, v128, v129\n" \
  "v_max3_u32 v197, v197, v130, v131\n" \
  "v_max3_u32 v197, v197, v132, v133\n" \
  "v_max3_u32 v197, v197, v134, v135\n" \
  "v_max3_u32 v197, v197, v136, v137\n" \
  "v_max3_u32 v197, v197, v138, v139\n" \
  "v_max3_u32 v197, v197, v140, v141\n" \
  "v_max3_u32 v197, v197, v142, v143\n" \
  "v_max3_u32 v197, v197, v144, v145\n" \
  "v_max3_u32 v197, v197, v146, v147\n" \
  "v_max3_u32 v197, v197, v148, v149\n" \
  "v_max3_u32 v197, v197, v150, v151\n" \
  "v_max3_u32 v197, v197, v152, v153\n" \
  "v_max3_u32 v197, v197, v154, v155\n" \
  "v_max3_u32 v197, v197, v156, v157\n" \
  "v_max3_u32 v197, v197, v158, v159\n" \
  "v_max_u32 v204, v160, v161\n" \
  "v_max3_u32 v204, v204, v162, v163\n" \
  "v_max3_u32 v204, v204, v164, v165\n" \
  "v_max3_u32 v204, v204, v166, v167\n" \
  "v_max3_u32 v204, v204, v168, v169\n" \
  "v_max3_u32 v204, v204, v170, v171\n" \
  "v_max3_u32 v204, v204, v172, v173\n" \
  "v_max3_u32 v204, v204, v174, v175\n" \
  "v_max3_u32 v204, v204, v176, v177\n" \
  "v_max3_u32 v204, v204, v178, v179\n" \
  "v_max3_u32 v204, v204, v180, v181\n" \
  "v_max3_u32 v204, v204, v182, v183\n" \
  "v_max3_u32 v204, v204, v184, v185\n" \
  "v_max3_u32 v204, v204, v186, v187\n" \
  "v_max3_u32 v204, v204, v188, v189\n" \
  "v_max3_u32 v204, v204, v190, v191\n" \
  "v_max3_u32 v196, v196, v197, v204\n" \
  "v_cmp_eq_u32 vcc, -1, v196\n" \
  "s_cbranch_vccz .LR_nan_%=\n" \
  "s_add_u32 s88, s88, 1\n" \
  "v_mov_b32 v196, s88\n" \
  "v_mov_b32 v197, %21\n" \
  "buffer_store_dwordx2 v[196:197], off, s[40:43], %31 sc1\n" \
  "s_waitcnt vmcnt(0)\n" \
  "s_and_b32 s63, s70, 255\n" \
  "s_cbranch_scc1 .LR_redo_%=\n" \
  "buffer_load_dwordx2 v[196:197], off, s[40:43], %27 offset:128 sc1\n" \
  "s_waitcnt vmcnt(0)\n" \
  "v_readfirstlane_b32 s63, v197\n" \
  "s_cmp_eq_u32 s63, %21\n" \
  "s_cbranch_scc1 .LR_nan_%=\n" \
  "s_cmp_lt_u32 s70, 262144\n" \
  "s_cbranch_scc1 .LR_redo_%=\n" \
  "v_mov_b32 v196, 1\n" \
  "v_mov_b32 v197, %21\n" \
  "buffer_store_dwordx2 v[196:197], off, s[40:43], %27 offset:128 sc1\n" \
  ".LR_nan_%=:\n" \
  "v_min_u32 v194, -2, v194\n" \
  "s_branch .LR_good_%=\n" \
  ".LR_fu_%=:\n" \
  "buffer_load_dwordx2 v[140:141], v146, s[40:43], 0 offen sc1\n" \
  "s_waitcnt vmcnt(0)\n" \
  "v_cmp_ne_u32 vcc, %21, v141\n" \
  "s_nop 1\n" \
  "v_cndmask_b32 v140, v140, 0, vcc\n" \
  "v_cmp_ge_i32 vcc, s48, v140\n" \
  "s_bitcmp1_b64 s[90:91], s48\n" \
  "s_cselect_b32 vcc_lo, 0, vcc_lo\n" \
  "s_cselect_b32 vcc_hi, vcc_hi, 0\n" \
  "s_mov_b64 vcc, vcc\n" \
  "s_cbranch_vccnz .LR_fu0_%=\n" \
  "s_cmp_lg_u32 %19, 3\n" \
  "s_cbranch_scc1 .LR_h8_%=\n" \
  "s_xor_b32 s60, s61, s75\n" \
  "buffer_load_dwordx4 v[40:43], v214, s[40:43], s60 offen sc1\n" \
  "s_xor_b32 s60, s60, 1024\n" \
  "buffer_load_dwordx4 v[44:47], v214, s[40:43], s60 offen sc1\n" \
  ".LR_h8_%=:\n" \
  "s_xor_b32 s60, s61, s64\n" \
  "buffer_load_dwordx4 v[0:3], v214, s[40:43], s60 offen sc1\n" \
  "s_xor_b32 s60, s60, 1024\n" \
  "buffer_load_dwordx4 v[4:7], v214, s[40:43], s60 offen sc1\n" \
  "s_xor_b32 s60, s61, s65\n" \
  "buffer_load_dwordx4 v[8:11], v214, s[40:43], s60 offen sc1\n" \
  "s_xor_b32 s60, s60, 1024\n" \
  "buffer_load_dwordx4 v[12:15], v214, s[40:43], s60 offen sc1\n" \
  "s_xor_b32 s60, s61, s67\n" \
  "buffer_load_dwordx4 v[16:19], v214, s[40:43], s60 offen sc1\n" \
  "s_xor_b32 s60, s60, 1024\n" \
  "buffer_load_dwordx4 v[20:23], v214, s[40:43], s60 offen sc1\n" \
  "s_cmp_lt_u32 s48, 62\n" \
  "s_cselect_b32 s60, s71, 2130706432\n" \
  "v_xor_b32 v142, s60, v221\n" \
  "v_xor_b32 v143, s60, v222\n" \
  "v_xor_b32 v144, s60, v223\n" \
  "buffer_store_dwordx4 v[136:139], v142, s[40:43], 0 offen sc1\n" \
  "buffer_store_dwordx4 v[136:139], v143, s[40:43], 0 offen sc1\n" \
  "buffer_store_dwordx4 v[136:139], v144, s[40:43], 0 offen sc1\n" \
  "s_barrier\n" \
  "s_xor_b32 s60, s61, s68\n" \
  "buffer_load_dwordx4 v[24:27], v214, s[40:43], s60 offen sc1\n" \
  "s_xor_b32 s60, s60, 1024\n" \
  "buffer_load_dwordx4 v[28:31], v214, s[40:43], s60 offen sc1\n" \
  "s_xor_b32 s60, s61, s74\n" \
  "buffer_load_dwordx4 v[32:35], v214, s[40:43], s60 offen sc1\n" \
  "s_xor_b32 s60, s60, 1024\n" \
  "buffer_load_dwordx4 v[36:39], v214, s[40:43], s60 offen sc1\n" \
  "buffer_load_dwordx2 v[140:141], v146, s[40:43], 0 offen sc1\n" \
  "s_waitcnt vmcnt(0)\n" \
  "v_mov_b32 v72, v24\n" \
  "v_mov_b32 v73, v28\n" \
  "v_mov_b32 v74, v25\n" \
  "v_mov_b32 v75, v29\n" \
  "v_mov_b32 v76, v26\n" \
  "v_mov_b32 v77, v30\n" \
  "v_mov_b32 v78, v27\n" \
  "v_mov_b32 v79, v31\n" \
  "ds_write_b128 v211, v[72:75] offset:6144\n" \
  "ds_write_b128 v211, v[76:79] offset:7168\n" \
  "v_mov_b32 v80, v32\n" \
  "v_mov_b32 v81, v36\n" \
  "v_mov_b32 v82, v33\n" \
  "v_mov_b32 v83, v37\n" \
  "v_mov_b32 v84, v34\n" \
  "v_mov_b32 v85, v38\n" \
  "v_mov_b32 v86, v35\n" \
  "v_mov_b32 v87, v39\n" \
  "ds_write_b128 v211, v[80:83] offset:8192\n" \
  "ds_write_b128 v211, v[84:87] offset:9216\n" \
  "s_branch .LR_fu_b_%=\n" \
  ".LR_fu0_%=:\n" \
  "s_cmp_lt_u32 s48, 62\n" \
  "s_cselect_b32 s60, s71, 2130706432\n" \
  "v_xor_b32 v142, s60, v221\n" \
  "v_xor_b32 v143, s60, v222\n" \
  "v_xor_b32 v144, s60, v223\n" \
  "buffer_store_dwordx4 v[136:139], v142, s[40:43], 0 offen sc1\n" \
  "buffer_store_dwordx4 v[136:139], v143, s[40:43], 0 offen sc1\n" \
  "buffer_store_dwordx4 v[136:139], v144, s[40:43], 0 offen sc1\n" \
  "s_barrier\n" \
  "s_sleep 0\n" \
  "s_mov_b32 s69, 0\n" \
  ".LR_fu1_%=:\n" \
  "buffer_load_dwordx2 v[140:141], v146, s[40:43], 0 offen sc1\n" \
  "s_waitcnt vmcnt(0)\n" \
  "v_cmp_ne_u32 vcc, %21, v141\n" \
  "s_nop 1\n" \
  "v_cndmask_b32 v140, v140, 0, vcc\n" \
  "v_cmp_ge_i32 vcc, s48, v140\n" \
  "s_bitcmp1_b64 s[90:91], s48\n" \
  "s_cselect_b32 vcc_lo, 0, vcc_lo\n" \
  "s_cselect_b32 vcc_hi, vcc_hi, 0\n" \
  "s_mov_b64 vcc, vcc\n" \
  "s_cbranch_vccz .LR_fu_x_%=\n" \
  "s_sleep 2\n" \
  "s_add_u32 s69, s69, 1\n" \
  "s_and_b32 s63, s69, 1023\n" \
  "s_cbranch_scc1 .LR_fu1_%=\n" \
  "s_cmp_gt_u32 s69, 2097152\n" \
  "s_cbranch_scc1 .LR_fu1_set_%=\n" \
  "buffer_load_dwordx2 v[196:197], off, s[40:43], %27 offset:128 sc1\n" \
  "s_waitcnt vmcnt(0)\n" \
  "v_readfirstlane_b32 s63, v197\n" \
  "s_cmp_eq_u32 s63, %21\n" \
  "s_cbranch_scc0 .LR_fu1_%=\n" \
  "s_branch .LR_fu_x_%=\n" \
  ".LR_fu1_set_%=:\n" \
  "v_mov_b32 v196, 1\n" \
  "v_mov_b32 v197, %21\n" \
  "buffer_store_dwordx2 v[196:197], off, s[40:43], %27 offset:128 sc1\n" \
  "s_waitcnt vmcnt(0)\n" \
  "s_branch .LR_fu_x_%=\n" \
  ".LR_fu_x_%=:\n" \
  "s_xor_b32 s60, s61, s68\n" \
  "buffer_load_dwordx4 v[24:27], v214, s[40:43], s60 offen sc1\n" \
  "s_xor_b32 s60, s60, 1024\n" \
  "buffer_load_dwordx4 v[28:31], v214, s[40:43], s60 offen sc1\n" \
  "s_xor_b32 s60, s61, s74\n" \
  "buffer_load_dwordx4 v[32:35], v214, s[40:43], s60 offen sc1\n" \
  "s_xor_b32 s60, s60, 1024\n" \
  "buffer_load_dwordx4 v[36:39], v214, s[40:43], s60 offen sc1\n" \
  "s_cmp_lg_u32 %19, 3\n" \
  "s_cbranch_scc1 .LR_h7_%=\n" \
  "s_xor_b32 s60, s61, s75\n" \
  "buffer_load_dwordx4 v[40:43], v214, s[40:43], s60 offen sc1\n" \
  "s_xor_b32 s60, s60, 1024\n" \
  "buffer_load_dwordx4 v[44:47], v214, s[40:43], s60 offen sc1\n" \
  ".LR_h7_%=:\n" \
  "s_xor_b32 s60, s61, s64\n" \
  "buffer_load_dwordx4 v[0:3], v214, s[40:43], s60 offen sc1\n" \
  "s_xor_b32 s60, s60, 1024\n" \
  "buffer_load_dwordx4 v[4:7], v214, s[40:43], s60 offen sc1\n" \
  "s_xor_b32 s60, s61, s65\n" \
  "buffer_load_dwordx4 v[8:11], v214, s[40:43], s60 offen sc1\n" \
  "s_xor_b32 s60, s60, 1024\n" \
  "buffer_load_dwordx4 v[12:15], v214, s[40:43], s60 offen sc1\n" \
  "s_xor_b32 s60, s61, s67\n" \
  "buffer_load_dwordx4 v[16:19], v214, s[40:43], s60 offen sc1\n" \
  "s_xor_b32 s60, s60, 1024\n" \
  "buffer_load_dwordx4 v[20:23], v214, s[40:43], s60 offen sc1\n" \
  "buffer_load_dwordx2 v[140:141], v146, s[40:43], 0 offen sc1\n" \
  "s_waitcnt vmcnt(7)\n" \
  "v_mov_b32 v72, v24\n" \
  "v_mov_b32 v73, v28\n" \
  "v_mov_b32 v74, v25\n" \
  "v_mov_b32 v75, v29\n" \
  "v_mov_b32 v76, v26\n" \
  "v_mov_b32 v77, v30\n" \
  "v_mov_b32 v78, v27\n" \
  "v_mov_b32 v79, v31\n" \
  "ds_write_b128 v211, v[72:75] offset:6144\n" \
  "ds_write_b128 v211, v[76:79] offset:7168\n" \
  "v_mov_b32 v80, v32\n" \
  "v_mov_b32 v81, v36\n" \
  "v_mov_b32 v82, v33\n" \
  "v_mov_b32 v83, v37\n" \
  "v_mov_b32 v84, v34\n" \
  "v_mov_b32 v85, v38\n" \
  "v_mov_b32 v86, v35\n" \
  "v_mov_b32 v87, v39\n" \
  "ds_write_b128 v211, v[80:83] offset:8192\n" \
  "ds_write_b128 v211, v[84:87] offset:9216\n" \
  "s_waitcnt vmcnt(0)\n" \
  "s_branch .LR_fu_b_%=\n" \
  ".LR_nb0_%=:\n" \
  "s_waitcnt vmcnt(0)\n" \
  "s_branch .LR_nb1_%=\n" \
  ".LR_help_%=:\n" \
  "s_mul_i32 s60, %19, 5\n" \
  "s_sub_u32 s60, s60, 5\n" \
  "s_lshl_b32 s61, s60, 11\n" \
  "s_add_u32 s61, s61, 8192\n" \
  "v_add_u32 v211, s61, v214\n" \
  "s_lshr_b32 s61, s60, 2\n" \
  "s_lshl_b32 s61, s61, 14\n" \
  "s_and_b32 s62, s60, 3\n" \
  "s_lshl_b32 s62, s62, 11\n" \
  "s_xor_b32 s61, s61, s62\n" \
  "s_xor_b32 s64, s61, %25\n" \
  "s_add_u32 s60, s60, 1\n" \
  "s_lshr_b32 s61, s60, 2\n" \
  "s_lshl_b32 s61, s61, 14\n" \
  "s_and_b32 s62, s60, 3\n" \
  "s_lshl_b32 s62, s62, 11\n" \
  "s_xor_b32 s61, s61, s62\n" \
  "s_xor_b32 s65, s61, %25\n" \
  "s_add_u32 s60, s60, 1\n" \
  "s_lshr_b32 s61, s60, 2\n" \
  "s_lshl_b32 s61, s61, 14\n" \
  "s_and_b32 s62, s60, 3\n" \
  "s_lshl_b32 s62, s62, 11\n" \
  "s_xor_b32 s61, s61, s62\n" \
  "s_xor_b32 s67, s61, %25\n" \
  "s_add_u32 s60, s60, 1\n" \
  "s_lshr_b32 s61, s60, 2\n" \
  "s_lshl_b32 s61, s61, 14\n" \
  "s_and_b32 s62, s60, 3\n" \
  "s_lshl_b32 s62, s62, 11\n" \
  "s_xor_b32 s61, s61, s62\n" \
  "s_xor_b32 s68, s61, %25\n" \
  "s_add_u32 s60, s60, 1\n" \
  "s_lshr_b32 s61, s60, 2\n" \
  "s_lshl_b32 s61, s61, 14\n" \
  "s_and_b32 s62, s60, 3\n" \
  "s_lshl_b32 s62, s62, 11\n" \
  "s_xor_b32 s61, s61, s62\n" \
  "s_xor_b32 s74, s61, %25\n" \
  "s_add_u32 s60, s60, 1\n" \
  "s_lshr_b32 s61, s60, 2\n" \
  "s_lshl_b32 s61, s61, 14\n" \
  "s_and_b32 s62, s60, 3\n" \
  "s_lshl_b32 s62, s62, 11\n" \
  "s_xor_b32 s61, s61, s62\n" \
  "s_xor_b32 s75, s61, %25\n" \
  "s_add_u32 s60, s60, 1\n" \
  "s_xor_b32 s71, s56, s57\n" \
  "s_movk_i32 s88, -32768\n" \
  "v_mov_b64 v[136:137], -1\n" \
  "v_mov_b64 v[138:139], -1\n" \
  "v_mov_b64 v[140:141], 0\n" \
  "v_mov_b32 v146, v220\n" \
  "s_mov_b32 s90, 11184810\n" \
  "s_mov_b32 s91, 0\n" \
  "s_mov_b64 vcc, -1\n" \
  "s_barrier\n" \
  ".LR_hl_%=:\n" \
  "s_and_b32 s61, s48, 15\n" \
  "s_cmp_ge_u32 s48, 64\n" \
  "s_cselect_b32 s60, 2048, 0\n" \
  "s_or_b32 s61, s61, s60\n" \
  "s_lshl_b32 s61, s61, 16\n" \
  "s_bfe_u32 s60, %32, 524288\n" \
  ".LR_hs_%=:\n" \
  "s_cmp_eq_u32 s60, 0\n" \
  "s_cbranch_scc1 .LR_hs_x_%=\n" \
  "s_sleep 1\n" \
  "s_sub_u32 s60, s60, 1\n" \
  "s_branch .LR_hs_%=\n" \
  ".LR_hs_x_%=:\n" \
  "s_xor_b32 s60, s71, s57\n" \
  "v_xor_b32 v145, s60, v224\n" \
  "buffer_store_dwordx4 v[136:139], v145, s[40:43], 0 offen sc1\n" \
  "s_cbranch_vccnz .LR_fu_%=\n" \
  "s_cmp_lg_u32 %19, 3\n" \
  "s_cbranch_scc1 .LR_h5_%=\n" \
  "s_xor_b32 s60, s61, s75\n" \
  "buffer_load_dwordx4 v[40:43], v214, s[40:43], s60 offen sc1\n" \
  "s_xor_b32 s60, s60, 1024\n" \
  "buffer_load_dwordx4 v[44:47], v214, s[40:43], s60 offen sc1\n" \
  ".LR_h5_%=:\n" \
  "s_xor_b32 s60, s61, s64\n" \
  "buffer_load_dwordx4 v[0:3], v214, s[40:43], s60 offen sc1\n" \
  "s_xor_b32 s60, s60, 1024\n" \
  "buffer_load_dwordx4 v[4:7], v214, s[40:43], s60 offen sc1\n" \
  "s_xor_b32 s60, s61, s65\n" \
  "buffer_load_dwordx4 v[8:11], v214, s[40:43], s60 offen sc1\n" \
  "s_xor_b32 s60, s60, 1024\n" \
  "buffer_load_dwordx4 v[12:15], v214, s[40:43], s60 offen sc1\n" \
  "s_xor_b32 s60, s61, s67\n" \
  "buffer_load_dwordx4 v[16:19], v214, s[40:43], s60 offen sc1\n" \
  "s_xor_b32 s60, s60, 1024\n" \
  "buffer_load_dwordx4 v[20:23], v214, s[40:43], s60 offen sc1\n" \
  "s_cmp_lt_u32 s48, 62\n" \
  "s_cselect_b32 s60, s71, 2130706432\n" \
  "v_xor_b32 v142, s60, v221\n" \
  "v_xor_b32 v143, s60, v222\n" \
  "v_xor_b32 v144, s60, v223\n" \
  "buffer_store_dwordx4 v[136:139], v142, s[40:43], 0 offen sc1\n" \
  "buffer_store_dwordx4 v[136:139], v143, s[40:43], 0 offen sc1\n" \
  "buffer_store_dwordx4 v[136:139], v144, s[40:43], 0 offen sc1\n" \
  "s_waitcnt vmcnt(9)\n" \
  "v_mov_b32 v72, v24\n" \
  "v_mov_b32 v73, v28\n" \
  "v_mov_b32 v74, v25\n" \
  "v_mov_b32 v75, v29\n" \
  "v_mov_b32 v76, v26\n" \
  "v_mov_b32 v77, v30\n" \
  "v_mov_b32 v78, v27\n" \
  "v_mov_b32 v79, v31\n" \
  "ds_write_b128 v211, v[72:75] offset:6144\n" \
  "ds_write_b128 v211, v[76:79] offset:7168\n" \
  "v_mov_b32 v80, v32\n" \
  "v_mov_b32 v81, v36\n" \
  "v_mov_b32 v82, v33\n" \
  "v_mov_b32 v83, v37\n" \
  "v_mov_b32 v84, v34\n" \
  "v_mov_b32 v85, v38\n" \
  "v_mov_b32 v86, v35\n" \
  "v_mov_b32 v87, v39\n" \
  "ds_write_b128 v211, v[80:83] offset:8192\n" \
  "ds_write_b128 v211, v[84:87] offset:9216\n" \
  "s_barrier\n" \
  ".LR_fu_b_%=:\n" \
  "s_add_u32 s48, s48, 1\n" \
  "v_cmp_ne_u32 vcc, %21, v141\n" \
  "s_nop 1\n" \
  "v_cndmask_b32 v140, v140, 0, vcc\n" \
  "v_cmp_ge_i32 vcc, s48, v140\n" \
  "s_bitcmp1_b64 s[90:91], s48\n" \
  "s_cselect_b32 vcc_lo, 0, vcc_lo\n" \
  "s_cselect_b32 vcc_hi, vcc_hi, 0\n" \
  "s_mov_b64 vcc, vcc\n" \
  "s_and_b32 s61, s48, 15\n" \
  "s_cmp_ge_u32 s48, 64\n" \
  "s_cselect_b32 s60, 2048, 0\n" \
  "s_or_b32 s61, s61, s60\n" \
  "s_lshl_b32 s61, s61, 16\n" \
  "s_cbranch_vccnz .LR_nb0_%=\n" \
  "s_bfe_u32 s60, %32, 524296\n" \
  ".LR_ht_%=:\n" \
  "s_cmp_eq_u32 s60, 0\n" \
  "s_cbranch_scc1 .LR_ht_x_%=\n" \
  "s_sleep 1\n" \
  "s_sub_u32 s60, s60, 1\n" \
  "s_branch .LR_ht_%=\n" \
  ".LR_ht_x_%=:\n" \
  "s_xor_b32 s60, s61, s68\n" \
  "buffer_load_dwordx4 v[24:27], v214, s[40:43], s60 offen sc1\n" \
  "s_xor_b32 s60, s60, 1024\n" \
  "buffer_load_dwordx4 v[28:31], v214, s[40:43], s60 offen sc1\n" \
  "s_xor_b32 s60, s61, s74\n" \
  "buffer_load_dwordx4 v[32:35], v214, s[40:43], s60 offen sc1\n" \
  "s_xor_b32 s60, s60, 1024\n" \
  "buffer_load_dwordx4 v[36:39], v214, s[40:43], s60 offen sc1\n" \
  "s_waitcnt vmcnt(4)\n" \
  ".LR_nb1_%=:\n" \
  "s_add_u32 s60, s48, 3\n" \
  "s_min_u32 s60, s60, 63\n" \
  "s_min_u32 s61, s60, 63\n" \
  "v_cmp_ge_i32 s[76:77], s60, v140\n" \
  "v_cmp_ge_i32 s[78:79], s61, v140\n" \
  "s_sub_u32 s61, s48, 24\n" \
  "s_cselect_b32 s60, 0, 32\n" \
  "s_cmp_lt_u32 s61, 37\n" \
  "s_cselect_b32 s77, 0, s77\n" \
  "v_mad_u32_u24 v146, s60, v218, v220\n" \
  "s_or_b32 s76, s78, s77\n" \
  "s_cmp_eq_u32 s76, 0\n" \
  "s_cbranch_scc1 .LR_nf_%=\n" \
  "buffer_load_dwordx2 v[140:141], v146, s[40:43], 0 offen sc1\n" \
  ".LR_nf_%=:\n" \
  "v_mov_b32 v48, v0\n" \
  "v_mov_b32 v49, v4\n" \
  "v_mov_b32 v50, v1\n" \
  "v_mov_b32 v51, v5\n" \
  "v_mov_b32 v52, v2\n" \
  "v_mov_b32 v53, v6\n" \
  "v_mov_b32 v54, v3\n" \
  "v_mov_b32 v55, v7\n" \
  "ds_write_b128 v211, v[48:51] offset:0\n" \
  "ds_write_b128 v211, v[52:55] offset:1024\n" \
  "v_mov_b32 v56, v8\n" \
  "v_mov_b32 v57, v12\n" \
  "v_mov_b32 v58, v9\n" \
  "v_mov_b32 v59, v13\n" \
  "v_mov_b32 v60, v10\n" \
  "v_mov_b32 v61, v14\n" \
  "v_mov_b32 v62, v11\n" \
  "v_mov_b32 v63, v15\n" \
  "ds_write_b128 v211, v[56:59] offset:2048\n" \
  "ds_write_b128 v211, v[60:63] offset:3072\n" \
  "v_mov_b32 v64, v16\n" \
  "v_mov_b32 v65, v20\n" \
  "v_mov_b32 v66, v17\n" \
  "v_mov_b32 v67, v21\n" \
  "v_mov_b32 v68, v18\n" \
  "v_mov_b32 v69, v22\n" \
  "v_mov_b32 v70, v19\n" \
  "v_mov_b32 v71, v23\n" \
  "ds_write_b128 v211, v[64:67] offset:4096\n" \
  "ds_write_b128 v211, v[68:71] offset:5120\n" \
  "s_cmp_lg_u32 %19, 3\n" \
  "s_cbranch_scc1 .LR_h6_%=\n" \
  "v_mov_b32 v88, v40\n" \
  "v_mov_b32 v89, v44\n" \
  "v_mov_b32 v90, v41\n" \
  "v_mov_b32 v91, v45\n" \
  "v_mov_b32 v92, v42\n" \
  "v_mov_b32 v93, v46\n" \
  "v_mov_b32 v94, v43\n" \
  "v_mov_b32 v95, v47\n" \
  "ds_write_b128 v211, v[88:91] offset:10240\n" \
  "ds_write_b128 v211, v[92:95] offset:11264\n" \
  ".LR_h6_%=:\n" \
  "s_sub_u32 s88, 0, s88\n" \
  "s_xor_b32 s71, s71, s56\n" \
  "s_mov_b32 s61, s56\n" \
  "s_mov_b32 s56, s57\n" \
  "s_mov_b32 s57, s61\n" \
  "s_waitcnt lgkmcnt(0)\n" \
  "v_add_u32 v211, s88, v211\n" \
  "s_barrier\n" \
  "s_cmp_lt_u32 s48, 63\n" \
  "s_cbranch_scc1 .LR_hl_%=\n" \
  "s_barrier\n" \
  ".LR_end_%=:\n" \
  "s_waitcnt vmcnt(0)\n"
#define R_ASM_VOPS "+{v212}"(ri.sidx), "+{v213}"(ri.cidx), "+{v214}"(ri.l16), "+{v215}"(ri.ax), "+{v216}"(ri.am), "+{v217}"(ri.mo), "+{v218}"(ri.sel), "+{v219}"(ri.to), "+{v220}"(ri.fu), "+{v221}"(ri.p0), "+{v222}"(ri.p1), "+{v223}"(ri.p2), "+{v224}"(ri.p3), "+{v225}"(ri.d0), "+{v226}"(ri.d1), "+{v227}"(ri.d2), "+{v228}"(ri.d3), "+{v229}"(ri.d4)
#define R_ASM_SOPS "s"(ri.ws), "s"(ri.w), "s"(ri.go), "s"(ri.tag), "s"(ri.o_va), "s"(ri.o_vb), "s"(ri.lab), "s"(ri.o_u), "s"(ri.kind), "s"(ri.o_ctl), "s"(ri.pa), "s"(ri.pb), "s"(ri.f_d), "s"(ri.f_r), "s"(ri.ph)
#define R_ASM_CLOB "v0", "v1", "v2", "v3", "v4", "v5", "v6", "v7", "v8", "v9", "v10", "v11", "v12", "v13", "v14", "v15", "v16", "v17", "v18", "v19", "v20", "v21", "v22", "v23", "v24", "v25", "v26", "v27", "v28", "v29", "v30", "v31", "v32", "v33", "v34", "v35", "v36", "v37", "v38", "v39", "v40", "v41", "v42", "v43", "v44", "v45", "v46", "v47", "v48", "v49", "v50", "v51", "v52", "v53", "v54", "v55", "v56", "v57", "v58", "v59", "v60", "v61", "v62", "v63", "v64", "v65", "v66", "v67", "v68", "v69", "v70", "v71", "v72", "v73", "v74", "v75", "v76", "v77", "v78", "v79", "v80", "v81", "v82", "v83", "v84", "v85", "v86", "v87", "v88", "v89", "v90", "v91", "v92", "v93", "v94", "v95", "v96", "v97", "v98", "v99", "v100", "v101", "v102", "v103", "v104", "v105", "v106", "v107", "v108", "v109", "v110", "v111", "v112", "v113", "v114", "v115", "v116", "v117", "v118", "v119", "v120", "v121", "v122", "v123", "v124", "v125", "v126", "v127", "v128", "v129", "v130", "v131", "v132", "v133", "v134", "v135", "v136", "v137", "v138", "v139", "v140", "v141", "v142", "v143", "v144", "v145", "v146", "v147", "v148", "v149", "v150", "v151", "v152", "v153", "v154", "v155", "v156", "v157", "v158", "v159", "v160", "v161", "v162", "v163", "v164", "v165", "v166", "v167", "v168", "v169", "v170", "v171", "v172", "v173", "v174", "v175", "v176", "v177", "v178", "v179", "v180", "v181", "v182", "v183", "v184", "v185", "v186", "v187", "v188", "v189", "v190", "v191", "v192", "v193", "v194", "v195", "v196", "v197", "v198", "v199", "v200", "v201", "v202", "v203", "v204", "v205", "v206", "v207", "v208", "v209", "v210", "v211", "a0", "a1", "a2", "a3", "a4", "a5", "a6", "a7", "a8", "a9", "a10", "a11", "a12", "a13", "a14", "a15", "a16", "a17", "a18", "a19", "a20", "a21", "a22", "a23", "a24", "a25", "a26", "a27", "a28", "a29", "a30", "a31", "a32", "a33", "a34", "a35", "a36", "a37", "a38", "a39", "a40", "a41", "a42", "a43", "a44", "a45", "a46", "a47", "a48", "a49", "a50", "a51", "a52", "a53", "a54", "a55", "a56", "a57", "a58", "a59", "a60", "a61", "a62", "a63", "a64", "a65", "a66", "a67", "a68", "a69", "a70", "a71", "a72", "a73", "a74", "a75", "a76", "a77", "a78", "a79", "a80", "a81", "a82", "a83", "a84", "a85", "a86", "a87", "a88", "a89", "a90", "a91", "a92", "a93", "a94", "a95", "a96", "a97", "a98", "a99", "a100", "a101", "a102", "a103", "a104", "a105", "a106", "a107", "a108", "a109", "a110", "a111", "a112", "a113", "a114", "a115", "a116", "a117", "a118", "a119", "a120", "a121", "a122", "a123", "a124", "a125", "a126", "a127", "s40", "s41", "s42", "s43", "s44", "s45", "s46", "s47", "s48", "s49", "s50", "s51", "s52", "s53", "s54", "s55", "s56", "s57", "s58", "s59", "s60", "s61", "s62", "s63", "s64", "s65", "s66", "s67", "s68", "s69", "s70", "s71", "s72", "s73", "s74", "s75", "s76", "s77", "s78", "s79", "s80", "s81", "s82", "s83", "s84", "s85", "s86", "s87", "s88", "s89", "s90", "s91", "s92", "s93", "s94", "s95", "s96", "s97", "s98", "s99", "vcc", "scc", "memory"
#define K_ASM_TEXT \
  "s_mov_b64 s[40:41], %30\n" \
  "s_and_b32 s41, s41, 0xffff\n" \
  "s_mov_b32 s42, 255852544\n" \
  "s_mov_b32 s43, 0x00020000\n" \
  "s_mov_b32 s47, 0x00020000\n" \
  "s_lshl_b32 s72, %34, 12\n" \
  "buffer_load_dword v184, off, s[40:43], %42 sc1\n" \
  "s_mov_b64 s[44:45], %31\n" \
  "s_and_b32 s45, s45, 0xffff\n" \
  "s_mov_b32 s46, 4718592\n" \
  "s_add_u32 s60, %35, 0\n" \
  "s_add_u32 m0, s72, 16384\n" \
  "s_nop 0\n" \
  "buffer_load_dwordx4 v170, s[44:47], s60 offen lds\n" \
  "buffer_load_dwordx4 v171, s[44:47], s60 offen offset:1024 lds\n" \
  "buffer_load_dwordx4 v172, s[44:47], s60 offen offset:2048 lds\n" \
  "buffer_load_dwordx4 v173, s[44:47], s60 offen offset:3072 lds\n" \
  "s_add_u32 s60, %35, 524288\n" \
  "s_add_u32 m0, s72, 32768\n" \
  "s_nop 0\n" \
  "buffer_load_dwordx4 v170, s[44:47], s60 offen lds\n" \
  "buffer_load_dwordx4 v171, s[44:47], s60 offen offset:1024 lds\n" \
  "buffer_load_dwordx4 v172, s[44:47], s60 offen offset:2048 lds\n" \
  "buffer_load_dwordx4 v173, s[44:47], s60 offen offset:3072 lds\n" \
  "s_add_u32 s60, %35, 1048576\n" \
  "s_add_u32 m0, s72, 49152\n" \
  "s_nop 0\n" \
  "buffer_load_dwordx4 v170, s[44:47], s60 offen lds\n" \
  "buffer_load_dwordx4 v171, s[44:47], s60 offen offset:1024 lds\n" \
  "buffer_load_dwordx4 v172, s[44:47], s60 offen offset:2048 lds\n" \
  "buffer_load_dwordx4 v173, s[44:47], s60 offen offset:3072 lds\n" \
  "s_add_u32 s60, %35, 1572864\n" \
  "s_add_u32 m0, s72, 65536\n" \
  "s_nop 0\n" \
  "buffer_load_dwordx4 v170, s[44:47], s60 offen lds\n" \
  "buffer_load_dwordx4 v171, s[44:47], s60 offen offset:1024 lds\n" \
  "buffer_load_dwordx4 v172, s[44:47], s60 offen offset:2048 lds\n" \
  "buffer_load_dwordx4 v173, s[44:47], s60 offen offset:3072 lds\n" \
  "s_add_u32 s60, %35, 2097152\n" \
  "s_add_u32 m0, s72, 81920\n" \
  "s_nop 0\n" \
  "buffer_load_dwordx4 v170, s[44:47], s60 offen lds\n" \
  "buffer_load_dwordx4 v171, s[44:47], s60 offen offset:1024 lds\n" \
  "buffer_load_dwordx4 v172, s[44:47], s60 offen offset:2048 lds\n" \
  "buffer_load_dwordx4 v173, s[44:47], s60 offen offset:3072 lds\n" \
  "s_add_u32 s60, %35, 2621440\n" \
  "s_add_u32 m0, s72, 98304\n" \
  "s_nop 0\n" \
  "buffer_load_dwordx4 v170, s[44:47], s60 offen lds\n" \
  "buffer_load_dwordx4 v171, s[44:47], s60 offen offset:1024 lds\n" \
  "buffer_load_dwordx4 v172, s[44:47], s60 offen offset:2048 lds\n" \
  "buffer_load_dwordx4 v173, s[44:47], s60 offen offset:3072 lds\n" \
  "s_add_u32 s60, %35, 3145728\n" \
  "s_add_u32 m0, s72, 114688\n" \
  "s_nop 0\n" \
  "buffer_load_dwordx4 v170, s[44:47], s60 offen lds\n" \
  "buffer_load_dwordx4 v171, s[44:47], s60 offen offset:1024 lds\n" \
  "buffer_load_dwordx4 v172, s[44:47], s60 offen offset:2048 lds\n" \
  "buffer_load_dwordx4 v173, s[44:47], s60 offen offset:3072 lds\n" \
  "s_add_u32 s60, %35, 3670016\n" \
  "s_add_u32 m0, s72, 131072\n" \
  "s_nop 0\n" \
  "buffer_load_dwordx4 v170, s[44:47], s60 offen lds\n" \
  "buffer_load_dwordx4 v171, s[44:47], s60 offen offset:1024 lds\n" \
  "buffer_load_dwordx4 v172, s[44:47], s60 offen offset:2048 lds\n" \
  "buffer_load_dwordx4 v173, s[44:47], s60 offen offset:3072 lds\n" \
  "s_add_u32 s60, %35, 4194304\n" \
  "s_add_u32 m0, s72, 147456\n" \
  "s_nop 0\n" \
  "buffer_load_dwordx4 v170, s[44:47], s60 offen lds\n" \
  "buffer_load_dwordx4 v171, s[44:47], s60 offen offset:1024 lds\n" \
  "buffer_load_dwordx4 v172, s[44:47], s60 offen offset:2048 lds\n" \
  "buffer_load_dwordx4 v173, s[44:47], s60 offen offset:3072 lds\n" \
  "s_waitcnt vmcnt(24)\n" \
  "s_barrier\n" \
  "v_add_u32 v186, 16384, v182\n" \
  "ds_read_b64_tr_b16 a[0:1], v186\n" \
  "ds_read_b64_tr_b16 a[2:3], v186 offset:1024\n" \
  "ds_read_b64_tr_b16 a[4:5], v186 offset:8192\n" \
  "ds_read_b64_tr_b16 a[6:7], v186 offset:9216\n" \
  "v_add_u32 v187, 16384, v183\n" \
  "ds_read_b64_tr_b16 a[72:73], v187\n" \
  "ds_read_b64_tr_b16 a[74:75], v187 offset:1024\n" \
  "ds_read_b64_tr_b16 a[76:77], v187 offset:8192\n" \
  "ds_read_b64_tr_b16 a[78:79], v187 offset:9216\n" \
  "v_add_u32 v186, 32768, v182\n" \
  "ds_read_b64_tr_b16 a[8:9], v186\n" \
  "ds_read_b64_tr_b16 a[10:11], v186 offset:1024\n" \
  "ds_read_b64_tr_b16 a[12:13], v186 offset:8192\n" \
  "ds_read_b64_tr_b16 a[14:15], v186 offset:9216\n" \
  "v_add_u32 v187, 32768, v183\n" \
  "ds_read_b64_tr_b16 a[80:81], v187\n" \
  "ds_read_b64_tr_b16 a[82:83], v187 offset:1024\n" \
  "ds_read_b64_tr_b16 a[84:85], v187 offset:8192\n" \
  "ds_read_b64_tr_b16 a[86:87], v187 offset:9216\n" \
  "v_add_u32 v186, 49152, v182\n" \
  "ds_read_b64_tr_b16 a[16:17], v186\n" \
  "ds_read_b64_tr_b16 a[18:19], v186 offset:1024\n" \
  "ds_read_b64_tr_b16 a[20:21], v186 offset:8192\n" \
  "ds_read_b64_tr_b16 a[22:23], v186 offset:9216\n" \
  "v_add_u32 v187, 49152, v183\n" \
  "ds_read_b64_tr_b16 a[88:89], v187\n" \
  "ds_read_b64_tr_b16 a[90:91], v187 offset:1024\n" \
  "ds_read_b64_tr_b16 a[92:93], v187 offset:8192\n" \
  "ds_read_b64_tr_b16 a[94:95], v187 offset:9216\n" \
  "s_waitcnt lgkmcnt(0)\n" \
  "s_barrier\n" \
  "v_readfirstlane_b32 s55, v184\n" \
  "s_add_u32 s55, s55, 1\n" \
  "s_mov_b64 s[44:45], %33\n" \
  "s_and_b32 s45, s45, 0xffff\n" \
  "s_mov_b32 s46, 2359296\n" \
  "s_mov_b32 s51, 0\n" \
  "s_mul_i32 s52, %34, 9216\n" \
  "s_add_u32 s52, s52, 16384\n" \
  "s_add_u32 m0, s52, 0\n" \
  "s_nop 0\n" \
  "buffer_load_dwordx4 v196, s[44:47], s51 offen lds\n" \
  "s_add_u32 m0, s52, 1024\n" \
  "s_nop 0\n" \
  "buffer_load_dwordx4 v197, s[44:47], s51 offen lds\n" \
  "s_add_u32 m0, s52, 2048\n" \
  "s_nop 0\n" \
  "buffer_load_dwordx4 v198, s[44:47], s51 offen lds\n" \
  "s_add_u32 m0, s52, 3072\n" \
  "s_nop 0\n" \
  "buffer_load_dwordx4 v199, s[44:47], s51 offen lds\n" \
  "s_add_u32 m0, s52, 4096\n" \
  "s_nop 0\n" \
  "buffer_load_dwordx4 v200, s[44:47], s51 offen lds\n" \
  "s_add_u32 m0, s52, 5120\n" \
  "s_nop 0\n" \
  "buffer_load_dwordx4 v201, s[44:47], s51 offen lds\n" \
  "s_add_u32 m0, s52, 6144\n" \
  "s_nop 0\n" \
  "buffer_load_dwordx4 v202, s[44:47], s51 offen lds\n" \
  "s_add_u32 m0, s52, 7168\n" \
  "s_nop 0\n" \
  "buffer_load_dwordx4 v203, s[44:47], s51 offen lds\n" \
  "s_add_u32 m0, s52, 8192\n" \
  "s_nop 0\n" \
  "buffer_load_dwordx4 v204, s[44:47], s51 offen lds\n" \
  "s_add_u32 s51, s51, 36864\n" \
  "s_min_u32 s51, s51, 2322432\n" \
  "s_add_u32 s52, s52, 36864\n" \
  "s_cmp_ge_u32 s52, 163840\n" \
  "s_cselect_b32 s60, 147456, 0\n" \
  "s_sub_u32 s52, s52, s60\n" \
  "s_waitcnt vmcnt(21)\n" \
  "s_barrier\n" \
  "v_add_u32 v186, 65536, v182\n" \
  "ds_read_b64_tr_b16 a[24:25], v186\n" \
  "ds_read_b64_tr_b16 a[26:27], v186 offset:1024\n" \
  "ds_read_b64_tr_b16 a[28:29], v186 offset:8192\n" \
  "ds_read_b64_tr_b16 a[30:31], v186 offset:9216\n" \
  "v_add_u32 v187, 65536, v183\n" \
  "ds_read_b64_tr_b16 a[96:97], v187\n" \
  "ds_read_b64_tr_b16 a[98:99], v187 offset:1024\n" \
  "ds_read_b64_tr_b16 a[100:101], v187 offset:8192\n" \
  "ds_read_b64_tr_b16 a[102:103], v187 offset:9216\n" \
  "v_add_u32 v186, 81920, v182\n" \
  "ds_read_b64_tr_b16 a[32:33], v186\n" \
  "ds_read_b64_tr_b16 a[34:35], v186 offset:1024\n" \
  "ds_read_b64_tr_b16 a[36:37], v186 offset:8192\n" \
  "ds_read_b64_tr_b16 a[38:39], v186 offset:9216\n" \
  "v_add_u32 v187, 81920, v183\n" \
  "ds_read_b64_tr_b16 a[104:105], v187\n" \
  "ds_read_b64_tr_b16 a[106:107], v187 offset:1024\n" \
  "ds_read_b64_tr_b16 a[108:109], v187 offset:8192\n" \
  "ds_read_b64_tr_b16 a[110:111], v187 offset:9216\n" \
  "v_add_u32 v186, 98304, v182\n" \
  "ds_read_b64_tr_b16 a[40:41], v186\n" \
  "ds_read_b64_tr_b16 a[42:43], v186 offset:1024\n" \
  "ds_read_b64_tr_b16 a[44:45], v186 offset:8192\n" \
  "ds_read_b64_tr_b16 a[46:47], v186 offset:9216\n" \
  "v_add_u32 v187, 98304, v183\n" \
  "ds_read_b64_tr_b16 a[112:113], v187\n" \
  "ds_read_b64_tr_b16 a[114:115], v187 offset:1024\n" \
  "ds_read_b64_tr_b16 a[116:117], v187 offset:8192\n" \
  "ds_read_b64_tr_b16 a[118:119], v187 offset:9216\n" \
  "s_waitcnt lgkmcnt(0)\n" \
  "s_barrier\n" \
  "buffer_load_dwordx2 v[184:185], off, s[40:43], %41 sc1\n" \
  "s_add_u32 m0, s52, 0\n" \
  "s_nop 0\n" \
  "buffer_load_dwordx4 v196, s[44:47], s51 offen lds\n" \
  "s_add_u32 m0, s52, 1024\n" \
  "s_nop 0\n" \
  "buffer_load_dwordx4 v197, s[44:47], s51 offen lds\n" \
  "s_add_u32 m0, s52, 2048\n" \
  "s_nop 0\n" \
  "buffer_load_dwordx4 v198, s[44:47], s51 offen lds\n" \
  "s_add_u32 m0, s52, 3072\n" \
  "s_nop 0\n" \
  "buffer_load_dwordx4 v199, s[44:47], s51 offen lds\n" \
  "s_add_u32 m0, s52, 4096\n" \
  "s_nop 0\n" \
  "buffer_load_dwordx4 v200, s[44:47], s51 offen lds\n" \
  "s_add_u32 m0, s52, 5120\n" \
  "s_nop 0\n" \
  "buffer_load_dwordx4 v201, s[44:47], s51 offen lds\n" \
  "s_add_u32 m0, s52, 6144\n" \
  "s_nop 0\n" \
  "buffer_load_dwordx4 v202, s[44:47], s51 offen lds\n" \
  "s_add_u32 m0, s52, 7168\n" \
  "s_nop 0\n" \
  "buffer_load_dwordx4 v203, s[44:47], s51 offen lds\n" \
  "s_add_u32 m0, s52, 8192\n" \
  "s_nop 0\n" \
  "buffer_load_dwordx4 v204, s[44:47], s51 offen lds\n" \
  "s_add_u32 s51, s51, 36864\n" \
  "s_min_u32 s51, s51, 2322432\n" \
  "s_add_u32 s52, s52, 36864\n" \
  "s_cmp_ge_u32 s52, 163840\n" \
  "s_cselect_b32 s60, 147456, 0\n" \
  "s_sub_u32 s52, s52, s60\n" \
  "s_waitcnt vmcnt(19)\n" \
  "s_barrier\n" \
  "v_add_u32 v186, 114688, v182\n" \
  "ds_read_b64_tr_b16 a[48:49], v186\n" \
  "ds_read_b64_tr_b16 a[50:51], v186 offset:1024\n" \
  "ds_read_b64_tr_b16 a[52:53], v186 offset:8192\n" \
  "ds_read_b64_tr_b16 a[54:55], v186 offset:9216\n" \
  "v_add_u32 v187, 114688, v183\n" \
  "ds_read_b64_tr_b16 a[120:121], v187\n" \
  "ds_read_b64_tr_b16 a[122:123], v187 offset:1024\n" \
  "ds_read_b64_tr_b16 a[124:125], v187 offset:8192\n" \
  "ds_read_b64_tr_b16 a[126:127], v187 offset:9216\n" \
  "v_add_u32 v186, 131072, v182\n" \
  "ds_read_b64_tr_b16 a[56:57], v186\n" \
  "ds_read_b64_tr_b16 a[58:59], v186 offset:1024\n" \
  "ds_read_b64_tr_b16 a[60:61], v186 offset:8192\n" \
  "ds_read_b64_tr_b16 a[62:63], v186 offset:9216\n" \
  "v_add_u32 v187, 131072, v183\n" \
  "ds_read_b64_tr_b16 a[128:129], v187\n" \
  "ds_read_b64_tr_b16 a[130:131], v187 offset:1024\n" \
  "ds_read_b64_tr_b16 a[132:133], v187 offset:8192\n" \
  "ds_read_b64_tr_b16 a[134:135], v187 offset:9216\n" \
  "v_add_u32 v186, 147456, v182\n" \
  "ds_read_b64_tr_b16 a[64:65], v186\n" \
  "ds_read_b64_tr_b16 a[66:67], v186 offset:1024\n" \
  "ds_read_b64_tr_b16 a[68:69], v186 offset:8192\n" \
  "ds_read_b64_tr_b16 a[70:71], v186 offset:9216\n" \
  "v_add_u32 v187, 147456, v183\n" \
  "ds_read_b64_tr_b16 a[136:137], v187\n" \
  "ds_read_b64_tr_b16 a[138:139], v187 offset:1024\n" \
  "ds_read_b64_tr_b16 a[140:141], v187 offset:8192\n" \
  "ds_read_b64_tr_b16 a[142:143], v187 offset:9216\n" \
  "s_waitcnt lgkmcnt(0)\n" \
  "s_mov_b32 s69, 0\n" \
  "s_waitcnt vmcnt(9)\n" \
  "s_branch .LK_go1_%=\n" \
  ".LK_go_%=:\n" \
  "buffer_load_dwordx2 v[184:185], off, s[40:43], %41 sc1\n" \
  "s_waitcnt vmcnt(0)\n" \
  ".LK_go1_%=:\n" \
  "v_readfirstlane_b32 s96, v184\n" \
  "v_readfirstlane_b32 s63, v185\n" \
  "s_cmp_eq_u32 s63, s55\n" \
  "s_cselect_b32 s96, s96, 0\n" \
  "s_cmp_ge_u32 s96, 0x80000000\n" \
  "s_cbranch_scc1 .LK_go_x_%=\n" \
  "s_sleep 4\n" \
  "s_add_u32 s69, s69, 1\n" \
  "s_cmp_lt_u32 s69, 2097152\n" \
  "s_cbranch_scc1 .LK_go_%=\n" \
  "v_mov_b32 v184, 1\n" \
  "v_mov_b32 v185, s55\n" \
  "buffer_store_dwordx2 v[184:185], off, s[40:43], %42 offset:128 sc1\n" \
  ".LK_go_x_%=:\n" \
  "s_and_b32 s60, s96, 2\n" \
  "s_lshl_b32 s60, s60, 12\n" \
  "s_xor_b32 s53, %39, s60\n" \
  "s_mov_b32 s54, %40\n" \
  "s_andn2_b32 s60, 2, s96\n" \
  "s_lshl_b32 s60, s60, 12\n" \
  "s_add_u32 s49, %37, s60\n" \
  "s_add_u32 s50, %38, s60\n" \
  "s_xor_b32 s66, %38, s60\n" \
  "s_xor_b32 s59, %44, s60\n" \
  "s_or_b32 s50, s50, 0x40000000\n" \
  "s_mov_b32 s57, 2130706432\n" \
  "s_mov_b32 s48, 0\n" \
  "v_mov_b32 v169, s55\n" \
  "v_mov_b32 v167, 2130706432\n" \
  "v_mov_b32 v166, -1\n" \
  "s_mov_b64 exec, 3\n" \
  "v_mov_b32 v167, s54\n" \
  "s_mov_b64 exec, 1\n" \
  "v_mov_b32 v167, s53\n" \
  "v_mov_b32 v166, 0\n" \
  "s_mov_b64 exec, -1\n" \
  "s_barrier\n" \
  "ds_read_b128 v[32:35], v191\n" \
  "ds_read_b128 v[36:39], v191 offset:18432\n" \
  "ds_read_b128 v[40:43], v192\n" \
  "ds_read_b128 v[44:47], v192 offset:18432\n" \
  "ds_read_b128 v[48:51], v191 offset:128\n" \
  "ds_read_b128 v[52:55], v191 offset:18560\n" \
  "s_waitcnt lgkmcnt(0)\n" \
  "s_sub_u32 s49, s49, 524288\n" \
  "s_or_b32 s49, s49, 0x40000000\n" \
  "s_mov_b32 s56, -1\n" \
  "s_mov_b32 s57, 0x10000\n" \
  "s_mov_b32 s71, 36864\n" \
  ".LK_ma_%=:\n" \
  "v_mfma_f32_16x16x32_bf16 v[64:67], a[0:3], v[32:35], 0\n" \
  "ds_read_b128 v[56:59], v192 offset:128\n" \
  "v_mfma_f32_16x16x32_bf16 v[72:75], a[0:3], v[36:39], 0\n" \
  "ds_read_b128 v[60:63], v192 offset:18560\n" \
  "s_lshr_b32 s62, s49, 8\n" \
  "v_mfma_f32_16x16x32_bf16 v[68:71], a[72:75], v[32:35], 0\n" \
  "s_add_u32 m0, s52, 0\n" \
  "s_and_b32 s62, s62, 8192\n" \
  "s_xor_b32 s62, s62, s49\n" \
  "v_mfma_f32_16x16x32_bf16 v[76:79], a[72:75], v[36:39], 0\n" \
  "buffer_load_dwordx4 v196, s[44:47], s51 offen lds\n" \
  "v_mfma_f32_16x16x32_bf16 v[64:67], a[4:7], v[40:43], v[64:67]\n" \
  "ds_read_b128 v[32:35], v191 offset:256\n" \
  "v_cvt_pk_bf16_f32 v96, v80, v81\n" \
  "v_mfma_f32_16x16x32_bf16 v[72:75], a[4:7], v[44:47], v[72:75]\n" \
  "ds_read_b128 v[36:39], v191 offset:18688\n" \
  "v_cvt_pk_bf16_f32 v97, v82, v83\n" \
  "v_mfma_f32_16x16x32_bf16 v[68:71], a[76:79], v[40:43], v[68:71]\n" \
  "v_cvt_pk_bf16_f32 v98, v84, v85\n" \
  "v_cvt_pk_bf16_f32 v99, v86, v87\n" \
  "s_lshl_b32 s63, s57, 14\n" \
  "v_mfma_f32_16x16x32_bf16 v[76:79], a[76:79], v[44:47], v[76:79]\n" \
  "s_xor_b32 s63, s63, s59\n" \
  "v_mfma_f32_16x16x32_bf16 v[64:67], a[8:11], v[48:51], v[64:67]\n" \
  "ds_read_b128 v[40:43], v192 offset:256\n" \
  "v_mfma_f32_16x16x32_bf16 v[72:75], a[8:11], v[52:55], v[72:75]\n" \
  "ds_read_b128 v[44:47], v192 offset:18688\n" \
  "v_mfma_f32_16x16x32_bf16 v[68:71], a[80:83], v[48:51], v[68:71]\n" \
  "s_add_u32 m0, s52, 1024\n" \
  "v_mfma_f32_16x16x32_bf16 v[76:79], a[80:83], v[52:55], v[76:79]\n" \
  "buffer_load_dwordx4 v197, s[44:47], s51 offen lds\n" \
  "s_waitcnt lgkmcnt(5)\n" \
  "v_mfma_f32_16x16x32_bf16 v[64:67], a[12:15], v[56:59], v[64:67]\n" \
  "ds_read_b128 v[48:51], v191 offset:384\n" \
  "s_waitcnt lgkmcnt(5)\n" \
  "v_mfma_f32_16x16x32_bf16 v[72:75], a[12:15], v[60:63], v[72:75]\n" \
  "ds_read_b128 v[52:55], v191 offset:18816\n" \
  "v_mfma_f32_16x16x32_bf16 v[68:71], a[84:87], v[56:59], v[68:71]\n" \
  "buffer_store_dwordx4 v[96:99], v190, s[40:43], s62 offen sc1\n" \
  "v_mfma_f32_16x16x32_bf16 v[76:79], a[84:87], v[60:63], v[76:79]\n" \
  "buffer_store_dwordx4 v[96:99], v190, s[40:43], s63 offen sc1\n" \
  "s_waitcnt lgkmcnt(5)\n" \
  "v_mfma_f32_16x16x32_bf16 v[64:67], a[16:19], v[32:35], v[64:67]\n" \
  "ds_read_b128 v[56:59], v192 offset:384\n" \
  "s_waitcnt lgkmcnt(5)\n" \
  "v_cvt_pk_bf16_f32 v100, v88, v89\n" \
  "v_mfma_f32_16x16x32_bf16 v[72:75], a[16:19], v[36:39], v[72:75]\n" \
  "ds_read_b128 v[60:63], v192 offset:18816\n" \
  "v_cvt_pk_bf16_f32 v101, v90, v91\n" \
  "v_mfma_f32_16x16x32_bf16 v[68:71], a[88:91], v[32:35], v[68:71]\n" \
  "s_add_u32 m0, s52, 2048\n" \
  "v_cvt_pk_bf16_f32 v102, v92, v93\n" \
  "v_cvt_pk_bf16_f32 v103, v94, v95\n" \
  "v_mfma_f32_16x16x32_bf16 v[76:79], a[88:91], v[36:39], v[76:79]\n" \
  "buffer_load_dwordx4 v198, s[44:47], s51 offen lds\n" \
  "s_waitcnt lgkmcnt(5)\n" \
  "v_mfma_f32_16x16x32_bf16 v[64:67], a[20:23], v[40:43], v[64:67]\n" \
  "ds_read_b128 v[32:35], v191 offset:512\n" \
  "s_waitcnt lgkmcnt(5)\n" \
  "v_mfma_f32_16x16x32_bf16 v[72:75], a[20:23], v[44:47], v[72:75]\n" \
  "ds_read_b128 v[36:39], v191 offset:18944\n" \
  "v_mfma_f32_16x16x32_bf16 v[68:71], a[92:95], v[40:43], v[68:71]\n" \
  "buffer_store_dwordx4 v[100:103], v190, s[40:43], s62 offen offset:1024 sc1\n" \
  "v_mfma_f32_16x16x32_bf16 v[76:79], a[92:95], v[44:47], v[76:79]\n" \
  "buffer_store_dwordx4 v[100:103], v190, s[40:43], s63 offen offset:1024 sc1\n" \
  "s_waitcnt lgkmcnt(5)\n" \
  "v_mfma_f32_16x16x32_bf16 v[64:67], a[24:27], v[48:51], v[64:67]\n" \
  "ds_read_b128 v[40:43], v192 offset:512\n" \
  "s_add_u32 s49, s49, 524288\n" \
  "s_waitcnt lgkmcnt(5)\n" \
  "v_mfma_f32_16x16x32_bf16 v[72:75], a[24:27], v[52:55], v[72:75]\n" \
  "ds_read_b128 v[44:47], v192 offset:18944\n" \
  "s_bitset0_b32 s49, 30\n" \
  "v_mfma_f32_16x16x32_bf16 v[68:71], a[96:99], v[48:51], v[68:71]\n" \
  "s_add_u32 m0, s52, 3072\n" \
  "v_mfma_f32_16x16x32_bf16 v[76:79], a[96:99], v[52:55], v[76:79]\n" \
  "buffer_load_dwordx4 v199, s[44:47], s51 offen lds\n" \
  "s_waitcnt lgkmcnt(5)\n" \
  "v_mfma_f32_16x16x32_bf16 v[64:67], a[28:31], v[56:59], v[64:67]\n" \
  "ds_read_b128 v[48:51], v191 offset:640\n" \
  "s_waitcnt lgkmcnt(5)\n" \
  "v_mfma_f32_16x16x32_bf16 v[72:75], a[28:31], v[60:63], v[72:75]\n" \
  "ds_read_b128 v[52:55], v191 offset:19072\n" \
  "v_mfma_f32_16x16x32_bf16 v[68:71], a[100:103], v[56:59], v[68:71]\n" \
  "s_cmp_eq_u32 s48, 20\n" \
  "s_cselect_b32 s60, 19922944, 0\n" \
  "s_add_u32 s49, s49, s60\n" \
  "v_mfma_f32_16x16x32_bf16 v[76:79], a[100:103], v[60:63], v[76:79]\n" \
  "s_add_u32 s58, s48, 1\n" \
  "s_and_b32 s58, s58, 3\n" \
  "s_cselect_b32 s58, 0, 147456\n" \
  "s_waitcnt lgkmcnt(5)\n" \
  "v_mfma_f32_16x16x32_bf16 v[64:67], a[32:35], v[32:35], v[64:67]\n" \
  "ds_read_b128 v[56:59], v192 offset:640\n" \
  "s_sub_u32 s58, 36864, s58\n" \
  "s_waitcnt lgkmcnt(5)\n" \
  "v_mfma_f32_16x16x32_bf16 v[72:75], a[32:35], v[36:39], v[72:75]\n" \
  "ds_read_b128 v[60:63], v192 offset:19072\n" \
  "v_add_u32 v165, s58, v191\n" \
  "v_mfma_f32_16x16x32_bf16 v[68:71], a[104:107], v[32:35], v[68:71]\n" \
  "s_add_u32 m0, s52, 4096\n" \
  "v_add_u32 v164, s58, v192\n" \
  "v_mfma_f32_16x16x32_bf16 v[76:79], a[104:107], v[36:39], v[76:79]\n" \
  "buffer_load_dwordx4 v200, s[44:47], s51 offen lds\n" \
  "s_waitcnt lgkmcnt(5)\n" \
  "v_mfma_f32_16x16x32_bf16 v[64:67], a[36:39], v[40:43], v[64:67]\n" \
  "ds_read_b128 v[32:35], v191 offset:768\n" \
  "s_waitcnt lgkmcnt(5)\n" \
  "v_mfma_f32_16x16x32_bf16 v[72:75], a[36:39], v[44:47], v[72:75]\n" \
  "ds_read_b128 v[36:39], v191 offset:19200\n" \
  "v_mfma_f32_16x16x32_bf16 v[68:71], a[108:111], v[40:43], v[68:71]\n" \
  "v_mfma_f32_16x16x32_bf16 v[76:79], a[108:111], v[44:47], v[76:79]\n" \
  "s_waitcnt lgkmcnt(5)\n" \
  "v_mfma_f32_16x16x32_bf16 v[64:67], a[40:43], v[48:51], v[64:67]\n" \
  "ds_read_b128 v[40:43], v192 offset:768\n" \
  "s_waitcnt lgkmcnt(5)\n" \
  "v_mfma_f32_16x16x32_bf16 v[72:75], a[40:43], v[52:55], v[72:75]\n" \
  "ds_read_b128 v[44:47], v192 offset:19200\n" \
  "v_mfma_f32_16x16x32_bf16 v[68:71], a[112:115], v[48:51], v[68:71]\n" \
  "s_add_u32 m0, s52, 5120\n" \
  "v_mfma_f32_16x16x32_bf16 v[76:79], a[112:115], v[52:55], v[76:79]\n" \
  "buffer_load_dwordx4 v201, s[44:47], s51 offen lds\n" \
  "s_waitcnt lgkmcnt(5)\n" \
  "v_mfma_f32_16x16x32_bf16 v[64:67], a[44:47], v[56:59], v[64:67]\n" \
  "ds_read_b128 v[48:51], v191 offset:896\n" \
  "s_waitcnt lgkmcnt(5)\n" \
  "v_mfma_f32_16x16x32_bf16 v[72:75], a[44:47], v[60:63], v[72:75]\n" \
  "ds_read_b128 v[52:55], v191 offset:19328\n" \
  "v_mfma_f32_16x16x32_bf16 v[68:71], a[116:119], v[56:59], v[68:71]\n" \
  "v_mfma_f32_16x16x32_bf16 v[76:79], a[116:119], v[60:63], v[76:79]\n" \
  "s_waitcnt lgkmcnt(5)\n" \
  "v_mfma_f32_16x16x32_bf16 v[64:67], a[48:51], v[32:35], v[64:67]\n" \
  "ds_read_b128 v[56:59], v192 offset:896\n" \
  "s_waitcnt lgkmcnt(5)\n" \
  "v_mfma_f32_16x16x32_bf16 v[72:75], a[48:51], v[36:39], v[72:75]\n" \
  "ds_read_b128 v[60:63], v192 offset:19328\n" \
  "v_mfma_f32_16x16x32_bf16 v[68:71], a[120:123], v[32:35], v[68:71]\n" \
  "s_add_u32 m0, s52, 6144\n" \
  "v_mfma_f32_16x16x32_bf16 v[76:79], a[120:123], v[36:39], v[76:79]\n" \
  "buffer_load_dwordx4 v202, s[44:47], s51 offen lds\n" \
  "s_waitcnt lgkmcnt(5)\n" \
  "v_mfma_f32_16x16x32_bf16 v[64:67], a[52:55], v[40:43], v[64:67]\n" \
  "ds_read_b128 v[32:35], v191 offset:1024\n" \
  "s_waitcnt lgkmcnt(5)\n" \
  "v_mfma_f32_16x16x32_bf16 v[72:75], a[52:55], v[44:47], v[72:75]\n" \
  "ds_read_b128 v[36:39], v191 offset:19456\n" \
  "v_mfma_f32_16x16x32_bf16 v[68:71], a[124:127], v[40:43], v[68:71]\n" \
  "s_waitcnt vmcnt(11)\n" \
  "s_barrier\n" \
  "v_mfma_f32_16x16x32_bf16 v[76:79], a[124:127], v[44:47], v[76:79]\n" \
  "v_mov_b32 v168, s56\n" \
  "s_mov_b64 exec, 1\n" \
  "buffer_store_dwordx2 v[168:169], v167, s[40:43], 0 offen sc1\n" \
  "s_mov_b64 exec, -1\n" \
  "s_waitcnt lgkmcnt(5)\n" \
  "v_mfma_f32_16x16x32_bf16 v[64:67], a[56:59], v[48:51], v[64:67]\n" \
  "ds_read_b128 v[40:43], v192 offset:1024\n" \
  "s_waitcnt lgkmcnt(5)\n" \
  "v_mfma_f32_16x16x32_bf16 v[72:75], a[56:59], v[52:55], v[72:75]\n" \
  "ds_read_b128 v[44:47], v192 offset:19456\n" \
  "v_mfma_f32_16x16x32_bf16 v[68:71], a[128:131], v[48:51], v[68:71]\n" \
  "s_add_u32 m0, s52, 7168\n" \
  "v_mfma_f32_16x16x32_bf16 v[76:79], a[128:131], v[52:55], v[76:79]\n" \
  "buffer_load_dwordx4 v203, s[44:47], s51 offen lds\n" \
  "s_waitcnt lgkmcnt(5)\n" \
  "v_mfma_f32_16x16x32_bf16 v[64:67], a[60:63], v[56:59], v[64:67]\n" \
  "ds_read_b128 v[48:51], v165\n" \
  "s_waitcnt lgkmcnt(5)\n" \
  "v_mfma_f32_16x16x32_bf16 v[72:75], a[60:63], v[60:63], v[72:75]\n" \
  "ds_read_b128 v[52:55], v165 offset:18432\n" \
  "v_mfma_f32_16x16x32_bf16 v[68:71], a[132:135], v[56:59], v[68:71]\n" \
  "s_cmp_eq_u32 s51, 700416\n" \
  "s_cselect_b32 s70, 1437696, s71\n" \
  "v_mfma_f32_16x16x32_bf16 v[76:79], a[132:135], v[60:63], v[76:79]\n" \
  "s_waitcnt lgkmcnt(5)\n" \
  "v_mfma_f32_16x16x32_bf16 v[64:67], a[64:67], v[32:35], v[64:67]\n" \
  "ds_read_b128 v[56:59], v164\n" \
  "s_waitcnt lgkmcnt(5)\n" \
  "v_mfma_f32_16x16x32_bf16 v[72:75], a[64:67], v[36:39], v[72:75]\n" \
  "ds_read_b128 v[60:63], v164 offset:18432\n" \
  "v_mfma_f32_16x16x32_bf16 v[68:71], a[136:139], v[32:35], v[68:71]\n" \
  "s_add_u32 m0, s52, 8192\n" \
  "v_mfma_f32_16x16x32_bf16 v[76:79], a[136:139], v[36:39], v[76:79]\n" \
  "buffer_load_dwordx4 v204, s[44:47], s51 offen lds\n" \
  "s_waitcnt lgkmcnt(5)\n" \
  "v_mfma_f32_16x16x32_bf16 v[64:67], a[68:71], v[40:43], v[64:67]\n" \
  "ds_read_b128 v[32:35], v165 offset:128\n" \
  "s_add_u32 s51, s51, s70\n" \
  "s_waitcnt lgkmcnt(5)\n" \
  "v_mfma_f32_16x16x32_bf16 v[72:75], a[68:71], v[44:47], v[72:75]\n" \
  "ds_read_b128 v[36:39], v165 offset:18560\n" \
  "s_min_u32 s51, s51, 2322432\n" \
  "v_mfma_f32_16x16x32_bf16 v[68:71], a[140:143], v[40:43], v[68:71]\n" \
  "s_add_u32 s52, s52, 36864\n" \
  "v_mfma_f32_16x16x32_bf16 v[76:79], a[140:143], v[44:47], v[76:79]\n" \
  "s_cmp_ge_u32 s52, 163840\n" \
  "s_cselect_b32 s60, 147456, 0\n" \
  "s_sub_u32 s52, s52, s60\n" \
  "v_mov_b32 v191, v165\n" \
  "v_mov_b32 v192, v164\n" \
  "s_cmp_le_u32 s48, 3\n" \
  "s_cbranch_scc1 .LK_mea_%=\n" \
  ".LK_mea_b_%=:\n" \
  "s_min_u32 s56, s48, 20\n" \
  "s_cselect_b32 s57, 0, 38\n" \
  "s_add_u32 s57, s57, s48\n" \
  "s_add_u32 s48, s48, 1\n" \
  "s_waitcnt lgkmcnt(5)\n" \
  "v_mfma_f32_16x16x32_bf16 v[80:83], a[0:3], v[48:51], 0\n" \
  "ds_read_b128 v[40:43], v192 offset:128\n" \
  "s_waitcnt lgkmcnt(5)\n" \
  "v_mfma_f32_16x16x32_bf16 v[88:91], a[0:3], v[52:55], 0\n" \
  "ds_read_b128 v[44:47], v192 offset:18560\n" \
  "s_lshr_b32 s62, s49, 8\n" \
  "v_mfma_f32_16x16x32_bf16 v[84:87], a[72:75], v[48:51], 0\n" \
  "s_add_u32 m0, s52, 0\n" \
  "s_and_b32 s62, s62, 8192\n" \
  "s_xor_b32 s62, s62, s49\n" \
  "v_mfma_f32_16x16x32_bf16 v[92:95], a[72:75], v[52:55], 0\n" \
  "buffer_load_dwordx4 v196, s[44:47], s51 offen lds\n" \
  "s_waitcnt lgkmcnt(5)\n" \
  "v_mfma_f32_16x16x32_bf16 v[80:83], a[4:7], v[56:59], v[80:83]\n" \
  "ds_read_b128 v[48:51], v191 offset:256\n" \
  "s_waitcnt lgkmcnt(5)\n" \
  "v_cvt_pk_bf16_f32 v96, v64, v65\n" \
  "v_mfma_f32_16x16x32_bf16 v[88:91], a[4:7], v[60:63], v[88:91]\n" \
  "ds_read_b128 v[52:55], v191 offset:18688\n" \
  "v_cvt_pk_bf16_f32 v97, v66, v67\n" \
  "v_mfma_f32_16x16x32_bf16 v[84:87], a[76:79], v[56:59], v[84:87]\n" \
  "v_cvt_pk_bf16_f32 v98, v68, v69\n" \
  "v_cvt_pk_bf16_f32 v99, v70, v71\n" \
  "s_lshl_b32 s63, s57, 14\n" \
  "v_mfma_f32_16x16x32_bf16 v[92:95], a[76:79], v[60:63], v[92:95]\n" \
  "s_xor_b32 s63, s63, s59\n" \
  "s_waitcnt lgkmcnt(5)\n" \
  "v_mfma_f32_16x16x32_bf16 v[80:83], a[8:11], v[32:35], v[80:83]\n" \
  "ds_read_b128 v[56:59], v192 offset:256\n" \
  "s_waitcnt lgkmcnt(5)\n" \
  "v_mfma_f32_16x16x32_bf16 v[88:91], a[8:11], v[36:39], v[88:91]\n" \
  "ds_read_b128 v[60:63], v192 offset:18688\n" \
  "v_mfma_f32_16x16x32_bf16 v[84:87], a[80:83], v[32:35], v[84:87]\n" \
  "s_add_u32 m0, s52, 1024\n" \
  "v_mfma_f32_16x16x32_bf16 v[92:95], a[80:83], v[36:39], v[92:95]\n" \
  "buffer_load_dwordx4 v197, s[44:47], s51 offen lds\n" \
  "s_waitcnt lgkmcnt(5)\n" \
  "v_mfma_f32_16x16x32_bf16 v[80:83], a[12:15], v[40:43], v[80:83]\n" \
  "ds_read_b128 v[32:35], v191 offset:384\n" \
  "s_waitcnt lgkmcnt(5)\n" \
  "v_mfma_f32_16x16x32_bf16 v[88:91], a[12:15], v[44:47], v[88:91]\n" \
  "ds_read_b128 v[36:39], v191 offset:18816\n" \
  "v_mfma_f32_16x16x32_bf16 v[84:87], a[84:87], v[40:43], v[84:87]\n" \
  "buffer_store_dwordx4 v[96:99], v190, s[40:43], s62 offen sc1\n" \
  "v_mfma_f32_16x16x32_bf16 v[92:95], a[84:87], v[44:47], v[92:95]\n" \
  "buffer_store_dwordx4 v[96:99], v190, s[40:43], s63 offen sc1\n" \
  "s_waitcnt lgkmcnt(5)\n" \
  "v_mfma_f32_16x16x32_bf16 v[80:83], a[16:19], v[48:51], v[80:83]\n" \
  "ds_read_b128 v[40:43], v192 offset:384\n" \
  "s_waitcnt lgkmcnt(5)\n" \
  "v_cvt_pk_bf16_f32 v100, v72, v73\n" \
  "v_mfma_f32_16x16x32_bf16 v[88:91], a[16:19], v[52:55], v[88:91]\n" \
  "ds_read_b128 v[44:47], v192 offset:18816\n" \
  "v_cvt_pk_bf16_f32 v101, v74, v75\n" \
  "v_mfma_f32_16x16x32_bf16 v[84:87], a[88:91], v[48:51], v[84:87]\n" \
  "s_add_u32 m0, s52, 2048\n" \
  "v_cvt_pk_bf16_f32 v102, v76, v77\n" \
  "v_cvt_pk_bf16_f32 v103, v78, v79\n" \
  "v_mfma_f32_16x16x32_bf16 v[92:95], a[88:91], v[52:55], v[92:95]\n" \
  "buffer_load_dwordx4 v198, s[44:47], s51 offen lds\n" \
  "s_waitcnt lgkmcnt(5)\n" \
  "v_mfma_f32_16x16x32_bf16 v[80:83], a[20:23], v[56:59], v[80:83]\n" \
  "ds_read_b128 v[48:51], v191 offset:512\n" \
  "s_waitcnt lgkmcnt(5)\n" \
  "v_mfma_f32_16x16x32_bf16 v[88:91], a[20:23], v[60:63], v[88:91]\n" \
  "ds_read_b128 v[52:55], v191 offset:18944\n" \
  "v_mfma_f32_16x16x32_bf16 v[84:87], a[92:95], v[56:59], v[84:87]\n" \
  "buffer_store_dwordx4 v[100:103], v190, s[40:43], s62 offen offset:1024 sc1\n" \
  "v_mfma_f32_16x16x32_bf16 v[92:95], a[92:95], v[60:63], v[92:95]\n" \
  "buffer_store_dwordx4 v[100:103], v190, s[40:43], s63 offen offset:1024 sc1\n" \
  "s_waitcnt lgkmcnt(5)\n" \
  "v_mfma_f32_16x16x32_bf16 v[80:83], a[24:27], v[32:35], v[80:83]\n" \
  "ds_read_b128 v[56:59], v192 offset:512\n" \
  "s_add_u32 s49, s49, 524288\n" \
  "s_waitcnt lgkmcnt(5)\n" \
  "v_mfma_f32_16x16x32_bf16 v[88:91], a[24:27], v[36:39], v[88:91]\n" \
  "ds_read_b128 v[60:63], v192 offset:18944\n" \
  "s_bitset0_b32 s49, 30\n" \
  "v_mfma_f32_16x16x32_bf16 v[84:87], a[96:99], v[32:35], v[84:87]\n" \
  "s_add_u32 m0, s52, 3072\n" \
  "v_mfma_f32_16x16x32_bf16 v[92:95], a[96:99], v[36:39], v[92:95]\n" \
  "buffer_load_dwordx4 v199, s[44:47], s51 offen lds\n" \
  "s_waitcnt lgkmcnt(5)\n" \
  "v_mfma_f32_16x16x32_bf16 v[80:83], a[28:31], v[40:43], v[80:83]\n" \
  "ds_read_b128 v[32:35], v191 offset:640\n" \
  "s_waitcnt lgkmcnt(5)\n" \
  "v_mfma_f32_16x16x32_bf16 v[88:91], a[28:31], v[44:47], v[88:91]\n" \
  "ds_read_b128 v[36:39], v191 offset:19072\n" \
  "v_mfma_f32_16x16x32_bf16 v[84:87], a[100:103], v[40:43], v[84:87]\n" \
  "s_cmp_eq_u32 s48, 20\n" \
  "s_cselect_b32 s60, 19922944, 0\n" \
  "s_add_u32 s49, s49, s60\n" \
  "v_mfma_f32_16x16x32_bf16 v[92:95], a[100:103], v[44:47], v[92:95]\n" \
  "s_add_u32 s58, s48, 1\n" \
  "s_and_b32 s58, s58, 3\n" \
  "s_cselect_b32 s58, 0, 147456\n" \
  "s_waitcnt lgkmcnt(5)\n" \
  "v_mfma_f32_16x16x32_bf16 v[80:83], a[32:35], v[48:51], v[80:83]\n" \
  "ds_read_b128 v[40:43], v192 offset:640\n" \
  "s_sub_u32 s58, 36864, s58\n" \
  "s_waitcnt lgkmcnt(5)\n" \
  "v_mfma_f32_16x16x32_bf16 v[88:91], a[32:35], v[52:55], v[88:91]\n" \
  "ds_read_b128 v[44:47], v192 offset:19072\n" \
  "v_add_u32 v165, s58, v191\n" \
  "v_mfma_f32_16x16x32_bf16 v[84:87], a[104:107], v[48:51], v[84:87]\n" \
  "s_add_u32 m0, s52, 4096\n" \
  "v_add_u32 v164, s58, v192\n" \
  "v_mfma_f32_16x16x32_bf16 v[92:95], a[104:107], v[52:55], v[92:95]\n" \
  "buffer_load_dwordx4 v200, s[44:47], s51 offen lds\n" \
  "s_waitcnt lgkmcnt(5)\n" \
  "v_mfma_f32_16x16x32_bf16 v[80:83], a[36:39], v[56:59], v[80:83]\n" \
  "ds_read_b128 v[48:51], v191 offset:768\n" \
  "s_waitcnt lgkmcnt(5)\n" \
  "v_mfma_f32_16x16x32_bf16 v[88:91], a[36:39], v[60:63], v[88:91]\n" \
  "ds_read_b128 v[52:55], v191 offset:19200\n" \
  "v_mfma_f32_16x16x32_bf16 v[84:87], a[108:111], v[56:59], v[84:87]\n" \
  "v_mfma_f32_16x16x32_bf16 v[92:95], a[108:111], v[60:63], v[92:95]\n" \
  "s_waitcnt lgkmcnt(5)\n" \
  "v_mfma_f32_16x16x32_bf16 v[80:83], a[40:43], v[32:35], v[80:83]\n" \
  "ds_read_b128 v[56:59], v192 offset:768\n" \
  "s_waitcnt lgkmcnt(5)\n" \
  "v_mfma_f32_16x16x32_bf16 v[88:91], a[40:43], v[36:39], v[88:91]\n" \
  "ds_read_b128 v[60:63], v192 offset:19200\n" \
  "v_mfma_f32_16x16x32_bf16 v[84:87], a[112:115], v[32:35], v[84:87]\n" \
  "s_add_u32 m0, s52, 5120\n" \
  "v_mfma_f32_16x16x32_bf16 v[92:95], a[112:115], v[36:39], v[92:95]\n" \
  "buffer_load_dwordx4 v201, s[44:47], s51 offen lds\n" \
  "s_waitcnt lgkmcnt(5)\n" \
  "v_mfma_f32_16x16x32_bf16 v[80:83], a[44:47], v[40:43], v[80:83]\n" \
  "ds_read_b128 v[32:35], v191 offset:896\n" \
  "s_waitcnt lgkmcnt(5)\n" \
  "v_mfma_f32_16x16x32_bf16 v[88:91], a[44:47], v[44:47], v[88:91]\n" \
  "ds_read_b128 v[36:39], v191 offset:19328\n" \
  "v_mfma_f32_16x16x32_bf16 v[84:87], a[116:119], v[40:43], v[84:87]\n" \
  "v_mfma_f32_16x16x32_bf16 v[92:95], a[116:119], v[44:47], v[92:95]\n" \
  "s_waitcnt lgkmcnt(5)\n" \
  "v_mfma_f32_16x16x32_bf16 v[80:83], a[48:51], v[48:51], v[80:83]\n" \
  "ds_read_b128 v[40:43], v192 offset:896\n" \
  "s_waitcnt lgkmcnt(5)\n" \
  "v_mfma_f32_16x16x32_bf16 v[88:91], a[48:51], v[52:55], v[88:91]\n" \
  "ds_read_b128 v[44:47], v192 offset:19328\n" \
  "v_mfma_f32_16x16x32_bf16 v[84:87], a[120:123], v[48:51], v[84:87]\n" \
  "s_add_u32 m0, s52, 6144\n" \
  "v_mfma_f32_16x16x32_bf16 v[92:95], a[120:123], v[52:55], v[92:95]\n" \
  "buffer_load_dwordx4 v202, s[44:47], s51 offen lds\n" \
  "s_waitcnt lgkmcnt(5)\n" \
  "v_mfma_f32_16x16x32_bf16 v[80:83], a[52:55], v[56:59], v[80:83]\n" \
  "ds_read_b128 v[48:51], v191 offset:1024\n" \
  "s_waitcnt lgkmcnt(5)\n" \
  "v_mfma_f32_16x16x32_bf16 v[88:91], a[52:55], v[60:63], v[88:91]\n" \
  "ds_read_b128 v[52:55], v191 offset:19456\n" \
  "v_mfma_f32_16x16x32_bf16 v[84:87], a[124:127], v[56:59], v[84:87]\n" \
  "s_waitcnt vmcnt(11)\n" \
  "s_barrier\n" \
  "v_mfma_f32_16x16x32_bf16 v[92:95], a[124:127], v[60:63], v[92:95]\n" \
  "v_mov_b32 v168, s56\n" \
  "s_mov_b64 exec, 1\n" \
  "buffer_store_dwordx2 v[168:169], v167, s[40:43], 0 offen sc1\n" \
  "s_mov_b64 exec, -1\n" \
  "s_waitcnt lgkmcnt(5)\n" \
  "v_mfma_f32_16x16x32_bf16 v[80:83], a[56:59], v[32:35], v[80:83]\n" \
  "ds_read_b128 v[56:59], v192 offset:1024\n" \
  "s_waitcnt lgkmcnt(5)\n" \
  "v_mfma_f32_16x16x32_bf16 v[88:91], a[56:59], v[36:39], v[88:91]\n" \
  "ds_read_b128 v[60:63], v192 offset:19456\n" \
  "v_mfma_f32_16x16x32_bf16 v[84:87], a[128:131], v[32:35], v[84:87]\n" \
  "s_add_u32 m0, s52, 7168\n" \
  "v_mfma_f32_16x16x32_bf16 v[92:95], a[128:131], v[36:39], v[92:95]\n" \
  "buffer_load_dwordx4 v203, s[44:47], s51 offen lds\n" \
  "s_waitcnt lgkmcnt(5)\n" \
  "v_mfma_f32_16x16x32_bf16 v[80:83], a[60:63], v[40:43], v[80:83]\n" \
  "ds_read_b128 v[32:35], v165\n" \
  "s_waitcnt lgkmcnt(5)\n" \
  "v_mfma_f32_16x16x32_bf16 v[88:91], a[60:63], v[44:47], v[88:91]\n" \
  "ds_read_b128 v[36:39], v165 offset:18432\n" \
  "v_mfma_f32_16x16x32_bf16 v[84:87], a[132:135], v[40:43], v[84:87]\n" \
  "s_cmp_eq_u32 s51, 700416\n" \
  "s_cselect_b32 s70, 1437696, s71\n" \
  "v_mfma_f32_16x16x32_bf16 v[92:95], a[132:135], v[44:47], v[92:95]\n" \
  "s_waitcnt lgkmcnt(5)\n" \
  "v_mfma_f32_16x16x32_bf16 v[80:83], a[64:67], v[48:51], v[80:83]\n" \
  "ds_read_b128 v[40:43], v164\n" \
  "s_waitcnt lgkmcnt(5)\n" \
  "v_mfma_f32_16x16x32_bf16 v[88:91], a[64:67], v[52:55], v[88:91]\n" \
  "ds_read_b128 v[44:47], v164 offset:18432\n" \
  "v_mfma_f32_16x16x32_bf16 v[84:87], a[136:139], v[48:51], v[84:87]\n" \
  "s_add_u32 m0, s52, 8192\n" \
  "v_mfma_f32_16x16x32_bf16 v[92:95], a[136:139], v[52:55], v[92:95]\n" \
  "buffer_load_dwordx4 v204, s[44:47], s51 offen lds\n" \
  "s_waitcnt lgkmcnt(5)\n" \
  "v_mfma_f32_16x16x32_bf16 v[80:83], a[68:71], v[56:59], v[80:83]\n" \
  "ds_read_b128 v[48:51], v165 offset:128\n" \
  "s_add_u32 s51, s51, s70\n" \
  "s_waitcnt lgkmcnt(5)\n" \
  "v_mfma_f32_16x16x32_bf16 v[88:91], a[68:71], v[60:63], v[88:91]\n" \
  "ds_read_b128 v[52:55], v165 offset:18560\n" \
  "s_min_u32 s51, s51, 2322432\n" \
  "v_mfma_f32_16x16x32_bf16 v[84:87], a[140:143], v[56:59], v[84:87]\n" \
  "s_add_u32 s52, s52, 36864\n" \
  "v_mfma_f32_16x16x32_bf16 v[92:95], a[140:143], v[60:63], v[92:95]\n" \
  "s_cmp_ge_u32 s52, 163840\n" \
  "s_cselect_b32 s60, 147456, 0\n" \
  "s_sub_u32 s52, s52, s60\n" \
  "v_mov_b32 v191, v165\n" \
  "v_mov_b32 v192, v164\n" \
  "s_cmp_le_u32 s48, 3\n" \
  "s_cbranch_scc1 .LK_meb_%=\n" \
  ".LK_meb_b_%=:\n" \
  "s_min_u32 s56, s48, 20\n" \
  "s_cselect_b32 s57, 0, 38\n" \
  "s_add_u32 s57, s57, s48\n" \
  "s_add_u32 s48, s48, 1\n" \
  "s_cmp_lt_u32 s48, 26\n" \
  "s_waitcnt lgkmcnt(0)\n" \
  "s_cbranch_scc1 .LK_ma_%=\n" \
  "s_nop 7\n" \
  "s_lshr_b32 s62, s49, 8\n" \
  "s_and_b32 s62, s62, 8192\n" \
  "s_xor_b32 s62, s62, s49\n" \
  "v_cvt_pk_bf16_f32 v96, v80, v81\n" \
  "v_cvt_pk_bf16_f32 v97, v82, v83\n" \
  "v_cvt_pk_bf16_f32 v98, v84, v85\n" \
  "v_cvt_pk_bf16_f32 v99, v86, v87\n" \
  "s_lshl_b32 s63, s57, 14\n" \
  "s_xor_b32 s63, s63, s59\n" \
  "buffer_store_dwordx4 v[96:99], v190, s[40:43], s62 offen sc1\n" \
  "buffer_store_dwordx4 v[96:99], v190, s[40:43], s63 offen sc1\n" \
  "v_cvt_pk_bf16_f32 v100, v88, v89\n" \
  "v_cvt_pk_bf16_f32 v101, v90, v91\n" \
  "v_cvt_pk_bf16_f32 v102, v92, v93\n" \
  "v_cvt_pk_bf16_f32 v103, v94, v95\n" \
  "buffer_store_dwordx4 v[100:103], v190, s[40:43], s62 offen offset:1024 sc1\n" \
  "buffer_store_dwordx4 v[100:103], v190, s[40:43], s63 offen offset:1024 sc1\n" \
  "s_sub_u32 s49, s49, 22544384\n" \
  "s_mov_b32 s48, 20\n" \
  "s_waitcnt vmcnt(0)\n" \
  "s_barrier\n" \
  "v_mov_b32 v168, 1\n" \
  "s_mov_b64 exec, 2\n" \
  "buffer_store_dwordx2 v[168:169], v167, s[40:43], 0 offen sc1\n" \
  "s_mov_b64 exec, -1\n" \
  "v_subrev_u32 v191, 73728, v191\n" \
  "v_subrev_u32 v192, 73728, v192\n" \
  "s_waitcnt vmcnt(0)\n" \
  "s_cmp_eq_u32 %43, 77\n" \
  "s_cbranch_scc1 .LK_end_%=\n" \
  "s_mov_b64 s[44:45], %32\n" \
  "s_and_b32 s45, s45, 0xffff\n" \
  "s_mov_b32 s46, 4718592\n" \
  "s_add_u32 s60, %35, 0\n" \
  "s_add_u32 m0, s72, 16384\n" \
  "s_nop 0\n" \
  "buffer_load_dwordx4 v170, s[44:47], s60 offen lds\n" \
  "buffer_load_dwordx4 v171, s[44:47], s60 offen offset:1024 lds\n" \
  "buffer_load_dwordx4 v172, s[44:47], s60 offen offset:2048 lds\n" \
  "buffer_load_dwordx4 v173, s[44:47], s60 offen offset:3072 lds\n" \
  "s_add_u32 s60, %35, 524288\n" \
  "s_add_u32 m0, s72, 32768\n" \
  "s_nop 0\n" \
  "buffer_load_dwordx4 v170, s[44:47], s60 offen lds\n" \
  "buffer_load_dwordx4 v171, s[44:47], s60 offen offset:1024 lds\n" \
  "buffer_load_dwordx4 v172, s[44:47], s60 offen offset:2048 lds\n" \
  "buffer_load_dwordx4 v173, s[44:47], s60 offen offset:3072 lds\n" \
  "s_add_u32 s60, %35, 1048576\n" \
  "s_add_u32 m0, s72, 49152\n" \
  "s_nop 0\n" \
  "buffer_load_dwordx4 v170, s[44:47], s60 offen lds\n" \
  "buffer_load_dwordx4 v171, s[44:47], s60 offen offset:1024 lds\n" \
  "buffer_load_dwordx4 v172, s[44:47], s60 offen offset:2048 lds\n" \
  "buffer_load_dwordx4 v173, s[44:47], s60 offen offset:3072 lds\n" \
  "s_add_u32 s60, %35, 1572864\n" \
  "s_add_u32 m0, s72, 65536\n" \
  "s_nop 0\n" \
  "buffer_load_dwordx4 v170, s[44:47], s60 offen lds\n" \
  "buffer_load_dwordx4 v171, s[44:47], s60 offen offset:1024 lds\n" \
  "buffer_load_dwordx4 v172, s[44:47], s60 offen offset:2048 lds\n" \
  "buffer_load_dwordx4 v173, s[44:47], s60 offen offset:3072 lds\n" \
  "s_add_u32 s60, %35, 2097152\n" \
  "s_add_u32 m0, s72, 81920\n" \
  "s_nop 0\n" \
  "buffer_load_dwordx4 v170, s[44:47], s60 offen lds\n" \
  "buffer_load_dwordx4 v171, s[44:47], s60 offen offset:1024 lds\n" \
  "buffer_load_dwordx4 v172, s[44:47], s60 offen offset:2048 lds\n" \
  "buffer_load_dwordx4 v173, s[44:47], s60 offen offset:3072 lds\n" \
  "s_add_u32 s60, %35, 2621440\n" \
  "s_add_u32 m0, s72, 98304\n" \
  "s_nop 0\n" \
  "buffer_load_dwordx4 v170, s[44:47], s60 offen lds\n" \
  "buffer_load_dwordx4 v171, s[44:47], s60 offen offset:1024 lds\n" \
  "buffer_load_dwordx4 v172, s[44:47], s60 offen offset:2048 lds\n" \
  "buffer_load_dwordx4 v173, s[44:47], s60 offen offset:3072 lds\n" \
  "s_add_u32 s60, %35, 3145728\n" \
  "s_add_u32 m0, s72, 114688\n" \
  "s_nop 0\n" \
  "buffer_load_dwordx4 v170, s[44:47], s60 offen lds\n" \
  "buffer_load_dwordx4 v171, s[44:47], s60 offen offset:1024 lds\n" \
  "buffer_load_dwordx4 v172, s[44:47], s60 offen offset:2048 lds\n" \
  "buffer_load_dwordx4 v173, s[44:47], s60 offen offset:3072 lds\n" \
  "s_add_u32 s60, %35, 3670016\n" \
  "s_add_u32 m0, s72, 131072\n" \
  "s_nop 0\n" \
  "buffer_load_dwordx4 v170, s[44:47], s60 offen lds\n" \
  "buffer_load_dwordx4 v171, s[44:47], s60 offen offset:1024 lds\n" \
  "buffer_load_dwordx4 v172, s[44:47], s60 offen offset:2048 lds\n" \
  "buffer_load_dwordx4 v173, s[44:47], s60 offen offset:3072 lds\n" \
  "s_add_u32 s60, %35, 4194304\n" \
  "s_add_u32 m0, s72, 147456\n" \
  "s_nop 0\n" \
  "buffer_load_dwordx4 v170, s[44:47], s60 offen lds\n" \
  "buffer_load_dwordx4 v171, s[44:47], s60 offen offset:1024 lds\n" \
  "buffer_load_dwordx4 v172, s[44:47], s60 offen offset:2048 lds\n" \
  "buffer_load_dwordx4 v173, s[44:47], s60 offen offset:3072 lds\n" \
  "s_waitcnt vmcnt(24)\n" \
  "s_barrier\n" \
  "v_add_u32 v186, 16384, v182\n" \
  "ds_read_b64_tr_b16 a[144:145], v186\n" \
  "ds_read_b64_tr_b16 a[146:147], v186 offset:1024\n" \
  "ds_read_b64_tr_b16 a[148:149], v186 offset:8192\n" \
  "ds_read_b64_tr_b16 a[150:151], v186 offset:9216\n" \
  "v_add_u32 v187, 16384, v183\n" \
  "ds_read_b64_tr_b16 a[216:217], v187\n" \
  "ds_read_b64_tr_b16 a[218:219], v187 offset:1024\n" \
  "ds_read_b64_tr_b16 a[220:221], v187 offset:8192\n" \
  "ds_read_b64_tr_b16 a[222:223], v187 offset:9216\n" \
  "v_add_u32 v186, 32768, v182\n" \
  "ds_read_b64_tr_b16 a[152:153], v186\n" \
  "ds_read_b64_tr_b16 a[154:155], v186 offset:1024\n" \
  "ds_read_b64_tr_b16 a[156:157], v186 offset:8192\n" \
  "ds_read_b64_tr_b16 a[158:159], v186 offset:9216\n" \
  "v_add_u32 v187, 32768, v183\n" \
  "ds_read_b64_tr_b16 a[224:225], v187\n" \
  "ds_read_b64_tr_b16 a[226:227], v187 offset:1024\n" \
  "ds_read_b64_tr_b16 a[228:229], v187 offset:8192\n" \
  "ds_read_b64_tr_b16 a[230:231], v187 offset:9216\n" \
  "v_add_u32 v186, 49152, v182\n" \
  "ds_read_b64_tr_b16 a[160:161], v186\n" \
  "ds_read_b64_tr_b16 a[162:163], v186 offset:1024\n" \
  "ds_read_b64_tr_b16 a[164:165], v186 offset:8192\n" \
  "ds_read_b64_tr_b16 a[166:167], v186 offset:9216\n" \
  "v_add_u32 v187, 49152, v183\n" \
  "ds_read_b64_tr_b16 a[232:233], v187\n" \
  "ds_read_b64_tr_b16 a[234:235], v187 offset:1024\n" \
  "ds_read_b64_tr_b16 a[236:237], v187 offset:8192\n" \
  "ds_read_b64_tr_b16 a[238:239], v187 offset:9216\n" \
  "s_waitcnt lgkmcnt(0)\n" \
  "s_barrier\n" \
  "s_mov_b64 s[44:45], %33\n" \
  "s_and_b32 s45, s45, 0xffff\n" \
  "s_mov_b32 s46, 2359296\n" \
  "s_mov_b32 s51, 737280\n" \
  "s_mul_i32 s52, %34, 9216\n" \
  "s_add_u32 s52, s52, 16384\n" \
  "s_add_u32 m0, s52, 0\n" \
  "s_nop 0\n" \
  "buffer_load_dwordx4 v196, s[44:47], s51 offen lds\n" \
  "s_add_u32 m0, s52, 1024\n" \
  "s_nop 0\n" \
  "buffer_load_dwordx4 v197, s[44:47], s51 offen lds\n" \
  "s_add_u32 m0, s52, 2048\n" \
  "s_nop 0\n" \
  "buffer_load_dwordx4 v198, s[44:47], s51 offen lds\n" \
  "s_add_u32 m0, s52, 3072\n" \
  "s_nop 0\n" \
  "buffer_load_dwordx4 v199, s[44:47], s51 offen lds\n" \
  "s_add_u32 m0, s52, 4096\n" \
  "s_nop 0\n" \
  "buffer_load_dwordx4 v200, s[44:47], s51 offen lds\n" \
  "s_add_u32 m0, s52, 5120\n" \
  "s_nop 0\n" \
  "buffer_load_dwordx4 v201, s[44:47], s51 offen lds\n" \
  "s_add_u32 m0, s52, 6144\n" \
  "s_nop 0\n" \
  "buffer_load_dwordx4 v202, s[44:47], s51 offen lds\n" \
  "s_add_u32 m0, s52, 7168\n" \
  "s_nop 0\n" \
  "buffer_load_dwordx4 v203, s[44:47], s51 offen lds\n" \
  "s_add_u32 m0, s52, 8192\n" \
  "s_nop 0\n" \
  "buffer_load_dwordx4 v204, s[44:47], s51 offen lds\n" \
  "s_add_u32 s51, s51, 36864\n" \
  "s_min_u32 s51, s51, 2322432\n" \
  "s_add_u32 s52, s52, 36864\n" \
  "s_cmp_ge_u32 s52, 163840\n" \
  "s_cselect_b32 s60, 147456, 0\n" \
  "s_sub_u32 s52, s52, s60\n" \
  "s_waitcnt vmcnt(21)\n" \
  "s_barrier\n" \
  "v_add_u32 v186, 65536, v182\n" \
  "ds_read_b64_tr_b16 a[168:169], v186\n" \
  "ds_read_b64_tr_b16 a[170:171], v186 offset:1024\n" \
  "ds_read_b64_tr_b16 a[172:173], v186 offset:8192\n" \
  "ds_read_b64_tr_b16 a[174:175], v186 offset:9216\n" \
  "v_add_u32 v187, 65536, v183\n" \
  "ds_read_b64_tr_b16 a[240:241], v187\n" \
  "ds_read_b64_tr_b16 a[242:243], v187 offset:1024\n" \
  "ds_read_b64_tr_b16 a[244:245], v187 offset:8192\n" \
  "ds_read_b64_tr_b16 a[246:247], v187 offset:9216\n" \
  "v_add_u32 v186, 81920, v182\n" \
  "ds_read_b64_tr_b16 a[176:177], v186\n" \
  "ds_read_b64_tr_b16 a[178:179], v186 offset:1024\n" \
  "ds_read_b64_tr_b16 a[180:181], v186 offset:8192\n" \
  "ds_read_b64_tr_b16 a[182:183], v186 offset:9216\n" \
  "v_add_u32 v187, 81920, v183\n" \
  "ds_read_b64_tr_b16 a[248:249], v187\n" \
  "ds_read_b64_tr_b16 a[250:251], v187 offset:1024\n" \
  "ds_read_b64_tr_b16 a[252:253], v187 offset:8192\n" \
  "ds_read_b64_tr_b16 a[254:255], v187 offset:9216\n" \
  "v_add_u32 v186, 98304, v182\n" \
  "ds_read_b64_tr_b16 a[184:185], v186\n" \
  "ds_read_b64_tr_b16 a[186:187], v186 offset:1024\n" \
  "ds_read_b64_tr_b16 a[188:189], v186 offset:8192\n" \
  "ds_read_b64_tr_b16 a[190:191], v186 offset:9216\n" \
  "v_add_u32 v187, 98304, v183\n" \
  "ds_read_b64_tr_b16 v[0:1], v187\n" \
  "ds_read_b64_tr_b16 v[2:3], v187 offset:1024\n" \
  "ds_read_b64_tr_b16 v[4:5], v187 offset:8192\n" \
  "ds_read_b64_tr_b16 v[6:7], v187 offset:9216\n" \
  "s_waitcnt lgkmcnt(0)\n" \
  "s_barrier\n" \
  "s_add_u32 m0, s52, 0\n" \
  "s_nop 0\n" \
  "buffer_load_dwordx4 v196, s[44:47], s51 offen lds\n" \
  "s_add_u32 m0, s52, 1024\n" \
  "s_nop 0\n" \
  "buffer_load_dwordx4 v197, s[44:47], s51 offen lds\n" \
  "s_add_u32 m0, s52, 2048\n" \
  "s_nop 0\n" \
  "buffer_load_dwordx4 v198, s[44:47], s51 offen lds\n" \
  "s_add_u32 m0, s52, 3072\n" \
  "s_nop 0\n" \
  "buffer_load_dwordx4 v199, s[44:47], s51 offen lds\n" \
  "s_add_u32 m0, s52, 4096\n" \
  "s_nop 0\n" \
  "buffer_load_dwordx4 v200, s[44:47], s51 offen lds\n" \
  "s_add_u32 m0, s52, 5120\n" \
  "s_nop 0\n" \
  "buffer_load_dwordx4 v201, s[44:47], s51 offen lds\n" \
  "s_add_u32 m0, s52, 6144\n" \
  "s_nop 0\n" \
  "buffer_load_dwordx4 v202, s[44:47], s51 offen lds\n" \
  "s_add_u32 m0, s52, 7168\n" \
  "s_nop 0\n" \
  "buffer_load_dwordx4 v203, s[44:47], s51 offen lds\n" \
  "s_add_u32 m0, s52, 8192\n" \
  "s_nop 0\n" \
  "buffer_load_dwordx4 v204, s[44:47], s51 offen lds\n" \
  "s_add_u32 s51, s51, 36864\n" \
  "s_min_u32 s51, s51, 2322432\n" \
  "s_add_u32 s52, s52, 36864\n" \
  "s_cmp_ge_u32 s52, 163840\n" \
  "s_cselect_b32 s60, 147456, 0\n" \
  "s_sub_u32 s52, s52, s60\n" \
  "s_waitcnt vmcnt(18)\n" \
  "s_barrier\n" \
  "v_add_u32 v186, 114688, v182\n" \
  "ds_read_b64_tr_b16 a[192:193], v186\n" \
  "ds_read_b64_tr_b16 a[194:195], v186 offset:1024\n" \
  "ds_read_b64_tr_b16 a[196:197], v186 offset:8192\n" \
  "ds_read_b64_tr_b16 a[198:199], v186 offset:9216\n" \
  "v_add_u32 v187, 114688, v183\n" \
  "ds_read_b64_tr_b16 v[8:9], v187\n" \
  "ds_read_b64_tr_b16 v[10:11], v187 offset:1024\n" \
  "ds_read_b64_tr_b16 v[12:13], v187 offset:8192\n" \
  "ds_read_b64_tr_b16 v[14:15], v187 offset:9216\n" \
  "v_add_u32 v186, 131072, v182\n" \
  "ds_read_b64_tr_b16 a[200:201], v186\n" \
  "ds_read_b64_tr_b16 a[202:203], v186 offset:1024\n" \
  "ds_read_b64_tr_b16 a[204:205], v186 offset:8192\n" \
  "ds_read_b64_tr_b16 a[206:207], v186 offset:9216\n" \
  "v_add_u32 v187, 131072, v183\n" \
  "ds_read_b64_tr_b16 v[16:17], v187\n" \
  "ds_read_b64_tr_b16 v[18:19], v187 offset:1024\n" \
  "ds_read_b64_tr_b16 v[20:21], v187 offset:8192\n" \
  "ds_read_b64_tr_b16 v[22:23], v187 offset:9216\n" \
  "v_add_u32 v186, 147456, v182\n" \
  "ds_read_b64_tr_b16 a[208:209], v186\n" \
  "ds_read_b64_tr_b16 a[210:211], v186 offset:1024\n" \
  "ds_read_b64_tr_b16 a[212:213], v186 offset:8192\n" \
  "ds_read_b64_tr_b16 a[214:215], v186 offset:9216\n" \
  "v_add_u32 v187, 147456, v183\n" \
  "ds_read_b64_tr_b16 v[24:25], v187\n" \
  "ds_read_b64_tr_b16 v[26:27], v187 offset:1024\n" \
  "ds_read_b64_tr_b16 v[28:29], v187 offset:8192\n" \
  "ds_read_b64_tr_b16 v[30:31], v187 offset:9216\n" \
  "s_waitcnt lgkmcnt(0)\n" \
  "s_waitcnt vmcnt(9)\n" \
  "s_barrier\n" \
  "ds_read_b128 v[32:35], v191\n" \
  "ds_read_b128 v[36:39], v191 offset:18432\n" \
  "ds_read_b128 v[40:43], v192\n" \
  "ds_read_b128 v[44:47], v192 offset:18432\n" \
  "ds_read_b128 v[48:51], v191 offset:128\n" \
  "ds_read_b128 v[52:55], v191 offset:18560\n" \
  "s_waitcnt lgkmcnt(0)\n" \
  ".LK_loop_%=:\n" \
  "v_mfma_f32_16x16x32_bf16 v[64:67], a[0:3], v[32:35], 0\n" \
  "ds_read_b128 v[56:59], v192 offset:128\n" \
  "v_mfma_f32_16x16x32_bf16 v[72:75], a[0:3], v[36:39], 0\n" \
  "ds_read_b128 v[60:63], v192 offset:18560\n" \
  "s_lshr_b32 s62, s50, 8\n" \
  "v_mfma_f32_16x16x32_bf16 v[68:71], a[72:75], v[32:35], 0\n" \
  "s_and_b32 s62, s62, 8192\n" \
  "s_xor_b32 s62, s62, s50\n" \
  "v_cvt_pk_bf16_f32 v96, v80, v81\n" \
  "v_mfma_f32_16x16x32_bf16 v[76:79], a[72:75], v[36:39], 0\n" \
  "v_cvt_pk_bf16_f32 v97, v82, v83\n" \
  "v_cvt_pk_bf16_f32 v98, v84, v85\n" \
  "v_mfma_f32_16x16x32_bf16 v[64:67], a[4:7], v[40:43], v[64:67]\n" \
  "ds_read_b128 v[32:35], v191 offset:256\n" \
  "v_cvt_pk_bf16_f32 v99, v86, v87\n" \
  "v_mfma_f32_16x16x32_bf16 v[72:75], a[4:7], v[44:47], v[72:75]\n" \
  "ds_read_b128 v[36:39], v191 offset:18688\n" \
  "v_mfma_f32_16x16x32_bf16 v[68:71], a[76:79], v[40:43], v[68:71]\n" \
  "buffer_store_dwordx4 v[96:99], v190, s[40:43], s62 offen sc1\n" \
  "v_mfma_f32_16x16x32_bf16 v[76:79], a[76:79], v[44:47], v[76:79]\n" \
  "v_cvt_pk_bf16_f32 v100, v88, v89\n" \
  "v_cvt_pk_bf16_f32 v101, v90, v91\n" \
  "v_mfma_f32_16x16x32_bf16 v[64:67], a[8:11], v[48:51], v[64:67]\n" \
  "ds_read_b128 v[40:43], v192 offset:256\n" \
  "v_cvt_pk_bf16_f32 v102, v92, v93\n" \
  "v_mfma_f32_16x16x32_bf16 v[72:75], a[8:11], v[52:55], v[72:75]\n" \
  "ds_read_b128 v[44:47], v192 offset:18688\n" \
  "v_cvt_pk_bf16_f32 v103, v94, v95\n" \
  "v_mfma_f32_16x16x32_bf16 v[68:71], a[80:83], v[48:51], v[68:71]\n" \
  "buffer_store_dwordx4 v[100:103], v190, s[40:43], s62 offen offset:1024 sc1\n" \
  "v_mfma_f32_16x16x32_bf16 v[76:79], a[80:83], v[52:55], v[76:79]\n" \
  "s_add_u32 s50, s50, 524288\n" \
  "s_bitset0_b32 s50, 30\n" \
  "s_waitcnt lgkmcnt(5)\n" \
  "v_mfma_f32_16x16x32_bf16 v[64:67], a[12:15], v[56:59], v[64:67]\n" \
  "ds_read_b128 v[48:51], v191 offset:384\n" \
  "s_waitcnt lgkmcnt(5)\n" \
  "v_mfma_f32_16x16x32_bf16 v[72:75], a[12:15], v[60:63], v[72:75]\n" \
  "ds_read_b128 v[52:55], v191 offset:18816\n" \
  "v_mfma_f32_16x16x32_bf16 v[68:71], a[84:87], v[56:59], v[68:71]\n" \
  "v_mfma_f32_16x16x32_bf16 v[76:79], a[84:87], v[60:63], v[76:79]\n" \
  "s_waitcnt lgkmcnt(5)\n" \
  "v_mfma_f32_16x16x32_bf16 v[64:67], a[16:19], v[32:35], v[64:67]\n" \
  "ds_read_b128 v[56:59], v192 offset:384\n" \
  "s_waitcnt lgkmcnt(5)\n" \
  "v_mfma_f32_16x16x32_bf16 v[72:75], a[16:19], v[36:39], v[72:75]\n" \
  "ds_read_b128 v[60:63], v192 offset:18816\n" \
  "v_mfma_f32_16x16x32_bf16 v[68:71], a[88:91], v[32:35], v[68:71]\n" \
  "v_mfma_f32_16x16x32_bf16 v[76:79], a[88:91], v[36:39], v[76:79]\n" \
  "s_waitcnt lgkmcnt(5)\n" \
  "v_mfma_f32_16x16x32_bf16 v[64:67], a[20:23], v[40:43], v[64:67]\n" \
  "ds_read_b128 v[32:35], v191 offset:512\n" \
  "s_waitcnt lgkmcnt(5)\n" \
  "v_mfma_f32_16x16x32_bf16 v[72:75], a[20:23], v[44:47], v[72:75]\n" \
  "ds_read_b128 v[36:39], v191 offset:18944\n" \
  "v_mfma_f32_16x16x32_bf16 v[68:71], a[92:95], v[40:43], v[68:71]\n" \
  "v_mfma_f32_16x16x32_bf16 v[76:79], a[92:95], v[44:47], v[76:79]\n" \
  "s_waitcnt lgkmcnt(5)\n" \
  "v_mfma_f32_16x16x32_bf16 v[64:67], a[24:27], v[48:51], v[64:67]\n" \
  "ds_read_b128 v[40:43], v192 offset:512\n" \
  "s_waitcnt lgkmcnt(5)\n" \
  "v_mfma_f32_16x16x32_bf16 v[72:75], a[24:27], v[52:55], v[72:75]\n" \
  "ds_read_b128 v[44:47], v192 offset:18944\n" \
  "v_mfma_f32_16x16x32_bf16 v[68:71], a[96:99], v[48:51], v[68:71]\n" \
  "v_mfma_f32_16x16x32_bf16 v[76:79], a[96:99], v[52:55], v[76:79]\n" \
  "s_waitcnt lgkmcnt(5)\n" \
  "v_mfma_f32_16x16x32_bf16 v[64:67], a[28:31], v[56:59], v[64:67]\n" \
  "ds_read_b128 v[48:51], v191 offset:640\n" \
  "s_waitcnt lgkmcnt(5)\n" \
  "v_mfma_f32_16x16x32_bf16 v[72:75], a[28:31], v[60:63], v[72:75]\n" \
  "ds_read_b128 v[52:55], v191 offset:19072\n" \
  "v_mfma_f32_16x16x32_bf16 v[68:71], a[100:103], v[56:59], v[68:71]\n" \
  "s_add_u32 m0, s52, 0\n" \
  "v_mfma_f32_16x16x32_bf16 v[76:79], a[100:103], v[60:63], v[76:79]\n" \
  "buffer_load_dwordx4 v196, s[44:47], s51 offen lds\n" \
  "s_waitcnt lgkmcnt(5)\n" \
  "v_mfma_f32_16x16x32_bf16 v[64:67], a[32:35], v[32:35], v[64:67]\n" \
  "ds_read_b128 v[56:59], v192 offset:640\n" \
  "s_waitcnt lgkmcnt(5)\n" \
  "v_mfma_f32_16x16x32_bf16 v[72:75], a[32:35], v[36:39], v[72:75]\n" \
  "ds_read_b128 v[60:63], v192 offset:19072\n" \
  "v_mfma_f32_16x16x32_bf16 v[68:71], a[104:107], v[32:35], v[68:71]\n" \
  "v_mfma_f32_16x16x32_bf16 v[76:79], a[104:107], v[36:39], v[76:79]\n" \
  "s_waitcnt lgkmcnt(5)\n" \
  "v_mfma_f32_16x16x32_bf16 v[64:67], a[36:39], v[40:43], v[64:67]\n" \
  "ds_read_b128 v[32:35], v191 offset:768\n" \
  "s_waitcnt lgkmcnt(5)\n" \
  "v_mfma_f32_16x16x32_bf16 v[72:75], a[36:39], v[44:47], v[72:75]\n" \
  "ds_read_b128 v[36:39], v191 offset:19200\n" \
  "v_mfma_f32_16x16x32_bf16 v[68:71], a[108:111], v[40:43], v[68:71]\n" \
  "s_add_u32 m0, s52, 1024\n" \
  "v_mfma_f32_16x16x32_bf16 v[76:79], a[108:111], v[44:47], v[76:79]\n" \
  "buffer_load_dwordx4 v197, s[44:47], s51 offen lds\n" \
  "s_waitcnt lgkmcnt(5)\n" \
  "v_mfma_f32_16x16x32_bf16 v[64:67], a[40:43], v[48:51], v[64:67]\n" \
  "ds_read_b128 v[40:43], v192 offset:768\n" \
  "s_waitcnt lgkmcnt(5)\n" \
  "v_mfma_f32_16x16x32_bf16 v[72:75], a[40:43], v[52:55], v[72:75]\n" \
  "ds_read_b128 v[44:47], v192 offset:19200\n" \
  "v_mfma_f32_16x16x32_bf16 v[68:71], a[112:115], v[48:51], v[68:71]\n" \
  "v_mfma_f32_16x16x32_bf16 v[76:79], a[112:115], v[52:55], v[76:79]\n" \
  "s_waitcnt lgkmcnt(5)\n" \
  "v_mfma_f32_16x16x32_bf16 v[64:67], a[44:47], v[56:59], v[64:67]\n" \
  "ds_read_b128 v[48:51], v191 offset:896\n" \
  "s_waitcnt lgkmcnt(5)\n" \
  "v_mfma_f32_16x16x32_bf16 v[72:75], a[44:47], v[60:63], v[72:75]\n" \
  "ds_read_b128 v[52:55], v191 offset:19328\n" \
  "v_mfma_f32_16x16x32_bf16 v[68:71], a[116:119], v[56:59], v[68:71]\n" \
  "s_add_u32 m0, s52, 2048\n" \
  "v_add_u32 v168, s48, v166\n" \
  "v_mfma_f32_16x16x32_bf16 v[76:79], a[116:119], v[60:63], v[76:79]\n" \
  "buffer_load_dwordx4 v198, s[44:47], s51 offen lds\n" \
  "s_waitcnt lgkmcnt(5)\n" \
  "v_mfma_f32_16x16x32_bf16 v[64:67], a[48:51], v[32:35], v[64:67]\n" \
  "ds_read_b128 v[56:59], v192 offset:896\n" \
  "s_waitcnt lgkmcnt(5)\n" \
  "v_mfma_f32_16x16x32_bf16 v[72:75], a[48:51], v[36:39], v[72:75]\n" \
  "ds_read_b128 v[60:63], v192 offset:19328\n" \
  "v_mfma_f32_16x16x32_bf16 v[68:71], a[120:123], v[32:35], v[68:71]\n" \
  "s_waitcnt vmcnt(5)\n" \
  "s_barrier\n" \
  "v_mfma_f32_16x16x32_bf16 v[76:79], a[120:123], v[36:39], v[76:79]\n" \
  "buffer_store_dwordx2 v[168:169], v167, s[40:43], 0 offen sc1\n" \
  "s_waitcnt lgkmcnt(5)\n" \
  "v_mfma_f32_16x16x32_bf16 v[64:67], a[52:55], v[40:43], v[64:67]\n" \
  "ds_read_b128 v[32:35], v191 offset:1024\n" \
  "s_waitcnt lgkmcnt(5)\n" \
  "v_mfma_f32_16x16x32_bf16 v[72:75], a[52:55], v[44:47], v[72:75]\n" \
  "ds_read_b128 v[36:39], v191 offset:19456\n" \
  "v_mfma_f32_16x16x32_bf16 v[68:71], a[124:127], v[40:43], v[68:71]\n" \
  "s_add_u32 m0, s52, 3072\n" \
  "v_mfma_f32_16x16x32_bf16 v[76:79], a[124:127], v[44:47], v[76:79]\n" \
  "buffer_load_dwordx4 v199, s[44:47], s51 offen lds\n" \
  "s_waitcnt lgkmcnt(5)\n" \
  "v_mfma_f32_16x16x32_bf16 v[64:67], a[56:59], v[48:51], v[64:67]\n" \
  "ds_read_b128 v[40:43], v192 offset:1024\n" \
  "s_waitcnt lgkmcnt(5)\n" \
  "v_mfma_f32_16x16x32_bf16 v[72:75], a[56:59], v[52:55], v[72:75]\n" \
  "ds_read_b128 v[44:47], v192 offset:19456\n" \
  "v_mfma_f32_16x16x32_bf16 v[68:71], a[128:131], v[48:51], v[68:71]\n" \
  "v_mfma_f32_16x16x32_bf16 v[76:79], a[128:131], v[52:55], v[76:79]\n" \
  "s_waitcnt lgkmcnt(5)\n" \
  "v_mfma_f32_16x16x32_bf16 v[64:67], a[60:63], v[56:59], v[64:67]\n" \
  "ds_read_b128 v[48:51], v191\n" \
  "s_waitcnt lgkmcnt(5)\n" \
  "v_mfma_f32_16x16x32_bf16 v[72:75], a[60:63], v[60:63], v[72:75]\n" \
  "ds_read_b128 v[52:55], v191 offset:18432\n" \
  "v_mfma_f32_16x16x32_bf16 v[68:71], a[132:135], v[56:59], v[68:71]\n" \
  "s_add_u32 m0, s52, 4096\n" \
  "v_mfma_f32_16x16x32_bf16 v[76:79], a[132:135], v[60:63], v[76:79]\n" \
  "buffer_load_dwordx4 v200, s[44:47], s51 offen lds\n" \
  "s_waitcnt lgkmcnt(5)\n" \
  "v_mfma_f32_16x16x32_bf16 v[64:67], a[64:67], v[32:35], v[64:67]\n" \
  "ds_read_b128 v[56:59], v192\n" \
  "s_waitcnt lgkmcnt(5)\n" \
  "v_mfma_f32_16x16x32_bf16 v[72:75], a[64:67], v[36:39], v[72:75]\n" \
  "ds_read_b128 v[60:63], v192 offset:18432\n" \
  "v_mfma_f32_16x16x32_bf16 v[68:71], a[136:139], v[32:35], v[68:71]\n" \
  "v_mfma_f32_16x16x32_bf16 v[76:79], a[136:139], v[36:39], v[76:79]\n" \
  "s_waitcnt lgkmcnt(5)\n" \
  "v_mfma_f32_16x16x32_bf16 v[64:67], a[68:71], v[40:43], v[64:67]\n" \
  "ds_read_b128 v[32:35], v191 offset:128\n" \
  "s_waitcnt lgkmcnt(5)\n" \
  "v_mfma_f32_16x16x32_bf16 v[72:75], a[68:71], v[44:47], v[72:75]\n" \
  "ds_read_b128 v[36:39], v191 offset:18560\n" \
  "v_mfma_f32_16x16x32_bf16 v[68:71], a[140:143], v[40:43], v[68:71]\n" \
  "v_mfma_f32_16x16x32_bf16 v[76:79], a[140:143], v[44:47], v[76:79]\n" \
  "s_waitcnt lgkmcnt(5)\n" \
  "v_mfma_f32_16x16x32_bf16 v[80:83], a[144:147], v[48:51], 0\n" \
  "ds_read_b128 v[40:43], v192 offset:128\n" \
  "s_waitcnt lgkmcnt(5)\n" \
  "v_mfma_f32_16x16x32_bf16 v[88:91], a[144:147], v[52:55], 0\n" \
  "ds_read_b128 v[44:47], v192 offset:18560\n" \
  "s_lshr_b32 s62, s49, 8\n" \
  "v_mfma_f32_16x16x32_bf16 v[84:87], a[216:219], v[48:51], 0\n" \
  "s_and_b32 s62, s62, 8192\n" \
  "s_xor_b32 s62, s62, s49\n" \
  "v_cvt_pk_bf16_f32 v96, v64, v65\n" \
  "v_mfma_f32_16x16x32_bf16 v[92:95], a[216:219], v[52:55], 0\n" \
  "s_waitcnt lgkmcnt(5)\n" \
  "v_cvt_pk_bf16_f32 v97, v66, v67\n" \
  "v_cvt_pk_bf16_f32 v98, v68, v69\n" \
  "v_mfma_f32_16x16x32_bf16 v[80:83], a[148:151], v[56:59], v[80:83]\n" \
  "ds_read_b128 v[48:51], v191 offset:256\n" \
  "s_waitcnt lgkmcnt(5)\n" \
  "v_cvt_pk_bf16_f32 v99, v70, v71\n" \
  "v_mfma_f32_16x16x32_bf16 v[88:91], a[148:151], v[60:63], v[88:91]\n" \
  "ds_read_b128 v[52:55], v191 offset:18688\n" \
  "s_lshl_b32 s63, s48, 14\n" \
  "v_mfma_f32_16x16x32_bf16 v[84:87], a[220:223], v[56:59], v[84:87]\n" \
  "s_xor_b32 s63, s63, s59\n" \
  "v_mfma_f32_16x16x32_bf16 v[92:95], a[220:223], v[60:63], v[92:95]\n" \
  "buffer_store_dwordx4 v[96:99], v190, s[40:43], s62 offen sc1\n" \
  "s_waitcnt lgkmcnt(5)\n" \
  "v_mfma_f32_16x16x32_bf16 v[80:83], a[152:155], v[32:35], v[80:83]\n" \
  "ds_read_b128 v[56:59], v192 offset:256\n" \
  "s_waitcnt lgkmcnt(5)\n" \
  "v_mfma_f32_16x16x32_bf16 v[88:91], a[152:155], v[36:39], v[88:91]\n" \
  "ds_read_b128 v[60:63], v192 offset:18688\n" \
  "v_mfma_f32_16x16x32_bf16 v[84:87], a[224:227], v[32:35], v[84:87]\n" \
  "buffer_store_dwordx4 v[96:99], v190, s[40:43], s63 offen sc1\n" \
  "v_mfma_f32_16x16x32_bf16 v[92:95], a[224:227], v[36:39], v[92:95]\n" \
  "s_waitcnt lgkmcnt(5)\n" \
  "v_cvt_pk_bf16_f32 v100, v72, v73\n" \
  "v_cvt_pk_bf16_f32 v101, v74, v75\n" \
  "v_mfma_f32_16x16x32_bf16 v[80:83], a[156:159], v[40:43], v[80:83]\n" \
  "ds_read_b128 v[32:35], v191 offset:384\n" \
  "s_waitcnt lgkmcnt(5)\n" \
  "v_cvt_pk_bf16_f32 v102, v76, v77\n" \
  "v_mfma_f32_16x16x32_bf16 v[88:91], a[156:159], v[44:47], v[88:91]\n" \
  "ds_read_b128 v[36:39], v191 offset:18816\n" \
  "v_cvt_pk_bf16_f32 v103, v78, v79\n" \
  "v_mfma_f32_16x16x32_bf16 v[84:87], a[228:231], v[40:43], v[84:87]\n" \
  "buffer_store_dwordx4 v[100:103], v190, s[40:43], s62 offen offset:1024 sc1\n" \
  "v_mfma_f32_16x16x32_bf16 v[92:95], a[228:231], v[44:47], v[92:95]\n" \
  "buffer_store_dwordx4 v[100:103], v190, s[40:43], s63 offen offset:1024 sc1\n" \
  "s_waitcnt lgkmcnt(5)\n" \
  "v_mfma_f32_16x16x32_bf16 v[80:83], a[160:163], v[48:51], v[80:83]\n" \
  "ds_read_b128 v[40:43], v192 offset:384\n" \
  "s_add_u32 s49, s49, 524288\n" \
  "s_waitcnt lgkmcnt(5)\n" \
  "v_mfma_f32_16x16x32_bf16 v[88:91], a[160:163], v[52:55], v[88:91]\n" \
  "ds_read_b128 v[44:47], v192 offset:18816\n" \
  "s_add_u32 s58, s48, 1\n" \
  "v_mfma_f32_16x16x32_bf16 v[84:87], a[232:235], v[48:51], v[84:87]\n" \
  "s_and_b32 s58, s58, 3\n" \
  "s_cselect_b32 s58, 0, 147456\n" \
  "s_sub_u32 s58, 36864, s58\n" \
  "v_mfma_f32_16x16x32_bf16 v[92:95], a[232:235], v[52:55], v[92:95]\n" \
  "s_waitcnt lgkmcnt(5)\n" \
  "v_add_u32 v165, s58, v191\n" \
  "v_add_u32 v164, s58, v192\n" \
  "v_mfma_f32_16x16x32_bf16 v[80:83], a[164:167], v[56:59], v[80:83]\n" \
  "ds_read_b128 v[48:51], v191 offset:512\n" \
  "s_waitcnt lgkmcnt(5)\n" \
  "v_mfma_f32_16x16x32_bf16 v[88:91], a[164:167], v[60:63], v[88:91]\n" \
  "ds_read_b128 v[52:55], v191 offset:18944\n" \
  "v_mfma_f32_16x16x32_bf16 v[84:87], a[236:239], v[56:59], v[84:87]\n" \
  "v_mfma_f32_16x16x32_bf16 v[92:95], a[236:239], v[60:63], v[92:95]\n" \
  "s_waitcnt lgkmcnt(5)\n" \
  "v_mfma_f32_16x16x32_bf16 v[80:83], a[168:171], v[32:35], v[80:83]\n" \
  "ds_read_b128 v[56:59], v192 offset:512\n" \
  "s_waitcnt lgkmcnt(5)\n" \
  "v_mfma_f32_16x16x32_bf16 v[88:91], a[168:171], v[36:39], v[88:91]\n" \
  "ds_read_b128 v[60:63], v192 offset:18944\n" \
  "v_mfma_f32_16x16x32_bf16 v[84:87], a[240:243], v[32:35], v[84:87]\n" \
  "v_mfma_f32_16x16x32_bf16 v[92:95], a[240:243], v[36:39], v[92:95]\n" \
  "s_waitcnt lgkmcnt(5)\n" \
  "v_mfma_f32_16x16x32_bf16 v[80:83], a[172:175], v[40:43], v[80:83]\n" \
  "ds_read_b128 v[32:35], v191 offset:640\n" \
  "s_waitcnt lgkmcnt(5)\n" \
  "v_mfma_f32_16x16x32_bf16 v[88:91], a[172:175], v[44:47], v[88:91]\n" \
  "ds_read_b128 v[36:39], v191 offset:19072\n" \
  "v_mfma_f32_16x16x32_bf16 v[84:87], a[244:247], v[40:43], v[84:87]\n" \
  "v_mfma_f32_16x16x32_bf16 v[92:95], a[244:247], v[44:47], v[92:95]\n" \
  "s_waitcnt lgkmcnt(5)\n" \
  "v_mfma_f32_16x16x32_bf16 v[80:83], a[176:179], v[48:51], v[80:83]\n" \
  "ds_read_b128 v[40:43], v192 offset:640\n" \
  "s_waitcnt lgkmcnt(5)\n" \
  "v_mfma_f32_16x16x32_bf16 v[88:91], a[176:179], v[52:55], v[88:91]\n" \
  "ds_read_b128 v[44:47], v192 offset:19072\n" \
  "v_mfma_f32_16x16x32_bf16 v[84:87], a[248:251], v[48:51], v[84:87]\n" \
  "s_add_u32 m0, s52, 5120\n" \
  "v_mfma_f32_16x16x32_bf16 v[92:95], a[248:251], v[52:55], v[92:95]\n" \
  "buffer_load_dwordx4 v201, s[44:47], s51 offen lds\n" \
  "s_waitcnt lgkmcnt(5)\n" \
  "v_mfma_f32_16x16x32_bf16 v[80:83], a[180:183], v[56:59], v[80:83]\n" \
  "ds_read_b128 v[48:51], v191 offset:768\n" \
  "s_waitcnt lgkmcnt(5)\n" \
  "v_mfma_f32_16x16x32_bf16 v[88:91], a[180:183], v[60:63], v[88:91]\n" \
  "ds_read_b128 v[52:55], v191 offset:19200\n" \
  "v_mfma_f32_16x16x32_bf16 v[84:87], a[252:255], v[56:59], v[84:87]\n" \
  "v_mfma_f32_16x16x32_bf16 v[92:95], a[252:255], v[60:63], v[92:95]\n" \
  "s_waitcnt lgkmcnt(5)\n" \
  "v_mfma_f32_16x16x32_bf16 v[80:83], a[184:187], v[32:35], v[80:83]\n" \
  "ds_read_b128 v[56:59], v192 offset:768\n" \
  "s_waitcnt lgkmcnt(5)\n" \
  "v_mfma_f32_16x16x32_bf16 v[88:91], a[184:187], v[36:39], v[88:91]\n" \
  "ds_read_b128 v[60:63], v192 offset:19200\n" \
  "v_mfma_f32_16x16x32_bf16 v[84:87], v[0:3], v[32:35], v[84:87]\n" \
  "s_add_u32 m0, s52, 6144\n" \
  "v_mfma_f32_16x16x32_bf16 v[92:95], v[0:3], v[36:39], v[92:95]\n" \
  "buffer_load_dwordx4 v202, s[44:47], s51 offen lds\n" \
  "s_waitcnt lgkmcnt(5)\n" \
  "v_mfma_f32_16x16x32_bf16 v[80:83], a[188:191], v[40:43], v[80:83]\n" \
  "ds_read_b128 v[32:35], v191 offset:896\n" \
  "s_waitcnt lgkmcnt(5)\n" \
  "v_mfma_f32_16x16x32_bf16 v[88:91], a[188:191], v[44:47], v[88:91]\n" \
  "ds_read_b128 v[36:39], v191 offset:19328\n" \
  "v_mfma_f32_16x16x32_bf16 v[84:87], v[4:7], v[40:43], v[84:87]\n" \
  "v_mfma_f32_16x16x32_bf16 v[92:95], v[4:7], v[44:47], v[92:95]\n" \
  "s_waitcnt lgkmcnt(5)\n" \
  "v_mfma_f32_16x16x32_bf16 v[80:83], a[192:195], v[48:51], v[80:83]\n" \
  "ds_read_b128 v[40:43], v192 offset:896\n" \
  "s_waitcnt lgkmcnt(5)\n" \
  "v_mfma_f32_16x16x32_bf16 v[88:91], a[192:195], v[52:55], v[88:91]\n" \
  "ds_read_b128 v[44:47], v192 offset:19328\n" \
  "v_mfma_f32_16x16x32_bf16 v[84:87], v[8:11], v[48:51], v[84:87]\n" \
  "s_add_u32 m0, s52, 7168\n" \
  "v_mfma_f32_16x16x32_bf16 v[92:95], v[8:11], v[52:55], v[92:95]\n" \
  "buffer_load_dwordx4 v203, s[44:47], s51 offen lds\n" \
  "s_waitcnt lgkmcnt(5)\n" \
  "v_mfma_f32_16x16x32_bf16 v[80:83], a[196:199], v[56:59], v[80:83]\n" \
  "ds_read_b128 v[48:51], v191 offset:1024\n" \
  "s_waitcnt lgkmcnt(5)\n" \
  "v_mfma_f32_16x16x32_bf16 v[88:91], a[196:199], v[60:63], v[88:91]\n" \
  "ds_read_b128 v[52:55], v191 offset:19456\n" \
  "v_mfma_f32_16x16x32_bf16 v[84:87], v[12:15], v[56:59], v[84:87]\n" \
  "v_mfma_f32_16x16x32_bf16 v[92:95], v[12:15], v[60:63], v[92:95]\n" \
  "s_waitcnt lgkmcnt(5)\n" \
  "v_mfma_f32_16x16x32_bf16 v[80:83], a[200:203], v[32:35], v[80:83]\n" \
  "ds_read_b128 v[56:59], v192 offset:1024\n" \
  "s_waitcnt lgkmcnt(5)\n" \
  "v_mfma_f32_16x16x32_bf16 v[88:91], a[200:203], v[36:39], v[88:91]\n" \
  "ds_read_b128 v[60:63], v192 offset:19456\n" \
  "v_mfma_f32_16x16x32_bf16 v[84:87], v[16:19], v[32:35], v[84:87]\n" \
  "s_add_u32 m0, s52, 8192\n" \
  "v_mfma_f32_16x16x32_bf16 v[92:95], v[16:19], v[36:39], v[92:95]\n" \
  "buffer_load_dwordx4 v204, s[44:47], s51 offen lds\n" \
  "s_waitcnt lgkmcnt(5)\n" \
  "v_mfma_f32_16x16x32_bf16 v[80:83], a[204:207], v[40:43], v[80:83]\n" \
  "ds_read_b128 v[32:35], v165\n" \
  "s_add_u32 s51, s51, 36864\n" \
  "s_waitcnt lgkmcnt(5)\n" \
  "v_mfma_f32_16x16x32_bf16 v[88:91], a[204:207], v[44:47], v[88:91]\n" \
  "ds_read_b128 v[36:39], v165 offset:18432\n" \
  "s_min_u32 s51, s51, 2322432\n" \
  "v_mfma_f32_16x16x32_bf16 v[84:87], v[20:23], v[40:43], v[84:87]\n" \
  "s_add_u32 s52, s52, 36864\n" \
  "v_mfma_f32_16x16x32_bf16 v[92:95], v[20:23], v[44:47], v[92:95]\n" \
  "s_cmp_ge_u32 s52, 163840\n" \
  "s_cselect_b32 s60, 147456, 0\n" \
  "s_sub_u32 s52, s52, s60\n" \
  "s_waitcnt lgkmcnt(5)\n" \
  "v_mfma_f32_16x16x32_bf16 v[80:83], a[208:211], v[48:51], v[80:83]\n" \
  "ds_read_b128 v[40:43], v164\n" \
  "s_waitcnt lgkmcnt(5)\n" \
  "v_mfma_f32_16x16x32_bf16 v[88:91], a[208:211], v[52:55], v[88:91]\n" \
  "ds_read_b128 v[44:47], v164 offset:18432\n" \
  "v_mfma_f32_16x16x32_bf16 v[84:87], v[24:27], v[48:51], v[84:87]\n" \
  "v_mfma_f32_16x16x32_bf16 v[92:95], v[24:27], v[52:55], v[92:95]\n" \
  "s_waitcnt lgkmcnt(5)\n" \
  "v_mfma_f32_16x16x32_bf16 v[80:83], a[212:215], v[56:59], v[80:83]\n" \
  "ds_read_b128 v[48:51], v165 offset:128\n" \
  "s_waitcnt lgkmcnt(5)\n" \
  "v_mfma_f32_16x16x32_bf16 v[88:91], a[212:215], v[60:63], v[88:91]\n" \
  "ds_read_b128 v[52:55], v165 offset:18560\n" \
  "v_mfma_f32_16x16x32_bf16 v[84:87], v[28:31], v[56:59], v[84:87]\n" \
  "v_mov_b32 v191, v165\n" \
  "v_mov_b32 v192, v164\n" \
  "v_mfma_f32_16x16x32_bf16 v[92:95], v[28:31], v[60:63], v[92:95]\n" \
  "s_add_u32 s48, s48, 1\n" \
  "s_cmp_lt_u32 s48, 58\n" \
  "s_waitcnt lgkmcnt(0)\n" \
  "s_cbranch_scc1 .LK_loop_%=\n" \
  "s_nop 7\n" \
  "s_lshr_b32 s62, s50, 8\n" \
  "s_and_b32 s62, s62, 8192\n" \
  "s_xor_b32 s62, s62, s50\n" \
  "v_cvt_pk_bf16_f32 v96, v80, v81\n" \
  "v_cvt_pk_bf16_f32 v97, v82, v83\n" \
  "v_cvt_pk_bf16_f32 v98, v84, v85\n" \
  "v_cvt_pk_bf16_f32 v99, v86, v87\n" \
  "buffer_store_dwordx4 v[96:99], v190, s[40:43], s62 offen sc1\n" \
  "v_cvt_pk_bf16_f32 v100, v88, v89\n" \
  "v_cvt_pk_bf16_f32 v101, v90, v91\n" \
  "v_cvt_pk_bf16_f32 v102, v92, v93\n" \
  "v_cvt_pk_bf16_f32 v103, v94, v95\n" \
  "buffer_store_dwordx4 v[100:103], v190, s[40:43], s62 offen offset:1024 sc1\n" \
  "s_waitcnt vmcnt(0)\n" \
  "s_barrier\n" \
  "s_mov_b32 s48, 64\n" \
  "s_mov_b32 s69, 0\n" \
  ".LK_ie_%=:\n" \
  "s_cmp_ge_u32 s96, 0xc0000000\n" \
  "s_cbranch_scc1 .LK_ie_x_%=\n" \
  "buffer_load_dwordx2 v[184:185], off, s[40:43], %41 sc1\n" \
  "s_waitcnt vmcnt(0)\n" \
  "v_readfirstlane_b32 s96, v184\n" \
  "s_add_u32 s69, s69, 1\n" \
  "s_cmp_lt_u32 s69, 2097152\n" \
  "s_cbranch_scc1 .LK_ie_%=\n" \
  ".LK_ie_x_%=:\n" \
  "v_mov_b32 v168, s48\n" \
  "buffer_store_dwordx2 v[168:169], v167, s[40:43], 0 offen sc1\n" \
  "s_branch .LK_end_%=\n" \
  ".LK_mea_%=:\n" \
  "s_waitcnt vmcnt(7)\n" \
  "s_barrier\n" \
  "v_mov_b32 v168, s48\n" \
  "s_mov_b64 exec, 1\n" \
  "buffer_store_dwordx2 v[168:169], v167, s[40:43], 0 offen sc1\n" \
  "s_mov_b64 exec, -1\n" \
  "s_branch .LK_mea_b_%=\n" \
  ".LK_meb_%=:\n" \
  "s_waitcnt vmcnt(7)\n" \
  "s_barrier\n" \
  "v_mov_b32 v168, s48\n" \
  "s_mov_b64 exec, 1\n" \
  "buffer_store_dwordx2 v[168:169], v167, s[40:43], 0 offen sc1\n" \
  "s_mov_b64 exec, -1\n" \
  "s_cmp_lg_u32 s48, 3\n" \
  "s_cbranch_scc1 .LK_in_x_%=\n" \
  "s_mov_b32 s69, 0\n" \
  ".LK_in_%=:\n" \
  "s_cmp_ge_u32 s96, 0xc0000000\n" \
  "s_cbranch_scc1 .LK_in_x_%=\n" \
  "buffer_load_dwordx2 v[184:185], off, s[40:43], %41 sc1\n" \
  "s_waitcnt vmcnt(0)\n" \
  "v_readfirstlane_b32 s96, v184\n" \
  "s_add_u32 s69, s69, 1\n" \
  "s_cmp_lt_u32 s69, 2097152\n" \
  "s_cbranch_scc1 .LK_in_%=\n" \
  ".LK_in_x_%=:\n" \
  "s_branch .LK_meb_b_%=\n" \
  ".LK_end_%=:\n" \
  "s_mov_b32 %0, s55\n"
#define K_FIRST 20
#define U_HELP 12
#define U_LAST 64
#define U_LEAD 14
#define VO_SWAP 1
static_assert(SDIR == 58, "generator");
#define U_ASM_TEXT \
  "s_mov_b64 s[40:41], %30\n" \
  "s_and_b32 s41, s41, 0xffff\n" \
  "s_mov_b32 s42, 255852544\n" \
  "s_mov_b32 s43, 0x00020000\n" \
  "s_mov_b32 s47, 0x00020000\n" \
  "s_lshl_b32 s72, %34, 12\n" \
  "buffer_load_dword v184, off, s[40:43], %42 sc1\n" \
  "s_and_b32 s73, %45, 255\n" \
  "s_bfe_u32 s74, %45, 0x80008\n" \
  "s_lshr_b32 s68, %45, 16\n" \
  "s_mov_b32 s75, s73\n" \
  "s_mov_b32 s70, 2\n" \
  "s_mov_b64 s[44:45], %31\n" \
  "s_and_b32 s45, s45, 0xffff\n" \
  "s_mov_b32 s46, 4718592\n" \
  "s_add_u32 s60, %35, 0\n" \
  "s_add_u32 m0, s72, 16384\n" \
  "s_nop 0\n" \
  "buffer_load_dwordx4 v170, s[44:47], s60 offen lds\n" \
  "buffer_load_dwordx4 v171, s[44:47], s60 offen offset:1024 lds\n" \
  "buffer_load_dwordx4 v172, s[44:47], s60 offen offset:2048 lds\n" \
  "buffer_load_dwordx4 v173, s[44:47], s60 offen offset:3072 lds\n" \
  "s_add_u32 s60, %35, 524288\n" \
  "s_add_u32 m0, s72, 32768\n" \
  "s_nop 0\n" \
  "buffer_load_dwordx4 v170, s[44:47], s60 offen lds\n" \
  "buffer_load_dwordx4 v171, s[44:47], s60 offen offset:1024 lds\n" \
  "buffer_load_dwordx4 v172, s[44:47], s60 offen offset:2048 lds\n" \
  "buffer_load_dwordx4 v173, s[44:47], s60 offen offset:3072 lds\n" \
  "s_add_u32 s60, %35, 1048576\n" \
  "s_add_u32 m0, s72, 49152\n" \
  "s_nop 0\n" \
  "buffer_load_dwordx4 v170, s[44:47], s60 offen lds\n" \
  "buffer_load_dwordx4 v171, s[44:47], s60 offen offset:1024 lds\n" \
  "buffer_load_dwordx4 v172, s[44:47], s60 offen offset:2048 lds\n" \
  "buffer_load_dwordx4 v173, s[44:47], s60 offen offset:3072 lds\n" \
  "s_add_u32 s60, %35, 1572864\n" \
  "s_add_u32 m0, s72, 65536\n" \
  "s_nop 0\n" \
  "buffer_load_dwordx4 v170, s[44:47], s60 offen lds\n" \
  "buffer_load_dwordx4 v171, s[44:47], s60 offen offset:1024 lds\n" \
  "buffer_load_dwordx4 v172, s[44:47], s60 offen offset:2048 lds\n" \
  "buffer_load_dwordx4 v173, s[44:47], s60 offen offset:3072 lds\n" \
  "s_add_u32 s60, %35, 2097152\n" \
  "s_add_u32 m0, s72, 81920\n" \
  "s_nop 0\n" \
  "buffer_load_dwordx4 v170, s[44:47], s60 offen lds\n" \
  "buffer_load_dwordx4 v171, s[44:47], s60 offen offset:1024 lds\n" \
  "buffer_load_dwordx4 v172, s[44:47], s60 offen offset:2048 lds\n" \
  "buffer_load_dwordx4 v173, s[44:47], s60 offen offset:3072 lds\n" \
  "s_add_u32 s60, %35, 2621440\n" \
  "s_add_u32 m0, s72, 98304\n" \
  "s_nop 0\n" \
  "buffer_load_dwordx4 v170, s[44:47], s60 offen lds\n" \
  "buffer_load_dwordx4 v171, s[44:47], s60 offen offset:1024 lds\n" \
  "buffer_load_dwordx4 v172, s[44:47], s60 offen offset:2048 lds\n" \
  "buffer_load_dwordx4 v173, s[44:47], s60 offen offset:3072 lds\n" \
  "s_add_u32 s60, %35, 3145728\n" \
  "s_add_u32 m0, s72, 114688\n" \
  "s_nop 0\n" \
  "buffer_load_dwordx4 v170, s[44:47], s60 offen lds\n" \
  "buffer_load_dwordx4 v171, s[44:47], s60 offen offset:1024 lds\n" \
  "buffer_load_dwordx4 v172, s[44:47], s60 offen offset:2048 lds\n" \
  "buffer_load_dwordx4 v173, s[44:47], s60 offen offset:3072 lds\n" \
  "s_add_u32 s60, %35, 3670016\n" \
  "s_add_u32 m0, s72, 131072\n" \
  "s_nop 0\n" \
  "buffer_load_dwordx4 v170, s[44:47], s60 offen lds\n" \
  "buffer_load_dwordx4 v171, s[44:47], s60 offen offset:1024 lds\n" \
  "buffer_load_dwordx4 v172, s[44:47], s60 offen offset:2048 lds\n" \
  "buffer_load_dwordx4 v173, s[44:47], s60 offen offset:3072 lds\n" \
  "s_add_u32 s60, %35, 4194304\n" \
  "s_add_u32 m0, s72, 147456\n" \
  "s_nop 0\n" \
  "buffer_load_dwordx4 v170, s[44:47], s60 offen lds\n" \
  "buffer_load_dwordx4 v171, s[44:47], s60 offen offset:1024 lds\n" \
  "buffer_load_dwordx4 v172, s[44:47], s60 offen offset:2048 lds\n" \
  "buffer_load_dwordx4 v173, s[44:47], s60 offen offset:3072 lds\n" \
  "s_waitcnt vmcnt(24)\n" \
  "s_barrier\n" \
  "v_add_u32 v186, 16384, v182\n" \
  "ds_read_b64_tr_b16 a[0:1], v186\n" \
  "ds_read_b64_tr_b16 a[2:3], v186 offset:1024\n" \
  "ds_read_b64_tr_b16 a[4:5], v186 offset:8192\n" \
  "ds_read_b64_tr_b16 a[6:7], v186 offset:9216\n" \
  "v_add_u32 v187, 16384, v183\n" \
  "ds_read_b64_tr_b16 a[72:73], v187\n" \
  "ds_read_b64_tr_b16 a[74:75], v187 offset:1024\n" \
  "ds_read_b64_tr_b16 a[76:77], v187 offset:8192\n" \
  "ds_read_b64_tr_b16 a[78:79], v187 offset:9216\n" \
  "v_add_u32 v186, 32768, v182\n" \
  "ds_read_b64_tr_b16 a[8:9], v186\n" \
  "ds_read_b64_tr_b16 a[10:11], v186 offset:1024\n" \
  "ds_read_b64_tr_b16 a[12:13], v186 offset:8192\n" \
  "ds_read_b64_tr_b16 a[14:15], v186 offset:9216\n" \
  "v_add_u32 v187, 32768, v183\n" \
  "ds_read_b64_tr_b16 a[80:81], v187\n" \
  "ds_read_b64_tr_b16 a[82:83], v187 offset:1024\n" \
  "ds_read_b64_tr_b16 a[84:85], v187 offset:8192\n" \
  "ds_read_b64_tr_b16 a[86:87], v187 offset:9216\n" \
  "v_add_u32 v186, 49152, v182\n" \
  "ds_read_b64_tr_b16 a[16:17], v186\n" \
  "ds_read_b64_tr_b16 a[18:19], v186 offset:1024\n" \
  "ds_read_b64_tr_b16 a[20:21], v186 offset:8192\n" \
  "ds_read_b64_tr_b16 a[22:23], v186 offset:9216\n" \
  "v_add_u32 v187, 49152, v183\n" \
  "ds_read_b64_tr_b16 a[88:89], v187\n" \
  "ds_read_b64_tr_b16 a[90:91], v187 offset:1024\n" \
  "ds_read_b64_tr_b16 a[92:93], v187 offset:8192\n" \
  "ds_read_b64_tr_b16 a[94:95], v187 offset:9216\n" \
  "s_waitcnt lgkmcnt(0)\n" \
  "s_barrier\n" \
  "s_mov_b64 s[44:45], %32\n" \
  "s_and_b32 s45, s45, 0xffff\n" \
  "s_mov_b32 s46, 4718592\n" \
  "s_add_u32 s60, %36, 0\n" \
  "s_add_u32 m0, s72, 16384\n" \
  "s_nop 0\n" \
  "buffer_load_dwordx4 v174, s[44:47], s60 offen lds\n" \
  "buffer_load_dwordx4 v175, s[44:47], s60 offen offset:1024 lds\n" \
  "buffer_load_dwordx4 v176, s[44:47], s60 offen offset:2048 lds\n" \
  "buffer_load_dwordx4 v177, s[44:47], s60 offen offset:3072 lds\n" \
  "s_add_u32 s60, %36, 73728\n" \
  "s_add_u32 m0, s72, 32768\n" \
  "s_nop 0\n" \
  "buffer_load_dwordx4 v174, s[44:47], s60 offen lds\n" \
  "buffer_load_dwordx4 v175, s[44:47], s60 offen offset:1024 lds\n" \
  "buffer_load_dwordx4 v176, s[44:47], s60 offen offset:2048 lds\n" \
  "buffer_load_dwordx4 v177, s[44:47], s60 offen offset:3072 lds\n" \
  "s_add_u32 s60, %36, 256\n" \
  "s_add_u32 m0, s72, 49152\n" \
  "s_nop 0\n" \
  "buffer_load_dwordx4 v174, s[44:47], s60 offen lds\n" \
  "buffer_load_dwordx4 v175, s[44:47], s60 offen offset:1024 lds\n" \
  "buffer_load_dwordx4 v176, s[44:47], s60 offen offset:2048 lds\n" \
  "buffer_load_dwordx4 v177, s[44:47], s60 offen offset:3072 lds\n" \
  "v_readfirstlane_b32 s55, v184\n" \
  "s_add_u32 s55, s55, 1\n" \
  "s_waitcnt vmcnt(24)\n" \
  "s_barrier\n" \
  "v_add_u32 v186, 65536, v182\n" \
  "ds_read_b64_tr_b16 a[24:25], v186\n" \
  "ds_read_b64_tr_b16 a[26:27], v186 offset:1024\n" \
  "ds_read_b64_tr_b16 a[28:29], v186 offset:8192\n" \
  "ds_read_b64_tr_b16 a[30:31], v186 offset:9216\n" \
  "v_add_u32 v187, 65536, v183\n" \
  "ds_read_b64_tr_b16 a[96:97], v187\n" \
  "ds_read_b64_tr_b16 a[98:99], v187 offset:1024\n" \
  "ds_read_b64_tr_b16 a[100:101], v187 offset:8192\n" \
  "ds_read_b64_tr_b16 a[102:103], v187 offset:9216\n" \
  "v_add_u32 v186, 81920, v182\n" \
  "ds_read_b64_tr_b16 a[32:33], v186\n" \
  "ds_read_b64_tr_b16 a[34:35], v186 offset:1024\n" \
  "ds_read_b64_tr_b16 a[36:37], v186 offset:8192\n" \
  "ds_read_b64_tr_b16 a[38:39], v186 offset:9216\n" \
  "v_add_u32 v187, 81920, v183\n" \
  "ds_read_b64_tr_b16 a[104:105], v187\n" \
  "ds_read_b64_tr_b16 a[106:107], v187 offset:1024\n" \
  "ds_read_b64_tr_b16 a[108:109], v187 offset:8192\n" \
  "ds_read_b64_tr_b16 a[110:111], v187 offset:9216\n" \
  "v_add_u32 v186, 98304, v182\n" \
  "ds_read_b64_tr_b16 a[40:41], v186\n" \
  "ds_read_b64_tr_b16 a[42:43], v186 offset:1024\n" \
  "ds_read_b64_tr_b16 a[44:45], v186 offset:8192\n" \
  "ds_read_b64_tr_b16 a[46:47], v186 offset:9216\n" \
  "v_add_u32 v187, 98304, v183\n" \
  "ds_read_b64_tr_b16 a[112:113], v187\n" \
  "ds_read_b64_tr_b16 a[114:115], v187 offset:1024\n" \
  "ds_read_b64_tr_b16 a[116:117], v187 offset:8192\n" \
  "ds_read_b64_tr_b16 a[118:119], v187 offset:9216\n" \
  "s_waitcnt lgkmcnt(0)\n" \
  "s_barrier\n" \
  "s_add_u32 s60, %36, 73984\n" \
  "s_add_u32 m0, s72, 65536\n" \
  "s_nop 0\n" \
  "buffer_load_dwordx4 v174, s[44:47], s60 offen lds\n" \
  "buffer_load_dwordx4 v175, s[44:47], s60 offen offset:1024 lds\n" \
  "buffer_load_dwordx4 v176, s[44:47], s60 offen offset:2048 lds\n" \
  "buffer_load_dwordx4 v177, s[44:47], s60 offen offset:3072 lds\n" \
  "s_add_u32 s60, %36, 512\n" \
  "s_add_u32 m0, s72, 81920\n" \
  "s_nop 0\n" \
  "buffer_load_dwordx4 v174, s[44:47], s60 offen lds\n" \
  "buffer_load_dwordx4 v175, s[44:47], s60 offen offset:1024 lds\n" \
  "buffer_load_dwordx4 v176, s[44:47], s60 offen offset:2048 lds\n" \
  "buffer_load_dwordx4 v177, s[44:47], s60 offen offset:3072 lds\n" \
  "s_add_u32 s60, %36, 74240\n" \
  "s_add_u32 m0, s72, 98304\n" \
  "s_nop 0\n" \
  "buffer_load_dwordx4 v174, s[44:47], s60 offen lds\n" \
  "buffer_load_dwordx4 v175, s[44:47], s60 offen offset:1024 lds\n" \
  "buffer_load_dwordx4 v176, s[44:47], s60 offen offset:2048 lds\n" \
  "buffer_load_dwordx4 v177, s[44:47], s60 offen offset:3072 lds\n" \
  "s_waitcnt vmcnt(24)\n" \
  "s_barrier\n" \
  "v_add_u32 v186, 114688, v182\n" \
  "ds_read_b64_tr_b16 a[48:49], v186\n" \
  "ds_read_b64_tr_b16 a[50:51], v186 offset:1024\n" \
  "ds_read_b64_tr_b16 a[52:53], v186 offset:8192\n" \
  "ds_read_b64_tr_b16 a[54:55], v186 offset:9216\n" \
  "v_add_u32 v187, 114688, v183\n" \
  "ds_read_b64_tr_b16 a[120:121], v187\n" \
  "ds_read_b64_tr_b16 a[122:123], v187 offset:1024\n" \
  "ds_read_b64_tr_b16 a[124:125], v187 offset:8192\n" \
  "ds_read_b64_tr_b16 a[126:127], v187 offset:9216\n" \
  "v_add_u32 v186, 131072, v182\n" \
  "ds_read_b64_tr_b16 a[56:57], v186\n" \
  "ds_read_b64_tr_b16 a[58:59], v186 offset:1024\n" \
  "ds_read_b64_tr_b16 a[60:61], v186 offset:8192\n" \
  "ds_read_b64_tr_b16 a[62:63], v186 offset:9216\n" \
  "v_add_u32 v187, 131072, v183\n" \
  "ds_read_b64_tr_b16 a[128:129], v187\n" \
  "ds_read_b64_tr_b16 a[130:131], v187 offset:1024\n" \
  "ds_read_b64_tr_b16 a[132:133], v187 offset:8192\n" \
  "ds_read_b64_tr_b16 a[134:135], v187 offset:9216\n" \
  "v_add_u32 v186, 147456, v182\n" \
  "ds_read_b64_tr_b16 a[64:65], v186\n" \
  "ds_read_b64_tr_b16 a[66:67], v186 offset:1024\n" \
  "ds_read_b64_tr_b16 a[68:69], v186 offset:8192\n" \
  "ds_read_b64_tr_b16 a[70:71], v186 offset:9216\n" \
  "v_add_u32 v187, 147456, v183\n" \
  "ds_read_b64_tr_b16 a[136:137], v187\n" \
  "ds_read_b64_tr_b16 a[138:139], v187 offset:1024\n" \
  "ds_read_b64_tr_b16 a[140:141], v187 offset:8192\n" \
  "ds_read_b64_tr_b16 a[142:143], v187 offset:9216\n" \
  "s_waitcnt lgkmcnt(0)\n" \
  "s_barrier\n" \
  "s_add_u32 s60, %36, 768\n" \
  "s_add_u32 m0, s72, 114688\n" \
  "s_nop 0\n" \
  "buffer_load_dwordx4 v174, s[44:47], s60 offen lds\n" \
  "buffer_load_dwordx4 v175, s[44:47], s60 offen offset:1024 lds\n" \
  "buffer_load_dwordx4 v176, s[44:47], s60 offen offset:2048 lds\n" \
  "buffer_load_dwordx4 v177, s[44:47], s60 offen offset:3072 lds\n" \
  "s_add_u32 s60, %36, 74496\n" \
  "s_add_u32 m0, s72, 131072\n" \
  "s_nop 0\n" \
  "buffer_load_dwordx4 v174, s[44:47], s60 offen lds\n" \
  "buffer_load_dwordx4 v175, s[44:47], s60 offen offset:1024 lds\n" \
  "buffer_load_dwordx4 v176, s[44:47], s60 offen offset:2048 lds\n" \
  "buffer_load_dwordx4 v177, s[44:47], s60 offen offset:3072 lds\n" \
  "s_add_u32 s60, %36, 0\n" \
  "s_add_u32 m0, s72, 147456\n" \
  "s_nop 0\n" \
  "buffer_load_dwordx4 v178, s[44:47], s60 offen lds\n" \
  "buffer_load_dwordx4 v179, s[44:47], s60 offen offset:1024 lds\n" \
  "buffer_load_dwordx4 v180, s[44:47], s60 offen offset:2048 lds\n" \
  "buffer_load_dwordx4 v181, s[44:47], s60 offen offset:3072 lds\n" \
  "s_waitcnt vmcnt(24)\n" \
  "s_barrier\n" \
  "v_add_u32 v186, 16384, v182\n" \
  "ds_read_b64_tr_b16 a[144:145], v186\n" \
  "ds_read_b64_tr_b16 a[146:147], v186 offset:1024\n" \
  "ds_read_b64_tr_b16 a[148:149], v186 offset:8192\n" \
  "ds_read_b64_tr_b16 a[150:151], v186 offset:9216\n" \
  "v_add_u32 v187, 16384, v183\n" \
  "ds_read_b64_tr_b16 a[160:161], v187\n" \
  "ds_read_b64_tr_b16 a[162:163], v187 offset:1024\n" \
  "ds_read_b64_tr_b16 a[164:165], v187 offset:8192\n" \
  "ds_read_b64_tr_b16 a[166:167], v187 offset:9216\n" \
  "v_add_u32 v186, 32768, v182\n" \
  "ds_read_b64_tr_b16 a[152:153], v186\n" \
  "ds_read_b64_tr_b16 a[154:155], v186 offset:1024\n" \
  "ds_read_b64_tr_b16 a[156:157], v186 offset:8192\n" \
  "ds_read_b64_tr_b16 a[158:159], v186 offset:9216\n" \
  "v_add_u32 v187, 32768, v183\n" \
  "ds_read_b64_tr_b16 a[168:169], v187\n" \
  "ds_read_b64_tr_b16 a[170:171], v187 offset:1024\n" \
  "ds_read_b64_tr_b16 a[172:173], v187 offset:8192\n" \
  "ds_read_b64_tr_b16 a[174:175], v187 offset:9216\n" \
  "v_add_u32 v186, 49152, v182\n" \
  "ds_read_b64_tr_b16 a[176:177], v186\n" \
  "ds_read_b64_tr_b16 a[178:179], v186 offset:1024\n" \
  "ds_read_b64_tr_b16 a[180:181], v186 offset:8192\n" \
  "ds_read_b64_tr_b16 a[182:183], v186 offset:9216\n" \
  "v_add_u32 v187, 49152, v183\n" \
  "ds_read_b64_tr_b16 a[192:193], v187\n" \
  "ds_read_b64_tr_b16 a[194:195], v187 offset:1024\n" \
  "ds_read_b64_tr_b16 a[196:197], v187 offset:8192\n" \
  "ds_read_b64_tr_b16 a[198:199], v187 offset:9216\n" \
  "s_waitcnt lgkmcnt(0)\n" \
  "s_barrier\n" \
  "s_mov_b64 s[44:45], %33\n" \
  "s_and_b32 s45, s45, 0xffff\n" \
  "s_mov_b32 s46, 2359296\n" \
  "s_mul_i32 s51, s73, 36864\n" \
  "s_mul_i32 s52, %34, 9216\n" \
  "s_add_u32 s52, s52, 16384\n" \
  "s_add_u32 m0, s52, 0\n" \
  "s_nop 0\n" \
  "buffer_load_dwordx4 v196, s[44:47], s51 offen lds\n" \
  "s_add_u32 m0, s52, 1024\n" \
  "s_nop 0\n" \
  "buffer_load_dwordx4 v197, s[44:47], s51 offen lds\n" \
  "s_add_u32 m0, s52, 2048\n" \
  "s_nop 0\n" \
  "buffer_load_dwordx4 v198, s[44:47], s51 offen lds\n" \
  "s_add_u32 m0, s52, 3072\n" \
  "s_nop 0\n" \
  "buffer_load_dwordx4 v199, s[44:47], s51 offen lds\n" \
  "s_add_u32 m0, s52, 4096\n" \
  "s_nop 0\n" \
  "buffer_load_dwordx4 v200, s[44:47], s51 offen lds\n" \
  "s_add_u32 m0, s52, 5120\n" \
  "s_nop 0\n" \
  "buffer_load_dwordx4 v201, s[44:47], s51 offen lds\n" \
  "s_add_u32 m0, s52, 6144\n" \
  "s_nop 0\n" \
  "buffer_load_dwordx4 v202, s[44:47], s51 offen lds\n" \
  "s_add_u32 m0, s52, 7168\n" \
  "s_nop 0\n" \
  "buffer_load_dwordx4 v203, s[44:47], s51 offen lds\n" \
  "s_add_u32 m0, s52, 8192\n" \
  "s_nop 0\n" \
  "buffer_load_dwordx4 v204, s[44:47], s51 offen lds\n" \
  "s_cmp_lt_u32 s51, 884736\n" \
  "s_cselect_b32 s60, 36864, 0\n" \
  "s_add_u32 s51, s51, s60\n" \
  "s_add_u32 s51, s51, 36864\n" \
  "s_min_u32 s51, s51, 2322432\n" \
  "s_add_u32 s52, s52, 36864\n" \
  "s_cmp_ge_u32 s52, 163840\n" \
  "s_cselect_b32 s60, 147456, 0\n" \
  "s_sub_u32 s52, s52, s60\n" \
  "s_waitcnt vmcnt(21)\n" \
  "s_barrier\n" \
  "v_add_u32 v186, 65536, v182\n" \
  "ds_read_b64_tr_b16 a[184:185], v186\n" \
  "ds_read_b64_tr_b16 a[186:187], v186 offset:1024\n" \
  "ds_read_b64_tr_b16 a[188:189], v186 offset:8192\n" \
  "ds_read_b64_tr_b16 a[190:191], v186 offset:9216\n" \
  "v_add_u32 v187, 65536, v183\n" \
  "ds_read_b64_tr_b16 a[200:201], v187\n" \
  "ds_read_b64_tr_b16 a[202:203], v187 offset:1024\n" \
  "ds_read_b64_tr_b16 a[204:205], v187 offset:8192\n" \
  "ds_read_b64_tr_b16 a[206:207], v187 offset:9216\n" \
  "v_add_u32 v186, 81920, v182\n" \
  "ds_read_b64_tr_b16 a[208:209], v186\n" \
  "ds_read_b64_tr_b16 a[210:211], v186 offset:1024\n" \
  "ds_read_b64_tr_b16 a[212:213], v186 offset:8192\n" \
  "ds_read_b64_tr_b16 a[214:215], v186 offset:9216\n" \
  "v_add_u32 v187, 81920, v183\n" \
  "ds_read_b64_tr_b16 a[224:225], v187\n" \
  "ds_read_b64_tr_b16 a[226:227], v187 offset:1024\n" \
  "ds_read_b64_tr_b16 a[228:229], v187 offset:8192\n" \
  "ds_read_b64_tr_b16 a[230:231], v187 offset:9216\n" \
  "v_add_u32 v186, 98304, v182\n" \
  "ds_read_b64_tr_b16 a[216:217], v186\n" \
  "ds_read_b64_tr_b16 a[218:219], v186 offset:1024\n" \
  "ds_read_b64_tr_b16 a[220:221], v186 offset:8192\n" \
  "ds_read_b64_tr_b16 a[222:223], v186 offset:9216\n" \
  "v_add_u32 v187, 98304, v183\n" \
  "ds_read_b64_tr_b16 a[232:233], v187\n" \
  "ds_read_b64_tr_b16 a[234:235], v187 offset:1024\n" \
  "ds_read_b64_tr_b16 a[236:237], v187 offset:8192\n" \
  "ds_read_b64_tr_b16 a[238:239], v187 offset:9216\n" \
  "s_waitcnt lgkmcnt(0)\n" \
  "s_barrier\n" \
  "buffer_load_dwordx2 v[184:185], off, s[40:43], %41 sc1\n" \
  "s_add_u32 m0, s52, 0\n" \
  "s_nop 0\n" \
  "buffer_load_dwordx4 v196, s[44:47], s51 offen lds\n" \
  "s_add_u32 m0, s52, 1024\n" \
  "s_nop 0\n" \
  "buffer_load_dwordx4 v197, s[44:47], s51 offen lds\n" \
  "s_add_u32 m0, s52, 2048\n" \
  "s_nop 0\n" \
  "buffer_load_dwordx4 v198, s[44:47], s51 offen lds\n" \
  "s_add_u32 m0, s52, 3072\n" \
  "s_nop 0\n" \
  "buffer_load_dwordx4 v199, s[44:47], s51 offen lds\n" \
  "s_add_u32 m0, s52, 4096\n" \
  "s_nop 0\n" \
  "buffer_load_dwordx4 v200, s[44:47], s51 offen lds\n" \
  "s_add_u32 m0, s52, 5120\n" \
  "s_nop 0\n" \
  "buffer_load_dwordx4 v201, s[44:47], s51 offen lds\n" \
  "s_add_u32 m0, s52, 6144\n" \
  "s_nop 0\n" \
  "buffer_load_dwordx4 v202, s[44:47], s51 offen lds\n" \
  "s_add_u32 m0, s52, 7168\n" \
  "s_nop 0\n" \
  "buffer_load_dwordx4 v203, s[44:47], s51 offen lds\n" \
  "s_add_u32 m0, s52, 8192\n" \
  "s_nop 0\n" \
  "buffer_load_dwordx4 v204, s[44:47], s51 offen lds\n" \
  "s_cmp_lt_u32 s51, 884736\n" \
  "s_cselect_b32 s60, 36864, 0\n" \
  "s_add_u32 s51, s51, s60\n" \
  "s_add_u32 s51, s51, 36864\n" \
  "s_min_u32 s51, s51, 2322432\n" \
  "s_add_u32 s52, s52, 36864\n" \
  "s_cmp_ge_u32 s52, 163840\n" \
  "s_cselect_b32 s60, 147456, 0\n" \
  "s_sub_u32 s52, s52, s60\n" \
  "s_waitcnt vmcnt(19)\n" \
  "s_barrier\n" \
  "v_add_u32 v186, 114688, v182\n" \
  "ds_read_b64_tr_b16 a[240:241], v186\n" \
  "ds_read_b64_tr_b16 a[242:243], v186 offset:1024\n" \
  "ds_read_b64_tr_b16 a[244:245], v186 offset:8192\n" \
  "ds_read_b64_tr_b16 a[246:247], v186 offset:9216\n" \
  "v_add_u32 v187, 114688, v183\n" \
  "ds_read_b64_tr_b16 v[0:1], v187\n" \
  "ds_read_b64_tr_b16 v[2:3], v187 offset:1024\n" \
  "ds_read_b64_tr_b16 v[4:5], v187 offset:8192\n" \
  "ds_read_b64_tr_b16 v[6:7], v187 offset:9216\n" \
  "v_add_u32 v186, 131072, v182\n" \
  "ds_read_b64_tr_b16 a[248:249], v186\n" \
  "ds_read_b64_tr_b16 a[250:251], v186 offset:1024\n" \
  "ds_read_b64_tr_b16 a[252:253], v186 offset:8192\n" \
  "ds_read_b64_tr_b16 a[254:255], v186 offset:9216\n" \
  "v_add_u32 v187, 131072, v183\n" \
  "ds_read_b64_tr_b16 v[8:9], v187\n" \
  "ds_read_b64_tr_b16 v[10:11], v187 offset:1024\n" \
  "ds_read_b64_tr_b16 v[12:13], v187 offset:8192\n" \
  "ds_read_b64_tr_b16 v[14:15], v187 offset:9216\n" \
  "v_add_u32 v186, 147456, v182\n" \
  "ds_read_b64_tr_b16 v[16:17], v186\n" \
  "ds_read_b64_tr_b16 v[18:19], v186 offset:1024\n" \
  "ds_read_b64_tr_b16 v[20:21], v186 offset:8192\n" \
  "ds_read_b64_tr_b16 v[22:23], v186 offset:9216\n" \
  "v_add_u32 v187, 147456, v183\n" \
  "ds_read_b64_tr_b16 v[24:25], v187\n" \
  "ds_read_b64_tr_b16 v[26:27], v187 offset:1024\n" \
  "ds_read_b64_tr_b16 v[28:29], v187 offset:8192\n" \
  "ds_read_b64_tr_b16 v[30:31], v187 offset:9216\n" \
  "s_waitcnt lgkmcnt(0)\n" \
  "s_mov_b32 s69, 0\n" \
  "s_waitcnt vmcnt(9)\n" \
  "s_branch .LU_go1_%=\n" \
  ".LU_go_%=:\n" \
  "buffer_load_dwordx2 v[184:185], off, s[40:43], %41 sc1\n" \
  "s_waitcnt vmcnt(0)\n" \
  ".LU_go1_%=:\n" \
  "v_readfirstlane_b32 s96, v184\n" \
  "v_readfirstlane_b32 s63, v185\n" \
  "s_cmp_eq_u32 s63, s55\n" \
  "s_cselect_b32 s96, s96, 0\n" \
  "s_cmp_ge_u32 s96, 0x80000000\n" \
  "s_cbranch_scc1 .LU_go_x_%=\n" \
  "s_sleep 4\n" \
  "s_add_u32 s69, s69, 1\n" \
  "s_cmp_lt_u32 s69, 2097152\n" \
  "s_cbranch_scc1 .LU_go_%=\n" \
  "v_mov_b32 v184, 1\n" \
  "v_mov_b32 v185, s55\n" \
  "buffer_store_dwordx2 v[184:185], off, s[40:43], %42 offset:128 sc1\n" \
  ".LU_go_x_%=:\n" \
  "s_and_b32 s60, s96, 2\n" \
  "s_lshl_b32 s60, s60, 12\n" \
  "s_xor_b32 s53, %39, s60\n" \
  "s_xor_b32 s54, %40, s60\n" \
  "s_andn2_b32 s60, 2, s96\n" \
  "s_lshl_b32 s60, s60, 12\n" \
  "s_add_u32 s49, %37, s60\n" \
  "s_add_u32 s50, %38, s60\n" \
  "s_xor_b32 s66, %38, s60\n" \
  "s_xor_b32 s59, %44, s60\n" \
  "s_or_b32 s50, s50, 0x40000000\n" \
  "s_mov_b32 s57, 2130706432\n" \
  "s_mov_b32 s48, 0\n" \
  "v_mov_b32 v169, s55\n" \
  "v_mov_b32 v167, 2130706432\n" \
  "v_mov_b32 v166, 0\n" \
  "s_mov_b64 exec, 7\n" \
  "v_mov_b32 v167, %46\n" \
  "s_mov_b64 exec, 3\n" \
  "v_mov_b32 v167, s54\n" \
  "s_mov_b64 exec, 1\n" \
  "v_mov_b32 v167, s53\n" \
  "v_mov_b32 v166, 0\n" \
  "s_mov_b64 exec, -1\n" \
  "s_barrier\n" \
  "ds_read_b128 v[32:35], v191\n" \
  "ds_read_b128 v[36:39], v191 offset:18432\n" \
  "ds_read_b128 v[40:43], v192\n" \
  "ds_read_b128 v[44:47], v192 offset:18432\n" \
  "ds_read_b128 v[48:51], v191 offset:128\n" \
  "ds_read_b128 v[52:55], v191 offset:18560\n" \
  "s_waitcnt lgkmcnt(0)\n" \
  "v_mov_b64 v[164:165], 0\n" \
  ".LU_loop_%=:\n" \
  "v_mfma_f32_16x16x32_bf16 v[64:67], v[32:35], a[0:3], 0\n" \
  "ds_read_b128 v[56:59], v192 offset:128\n" \
  "v_mfma_f32_16x16x32_bf16 v[72:75], v[36:39], a[0:3], 0\n" \
  "ds_read_b128 v[60:63], v192 offset:18560\n" \
  "v_mfma_f32_16x16x32_bf16 v[68:71], v[32:35], a[72:75], 0\n" \
  "v_cvt_pk_bf16_f32 v104, v88, v89\n" \
  "v_cvt_pk_bf16_f32 v105, v90, v91\n" \
  "v_mfma_f32_16x16x32_bf16 v[76:79], v[36:39], a[72:75], 0\n" \
  "v_cvt_pk_bf16_f32 v106, v92, v93\n" \
  "v_cvt_pk_bf16_f32 v107, v94, v95\n" \
  "s_mov_b32 s63, s57\n" \
  "v_mfma_f32_16x16x32_bf16 v[64:67], v[40:43], a[4:7], v[64:67]\n" \
  "ds_read_b128 v[32:35], v191 offset:256\n" \
  "v_mfma_f32_16x16x32_bf16 v[72:75], v[44:47], a[4:7], v[72:75]\n" \
  "ds_read_b128 v[36:39], v191 offset:18688\n" \
  "v_mfma_f32_16x16x32_bf16 v[68:71], v[40:43], a[76:79], v[68:71]\n" \
  "buffer_store_dwordx4 v[104:107], v190, s[40:43], s63 offen sc1\n" \
  "v_mfma_f32_16x16x32_bf16 v[76:79], v[44:47], a[76:79], v[76:79]\n" \
  "v_mfma_f32_16x16x32_bf16 v[64:67], v[48:51], a[8:11], v[64:67]\n" \
  "ds_read_b128 v[40:43], v192 offset:256\n" \
  "v_mfma_f32_16x16x32_bf16 v[72:75], v[52:55], a[8:11], v[72:75]\n" \
  "ds_read_b128 v[44:47], v192 offset:18688\n" \
  "v_mfma_f32_16x16x32_bf16 v[68:71], v[48:51], a[80:83], v[68:71]\n" \
  "s_add_u32 m0, s52, 0\n" \
  "v_mfma_f32_16x16x32_bf16 v[76:79], v[52:55], a[80:83], v[76:79]\n" \
  "buffer_load_dwordx4 v196, s[44:47], s51 offen lds\n" \
  "s_waitcnt lgkmcnt(5)\n" \
  "v_mfma_f32_16x16x32_bf16 v[64:67], v[56:59], a[12:15], v[64:67]\n" \
  "ds_read_b128 v[48:51], v191 offset:384\n" \
  "s_mov_b32 s58, s75\n" \
  "s_waitcnt lgkmcnt(5)\n" \
  "v_mfma_f32_16x16x32_bf16 v[72:75], v[60:63], a[12:15], v[72:75]\n" \
  "ds_read_b128 v[52:55], v191 offset:18816\n" \
  "s_and_b32 s58, s58, 15\n" \
  "v_mfma_f32_16x16x32_bf16 v[68:71], v[56:59], a[84:87], v[68:71]\n" \
  "s_lshl_b32 s58, s58, 16\n" \
  "s_xor_b32 s50, s58, s66\n" \
  "s_xor_b32 s57, s58, s59\n" \
  "v_mfma_f32_16x16x32_bf16 v[76:79], v[60:63], a[84:87], v[76:79]\n" \
  "v_mul_i32_i24 v161, s70, v166\n" \
  "s_cmp_lt_u32 s73, 24\n" \
  "s_cselect_b32 s70, 2, 1\n" \
  "s_waitcnt lgkmcnt(5)\n" \
  "v_mfma_f32_16x16x32_bf16 v[64:67], v[32:35], a[16:19], v[64:67]\n" \
  "ds_read_b128 v[56:59], v192 offset:384\n" \
  "s_lshl_b32 s71, s70, 19\n" \
  "s_waitcnt lgkmcnt(5)\n" \
  "v_mfma_f32_16x16x32_bf16 v[72:75], v[36:39], a[16:19], v[72:75]\n" \
  "ds_read_b128 v[60:63], v192 offset:18816\n" \
  "s_mov_b32 s75, s73\n" \
  "v_mfma_f32_16x16x32_bf16 v[68:71], v[32:35], a[88:91], v[68:71]\n" \
  "s_add_u32 m0, s52, 1024\n" \
  "v_mfma_f32_16x16x32_bf16 v[76:79], v[36:39], a[88:91], v[76:79]\n" \
  "buffer_load_dwordx4 v197, s[44:47], s51 offen lds\n" \
  "s_waitcnt lgkmcnt(5)\n" \
  "v_mfma_f32_16x16x32_bf16 v[64:67], v[40:43], a[20:23], v[64:67]\n" \
  "ds_read_b128 v[32:35], v191 offset:512\n" \
  "s_waitcnt lgkmcnt(5)\n" \
  "v_mfma_f32_16x16x32_bf16 v[72:75], v[44:47], a[20:23], v[72:75]\n" \
  "ds_read_b128 v[36:39], v191 offset:18944\n" \
  "v_mfma_f32_16x16x32_bf16 v[68:71], v[40:43], a[92:95], v[68:71]\n" \
  "v_mfma_f32_16x16x32_bf16 v[76:79], v[44:47], a[92:95], v[76:79]\n" \
  "s_waitcnt lgkmcnt(5)\n" \
  "v_mfma_f32_16x16x32_bf16 v[64:67], v[48:51], a[24:27], v[64:67]\n" \
  "ds_read_b128 v[40:43], v192 offset:512\n" \
  "s_waitcnt lgkmcnt(5)\n" \
  "v_xor_b32 v160, 8, v194\n" \
  "v_mfma_f32_16x16x32_bf16 v[72:75], v[52:55], a[24:27], v[72:75]\n" \
  "ds_read_b128 v[44:47], v192 offset:18944\n" \
  "v_mfma_f32_16x16x32_bf16 v[68:71], v[48:51], a[96:99], v[68:71]\n" \
  "s_add_u32 m0, s52, 2048\n" \
  "v_add_u32 v168, s73, v161\n" \
  "v_mfma_f32_16x16x32_bf16 v[76:79], v[52:55], a[96:99], v[76:79]\n" \
  "buffer_load_dwordx4 v198, s[44:47], s51 offen lds\n" \
  "s_waitcnt lgkmcnt(5)\n" \
  "v_mfma_f32_16x16x32_bf16 v[64:67], v[56:59], a[28:31], v[64:67]\n" \
  "ds_read_b128 v[48:51], v191 offset:640\n" \
  "s_waitcnt lgkmcnt(5)\n" \
  "v_mfma_f32_16x16x32_bf16 v[72:75], v[60:63], a[28:31], v[72:75]\n" \
  "ds_read_b128 v[52:55], v191 offset:19072\n" \
  "v_mfma_f32_16x16x32_bf16 v[68:71], v[56:59], a[100:103], v[68:71]\n" \
  "s_waitcnt vmcnt(11)\n" \
  "s_barrier\n" \
  "v_mfma_f32_16x16x32_bf16 v[76:79], v[60:63], a[100:103], v[76:79]\n" \
  "ds_read_b64_tr_b16 v[104:105], v194\n" \
  "s_waitcnt lgkmcnt(6)\n" \
  "v_mfma_f32_16x16x32_bf16 v[64:67], v[32:35], a[32:35], v[64:67]\n" \
  "ds_read_b128 v[56:59], v192 offset:640\n" \
  "s_waitcnt lgkmcnt(6)\n" \
  "v_mfma_f32_16x16x32_bf16 v[72:75], v[36:39], a[32:35], v[72:75]\n" \
  "ds_read_b128 v[60:63], v192 offset:19072\n" \
  "v_mfma_f32_16x16x32_bf16 v[68:71], v[32:35], a[104:107], v[68:71]\n" \
  "s_add_u32 m0, s52, 3072\n" \
  "ds_read_b64_tr_b16 v[106:107], v194 offset:256\n" \
  "v_mfma_f32_16x16x32_bf16 v[76:79], v[36:39], a[104:107], v[76:79]\n" \
  "buffer_load_dwordx4 v199, s[44:47], s51 offen lds\n" \
  "s_waitcnt lgkmcnt(7)\n" \
  "v_mfma_f32_16x16x32_bf16 v[64:67], v[40:43], a[36:39], v[64:67]\n" \
  "ds_read_b128 v[32:35], v191 offset:768\n" \
  "s_waitcnt lgkmcnt(7)\n" \
  "v_mfma_f32_16x16x32_bf16 v[72:75], v[44:47], a[36:39], v[72:75]\n" \
  "ds_read_b128 v[36:39], v191 offset:19200\n" \
  "v_mfma_f32_16x16x32_bf16 v[68:71], v[40:43], a[108:111], v[68:71]\n" \
  "s_cmp_le_u32 s48, 7\n" \
  "s_cbranch_scc1 .LU_e2_%=\n" \
  ".LU_e2_b_%=:\n" \
  "v_mfma_f32_16x16x32_bf16 v[76:79], v[44:47], a[108:111], v[76:79]\n" \
  "ds_read_b64_tr_b16 v[108:109], v160\n" \
  "s_waitcnt lgkmcnt(8)\n" \
  "v_mfma_f32_16x16x32_bf16 v[64:67], v[48:51], a[40:43], v[64:67]\n" \
  "ds_read_b128 v[40:43], v192 offset:768\n" \
  "s_waitcnt lgkmcnt(8)\n" \
  "v_mfma_f32_16x16x32_bf16 v[72:75], v[52:55], a[40:43], v[72:75]\n" \
  "ds_read_b128 v[44:47], v192 offset:19200\n" \
  "v_mfma_f32_16x16x32_bf16 v[68:71], v[48:51], a[112:115], v[68:71]\n" \
  "s_add_u32 m0, s52, 4096\n" \
  "ds_read_b64_tr_b16 v[110:111], v160 offset:256\n" \
  "v_mfma_f32_16x16x32_bf16 v[76:79], v[52:55], a[112:115], v[76:79]\n" \
  "buffer_load_dwordx4 v200, s[44:47], s51 offen lds\n" \
  "s_waitcnt lgkmcnt(8)\n" \
  "v_mfma_f32_16x16x32_bf16 v[64:67], v[56:59], a[44:47], v[64:67]\n" \
  "ds_read_b128 v[48:51], v191 offset:896\n" \
  "s_waitcnt lgkmcnt(8)\n" \
  "v_mfma_f32_16x16x32_bf16 v[72:75], v[60:63], a[44:47], v[72:75]\n" \
  "ds_read_b128 v[52:55], v191 offset:19328\n" \
  "v_mfma_f32_16x16x32_bf16 v[68:71], v[56:59], a[116:119], v[68:71]\n" \
  "ds_read_b64_tr_b16 v[112:113], v194 offset:2048\n" \
  "v_mfma_f32_16x16x32_bf16 v[76:79], v[60:63], a[116:119], v[76:79]\n" \
  "ds_read_b64_tr_b16 v[114:115], v194 offset:2304\n" \
  "s_waitcnt lgkmcnt(9)\n" \
  "v_mfma_f32_16x16x32_bf16 v[64:67], v[32:35], a[48:51], v[64:67]\n" \
  "ds_read_b128 v[56:59], v192 offset:896\n" \
  "s_waitcnt lgkmcnt(9)\n" \
  "v_mfma_f32_16x16x32_bf16 v[72:75], v[36:39], a[48:51], v[72:75]\n" \
  "ds_read_b128 v[60:63], v192 offset:19328\n" \
  "v_mfma_f32_16x16x32_bf16 v[68:71], v[32:35], a[120:123], v[68:71]\n" \
  "s_add_u32 m0, s52, 5120\n" \
  "ds_read_b64_tr_b16 v[116:117], v160 offset:2048\n" \
  "v_mfma_f32_16x16x32_bf16 v[76:79], v[36:39], a[120:123], v[76:79]\n" \
  "buffer_load_dwordx4 v201, s[44:47], s51 offen lds\n" \
  "s_waitcnt lgkmcnt(9)\n" \
  "v_mfma_f32_16x16x32_bf16 v[64:67], v[40:43], a[52:55], v[64:67]\n" \
  "ds_read_b128 v[32:35], v191 offset:1024\n" \
  "s_waitcnt lgkmcnt(9)\n" \
  "v_mfma_f32_16x16x32_bf16 v[72:75], v[44:47], a[52:55], v[72:75]\n" \
  "ds_read_b128 v[36:39], v191 offset:19456\n" \
  "v_mfma_f32_16x16x32_bf16 v[68:71], v[40:43], a[124:127], v[68:71]\n" \
  "ds_read_b64_tr_b16 v[118:119], v160 offset:2304\n" \
  "v_mfma_f32_16x16x32_bf16 v[76:79], v[44:47], a[124:127], v[76:79]\n" \
  "ds_read_b64_tr_b16 v[120:121], v194 offset:4096\n" \
  "s_waitcnt lgkmcnt(10)\n" \
  "v_mfma_f32_16x16x32_bf16 v[64:67], v[48:51], a[56:59], v[64:67]\n" \
  "ds_read_b128 v[40:43], v192 offset:1024\n" \
  "s_waitcnt lgkmcnt(10)\n" \
  "v_mfma_f32_16x16x32_bf16 v[72:75], v[52:55], a[56:59], v[72:75]\n" \
  "ds_read_b128 v[44:47], v192 offset:19456\n" \
  "v_mfma_f32_16x16x32_bf16 v[68:71], v[48:51], a[128:131], v[68:71]\n" \
  "s_add_u32 m0, s52, 6144\n" \
  "ds_read_b64_tr_b16 v[122:123], v194 offset:4352\n" \
  "v_mfma_f32_16x16x32_bf16 v[76:79], v[52:55], a[128:131], v[76:79]\n" \
  "buffer_load_dwordx4 v202, s[44:47], s51 offen lds\n" \
  "s_waitcnt lgkmcnt(9)\n" \
  "v_mfma_f32_16x16x32_bf16 v[64:67], v[56:59], a[60:63], v[64:67]\n" \
  "ds_read_b64_tr_b16 v[124:125], v160 offset:4096\n" \
  "s_waitcnt lgkmcnt(9)\n" \
  "v_mfma_f32_16x16x32_bf16 v[72:75], v[60:63], a[60:63], v[72:75]\n" \
  "ds_read_b64_tr_b16 v[126:127], v160 offset:4352\n" \
  "v_mfma_f32_16x16x32_bf16 v[68:71], v[56:59], a[132:135], v[68:71]\n" \
  "s_add_u32 m0, s52, 7168\n" \
  "ds_read_b64_tr_b16 v[128:129], v194 offset:6144\n" \
  "v_mfma_f32_16x16x32_bf16 v[76:79], v[60:63], a[132:135], v[76:79]\n" \
  "buffer_load_dwordx4 v203, s[44:47], s51 offen lds\n" \
  "s_waitcnt lgkmcnt(9)\n" \
  "v_mfma_f32_16x16x32_bf16 v[64:67], v[32:35], a[64:67], v[64:67]\n" \
  "ds_read_b64_tr_b16 v[130:131], v194 offset:6400\n" \
  "s_waitcnt lgkmcnt(9)\n" \
  "v_mfma_f32_16x16x32_bf16 v[72:75], v[36:39], a[64:67], v[72:75]\n" \
  "ds_read_b64_tr_b16 v[132:133], v160 offset:6144\n" \
  "v_mfma_f32_16x16x32_bf16 v[68:71], v[32:35], a[136:139], v[68:71]\n" \
  "ds_read_b64_tr_b16 v[134:135], v160 offset:6400\n" \
  "v_mfma_f32_16x16x32_bf16 v[76:79], v[36:39], a[136:139], v[76:79]\n" \
  "s_waitcnt lgkmcnt(8)\n" \
  "v_mfma_f32_16x16x32_bf16 v[64:67], v[40:43], a[68:71], v[64:67]\n" \
  "s_waitcnt lgkmcnt(7)\n" \
  "v_mfma_f32_16x16x32_bf16 v[72:75], v[44:47], a[68:71], v[72:75]\n" \
  "s_waitcnt vmcnt(8)\n" \
  "buffer_store_dwordx2 v[168:169], v167, s[40:43], 0 offen sc1\n" \
  "v_mfma_f32_16x16x32_bf16 v[68:71], v[40:43], a[140:143], v[68:71]\n" \
  "s_add_u32 m0, s52, 8192\n" \
  "v_mfma_f32_16x16x32_bf16 v[76:79], v[44:47], a[140:143], v[76:79]\n" \
  "buffer_load_dwordx4 v204, s[44:47], s51 offen lds\n" \
  "s_cmp_le_u32 s48, 5\n" \
  "s_cbranch_scc1 .LU_first_%=\n" \
  ".LU_phb_%=:\n" \
  "v_mfma_f32_16x16x32_bf16 v[88:91], v[104:107], a[144:147], 0\n" \
  "v_mfma_f32_16x16x32_bf16 v[92:95], v[108:111], a[144:147], 0\n" \
  "s_lshr_b32 s62, s49, 8\n" \
  "s_and_b32 s62, s62, 8192\n" \
  "s_xor_b32 s62, s62, s49\n" \
  "v_mfma_f32_16x16x32_bf16 v[88:91], v[112:115], a[148:151], v[88:91]\n" \
  "v_cvt_pk_bf16_f32 v80, v64, v65\n" \
  "v_cvt_pk_bf16_f32 v81, v66, v67\n" \
  "v_mfma_f32_16x16x32_bf16 v[92:95], v[116:119], a[148:151], v[92:95]\n" \
  "s_waitcnt lgkmcnt(6)\n" \
  "v_cvt_pk_bf16_f32 v82, v72, v73\n" \
  "v_cvt_pk_bf16_f32 v83, v74, v75\n" \
  "v_mfma_f32_16x16x32_bf16 v[88:91], v[120:123], a[152:155], v[88:91]\n" \
  "buffer_store_dwordx4 v[80:83], v190, s[40:43], s62 offen sc1\n" \
  "s_waitcnt lgkmcnt(4)\n" \
  "v_mfma_f32_16x16x32_bf16 v[92:95], v[124:127], a[152:155], v[92:95]\n" \
  "ds_write_b128 v193, v[80:83]\n" \
  "s_waitcnt lgkmcnt(3)\n" \
  "v_mfma_f32_16x16x32_bf16 v[88:91], v[128:131], a[156:159], v[88:91]\n" \
  "s_waitcnt lgkmcnt(1)\n" \
  "v_cvt_pk_bf16_f32 v84, v76, v77\n" \
  "v_cvt_pk_bf16_f32 v85, v78, v79\n" \
  "v_mfma_f32_16x16x32_bf16 v[92:95], v[132:135], a[156:159], v[92:95]\n" \
  "v_cvt_pk_bf16_f32 v86, v68, v69\n" \
  "v_cvt_pk_bf16_f32 v87, v70, v71\n" \
  "v_mfma_f32_16x16x32_bf16 v[96:99], v[104:107], a[160:163], 0\n" \
  "buffer_store_dwordx4 v[84:87], v190, s[40:43], s62 offen offset:1024 sc1\n" \
  "v_mfma_f32_16x16x32_bf16 v[100:103], v[108:111], a[160:163], 0\n" \
  "ds_write_b128 v193, v[84:87] offset:1024\n" \
  "v_mfma_f32_16x16x32_bf16 v[96:99], v[112:115], a[164:167], v[96:99]\n" \
  "v_cvt_pk_bf16_f32 v136, v88, v89\n" \
  "v_cvt_pk_bf16_f32 v137, v90, v91\n" \
  "s_add_u32 s58, s48, 1\n" \
  "v_mfma_f32_16x16x32_bf16 v[100:103], v[116:119], a[164:167], v[100:103]\n" \
  "v_cvt_pk_bf16_f32 v138, v92, v93\n" \
  "v_cvt_pk_bf16_f32 v139, v94, v95\n" \
  "s_xor_b32 s63, s50, 0\n" \
  "v_mfma_f32_16x16x32_bf16 v[96:99], v[120:123], a[168:171], v[96:99]\n" \
  "s_add_u32 s63, s63, 0\n" \
  "s_and_b32 s58, s58, 3\n" \
  "s_cselect_b32 s58, 0, 147456\n" \
  "v_mfma_f32_16x16x32_bf16 v[100:103], v[124:127], a[168:171], v[100:103]\n" \
  "buffer_store_dwordx4 v[136:139], v190, s[40:43], s63 offen sc1\n" \
  "v_mfma_f32_16x16x32_bf16 v[96:99], v[128:131], a[172:175], v[96:99]\n" \
  "s_sub_u32 s58, 36864, s58\n" \
  "v_add_u32 v163, s58, v191\n" \
  "v_add_u32 v162, s58, v192\n" \
  "v_mfma_f32_16x16x32_bf16 v[100:103], v[132:135], a[172:175], v[100:103]\n" \
  "s_cmp_lt_u32 s51, 884736\n" \
  "s_cselect_b32 s60, 36864, 0\n" \
  "s_add_u32 s51, s51, s60\n" \
  "v_mfma_f32_16x16x32_bf16 v[88:91], v[104:107], a[176:179], 0\n" \
  "s_add_u32 s51, s51, 36864\n" \
  "s_min_u32 s51, s51, 2322432\n" \
  "s_add_u32 s52, s52, 36864\n" \
  "v_mfma_f32_16x16x32_bf16 v[92:95], v[108:111], a[176:179], 0\n" \
  "s_cmp_ge_u32 s52, 163840\n" \
  "s_cselect_b32 s60, 147456, 0\n" \
  "s_sub_u32 s52, s52, s60\n" \
  "v_mfma_f32_16x16x32_bf16 v[88:91], v[112:115], a[180:183], v[88:91]\n" \
  "v_cvt_pk_bf16_f32 v136, v96, v97\n" \
  "v_cvt_pk_bf16_f32 v137, v98, v99\n" \
  "v_mfma_f32_16x16x32_bf16 v[92:95], v[116:119], a[180:183], v[92:95]\n" \
  "v_cvt_pk_bf16_f32 v138, v100, v101\n" \
  "v_cvt_pk_bf16_f32 v139, v102, v103\n" \
  "s_xor_b32 s63, s50, 1024\n" \
  "v_mfma_f32_16x16x32_bf16 v[88:91], v[120:123], a[184:187], v[88:91]\n" \
  "s_add_u32 s63, s63, 1048576\n" \
  "v_mfma_f32_16x16x32_bf16 v[92:95], v[124:127], a[184:187], v[92:95]\n" \
  "buffer_store_dwordx4 v[136:139], v190, s[40:43], s63 offen sc1\n" \
  "v_mfma_f32_16x16x32_bf16 v[88:91], v[128:131], a[188:191], v[88:91]\n" \
  "s_nop 2\n" \
  "v_mfma_f32_16x16x32_bf16 v[92:95], v[132:135], a[188:191], v[92:95]\n" \
  "s_nop 0\n" \
  "s_nop 0\n" \
  "v_mfma_f32_16x16x32_bf16 v[96:99], v[104:107], a[192:195], 0\n" \
  "v_mfma_f32_16x16x32_bf16 v[100:103], v[108:111], a[192:195], 0\n" \
  "v_mfma_f32_16x16x32_bf16 v[96:99], v[112:115], a[196:199], v[96:99]\n" \
  "v_cvt_pk_bf16_f32 v136, v88, v89\n" \
  "v_cvt_pk_bf16_f32 v137, v90, v91\n" \
  "v_mfma_f32_16x16x32_bf16 v[100:103], v[116:119], a[196:199], v[100:103]\n" \
  "v_cvt_pk_bf16_f32 v138, v92, v93\n" \
  "v_cvt_pk_bf16_f32 v139, v94, v95\n" \
  "s_xor_b32 s63, s50, 278528\n" \
  "v_mfma_f32_16x16x32_bf16 v[96:99], v[120:123], a[200:203], v[96:99]\n" \
  "s_add_u32 s63, s63, 8388608\n" \
  "v_mfma_f32_16x16x32_bf16 v[100:103], v[124:127], a[200:203], v[100:103]\n" \
  "buffer_store_dwordx4 v[136:139], v190, s[40:43], s63 offen sc1\n" \
  "v_mfma_f32_16x16x32_bf16 v[96:99], v[128:131], a[204:207], v[96:99]\n" \
  "v_readfirstlane_b32 s58, v164\n" \
  "v_readfirstlane_b32 s61, v165\n" \
  "v_mfma_f32_16x16x32_bf16 v[100:103], v[132:135], a[204:207], v[100:103]\n" \
  "s_cmp_eq_u32 s61, s55\n" \
  "s_cselect_b32 s58, s58, 0\n" \
  "s_add_u32 s58, s58, s68\n" \
  "v_mfma_f32_16x16x32_bf16 v[88:91], v[104:107], a[208:211], 0\n" \
  "s_cmp_lt_u32 s58, s73\n" \
  "s_cbranch_scc1 .LU_fc_%=\n" \
  ".LU_fc_b_%=:\n" \
  "v_mfma_f32_16x16x32_bf16 v[92:95], v[108:111], a[208:211], 0\n" \
  "buffer_load_dwordx2 v[164:165], off, s[40:43], %43 sc1\n" \
  "v_mfma_f32_16x16x32_bf16 v[88:91], v[112:115], a[212:215], v[88:91]\n" \
  "v_cvt_pk_bf16_f32 v136, v96, v97\n" \
  "v_cvt_pk_bf16_f32 v137, v98, v99\n" \
  "v_mfma_f32_16x16x32_bf16 v[92:95], v[116:119], a[212:215], v[92:95]\n" \
  "v_cvt_pk_bf16_f32 v138, v100, v101\n" \
  "v_cvt_pk_bf16_f32 v139, v102, v103\n" \
  "s_xor_b32 s63, s50, 279552\n" \
  "v_mfma_f32_16x16x32_bf16 v[88:91], v[120:123], a[216:219], v[88:91]\n" \
  "s_add_u32 s63, s63, 9437184\n" \
  "v_mfma_f32_16x16x32_bf16 v[92:95], v[124:127], a[216:219], v[92:95]\n" \
  "buffer_store_dwordx4 v[136:139], v190, s[40:43], s63 offen sc1\n" \
  "v_mfma_f32_16x16x32_bf16 v[88:91], v[128:131], a[220:223], v[88:91]\n" \
  "s_nop 2\n" \
  "v_mfma_f32_16x16x32_bf16 v[92:95], v[132:135], a[220:223], v[92:95]\n" \
  "s_nop 0\n" \
  "s_nop 0\n" \
  "v_mfma_f32_16x16x32_bf16 v[96:99], v[104:107], a[224:227], 0\n" \
  "v_mfma_f32_16x16x32_bf16 v[100:103], v[108:111], a[224:227], 0\n" \
  "v_mfma_f32_16x16x32_bf16 v[96:99], v[112:115], a[228:231], v[96:99]\n" \
  "v_cvt_pk_bf16_f32 v136, v88, v89\n" \
  "v_cvt_pk_bf16_f32 v137, v90, v91\n" \
  "v_mfma_f32_16x16x32_bf16 v[100:103], v[116:119], a[228:231], v[100:103]\n" \
  "v_cvt_pk_bf16_f32 v138, v92, v93\n" \
  "v_cvt_pk_bf16_f32 v139, v94, v95\n" \
  "s_xor_b32 s63, s50, 557056\n" \
  "v_mfma_f32_16x16x32_bf16 v[96:99], v[120:123], a[232:235], v[96:99]\n" \
  "s_add_u32 s63, s63, 16777216\n" \
  "v_mfma_f32_16x16x32_bf16 v[100:103], v[124:127], a[232:235], v[100:103]\n" \
  "buffer_store_dwordx4 v[136:139], v190, s[40:43], s63 offen sc1\n" \
  "v_mfma_f32_16x16x32_bf16 v[96:99], v[128:131], a[236:239], v[96:99]\n" \
  "s_nop 2\n" \
  "v_mfma_f32_16x16x32_bf16 v[100:103], v[132:135], a[236:239], v[100:103]\n" \
  "s_nop 0\n" \
  "s_nop 0\n" \
  "v_mfma_f32_16x16x32_bf16 v[88:91], v[104:107], a[240:243], 0\n" \
  "v_mfma_f32_16x16x32_bf16 v[92:95], v[108:111], a[240:243], 0\n" \
  "v_mfma_f32_16x16x32_bf16 v[88:91], v[112:115], a[244:247], v[88:91]\n" \
  "v_cvt_pk_bf16_f32 v136, v96, v97\n" \
  "v_cvt_pk_bf16_f32 v137, v98, v99\n" \
  "v_mfma_f32_16x16x32_bf16 v[92:95], v[116:119], a[244:247], v[92:95]\n" \
  "v_cvt_pk_bf16_f32 v138, v100, v101\n" \
  "v_cvt_pk_bf16_f32 v139, v102, v103\n" \
  "s_xor_b32 s63, s50, 558080\n" \
  "v_mfma_f32_16x16x32_bf16 v[88:91], v[120:123], a[248:251], v[88:91]\n" \
  "s_add_u32 s63, s63, 17825792\n" \
  "v_mfma_f32_16x16x32_bf16 v[92:95], v[124:127], a[248:251], v[92:95]\n" \
  "buffer_store_dwordx4 v[136:139], v190, s[40:43], s63 offen sc1\n" \
  "v_mfma_f32_16x16x32_bf16 v[88:91], v[128:131], a[252:255], v[88:91]\n" \
  "v_mfma_f32_16x16x32_bf16 v[92:95], v[132:135], a[252:255], v[92:95]\n" \
  "v_mfma_f32_16x16x32_bf16 v[96:99], v[104:107], v[0:3], 0\n" \
  "ds_read_b128 v[32:35], v163\n" \
  "ds_read_b128 v[36:39], v163 offset:18432\n" \
  "v_mfma_f32_16x16x32_bf16 v[100:103], v[108:111], v[0:3], 0\n" \
  "ds_read_b128 v[40:43], v162\n" \
  "ds_read_b128 v[44:47], v162 offset:18432\n" \
  "v_mfma_f32_16x16x32_bf16 v[96:99], v[112:115], v[4:7], v[96:99]\n" \
  "v_cvt_pk_bf16_f32 v136, v88, v89\n" \
  "v_cvt_pk_bf16_f32 v137, v90, v91\n" \
  "v_mfma_f32_16x16x32_bf16 v[100:103], v[116:119], v[4:7], v[100:103]\n" \
  "v_cvt_pk_bf16_f32 v138, v92, v93\n" \
  "v_cvt_pk_bf16_f32 v139, v94, v95\n" \
  "s_xor_b32 s63, s50, 835584\n" \
  "v_mfma_f32_16x16x32_bf16 v[96:99], v[120:123], v[8:11], v[96:99]\n" \
  "s_add_u32 s63, s63, 25165824\n" \
  "ds_read_b128 v[48:51], v163 offset:128\n" \
  "v_mfma_f32_16x16x32_bf16 v[100:103], v[124:127], v[8:11], v[100:103]\n" \
  "buffer_store_dwordx4 v[136:139], v190, s[40:43], s63 offen sc1\n" \
  "v_mfma_f32_16x16x32_bf16 v[96:99], v[128:131], v[12:15], v[96:99]\n" \
  "ds_read_b128 v[52:55], v163 offset:18560\n" \
  "v_mfma_f32_16x16x32_bf16 v[100:103], v[132:135], v[12:15], v[100:103]\n" \
  "v_mov_b32 v191, v163\n" \
  "v_mov_b32 v192, v162\n" \
  "v_mfma_f32_16x16x32_bf16 v[88:91], v[104:107], v[16:19], 0\n" \
  "v_xor_b32 v193, 8192, v193\n" \
  "v_xor_b32 v194, 8192, v194\n" \
  "s_add_u32 s49, s49, s71\n" \
  "v_mfma_f32_16x16x32_bf16 v[92:95], v[108:111], v[16:19], 0\n" \
  "v_mfma_f32_16x16x32_bf16 v[88:91], v[112:115], v[20:23], v[88:91]\n" \
  "v_cvt_pk_bf16_f32 v104, v96, v97\n" \
  "v_cvt_pk_bf16_f32 v105, v98, v99\n" \
  "v_mfma_f32_16x16x32_bf16 v[92:95], v[116:119], v[20:23], v[92:95]\n" \
  "v_cvt_pk_bf16_f32 v106, v100, v101\n" \
  "v_cvt_pk_bf16_f32 v107, v102, v103\n" \
  "s_xor_b32 s63, s50, 836608\n" \
  "v_mfma_f32_16x16x32_bf16 v[88:91], v[120:123], v[24:27], v[88:91]\n" \
  "s_add_u32 s63, s63, 26214400\n" \
  "v_mfma_f32_16x16x32_bf16 v[92:95], v[124:127], v[24:27], v[92:95]\n" \
  "buffer_store_dwordx4 v[104:107], v190, s[40:43], s63 offen sc1\n" \
  "v_mfma_f32_16x16x32_bf16 v[88:91], v[128:131], v[28:31], v[88:91]\n" \
  "v_mfma_f32_16x16x32_bf16 v[92:95], v[132:135], v[28:31], v[92:95]\n" \
  "s_cmp_lt_u32 s48, 1\n" \
  "s_cbranch_scc1 .LU_now_%=\n" \
  ".LU_first_b_%=:\n" \
  "s_add_u32 s48, s48, 1\n" \
  "s_add_u32 s73, s73, s70\n" \
  "s_cmp_eq_u32 s73, s74\n" \
  "s_cselect_b32 s49, 2130706432, s49\n" \
  "s_cmp_le_u32 s48, 5\n" \
  "s_addc_u32 s60, s73, 0\n" \
  "s_cmp_le_u32 s60, s74\n" \
  "s_waitcnt lgkmcnt(0)\n" \
  "s_cbranch_scc1 .LU_loop_%=\n" \
  "s_nop 7\n" \
  "v_cvt_pk_bf16_f32 v104, v88, v89\n" \
  "v_cvt_pk_bf16_f32 v105, v90, v91\n" \
  "v_cvt_pk_bf16_f32 v106, v92, v93\n" \
  "v_cvt_pk_bf16_f32 v107, v94, v95\n" \
  "s_mov_b32 s63, s57\n" \
  "buffer_store_dwordx4 v[104:107], v190, s[40:43], s63 offen sc1\n" \
  "s_waitcnt vmcnt(0)\n" \
  "s_barrier\n" \
  "s_mov_b32 s48, s74\n" \
  "s_mov_b32 s69, 0\n" \
  ".LU_ie_%=:\n" \
  "s_cmp_ge_u32 s96, 0xc0000000\n" \
  "s_cbranch_scc1 .LU_ie_x_%=\n" \
  "buffer_load_dwordx2 v[184:185], off, s[40:43], %41 sc1\n" \
  "s_waitcnt vmcnt(0)\n" \
  "v_readfirstlane_b32 s96, v184\n" \
  "s_add_u32 s69, s69, 1\n" \
  "s_cmp_lt_u32 s69, 2097152\n" \
  "s_cbranch_scc1 .LU_ie_%=\n" \
  ".LU_ie_x_%=:\n" \
  "v_mov_b32 v168, s48\n" \
  "buffer_store_dwordx2 v[168:169], v167, s[40:43], 0 offen sc1\n" \
  "s_branch .LU_end_%=\n" \
  ".LU_e2_%=:\n" \
  "s_waitcnt vmcnt(4)\n" \
  "v_add_u32 v168, s73, v161\n" \
  "s_mov_b64 exec, 2\n" \
  "buffer_store_dwordx2 v[168:169], v167, s[40:43], 0 offen sc1\n" \
  "s_mov_b64 exec, -1\n" \
  "v_readfirstlane_b32 s96, v184\n" \
  "s_cmp_lg_u32 s48, 7\n" \
  "s_cbranch_scc1 .LU_in_x_%=\n" \
  "s_mov_b32 s69, 0\n" \
  ".LU_in_%=:\n" \
  "s_cmp_ge_u32 s96, 0xc0000000\n" \
  "s_cbranch_scc1 .LU_in_x_%=\n" \
  "buffer_load_dwordx2 v[184:185], off, s[40:43], %41 sc1\n" \
  "s_waitcnt vmcnt(0)\n" \
  "v_readfirstlane_b32 s96, v184\n" \
  "s_add_u32 s69, s69, 1\n" \
  "s_cmp_lt_u32 s69, 2097152\n" \
  "s_cbranch_scc1 .LU_in_%=\n" \
  ".LU_in_x_%=:\n" \
  "buffer_load_dwordx2 v[184:185], off, s[40:43], %41 sc1\n" \
  "s_branch .LU_e2_b_%=\n" \
  ".LU_fc_%=:\n" \
  "s_mov_b32 s69, 0\n" \
  ".LU_fc1_%=:\n" \
  "s_sleep 8\n" \
  "buffer_load_dwordx2 v[164:165], off, s[40:43], %43 sc1\n" \
  "s_waitcnt vmcnt(0)\n" \
  "v_readfirstlane_b32 s58, v164\n" \
  "v_readfirstlane_b32 s61, v165\n" \
  "s_cmp_eq_u32 s61, s55\n" \
  "s_cselect_b32 s58, s58, 0\n" \
  "s_add_u32 s58, s58, s68\n" \
  "s_cmp_lt_u32 s58, s73\n" \
  "s_cbranch_scc0 .LU_fc_b_%=\n" \
  "s_add_u32 s69, s69, 1\n" \
  "s_cmp_lt_u32 s69, 524288\n" \
  "s_cbranch_scc1 .LU_fc1_%=\n" \
  "v_mov_b32 v164, 1\n" \
  "v_mov_b32 v165, s55\n" \
  "buffer_store_dwordx2 v[164:165], off, s[40:43], %42 offset:128 sc1\n" \
  "s_waitcnt vmcnt(0)\n" \
  "v_mov_b32 v164, 100\n" \
  "s_branch .LU_fc_b_%=\n" \
  ".LU_first_%=:\n" \
  "s_waitcnt lgkmcnt(0)\n" \
  "s_nop 7\n" \
  "s_lshr_b32 s62, s49, 8\n" \
  "s_and_b32 s62, s62, 8192\n" \
  "s_xor_b32 s62, s62, s49\n" \
  "v_cvt_pk_bf16_f32 v80, v64, v65\n" \
  "v_cvt_pk_bf16_f32 v81, v66, v67\n" \
  "v_cvt_pk_bf16_f32 v82, v72, v73\n" \
  "v_cvt_pk_bf16_f32 v83, v74, v75\n" \
  "buffer_store_dwordx4 v[80:83], v190, s[40:43], s62 offen sc1\n" \
  "ds_write_b128 v193, v[80:83]\n" \
  "v_cvt_pk_bf16_f32 v84, v76, v77\n" \
  "v_cvt_pk_bf16_f32 v85, v78, v79\n" \
  "v_cvt_pk_bf16_f32 v86, v68, v69\n" \
  "v_cvt_pk_bf16_f32 v87, v70, v71\n" \
  "buffer_store_dwordx4 v[84:87], v190, s[40:43], s62 offen offset:1024 sc1\n" \
  "ds_write_b128 v193, v[84:87] offset:1024\n" \
  "s_cmp_eq_u32 s48, 5\n" \
  "s_cbranch_scc1 .LU_skip_%=\n" \
  "v_xor_b32 v160, 8, v194\n" \
  "s_waitcnt lgkmcnt(0)\n" \
  "s_barrier\n" \
  "v_xor_b32 v194, 8192, v194\n" \
  "v_xor_b32 v160, 8192, v160\n" \
  "ds_read_b64_tr_b16 v[104:105], v194\n" \
  "ds_read_b64_tr_b16 v[106:107], v194 offset:256\n" \
  "ds_read_b64_tr_b16 v[108:109], v160\n" \
  "ds_read_b64_tr_b16 v[110:111], v160 offset:256\n" \
  "ds_read_b64_tr_b16 v[112:113], v194 offset:2048\n" \
  "ds_read_b64_tr_b16 v[114:115], v194 offset:2304\n" \
  "ds_read_b64_tr_b16 v[116:117], v160 offset:2048\n" \
  "ds_read_b64_tr_b16 v[118:119], v160 offset:2304\n" \
  "ds_read_b64_tr_b16 v[120:121], v194 offset:4096\n" \
  "ds_read_b64_tr_b16 v[122:123], v194 offset:4352\n" \
  "ds_read_b64_tr_b16 v[124:125], v160 offset:4096\n" \
  "ds_read_b64_tr_b16 v[126:127], v160 offset:4352\n" \
  "ds_read_b64_tr_b16 v[128:129], v194 offset:6144\n" \
  "ds_read_b64_tr_b16 v[130:131], v194 offset:6400\n" \
  "ds_read_b64_tr_b16 v[132:133], v160 offset:6144\n" \
  "ds_read_b64_tr_b16 v[134:135], v160 offset:6400\n" \
  "v_xor_b32 v194, 8192, v194\n" \
  "s_mov_b32 s58, s73\n" \
  "s_and_b32 s58, s58, 15\n" \
  "s_lshl_b32 s58, s58, 16\n" \
  "s_xor_b32 s50, s58, s66\n" \
  "s_xor_b32 s57, s58, s59\n" \
  "s_waitcnt lgkmcnt(0)\n" \
  "s_branch .LU_phb_%=\n" \
  ".LU_skip_%=:\n" \
  "s_mov_b64 exec, 6\n" \
  "v_mov_b32 v166, -1\n" \
  "s_mov_b64 exec, -1\n" \
  "s_add_u32 s58, s48, 1\n" \
  "s_and_b32 s58, s58, 3\n" \
  "s_cselect_b32 s58, 0, 147456\n" \
  "s_sub_u32 s58, 36864, s58\n" \
  "v_add_u32 v163, s58, v191\n" \
  "v_add_u32 v162, s58, v192\n" \
  "s_cmp_lt_u32 s51, 884736\n" \
  "s_cselect_b32 s60, 36864, 0\n" \
  "s_add_u32 s51, s51, s60\n" \
  "s_add_u32 s51, s51, 36864\n" \
  "s_min_u32 s51, s51, 2322432\n" \
  "s_add_u32 s52, s52, 36864\n" \
  "s_cmp_ge_u32 s52, 163840\n" \
  "s_cselect_b32 s60, 147456, 0\n" \
  "s_sub_u32 s52, s52, s60\n" \
  "ds_read_b128 v[32:35], v163\n" \
  "ds_read_b128 v[36:39], v163 offset:18432\n" \
  "ds_read_b128 v[40:43], v162\n" \
  "ds_read_b128 v[44:47], v162 offset:18432\n" \
  "ds_read_b128 v[48:51], v163 offset:128\n" \
  "ds_read_b128 v[52:55], v163 offset:18560\n" \
  "v_mov_b32 v191, v163\n" \
  "v_mov_b32 v192, v162\n" \
  "v_xor_b32 v193, 8192, v193\n" \
  "v_xor_b32 v194, 8192, v194\n" \
  "s_add_u32 s49, s49, s71\n" \
  "s_mov_b32 s57, 2130706432\n" \
  "s_mov_b32 s69, 0\n" \
  ".LU_sk1_%=:\n" \
  "buffer_load_dwordx2 v[164:165], off, s[40:43], %43 sc1\n" \
  "s_waitcnt vmcnt(0)\n" \
  "v_readfirstlane_b32 s58, v164\n" \
  "v_readfirstlane_b32 s61, v165\n" \
  "s_cmp_eq_u32 s61, s55\n" \
  "s_cselect_b32 s58, s58, 0\n" \
  "s_add_u32 s58, s58, s68\n" \
  "s_cmp_lt_u32 s58, s73\n" \
  "s_cbranch_scc0 .LU_first_b_%=\n" \
  "v_mov_b32 v168, s73\n" \
  "s_mov_b64 exec, 2\n" \
  "buffer_store_dwordx2 v[168:169], v167, s[40:43], 0 offen sc1\n" \
  "s_mov_b64 exec, -1\n" \
  "s_sleep 16\n" \
  "s_add_u32 s69, s69, 1\n" \
  "s_cmp_lt_u32 s69, 524288\n" \
  "s_cbranch_scc1 .LU_sk1_%=\n" \
  "v_mov_b32 v164, 1\n" \
  "v_mov_b32 v165, s55\n" \
  "buffer_store_dwordx2 v[164:165], off, s[40:43], %42 offset:128 sc1\n" \
  "s_waitcnt vmcnt(0)\n" \
  "s_branch .LU_first_b_%=\n" \
  ".LU_now_%=:\n" \
  "s_nop 7\n" \
  "v_cvt_pk_bf16_f32 v104, v88, v89\n" \
  "v_cvt_pk_bf16_f32 v105, v90, v91\n" \
  "v_cvt_pk_bf16_f32 v106, v92, v93\n" \
  "v_cvt_pk_bf16_f32 v107, v94, v95\n" \
  "s_mov_b32 s63, s57\n" \
  "buffer_store_dwordx4 v[104:107], v190, s[40:43], s63 offen sc1\n" \
  "s_add_u32 s60, s73, 1\n" \
  "v_mov_b32 v168, s60\n" \
  "s_mov_b64 exec, 6\n" \
  "s_waitcnt vmcnt(0)\n" \
  "buffer_store_dwordx2 v[168:169], v167, s[40:43], 0 offen sc1\n" \
  "s_mov_b64 exec, -1\n" \
  "s_branch .LU_first_b_%=\n" \
  ".LU_end_%=:\n" \
  "s_mov_b32 %0, s55\n"
#define P_ASM_VOPS "=&s"(tag), "+{v190}"(pi.l16), "+{v191}"(pi.xr0), "+{v192}"(pi.xr1), "+{v193}"(pi.ew), "+{v194}"(pi.er), "+{v195}"(pi.uo), "+{v196}"(pi.xs0), "+{v197}"(pi.xs1), "+{v198}"(pi.xs2), "+{v199}"(pi.xs3), "+{v200}"(pi.xs4), "+{v201}"(pi.xs5), "+{v202}"(pi.xs6), "+{v203}"(pi.xs7), "+{v204}"(pi.xs8), "+{v170}"(pi.vq0), "+{v171}"(pi.vq1), "+{v172}"(pi.vq2), "+{v173}"(pi.vq3), "+{v174}"(pi.vo0), "+{v175}"(pi.vo1), "+{v176}"(pi.vo2), "+{v177}"(pi.vo3), "+{v178}"(pi.v50), "+{v179}"(pi.v51), "+{v180}"(pi.v52), "+{v181}"(pi.v53), "+{v182}"(pi.tro0), "+{v183}"(pi.tro1)
#define P_ASM_SOPS "s"(pi.ws), "s"(pi.Wa), "s"(pi.Wb), "s"(pi.x), "s"(pi.w), "s"(pi.o_a), "s"(pi.o_b), "s"(pi.o_p), "s"(pi.o_q), "s"(pi.f_a), "s"(pi.f_b), "s"(pi.f_go), "s"(pi.o_ctl), "s"(pi.f_c), "s"(pi.u8), "s"(pi.rng), "s"(pi.f_d)
#define P_ASM_CLOB "v0", "v1", "v2", "v3", "v4", "v5", "v6", "v7", "v8", "v9", "v10", "v11", "v12", "v13", "v14", "v15", "v16", "v17", "v18", "v19", "v20", "v21", "v22", "v23", "v24", "v25", "v26", "v27", "v28", "v29", "v30", "v31", "v32", "v33", "v34", "v35", "v36", "v37", "v38", "v39", "v40", "v41", "v42", "v43", "v44", "v45", "v46", "v47", "v48", "v49", "v50", "v51", "v52", "v53", "v54", "v55", "v56", "v57", "v58", "v59", "v60", "v61", "v62", "v63", "v64", "v65", "v66", "v67", "v68", "v69", "v70", "v71", "v72", "v73", "v74", "v75", "v76", "v77", "v78", "v79", "v80", "v81", "v82", "v83", "v84", "v85", "v86", "v87", "v88", "v89", "v90", "v91", "v92", "v93", "v94", "v95", "v96", "v97", "v98", "v99", "v100", "v101", "v102", "v103", "v104", "v105", "v106", "v107", "v108", "v109", "v110", "v111", "v112", "v113", "v114", "v115", "v116", "v117", "v118", "v119", "v120", "v121", "v122", "v123", "v124", "v125", "v126", "v127", "v128", "v129", "v130", "v131", "v132", "v133", "v134", "v135", "v136", "v137", "v138", "v139", "v140", "v141", "v142", "v143", "v144", "v145", "v146", "v147", "v148", "v149", "v150", "v151", "v152", "v153", "v154", "v155", "v156", "v157", "v158", "v159", "v160", "v161", "v162", "v163", "v164", "v165", "v166", "v167", "v168", "v169", "v184", "v185", "v186", "v187", "v188", "v189", "a0", "a1", "a2", "a3", "a4", "a5", "a6", "a7", "a8", "a9", "a10", "a11", "a12", "a13", "a14", "a15", "a16", "a17", "a18", "a19", "a20", "a21", "a22", "a23", "a24", "a25", "a26", "a27", "a28", "a29", "a30", "a31", "a32", "a33", "a34", "a35", "a36", "a37", "a38", "a39", "a40", "a41", "a42", "a43", "a44", "a45", "a46", "a47", "a48", "a49", "a50", "a51", "a52", "a53", "a54", "a55", "a56", "a57", "a58", "a59", "a60", "a61", "a62", "a63", "a64", "a65", "a66", "a67", "a68", "a69", "a70", "a71", "a72", "a73", "a74", "a75", "a76", "a77", "a78", "a79", "a80", "a81", "a82", "a83", "a84", "a85", "a86", "a87", "a88", "a89", "a90", "a91", "a92", "a93", "a94", "a95", "a96", "a97", "a98", "a99", "a100", "a101", "a102", "a103", "a104", "a105", "a106", "a107", "a108", "a109", "a110", "a111", "a112", "a113", "a114", "a115", "a116", "a117", "a118", "a119", "a120", "a121", "a122", "a123", "a124", "a125", "a126", "a127", "a128", "a129", "a130", "a131", "a132", "a133", "a134", "a135", "a136", "a137", "a138", "a139", "a140", "a141", "a142", "a143", "a144", "a145", "a146", "a147", "a148", "a149", "a150", "a151", "a152", "a153", "a154", "a155", "a156", "a157", "a158", "a159", "a160", "a161", "a162", "a163", "a164", "a165", "a166", "a167", "a168", "a169", "a170", "a171", "a172", "a173", "a174", "a175", "a176", "a177", "a178", "a179", "a180", "a181", "a182", "a183", "a184", "a185", "a186", "a187", "a188", "a189", "a190", "a191", "a192", "a193", "a194", "a195", "a196", "a197", "a198", "a199", "a200", "a201", "a202", "a203", "a204", "a205", "a206", "a207", "a208", "a209", "a210", "a211", "a212", "a213", "a214", "a215", "a216", "a217", "a218", "a219", "a220", "a221", "a222", "a223", "a224", "a225", "a226", "a227", "a228", "a229", "a230", "a231", "a232", "a233", "a234", "a235", "a236", "a237", "a238", "a239", "a240", "a241", "a242", "a243", "a244", "a245", "a246", "a247", "a248", "a249", "a250", "a251", "a252", "a253", "a254", "a255", "s40", "s41", "s42", "s43", "s44", "s45", "s46", "s47", "s48", "s49", "s50", "s51", "s52", "s53", "s54", "s55", "s56", "s57", "s58", "s59", "s60", "s61", "s62", "s63", "s64", "s65", "s66", "s67", "s68", "s69", "s70", "s71", "s72", "s73", "s74", "s75", "s76", "s77", "s78", "s79", "s80", "s81", "s82", "s83", "s84", "s85", "s86", "s87", "s88", "s89", "s90", "s91", "s92", "s93", "s94", "s95", "s96", "s97", "s98", "s99", "vcc", "scc", "memory"
#define O_ASM_TEXT \
  "s_mov_b64 s[40:41], %11\n" \
  "s_and_b32 s41, s41, 0xffff\n" \
  "s_mov_b32 s42, 255852544\n" \
  "s_mov_b32 s43, 0x00020000\n" \
  "s_mov_b64 s[44:45], %12\n" \
  "s_and_b32 s45, s45, 0xffff\n" \
  "s_mov_b32 s46, 4718592\n" \
  "s_mov_b32 s47, 0x00020000\n" \
  "s_mul_i32 s62, %13, 24576\n" \
  "s_add_u32 s60, %15, 0\n" \
  "s_add_u32 m0, s62, 0\n" \
  "s_nop 0\n" \
  "buffer_load_dwordx4 v194, s[44:47], s60 offen lds\n" \
  "s_add_u32 m0, s62, 1024\n" \
  "s_nop 0\n" \
  "buffer_load_dwordx4 v195, s[44:47], s60 offen lds\n" \
  "s_add_u32 m0, s62, 2048\n" \
  "s_nop 0\n" \
  "buffer_load_dwordx4 v196, s[44:47], s60 offen lds\n" \
  "s_add_u32 s60, %15, 18432\n" \
  "s_add_u32 m0, s62, 3072\n" \
  "s_nop 0\n" \
  "buffer_load_dwordx4 v194, s[44:47], s60 offen lds\n" \
  "s_add_u32 m0, s62, 4096\n" \
  "s_nop 0\n" \
  "buffer_load_dwordx4 v195, s[44:47], s60 offen lds\n" \
  "s_add_u32 m0, s62, 5120\n" \
  "s_nop 0\n" \
  "buffer_load_dwordx4 v196, s[44:47], s60 offen lds\n" \
  "s_add_u32 s60, %15, 36864\n" \
  "s_add_u32 m0, s62, 6144\n" \
  "s_nop 0\n" \
  "buffer_load_dwordx4 v194, s[44:47], s60 offen lds\n" \
  "s_add_u32 m0, s62, 7168\n" \
  "s_nop 0\n" \
  "buffer_load_dwordx4 v195, s[44:47], s60 offen lds\n" \
  "s_add_u32 m0, s62, 8192\n" \
  "s_nop 0\n" \
  "buffer_load_dwordx4 v196, s[44:47], s60 offen lds\n" \
  "s_add_u32 s60, %15, 55296\n" \
  "s_add_u32 m0, s62, 9216\n" \
  "s_nop 0\n" \
  "buffer_load_dwordx4 v194, s[44:47], s60 offen lds\n" \
  "s_add_u32 m0, s62, 10240\n" \
  "s_nop 0\n" \
  "buffer_load_dwordx4 v195, s[44:47], s60 offen lds\n" \
  "s_add_u32 m0, s62, 11264\n" \
  "s_nop 0\n" \
  "buffer_load_dwordx4 v196, s[44:47], s60 offen lds\n" \
  "s_add_u32 s60, %15, 73728\n" \
  "s_add_u32 m0, s62, 12288\n" \
  "s_nop 0\n" \
  "buffer_load_dwordx4 v194, s[44:47], s60 offen lds\n" \
  "s_add_u32 m0, s62, 13312\n" \
  "s_nop 0\n" \
  "buffer_load_dwordx4 v195, s[44:47], s60 offen lds\n" \
  "s_add_u32 m0, s62, 14336\n" \
  "s_nop 0\n" \
  "buffer_load_dwordx4 v196, s[44:47], s60 offen lds\n" \
  "s_add_u32 s60, %15, 92160\n" \
  "s_add_u32 m0, s62, 15360\n" \
  "s_nop 0\n" \
  "buffer_load_dwordx4 v194, s[44:47], s60 offen lds\n" \
  "s_add_u32 m0, s62, 16384\n" \
  "s_nop 0\n" \
  "buffer_load_dwordx4 v195, s[44:47], s60 offen lds\n" \
  "s_add_u32 m0, s62, 17408\n" \
  "s_nop 0\n" \
  "buffer_load_dwordx4 v196, s[44:47], s60 offen lds\n" \
  "s_add_u32 s60, %15, 110592\n" \
  "s_add_u32 m0, s62, 18432\n" \
  "s_nop 0\n" \
  "buffer_load_dwordx4 v194, s[44:47], s60 offen lds\n" \
  "s_add_u32 m0, s62, 19456\n" \
  "s_nop 0\n" \
  "buffer_load_dwordx4 v195, s[44:47], s60 offen lds\n" \
  "s_add_u32 m0, s62, 20480\n" \
  "s_nop 0\n" \
  "buffer_load_dwordx4 v196, s[44:47], s60 offen lds\n" \
  "s_add_u32 s60, %15, 129024\n" \
  "s_add_u32 m0, s62, 21504\n" \
  "s_nop 0\n" \
  "buffer_load_dwordx4 v194, s[44:47], s60 offen lds\n" \
  "s_add_u32 m0, s62, 22528\n" \
  "s_nop 0\n" \
  "buffer_load_dwordx4 v195, s[44:47], s60 offen lds\n" \
  "s_add_u32 m0, s62, 23552\n" \
  "s_nop 0\n" \
  "buffer_load_dwordx4 v196, s[44:47], s60 offen lds\n" \
  "s_waitcnt vmcnt(0)\n" \
  "ds_read_b64_tr_b16 a[0:1], v197\n" \
  "ds_read_b64_tr_b16 a[2:3], v197 offset:3072\n" \
  "ds_read_b64_tr_b16 a[4:5], v197 offset:32\n" \
  "ds_read_b64_tr_b16 a[6:7], v197 offset:3104\n" \
  "ds_read_b64_tr_b16 a[8:9], v197 offset:64\n" \
  "ds_read_b64_tr_b16 a[10:11], v197 offset:3136\n" \
  "ds_read_b64_tr_b16 a[12:13], v197 offset:96\n" \
  "ds_read_b64_tr_b16 a[14:15], v197 offset:3168\n" \
  "ds_read_b64_tr_b16 a[16:17], v197 offset:128\n" \
  "ds_read_b64_tr_b16 a[18:19], v197 offset:3200\n" \
  "ds_read_b64_tr_b16 a[20:21], v197 offset:160\n" \
  "ds_read_b64_tr_b16 a[22:23], v197 offset:3232\n" \
  "ds_read_b64_tr_b16 a[24:25], v197 offset:6144\n" \
  "ds_read_b64_tr_b16 a[26:27], v197 offset:9216\n" \
  "ds_read_b64_tr_b16 a[28:29], v197 offset:6176\n" \
  "ds_read_b64_tr_b16 a[30:31], v197 offset:9248\n" \
  "ds_read_b64_tr_b16 a[32:33], v197 offset:6208\n" \
  "ds_read_b64_tr_b16 a[34:35], v197 offset:9280\n" \
  "ds_read_b64_tr_b16 a[36:37], v197 offset:6240\n" \
  "ds_read_b64_tr_b16 a[38:39], v197 offset:9312\n" \
  "ds_read_b64_tr_b16 a[40:41], v197 offset:6272\n" \
  "ds_read_b64_tr_b16 a[42:43], v197 offset:9344\n" \
  "ds_read_b64_tr_b16 a[44:45], v197 offset:6304\n" \
  "ds_read_b64_tr_b16 a[46:47], v197 offset:9376\n" \
  "ds_read_b64_tr_b16 a[48:49], v197 offset:12288\n" \
  "ds_read_b64_tr_b16 a[50:51], v197 offset:15360\n" \
  "ds_read_b64_tr_b16 a[52:53], v197 offset:12320\n" \
  "ds_read_b64_tr_b16 a[54:55], v197 offset:15392\n" \
  "ds_read_b64_tr_b16 a[56:57], v197 offset:12352\n" \
  "ds_read_b64_tr_b16 a[58:59], v197 offset:15424\n" \
  "ds_read_b64_tr_b16 a[60:61], v197 offset:12384\n" \
  "ds_read_b64_tr_b16 a[62:63], v197 offset:15456\n" \
  "ds_read_b64_tr_b16 a[64:65], v197 offset:12416\n" \
  "ds_read_b64_tr_b16 a[66:67], v197 offset:15488\n" \
  "ds_read_b64_tr_b16 a[68:69], v197 offset:12448\n" \
  "ds_read_b64_tr_b16 a[70:71], v197 offset:15520\n" \
  "ds_read_b64_tr_b16 a[72:73], v197 offset:18432\n" \
  "ds_read_b64_tr_b16 a[74:75], v197 offset:21504\n" \
  "ds_read_b64_tr_b16 a[76:77], v197 offset:18464\n" \
  "ds_read_b64_tr_b16 a[78:79], v197 offset:21536\n" \
  "ds_read_b64_tr_b16 a[80:81], v197 offset:18496\n" \
  "ds_read_b64_tr_b16 a[82:83], v197 offset:21568\n" \
  "ds_read_b64_tr_b16 a[84:85], v197 offset:18528\n" \
  "ds_read_b64_tr_b16 a[86:87], v197 offset:21600\n" \
  "ds_read_b64_tr_b16 a[88:89], v197 offset:18560\n" \
  "ds_read_b64_tr_b16 a[90:91], v197 offset:21632\n" \
  "ds_read_b64_tr_b16 a[92:93], v197 offset:18592\n" \
  "ds_read_b64_tr_b16 a[94:95], v197 offset:21664\n" \
  "s_waitcnt lgkmcnt(0)\n" \
  "s_add_u32 s60, %15, 147456\n" \
  "s_add_u32 m0, s62, 0\n" \
  "s_nop 0\n" \
  "buffer_load_dwordx4 v194, s[44:47], s60 offen lds\n" \
  "s_add_u32 m0, s62, 1024\n" \
  "s_nop 0\n" \
  "buffer_load_dwordx4 v195, s[44:47], s60 offen lds\n" \
  "s_add_u32 m0, s62, 2048\n" \
  "s_nop 0\n" \
  "buffer_load_dwordx4 v196, s[44:47], s60 offen lds\n" \
  "s_add_u32 s60, %15, 165888\n" \
  "s_add_u32 m0, s62, 3072\n" \
  "s_nop 0\n" \
  "buffer_load_dwordx4 v194, s[44:47], s60 offen lds\n" \
  "s_add_u32 m0, s62, 4096\n" \
  "s_nop 0\n" \
  "buffer_load_dwordx4 v195, s[44:47], s60 offen lds\n" \
  "s_add_u32 m0, s62, 5120\n" \
  "s_nop 0\n" \
  "buffer_load_dwordx4 v196, s[44:47], s60 offen lds\n" \
  "s_add_u32 s60, %15, 184320\n" \
  "s_add_u32 m0, s62, 6144\n" \
  "s_nop 0\n" \
  "buffer_load_dwordx4 v194, s[44:47], s60 offen lds\n" \
  "s_add_u32 m0, s62, 7168\n" \
  "s_nop 0\n" \
  "buffer_load_dwordx4 v195, s[44:47], s60 offen lds\n" \
  "s_add_u32 m0, s62, 8192\n" \
  "s_nop 0\n" \
  "buffer_load_dwordx4 v196, s[44:47], s60 offen lds\n" \
  "s_add_u32 s60, %15, 202752\n" \
  "s_add_u32 m0, s62, 9216\n" \
  "s_nop 0\n" \
  "buffer_load_dwordx4 v194, s[44:47], s60 offen lds\n" \
  "s_add_u32 m0, s62, 10240\n" \
  "s_nop 0\n" \
  "buffer_load_dwordx4 v195, s[44:47], s60 offen lds\n" \
  "s_add_u32 m0, s62, 11264\n" \
  "s_nop 0\n" \
  "buffer_load_dwordx4 v196, s[44:47], s60 offen lds\n" \
  "s_add_u32 s60, %15, 221184\n" \
  "s_add_u32 m0, s62, 12288\n" \
  "s_nop 0\n" \
  "buffer_load_dwordx4 v194, s[44:47], s60 offen lds\n" \
  "s_add_u32 m0, s62, 13312\n" \
  "s_nop 0\n" \
  "buffer_load_dwordx4 v195, s[44:47], s60 offen lds\n" \
  "s_add_u32 m0, s62, 14336\n" \
  "s_nop 0\n" \
  "buffer_load_dwordx4 v196, s[44:47], s60 offen lds\n" \
  "s_add_u32 s60, %15, 239616\n" \
  "s_add_u32 m0, s62, 15360\n" \
  "s_nop 0\n" \
  "buffer_load_dwordx4 v194, s[44:47], s60 offen lds\n" \
  "s_add_u32 m0, s62, 16384\n" \
  "s_nop 0\n" \
  "buffer_load_dwordx4 v195, s[44:47], s60 offen lds\n" \
  "s_add_u32 m0, s62, 17408\n" \
  "s_nop 0\n" \
  "buffer_load_dwordx4 v196, s[44:47], s60 offen lds\n" \
  "s_add_u32 s60, %15, 258048\n" \
  "s_add_u32 m0, s62, 18432\n" \
  "s_nop 0\n" \
  "buffer_load_dwordx4 v194, s[44:47], s60 offen lds\n" \
  "s_add_u32 m0, s62, 19456\n" \
  "s_nop 0\n" \
  "buffer_load_dwordx4 v195, s[44:47], s60 offen lds\n" \
  "s_add_u32 m0, s62, 20480\n" \
  "s_nop 0\n" \
  "buffer_load_dwordx4 v196, s[44:47], s60 offen lds\n" \
  "s_add_u32 s60, %15, 276480\n" \
  "s_add_u32 m0, s62, 21504\n" \
  "s_nop 0\n" \
  "buffer_load_dwordx4 v194, s[44:47], s60 offen lds\n" \
  "s_add_u32 m0, s62, 22528\n" \
  "s_nop 0\n" \
  "buffer_load_dwordx4 v195, s[44:47], s60 offen lds\n" \
  "s_add_u32 m0, s62, 23552\n" \
  "s_nop 0\n" \
  "buffer_load_dwordx4 v196, s[44:47], s60 offen lds\n" \
  "s_waitcnt vmcnt(0)\n" \
  "ds_read_b64_tr_b16 a[96:97], v197\n" \
  "ds_read_b64_tr_b16 a[98:99], v197 offset:3072\n" \
  "ds_read_b64_tr_b16 a[100:101], v197 offset:32\n" \
  "ds_read_b64_tr_b16 a[102:103], v197 offset:3104\n" \
  "ds_read_b64_tr_b16 a[104:105], v197 offset:64\n" \
  "ds_read_b64_tr_b16 a[106:107], v197 offset:3136\n" \
  "ds_read_b64_tr_b16 a[108:109], v197 offset:96\n" \
  "ds_read_b64_tr_b16 a[110:111], v197 offset:3168\n" \
  "ds_read_b64_tr_b16 a[112:113], v197 offset:128\n" \
  "ds_read_b64_tr_b16 a[114:115], v197 offset:3200\n" \
  "ds_read_b64_tr_b16 a[116:117], v197 offset:160\n" \
  "ds_read_b64_tr_b16 a[118:119], v197 offset:3232\n" \
  "ds_read_b64_tr_b16 a[120:121], v197 offset:6144\n" \
  "ds_read_b64_tr_b16 a[122:123], v197 offset:9216\n" \
  "ds_read_b64_tr_b16 a[124:125], v197 offset:6176\n" \
  "ds_read_b64_tr_b16 a[126:127], v197 offset:9248\n" \
  "ds_read_b64_tr_b16 a[128:129], v197 offset:6208\n" \
  "ds_read_b64_tr_b16 a[130:131], v197 offset:9280\n" \
  "ds_read_b64_tr_b16 a[132:133], v197 offset:6240\n" \
  "ds_read_b64_tr_b16 a[134:135], v197 offset:9312\n" \
  "ds_read_b64_tr_b16 a[136:137], v197 offset:6272\n" \
  "ds_read_b64_tr_b16 a[138:139], v197 offset:9344\n" \
  "ds_read_b64_tr_b16 a[140:141], v197 offset:6304\n" \
  "ds_read_b64_tr_b16 a[142:143], v197 offset:9376\n" \
  "ds_read_b64_tr_b16 a[144:145], v197 offset:12288\n" \
  "ds_read_b64_tr_b16 a[146:147], v197 offset:15360\n" \
  "ds_read_b64_tr_b16 a[148:149], v197 offset:12320\n" \
  "ds_read_b64_tr_b16 a[150:151], v197 offset:15392\n" \
  "ds_read_b64_tr_b16 a[152:153], v197 offset:12352\n" \
  "ds_read_b64_tr_b16 a[154:155], v197 offset:15424\n" \
  "ds_read_b64_tr_b16 a[156:157], v197 offset:12384\n" \
  "ds_read_b64_tr_b16 a[158:159], v197 offset:15456\n" \
  "ds_read_b64_tr_b16 a[160:161], v197 offset:12416\n" \
  "ds_read_b64_tr_b16 a[162:163], v197 offset:15488\n" \
  "ds_read_b64_tr_b16 a[164:165], v197 offset:12448\n" \
  "ds_read_b64_tr_b16 a[166:167], v197 offset:15520\n" \
  "ds_read_b64_tr_b16 a[168:169], v197 offset:18432\n" \
  "ds_read_b64_tr_b16 a[170:171], v197 offset:21504\n" \
  "ds_read_b64_tr_b16 a[172:173], v197 offset:18464\n" \
  "ds_read_b64_tr_b16 a[174:175], v197 offset:21536\n" \
  "ds_read_b64_tr_b16 a[176:177], v197 offset:18496\n" \
  "ds_read_b64_tr_b16 a[178:179], v197 offset:21568\n" \
  "ds_read_b64_tr_b16 a[180:181], v197 offset:18528\n" \
  "ds_read_b64_tr_b16 a[182:183], v197 offset:21600\n" \
  "ds_read_b64_tr_b16 a[184:185], v197 offset:18560\n" \
  "ds_read_b64_tr_b16 a[186:187], v197 offset:21632\n" \
  "ds_read_b64_tr_b16 a[188:189], v197 offset:18592\n" \
  "ds_read_b64_tr_b16 a[190:191], v197 offset:21664\n" \
  "s_waitcnt lgkmcnt(0)\n" \
  "s_barrier\n" \
  "s_mov_b32 s56, %20\n" \
  "s_mov_b32 s48, 0\n" \
  "s_mov_b32 s53, 2130706432\n" \
  "v_mov_b32 v187, %14\n" \
  "s_branch .LO_fl_%=\n" \
  ".LO_fl_b_%=:\n" \
  "s_and_b32 s48, s56, 255\n" \
  "s_bfe_u32 s55, s56, 0x80008\n" \
  "s_lshr_b32 s56, s56, 16\n" \
  "s_lshl_b32 s49, s48, 19\n" \
  "s_add_u32 s49, s49, %16\n" \
  "s_mul_i32 s50, s48, 393216\n" \
  "s_add_u32 s50, s50, %17\n" \
  "s_mov_b32 s53, 2130706432\n" \
  "s_mov_b32 s94, s48\n" \
  "v_mov_b32 v191, v199\n" \
  "v_mov_b32 v192, v200\n" \
  "s_mov_b32 s69, 0\n" \
  ".LO_p0_%=:\n" \
  "buffer_load_dwordx2 v[188:189], v193, s[40:43], 0 offen sc1\n" \
  "s_waitcnt vmcnt(0)\n" \
  "v_cmp_ne_u32 vcc, %14, v189\n" \
  "s_nop 1\n" \
  "v_cndmask_b32 v188, v188, 0, vcc\n" \
  "v_cmp_ge_i32 vcc, s94, v188\n" \
  "s_cbranch_vccz .LO_p0_x_%=\n" \
  "s_sleep 2\n" \
  "s_add_u32 s69, s69, 1\n" \
  "s_and_b32 s63, s69, 1023\n" \
  "s_cbranch_scc1 .LO_p0_%=\n" \
  "buffer_load_dwordx2 v[188:189], off, s[40:43], %19 offset:128 sc1\n" \
  "s_waitcnt vmcnt(0)\n" \
  "v_readfirstlane_b32 s63, v189\n" \
  "s_cmp_eq_u32 s63, %14\n" \
  "s_cbranch_scc1 .LO_p0_x_%=\n" \
  "s_cmp_le_u32 s69, 2097152\n" \
  "s_cbranch_scc1 .LO_p0_%=\n" \
  "v_mov_b32 v188, 1\n" \
  "v_mov_b32 v189, %14\n" \
  "buffer_store_dwordx2 v[188:189], off, s[40:43], %19 offset:128 sc1\n" \
  "s_waitcnt vmcnt(0)\n" \
  ".LO_p0_x_%=:\n" \
  "s_lshr_b32 s76, s49, 8\n" \
  "s_and_b32 s76, s76, 8192\n" \
  "s_xor_b32 s76, s76, s49\n" \
  "s_add_u32 s77, s76, 4096\n" \
  "s_add_u32 s78, s76, 16384\n" \
  "s_add_u32 s79, s76, 20480\n" \
  "s_add_u32 s49, s49, 524288\n" \
  "buffer_load_dwordx4 v[0:3], v190, s[40:43], s76 offen sc1\n" \
  "buffer_load_dwordx4 v[8:11], v190, s[40:43], s76 offen offset:2048 sc1\n" \
  "buffer_load_dwordx4 v[16:19], v190, s[40:43], s77 offen sc1\n" \
  "buffer_load_dwordx4 v[24:27], v190, s[40:43], s77 offen offset:2048 sc1\n" \
  "buffer_load_dwordx4 v[32:35], v190, s[40:43], s78 offen sc1\n" \
  "buffer_load_dwordx4 v[40:43], v190, s[40:43], s78 offen offset:2048 sc1\n" \
  "buffer_load_dwordx4 v[48:51], v190, s[40:43], s79 offen sc1\n" \
  "buffer_load_dwordx4 v[56:59], v190, s[40:43], s79 offen offset:2048 sc1\n" \
  "buffer_load_dwordx4 v[4:7], v190, s[40:43], s76 offen offset:1024 sc1\n" \
  "buffer_load_dwordx4 v[12:15], v190, s[40:43], s76 offen offset:3072 sc1\n" \
  "buffer_load_dwordx4 v[20:23], v190, s[40:43], s77 offen offset:1024 sc1\n" \
  "buffer_load_dwordx4 v[28:31], v190, s[40:43], s77 offen offset:3072 sc1\n" \
  "buffer_load_dwordx4 v[36:39], v190, s[40:43], s78 offen offset:1024 sc1\n" \
  "buffer_load_dwordx4 v[44:47], v190, s[40:43], s78 offen offset:3072 sc1\n" \
  "buffer_load_dwordx4 v[52:55], v190, s[40:43], s79 offen offset:1024 sc1\n" \
  "buffer_load_dwordx4 v[60:63], v190, s[40:43], s79 offen offset:3072 sc1\n" \
  "s_lshr_b32 s76, s49, 8\n" \
  "s_and_b32 s76, s76, 8192\n" \
  "s_xor_b32 s76, s76, s49\n" \
  "s_add_u32 s77, s76, 4096\n" \
  "s_add_u32 s78, s76, 16384\n" \
  "s_add_u32 s79, s76, 20480\n" \
  "s_add_u32 s49, s49, 524288\n" \
  "s_add_u32 s94, s94, 1\n" \
  "s_waitcnt vmcnt(0)\n" \
  ".LO_loop_%=:\n" \
  "s_waitcnt vmcnt(19)\n" \
  "v_cmp_ne_u32 vcc, %14, v189\n" \
  "s_nop 1\n" \
  "v_cndmask_b32 v188, v188, 0, vcc\n" \
  "v_cmp_ge_i32 vcc, s94, v188\n" \
  "s_mov_b32 s54, 0\n" \
  "s_cbranch_vccnz .LO_pl_%=\n" \
  ".LO_pl_b_%=:\n" \
  "s_waitcnt vmcnt(18)\n" \
  "v_mfma_f32_16x16x32_bf16 v[64:67], a[0:3], v[0:3], 0\n" \
  "buffer_load_dwordx2 v[188:189], v193, s[40:43], 0 offen sc1\n" \
  "v_mfma_f32_16x16x32_bf16 v[68:71], a[4:7], v[0:3], 0\n" \
  "s_waitcnt lgkmcnt(3)\n" \
  "v_mfma_f32_16x16x32_bf16 v[72:75], a[8:11], v[0:3], 0\n" \
  "ds_write_b128 v191, v[88:91] offset:6144\n" \
  "s_waitcnt lgkmcnt(3)\n" \
  "v_mfma_f32_16x16x32_bf16 v[76:79], a[12:15], v[0:3], 0\n" \
  "s_waitcnt lgkmcnt(2)\n" \
  "v_mfma_f32_16x16x32_bf16 v[80:83], a[16:19], v[0:3], 0\n" \
  "s_waitcnt lgkmcnt(1)\n" \
  "v_mfma_f32_16x16x32_bf16 v[84:87], a[20:23], v[0:3], 0\n" \
  "buffer_load_dwordx4 v[0:3], v190, s[40:43], s76 offen sc1\n" \
  "s_waitcnt vmcnt(19)\n" \
  "v_mfma_f32_16x16x32_bf16 v[64:67], a[24:27], v[8:11], v[64:67]\n" \
  "v_mfma_f32_16x16x32_bf16 v[68:71], a[28:31], v[8:11], v[68:71]\n" \
  "v_mfma_f32_16x16x32_bf16 v[72:75], a[32:35], v[8:11], v[72:75]\n" \
  "ds_write_b128 v191, v[92:95] offset:7168\n" \
  "v_mfma_f32_16x16x32_bf16 v[76:79], a[36:39], v[8:11], v[76:79]\n" \
  "v_mfma_f32_16x16x32_bf16 v[80:83], a[40:43], v[8:11], v[80:83]\n" \
  "v_mfma_f32_16x16x32_bf16 v[84:87], a[44:47], v[8:11], v[84:87]\n" \
  "buffer_load_dwordx4 v[8:11], v190, s[40:43], s76 offen offset:2048 sc1\n" \
  "s_waitcnt vmcnt(19)\n" \
  "v_mfma_f32_16x16x32_bf16 v[64:67], a[48:51], v[16:19], v[64:67]\n" \
  "v_mfma_f32_16x16x32_bf16 v[68:71], a[52:55], v[16:19], v[68:71]\n" \
  "v_mfma_f32_16x16x32_bf16 v[72:75], a[56:59], v[16:19], v[72:75]\n" \
  "ds_write_b128 v191, v[96:99] offset:8192\n" \
  "v_mfma_f32_16x16x32_bf16 v[76:79], a[60:63], v[16:19], v[76:79]\n" \
  "v_mfma_f32_16x16x32_bf16 v[80:83], a[64:67], v[16:19], v[80:83]\n" \
  "v_mfma_f32_16x16x32_bf16 v[84:87], a[68:71], v[16:19], v[84:87]\n" \
  "buffer_load_dwordx4 v[16:19], v190, s[40:43], s77 offen sc1\n" \
  "s_waitcnt vmcnt(19)\n" \
  "v_mfma_f32_16x16x32_bf16 v[64:67], a[72:75], v[24:27], v[64:67]\n" \
  "v_mfma_f32_16x16x32_bf16 v[68:71], a[76:79], v[24:27], v[68:71]\n" \
  "v_mfma_f32_16x16x32_bf16 v[72:75], a[80:83], v[24:27], v[72:75]\n" \
  "ds_write_b128 v191, v[100:103] offset:9216\n" \
  "v_mfma_f32_16x16x32_bf16 v[76:79], a[84:87], v[24:27], v[76:79]\n" \
  "v_mfma_f32_16x16x32_bf16 v[80:83], a[88:91], v[24:27], v[80:83]\n" \
  "v_mfma_f32_16x16x32_bf16 v[84:87], a[92:95], v[24:27], v[84:87]\n" \
  "buffer_load_dwordx4 v[24:27], v190, s[40:43], s77 offen offset:2048 sc1\n" \
  "s_waitcnt vmcnt(19)\n" \
  "v_mfma_f32_16x16x32_bf16 v[64:67], a[96:99], v[32:35], v[64:67]\n" \
  "v_mfma_f32_16x16x32_bf16 v[68:71], a[100:103], v[32:35], v[68:71]\n" \
  "v_mfma_f32_16x16x32_bf16 v[72:75], a[104:107], v[32:35], v[72:75]\n" \
  "ds_write_b128 v191, v[104:107] offset:10240\n" \
  "v_mfma_f32_16x16x32_bf16 v[76:79], a[108:111], v[32:35], v[76:79]\n" \
  "v_mfma_f32_16x16x32_bf16 v[80:83], a[112:115], v[32:35], v[80:83]\n" \
  "v_mfma_f32_16x16x32_bf16 v[84:87], a[116:119], v[32:35], v[84:87]\n" \
  "buffer_load_dwordx4 v[32:35], v190, s[40:43], s78 offen sc1\n" \
  "s_waitcnt vmcnt(19)\n" \
  "v_mfma_f32_16x16x32_bf16 v[64:67], a[120:123], v[40:43], v[64:67]\n" \
  "v_mfma_f32_16x16x32_bf16 v[68:71], a[124:127], v[40:43], v[68:71]\n" \
  "v_mfma_f32_16x16x32_bf16 v[72:75], a[128:131], v[40:43], v[72:75]\n" \
  "ds_write_b128 v191, v[108:111] offset:11264\n" \
  "v_mfma_f32_16x16x32_bf16 v[76:79], a[132:135], v[40:43], v[76:79]\n" \
  "v_xor_b32 v191, 65536, v191\n" \
  "v_mfma_f32_16x16x32_bf16 v[80:83], a[136:139], v[40:43], v[80:83]\n" \
  "v_mfma_f32_16x16x32_bf16 v[84:87], a[140:143], v[40:43], v[84:87]\n" \
  "buffer_load_dwordx4 v[40:43], v190, s[40:43], s78 offen offset:2048 sc1\n" \
  "s_waitcnt vmcnt(19)\n" \
  "v_mfma_f32_16x16x32_bf16 v[64:67], a[144:147], v[48:51], v[64:67]\n" \
  "v_mfma_f32_16x16x32_bf16 v[68:71], a[148:151], v[48:51], v[68:71]\n" \
  "v_mfma_f32_16x16x32_bf16 v[72:75], a[152:155], v[48:51], v[72:75]\n" \
  "v_mfma_f32_16x16x32_bf16 v[76:79], a[156:159], v[48:51], v[76:79]\n" \
  "s_waitcnt lgkmcnt(0)\n" \
  "s_waitcnt vmcnt(8)\n" \
  "s_barrier\n" \
  "v_mfma_f32_16x16x32_bf16 v[80:83], a[160:163], v[48:51], v[80:83]\n" \
  "ds_read_b128 v[120:123], v192\n" \
  "v_mfma_f32_16x16x32_bf16 v[84:87], a[164:167], v[48:51], v[84:87]\n" \
  "buffer_load_dwordx4 v[48:51], v190, s[40:43], s79 offen sc1\n" \
  "v_mfma_f32_16x16x32_bf16 v[64:67], a[168:171], v[56:59], v[64:67]\n" \
  "ds_read_b128 v[112:115], v192 offset:12288\n" \
  "v_mfma_f32_16x16x32_bf16 v[68:71], a[172:175], v[56:59], v[68:71]\n" \
  "s_add_i32 s60, s48, -1\n" \
  "v_mov_b32 v186, s60\n" \
  "s_mov_b64 exec, 1\n" \
  "buffer_store_dwordx2 v[186:187], off, s[40:43], %18 sc1\n" \
  "s_mov_b64 exec, -1\n" \
  "v_mfma_f32_16x16x32_bf16 v[72:75], a[176:179], v[56:59], v[72:75]\n" \
  "ds_read_b128 v[116:119], v192 offset:24576\n" \
  "v_mfma_f32_16x16x32_bf16 v[76:79], a[180:183], v[56:59], v[76:79]\n" \
  "ds_read_b128 v[124:127], v192 offset:36864\n" \
  "v_mfma_f32_16x16x32_bf16 v[80:83], a[184:187], v[56:59], v[80:83]\n" \
  "ds_read_b128 v[128:131], v192 offset:1024\n" \
  "v_mfma_f32_16x16x32_bf16 v[84:87], a[188:191], v[56:59], v[84:87]\n" \
  "buffer_load_dwordx4 v[56:59], v190, s[40:43], s79 offen offset:2048 sc1\n" \
  "v_mfma_f32_16x16x32_bf16 v[88:91], a[0:3], v[4:7], 0\n" \
  "ds_read_b128 v[132:135], v192 offset:13312\n" \
  "v_mfma_f32_16x16x32_bf16 v[92:95], a[4:7], v[4:7], 0\n" \
  "ds_read_b128 v[136:139], v192 offset:25600\n" \
  "v_mfma_f32_16x16x32_bf16 v[96:99], a[8:11], v[4:7], 0\n" \
  "ds_write_b128 v191, v[64:67]\n" \
  "v_mfma_f32_16x16x32_bf16 v[100:103], a[12:15], v[4:7], 0\n" \
  "ds_read_b128 v[140:143], v192 offset:37888\n" \
  "v_mfma_f32_16x16x32_bf16 v[104:107], a[16:19], v[4:7], 0\n" \
  "ds_read_b128 v[144:147], v192 offset:2048\n" \
  "v_mfma_f32_16x16x32_bf16 v[108:111], a[20:23], v[4:7], 0\n" \
  "buffer_load_dwordx4 v[4:7], v190, s[40:43], s76 offen offset:1024 sc1\n" \
  "v_mfma_f32_16x16x32_bf16 v[88:91], a[24:27], v[12:15], v[88:91]\n" \
  "ds_read_b128 v[148:151], v192 offset:14336\n" \
  "v_mfma_f32_16x16x32_bf16 v[92:95], a[28:31], v[12:15], v[92:95]\n" \
  "ds_read_b128 v[152:155], v192 offset:26624\n" \
  "v_mfma_f32_16x16x32_bf16 v[96:99], a[32:35], v[12:15], v[96:99]\n" \
  "ds_write_b128 v191, v[68:71] offset:1024\n" \
  "v_mfma_f32_16x16x32_bf16 v[100:103], a[36:39], v[12:15], v[100:103]\n" \
  "ds_read_b128 v[156:159], v192 offset:38912\n" \
  "v_mfma_f32_16x16x32_bf16 v[104:107], a[40:43], v[12:15], v[104:107]\n" \
  "s_waitcnt lgkmcnt(12)\n" \
  "v_add_f32 v120, v120, v112\n" \
  "v_add_f32 v121, v121, v113\n" \
  "v_mfma_f32_16x16x32_bf16 v[108:111], a[44:47], v[12:15], v[108:111]\n" \
  "buffer_load_dwordx4 v[12:15], v190, s[40:43], s76 offen offset:3072 sc1\n" \
  "v_mfma_f32_16x16x32_bf16 v[88:91], a[48:51], v[20:23], v[88:91]\n" \
  "v_add_f32 v122, v122, v114\n" \
  "v_add_f32 v123, v123, v115\n" \
  "v_mfma_f32_16x16x32_bf16 v[92:95], a[52:55], v[20:23], v[92:95]\n" \
  "s_waitcnt lgkmcnt(10)\n" \
  "v_add_f32 v116, v116, v124\n" \
  "v_add_f32 v117, v117, v125\n" \
  "v_mfma_f32_16x16x32_bf16 v[96:99], a[56:59], v[20:23], v[96:99]\n" \
  "ds_write_b128 v191, v[72:75] offset:2048\n" \
  "v_mfma_f32_16x16x32_bf16 v[100:103], a[60:63], v[20:23], v[100:103]\n" \
  "v_add_f32 v118, v118, v126\n" \
  "v_add_f32 v119, v119, v127\n" \
  "v_mfma_f32_16x16x32_bf16 v[104:107], a[64:67], v[20:23], v[104:107]\n" \
  "v_add_f32 v120, v120, v116\n" \
  "v_add_f32 v121, v121, v117\n" \
  "v_mfma_f32_16x16x32_bf16 v[108:111], a[68:71], v[20:23], v[108:111]\n" \
  "buffer_load_dwordx4 v[20:23], v190, s[40:43], s77 offen offset:1024 sc1\n" \
  "v_mfma_f32_16x16x32_bf16 v[88:91], a[72:75], v[28:31], v[88:91]\n" \
  "v_add_f32 v122, v122, v118\n" \
  "v_add_f32 v123, v123, v119\n" \
  "v_mfma_f32_16x16x32_bf16 v[92:95], a[76:79], v[28:31], v[92:95]\n" \
  "v_cvt_pk_bf16_f32 v112, v120, v121\n" \
  "v_cvt_pk_bf16_f32 v113, v122, v123\n" \
  "v_mfma_f32_16x16x32_bf16 v[96:99], a[80:83], v[28:31], v[96:99]\n" \
  "ds_write_b128 v191, v[76:79] offset:3072\n" \
  "v_mfma_f32_16x16x32_bf16 v[100:103], a[84:87], v[28:31], v[100:103]\n" \
  "s_waitcnt lgkmcnt(10)\n" \
  "v_add_f32 v128, v128, v132\n" \
  "v_add_f32 v129, v129, v133\n" \
  "v_mfma_f32_16x16x32_bf16 v[104:107], a[88:91], v[28:31], v[104:107]\n" \
  "v_add_f32 v130, v130, v134\n" \
  "v_add_f32 v131, v131, v135\n" \
  "v_mfma_f32_16x16x32_bf16 v[108:111], a[92:95], v[28:31], v[108:111]\n" \
  "buffer_load_dwordx4 v[28:31], v190, s[40:43], s77 offen offset:3072 sc1\n" \
  "v_mfma_f32_16x16x32_bf16 v[88:91], a[96:99], v[36:39], v[88:91]\n" \
  "s_waitcnt lgkmcnt(7)\n" \
  "v_add_f32 v136, v136, v140\n" \
  "v_add_f32 v137, v137, v141\n" \
  "v_mfma_f32_16x16x32_bf16 v[92:95], a[100:103], v[36:39], v[92:95]\n" \
  "v_add_f32 v138, v138, v142\n" \
  "v_add_f32 v139, v139, v143\n" \
  "v_mfma_f32_16x16x32_bf16 v[96:99], a[104:107], v[36:39], v[96:99]\n" \
  "ds_write_b128 v191, v[80:83] offset:4096\n" \
  "v_mfma_f32_16x16x32_bf16 v[100:103], a[108:111], v[36:39], v[100:103]\n" \
  "v_add_f32 v128, v128, v136\n" \
  "v_add_f32 v129, v129, v137\n" \
  "v_mfma_f32_16x16x32_bf16 v[104:107], a[112:115], v[36:39], v[104:107]\n" \
  "v_add_f32 v130, v130, v138\n" \
  "v_add_f32 v131, v131, v139\n" \
  "v_mfma_f32_16x16x32_bf16 v[108:111], a[116:119], v[36:39], v[108:111]\n" \
  "buffer_load_dwordx4 v[36:39], v190, s[40:43], s78 offen offset:1024 sc1\n" \
  "v_mfma_f32_16x16x32_bf16 v[88:91], a[120:123], v[44:47], v[88:91]\n" \
  "v_cvt_pk_bf16_f32 v114, v128, v129\n" \
  "v_cvt_pk_bf16_f32 v115, v130, v131\n" \
  "v_mfma_f32_16x16x32_bf16 v[92:95], a[124:127], v[44:47], v[92:95]\n" \
  "buffer_store_dwordx4 v[112:115], v190, s[40:43], s53 offen sc1\n" \
  "v_mfma_f32_16x16x32_bf16 v[96:99], a[128:131], v[44:47], v[96:99]\n" \
  "ds_write_b128 v191, v[84:87] offset:5120\n" \
  "v_mfma_f32_16x16x32_bf16 v[100:103], a[132:135], v[44:47], v[100:103]\n" \
  "s_waitcnt lgkmcnt(7)\n" \
  "v_add_f32 v144, v144, v148\n" \
  "v_add_f32 v145, v145, v149\n" \
  "v_mfma_f32_16x16x32_bf16 v[104:107], a[136:139], v[44:47], v[104:107]\n" \
  "v_add_f32 v146, v146, v150\n" \
  "v_add_f32 v147, v147, v151\n" \
  "v_mfma_f32_16x16x32_bf16 v[108:111], a[140:143], v[44:47], v[108:111]\n" \
  "buffer_load_dwordx4 v[44:47], v190, s[40:43], s78 offen offset:3072 sc1\n" \
  "v_mfma_f32_16x16x32_bf16 v[88:91], a[144:147], v[52:55], v[88:91]\n" \
  "s_waitcnt lgkmcnt(4)\n" \
  "v_add_f32 v152, v152, v156\n" \
  "v_add_f32 v153, v153, v157\n" \
  "v_mfma_f32_16x16x32_bf16 v[92:95], a[148:151], v[52:55], v[92:95]\n" \
  "v_add_f32 v154, v154, v158\n" \
  "v_add_f32 v155, v155, v159\n" \
  "v_mfma_f32_16x16x32_bf16 v[96:99], a[152:155], v[52:55], v[96:99]\n" \
  "v_mfma_f32_16x16x32_bf16 v[100:103], a[156:159], v[52:55], v[100:103]\n" \
  "v_add_f32 v144, v144, v152\n" \
  "v_add_f32 v145, v145, v153\n" \
  "v_mfma_f32_16x16x32_bf16 v[104:107], a[160:163], v[52:55], v[104:107]\n" \
  "v_add_f32 v146, v146, v154\n" \
  "v_add_f32 v147, v147, v155\n" \
  "v_mfma_f32_16x16x32_bf16 v[108:111], a[164:167], v[52:55], v[108:111]\n" \
  "buffer_load_dwordx4 v[52:55], v190, s[40:43], s79 offen offset:1024 sc1\n" \
  "s_waitcnt vmcnt(18)\n" \
  "v_mfma_f32_16x16x32_bf16 v[88:91], a[168:171], v[60:63], v[88:91]\n" \
  "v_cvt_pk_bf16_f32 v116, v144, v145\n" \
  "v_cvt_pk_bf16_f32 v117, v146, v147\n" \
  "v_mfma_f32_16x16x32_bf16 v[92:95], a[172:175], v[60:63], v[92:95]\n" \
  "buffer_store_dwordx2 v[116:117], v198, s[40:43], s53 offen offset:1024 sc1\n" \
  "v_mfma_f32_16x16x32_bf16 v[96:99], a[176:179], v[60:63], v[96:99]\n" \
  "s_lshr_b32 s53, s50, 8\n" \
  "s_and_b32 s53, s53, 8192\n" \
  "s_xor_b32 s53, s53, s50\n" \
  "s_add_u32 s50, s50, 393216\n" \
  "v_xor_b32 v192, 65536, v192\n" \
  "v_mfma_f32_16x16x32_bf16 v[100:103], a[180:183], v[60:63], v[100:103]\n" \
  "v_mfma_f32_16x16x32_bf16 v[104:107], a[184:187], v[60:63], v[104:107]\n" \
  "v_mfma_f32_16x16x32_bf16 v[108:111], a[188:191], v[60:63], v[108:111]\n" \
  "buffer_load_dwordx4 v[60:63], v190, s[40:43], s79 offen offset:3072 sc1\n" \
  "s_cmp_lg_u32 s54, 0\n" \
  "s_cbranch_scc1 .LO_ft_%=\n" \
  ".LO_ft_b_%=:\n" \
  "s_lshr_b32 s76, s49, 8\n" \
  "s_and_b32 s76, s76, 8192\n" \
  "s_xor_b32 s76, s76, s49\n" \
  "s_add_u32 s77, s76, 4096\n" \
  "s_add_u32 s78, s76, 16384\n" \
  "s_add_u32 s79, s76, 20480\n" \
  "s_add_u32 s49, s49, 524288\n" \
  "s_add_u32 s48, s48, 1\n" \
  "s_add_u32 s94, s48, 1\n" \
  "s_cmp_lt_u32 s94, s55\n" \
  "s_cselect_b32 s94, s94, -1\n" \
  "s_cmp_lt_u32 s48, s55\n" \
  "s_cbranch_scc1 .LO_loop_%=\n" \
  ".LO_fl_%=:\n" \
  "s_nop 7\n" \
  "ds_write_b128 v191, v[88:91] offset:6144\n" \
  "ds_write_b128 v191, v[92:95] offset:7168\n" \
  "ds_write_b128 v191, v[96:99] offset:8192\n" \
  "ds_write_b128 v191, v[100:103] offset:9216\n" \
  "ds_write_b128 v191, v[104:107] offset:10240\n" \
  "ds_write_b128 v191, v[108:111] offset:11264\n" \
  "s_waitcnt lgkmcnt(0)\n" \
  "s_barrier\n" \
  "ds_read_b128 v[120:123], v192\n" \
  "ds_read_b128 v[112:115], v192 offset:12288\n" \
  "ds_read_b128 v[116:119], v192 offset:24576\n" \
  "ds_read_b128 v[124:127], v192 offset:36864\n" \
  "ds_read_b128 v[128:131], v192 offset:1024\n" \
  "ds_read_b128 v[132:135], v192 offset:13312\n" \
  "ds_read_b128 v[136:139], v192 offset:25600\n" \
  "ds_read_b128 v[140:143], v192 offset:37888\n" \
  "ds_read_b128 v[144:147], v192 offset:2048\n" \
  "ds_read_b128 v[148:151], v192 offset:14336\n" \
  "ds_read_b128 v[152:155], v192 offset:26624\n" \
  "ds_read_b128 v[156:159], v192 offset:38912\n" \
  "s_waitcnt lgkmcnt(10)\n" \
  "v_add_f32 v120, v120, v112\n" \
  "v_add_f32 v121, v121, v113\n" \
  "v_add_f32 v122, v122, v114\n" \
  "v_add_f32 v123, v123, v115\n" \
  "s_waitcnt lgkmcnt(8)\n" \
  "v_add_f32 v116, v116, v124\n" \
  "v_add_f32 v117, v117, v125\n" \
  "v_add_f32 v118, v118, v126\n" \
  "v_add_f32 v119, v119, v127\n" \
  "v_add_f32 v120, v120, v116\n" \
  "v_add_f32 v121, v121, v117\n" \
  "v_add_f32 v122, v122, v118\n" \
  "v_add_f32 v123, v123, v119\n" \
  "v_cvt_pk_bf16_f32 v112, v120, v121\n" \
  "v_cvt_pk_bf16_f32 v113, v122, v123\n" \
  "s_waitcnt lgkmcnt(6)\n" \
  "v_add_f32 v128, v128, v132\n" \
  "v_add_f32 v129, v129, v133\n" \
  "v_add_f32 v130, v130, v134\n" \
  "v_add_f32 v131, v131, v135\n" \
  "s_waitcnt lgkmcnt(4)\n" \
  "v_add_f32 v136, v136, v140\n" \
  "v_add_f32 v137, v137, v141\n" \
  "v_add_f32 v138, v138, v142\n" \
  "v_add_f32 v139, v139, v143\n" \
  "v_add_f32 v128, v128, v136\n" \
  "v_add_f32 v129, v129, v137\n" \
  "v_add_f32 v130, v130, v138\n" \
  "v_add_f32 v131, v131, v139\n" \
  "v_cvt_pk_bf16_f32 v114, v128, v129\n" \
  "v_cvt_pk_bf16_f32 v115, v130, v131\n" \
  "buffer_store_dwordx4 v[112:115], v190, s[40:43], s53 offen sc1\n" \
  "s_waitcnt lgkmcnt(2)\n" \
  "v_add_f32 v144, v144, v148\n" \
  "v_add_f32 v145, v145, v149\n" \
  "v_add_f32 v146, v146, v150\n" \
  "v_add_f32 v147, v147, v151\n" \
  "s_waitcnt lgkmcnt(0)\n" \
  "v_add_f32 v152, v152, v156\n" \
  "v_add_f32 v153, v153, v157\n" \
  "v_add_f32 v154, v154, v158\n" \
  "v_add_f32 v155, v155, v159\n" \
  "v_add_f32 v144, v144, v152\n" \
  "v_add_f32 v145, v145, v153\n" \
  "v_add_f32 v146, v146, v154\n" \
  "v_add_f32 v147, v147, v155\n" \
  "v_cvt_pk_bf16_f32 v116, v144, v145\n" \
  "v_cvt_pk_bf16_f32 v117, v146, v147\n" \
  "buffer_store_dwordx2 v[116:117], v198, s[40:43], s53 offen offset:1024 sc1\n" \
  "s_waitcnt vmcnt(0)\n" \
  "s_barrier\n" \
  "s_add_i32 s60, s48, 0\n" \
  "v_mov_b32 v186, s60\n" \
  "s_mov_b64 exec, 1\n" \
  "buffer_store_dwordx2 v[186:187], off, s[40:43], %18 sc1\n" \
  "s_mov_b64 exec, -1\n" \
  "s_cmp_lg_u32 s56, 0\n" \
  "s_cbranch_scc1 .LO_fl_b_%=\n" \
  "s_branch .LO_end_%=\n" \
  ".LO_pl_%=:\n" \
  "s_add_u32 s60, s48, 2\n" \
  "s_cmp_lg_u32 s60, s55\n" \
  "s_cbranch_scc1 .LO_ft_%=\n" \
  "s_mov_b32 s54, s76\n" \
  "s_mov_b32 s76, 2130706432\n" \
  "s_mov_b32 s77, 2130706432\n" \
  "s_mov_b32 s78, 2130706432\n" \
  "s_mov_b32 s79, 2130706432\n" \
  "s_branch .LO_pl_b_%=\n" \
  ".LO_ft_%=:\n" \
  "s_mov_b32 s69, 0\n" \
  ".LO_p1_%=:\n" \
  "buffer_load_dwordx2 v[188:189], v193, s[40:43], 0 offen sc1\n" \
  "s_waitcnt vmcnt(0)\n" \
  "v_cmp_ne_u32 vcc, %14, v189\n" \
  "s_nop 1\n" \
  "v_cndmask_b32 v188, v188, 0, vcc\n" \
  "v_cmp_ge_i32 vcc, s94, v188\n" \
  "s_cbranch_vccz .LO_p1_x_%=\n" \
  "s_sleep 2\n" \
  "s_add_u32 s69, s69, 1\n" \
  "s_and_b32 s63, s69, 1023\n" \
  "s_cbranch_scc1 .LO_p1_%=\n" \
  "buffer_load_dwordx2 v[188:189], off, s[40:43], %19 offset:128 sc1\n" \
  "s_waitcnt vmcnt(0)\n" \
  "v_readfirstlane_b32 s63, v189\n" \
  "s_cmp_eq_u32 s63, %14\n" \
  "s_cbranch_scc1 .LO_p1_x_%=\n" \
  "s_cmp_le_u32 s69, 2097152\n" \
  "s_cbranch_scc1 .LO_p1_%=\n" \
  "v_mov_b32 v188, 1\n" \
  "v_mov_b32 v189, %14\n" \
  "buffer_store_dwordx2 v[188:189], off, s[40:43], %19 offset:128 sc1\n" \
  "s_waitcnt vmcnt(0)\n" \
  ".LO_p1_x_%=:\n" \
  "s_cmp_eq_u32 s54, 0\n" \
  "s_cbranch_scc1 .LO_pl_b_%=\n" \
  "s_mov_b32 s76, s54\n" \
  "s_add_u32 s77, s76, 4096\n" \
  "s_add_u32 s78, s76, 16384\n" \
  "s_add_u32 s79, s76, 20480\n" \
  "buffer_load_dwordx4 v[0:3], v190, s[40:43], s76 offen sc1\n" \
  "buffer_load_dwordx4 v[8:11], v190, s[40:43], s76 offen offset:2048 sc1\n" \
  "buffer_load_dwordx4 v[16:19], v190, s[40:43], s77 offen sc1\n" \
  "buffer_load_dwordx4 v[24:27], v190, s[40:43], s77 offen offset:2048 sc1\n" \
  "buffer_load_dwordx4 v[32:35], v190, s[40:43], s78 offen sc1\n" \
  "buffer_load_dwordx4 v[40:43], v190, s[40:43], s78 offen offset:2048 sc1\n" \
  "buffer_load_dwordx4 v[48:51], v190, s[40:43], s79 offen sc1\n" \
  "buffer_load_dwordx4 v[56:59], v190, s[40:43], s79 offen offset:2048 sc1\n" \
  "buffer_load_dwordx4 v[4:7], v190, s[40:43], s76 offen offset:1024 sc1\n" \
  "buffer_load_dwordx4 v[12:15], v190, s[40:43], s76 offen offset:3072 sc1\n" \
  "buffer_load_dwordx4 v[20:23], v190, s[40:43], s77 offen offset:1024 sc1\n" \
  "buffer_load_dwordx4 v[28:31], v190, s[40:43], s77 offen offset:3072 sc1\n" \
  "buffer_load_dwordx4 v[36:39], v190, s[40:43], s78 offen offset:1024 sc1\n" \
  "buffer_load_dwordx4 v[44:47], v190, s[40:43], s78 offen offset:3072 sc1\n" \
  "buffer_load_dwordx4 v[52:55], v190, s[40:43], s79 offen offset:1024 sc1\n" \
  "buffer_load_dwordx4 v[60:63], v190, s[40:43], s79 offen offset:3072 sc1\n" \
  "s_waitcnt vmcnt(8)\n" \
  "s_branch .LO_ft_b_%=\n" \
  ".LO_end_%=:\n"
#define O_ASM_VOPS "+{v190}"(oi.l16), "+{v191}"(oi.rw), "+{v192}"(oi.rr), "+{v193}"(oi.fo), "+{v194}"(oi.wv0), "+{v195}"(oi.wv1), "+{v196}"(oi.wv2), "+{v197}"(oi.tro), "+{v198}"(oi.l8), "+{v199}"(oi.rw0), "+{v200}"(oi.rr0)
#define O_ASM_SOPS "s"(oi.ws), "s"(oi.Wo), "s"(oi.w), "s"(oi.tag), "s"(oi.o_w), "s"(oi.o_o), "s"(oi.o_a), "s"(oi.f_a), "s"(oi.o_ctl), "s"(oi.ns)
#define O_ASM_CLOB "v0", "v1", "v2", "v3", "v4", "v5", "v6", "v7", "v8", "v9", "v10", "v11", "v12", "v13", "v14", "v15", "v16", "v17", "v18", "v19", "v20", "v21", "v22", "v23", "v24", "v25", "v26", "v27", "v28", "v29", "v30", "v31", "v32", "v33", "v34", "v35", "v36", "v37", "v38", "v39", "v40", "v41", "v42", "v43", "v44", "v45", "v46", "v47", "v48", "v49", "v50", "v51", "v52", "v53", "v54", "v55", "v56", "v57", "v58", "v59", "v60", "v61", "v62", "v63", "v64", "v65", "v66", "v67", "v68", "v69", "v70", "v71", "v72", "v73", "v74", "v75", "v76", "v77", "v78", "v79", "v80", "v81", "v82", "v83", "v84", "v85", "v86", "v87", "v88", "v89", "v90", "v91", "v92", "v93", "v94", "v95", "v96", "v97", "v98", "v99", "v100", "v101", "v102", "v103", "v104", "v105", "v106", "v107", "v108", "v109", "v110", "v111", "v112", "v113", "v114", "v115", "v116", "v117", "v118", "v119", "v120", "v121", "v122", "v123", "v124", "v125", "v126", "v127", "v128", "v129", "v130", "v131", "v132", "v133", "v134", "v135", "v136", "v137", "v138", "v139", "v140", "v141", "v142", "v143", "v144", "v145", "v146", "v147", "v148", "v149", "v150", "v151", "v152", "v153", "v154", "v155", "v156", "v157", "v158", "v159", "v160", "v161", "v162", "v163", "v164", "v165", "v166", "v167", "v168", "v169", "v170", "v171", "v172", "v173", "v174", "v175", "v176", "v177", "v178", "v179", "v180", "v181", "v182", "v183", "v184", "v185", "v186", "v187", "v188", "v189", "a0", "a1", "a2", "a3", "a4", "a5", "a6", "a7", "a8", "a9", "a10", "a11", "a12", "a13", "a14", "a15", "a16", "a17", "a18", "a19", "a20", "a21", "a22", "a23", "a24", "a25", "a26", "a27", "a28", "a29", "a30", "a31", "a32", "a33", "a34", "a35", "a36", "a37", "a38", "a39", "a40", "a41", "a42", "a43", "a44", "a45", "a46", "a47", "a48", "a49", "a50", "a51", "a52", "a53", "a54", "a55", "a56", "a57", "a58", "a59", "a60", "a61", "a62", "a63", "a64", "a65", "a66", "a67", "a68", "a69", "a70", "a71", "a72", "a73", "a74", "a75", "a76", "a77", "a78", "a79", "a80", "a81", "a82", "a83", "a84", "a85", "a86", "a87", "a88", "a89", "a90", "a91", "a92", "a93", "a94", "a95", "a96", "a97", "a98", "a99", "a100", "a101", "a102", "a103", "a104", "a105", "a106", "a107", "a108", "a109", "a110", "a111", "a112", "a113", "a114", "a115", "a116", "a117", "a118", "a119", "a120", "a121", "a122", "a123", "a124", "a125", "a126", "a127", "a128", "a129", "a130", "a131", "a132", "a133", "a134", "a135", "a136", "a137", "a138", "a139", "a140", "a141", "a142", "a143", "a144", "a145", "a146", "a147", "a148", "a149", "a150", "a151", "a152", "a153", "a154", "a155", "a156", "a157", "a158", "a159", "a160", "a161", "a162", "a163", "a164", "a165", "a166", "a167", "a168", "a169", "a170", "a171", "a172", "a173", "a174", "a175", "a176", "a177", "a178", "a179", "a180", "a181", "a182", "a183", "a184", "a185", "a186", "a187", "a188", "a189", "a190", "a191", "s40", "s41", "s42", "s43", "s44", "s45", "s46", "s47", "s48", "s49", "s50", "s51", "s52", "s53", "s54", "s55", "s56", "s57", "s58", "s59", "s60", "s61", "s62", "s63", "s64", "s65", "s66", "s67", "s68", "s69", "s70", "s71", "s72", "s73", "s74", "s75", "s76", "s77", "s78", "s79", "s80", "s81", "s82", "s83", "s84", "s85", "s86", "s87", "s88", "s89", "s90", "s91", "s92", "s93", "s94", "s95", "s96", "s97", "s98", "s99", "vcc", "scc", "memory"
#define F_ASM_TEXT \
  "s_mov_b64 s[40:41], %31\n" \
  "s_and_b32 s41, s41, 0xffff\n" \
  "s_mov_b32 s42, 255852544\n" \
  "s_mov_b32 s43, 0x00020000\n" \
  "s_mov_b64 s[44:45], %32\n" \
  "s_and_b32 s45, s45, 0xffff\n" \
  "s_mov_b32 s46, 2359296\n" \
  "s_mov_b32 s47, 0x00020000\n" \
  "s_mov_b32 s48, 0\n" \
  "s_mov_b32 s58, 0\n" \
  "s_mov_b32 s59, 0\n" \
  "s_mov_b32 s49, %37\n" \
  "s_mov_b32 s50, %38\n" \
  "s_mov_b32 s51, %36\n" \
  "s_mov_b32 s56, %39\n" \
  "s_mov_b32 s57, %40\n" \
  "s_mov_b32 s64, %44\n" \
  "v_not_b32 v197, %33\n" \
  "v_mov_b32 v62, -1\n" \
  ".LF_pre_%=:\n" \
  "v_readfirstlane_b32 s60, v197\n" \
  "s_cmp_eq_u32 s60, %33\n" \
  "s_cbranch_scc1 .LF_fu_x_%=\n" \
  ".LF_fu_%=:\n" \
  "buffer_load_dwordx2 v[196:197], off, s[40:43], s64 sc1\n" \
  "buffer_load_dwordx2 v[62:63], off, s[40:43], s50 offset:256 sc1\n" \
  "s_memrealtime s[76:77]\n" \
  "s_waitcnt vmcnt(0) lgkmcnt(0)\n" \
  "v_readfirstlane_b32 s60, v197\n" \
  "s_cmp_eq_u32 s60, %33\n" \
  "s_cbranch_scc1 .LF_fu_x_%=\n" \
  "v_readfirstlane_b32 s60, v62\n" \
  "s_sub_u32 s60, s60, 58\n" \
  "s_sub_u32 s60, s60, s48\n" \
  "s_cmp_lt_u32 s48, s58\n" \
  "s_cselect_b32 s60, s60, -1\n" \
  "v_readfirstlane_b32 s61, v63\n" \
  "s_sub_u32 s61, s61, s76\n" \
  "s_cmp_lt_i32 s61, -60\n" \
  "s_cselect_b32 s60, s60, -1\n" \
  "s_cmp_lt_u32 s60, 8\n" \
  "s_cbranch_scc1 .LF_loop_%=\n" \
  "s_sleep 6\n" \
  "s_add_u32 s59, s59, 1\n" \
  "s_cmp_lt_u32 s59, 131072\n" \
  "s_cbranch_scc1 .LF_fu_%=\n" \
  "v_mov_b32 v32, 1\n" \
  "v_mov_b32 v33, %33\n" \
  "buffer_store_dwordx2 v[32:33], off, s[40:43], %41 offset:128 sc1\n" \
  "s_waitcnt vmcnt(0)\n" \
  ".LF_fu_x_%=:\n" \
  "v_readfirstlane_b32 s60, v212\n" \
  "s_cmp_eq_u32 s60, 2130706432\n" \
  "s_cbranch_scc1 .LF_fl_%=\n" \
  "s_add_u32 s61, s58, 58\n" \
  "s_and_b32 s61, s61, 15\n" \
  "s_cmp_ge_u32 s58, 6\n" \
  "s_cselect_b32 s60, 2048, 0\n" \
  "s_or_b32 s61, s61, s60\n" \
  "s_lshl_b32 s61, s61, 16\n" \
  "s_xor_b32 s61, s61, %34\n" \
  "s_lshl_b32 s62, s58, 17\n" \
  "s_add_u32 s62, s62, %35\n" \
  "s_xor_b32 s60, s61, 0\n" \
  "buffer_load_dwordx4 v[32:35], v202, s[40:43], s60 offen sc1\n" \
  "s_sleep 1\n" \
  "buffer_load_dwordx4 v[64:67], v203, s[40:43], s62 offen sc1\n" \
  "s_sleep 1\n" \
  "buffer_load_dwordx4 v[68:71], v203, s[40:43], s62 offen offset:1024 sc1\n" \
  "s_sleep 1\n" \
  "s_xor_b32 s60, s61, 1024\n" \
  "buffer_load_dwordx4 v[36:39], v202, s[40:43], s60 offen sc1\n" \
  "s_sleep 1\n" \
  "buffer_load_dwordx4 v[72:75], v203, s[40:43], s62 offen offset:2048 sc1\n" \
  "s_sleep 1\n" \
  "buffer_load_dwordx4 v[76:79], v203, s[40:43], s62 offen offset:3072 sc1\n" \
  "s_sleep 1\n" \
  "s_xor_b32 s60, s61, 2048\n" \
  "buffer_load_dwordx4 v[40:43], v202, s[40:43], s60 offen sc1\n" \
  "s_sleep 1\n" \
  "buffer_load_dwordx4 v[80:83], v204, s[40:43], s62 offen sc1\n" \
  "s_sleep 1\n" \
  "buffer_load_dwordx4 v[84:87], v204, s[40:43], s62 offen offset:1024 sc1\n" \
  "s_sleep 1\n" \
  "s_xor_b32 s60, s61, 3072\n" \
  "buffer_load_dwordx4 v[44:47], v202, s[40:43], s60 offen sc1\n" \
  "s_sleep 1\n" \
  "buffer_load_dwordx4 v[88:91], v204, s[40:43], s62 offen offset:2048 sc1\n" \
  "s_sleep 1\n" \
  "buffer_load_dwordx4 v[92:95], v204, s[40:43], s62 offen offset:3072 sc1\n" \
  "s_sleep 1\n" \
  "s_xor_b32 s60, s61, 4096\n" \
  "buffer_load_dwordx4 v[48:51], v202, s[40:43], s60 offen sc1\n" \
  "s_sleep 1\n" \
  "buffer_load_dwordx4 v[96:99], v205, s[40:43], s62 offen sc1\n" \
  "s_sleep 1\n" \
  "buffer_load_dwordx4 v[100:103], v205, s[40:43], s62 offen offset:1024 sc1\n" \
  "s_sleep 1\n" \
  "s_xor_b32 s60, s61, 5120\n" \
  "buffer_load_dwordx4 v[52:55], v202, s[40:43], s60 offen sc1\n" \
  "s_sleep 1\n" \
  "buffer_load_dwordx4 v[104:107], v205, s[40:43], s62 offen offset:2048 sc1\n" \
  "s_sleep 1\n" \
  "buffer_load_dwordx4 v[108:111], v205, s[40:43], s62 offen offset:3072 sc1\n" \
  "s_sleep 1\n" \
  "s_xor_b32 s60, s61, 6144\n" \
  "buffer_load_dwordx4 v[56:59], v202, s[40:43], s60 offen sc1\n" \
  "s_sleep 1\n" \
  "buffer_load_dwordx4 v[112:115], v206, s[40:43], s62 offen sc1\n" \
  "s_sleep 1\n" \
  "buffer_load_dwordx4 v[116:119], v206, s[40:43], s62 offen offset:1024 sc1\n" \
  "s_sleep 1\n" \
  "s_xor_b32 s60, s61, 7168\n" \
  "buffer_load_dwordx4 v[60:63], v202, s[40:43], s60 offen sc1\n" \
  "s_sleep 1\n" \
  "buffer_load_dwordx4 v[120:123], v206, s[40:43], s62 offen offset:2048 sc1\n" \
  "s_sleep 1\n" \
  "buffer_load_dwordx4 v[124:127], v206, s[40:43], s62 offen offset:3072 sc1\n" \
  "s_sleep 1\n" \
  "s_add_u32 s64, s64, 128\n" \
  ".LF_fl_b_%=:\n" \
  "v_mov_b64 v[128:129], 0\n" \
  "v_mov_b64 v[130:131], 0\n" \
  "v_mov_b64 v[132:133], 0\n" \
  "v_mov_b64 v[134:135], 0\n" \
  "v_mov_b64 v[136:137], 0\n" \
  "v_mov_b64 v[138:139], 0\n" \
  "v_mov_b64 v[140:141], 0\n" \
  "v_mov_b64 v[142:143], 0\n" \
  "v_mov_b64 v[144:145], 0\n" \
  "v_mov_b64 v[146:147], 0\n" \
  "v_mov_b64 v[148:149], 0\n" \
  "v_mov_b64 v[150:151], 0\n" \
  "v_mov_b64 v[152:153], 0\n" \
  "v_mov_b64 v[154:155], 0\n" \
  "v_mov_b64 v[156:157], 0\n" \
  "v_mov_b64 v[158:159], 0\n" \
  "v_mov_b64 v[160:161], 0\n" \
  "v_mov_b64 v[162:163], 0\n" \
  "v_mov_b64 v[164:165], 0\n" \
  "v_mov_b64 v[166:167], 0\n" \
  "v_mov_b64 v[168:169], 0\n" \
  "v_mov_b64 v[170:171], 0\n" \
  "v_mov_b64 v[172:173], 0\n" \
  "v_mov_b64 v[174:175], 0\n" \
  "v_mov_b64 v[176:177], 0\n" \
  "v_mov_b64 v[178:179], 0\n" \
  "v_mov_b64 v[180:181], 0\n" \
  "v_mov_b64 v[182:183], 0\n" \
  "v_mov_b64 v[184:185], 0\n" \
  "v_mov_b64 v[186:187], 0\n" \
  "v_mov_b64 v[188:189], 0\n" \
  "v_mov_b64 v[190:191], 0\n" \
  "s_waitcnt vmcnt(0)\n" \
  "v_mov_b32 v0, v32\n" \
  "v_mov_b32 v1, v36\n" \
  "v_mov_b32 v2, v33\n" \
  "v_mov_b32 v3, v37\n" \
  "v_mov_b32 v4, v34\n" \
  "v_mov_b32 v5, v38\n" \
  "v_mov_b32 v6, v35\n" \
  "v_mov_b32 v7, v39\n" \
  "v_mov_b32 v8, v40\n" \
  "v_mov_b32 v9, v44\n" \
  "v_mov_b32 v10, v41\n" \
  "v_mov_b32 v11, v45\n" \
  "v_mov_b32 v12, v42\n" \
  "v_mov_b32 v13, v46\n" \
  "v_mov_b32 v14, v43\n" \
  "v_mov_b32 v15, v47\n" \
  "v_mov_b32 v16, v48\n" \
  "v_mov_b32 v17, v52\n" \
  "v_mov_b32 v18, v49\n" \
  "v_mov_b32 v19, v53\n" \
  "v_mov_b32 v20, v50\n" \
  "v_mov_b32 v21, v54\n" \
  "v_mov_b32 v22, v51\n" \
  "v_mov_b32 v23, v55\n" \
  "v_mov_b32 v24, v56\n" \
  "v_mov_b32 v25, v60\n" \
  "v_mov_b32 v26, v57\n" \
  "v_mov_b32 v27, v61\n" \
  "v_mov_b32 v28, v58\n" \
  "v_mov_b32 v29, v62\n" \
  "v_mov_b32 v30, v59\n" \
  "v_mov_b32 v31, v63\n" \
  "s_nop 1\n" \
  "v_smfmac_f32_16x16x64_bf16 v[128:131], v[64:67], v[0:7], v200\n" \
  "v_smfmac_f32_16x16x64_bf16 v[132:135], v[68:71], v[8:15], v200\n" \
  "v_smfmac_f32_16x16x64_bf16 v[136:139], v[72:75], v[16:23], v200\n" \
  "v_smfmac_f32_16x16x64_bf16 v[140:143], v[76:79], v[24:31], v200\n" \
  "v_smfmac_f32_16x16x64_bf16 v[144:147], v[80:83], v[0:7], v200\n" \
  "v_smfmac_f32_16x16x64_bf16 v[148:151], v[84:87], v[8:15], v200\n" \
  "v_smfmac_f32_16x16x64_bf16 v[152:155], v[88:91], v[16:23], v200\n" \
  "v_smfmac_f32_16x16x64_bf16 v[156:159], v[92:95], v[24:31], v200\n" \
  "v_smfmac_f32_16x16x64_bf16 v[160:163], v[96:99], v[0:7], v200\n" \
  "v_smfmac_f32_16x16x64_bf16 v[164:167], v[100:103], v[8:15], v200\n" \
  "v_smfmac_f32_16x16x64_bf16 v[168:171], v[104:107], v[16:23], v200\n" \
  "v_smfmac_f32_16x16x64_bf16 v[172:175], v[108:111], v[24:31], v200\n" \
  "v_smfmac_f32_16x16x64_bf16 v[176:179], v[112:115], v[0:7], v200\n" \
  "v_smfmac_f32_16x16x64_bf16 v[180:183], v[116:119], v[8:15], v200\n" \
  "v_smfmac_f32_16x16x64_bf16 v[184:187], v[120:123], v[16:23], v200\n" \
  "v_smfmac_f32_16x16x64_bf16 v[188:191], v[124:127], v[24:31], v200\n" \
  "s_add_u32 s58, s58, 1\n" \
  "buffer_load_dwordx2 v[62:63], off, s[40:43], s50 offset:256 sc1\n" \
  "buffer_load_dwordx2 v[196:197], off, s[40:43], s64 sc1\n" \
  "s_cmp_eq_u32 s58, 6\n" \
  "s_cbranch_scc1 .LF_ne_%=\n" \
  ".LF_ne_%=:\n" \
  "s_nop 7\n" \
  "v_cvt_pk_bf16_f32 v128, v128, v129\n" \
  "v_cvt_pk_bf16_f32 v129, v130, v131\n" \
  "v_cvt_pk_bf16_f32 v130, v132, v133\n" \
  "v_cvt_pk_bf16_f32 v131, v134, v135\n" \
  "v_cvt_pk_bf16_f32 v132, v136, v137\n" \
  "v_cvt_pk_bf16_f32 v133, v138, v139\n" \
  "v_cvt_pk_bf16_f32 v134, v140, v141\n" \
  "v_cvt_pk_bf16_f32 v135, v142, v143\n" \
  "v_cvt_pk_bf16_f32 v136, v144, v145\n" \
  "v_cvt_pk_bf16_f32 v137, v146, v147\n" \
  "v_cvt_pk_bf16_f32 v138, v148, v149\n" \
  "v_cvt_pk_bf16_f32 v139, v150, v151\n" \
  "v_cvt_pk_bf16_f32 v140, v152, v153\n" \
  "v_cvt_pk_bf16_f32 v141, v154, v155\n" \
  "v_cvt_pk_bf16_f32 v142, v156, v157\n" \
  "v_cvt_pk_bf16_f32 v143, v158, v159\n" \
  "v_cvt_pk_bf16_f32 v144, v160, v161\n" \
  "v_cvt_pk_bf16_f32 v145, v162, v163\n" \
  "v_cvt_pk_bf16_f32 v146, v164, v165\n" \
  "v_cvt_pk_bf16_f32 v147, v166, v167\n" \
  "v_cvt_pk_bf16_f32 v148, v168, v169\n" \
  "v_cvt_pk_bf16_f32 v149, v170, v171\n" \
  "v_cvt_pk_bf16_f32 v150, v172, v173\n" \
  "v_cvt_pk_bf16_f32 v151, v174, v175\n" \
  "v_cvt_pk_bf16_f32 v152, v176, v177\n" \
  "v_cvt_pk_bf16_f32 v153, v178, v179\n" \
  "v_cvt_pk_bf16_f32 v154, v180, v181\n" \
  "v_cvt_pk_bf16_f32 v155, v182, v183\n" \
  "v_cvt_pk_bf16_f32 v156, v184, v185\n" \
  "v_cvt_pk_bf16_f32 v157, v186, v187\n" \
  "v_cvt_pk_bf16_f32 v158, v188, v189\n" \
  "v_cvt_pk_bf16_f32 v159, v190, v191\n" \
  "s_cmp_eq_u32 s58, 1\n" \
  "s_cbranch_scc1 .LF_w1_%=\n" \
  "s_cmp_eq_u32 s58, 2\n" \
  "s_cbranch_scc1 .LF_w2_%=\n" \
  "s_cmp_eq_u32 s58, 3\n" \
  "s_cbranch_scc1 .LF_w3_%=\n" \
  "s_cmp_eq_u32 s58, 4\n" \
  "s_cbranch_scc1 .LF_w4_%=\n" \
  "s_cmp_eq_u32 s58, 5\n" \
  "s_cbranch_scc1 .LF_w5_%=\n" \
  "v_accvgpr_write_b32 a160, v128\n" \
  "v_accvgpr_write_b32 a161, v129\n" \
  "v_accvgpr_write_b32 a162, v130\n" \
  "v_accvgpr_write_b32 a163, v131\n" \
  "v_accvgpr_write_b32 a164, v132\n" \
  "v_accvgpr_write_b32 a165, v133\n" \
  "v_accvgpr_write_b32 a166, v134\n" \
  "v_accvgpr_write_b32 a167, v135\n" \
  "v_accvgpr_write_b32 a168, v136\n" \
  "v_accvgpr_write_b32 a169, v137\n" \
  "v_accvgpr_write_b32 a170, v138\n" \
  "v_accvgpr_write_b32 a171, v139\n" \
  "v_accvgpr_write_b32 a172, v140\n" \
  "v_accvgpr_write_b32 a173, v141\n" \
  "v_accvgpr_write_b32 a174, v142\n" \
  "v_accvgpr_write_b32 a175, v143\n" \
  "v_accvgpr_write_b32 a176, v144\n" \
  "v_accvgpr_write_b32 a177, v145\n" \
  "v_accvgpr_write_b32 a178, v146\n" \
  "v_accvgpr_write_b32 a179, v147\n" \
  "v_accvgpr_write_b32 a180, v148\n" \
  "v_accvgpr_write_b32 a181, v149\n" \
  "v_accvgpr_write_b32 a182, v150\n" \
  "v_accvgpr_write_b32 a183, v151\n" \
  "v_accvgpr_write_b32 a184, v152\n" \
  "v_accvgpr_write_b32 a185, v153\n" \
  "v_accvgpr_write_b32 a186, v154\n" \
  "v_accvgpr_write_b32 a187, v155\n" \
  "v_accvgpr_write_b32 a188, v156\n" \
  "v_accvgpr_write_b32 a189, v157\n" \
  "v_accvgpr_write_b32 a190, v158\n" \
  "v_accvgpr_write_b32 a191, v159\n" \
  ".LF_w_b_%=:\n" \
  "s_cmp_lt_u32 s58, 6\n" \
  "s_waitcnt vmcnt(0)\n" \
  "s_cbranch_scc1 .LF_pre_%=\n" \
  "s_waitcnt vmcnt(0)\n" \
  ".LF_loop_%=:\n" \
  "v_mov_b64 v[40:41], 0\n" \
  "v_mov_b64 v[42:43], 0\n" \
  "v_mov_b64 v[44:45], 0\n" \
  "v_mov_b64 v[46:47], 0\n" \
  "v_mov_b64 v[48:49], 0\n" \
  "v_mov_b64 v[50:51], 0\n" \
  "v_mov_b64 v[52:53], 0\n" \
  "v_mov_b64 v[54:55], 0\n" \
  "s_cmp_lg_u32 %43, 0\n" \
  "s_cbranch_scc1 .LF_go_%=\n" \
  "s_mov_b32 s69, 0\n" \
  "s_add_u32 s53, s48, 58\n" \
  "s_cmp_eq_u32 s48, 5\n" \
  "s_cselect_b32 s54, 16, 0\n" \
  "s_lshr_b32 s54, %42, s54\n" \
  "s_and_b32 s54, s54, 0x7fff\n" \
  "s_branch .LF_tw1_%=\n" \
  ".LF_tw_%=:\n" \
  "buffer_load_dwordx2 v[62:63], off, s[40:43], s50 offset:256 sc1\n" \
  "s_waitcnt vmcnt(0)\n" \
  ".LF_tw1_%=:\n" \
  "s_memrealtime s[76:77]\n" \
  "v_readfirstlane_b32 s62, v62\n" \
  "v_readfirstlane_b32 s61, v63\n" \
  "s_add_u32 s55, s61, s54\n" \
  "s_waitcnt lgkmcnt(0)\n" \
  "s_sub_u32 s60, s55, s76\n" \
  "s_cmp_gt_i32 s62, s53\n" \
  "s_cbranch_scc1 .LF_gq_%=\n" \
  "s_cmp_eq_u32 s62, s53\n" \
  "s_cselect_b32 s60, s60, 999\n" \
  "s_cmp_lt_i32 s60, 400\n" \
  "s_cbranch_scc1 .LF_clk_%=\n" \
  "s_sleep 8\n" \
  "s_add_u32 s69, s69, 1\n" \
  "s_cmp_lt_u32 s69, 20000\n" \
  "s_cbranch_scc1 .LF_tw_%=\n" \
  "s_branch .LF_gq_%=\n" \
  ".LF_clk_%=:\n" \
  "s_cmp_le_i32 s60, 3\n" \
  "s_cbranch_scc1 .LF_gq_%=\n" \
  "s_sleep 1\n" \
  "s_cmp_le_i32 s60, 30\n" \
  "s_cbranch_scc1 .LF_cl2_%=\n" \
  "s_sleep 6\n" \
  ".LF_cl2_%=:\n" \
  "s_memrealtime s[76:77]\n" \
  "s_waitcnt lgkmcnt(0)\n" \
  "s_sub_u32 s60, s55, s76\n" \
  "s_branch .LF_clk_%=\n" \
  ".LF_gq_%=:\n" \
  "s_cmp_eq_u32 s48, 5\n" \
  "s_cbranch_scc1 .LF_go_%=\n" \
  "buffer_load_dword v60, v212, s[40:43], 0 offen sc1\n" \
  "s_and_b32 s61, %45, 255\n" \
  "s_waitcnt vmcnt(0)\n" \
  "s_memrealtime s[76:77]\n" \
  "v_readlane_b32 s80, v60, 0\n" \
  "v_readlane_b32 s81, v60, 1\n" \
  "v_readlane_b32 s82, v60, 2\n" \
  "v_readlane_b32 s83, v60, 3\n" \
  "s_waitcnt lgkmcnt(0)\n" \
  "s_sub_u32 s61, s61, s76\n" \
  "s_add_u32 s80, s80, s61\n" \
  "s_add_u32 s81, s81, s61\n" \
  "s_add_u32 s82, s82, s61\n" \
  "s_add_u32 s83, s83, s61\n" \
  "s_min_u32 s80, s80, s81\n" \
  "s_min_u32 s82, s82, s83\n" \
  "s_min_u32 s80, s80, s82\n" \
  "s_lshr_b32 s61, %45, 8\n" \
  "s_cmp_gt_u32 s80, 300\n" \
  "s_cselect_b32 s80, 0, s80\n" \
  "s_sub_u32 s80, s80, s61\n" \
  "s_add_u32 s81, s76, s80\n" \
  ".LF_qf_%=:\n" \
  "s_cmp_le_i32 s80, 0\n" \
  "s_cbranch_scc1 .LF_qf_x_%=\n" \
  "s_sleep 2\n" \
  "s_memrealtime s[76:77]\n" \
  "s_waitcnt lgkmcnt(0)\n" \
  "s_sub_u32 s80, s81, s76\n" \
  "s_branch .LF_qf_%=\n" \
  ".LF_qf_x_%=:\n" \
  ".LF_go_%=:\n" \
  "s_barrier\n" \
  "s_mov_b32 s69, 0\n" \
  "s_lshr_b32 s52, s49, 8\n" \
  "s_and_b32 s52, s52, 8192\n" \
  "s_xor_b32 s52, s52, s49\n" \
  "s_xor_b32 s60, s50, s56\n" \
  "buffer_load_dwordx2 v[62:63], off, s[40:43], s60 offset:256 sc1\n" \
  ".LF_rd_%=:\n" \
  "buffer_load_dwordx4 v[0:3], v202, s[40:43], s52 offen sc1\n" \
  "buffer_load_dwordx4 v[4:7], v202, s[40:43], s52 offen offset:1024 sc1\n" \
  "s_mov_b32 s60, s51\n" \
  "buffer_load_dwordx4 v[8:11], v207, s[40:43], s60 offen sc1\n" \
  "s_add_u32 s60, s60, 4096\n" \
  "buffer_load_dwordx4 v[24:27], v207, s[40:43], s60 offen sc1\n" \
  "s_add_u32 s60, s60, 12288\n" \
  "buffer_load_dwordx4 v[12:15], v207, s[40:43], s60 offen sc1\n" \
  "s_add_u32 s60, s60, 4096\n" \
  "buffer_load_dwordx4 v[28:31], v207, s[40:43], s60 offen sc1\n" \
  "s_add_u32 s60, s60, 12288\n" \
  "buffer_load_dwordx4 v[16:19], v207, s[40:43], s60 offen sc1\n" \
  "s_add_u32 s60, s60, 4096\n" \
  "buffer_load_dwordx4 v[32:35], v207, s[40:43], s60 offen sc1\n" \
  "s_add_u32 s60, s60, 12288\n" \
  "buffer_load_dwordx4 v[20:23], v207, s[40:43], s60 offen sc1\n" \
  "s_add_u32 s60, s60, 4096\n" \
  "buffer_load_dwordx4 v[36:39], v207, s[40:43], s60 offen sc1\n" \
  "s_cmp_eq_u32 s48, 1\n" \
  "s_cbranch_scc1 .LF_r1_%=\n" \
  "s_cmp_eq_u32 s48, 2\n" \
  "s_cbranch_scc1 .LF_r2_%=\n" \
  "s_cmp_eq_u32 s48, 3\n" \
  "s_cbranch_scc1 .LF_r3_%=\n" \
  "s_cmp_eq_u32 s48, 4\n" \
  "s_cbranch_scc1 .LF_r4_%=\n" \
  "s_cmp_eq_u32 s48, 5\n" \
  "s_cbranch_scc1 .LF_r5_%=\n" \
  "v_mov_b32 v192, v213\n" \
  "v_mov_b32 v193, v214\n" \
  "v_mov_b32 v194, v215\n" \
  "v_accvgpr_read_b32 v128, a0\n" \
  "v_accvgpr_read_b32 v129, a1\n" \
  "v_accvgpr_read_b32 v130, a2\n" \
  "v_accvgpr_read_b32 v131, a3\n" \
  "v_accvgpr_read_b32 v132, a4\n" \
  "v_accvgpr_read_b32 v133, a5\n" \
  "v_accvgpr_read_b32 v134, a6\n" \
  "v_accvgpr_read_b32 v135, a7\n" \
  "v_accvgpr_read_b32 v136, a8\n" \
  "v_accvgpr_read_b32 v137, a9\n" \
  "v_accvgpr_read_b32 v138, a10\n" \
  "v_accvgpr_read_b32 v139, a11\n" \
  "v_accvgpr_read_b32 v140, a12\n" \
  "v_accvgpr_read_b32 v141, a13\n" \
  "v_accvgpr_read_b32 v142, a14\n" \
  "v_accvgpr_read_b32 v143, a15\n" \
  "v_accvgpr_read_b32 v144, a16\n" \
  "v_accvgpr_read_b32 v145, a17\n" \
  "v_accvgpr_read_b32 v146, a18\n" \
  "v_accvgpr_read_b32 v147, a19\n" \
  "v_accvgpr_read_b32 v148, a20\n" \
  "v_accvgpr_read_b32 v149, a21\n" \
  "v_accvgpr_read_b32 v150, a22\n" \
  "v_accvgpr_read_b32 v151, a23\n" \
  "v_accvgpr_read_b32 v152, a24\n" \
  "v_accvgpr_read_b32 v153, a25\n" \
  "v_accvgpr_read_b32 v154, a26\n" \
  "v_accvgpr_read_b32 v155, a27\n" \
  "v_accvgpr_read_b32 v156, a28\n" \
  "v_accvgpr_read_b32 v157, a29\n" \
  "v_accvgpr_read_b32 v158, a30\n" \
  "v_accvgpr_read_b32 v159, a31\n" \
  ".LF_r_b_%=:\n" \
  "s_waitcnt vmcnt(8)\n" \
  "v_permlane16_swap_b32 v0, v1\n" \
  "v_permlane16_swap_b32 v2, v3\n" \
  "v_permlane16_swap_b32 v4, v5\n" \
  "v_permlane16_swap_b32 v6, v7\n" \
  "s_mov_b64 s[78:79], 0\n" \
  "s_waitcnt vmcnt(7)\n" \
  "v_smfmac_f32_16x16x64_bf16 v[40:43], v[8:11], v[0:7], v200\n" \
  "s_waitcnt vmcnt(6)\n" \
  "v_smfmac_f32_16x16x64_bf16 v[40:43], v[24:27], v[128:135], v201\n" \
  "s_waitcnt vmcnt(5)\n" \
  "v_smfmac_f32_16x16x64_bf16 v[44:47], v[12:15], v[0:7], v200\n" \
  "s_waitcnt vmcnt(4)\n" \
  "v_smfmac_f32_16x16x64_bf16 v[44:47], v[28:31], v[136:143], v201\n" \
  "s_waitcnt vmcnt(3)\n" \
  "v_smfmac_f32_16x16x64_bf16 v[48:51], v[16:19], v[0:7], v200\n" \
  "s_waitcnt vmcnt(2)\n" \
  "v_smfmac_f32_16x16x64_bf16 v[48:51], v[32:35], v[144:151], v201\n" \
  "s_waitcnt vmcnt(1)\n" \
  "v_smfmac_f32_16x16x64_bf16 v[52:55], v[20:23], v[0:7], v200\n" \
  "s_waitcnt vmcnt(0)\n" \
  "v_smfmac_f32_16x16x64_bf16 v[52:55], v[36:39], v[152:159], v201\n" \
  "s_nop 1\n" \
  "v_permlane32_swap_b32 v40, v42\n" \
  "v_permlane32_swap_b32 v41, v43\n" \
  "v_add_f32 v40, v40, v42\n" \
  "v_add_f32 v41, v41, v43\n" \
  "v_cmp_u_f32 vcc, v40, v41\n" \
  "ds_write2st64_b32 v208, v40, v41 offset0:0 offset1:4\n" \
  "s_or_b64 s[78:79], s[78:79], vcc\n" \
  "v_permlane32_swap_b32 v44, v46\n" \
  "v_permlane32_swap_b32 v45, v47\n" \
  "v_add_f32 v44, v44, v46\n" \
  "v_add_f32 v45, v45, v47\n" \
  "v_cmp_u_f32 vcc, v44, v45\n" \
  "ds_write2st64_b32 v208, v44, v45 offset0:8 offset1:12\n" \
  "s_or_b64 s[78:79], s[78:79], vcc\n" \
  "v_permlane32_swap_b32 v48, v50\n" \
  "v_permlane32_swap_b32 v49, v51\n" \
  "v_add_f32 v48, v48, v50\n" \
  "v_add_f32 v49, v49, v51\n" \
  "v_cmp_u_f32 vcc, v48, v49\n" \
  "ds_write2st64_b32 v208, v48, v49 offset0:16 offset1:20\n" \
  "s_or_b64 s[78:79], s[78:79], vcc\n" \
  "v_permlane32_swap_b32 v52, v54\n" \
  "v_permlane32_swap_b32 v53, v55\n" \
  "v_add_f32 v52, v52, v54\n" \
  "v_add_f32 v53, v53, v55\n" \
  "v_cmp_u_f32 vcc, v52, v53\n" \
  "ds_write2st64_b32 v208, v52, v53 offset0:24 offset1:28\n" \
  "s_or_b64 s[78:79], s[78:79], vcc\n" \
  "s_cbranch_scc1 .LF_bad_%=\n" \
  ".LF_ok_%=:\n" \
  "s_waitcnt lgkmcnt(0)\n" \
  "s_barrier\n" \
  "ds_read_b128 v[40:43], v209\n" \
  "ds_read_b128 v[44:47], v209 offset:512\n" \
  "s_waitcnt lgkmcnt(1)\n" \
  "v_add_f32 v40, v40, v42\n" \
  "v_add_f32 v41, v41, v43\n" \
  "s_waitcnt lgkmcnt(0)\n" \
  "v_add_f32 v44, v44, v46\n" \
  "v_add_f32 v45, v45, v47\n" \
  "v_add_f32 v40, v40, v44\n" \
  "v_add_f32 v41, v41, v45\n" \
  "v_cvt_pk_bf16_f32 v40, v40, v41\n" \
  "v_and_b32 v41, 0xffff0000, v40\n" \
  "v_lshlrev_b32 v40, 16, v40\n" \
  "v_mul_f32 v40, v192, v40\n" \
  "v_mul_f32 v41, v192, v41\n" \
  "v_cvt_pk_bf16_f32 v40, v40, v41\n" \
  "v_and_b32 v41, 0xffff0000, v40\n" \
  "v_lshlrev_b32 v40, 16, v40\n" \
  "v_add_f32 v40, v40, v193\n" \
  "v_add_f32 v41, v41, v194\n" \
  "v_cvt_pk_bf16_f32 v237, v40, v41\n" \
  "s_cmp_eq_u32 s48, 5\n" \
  "s_cbranch_scc1 .LF_out_%=\n" \
  "v_mov_b32 v232, v233\n" \
  "v_mov_b32 v233, v234\n" \
  "v_mov_b32 v234, v235\n" \
  "v_mov_b32 v235, v236\n" \
  "v_mov_b32 v236, v237\n" \
  "v_xor_b32 v208, 8192, v208\n" \
  "v_xor_b32 v209, 8192, v209\n" \
  "s_add_u32 s49, s49, 1048576\n" \
  "s_xor_b32 s50, s50, s56\n" \
  "s_mov_b32 s60, s56\n" \
  "s_mov_b32 s56, s57\n" \
  "s_mov_b32 s57, s60\n" \
  "s_add_u32 s51, s51, 65536\n" \
  "s_add_u32 s48, s48, 1\n" \
  "s_cmp_lt_u32 s58, 6\n" \
  "s_cbranch_scc1 .LF_fu_%=\n" \
  "s_branch .LF_loop_%=\n" \
  ".LF_out_%=:\n" \
  "s_mov_b32 s60, 184320\n" \
  "buffer_store_dword v237, v210, s[44:47], s60 offen\n" \
  "s_mov_b32 s60, 147456\n" \
  "buffer_store_dword v236, v210, s[44:47], s60 offen\n" \
  "s_mov_b32 s60, 110592\n" \
  "buffer_store_dword v235, v210, s[44:47], s60 offen\n" \
  "s_mov_b32 s60, 73728\n" \
  "buffer_store_dword v234, v210, s[44:47], s60 offen\n" \
  "s_mov_b32 s60, 36864\n" \
  "buffer_store_dword v233, v210, s[44:47], s60 offen\n" \
  "s_mov_b32 s60, 0\n" \
  "buffer_store_dword v232, v210, s[44:47], s60 offen\n" \
  "s_branch .LF_end_%=\n" \
  ".LF_fl_%=:\n" \
  "s_add_u32 s61, s58, 58\n" \
  "s_and_b32 s61, s61, 15\n" \
  "s_cmp_ge_u32 s58, 6\n" \
  "s_cselect_b32 s60, 2048, 0\n" \
  "s_or_b32 s61, s61, s60\n" \
  "s_lshl_b32 s61, s61, 16\n" \
  "s_xor_b32 s61, s61, %34\n" \
  "s_lshl_b32 s62, s58, 17\n" \
  "s_add_u32 s62, s62, %35\n" \
  "s_xor_b32 s60, s61, 0\n" \
  "buffer_load_dwordx4 v[32:35], v202, s[40:43], s60 offen sc1\n" \
  "buffer_load_dwordx4 v[64:67], v203, s[40:43], s62 offen sc1\n" \
  "buffer_load_dwordx4 v[68:71], v203, s[40:43], s62 offen offset:1024 sc1\n" \
  "s_xor_b32 s60, s61, 1024\n" \
  "buffer_load_dwordx4 v[36:39], v202, s[40:43], s60 offen sc1\n" \
  "buffer_load_dwordx4 v[72:75], v203, s[40:43], s62 offen offset:2048 sc1\n" \
  "buffer_load_dwordx4 v[76:79], v203, s[40:43], s62 offen offset:3072 sc1\n" \
  "s_xor_b32 s60, s61, 2048\n" \
  "buffer_load_dwordx4 v[40:43], v202, s[40:43], s60 offen sc1\n" \
  "buffer_load_dwordx4 v[80:83], v204, s[40:43], s62 offen sc1\n" \
  "buffer_load_dwordx4 v[84:87], v204, s[40:43], s62 offen offset:1024 sc1\n" \
  "s_xor_b32 s60, s61, 3072\n" \
  "buffer_load_dwordx4 v[44:47], v202, s[40:43], s60 offen sc1\n" \
  "buffer_load_dwordx4 v[88:91], v204, s[40:43], s62 offen offset:2048 sc1\n" \
  "buffer_load_dwordx4 v[92:95], v204, s[40:43], s62 offen offset:3072 sc1\n" \
  "s_xor_b32 s60, s61, 4096\n" \
  "buffer_load_dwordx4 v[48:51], v202, s[40:43], s60 offen sc1\n" \
  "buffer_load_dwordx4 v[96:99], v205, s[40:43], s62 offen sc1\n" \
  "buffer_load_dwordx4 v[100:103], v205, s[40:43], s62 offen offset:1024 sc1\n" \
  "s_xor_b32 s60, s61, 5120\n" \
  "buffer_load_dwordx4 v[52:55], v202, s[40:43], s60 offen sc1\n" \
  "buffer_load_dwordx4 v[104:107], v205, s[40:43], s62 offen offset:2048 sc1\n" \
  "buffer_load_dwordx4 v[108:111], v205, s[40:43], s62 offen offset:3072 sc1\n" \
  "s_xor_b32 s60, s61, 6144\n" \
  "buffer_load_dwordx4 v[56:59], v202, s[40:43], s60 offen sc1\n" \
  "buffer_load_dwordx4 v[112:115], v206, s[40:43], s62 offen sc1\n" \
  "buffer_load_dwordx4 v[116:119], v206, s[40:43], s62 offen offset:1024 sc1\n" \
  "s_xor_b32 s60, s61, 7168\n" \
  "buffer_load_dwordx4 v[60:63], v202, s[40:43], s60 offen sc1\n" \
  "buffer_load_dwordx4 v[120:123], v206, s[40:43], s62 offen offset:2048 sc1\n" \
  "buffer_load_dwordx4 v[124:127], v206, s[40:43], s62 offen offset:3072 sc1\n" \
  "s_add_u32 s64, s64, 128\n" \
  "s_branch .LF_fl_b_%=\n" \
  ".LF_w1_%=:\n" \
  "v_accvgpr_write_b32 a0, v128\n" \
  "v_accvgpr_write_b32 a1, v129\n" \
  "v_accvgpr_write_b32 a2, v130\n" \
  "v_accvgpr_write_b32 a3, v131\n" \
  "v_accvgpr_write_b32 a4, v132\n" \
  "v_accvgpr_write_b32 a5, v133\n" \
  "v_accvgpr_write_b32 a6, v134\n" \
  "v_accvgpr_write_b32 a7, v135\n" \
  "v_accvgpr_write_b32 a8, v136\n" \
  "v_accvgpr_write_b32 a9, v137\n" \
  "v_accvgpr_write_b32 a10, v138\n" \
  "v_accvgpr_write_b32 a11, v139\n" \
  "v_accvgpr_write_b32 a12, v140\n" \
  "v_accvgpr_write_b32 a13, v141\n" \
  "v_accvgpr_write_b32 a14, v142\n" \
  "v_accvgpr_write_b32 a15, v143\n" \
  "v_accvgpr_write_b32 a16, v144\n" \
  "v_accvgpr_write_b32 a17, v145\n" \
  "v_accvgpr_write_b32 a18, v146\n" \
  "v_accvgpr_write_b32 a19, v147\n" \
  "v_accvgpr_write_b32 a20, v148\n" \
  "v_accvgpr_write_b32 a21, v149\n" \
  "v_accvgpr_write_b32 a22, v150\n" \
  "v_accvgpr_write_b32 a23, v151\n" \
  "v_accvgpr_write_b32 a24, v152\n" \
  "v_accvgpr_write_b32 a25, v153\n" \
  "v_accvgpr_write_b32 a26, v154\n" \
  "v_accvgpr_write_b32 a27, v155\n" \
  "v_accvgpr_write_b32 a28, v156\n" \
  "v_accvgpr_write_b32 a29, v157\n" \
  "v_accvgpr_write_b32 a30, v158\n" \
  "v_accvgpr_write_b32 a31, v159\n" \
  "s_branch .LF_w_b_%=\n" \
  ".LF_w2_%=:\n" \
  "v_accvgpr_write_b32 a32, v128\n" \
  "v_accvgpr_write_b32 a33, v129\n" \
  "v_accvgpr_write_b32 a34, v130\n" \
  "v_accvgpr_write_b32 a35, v131\n" \
  "v_accvgpr_write_b32 a36, v132\n" \
  "v_accvgpr_write_b32 a37, v133\n" \
  "v_accvgpr_write_b32 a38, v134\n" \
  "v_accvgpr_write_b32 a39, v135\n" \
  "v_accvgpr_write_b32 a40, v136\n" \
  "v_accvgpr_write_b32 a41, v137\n" \
  "v_accvgpr_write_b32 a42, v138\n" \
  "v_accvgpr_write_b32 a43, v139\n" \
  "v_accvgpr_write_b32 a44, v140\n" \
  "v_accvgpr_write_b32 a45, v141\n" \
  "v_accvgpr_write_b32 a46, v142\n" \
  "v_accvgpr_write_b32 a47, v143\n" \
  "v_accvgpr_write_b32 a48, v144\n" \
  "v_accvgpr_write_b32 a49, v145\n" \
  "v_accvgpr_write_b32 a50, v146\n" \
  "v_accvgpr_write_b32 a51, v147\n" \
  "v_accvgpr_write_b32 a52, v148\n" \
  "v_accvgpr_write_b32 a53, v149\n" \
  "v_accvgpr_write_b32 a54, v150\n" \
  "v_accvgpr_write_b32 a55, v151\n" \
  "v_accvgpr_write_b32 a56, v152\n" \
  "v_accvgpr_write_b32 a57, v153\n" \
  "v_accvgpr_write_b32 a58, v154\n" \
  "v_accvgpr_write_b32 a59, v155\n" \
  "v_accvgpr_write_b32 a60, v156\n" \
  "v_accvgpr_write_b32 a61, v157\n" \
  "v_accvgpr_write_b32 a62, v158\n" \
  "v_accvgpr_write_b32 a63, v159\n" \
  "s_branch .LF_w_b_%=\n" \
  ".LF_w3_%=:\n" \
  "v_accvgpr_write_b32 a64, v128\n" \
  "v_accvgpr_write_b32 a65, v129\n" \
  "v_accvgpr_write_b32 a66, v130\n" \
  "v_accvgpr_write_b32 a67, v131\n" \
  "v_accvgpr_write_b32 a68, v132\n" \
  "v_accvgpr_write_b32 a69, v133\n" \
  "v_accvgpr_write_b32 a70, v134\n" \
  "v_accvgpr_write_b32 a71, v135\n" \
  "v_accvgpr_write_b32 a72, v136\n" \
  "v_accvgpr_write_b32 a73, v137\n" \
  "v_accvgpr_write_b32 a74, v138\n" \
  "v_accvgpr_write_b32 a75, v139\n" \
  "v_accvgpr_write_b32 a76, v140\n" \
  "v_accvgpr_write_b32 a77, v141\n" \
  "v_accvgpr_write_b32 a78, v142\n" \
  "v_accvgpr_write_b32 a79, v143\n" \
  "v_accvgpr_write_b32 a80, v144\n" \
  "v_accvgpr_write_b32 a81, v145\n" \
  "v_accvgpr_write_b32 a82, v146\n" \
  "v_accvgpr_write_b32 a83, v147\n" \
  "v_accvgpr_write_b32 a84, v148\n" \
  "v_accvgpr_write_b32 a85, v149\n" \
  "v_accvgpr_write_b32 a86, v150\n" \
  "v_accvgpr_write_b32 a87, v151\n" \
  "v_accvgpr_write_b32 a88, v152\n" \
  "v_accvgpr_write_b32 a89, v153\n" \
  "v_accvgpr_write_b32 a90, v154\n" \
  "v_accvgpr_write_b32 a91, v155\n" \
  "v_accvgpr_write_b32 a92, v156\n" \
  "v_accvgpr_write_b32 a93, v157\n" \
  "v_accvgpr_write_b32 a94, v158\n" \
  "v_accvgpr_write_b32 a95, v159\n" \
  "s_branch .LF_w_b_%=\n" \
  ".LF_w4_%=:\n" \
  "v_accvgpr_write_b32 a96, v128\n" \
  "v_accvgpr_write_b32 a97, v129\n" \
  "v_accvgpr_write_b32 a98, v130\n" \
  "v_accvgpr_write_b32 a99, v131\n" \
  "v_accvgpr_write_b32 a100, v132\n" \
  "v_accvgpr_write_b32 a101, v133\n" \
  "v_accvgpr_write_b32 a102, v134\n" \
  "v_accvgpr_write_b32 a103, v135\n" \
  "v_accvgpr_write_b32 a104, v136\n" \
  "v_accvgpr_write_b32 a105, v137\n" \
  "v_accvgpr_write_b32 a106, v138\n" \
  "v_accvgpr_write_b32 a107, v139\n" \
  "v_accvgpr_write_b32 a108, v140\n" \
  "v_accvgpr_write_b32 a109, v141\n" \
  "v_accvgpr_write_b32 a110, v142\n" \
  "v_accvgpr_write_b32 a111, v143\n" \
  "v_accvgpr_write_b32 a112, v144\n" \
  "v_accvgpr_write_b32 a113, v145\n" \
  "v_accvgpr_write_b32 a114, v146\n" \
  "v_accvgpr_write_b32 a115, v147\n" \
  "v_accvgpr_write_b32 a116, v148\n" \
  "v_accvgpr_write_b32 a117, v149\n" \
  "v_accvgpr_write_b32 a118, v150\n" \
  "v_accvgpr_write_b32 a119, v151\n" \
  "v_accvgpr_write_b32 a120, v152\n" \
  "v_accvgpr_write_b32 a121, v153\n" \
  "v_accvgpr_write_b32 a122, v154\n" \
  "v_accvgpr_write_b32 a123, v155\n" \
  "v_accvgpr_write_b32 a124, v156\n" \
  "v_accvgpr_write_b32 a125, v157\n" \
  "v_accvgpr_write_b32 a126, v158\n" \
  "v_accvgpr_write_b32 a127, v159\n" \
  "s_branch .LF_w_b_%=\n" \
  ".LF_w5_%=:\n" \
  "v_accvgpr_write_b32 a128, v128\n" \
  "v_accvgpr_write_b32 a129, v129\n" \
  "v_accvgpr_write_b32 a130, v130\n" \
  "v_accvgpr_write_b32 a131, v131\n" \
  "v_accvgpr_write_b32 a132, v132\n" \
  "v_accvgpr_write_b32 a133, v133\n" \
  "v_accvgpr_write_b32 a134, v134\n" \
  "v_accvgpr_write_b32 a135, v135\n" \
  "v_accvgpr_write_b32 a136, v136\n" \
  "v_accvgpr_write_b32 a137, v137\n" \
  "v_accvgpr_write_b32 a138, v138\n" \
  "v_accvgpr_write_b32 a139, v139\n" \
  "v_accvgpr_write_b32 a140, v140\n" \
  "v_accvgpr_write_b32 a141, v141\n" \
  "v_accvgpr_write_b32 a142, v142\n" \
  "v_accvgpr_write_b32 a143, v143\n" \
  "v_accvgpr_write_b32 a144, v144\n" \
  "v_accvgpr_write_b32 a145, v145\n" \
  "v_accvgpr_write_b32 a146, v146\n" \
  "v_accvgpr_write_b32 a147, v147\n" \
  "v_accvgpr_write_b32 a148, v148\n" \
  "v_accvgpr_write_b32 a149, v149\n" \
  "v_accvgpr_write_b32 a150, v150\n" \
  "v_accvgpr_write_b32 a151, v151\n" \
  "v_accvgpr_write_b32 a152, v152\n" \
  "v_accvgpr_write_b32 a153, v153\n" \
  "v_accvgpr_write_b32 a154, v154\n" \
  "v_accvgpr_write_b32 a155, v155\n" \
  "v_accvgpr_write_b32 a156, v156\n" \
  "v_accvgpr_write_b32 a157, v157\n" \
  "v_accvgpr_write_b32 a158, v158\n" \
  "v_accvgpr_write_b32 a159, v159\n" \
  "s_branch .LF_w_b_%=\n" \
  ".LF_r1_%=:\n" \
  "v_mov_b32 v192, v216\n" \
  "v_mov_b32 v193, v217\n" \
  "v_mov_b32 v194, v218\n" \
  "v_accvgpr_read_b32 v128, a32\n" \
  "v_accvgpr_read_b32 v129, a33\n" \
  "v_accvgpr_read_b32 v130, a34\n" \
  "v_accvgpr_read_b32 v131, a35\n" \
  "v_accvgpr_read_b32 v132, a36\n" \
  "v_accvgpr_read_b32 v133, a37\n" \
  "v_accvgpr_read_b32 v134, a38\n" \
  "v_accvgpr_read_b32 v135, a39\n" \
  "v_accvgpr_read_b32 v136, a40\n" \
  "v_accvgpr_read_b32 v137, a41\n" \
  "v_accvgpr_read_b32 v138, a42\n" \
  "v_accvgpr_read_b32 v139, a43\n" \
  "v_accvgpr_read_b32 v140, a44\n" \
  "v_accvgpr_read_b32 v141, a45\n" \
  "v_accvgpr_read_b32 v142, a46\n" \
  "v_accvgpr_read_b32 v143, a47\n" \
  "v_accvgpr_read_b32 v144, a48\n" \
  "v_accvgpr_read_b32 v145, a49\n" \
  "v_accvgpr_read_b32 v146, a50\n" \
  "v_accvgpr_read_b32 v147, a51\n" \
  "v_accvgpr_read_b32 v148, a52\n" \
  "v_accvgpr_read_b32 v149, a53\n" \
  "v_accvgpr_read_b32 v150, a54\n" \
  "v_accvgpr_read_b32 v151, a55\n" \
  "v_accvgpr_read_b32 v152, a56\n" \
  "v_accvgpr_read_b32 v153, a57\n" \
  "v_accvgpr_read_b32 v154, a58\n" \
  "v_accvgpr_read_b32 v155, a59\n" \
  "v_accvgpr_read_b32 v156, a60\n" \
  "v_accvgpr_read_b32 v157, a61\n" \
  "v_accvgpr_read_b32 v158, a62\n" \
  "v_accvgpr_read_b32 v159, a63\n" \
  "s_branch .LF_r_b_%=\n" \
  ".LF_r2_%=:\n" \
  "v_mov_b32 v192, v219\n" \
  "v_mov_b32 v193, v220\n" \
  "v_mov_b32 v194, v221\n" \
  "v_accvgpr_read_b32 v128, a64\n" \
  "v_accvgpr_read_b32 v129, a65\n" \
  "v_accvgpr_read_b32 v130, a66\n" \
  "v_accvgpr_read_b32 v131, a67\n" \
  "v_accvgpr_read_b32 v132, a68\n" \
  "v_accvgpr_read_b32 v133, a69\n" \
  "v_accvgpr_read_b32 v134, a70\n" \
  "v_accvgpr_read_b32 v135, a71\n" \
  "v_accvgpr_read_b32 v136, a72\n" \
  "v_accvgpr_read_b32 v137, a73\n" \
  "v_accvgpr_read_b32 v138, a74\n" \
  "v_accvgpr_read_b32 v139, a75\n" \
  "v_accvgpr_read_b32 v140, a76\n" \
  "v_accvgpr_read_b32 v141, a77\n" \
  "v_accvgpr_read_b32 v142, a78\n" \
  "v_accvgpr_read_b32 v143, a79\n" \
  "v_accvgpr_read_b32 v144, a80\n" \
  "v_accvgpr_read_b32 v145, a81\n" \
  "v_accvgpr_read_b32 v146, a82\n" \
  "v_accvgpr_read_b32 v147, a83\n" \
  "v_accvgpr_read_b32 v148, a84\n" \
  "v_accvgpr_read_b32 v149, a85\n" \
  "v_accvgpr_read_b32 v150, a86\n" \
  "v_accvgpr_read_b32 v151, a87\n" \
  "v_accvgpr_read_b32 v152, a88\n" \
  "v_accvgpr_read_b32 v153, a89\n" \
  "v_accvgpr_read_b32 v154, a90\n" \
  "v_accvgpr_read_b32 v155, a91\n" \
  "v_accvgpr_read_b32 v156, a92\n" \
  "v_accvgpr_read_b32 v157, a93\n" \
  "v_accvgpr_read_b32 v158, a94\n" \
  "v_accvgpr_read_b32 v159, a95\n" \
  "s_branch .LF_r_b_%=\n" \
  ".LF_r3_%=:\n" \
  "v_mov_b32 v192, v222\n" \
  "v_mov_b32 v193, v223\n" \
  "v_mov_b32 v194, v224\n" \
  "v_accvgpr_read_b32 v128, a96\n" \
  "v_accvgpr_read_b32 v129, a97\n" \
  "v_accvgpr_read_b32 v130, a98\n" \
  "v_accvgpr_read_b32 v131, a99\n" \
  "v_accvgpr_read_b32 v132, a100\n" \
  "v_accvgpr_read_b32 v133, a101\n" \
  "v_accvgpr_read_b32 v134, a102\n" \
  "v_accvgpr_read_b32 v135, a103\n" \
  "v_accvgpr_read_b32 v136, a104\n" \
  "v_accvgpr_read_b32 v137, a105\n" \
  "v_accvgpr_read_b32 v138, a106\n" \
  "v_accvgpr_read_b32 v139, a107\n" \
  "v_accvgpr_read_b32 v140, a108\n" \
  "v_accvgpr_read_b32 v141, a109\n" \
  "v_accvgpr_read_b32 v142, a110\n" \
  "v_accvgpr_read_b32 v143, a111\n" \
  "v_accvgpr_read_b32 v144, a112\n" \
  "v_accvgpr_read_b32 v145, a113\n" \
  "v_accvgpr_read_b32 v146, a114\n" \
  "v_accvgpr_read_b32 v147, a115\n" \
  "v_accvgpr_read_b32 v148, a116\n" \
  "v_accvgpr_read_b32 v149, a117\n" \
  "v_accvgpr_read_b32 v150, a118\n" \
  "v_accvgpr_read_b32 v151, a119\n" \
  "v_accvgpr_read_b32 v152, a120\n" \
  "v_accvgpr_read_b32 v153, a121\n" \
  "v_accvgpr_read_b32 v154, a122\n" \
  "v_accvgpr_read_b32 v155, a123\n" \
  "v_accvgpr_read_b32 v156, a124\n" \
  "v_accvgpr_read_b32 v157, a125\n" \
  "v_accvgpr_read_b32 v158, a126\n" \
  "v_accvgpr_read_b32 v159, a127\n" \
  "s_branch .LF_r_b_%=\n" \
  ".LF_r4_%=:\n" \
  "v_mov_b32 v192, v225\n" \
  "v_mov_b32 v193, v226\n" \
  "v_mov_b32 v194, v227\n" \
  "v_accvgpr_read_b32 v128, a128\n" \
  "v_accvgpr_read_b32 v129, a129\n" \
  "v_accvgpr_read_b32 v130, a130\n" \
  "v_accvgpr_read_b32 v131, a131\n" \
  "v_accvgpr_read_b32 v132, a132\n" \
  "v_accvgpr_read_b32 v133, a133\n" \
  "v_accvgpr_read_b32 v134, a134\n" \
  "v_accvgpr_read_b32 v135, a135\n" \
  "v_accvgpr_read_b32 v136, a136\n" \
  "v_accvgpr_read_b32 v137, a137\n" \
  "v_accvgpr_read_b32 v138, a138\n" \
  "v_accvgpr_read_b32 v139, a139\n" \
  "v_accvgpr_read_b32 v140, a140\n" \
  "v_accvgpr_read_b32 v141, a141\n" \
  "v_accvgpr_read_b32 v142, a142\n" \
  "v_accvgpr_read_b32 v143, a143\n" \
  "v_accvgpr_read_b32 v144, a144\n" \
  "v_accvgpr_read_b32 v145, a145\n" \
  "v_accvgpr_read_b32 v146, a146\n" \
  "v_accvgpr_read_b32 v147, a147\n" \
  "v_accvgpr_read_b32 v148, a148\n" \
  "v_accvgpr_read_b32 v149, a149\n" \
  "v_accvgpr_read_b32 v150, a150\n" \
  "v_accvgpr_read_b32 v151, a151\n" \
  "v_accvgpr_read_b32 v152, a152\n" \
  "v_accvgpr_read_b32 v153, a153\n" \
  "v_accvgpr_read_b32 v154, a154\n" \
  "v_accvgpr_read_b32 v155, a155\n" \
  "v_accvgpr_read_b32 v156, a156\n" \
  "v_accvgpr_read_b32 v157, a157\n" \
  "v_accvgpr_read_b32 v158, a158\n" \
  "v_accvgpr_read_b32 v159, a159\n" \
  "s_branch .LF_r_b_%=\n" \
  ".LF_r5_%=:\n" \
  "v_mov_b32 v192, v228\n" \
  "v_mov_b32 v193, v229\n" \
  "v_mov_b32 v194, v230\n" \
  "v_accvgpr_read_b32 v128, a160\n" \
  "v_accvgpr_read_b32 v129, a161\n" \
  "v_accvgpr_read_b32 v130, a162\n" \
  "v_accvgpr_read_b32 v131, a163\n" \
  "v_accvgpr_read_b32 v132, a164\n" \
  "v_accvgpr_read_b32 v133, a165\n" \
  "v_accvgpr_read_b32 v134, a166\n" \
  "v_accvgpr_read_b32 v135, a167\n" \
  "v_accvgpr_read_b32 v136, a168\n" \
  "v_accvgpr_read_b32 v137, a169\n" \
  "v_accvgpr_read_b32 v138, a170\n" \
  "v_accvgpr_read_b32 v139, a171\n" \
  "v_accvgpr_read_b32 v140, a172\n" \
  "v_accvgpr_read_b32 v141, a173\n" \
  "v_accvgpr_read_b32 v142, a174\n" \
  "v_accvgpr_read_b32 v143, a175\n" \
  "v_accvgpr_read_b32 v144, a176\n" \
  "v_accvgpr_read_b32 v145, a177\n" \
  "v_accvgpr_read_b32 v146, a178\n" \
  "v_accvgpr_read_b32 v147, a179\n" \
  "v_accvgpr_read_b32 v148, a180\n" \
  "v_accvgpr_read_b32 v149, a181\n" \
  "v_accvgpr_read_b32 v150, a182\n" \
  "v_accvgpr_read_b32 v151, a183\n" \
  "v_accvgpr_read_b32 v152, a184\n" \
  "v_accvgpr_read_b32 v153, a185\n" \
  "v_accvgpr_read_b32 v154, a186\n" \
  "v_accvgpr_read_b32 v155, a187\n" \
  "v_accvgpr_read_b32 v156, a188\n" \
  "v_accvgpr_read_b32 v157, a189\n" \
  "v_accvgpr_read_b32 v158, a190\n" \
  "v_accvgpr_read_b32 v159, a191\n" \
  "s_branch .LF_r_b_%=\n" \
  ".LF_bad_%=:\n" \
  "v_max_u32 v60, v0, v1\n" \
  "v_max3_u32 v60, v60, v2, v3\n" \
  "v_max3_u32 v60, v60, v4, v5\n" \
  "v_max3_u32 v60, v60, v6, v7\n" \
  "v_max3_u32 v60, v60, v8, v9\n" \
  "v_max3_u32 v60, v60, v10, v11\n" \
  "v_max3_u32 v60, v60, v12, v13\n" \
  "v_max3_u32 v60, v60, v14, v15\n" \
  "v_max3_u32 v60, v60, v16, v17\n" \
  "v_max3_u32 v60, v60, v18, v19\n" \
  "v_max3_u32 v60, v60, v20, v21\n" \
  "v_max3_u32 v60, v60, v22, v23\n" \
  "v_max3_u32 v60, v60, v24, v25\n" \
  "v_max3_u32 v60, v60, v26, v27\n" \
  "v_max3_u32 v60, v60, v28, v29\n" \
  "v_max3_u32 v60, v60, v30, v31\n" \
  "v_max3_u32 v60, v60, v32, v33\n" \
  "v_max3_u32 v60, v60, v34, v35\n" \
  "v_max3_u32 v60, v60, v36, v37\n" \
  "v_max3_u32 v60, v60, v38, v39\n" \
  "v_cmp_eq_u32 vcc, -1, v60\n" \
  "s_cbranch_vccz .LF_ok_%=\n" \
  "v_mov_b64 v[40:41], 0\n" \
  "v_mov_b64 v[42:43], 0\n" \
  "v_mov_b64 v[44:45], 0\n" \
  "v_mov_b64 v[46:47], 0\n" \
  "v_mov_b64 v[48:49], 0\n" \
  "v_mov_b64 v[50:51], 0\n" \
  "v_mov_b64 v[52:53], 0\n" \
  "v_mov_b64 v[54:55], 0\n" \
  "s_sleep 2\n" \
  "s_cmp_eq_u32 s48, 5\n" \
  "s_cbranch_scc1 .LF_bd1_%=\n" \
  "s_sleep 20\n" \
  ".LF_bd1_%=:\n" \
  "s_add_u32 s69, s69, 1\n" \
  "s_and_b32 s63, s69, 1023\n" \
  "s_cbranch_scc1 .LF_rd_%=\n" \
  "buffer_load_dwordx2 v[60:61], off, s[40:43], %41 offset:128 sc1\n" \
  "s_waitcnt vmcnt(0)\n" \
  "v_readfirstlane_b32 s63, v61\n" \
  "s_cmp_eq_u32 s63, %33\n" \
  "s_cbranch_scc1 .LF_ok_%=\n" \
  "s_cmp_le_u32 s69, 2097152\n" \
  "s_cbranch_scc1 .LF_rd_%=\n" \
  "v_mov_b32 v60, 1\n" \
  "v_mov_b32 v61, %33\n" \
  "buffer_store_dwordx2 v[60:61], off, s[40:43], %41 offset:128 sc1\n" \
  "s_branch .LF_ok_%=\n" \
  ".LF_end_%=:\n"
#define F_ASM_VOPS "+{v200}"(fi.sidx), "+{v201}"(fi.cidx), "+{v202}"(fi.l16), "+{v203}"(fi.ae0), "+{v204}"(fi.ae1), "+{v205}"(fi.ae2), "+{v206}"(fi.ae3), "+{v207}"(fi.am), "+{v208}"(fi.lw), "+{v209}"(fi.lr), "+{v210}"(fi.oo), "+{v211}"(fi.d0), "+{v212}"(fi.tq), "+{v213}"(fi.g[0]), "+{v214}"(fi.b[0]), "+{v215}"(fi.c[0]), "+{v216}"(fi.g[1]), "+{v217}"(fi.b[1]), "+{v218}"(fi.c[1]), "+{v219}"(fi.g[2]), "+{v220}"(fi.b[2]), "+{v221}"(fi.c[2]), "+{v222}"(fi.g[3]), "+{v223}"(fi.b[3]), "+{v224}"(fi.c[3]), "+{v225}"(fi.g[4]), "+{v226}"(fi.b[4]), "+{v227}"(fi.c[4]), "+{v228}"(fi.g[5]), "+{v229}"(fi.b[5]), "+{v230}"(fi.c[5])
#define F_ASM_SOPS "s"(fi.ws), "s"(fi.out), "s"(fi.tag), "s"(fi.o_u), "s"(fi.o_e), "s"(fi.o_x), "s"(fi.o_va), "s"(fi.o_tw), "s"(fi.xa), "s"(fi.xb), "s"(fi.o_ctl), "s"(fi.pa), "s"(fi.w), "s"(fi.f_w), "s"(fi.pq)
#define F_ASM_CLOB "v0", "v1", "v2", "v3", "v4", "v5", "v6", "v7", "v8", "v9", "v10", "v11", "v12", "v13", "v14", "v15", "v16", "v17", "v18", "v19", "v20", "v21", "v22", "v23", "v24", "v25", "v26", "v27", "v28", "v29", "v30", "v31", "v32", "v33", "v34", "v35", "v36", "v37", "v38", "v39", "v40", "v41", "v42", "v43", "v44", "v45", "v46", "v47", "v48", "v49", "v50", "v51", "v52", "v53", "v54", "v55", "v56", "v57", "v58", "v59", "v60", "v61", "v62", "v63", "v64", "v65", "v66", "v67", "v68", "v69", "v70", "v71", "v72", "v73", "v74", "v75", "v76", "v77", "v78", "v79", "v80", "v81", "v82", "v83", "v84", "v85", "v86", "v87", "v88", "v89", "v90", "v91", "v92", "v93", "v94", "v95", "v96", "v97", "v98", "v99", "v100", "v101", "v102", "v103", "v104", "v105", "v106", "v107", "v108", "v109", "v110", "v111", "v112", "v113", "v114", "v115", "v116", "v117", "v118", "v119", "v120", "v121", "v122", "v123", "v124", "v125", "v126", "v127", "v128", "v129", "v130", "v131", "v132", "v133", "v134", "v135", "v136", "v137", "v138", "v139", "v140", "v141", "v142", "v143", "v144", "v145", "v146", "v147", "v148", "v149", "v150", "v151", "v152", "v153", "v154", "v155", "v156", "v157", "v158", "v159", "v160", "v161", "v162", "v163", "v164", "v165", "v166", "v167", "v168", "v169", "v170", "v171", "v172", "v173", "v174", "v175", "v176", "v177", "v178", "v179", "v180", "v181", "v182", "v183", "v184", "v185", "v186", "v187", "v188", "v189", "v190", "v191", "v192", "v193", "v194", "v195", "v196", "v197", "v232", "v233", "v234", "v235", "v236", "v237", "a0", "a1", "a2", "a3", "a4", "a5", "a6", "a7", "a8", "a9", "a10", "a11", "a12", "a13", "a14", "a15", "a16", "a17", "a18", "a19", "a20", "a21", "a22", "a23", "a24", "a25", "a26", "a27", "a28", "a29", "a30", "a31", "a32", "a33", "a34", "a35", "a36", "a37", "a38", "a39", "a40", "a41", "a42", "a43", "a44", "a45", "a46", "a47", "a48", "a49", "a50", "a51", "a52", "a53", "a54", "a55", "a56", "a57", "a58", "a59", "a60", "a61", "a62", "a63", "a64", "a65", "a66", "a67", "a68", "a69", "a70", "a71", "a72", "a73", "a74", "a75", "a76", "a77", "a78", "a79", "a80", "a81", "a82", "a83", "a84", "a85", "a86", "a87", "a88", "a89", "a90", "a91", "a92", "a93", "a94", "a95", "a96", "a97", "a98", "a99", "a100", "a101", "a102", "a103", "a104", "a105", "a106", "a107", "a108", "a109", "a110", "a111", "a112", "a113", "a114", "a115", "a116", "a117", "a118", "a119", "a120", "a121", "a122", "a123", "a124", "a125", "a126", "a127", "a128", "a129", "a130", "a131", "a132", "a133", "a134", "a135", "a136", "a137", "a138", "a139", "a140", "a141", "a142", "a143", "a144", "a145", "a146", "a147", "a148", "a149", "a150", "a151", "a152", "a153", "a154", "a155", "a156", "a157", "a158", "a159", "a160", "a161", "a162", "a163", "a164", "a165", "a166", "a167", "a168", "a169", "a170", "a171", "a172", "a173", "a174", "a175", "a176", "a177", "a178", "a179", "a180", "a181", "a182", "a183", "a184", "a185", "a186", "a187", "a188", "a189", "a190", "a191", "s40", "s41", "s42", "s43", "s44", "s45", "s46", "s47", "s48", "s49", "s50", "s51", "s52", "s53", "s54", "s55", "s56", "s57", "s58", "s59", "s60", "s61", "s62", "s63", "s64", "s65", "s66", "s67", "s68", "s69", "s70", "s71", "s72", "s73", "s74", "s75", "s76", "s77", "s78", "s79", "s80", "s81", "s82", "s83", "s84", "s85", "s86", "s87", "s88", "s89", "s90", "s91", "s92", "s93", "s94", "s95", "s96", "s97", "s98", "s99", "vcc", "scc", "memory"

struct RedIn {
  unsigned sidx, cidx, l16, ax, am, mo, sel, to, fu, p0, p1, p2, p3, d0, d1, d2, d3, d4;
  const void* ws;
  unsigned w, go, tag, o_va, o_vb, lab, o_u, kind, o_ctl, pa, pb, f_d, f_r, ph;
};
__device__ __forceinline__ void red_role(const Params& p, int w, int T, char* smem) {
  const int grp = T & 3;
  CODE_PAD(PAD_RED);
  char* const ws = p.ws;
  Ctx cx = {ws, 0u};
  const int l = lane_id(), n = l & 15, g = l >> 4;
  comm_init(cx, grp * 9 + (T >> 2), w, smem);
  Bulk bk;
  bk.init(cx, grp);
  bk.pl.load(cx, grp);
  const Place& pl = bk.pl;
  char* const base = comm_base(ws) - CO;
  const unsigned dl = (unsigned)(ws - base);
  RedIn ri;
  ri.sidx = (n & 8) ? 0xEEEEu : 0x4444u;
  ri.cidx = (n & 3) * 0x5555u;
  ri.l16 = l * 16;
  ri.ax = W_PX * 32768 + (n >> 3) * 512 + g * 128 + (n & 7) * 16;
  ri.am = W_PM * 32768 + l * 16;
  const unsigned mt = (W_MEM + (T >> 4)) * 32768 + (T & 15) * 256;
  ri.mo = mt + (2 * (g & 1) + (g >> 1)) * 64 + (n >> 3) * 32 + (n & 1) * 16 + ((n >> 1) & 3) * 4;
  ri.sel = w ? l >> 5 : (n & 1) ? 0x03020706u : 0x05040100u;
  ri.to = l < 4 ? CO + TW_OFF + (16384u ^ pl.dyn(l)) + 256 : 0x7F000000u;
  ri.fu = CO + pl.near(N_U + min(10 * w - 10 + min(n, 11), w == 3 ? 31 : 10 * w - 1)) + (T < 32 ? (T >> 1) & 3 : T & 3) * 8 + (l & 32);
  ri.d0 = 0; ri.d1 = 0; ri.d2 = 0; ri.d3 = 0; ri.d4 = 0;
  ri.ws = base; ri.w = w; ri.go = pl.go; ri.tag = cx.tag; ri.lab = grp; ri.o_ctl = dl;
  ri.o_va = __builtin_amdgcn_readfirstlane(CO + (win_static(W_VO + 2 * (T >> 2)) ^ pl.dyn(grp)));
  ri.o_vb = __builtin_amdgcn_readfirstlane(CO + (win_static(W_VO + 2 * (T >> 2) + 1) ^ pl.dyn(grp)));
  auto dead = [&](int i) { return i < 0 ? CO + (16384u ^ pl.dyn(g)) + mt + n * 16 : (i < 4 ? ri.o_va : ri.o_vb) + (i & 3) * 1024 + l * 16; };
  ri.p0 = w ? dead(3 * w - 4) : T < 8 && l < 4 ? ri.to - 116 : 0x7F000000u;
  ri.p1 = w ? dead(3 * w - 3) : l < 4 ? 0u : 0x7F000000u; ri.p2 = dead(3 * w - 2);
  const int pz = (T >> 2) * 3 + w - 1;
  ri.p3 = pz < 24 ? CO + (16384u ^ pl.dyn(grp)) + (W_PX + (pz >> 2)) * 32768 + (pz & 3) * 1024 + l * 16 : 0x7F000000u;
  ri.o_u = __builtin_amdgcn_readfirstlane(u_tile(T, 0, 0) ^ (~bk.w13 & 8192u));
  ri.kind = __builtin_amdgcn_readfirstlane(T == 35 ? CO + TW_OFF + (16384u ^ pl.dyn(0)) + 512 : 0u);
  ri.pa = p.vlead << 16;
  ri.pb = p.dret | (p.late_r << 16);
  ri.ph = p.hdel | (p.hdl2 << 8) | 0x10000;
  ri.f_d = __builtin_amdgcn_readfirstlane(T < 32 ? CO + pl.near(N_RD + T) : 0x7F000000u);
  ri.f_r = dl + F_R * 128 + T * 8;
  asm volatile(R_ASM_TEXT : R_ASM_VOPS : R_ASM_SOPS : R_ASM_CLOB);
}

__device__ __forceinline__ int c_line(int h) { return (h >> 2) * 512 + (h & 1) * 256 + (h & 2) * 64; }
struct ChainIn {
  unsigned sidx, l16, qw, kw, fq, fk, kxo, pst, ps2, lx, sid2, qxo, sto, plx, mk, ck0, ck2, hx, viw, fva, pk3, fvs, uo, po0, po1, po2, co0, co1, co2;
  unsigned vq0, vq1, vq2, vq3, vo0, vo1, vo2, vo3, v50, v51, v52, v53, tro0, tro1, tro2, tro3, tro4, m0o;
  const void *ws, *Wa, *Wb, *mem0;
  unsigned w, o_a, o_b, o_nx, o_exp, lab, f_p, f_m, f_dur, f_go, o_ctl, o_dbg, dret, o_qx, f_s, f_r;
};

template <bool SC>
__device__ __forceinline__ void chain_role(const Params& p, int w, int h, int grp) {
  constexpr unsigned QT = 9216, KT = 13312, SX = 16384, VI = 9216, VX = 16384;
  const int l = lane_id(), n = l & 15, g = l >> 4, q4 = (l >> 2) & 3, p4 = l & 3;
  const int ty = (n >> 1) & 1, row = 2 * (n >> 2) + (n & 1), cid = h * 2 + (SC ? 0 : 1);
  char* const base = comm_base(p.ws) - CO;
  const unsigned dl = (unsigned)(p.ws - base);
  ChainIn ci;
  ci.sidx = (n & 2) ? 0xEEEEu : 0x4444u;
  ci.l16 = l * 16;
  ci.m0o = row * (DM * 2) + g * 32 + ty * 16;
  if (SC) {
    ci.qw = QT + (32 * w + n) * 16 + g * 4;
    ci.kw = KT + (32 * w + n) * 16 + (((g & 1) << 1) | (g >> 1)) * 4;
    ci.fq = QT + (4 * g + q4) * 16 + (p4 & 1) * 8;
    ci.fk = KT + (4 * g + q4) * 16 + (p4 >> 1) * 8;
    ci.kxo = (g * 16 + 8 * (q4 & 1) + 4 * (q4 >> 1) + p4) * 16;
    const unsigned ex = CO + win_static(W_PX + (h >> 3)) + (h & 7) * 512 + (g & 1) * 128 + (n & 7) * 16 + (g >> 1) * 8 + (n >> 3);
    if (w < 2) ci.pst = ex + w * 256;
    else if (g & 1) ci.pst = CO + win_static(W_PC) + c_line(h) + (n & 7) * 16 + (((n >> 2) ^ (g >> 1)) & 1) * 8 + 2 * (w & 1) + (n >> 3);
    else ci.pst = CO + win_static(W_PM) + (h >> 1) * 256 + (VO_SWAP ? (g >> 1) * 8 + (h & 1) * 128 : (g >> 1) * 128 + (h & 1) * 8) + (n & 7) * 16 + 2 * (w & 1) + (n >> 3);
    ci.ps2 = w < 2 ? ci.pst + 2 : ex + 2 * (w & 1);
    ci.mk = (g & 1) ? 0u : 0x3F800000u;
    ci.ck0 = g == 1 && !(h & 2) ? 0x3F800000u : 0u;
    ci.ck2 = g == 1 && (h & 2) ? 0x3F800000u : 0u;
    ci.hx = SX + 2048 + (w & 1) * 512 + l * 8;
    ci.lx = SX + l * 8 + (w == 1 ? 4 : 0);
    ci.qxo = (w >> 1) * 1024 + (g * 16 + 8 * (w & 1) + (n & 7)) * 16;
    ci.sto = 0x80000 + h * 256 + (8 * w + (n & 7)) * 8;
    ci.plx = n >= 8 ? 0x7F000000u : w * 16384 + n * 16 + ((g & 1) ? 4096 + c_line(h) + (((n >> 2) ^ (g >> 1)) & 1) * 8 : h * 128 + (g >> 1) * 8);
  } else {
    ci.viw = VI + (32 * w + n) * 16 + g * 4;
    ci.fva = VI + (4 * g + q4) * 16 + (p4 & 1) * 8;
    ci.pk3 = VI + n * 16 + g * 4; ci.fvs = w * 1024 + l * 16;
    ci.uo = w == 1 ? l * 16 : 0x7F000000u;
    auto vo_at = [&](int T0) {
      const int T = T0 + 2 * w + (g >> 1);
      return CO + win_static(W_VO + 2 * (T >> 2) + (h >> 4)) + (h & 15) * 256 + n * 16 + (T & 3);
    };
    ci.po0 = vo_at(8 * (g & 1)); ci.po1 = vo_at(16 + 8 * (g & 1));
    auto co_at = [&](int T0) { return (T0 + 2 * w + (g >> 1)) * 16384 + h * 256 + n * 16; };
    ci.co0 = co_at(8 * (g & 1)); ci.co1 = co_at(16 + 8 * (g & 1)); ci.co2 = co_at(w == 0 ? 16 : w == 1 ? 22 : 28) + (g & 1) * 8;
    if (w == 0) ci.po1 = h < 4 && l < 4 ? CO + TW_OFF + 16384 + 384 + l : 0x7F000000u;
    ci.po2 = vo_at(w == 0 ? 16 : w == 1 ? 22 : 28) + (g & 1) * 8;
  }
  ci.sid2 = (n & 8) ? 0xEEEEu : 0x4444u;
  unsigned vq[4], vo[4], v5[4];
#pragma unroll
  for (int i = 0; i < 4; ++i) {
    const int ch = n ^ (2 * ((g & 1) | (i << 1))), tp = ch >> 1;
    vq[i] = (g + 4 * i) * (HD * 2) + ch * 16 - 1024 * i;
    vo[i] = (g + 4 * i) * (DM * 2) + ch * 16 - 1024 * i;
    v5[i] = (g + 4 * i + 16 - 16 * (tp >> 2)) * (DM * 2) + (32 + (tp & 3)) * 32 + (ch & 1) * 16 - 1024 * i;
  }
  ci.vq0 = vq[0]; ci.vq1 = vq[1]; ci.vq2 = vq[2]; ci.vq3 = vq[3];
  ci.vo0 = vo[0]; ci.vo1 = vo[1]; ci.vo2 = vo[2]; ci.vo3 = vo[3];
  ci.v50 = v5[0]; ci.v51 = v5[1]; ci.v52 = v5[2]; ci.v53 = v5[3];
  {
    const int rowl = 16 * (g >> 1) + 8 * (q4 >> 1) + 4 * (g & 1) + (q4 & 1), rho = (rowl & 1) | (((rowl >> 2) & 3) << 1);
    ci.tro0 = rowl * 256 + (((4 * w + (p4 >> 1)) ^ (2 * rho)) * 16) + (p4 & 1) * 8;
    ci.tro1 = rowl * 256 + (((4 * w + 2 + (p4 >> 1)) ^ (2 * rho)) * 16) + (p4 & 1) * 8;
    const int row2 = 16 * (g & 1) + 4 * (g >> 1) + (q4 & 1), rho2 = (row2 & 1) | (((row2 >> 2) & 3) << 1);
    ci.tro2 = row2 * 256 + (((4 * w + 2 * (q4 >> 1) + (p4 >> 1)) ^ (2 * rho2)) * 16) + (p4 & 1) * 8;
    ci.tro3 = w < 2 ? (ci.tro2 ^ (w * 64)) + 7 * 16384 : ci.tro2 + 9 * 16384;
    ci.tro4 = w < 2 ? ci.tro3 + 16384 : ci.tro3 ^ 128u;
  }
  ci.ws = base; ci.Wa = SC ? p.Wq : p.Wv; ci.Wb = SC ? p.Wk : p.Wo; ci.mem0 = p.mem0;
  ci.w = w;
  ci.o_a = 16 * w * (HD * 2) + h * 256;
  ci.o_b = (h * DH + 32 * w) * (DM * 2);
  ci.o_nx = near_head(B_K, h) + (w & 1) * 4096;
  ci.o_exp = near_head(B_K, h) + (SC ? 0 : 4096);
  ci.o_qx = PB + 2 * B_O + (SC ? h * 16384 : 0); ci.f_s = __builtin_amdgcn_readfirstlane(dl + (F_S + h) * 128);
  ci.f_r = dl + F_R * 128;
  ci.lab = grp;
  ci.f_p = __builtin_amdgcn_readfirstlane(CO + near_static(N_K + h) + (SC ? 0 : 8));
  ci.f_m = SC ? dl + (F_M + cid) * 128 + (w & 1) * 8 : w == 1 ? dl + (F_M + cid) * 128 : 0x7F000000u; ci.f_dur = __builtin_amdgcn_readfirstlane(CO + near_static(N_DUR + h));
  ci.f_go = dl + (F_GO + grp) * 128; ci.o_ctl = dl;
  ci.o_dbg = dl + (unsigned)WS_END + (cid * 4 + w) * 4096;
  ci.dret = p.dret | (p.late_s << 16);
  if (SC) asm volatile(X_ASM_TEXT M_ASM_TEXT : X_ASM_VOPS : X_ASM_SOPS : X_ASM_CLOB);
  else asm volatile(W_ASM_TEXT V_ASM_TEXT : V_ASM_VOPS : V_ASM_SOPS : V_ASM_CLOB);
}

struct ProjIn {
  unsigned l16, xr0, xr1, ew, er, uo, xs0, xs1, xs2, xs3, xs4, xs5, xs6, xs7, xs8;
  unsigned vq0, vq1, vq2, vq3, vo0, vo1, vo2, vo3, v50, v51, v52, v53, tro0, tro1;
  const void *ws, *Wa, *Wb, *x;
  unsigned w, o_a, o_b, o_p, o_q, f_a, f_b, f_go, o_ctl, f_c, u8, rng, f_d;
};
template <bool VU, int QO = 0>
__device__ __forceinline__ unsigned proj_role(const Params& p, int w, int h) {
  const int l = lane_id(), n = l & 15, g = l >> 4, q4 = (l >> 2) & 3, p4 = l & 3;
  char* const base = comm_base(p.ws) - CO;
  const unsigned dl = (unsigned)(p.ws - base);
  ProjIn pi;
  pi.l16 = l * 16; pi.uo = 0;
  unsigned xs[9];
#pragma unroll
  for (int i = 0; i < 9; ++i) {
    const int ci = (9 * w + i) * 64 + l, r = ci / 72, cc = ci - r * 72;
    xs[i] = (r * 72 + (cc ^ ((r >> 1) & 7))) * 16;
  }
  pi.xs0 = xs[0]; pi.xs1 = xs[1]; pi.xs2 = xs[2]; pi.xs3 = xs[3]; pi.xs4 = xs[4]; pi.xs5 = xs[5]; pi.xs6 = xs[6]; pi.xs7 = xs[7]; pi.xs8 = xs[8];
  pi.xr0 = 16384 + n * 1152 + ((g ^ ((n >> 1) & 7)) * 16); pi.xr1 = 16384 + n * 1152 + (((4 + g) ^ ((n >> 1) & 7)) * 16);
  pi.ew = w * 2048 + n * 64 + ((g ^ ((n >> 1) & 1) ^ ((n >> 2) & 2)) * 16);
  pi.er = 8192 + (g >> 1) * 1032 + (8 * (g & 1) + q4) * 64 + ((p4 ^ (q4 >> 1) ^ (2 * (g & 1))) * 16);
  unsigned vq[4], vo[4], v5[4];
#pragma unroll
  for (int i = 0; i < 4; ++i) {
    const int ch = n ^ (2 * (g | ((i >> 1) << 2))), tp = ch >> 1;
    vq[i] = (g + 4 * i) * (HD * 2) + ch * 16 - 1024 * i;
    vo[i] = (g + 4 * i) * (DM * 2) + ch * 16 - 1024 * i;
    v5[i] = (g + 4 * i + 64 * (tp & 1)) * (DM * 2) + (32 + (tp >> 1)) * 32 + (ch & 1) * 16 - 1024 * i;
  }
  pi.vq0 = vq[0]; pi.vq1 = vq[1]; pi.vq2 = vq[2]; pi.vq3 = vq[3];
  pi.vo0 = vo[0]; pi.vo1 = vo[1]; pi.vo2 = vo[2]; pi.vo3 = vo[3];
  pi.v50 = v5[0]; pi.v51 = v5[1]; pi.v52 = v5[2]; pi.v53 = v5[3];
  const int rho = q4 | ((g & 1) << 2);
  pi.tro0 = (8 * g + q4) * 256 + (((4 * w + (p4 >> 1)) ^ (2 * rho)) * 16) + (p4 & 1) * 8;
  pi.tro1 = (8 * g + q4) * 256 + (((4 * w + 2 + (p4 >> 1)) ^ (2 * rho)) * 16) + (p4 & 1) * 8;
  pi.ws = base; pi.Wa = VU ? p.Wv : QO ? p.Wq : p.Wk; pi.Wb = VU ? p.Wo : p.Wq; pi.x = p.x;
  pi.w = w;
  pi.o_a = 16 * w * (HD * 2) + h * 256;
  pi.o_b = (h * DH + 16 * w) * (DM * 2);
  pi.o_p = PB + 2 * (VU ? B_V : QO ? B_Q : B_K) + h * 16384 + w * 2048 + (QO == 2 ? 0x40000000u : VU && QO ? 524288 : 0);
  pi.o_q = VU ? u_tile(2 * w, h >> 3, h & 7) ^ 8192u ^ (QO == 2 ? 128u << 20 : 0u) : PB + 2 * B_Q + h * 16384 + w * 2048 + (K_FIRST - 1) * 524288;
  pi.f_a = __builtin_amdgcn_readfirstlane(w || (QO && !VU) || QO == 2 ? 0x7F000000u : CO + near_static(N_K + h) + (VU ? 8 + 8 * QO : 0));
  pi.f_b = __builtin_amdgcn_readfirstlane(VU ? CO + near_static(N_U + h) + w * 8 + 32 * QO : w || QO ? 0x7F000000u : dl + (F_Q + h) * 128);
  pi.f_go = dl + (F_GO + 4 + (h & 3)) * 128; pi.o_ctl = dl;
  pi.f_c = VU ? dl + (F_M + 2 * h + 1) * 128 : QO ? 77 : 78;
  pi.f_d = __builtin_amdgcn_readfirstlane(QO == 1 ? 0x7F000000u : dl + (F_U + h) * 128 + w * 8 + 16 * QO);
  pi.rng = QO == 2 ? U_LAST | NSEG << 8 | 99 << 16 : (QO ? 1 | (2 * U_HELP + 1) << 8 : U_LAST << 8) | U_LEAD << 16;
  pi.u8 = QO && !VU ? 0x7F000000u : (VU ? u_tile(32 + w, h >> 3, h & 7) : near_head(B_K, h) + w * 2048) ^ 8192u ^ (QO == 2 ? 128u << 20 : 0u);
  unsigned tag;
  if (!VU) sleep_n(QO ? p.q0 : p.pj0);
  if (VU) asm volatile(U_ASM_TEXT : P_ASM_VOPS : P_ASM_SOPS : P_ASM_CLOB);
  else asm volatile(K_ASM_TEXT : P_ASM_VOPS : P_ASM_SOPS : P_ASM_CLOB);
  return tag;
}

constexpr int NS4 = 67;
__device__ __forceinline__ unsigned y_at(const Bulk& bk, int quad, unsigned o) { return bk.at(B_YT + quad * 16384 + o) ^ (quad >= Y_NEAR && quad < Y_FAR ? 8192u : 0u); }
__device__ __forceinline__ void mix_role(const Params& p, unsigned tag, int w, int ct, int s0, int ds, int s1, bool late) {
  const int l = lane_id(), n = l & 15, g = l >> 4;
  const Ctx cx = {p.ws, tag};
  Bulk bk;
  bk.init(cx, 4 + (ct & 3));
  const int tr = (w >> 1) * 16 + n, dc = ct * 96 + (w & 1) * 48 + 4 * g;
  const rsrc_t rx = mk_rsrc(p.x, (size_t)SL * DM * 2), rout = mk_rsrc(p.out, (size_t)SL * DM * 2);
  const int ro = (((w >> 1) * 16 + (l >> 2)) * DM + ct * 96 + (w & 1) * 48 + 4 * (l & 3)) * 2, pin = (4 * n + g) * 4, pout = (16 * (l & 3) + (l >> 2)) * 4;
  auto perm = [](int a, u32x2 v) { return (u32x2){(unsigned)__builtin_amdgcn_ds_bpermute(a, v[0]), (unsigned)__builtin_amdgcn_ds_bpermute(a, v[1])}; };
  u32x2 dv[3];
  unsigned yo[3];
#pragma unroll
  for (int i = 0; i < 3; ++i) { dv[i] = *(const u32x2*)(p.dsk + dc + 16 * i); yo[i] = y_at(bk, (dc + 16 * i) >> 2, tr * 8); }
  struct In { u32x4 a[4]; u32x2 b[4]; u32x2 x[3], y[3]; float g; bool ok; };
  auto ldb = [&](In& in, int sg) {
    in.g = __uint_as_float(__builtin_amdgcn_raw_buffer_load_b32(bk.rs, tr * 4, bk.at(B_G) + sg * 128, 16));
#pragma unroll
    for (int i = 0; i < 3; ++i) {
      in.y[i] = __builtin_amdgcn_raw_buffer_load_b64(bk.rs, yo[i], (sg >> 5) * 16384 + (sg & 31) * 256, 16);
      in.x[i] = __builtin_amdgcn_raw_buffer_load_b64(rx, ro + 32 * i, sg * (32 * DM * 2), 0);
    }
  };
  int seen = 0;
  const int ob0 = late ? NSEG : p.ob0, obn = p.ob1 - p.ob0, fa = F_A + (late ? 24 : 0) + ct * 4;
  auto seg = [=](int i) { return min(i < ob0 ? i : i + obn, NSEG - 1); };
  auto lda = [&](In& in, int sg) {
    in.ok = seen > sg;
    const unsigned l8 = in.ok ? l * 8 : 0x3F000000u;
#pragma unroll
    for (int kq = 0; kq < 4; ++kq) {
      const unsigned o = bk.at(B_AP + ((sg * 6 + ct) * 4 + kq) * 8192 + w * 2048);
      in.a[kq] = ld128(bk.rs, 2 * l8, o);
      in.b[kq] = __builtin_amdgcn_raw_buffer_load_b64(bk.rs, l8, o + 1024, 16);
    }
  };
  auto step = [&](In& in, In& n2, int k) {
    const int sg = seg(k);
    if (!in.ok) {
      wait_ge_n<2>(cx, fa, 1, 4, sg + 1, seen);
      lda(in, sg);
#pragma unroll
      for (int i = 0; i < 4; ++i) asm volatile("" : "+v"(in.a[i]), "+v"(in.b[i]));
    }
    ldb(n2, seg(k + 2 * ds));
    lda(n2, seg(k + 2 * ds));
    SB();
#pragma unroll
    for (int i = 0; i < 3; ++i) {
      float o[4], b[4];
      const u32x2 xv = perm(pin, in.x[i]);
#pragma unroll
      for (int j = 0; j < 4; ++j) {
        auto part = [&](int kq) { const unsigned u = i < 2 ? in.a[kq][2 * (i & 1) + (j >> 1)] : in.b[kq][j >> 1]; return (j & 1) ? hi16(u) : lo16(u); };
        const float av = (part(0) + part(1)) + (part(2) + part(3));
        const float y = (j & 1) ? hi16(in.y[i][j >> 1]) : lo16(in.y[i][j >> 1]);
        const float xf = (j & 1) ? hi16(xv[j >> 1]) : lo16(xv[j >> 1]), df = (j & 1) ? hi16(dv[i][j >> 1]) : lo16(dv[i][j >> 1]);
        o[j] = rbf(in.g * rbf(av));
        b[j] = rbf(1.0f - in.g) * rbf(y + rbf(xf * df));
      }
      u32x2 bp = {pk2(b[0], b[1]), pk2(b[2], b[3])};
      asm volatile("" : "+v"(bp));
#pragma unroll
      for (int j = 0; j < 4; ++j) o[j] += (j & 1) ? hi16(bp[j >> 1]) : lo16(bp[j >> 1]);
      __builtin_amdgcn_raw_buffer_store_b64(perm(pout, (u32x2){pk2(o[0], o[1]), pk2(o[2], o[3])}), rout, ro + 32 * i, sg * (32 * DM * 2), OUT_AUX);
    }
    return seg(k + ds) < s1;
  };
  wait_all<30>(cx, F_W, 1);
  In a, b, c;
  wait_ge_n<2>(cx, fa, 1, 4, 1, seen);
  ldb(a, seg(s0)); lda(a, seg(s0));
  ldb(b, seg(s0 + ds)); lda(b, seg(s0 + ds));
  for (int sg = s0; step(a, c, sg) && step(b, a, sg + ds) && step(c, b, sg + 2 * ds); sg += 3 * ds) {}
}

__device__ __forceinline__ void gate_rows(const Params& p, const Ctx& cx, const Bulk& bk, int blk) {
  const int l = lane_id(), n = l & 15, g = l >> 4;
  const rsrc_t rx = mk_rsrc(p.x, (size_t)SL * DM * 2), rg = mk_rsrc(p.gw, DM * 2);
  const int t = blk * 16 + g;
  u32x4 gw[4], xv[4][4];
  u32x2 gt, xt[4];
#pragma unroll
  for (int k = 0; k < 4; ++k) gw[k] = __builtin_amdgcn_raw_buffer_load_b128(rg, n * 16, k * 256, 0);
  gt = __builtin_amdgcn_raw_buffer_load_b64(rg, n * 8, 1024, 0);
#pragma unroll
  for (int r = 0; r < 4; ++r) {
#pragma unroll
    for (int k = 0; k < 4; ++k) xv[r][k] = __builtin_amdgcn_raw_buffer_load_b128(rx, t * (DM * 2) + n * 16, r * 4 * (DM * 2) + k * 256, 0);
    xt[r] = __builtin_amdgcn_raw_buffer_load_b64(rx, t * (DM * 2) + n * 8, r * 4 * (DM * 2) + 1024, 0);
  }
  SB();
#pragma unroll
  for (int r = 0; r < 4; ++r) {
    float acc = 0.f;
    auto term = [&](unsigned a, unsigned b) { const unsigned u = pk2(lo16(a) * lo16(b), hi16(a) * hi16(b)); acc += lo16(u) + hi16(u); };
#pragma unroll
    for (int k = 0; k < 4; ++k)
#pragma unroll
      for (int j = 0; j < 4; ++j) term(xv[r][k][j], gw[k][j]);
#pragma unroll
    for (int j = 0; j < 2; ++j) term(xt[r][j], gt[j]);
#pragma unroll
    for (int m = 8; m >= 1; m >>= 1) acc += __shfl_xor(acc, m);
    const float gg = rbf(1.0f / (1.0f + expf(-rbf(acc))));
    if (n == 0) __builtin_amdgcn_raw_buffer_store_b32(__float_as_uint(gg), bk.rs, (t + 4 * r) * 4, bk.at(B_G), 16);
  }
  WAIT_VM(0);
  if (l == 0) flag_put(cx, flag_ptr(p.ws, F_G) + blk, 1u);
}

__device__ __forceinline__ void att_role(const Params& p, unsigned tag, int w, int h, int sa, int sb) {
  CODE_PAD(PAD_ATT);
  char* const ws = p.ws;
  const int l = lane_id(), n = l & 15, g = l >> 4;
  const Ctx cx = {ws, tag};
  Bulk bk;
  bk.init(cx, 4 + (h & 3));
  const unsigned l16 = l * 16, vmo = (n + 16 * (((g & 1) << 1) | (g >> 1))) * 16;
  const float scale = 0.08838834764831845f;
  auto rnd2 = [&](float a, float b, float& ra, float& rb) {
    unsigned u = pk2(a, b);
    u = pk2(lo16(u) * scale, hi16(u) * scale);
    ra = lo16(u); rb = hi16(u);
  };
  auto soft = [&](const f32x4* st, int qi, u32x4& px, u32x2& pm) {
    const float neg = rbf(-1000000000.0f);
    float v12[12];
    rnd2((g & 1) ? st[0][2] : st[0][0], (g & 1) ? st[0][3] : st[0][1], v12[0], v12[1]);
    v12[2] = v12[0]; v12[3] = v12[1];
#pragma unroll
    for (int j = 0; j < 4; j += 2) { rnd2(st[1][j], st[1][j + 1], v12[4 + j], v12[5 + j]); rnd2(st[2][j], st[2][j + 1], v12[8 + j], v12[9 + j]); }
#pragma unroll
    for (int j = 0; j < 4; ++j) {
      v12[4 + j] = (4 * g + j <= qi) ? v12[4 + j] : neg;
      v12[8 + j] = (16 + 4 * g + j <= qi) ? v12[8 + j] : neg;
    }
    float mx = v12[0];
#pragma unroll
    for (int j = 1; j < 12; ++j) mx = fmaxf(mx, v12[j]);
    mx = red_max_g(mx);
#pragma unroll
    for (int j = 0; j < 12; ++j) v12[j] = __builtin_amdgcn_exp2f((v12[j] - mx) * 1.4426950408889634f);
    float sum = v12[0] + v12[1];
#pragma unroll
    for (int j = 4; j < 12; ++j) sum += v12[j];
    sum = red_sum_g(sum);
    const float inv = __builtin_amdgcn_rcpf(sum);
    px = (u32x4){pk2(v12[4] * inv, v12[5] * inv), pk2(v12[6] * inv, v12[7] * inv), pk2(v12[8] * inv, v12[9] * inv), pk2(v12[10] * inv, v12[11] * inv)};
    pm = (u32x2){pk2(v12[0] * inv, v12[1] * inv), 0u};
  };
  int seenp = 0, seenm = 0;
  auto wait_proj = [&](int target) {
    unsigned spins = 0;
    while (seenp < target) {
      const int v = (int)flag_get(cx, bk.proj_flag(cx, min(l, 3), h)), v2 = __builtin_amdgcn_readlane(v, 3);
      seenp = min(min(max(__builtin_amdgcn_readlane(v, 0), K_FIRST), __builtin_amdgcn_readlane(v, 1)), min(__builtin_amdgcn_readlane(v, 2), v2 < 2 * U_HELP ? v2 : NSEG));
      if (seenp < target) { __builtin_amdgcn_s_sleep(4); if (spin_fail(spins, cx)) break; }
    }
    __builtin_amdgcn_fence(__ATOMIC_ACQUIRE, "wavefront");
  };
  GAS u64* const fm = flag_ptr(ws, F_M + 2 * h + ((l >> 1) & 1)) + (l & 1 & ~(l >> 1));
  auto min8 = [&](u64 f) {
    int v = (unsigned)(f >> 32) == cx.tag ? (int)(unsigned)f : 0;
#pragma unroll
    for (int m = 1; m < 4; m <<= 1) v = min(v, __shfl_xor(v, m));
    return __builtin_amdgcn_readfirstlane(v);
  };
  auto wait_m = [&](int target) {
    unsigned spins = 0;
    while (seenm < target) {
      seenm = min8(__hip_atomic_load(fm, RLX_AGENT));
      if (seenm < target) { __builtin_amdgcn_s_sleep(2); if (spin_fail(spins, cx)) break; }
    }
    __builtin_amdgcn_fence(__ATOMIC_ACQUIRE, "wavefront");
  };
  bf16x8 qn[4][2], kx[4][2], vn[8];
  auto ahead = [&](int s) {
    const unsigned so = (s * NH + h) * 8192, oq = bk.at(B_Q + so), ok = bk.at(B_K + so), ov = bk.at(B_V + so);
#pragma unroll
    for (int k = 0; k < 8; ++k) {
      qn[k >> 1][k & 1] = ldfrag(bk.rs, l16, oq + k * 1024);
      kx[k >> 1][k & 1] = ldfrag(bk.rs, l16, ok + k * 1024);
    }
#pragma unroll
    for (int et = 0; et < 8; ++et) vn[et] = ldfrag(bk.rs, l16, ov + et * 1024);
  };
  bf16x8 km[4];
  u32x2 vm[4];
  auto ldm = [&](int s) {
    const unsigned om = near_head(B_K, h) ^ (~bk.w13 & 8192u) ^ ((s ? s - 1 : 8192) << 14);
#pragma unroll
    for (int ks = 0; ks < 4; ++ks) km[ks] = ldfrag(bk.rs, l16, om + ks * 1024);
#pragma unroll
    for (int k = 0; k < 4; ++k) vm[k] = __builtin_amdgcn_raw_buffer_load_b64(bk.rs, vmo, om + 4096 + k * 1024, 16);
  };
  if (w && !sa && l == 0) flag_put(cx, flag_ptr(ws, F_O + h) + w, w);
  wait_proj(sa + w + 1);
  ahead(sa + w);
  wait_m(sa + w + 1);
  ldm(sa + w);
  bool pend = false, fq = false, gate = true;
  u64 fw = 0;
  for (int s = sa + w; s < sb; s += 4) {
    bf16x8 vx[8];
    u32x2 vc[4];
    f32x4 st[2][3] = {};
#pragma unroll
    for (int ks = 0; ks < 4; ++ks) { st[0][1] = MF(kx[ks][0], qn[ks][0], st[0][1]); st[1][1] = MF(kx[ks][0], qn[ks][1], st[1][1]); st[1][2] = MF(kx[ks][1], qn[ks][1], st[1][2]); }
#pragma unroll
    for (int ks = 0; ks < 4; ++ks) { st[0][0] = MF(km[ks], qn[ks][0], st[0][0]); st[1][0] = MF(km[ks], qn[ks][1], st[1][0]); vc[ks] = vm[ks]; vx[ks] = vn[ks]; vx[ks + 4] = vn[ks + 4]; }
    SB();
    if (fq) {
      seenm = max(seenm, min8(fw));
      __builtin_amdgcn_fence(__ATOMIC_ACQUIRE, "wavefront");
    }
    const bool more = s + 4 < sb;
    bool have = more && seenm >= s + 5 && seenp >= s + 5;
    fq = have;
    if (have) {
      ldm(s + 4);
      fw = __hip_atomic_load(fm, RLX_AGENT);
      ahead(s + 4);
    }
    SB();
    const unsigned oo = bk.at(B_O + (s * NH + h) * 8192);
#pragma unroll
    for (int qt = 0; qt < 2; ++qt) {
      u32x4 px;
      u32x2 pm;
      soft(st[qt], qt * 16 + n, px, pm);
      const u32x4 pxs = {px[2], px[3], px[0], px[1]};
      f32x4 o[8];
#pragma unroll
      for (int et = 0; et < 8; ++et) {
        const f32x4 z = {};
        o[et] = MF(vx[et], __builtin_bit_cast(bf16x8, (et & 1) ? pxs : px), z);
        o[et] = MF(zext8((u32x2){vc[et >> 1][et & 1], 0u}), zext8(pm), o[et]);
      }
      if (qt == 0 && pend) {
        if (have) WAIT_VM(24); else WAIT_VM(0);
        if (l == 0) flag_put(cx, flag_ptr(ws, F_O + h) + w, s);
      }
#pragma unroll
      for (int k = 0; k < 4; ++k) st128(pack8(o[2 * k], o[2 * k + 1]), bk.rs, l16, oo + qt * 1024 + k * 2048);
    }
    SB();
    pend = true;
    if (!have) {
      pend = false;
      WAIT_VM(0);
      if (l == 0) flag_put(cx, flag_ptr(ws, F_O + h) + w, s + 4);
      if (gate && (s >= 20 || !more)) { gate_rows(p, cx, bk, 4 * h + w); gate = false; }
      if (more) { wait_proj(s + 5); ahead(s + 4); wait_m(s + 5); ldm(s + 4); }
    }
  }
}

__device__ __forceinline__ void xprep_role(const Params& p, unsigned tag, int w, int h0) {
  const int l = lane_id(), n = l & 15, g = l >> 4, qt = w & 1, par = w >> 1;
  const Ctx cx = {p.ws, tag};
  Bulk bk;
  bk.init(cx, 4 + (h0 & 3));
  const unsigned l16 = l * 16;
  const unsigned xo = X_AT + ((bk.w13 ^ 8192u) & 8192u);
  for (int i = 0; i < NDIR * 9; ++i)
    st128((u32x4){~0u, ~0u, ~0u, ~0u}, bk.rs, g * 16384 + (h0 + 8 * w) * 256 + n * 16, (bk.at(B_O + (i / 9) * 524288) ^ 8192u) + (i % 9) * 65536);
  for (unsigned spins = 0; __any(flag_get(cx, (l & 4) ? flag_ptr(p.ws, F_Q + h0 + 8 * (l & 3)) : flag_ptr(p.ws, F_Y) + 35 + h0 + 8 * (l & 3)) == 0u) && !spin_fail(spins, cx);) __builtin_amdgcn_s_sleep(8);
  __builtin_amdgcn_fence(__ATOMIC_ACQUIRE, "wavefront");
#pragma unroll 1
  for (int h = h0; h < NH; h += 8) {
  bf16x8 qq[NDIR / 2][4], kk[NDIR / 2][8];
#pragma unroll
  for (int i2 = 0; i2 < NDIR; i2 += 2) {
    const unsigned so = ((SDIR + i2 + par) * NH + h) * 8192, oq = bk.at(B_Q + so) + qt * 1024, ok = bk.at(B_K + so);
#pragma unroll
    for (int ks = 0; ks < 4; ++ks) {
      qq[i2 >> 1][ks] = ldfrag(bk.rs, l16, oq + ks * 2048);
      kk[i2 >> 1][2 * ks] = ldfrag(bk.rs, l16, ok + ks * 2048); kk[i2 >> 1][2 * ks + 1] = ldfrag(bk.rs, l16, ok + ks * 2048 + 1024);
    }
  }
  SB();
#pragma unroll
  for (int i2 = 0; i2 < NDIR; i2 += 2) {
    const int i = i2 + par;
    const unsigned oc = (bk.at(B_O + ((SDIR + i) * NH + h) * 8192) ^ 8192u) + qt * 1024;
    f32x4 a = {}, b = {};
#pragma unroll
    for (int ks = 0; ks < 4; ++ks) {
      const bf16x8 q = qq[i2 >> 1][ks];
      st128(__builtin_bit_cast(u32x4, q), bk.rs, l16, oc + ks * 2048);
      a = MF(kk[i2 >> 1][2 * ks], q, a); b = MF(kk[i2 >> 1][2 * ks + 1], q, b);
    }
    float mx = -__builtin_inff(), sum = 0.f;
#pragma unroll
    for (int j = 0; j < 4; ++j) {
      a[j] = 4 * g + j <= 16 * qt + n ? a[j] : -__builtin_inff(); b[j] = 16 + 4 * g + j <= 16 * qt + n ? b[j] : -__builtin_inff();
      mx = fmaxf(mx, fmaxf(a[j], b[j]));
    }
    const float c = 0.08838834764831845f * 1.4426950408889634f, nx = -red_max_g(mx) * c;
#pragma unroll
    for (int j = 0; j < 4; ++j) { a[j] = __builtin_amdgcn_exp2f(__builtin_fmaf(a[j], c, nx)); b[j] = __builtin_amdgcn_exp2f(__builtin_fmaf(b[j], c, nx)); sum += a[j] + b[j]; }
    sum = red_sum_g(sum);
    const unsigned eo = xo + 0x100000 + (i * 4 + 2 * qt + (n >> 3)) * 32768 + (h >> 4) * 16384 + (h & 15) * 512 + (g >> 1) * 128 + (n & 7) * 16 + (g & 1) * 8;
    __builtin_amdgcn_raw_buffer_store_b64((u32x2){pk2(a[0], a[1]), pk2(a[2], a[3])}, bk.rs, eo, 0, 16);
    __builtin_amdgcn_raw_buffer_store_b64((u32x2){pk2(b[0], b[1]), pk2(b[2], b[3])}, bk.rs, eo, 256, 16);
    if (g == 0) __builtin_amdgcn_raw_buffer_store_b64((u32x2){__float_as_uint(nx), __float_as_uint(sum)}, bk.rs, h * 256 + (16 * qt + n) * 8, xo + 0x80000 + i * 65536, 16);
    __builtin_amdgcn_raw_buffer_store_b64((u32x2){0xFFFFFFFFu, 0xFFFFFFFFu}, bk.rs, (2 * qt + (g >> 1)) * 16384 + ((g & 1) ? 4096 + c_line(h) : h * 128) + n * 8, xo + i * 65536, 16);
  }
  }
  WAIT_VM(0);
  BARRIER();
  if (l == 0) flag_put(cx, flag_ptr(p.ws, F_S + h0 + 8 * w), 1u);
}

struct OutIn {
  unsigned l16, rw, rr, fo, wv0, wv1, wv2, tro, l8, rw0, rr0;
  const void *ws, *Wo;
  unsigned w, tag, o_w, o_o, o_a, f_a, o_ctl, ns;
};
__device__ __forceinline__ void op_role(const Params& p, unsigned tag, int w, int ct, int kq, int grp, bool late) {
  const Ctx cx = {p.ws, tag};
  Bulk bk;
  bk.init(cx, grp);
  const int l = lane_id(), g = l >> 4, q4 = (l >> 2) & 3, p4 = l & 3, h0 = kq * 8 + w * 2;
  char* const base = comm_base(p.ws) - CO;
  const unsigned dl = (unsigned)(p.ws - base);
  OutIn oi;
  oi.l16 = l * 16; oi.l8 = l * 8;
  oi.rw = 65536 + w * 12288 + l * 16;
  oi.rr = 65536 + w * 3072 + l * 16;
  oi.fo = dl + (F_O + h0 + ((l >> 2) & 1)) * 128 + (l & 3) * 8;
  auto piece = [&](int i) { const int idx = i * 64 + l, row = idx / 12; return row * (DM * 2) + (idx - row * 12) * 16; };
  oi.wv0 = piece(0); oi.wv1 = piece(1); oi.wv2 = piece(2);
  oi.tro = w * 24576 + (4 * g + q4) * 192 + p4 * 8;
  oi.ws = base; oi.Wo = p.Wo; oi.w = w; oi.tag = __builtin_amdgcn_readfirstlane(tag); oi.o_ctl = dl;
  oi.ns = __builtin_amdgcn_readfirstlane(late ? p.ob0 | p.ob1 << 8 : p.ob0 << 8 | p.ob1 << 16 | SDIR << 24);
  oi.rw0 = oi.rw; oi.rr0 = oi.rr;
  oi.o_w = __builtin_amdgcn_readfirstlane((kq * 1024 + w * 256) * (DM * 2) + ct * 192);
  oi.o_o = __builtin_amdgcn_readfirstlane(bk.w13 + 2 * B_O + h0 * 16384);
  oi.o_a = __builtin_amdgcn_readfirstlane(bk.w13 + 2 * B_AP + (ct * 4 + kq) * 16384 + w * 2048);
  oi.f_a = __builtin_amdgcn_readfirstlane(w ? 0x7F000000u : dl + (F_A + ct * 4 + kq) * 128 + (late ? 24 * 128 : 0));
  asm volatile(O_ASM_TEXT : O_ASM_VOPS : O_ASM_SOPS : O_ASM_CLOB);
}

typedef __attribute__((ext_vector_type(16))) float f32x16;
constexpr int S4_PRM = 0;
constexpr int S4_KD = S4_PRM + 2560;
constexpr int S4_KH = S4_KD + 2624;
constexpr int S4_U = S4_KH + 5200;
constexpr int S4_USTR = 4352 + 64;
constexpr int S4_WSZ = S4_U + 4 * S4_USTR;
constexpr int S4_RAW = 4 * S4_WSZ;

#define S4M(i) do {} while (0)
struct S4In { u32x2 u[8]; unsigned la, ai, cr, ci, dt; };
__device__ __forceinline__ float s4_params(LAS char* my, const S4In& in) {
  const int l = lane_id();
  const u16 pl_[2] = {(u16)in.la, (u16)(in.la >> 16)}, pa_[2] = {(u16)in.ai, (u16)(in.ai >> 16)}, pr_[2] = {(u16)in.cr, (u16)(in.cr >> 16)}, pi_[2] = {(u16)in.ci, (u16)(in.ci >> 16)};
  const float dt = rbf(expf(bf2f((u16)in.dt)));
  f32x4 pr[2];
#pragma unroll
  for (int k = 0; k < 2; ++k) {
    const float a = -rbf(expf(bf2f(pl_[k]))) * dt;
    const float b = bf2f(pa_[k]) * dt;
    const float c_hi = 0.15915494f, c_lo = (float)(0.15915494309189535 - (double)0.15915494f);
    const float bh = b * c_hi;
    const float bl = __builtin_fmaf(b, c_hi, -bh) + b * c_lo;
    const float cr = bf2f(pr_[k]), ci = bf2f(pi_[k]);
    pr[k] = (f32x4){a * 1.4426950408889634f, bh, bl, log2f(hypotf(cr, ci))};
    *(LAS float*)(my + S4_PRM + 2048 + (l + 64 * k) * 4) = -atan2f(ci, cr) * c_hi;
  }
  float top = fmaxf(fmaxf(pr[0][3], pr[1][3]), -100.f);
#pragma unroll
  for (int m = 1; m < 64; m <<= 1) top = fmaxf(top, __shfl_xor(top, m));
  top = ceilf(top);
#pragma unroll
  for (int k = 0; k < 2; ++k) { pr[k][3] += 14.f - top; *(LAS f32x4*)(my + S4_PRM + (l + 64 * k) * 16) = pr[k]; }
  return exp2f(top - 27.f) * dt;
}
typedef __attribute__((ext_vector_type(2))) _Float16 f16x2;
typedef __attribute__((ext_vector_type(8))) _Float16 f16x8;
__device__ __forceinline__ void s4_modes(LAS char* my, int q0, int q1, f32x4 (&D)[3]) {
  const int l = lane_id(), i = l & 15, kk = l >> 4;
  const f32x2 pe[2] = {{(float)(8 * i), (float)(128 * (i >> 3) + (i & 7))},
                       {(float)(i < 8 ? 256 + 128 * (i >> 2) + 2 * (i & 3) : 2048), (float)(i < 8 ? 512 + 128 * (i >> 1) + 4 * (i & 1) : 128 * i)}};
  const f32x2 pp[2] = {{-pe[0][0], pe[0][1]}, pe[1]};
#pragma unroll 1
  for (int q = q0; q < q1; ++q) {
    u32x4 op[4];
    const f32x4 pc = *(LAS f32x4*)(my + S4_PRM + 2048 + (4 * q + kk) * 16);
#pragma unroll
    for (int j = 0; j < 4; ++j) {
      const f32x4 pr = *(LAS f32x4*)(my + S4_PRM + (16 * q + 4 * kk + j) * 16);
#pragma unroll
      for (int m = 0; m < 2; ++m) {
        const f32x2 ea = __builtin_elementwise_fma((f32x2){pr[0], pr[0]}, pe[m], (f32x2){m ? 14.f : pr[3], 14.f});
        const f32x2 t = pp[m] * pr[1];
        f32x2 fr = __builtin_elementwise_fma((f32x2){pr[1], pr[1]}, pp[m], -(f32x2){__builtin_rintf(t[0]), __builtin_rintf(t[1])});
        fr = __builtin_elementwise_fma((f32x2){pr[2], pr[2]}, pp[m], fr);
        if (!m) fr[0] += pc[j];
#pragma unroll
        for (int h = 0; h < 2; ++h) {
          const f32x2 v = (f32x2){__builtin_amdgcn_cosf(fr[h]), __builtin_amdgcn_sinf(fr[h])} * __builtin_amdgcn_exp2f(ea[h]);
          op[2 * m + h][j] = __builtin_bit_cast(unsigned, __builtin_convertvector(v, f16x2));
        }
      }
    }
#pragma unroll
    for (int m = 0; m < 3; ++m) D[m] = __builtin_amdgcn_mfma_f32_16x16x32_f16(__builtin_bit_cast(f16x8, op[m + 1]), __builtin_bit_cast(f16x8, op[0]), D[m], 0, 0, 0);
  }
}
__device__ __forceinline__ void s4d_task(const Ctx& cx, const Bulk& bk, unsigned yflip, int gentle, int w, int task, char* smem, const S4In& in, char* ws, int q0, f32x4 (&D)[3], float ksc) {
  const int l = lane_id(), tid = w * 64 + l;
  LAS char* const lds = (LAS char*)smem;
  LAS char* const my = lds + w * S4_WSZ;
  S4M(2);
  s4_modes(my, q0, 8, D);
  {
    const int i = l & 15, kk = l >> 4;
#pragma unroll
    for (int r = 0; r < 4; ++r) {
      const int row = 4 * kk + r;
      *(LAS float*)(my + S4_KD + (128 * (row >> 3) + (row & 7) + 8 * i) * 4) = ksc * D[0][r];
      *(LAS float*)(my + S4_KD + (row < 8 ? 256 + 64 * kk + r + 4 * i : 640 + i) * 4) = ksc * D[1][r];
      *(LAS float*)(my + S4_KD + (row < 8 ? 384 + 32 * (row >> 1) + (row & 1) + 2 * i : 384 + 16 * row + i) * 4) = ksc * D[2][r];
    }
  }
  S4M(3);
#pragma unroll
  for (int k = 0; k < 2; ++k)
#pragma unroll
    for (int j = 0; j < 8; j += 4) *(LAS u32x4*)(lds + S4_RAW + k * 8208 + tid * 32 + j * 4) = (u32x4){in.u[j][k], in.u[j + 1][k], in.u[j + 2][k], in.u[j + 3][k]};
  BARRIER();
  u32x2 r[11];
#pragma unroll
  for (int k = 0; k < 2; ++k)
#pragma unroll
    for (int j = 0; j < 12; j += 4) {
      const u32x4 v = *(LAS u32x4*)(lds + S4_RAW + k * 8208 + tid * 32 + j * 4);
#pragma unroll
      for (int i = 0; i < 4; ++i) if (j + i < 11) r[j + i][k] = v[i];
    }
#pragma unroll
  for (int ch = 0; ch < 4; ++ch) {
    unsigned pr[10];
#pragma unroll
    for (int j = 0; j < 10; ++j) pr[j] = __builtin_amdgcn_perm(r[j + 1][ch >> 1], r[j][ch >> 1], (ch & 1) ? 0x07060302u : 0x05040100u);
    if (tid == 255) { pr[7] &= 0xFFFFu; pr[8] = 0u; pr[9] = 0u; }
    LAS char* ub = lds + ch * S4_WSZ + S4_U + 64 + tid * 16;
#pragma unroll
    for (int sft = 0; sft < 4; ++sft) *(LAS u32x4*)(ub + sft * S4_USTR) = (u32x4){pr[sft], pr[sft + 2], pr[sft + 4], pr[sft + 6]};
    if (tid < 4) {
#pragma unroll
      for (int sft = 0; sft < 4; ++sft) {
        const u32x4 v = {0u, 0u, sft == 3 ? pr[0] << 16 : 0u, sft == 0 ? 0u : sft == 1 ? pr[0] << 16 : pr[sft - 2]};
        *(LAS u32x4*)(lds + ch * S4_WSZ + S4_U + sft * S4_USTR + (3 - tid) * 16) = tid == 0 ? v : (u32x4){0u, 0u, 0u, 0u};
      }
    }
  }
  {
    if (l < 5) {
      *(LAS u32x4*)(my + S4_KH + l * 16) = (u32x4){0u, 0u, 0u, 0u};
    }
#pragma unroll
    for (int k = 0; k < 4; ++k) {
      const int grp = l + 64 * k;
      float v[8];
#pragma unroll
      for (int j = 0; j < 8; j += 2) {
        const unsigned c = pk2((float)(8 * grp + j), (float)(8 * grp + j + 1)) - 0x42804280u;
        const bool low = k == 0 && grp < 16;
        v[j] = *(LAS float*)(my + S4_KD + (low ? 8 * grp + j : c & 0xFFFFu) * 4);
        v[j + 1] = *(LAS float*)(my + S4_KD + (low ? 8 * grp + j + 1 : c >> 16) * 4);
      }
      *(LAS u32x4*)(my + S4_KH + 80 + (grp >> 2) * 80 + (grp & 3) * 16) = (u32x4){pk2(v[7], v[6]), pk2(v[5], v[4]), pk2(v[3], v[2]), pk2(v[1], v[0])};
    }
  }
  __syncthreads();
  S4M(4);
  {
    const int r = l & 31, hh = l >> 5, sft = (r + 1) & 3;
    unsigned ua = w * S4_WSZ + S4_U + sft * S4_USTR + (r + 25 - sft - 8 * hh - 16) * 2;
    const unsigned kz = w * S4_WSZ + S4_KH + hh * 16;
    unsigned kb1 = kz + (33 + r) * 80;
    unsigned kb0 = kz + (1 + r) * 80;
    f32x16 acc0 = {}, acc1 = {};
    unsigned ub = ua + 72;
    asm volatile("" : "+v"(ub));
    struct Ops { bf16x8 a0, a1, b0, b1; };
    auto ld = [&](int ks) {
      Ops o;
      const unsigned uo = ua + (1 - ks) * 32;
      o.a0 = cat2(*(LAS bf16x4*)(lds + uo), *(LAS bf16x4*)(lds + uo + 8));
      o.a1 = cat2(*(LAS bf16x4*)(lds + uo + 2048), *(LAS bf16x4*)(lds + uo + 2056));
      o.b0 = *(LAS bf16x8*)(lds + kb0 + ks * 32);
      o.b1 = *(LAS bf16x8*)(lds + kb1 + ks * 32);
      return o;
    };
    auto step = [&](const Ops& o, Ops& nx, int ks) {
      const unsigned uo = ua + (1 - ks) * 32, up = ub + (1 - ks) * 32;
      acc1 = __builtin_amdgcn_mfma_f32_32x32x16_bf16(o.a0, o.b1, acc1, 0, 0, 0); SB();
      nx.a0 = cat2(*(LAS bf16x4*)(lds + uo), *(LAS bf16x4*)(lds + up)); nx.b1 = *(LAS bf16x8*)(lds + kb1 + ks * 32); SB();
      acc0 = __builtin_amdgcn_mfma_f32_32x32x16_bf16(o.a0, o.b0, acc0, 0, 0, 0); SB();
      nx.b0 = *(LAS bf16x8*)(lds + kb0 + ks * 32);
      nx.a1 = cat2(*(LAS bf16x4*)(lds + uo + 2048), *(LAS bf16x4*)(lds + up + 2048));
      if (ks) {
        ub += 64;
        ua += 64;
        kb1 -= 80;
        kb0 = max(kb0 - 80, kz);
      }
      SB();
      acc1 = __builtin_amdgcn_mfma_f32_32x32x16_bf16(o.a1, o.b0, acc1, 0, 0, 0); SB();
    };
    Ops e0 = ld(0), e1 = ld(1), o0, o1;
    ua += 64; kb1 -= 80; kb0 = max(kb0 - 80, kz);
#pragma unroll 1
    for (int D = 0; D < 32; D += 2) {
      step(e0, o0, 0);
      step(e1, o1, 1);
      step(o0, e0, 0);
      step(o1, e1, 1);
      sleep_n(gentle);
    }
    S4M(5);
#pragma unroll
    for (int j = 0; j < 4; ++j) {
      const unsigned off = (32 * r + 8 * j + 4 * hh) * 2;
      *(LAS u32x2*)(my + off) = (u32x2){pk2(acc0[4 * j], acc0[4 * j + 1]), pk2(acc0[4 * j + 2], acc0[4 * j + 3])};
      *(LAS u32x2*)(my + off + 2048) = (u32x2){pk2(acc1[4 * j], acc1[4 * j + 1]), pk2(acc1[4 * j + 2], acc1[4 * j + 3])};
    }
    __syncthreads();
#pragma unroll
    for (int j = 0; j < 2; ++j) {
      u32x2 c[4];
#pragma unroll
      for (int k = 0; k < 4; ++k) c[k] = *(LAS u32x2*)(lds + k * S4_WSZ + (j * 256 + tid) * 8);
      const unsigned o = bk.at(B_YT + task * 16384 + j * 8192) ^ yflip;
#pragma unroll
      for (int k = 0; k < 2; ++k)
        st128((u32x4){__builtin_amdgcn_perm(c[1][k], c[0][k], 0x05040100u), __builtin_amdgcn_perm(c[3][k], c[2][k], 0x05040100u),
                      __builtin_amdgcn_perm(c[1][k], c[0][k], 0x07060302u), __builtin_amdgcn_perm(c[3][k], c[2][k], 0x07060302u)}, bk.rs, tid * 32 + k * 16, o);
    }
  }
  __syncthreads();
  S4M(6);
}

__device__ __forceinline__ void warm_l2(const u16* W, unsigned off, bool rows256, int w) {
  const rsrc_t rw = mk_rsrc(W, (size_t)DM * HD * 2);
  const int l = lane_id();
  const unsigned vo = rows256 ? (l >> 4) * (HD * 2) + (l & 15) * 16 : l * 16, st = rows256 ? 4 * HD * 2 : 1024;
  u32x4 v[36];
#pragma unroll
  for (int i = 0; i < 36; ++i) v[i] = __builtin_amdgcn_raw_buffer_load_b128(rw, vo, off + (i * 4 + w) * st, 0);
#pragma unroll
  for (int i = 0; i < 36; ++i) asm volatile("" :: "v"(v[i]));
}

__device__ __forceinline__ void x_ahead(const Params& p, int w, int idx) {
  const rsrc_t rx = mk_rsrc(p.x, (size_t)SL * DM * 2);
  for (int i = 0; i < p.xpre; ++i) dma16(rx, lane_id() * 16, ((((idx - 35) >> 2) * 4 + w) + i * 32) * 1024, LDS_BYTES - 1024);
}
__device__ __forceinline__ unsigned s4_role(const Params& p, int w, int idx, char* smem) {
  CODE_PAD(PAD_S4);
  char* const ws = p.ws;
  const int l = lane_id(), tid = w * 64 + l;
  const bool near = idx >= 8 && idx < 35;
  const int r_ = idx - 8, t0 = 5 * r_ + (r_ + 2) / 3;
  const int t1 = t0 + 5 + (r_ % 3 == 0) - p.s4skip;
  const rsrc_t rx = mk_rsrc(p.x, (size_t)SL * DM * 2);
  S4In in[6];
#pragma unroll
  for (int k = 0; k < 6; ++k) {
    const int d = min(t0 + k, t1 - 1) * 4 + w, o = d * (NST / 2) + l;
    in[k].la = ((const unsigned*)p.lar)[o]; in[k].ai = ((const unsigned*)p.aim)[o]; in[k].cr = ((const unsigned*)p.cre)[o]; in[k].ci = ((const unsigned*)p.cim)[o];
    in[k].dt = __builtin_amdgcn_raw_buffer_load_b16(mk_rsrc(p.ldt, DM * 2), d * 2, 0, 0);
  }
  const unsigned gen = gen_load(ws);
  LAS char* const my = (LAS char*)smem + w * S4_WSZ;
  float ksc = s4_params(my, in[0]);
  f32x4 D[3] = {};
  const int qs = near ? p.s4q : p.s4f;
  s4_modes(my, 0, qs, D);
#pragma unroll
  for (int m = 0; m < 6; ++m)
#pragma unroll
    for (int j = 0; j < 8; ++j) in[m].u[j] = __builtin_amdgcn_raw_buffer_load_b64(rx, t0 + m < t1 ? (tid * 8 + j) * (DM * 2) : 0x7F000000u, (t0 + m) * 8, 0);
  const Ctx cx = {ws, __builtin_amdgcn_readfirstlane(gen) + 1u};
  Bulk bk;
  bk.init(cx, idx & 7);
  if (near) {
    unsigned z[3];
#pragma unroll
    for (int m = 0; m < 3; ++m) z[m] = __builtin_amdgcn_raw_buffer_load_b32(bk.rs, l * 128, bk.at(B_YT + t0 * 16384 + (4 * m + w) * 8192) ^ 8192u, 0);
    asm volatile("" :: "v"(z[0]), "v"(z[1]), "v"(z[2]));
    sleep_n((r_ % 3 ? r_ % 7 : 0) * p.stag);
  }
  s4_modes(my, qs, 8, D);
#pragma unroll 1
  for (int k = t0; k < t1; ++k) {
    s4d_task(cx, bk, near ? 8192u : 0u, near ? p.gentle : 0, w, k, smem, in[0], ws, k == t0 ? 8 : 0, D, ksc);
    in[0] = in[1]; in[1] = in[2]; in[2] = in[3]; in[3] = in[4]; in[4] = in[5];
    ksc = s4_params(my, in[0]);
    D[0] = D[1] = D[2] = (f32x4){0.f, 0.f, 0.f, 0.f};
  }
  WAIT_VM(0);
  BARRIER();
  if (tid == 0) flag_put(cx, flag_ptr(ws, F_Y) + idx, 1u);
  return cx.tag;
}
struct FinIn {
  unsigned sidx, cidx, l16, ae0, ae1, ae2, ae3, am, lw, lr, oo, d0, tq;
  float g[NDIR], b[NDIR], c[NDIR];
  const void *ws, *out;
  unsigned tag, o_u, o_e, o_x, o_va, o_tw, xa, xb, o_ctl, pa, w, f_w, pq;
};
__device__ __forceinline__ void watch_role(const Params& p, unsigned tag) {
  const int l = lane_id();
  const Ctx cx = {p.ws, tag};
  for (unsigned spins = 0; __any(flag_get(cx, flag_ptr(p.ws, F_Y) + min(8 + l, NS4 - 1)) == 0u) && !spin_fail(spins, cx);) __builtin_amdgcn_s_sleep(30);
  wait_all<30>(cx, F_G, 64);
  wait_all<30>(cx, F_G + 4, 64);
  unsigned spins = 0;
  while (__any(flag_get(cx, flag_ptr(p.ws, F_S + (l & 31))) == 0u) && !spin_fail(spins, cx)) __builtin_amdgcn_s_sleep(30);
  publish(cx, 0, F_W, 1);
  for (int i = 0; i < NDIR; ++i) {
    GAS u64* const f = flag_ptr(p.ws, F_U + (l & 31)) + (l >> 5) * 2 + (SDIR + i >= U_LAST ? 4 : 0);
    while (__any((int)min(flag_get(cx, f), flag_get(cx, f + 1)) <= SDIR + i) && !spin_fail(spins, cx)) __builtin_amdgcn_s_sleep(8);
    publish(cx, 0, F_W + 1 + i, 1);
  }
}
struct FinX { unsigned x[NDIR], d; };
__device__ __forceinline__ int fin_col(int T) { const int l = lane_id(); return T * 16 + 8 * ((l >> 3) & 1) + 2 * (l & 3); }
__device__ __forceinline__ int fin_row(int w) { const int l = lane_id(); return SDIR * 32 + 8 * w + 2 * (l >> 4) + ((l >> 2) & 1); }
__device__ __forceinline__ FinX fin_x(const Params& p, int w, int T) {
  FinX f;
#pragma unroll
  for (int i = 0; i < NDIR; ++i) f.x[i] = *(const unsigned*)(p.x + (size_t)(fin_row(w) + 32 * i) * DM + fin_col(T));
  f.d = *(const unsigned*)(p.dsk + fin_col(T));
  return f;
}
__device__ __forceinline__ void fin_role(const Params& p, unsigned tag, int w, int T, const FinX& fx) {
  char* const ws = p.ws;
  const int l = lane_id(), n = l & 15, g = l >> 4;
  const Ctx cx = {ws, tag};
  Bulk bk;
  bk.init(cx, T & 7);
  bk.pl.load(cx, T & 7);
  const Place& pl = bk.pl;
  FinIn fi;
  if (w == 0) wait_all<30>(cx, F_W, 1);
  BARRIER();
  const int d = fin_col(T), r0 = fin_row(w);
  float (&gg)[NDIR] = fi.g, (&b0)[NDIR] = fi.b, (&b1)[NDIR] = fi.c;
#pragma unroll
  for (int i = 0; i < NDIR; ++i) {
    const int t = r0 + 32 * i;
    gg[i] = __uint_as_float(__builtin_amdgcn_raw_buffer_load_b32(bk.rs, t * 4, bk.at(B_G), 16));
    const unsigned yv = __builtin_amdgcn_raw_buffer_load_b32(bk.rs, y_at(bk, d >> 2, t * 8 + (d & 2) * 2), 0, 16);
    const unsigned xv = fx.x[i], dv = fx.d;
    b0[i] = rbf(rbf(1.0f - gg[i]) * rbf(lo16(yv) + rbf(lo16(xv) * lo16(dv)))); b1[i] = rbf(rbf(1.0f - gg[i]) * rbf(hi16(yv) + rbf(hi16(xv) * hi16(dv))));
  }
#pragma unroll
  for (int i = 0; i < NDIR; ++i) asm volatile("" : "+v"(b0[i]), "+v"(b1[i]));
  char* const base = comm_base(ws) - CO;
  fi.sidx = (n & 8) ? 0xEEEEu : 0x4444u;
  fi.l16 = l * 16; fi.d0 = 0;
  fi.cidx = (n & 3) * 0x5555u;
  auto ae = [&](int j) { return g <= j ? j * 32768 + (n >> 3) * 512 + g * 128 + (n & 7) * 16 : 0x7F000000u; };
  fi.ae0 = ae(0); fi.ae1 = ae(1); fi.ae2 = ae(2); fi.ae3 = ae(3);
  fi.am = w * 1024 + l * 16;
  fi.lw = (w >> 1) * 512 + (2 * (g & 1) + (g >> 1)) * 128 + (n >> 1) * 16 + (w & 1) * 8 + (n & 1) * 4;
  fi.lr = w * 2048 + ((l >> 2) & 1) * 1024 + g * 128 + (((l >> 3) & 1) * 4 + (l & 3)) * 16;
  fi.oo = (r0 * DM + d) * 2;
  fi.f_w = (unsigned)(ws - base) + (F_W + 1) * 128;
  fi.ws = base; fi.out = p.out; fi.tag = __builtin_amdgcn_readfirstlane(tag); fi.o_ctl = (unsigned)(ws - base); fi.xa = pl.slot(SDIR - 1) ^ pl.slot(SDIR); fi.xb = pl.slot(SDIR) ^ pl.slot(SDIR + 1);
  fi.o_u = __builtin_amdgcn_readfirstlane(u_tile(T, w, 0) ^ (~bk.w13 & 8192u));
  fi.o_x = X_AT + ((bk.w13 ^ 8192u) & 8192u);
  fi.o_e = __builtin_amdgcn_readfirstlane(fi.o_x + 0x100000 + (w >> 1) * 16384 + (w & 1) * 4096);
  fi.o_va = __builtin_amdgcn_readfirstlane((bk.w13 ^ 8192u) + 2 * B_O + T * 16384 + w * 2048);
  fi.o_tw = __builtin_amdgcn_readfirstlane(CO + TW_OFF + (16384u ^ pl.dyn(T & 3) ^ pl.slot(SDIR - 1)));
  fi.tq = T == 0 || T >= 28 ? 0x7F000000u : CO + TW_OFF + 260 + (16384u ^ pl.dyn(T & 3) ^ pl.slot(l));
  fi.pq = p.qb | ((p.qb - p.qa) << 8);
  fi.pa = p.xt0 | (p.xt1 << 16); fi.w = w;
  asm volatile(F_ASM_TEXT : F_ASM_VOPS : F_ASM_SOPS : F_ASM_CLOB);
  if (T == 0 && w == 0 && lane_id() == 0) __hip_atomic_store((GAS unsigned*)flag_ptr(ws, F_GEN), tag, RLX_AGENT);
}

__device__ __forceinline__ void code_ahead(int i, int n) {
  const char* pc;
  unsigned back, len;
  asm volatile("s_getpc_b64 %0\n.Lcp_%=:\n\ts_mov_b32 %1, .Lcp_%=-_Z6k_mega6Params\n\ts_mov_b32 %2, k_mega_end-_Z6k_mega6Params" : "=s"(pc), "=s"(back), "=s"(len));
  const rsrc_t rc = mk_rsrc(pc - back, len);
  for (unsigned o = i * 1024; o < len; o += n * 1024) dma16(rc, lane_id() * 16, o, LDS_BYTES - 1024);
  WAIT_VM(0);
}

__global__ void __launch_bounds__(256, 1) k_mega(Params p) {
  extern __shared__ __attribute__((aligned(16))) char smem[];
  const int x = blockIdx.x & 7, j = blockIdx.x >> 3;
  const int w = __builtin_amdgcn_readfirstlane(threadIdx.x >> 6);
  if (w == 1 && blockIdx.x != 128 && ((p.tlb >> (x < 4 ? (j < 16 ? 0 : j < 25 ? 1 : 2) : (j & 24) == 16 ? 4 : 3)) & 1)) touch_pages(p.ws, j);
  if (x < 4 && (j < 16 || j > 24)) sleep_n(x ? p.v0 : p.s0);
  if (x < 4 && j < 8) { chain_role<true>(p, w, x + 4 * j, x); return; }
  else if (x < 4 && j < 16) { chain_role<false>(p, w, x + 4 * (j - 8), x); return; }
  else if (x < 4 && j < 25) red_role(p, w, x + 4 * (j - 16), smem);
  else if (x == 0 && j == 25) {
    if (w == 0) keep_role(p);
    else if (p.cpf & 4) code_ahead(w - 1, 3);
  }
  else {
    unsigned tag;
    int T = -1, opl = -1, mix = -1, ms0 = 0, mds = 1, ms1 = p.mixs;
    FinX fx;
    if (x >= 4 && j < 16) {
      const int h = (x & 3) + 4 * (j & 7);
      tag = j >= 8 ? proj_role<false>(p, w, h) : proj_role<true>(p, w, h);
      if (j >= 8 && h < 24) opl = h, mix = h >> 2, ms0 = p.ob0 + (h & 3), mds = 4, ms1 = p.ob1;
      else if (h < 30) mix = h % 6, ms0 = p.mixs + (j < 8 ? 1 + h / 6 : 0), mds = 6, ms1 = SDIR;
      if (j >= 8 && h >= 30) { T = (h - 30) * 35; fx = fin_x(p, w, T); }
    } else {
      if (x >= 4 && j < 24) {
        const int h = (x & 3) + 4 * (j & 7);
        if (p.cpf & 1) code_ahead((j & 7) * 4 + w, 32);
        if (p.wl2 & 1) warm_l2(p.Wo, h * DH * DM * 2, false, w);
        x_ahead(p, w, 35 + h);
        if (p.wl2 & 2) {
          const int i = h * 4 + w, l = lane_id();
          const rsrc_t ro = mk_rsrc(p.out, (size_t)SL * DM * 2), rc = mk_rsrc(p.ws, CTL_BYTES), rb = mk_rsrc(comm_base(p.ws) - CO + PB + 2 * B_G, 16384);
          unsigned z[5];
#pragma unroll
          for (int k = 0; k < 3; ++k) z[k] = __builtin_amdgcn_raw_buffer_load_b32(ro, l * 128, (i + 128 * k) * 8192, 0);
          z[3] = __builtin_amdgcn_raw_buffer_load_b32(rc, l * 128, i * 8192, 16);
          z[4] = __builtin_amdgcn_raw_buffer_load_b32(rb, l * 128, (i ^ 8) * 8192, 16);
          asm volatile("" :: "v"(z[0]), "v"(z[1]), "v"(z[2]), "v"(z[3]), "v"(z[4]));
        }
        WAIT_VM(0);
        BARRIER();
        tag = proj_role<false, 1>(p, w, h);
        if (w == 0 && lane_id() == 0) flag_put({p.ws, tag}, flag_ptr(p.ws, F_Y) + 35 + h, 1u);
        att_role(p, tag, w, h, 0, SDIR);
      } else {
        if (x >= 4) {
          tag = proj_role<true, 1>(p, w, (x & 3) + 4 * (j & 7));
          if (j >= 30) xprep_role(p, tag, w, (x & 3) + 4 * (j & 1));
        }
        if (x >= 4 && j < 30) opl = (j - 24) * 4 + (x & 3);
        else {
          if (x < 4 && j >= 26 && (p.cpf & 2)) code_ahead((j - 26) * 4 + w, 24);
          T = x < 4 ? (j - 25) * 4 + x : (j - 23) * 4 + (x & 3);
          fx = fin_x(p, w, T);
#pragma unroll
          for (int i = 0; i < NDIR; ++i) asm volatile("" : "+v"(fx.x[i]), "+v"(fx.d));
          if (x < 4) tag = s4_role(p, w, 7 + x * 7 + (j - 25), smem);
          if (T == 35) { if (w == 0) watch_role(p, tag); T = -1; }
          if (T > 28 && p.mixs) mix = T - 29;
        }
      }
    }
    if (opl >= 0) op_role(p, tag, w, opl >> 2, opl & 3, x, j < 16);
    if (mix >= 0) mix_role(p, tag, w, mix, ms0, mds, ms1, opl >= 0);
    if (T >= 0) fin_role(p, tag, w, T, fx);
  }
}

extern "C" __device__ __attribute__((used, noinline, visibility("hidden"))) void k_mega_end() {}

extern "C" void kernel_launch(void* const* d_in, const int* in_sizes, int n_in, void* d_out, int out_size, void* d_ws, size_t ws_size,
                              hipStream_t stream) {
  Params p;
  p.x = (const u16*)d_in[0]; p.mem0 = (const u16*)d_in[1];
  p.Wq = (const u16*)d_in[2]; p.Wk = (const u16*)d_in[3]; p.Wv = (const u16*)d_in[4]; p.Wo = (const u16*)d_in[5];
  p.lar = (const u16*)d_in[8]; p.aim = (const u16*)d_in[9]; p.cre = (const u16*)d_in[10]; p.cim = (const u16*)d_in[11];
  p.ldt = (const u16*)d_in[12]; p.dsk = (const u16*)d_in[13]; p.gw = (const u16*)d_in[14];
  p.out = (u16*)d_out; p.ws = (char*)d_ws;
  (void)hipFuncSetAttribute((const void*)k_mega, hipFuncAttributeMaxDynamicSharedMemorySize, LDS_BYTES);
  {
    k_mega<<<256, 256, LDS_BYTES, stream>>>(p);
  }
}
```

```cpp
#include <hip/hip_runtime.h>
#include <hip/hip_bf16.h>
#include <stdint.h>
#include <type_traits>

typedef __attribute__((ext_vector_type(2))) __bf16 bf16x2;
typedef __attribute__((ext_vector_type(4))) __bf16 bf16x4;
typedef __attribute__((ext_vector_type(8))) __bf16 bf16x8;
typedef __attribute__((ext_vector_type(2))) float f32x2;
typedef __attribute__((ext_vector_type(4))) float f32x4;
typedef __attribute__((ext_vector_type(2))) unsigned u32x2;
typedef __attribute__((ext_vector_type(4))) unsigned u32x4;
typedef unsigned short u16;
typedef __amdgpu_buffer_rsrc_t rsrc_t;
#define LAS __attribute__((address_space(3)))
#define GAS __attribute__((address_space(1)))

constexpr int SL = 2048, DM = 576, NH = 32, DH = 128, HD = 4096, NSEG = 64, NST = 128;

constexpr size_t WS_CTL = 0;
constexpr int CTL_BYTES = 65536;
constexpr int F_GEN = 0, F_TMO = 1, F_GO = 2, F_XCC = 10, F_PRB = 14, F_INITW = 16, F_Y = 20;
constexpr int F_U = 320;
constexpr int F_W = 360;
constexpr int F_R = 300;
constexpr int F_M = 65, F_O = 129, F_A = 200, F_Q = 256;
constexpr int W_VO = 1, W_PX = 19, W_PM = 23, W_PC = 24, W_MEM = 25;
constexpr int N_DUR = 0, N_K = 32, N_U = 64, N_RD = 96;
constexpr size_t COMM_BYTES = (size_t)8 << 20;
constexpr unsigned FR_BYTES = SL * HD * 2;
constexpr unsigned B_K = 0;
constexpr unsigned B_V = FR_BYTES;
constexpr unsigned B_U = FR_BYTES;
constexpr unsigned B_O = 3 * FR_BYTES;
constexpr unsigned B_Q = 4 * FR_BYTES;
constexpr unsigned B_AP = 5 * FR_BYTES + NH * 8192;
constexpr unsigned B_YT = B_AP + NSEG * 24 * 16384;
constexpr unsigned B_G = B_YT + DM * SL * 2;
constexpr int SDIR = 58, NDIR = NSEG - SDIR;
constexpr unsigned X_AT = 8u << 20;
constexpr int Y_NEAR = 0, Y_FAR = 144, F_G = 310;
constexpr int F_S = 26;
#define RG_U 3
#define UNEAR 8192u
#define NCOPY 4
#define RG_QKV 63
#define RG_M 63
#define RG_O 63
#define RG_AP 63
#define PB_MB 12u
constexpr unsigned CO = 4u << 20, PB = PB_MB << 20, BASE_BYTES = 244u << 20;
constexpr unsigned U_AT = (128u << 20) - PB;
constexpr size_t WS_END = (size_t)240 << 20;
constexpr int LDS_BYTES = 163840;
constexpr unsigned SPIN_CAP = 1u << 21;

struct Params {
  const u16 *x, *mem0, *Wq, *Wk, *Wv, *Wo, *lar, *aim, *cre, *cim, *ldt, *dsk, *gw;
  u16* out;
  char* ws;
#define TUNE_LIST(X) X(keep_at, "KD", 20) X(t_a, "TA", -6) X(t_first, "TY", 0) X(t_d, "TD", 11) X(rd0, "RD0", 52) X(gentle, "GENTLE", 0) X(dur_dec, "DDEC", 8) X(dur_inc, "DINC", 8) X(dur0, "DP0", 75) X(boost, "BOOST", 3) X(stag, "STAG", 40) X(dret, "DRET", 76) X(cpf, "CPF", 3) X(sa, "SA", 8) X(sb, "SB", 53) X(sa2, "SA2", 18) X(x2, "X2", 4) X(g2, "G2", 2) X(xg0, "XG0", 8) X(xg1, "XG1", 5) X(xtra, "XTRA", 9) X(s4q, "S4Q", 0) X(s4f, "S4F", 8) X(wl2, "WL2", 3) X(s0, "S0", 30) X(v0, "V0", 0) X(xl0, "XL0", 56) X(tlb, "TLB", 0) X(xt0, "XT0", 110) X(xt1, "XT1", 80) X(vlead, "VLEAD", 23) X(xpre, "XPRE", 7) X(pj0, "PJ0", 0) X(xlast, "XLAST", 8) X(hdel, "HDEL", 0) X(hdl2, "HDL2", 0) X(q0, "Q0", 30) X(s4skip, "S4SKIP", 0) X(mixs, "MIXS", 0) X(qa, "QA", 6) X(qb, "QB", 24) X(ob0, "OB0", 0) X(ob1, "OB1", 6) X(fsl, "FSL", 2) X(late_r, "LR", 8) X(late_s, "LS", 8)
#define TUNE_DECL(n, e, v) static constexpr int n = v;
  TUNE_LIST(TUNE_DECL)
};

__device__ __forceinline__ int lane_id() {
  int z = 0;
  asm volatile("" : "+v"(z));
  return __builtin_amdgcn_mbcnt_hi(~0u, __builtin_amdgcn_mbcnt_lo(~0u, z));
}
__device__ __forceinline__ float bf2f(u16 x) { return __uint_as_float((unsigned)x << 16); }
__device__ __forceinline__ float rbf(float x) { return (float)(__bf16)x; }
__device__ __forceinline__ unsigned pk2(float a, float b) {
  f32x2 v = {a, b};
  return __builtin_bit_cast(unsigned, __builtin_convertvector(v, bf16x2));
}
__device__ __forceinline__ float lo16(unsigned p) { return __uint_as_float(p << 16); }
__device__ __forceinline__ float hi16(unsigned p) { return __uint_as_float(p & 0xFFFF0000u); }
__device__ __forceinline__ bf16x8 zext8(u32x2 v) {
  u32x4 z = {v[0], v[1], 0u, 0u};
  return __builtin_bit_cast(bf16x8, z);
}
__device__ __forceinline__ bf16x4 tr_read(const char* smem_base, unsigned off) {
  return __builtin_amdgcn_ds_read_tr16_b64_v4bf16((LAS bf16x4*)((LAS char*)smem_base + off));
}
__device__ __forceinline__ float red_max_g(float x) {
  auto r = __builtin_amdgcn_permlane16_swap(__float_as_uint(x), __float_as_uint(x), false, false);
  x = fmaxf(__uint_as_float(r[0]), __uint_as_float(r[1]));
  auto q = __builtin_amdgcn_permlane32_swap(__float_as_uint(x), __float_as_uint(x), false, false);
  return fmaxf(__uint_as_float(q[0]), __uint_as_float(q[1]));
}
__device__ __forceinline__ float red_sum_g(float x) {
  auto r = __builtin_amdgcn_permlane16_swap(__float_as_uint(x), __float_as_uint(x), false, false);
  x = __uint_as_float(r[0]) + __uint_as_float(r[1]);
  auto q = __builtin_amdgcn_permlane32_swap(__float_as_uint(x), __float_as_uint(x), false, false);
  return __uint_as_float(q[0]) + __uint_as_float(q[1]);
}
__device__ __forceinline__ rsrc_t mk_rsrc(const void* p, size_t bytes) {
  return __builtin_amdgcn_make_buffer_rsrc((void*)p, 0, (int)bytes, 0x00020000);
}

#define STR2(x) #x
#define STR(x) STR2(x)
#define CODE_PAD(N) asm volatile("s_branch .Lpad_%=\n\t.fill " STR(N) ", 4, 0xBF800000\n\t.Lpad_%=:" ::: "memory")
#define PAD_RED 0
#define PAD_ATT 0
#define PAD_OP 0
#define PAD_S4 0
#define PAD_CH 0
#define WAIT_VM(n) asm volatile("s_waitcnt vmcnt(" #n ")" ::: "memory")
#define WAIT_LGKM0() asm volatile("s_waitcnt lgkmcnt(0)" ::: "memory")
#define BARRIER() do { WAIT_LGKM0(); __builtin_amdgcn_s_barrier(); asm volatile("" ::: "memory"); } while (0)
#define SB() __builtin_amdgcn_sched_barrier(0)
#define MF(a, b, c) __builtin_amdgcn_mfma_f32_16x16x32_bf16(a, b, c, 0, 0, 0)
#define RLX_AGENT __ATOMIC_RELAXED, __HIP_MEMORY_SCOPE_AGENT
#define DEV_COUNT(i, cond) do {} while (0)
#define DEV_COUNT_DECL do {} while (0)
#define DEV_COUNT_FLUSH() do {} while (0)
__device__ __forceinline__ bf16x8 cat2(bf16x4 a, bf16x4 b) { return __builtin_shufflevector(a, b, 0, 1, 2, 3, 4, 5, 6, 7); }
__device__ __forceinline__ u32x4 pack8(const f32x4& a, const f32x4& b) {
  return (u32x4){pk2(a[0], a[1]), pk2(a[2], a[3]), pk2(b[0], b[1]), pk2(b[2], b[3])};
}
__device__ __forceinline__ void dma16(rsrc_t rs, unsigned voff, unsigned soff, unsigned lds_addr) {
  unsigned keep;
  asm volatile("s_mov_b32 %0, m0\n\ts_mov_b32 m0, %3\n\ts_nop 0\n\tbuffer_load_dwordx4 %1, %2, %4 offen lds\n\ts_mov_b32 m0, %0"
               : "=&s"(keep) : "v"(voff), "s"(rs), "s"(lds_addr), "s"(soff) : "memory");
}
__device__ __forceinline__ void st128(u32x4 v, rsrc_t rs, unsigned voff, unsigned soff) {
  __builtin_amdgcn_raw_buffer_store_b128(v, rs, voff, soff, 16);
  asm volatile("s_nop 1" ::"v"(v) : "memory");
}
__device__ __forceinline__ u32x4 ld128(rsrc_t rs, unsigned voff, unsigned soff) { return __builtin_amdgcn_raw_buffer_load_b128(rs, voff, soff, 16); }
__device__ __forceinline__ bf16x8 ldfrag(rsrc_t rs, unsigned voff, unsigned soff) { return __builtin_bit_cast(bf16x8, ld128(rs, voff, soff)); }
__device__ __forceinline__ void sleep_n(int n) { for (int i = 0; i < n; ++i) __builtin_amdgcn_s_sleep(1); }

__device__ __forceinline__ char* comm_base(char* ws) {
  return (char*)(((uintptr_t)ws + CTL_BYTES + ((2u << 20) - 1)) & ~(uintptr_t)((2u << 20) - 1));
}
__device__ __forceinline__ unsigned rt_now() {
  unsigned long long t;
  asm volatile("s_memrealtime %0\n\ts_waitcnt lgkmcnt(0)" : "=s"(t) :: "memory");
  return (unsigned)t;
}
__device__ __forceinline__ int wait_rt(unsigned t) {
  int d = (int)(t - rt_now());
  if (d <= 0 || d > 2000) return -d;
  for (d -= 14; d > 0; d -= 3) __builtin_amdgcn_s_sleep(1);
  do { d = (int)(t - rt_now()); } while (d > 0);
  return -d;
}
__device__ __forceinline__ unsigned xcc_id() { return (unsigned)__builtin_amdgcn_s_getreg(20 | (3 << 11)) & 7u; }
typedef unsigned long long u64;
struct Ctx { char* ws; unsigned tag; };
__device__ __forceinline__ GAS u64* flag_ptr(char* ws, int idx) { return (GAS u64*)(ws + WS_CTL + idx * 128); }
__device__ __forceinline__ unsigned gen_load(char* ws) { return __hip_atomic_load((GAS unsigned*)flag_ptr(ws, F_GEN), RLX_AGENT); }
__device__ __forceinline__ unsigned flag_get(const Ctx& cx, GAS u64* p) {
  const u64 v = __hip_atomic_load(p, RLX_AGENT);
  return (unsigned)(v >> 32) == cx.tag ? (unsigned)v : 0u;
}
__device__ __forceinline__ void flag_put(const Ctx& cx, GAS u64* p, unsigned v) { __hip_atomic_store(p, ((u64)cx.tag << 32) | v, RLX_AGENT); }
__device__ __forceinline__ bool spin_fail(unsigned& spins, const Ctx& cx) {
  ++spins;
  if (spins > SPIN_CAP) { flag_put(cx, flag_ptr(cx.ws, F_TMO), 1u); return true; }
  return (spins & 1023u) == 0 && __builtin_amdgcn_readfirstlane(flag_get(cx, flag_ptr(cx.ws, F_TMO)));
}
template <int SLEEP = 1>
__device__ __forceinline__ void wait_ge_n(const Ctx& cx, int idx, int stride, int cnt, int target, int& seen) {
  unsigned spins = 0;
  const int k = lane_id() & 3;
  while (seen < target) {
    int v = (int)flag_get(cx, flag_ptr(cx.ws, idx + (k < cnt ? k : 0) * stride));
    seen = min(min(__builtin_amdgcn_readlane(v, 0), __builtin_amdgcn_readlane(v, 1)), min(__builtin_amdgcn_readlane(v, 2), __builtin_amdgcn_readlane(v, 3)));
    if (seen < target) {
      __builtin_amdgcn_s_sleep(SLEEP);
      if (spin_fail(spins, cx)) break;
    }
  }
  __builtin_amdgcn_fence(__ATOMIC_ACQUIRE, "wavefront");
}
template <int SLEEP>
__device__ __forceinline__ void wait_all(const Ctx& cx, int idx, int cnt) {
  unsigned spins = 0;
  const int k = lane_id();
  while (__any(flag_get(cx, flag_ptr(cx.ws, idx) + (k < cnt ? k : 0)) == 0u)) {
    __builtin_amdgcn_s_sleep(SLEEP);
    if (spin_fail(spins, cx)) break;
  }
  __builtin_amdgcn_fence(__ATOMIC_ACQUIRE, "wavefront");
}
__device__ __forceinline__ void publish(const Ctx& cx, int w, int idx, int value) {
  if (w == 0 && lane_id() == 0) flag_put(cx, flag_ptr(cx.ws, idx), (unsigned)value);
}

constexpr unsigned X1 = 0x101000u, TW_OFF = 27 * 32768 + 1024;
__device__ __forceinline__ unsigned win_static(int win) { return win * 32768 + (win >> 4) * 16384; }
__device__ __forceinline__ unsigned near_static(int i) { return (win_static(28 + (i >> 5)) ^ ((i & 32) << 7) ^ ((i & 64) << 8)) + (i & 31) * 128; }
struct Place {
  unsigned go;
  __device__ __forceinline__ void load(const Ctx& cx, int grp, unsigned need = 0xC0000000u) {
    unsigned spins = 0;
    while ((go = __builtin_amdgcn_readfirstlane(flag_get(cx, flag_ptr(cx.ws, F_GO + grp)))) < need && !spin_fail(spins, cx)) __builtin_amdgcn_s_sleep(8);
    __builtin_amdgcn_fence(__ATOMIC_ACQUIRE, "wavefront");
  }
  __device__ __forceinline__ unsigned dyn(int x) const { return ((go >> (4 * x)) & 7u) << 12; }
  __device__ __forceinline__ unsigned tog() const { return (((go >> 16) & 7u) << 12) | (2u << 20); }
  __device__ __forceinline__ unsigned slot(int s) const { return (((s ^ (s >> 1)) & 1) ? tog() : 0u) ^ ((s & 2) ? X1 : 0u); }
  __device__ __forceinline__ unsigned near(int i) const { return near_static(i) ^ (dyn(0) & 8192u); }
};
#define BULK_FLIP 0u
#define OUT_AUX 0
#define PROBE_N 6
struct Bulk {
  rsrc_t rs;
  unsigned w13;
  Place pl;
  __device__ __forceinline__ GAS u64* proj_flag(const Ctx& cx, int m, int h) const {
    return m == 0 ? flag_ptr(cx.ws, F_Q + h) : (GAS u64*)(comm_base(cx.ws) + pl.near(N_K + h) + (m - 1) * 8);
  }
  __device__ __forceinline__ void init(const Ctx& cx, int grp, u64 early = 0) {
    pl.go = __builtin_amdgcn_readfirstlane((unsigned)(early >> 32) == cx.tag ? (unsigned)early : 0u);
    if (pl.go < 0x80000000u) pl.load(cx, grp, 0x80000000u);
    rs = mk_rsrc(comm_base(cx.ws) - CO, BASE_BYTES);
    w13 = PB + ((~pl.go ^ BULK_FLIP) & 2u) * 4096;
  }
  __device__ __forceinline__ unsigned at(unsigned o) const {
    const unsigned q = w13 + ((o >> 13) << 14) + (o & 8191u);
    return q ^ ((q >> 8) & 8192u);
  }
};
__device__ __forceinline__ unsigned near_head(unsigned b, int h) { return PB + 2 * b + h * (1u << 20) + ((h >> 1) & 1) * 8192 + ((2 * h) & 63) * 16384; }
__device__ __forceinline__ unsigned u_tile(int T, int hb, int h7) {
  const int wv = T < 32 ? (T >> 1) & 3 : T & 3;
  return PB + 2 * B_U + T * (1u << 20) + ((T >> 1) & 1) * 8192 + (((T >> 3) & 3) * 4 + wv) * 65536 + (hb ^ ((T >> 3) & 3)) * 16384 + (h7 ^ (2 * wv + (T < 32 ? T & 1 : 0))) * 1024;
}
__device__ __forceinline__ unsigned xcc_page(unsigned xc) { return ((xc >> 1) & 1u) | ((xc >> 2) << 1) | ((xc & 1u) << 2); }

__device__ __forceinline__ void touch_pages(char* ws, int i) {
  const rsrc_t rs = mk_rsrc(comm_base(ws) - CO, BASE_BYTES);
  const unsigned o = lane_id() * (2u << 20) + (1u << 20) + (i & 63) * 128;
  const unsigned a = __builtin_amdgcn_raw_buffer_load_b32(rs, o, 4u << 20, 0), b = __builtin_amdgcn_raw_buffer_load_b32(rs, o, 132u << 20, 0);
  asm volatile("" :: "v"(a), "v"(b));
}

__device__ __forceinline__ void comm_init(Ctx& cx, int rid, int w, char* smem, bool all) {
  const int l = lane_id(), tid = w * 64 + l;
  const rsrc_t rs = mk_rsrc(comm_base(cx.ws), COMM_BYTES);
  const unsigned gen = gen_load(cx.ws);
  if (rid == 0) {
    LAS unsigned* const best = (LAS unsigned*)smem + 16;
    const unsigned base = w * 1024, r0 = rt_now(), c0 = (unsigned)__builtin_amdgcn_s_memtime();
    best[32] = 0u;
#pragma unroll
    for (int k = 0; k < 8; ++k) __builtin_amdgcn_raw_buffer_store_b128((u32x4){0u, 0u, 0u, 0u}, rs, base + k * 4096 + l * 16, 0, 16);
    WAIT_VM(0);
    BARRIER();
    unsigned mine = 0xFFFFu;
    auto sample = [&](int pg, int k) {
      unsigned long long t0, t1;
      asm volatile("s_memtime %0\n\ts_waitcnt lgkmcnt(0)" : "=s"(t0) :: "memory");
      const unsigned v = __builtin_amdgcn_raw_buffer_load_b32(rs, base + pg * 4096 + k * 128, 0, 16);
      asm volatile("s_waitcnt vmcnt(0)\n\ts_memtime %0\n\ts_waitcnt lgkmcnt(0)" : "=s"(t1) : "v"(v) : "memory");
      if (l == pg) mine = min(mine, (unsigned)(t1 - t0));
      return (unsigned)(t1 - t0);
    };
    auto pages = [&]() {
      if (l < 8) best[w * 8 + l] = mine;
      __syncthreads();
      const int k8 = l & 7;
      const unsigned b = min(min(best[k8], best[8 + k8]), min(best[16 + k8], best[24 + k8]));
      __syncthreads();
      return b;
    };
    auto page = [](int j) { return (j & 1) | ((j & 2) << 1); };
    const unsigned lim = 25u * ((unsigned)__builtin_amdgcn_s_memtime() - c0) / max(rt_now() - r0, 1u);
    cx.tag = __builtin_amdgcn_readfirstlane(gen) + 1u;
    volatile LAS unsigned* const found = best + 32;
    for (int k = 0; k < 40 && !__builtin_amdgcn_readfirstlane(*found); ++k) {
      const int pg = page(w + (k >> 1)) | ((k & 1) << 1);
      if (sample(pg, k >> 3) < lim && l == 0) atomicCAS((unsigned*)found, 0u, 1u | (pg & 2));
    }
    unsigned b = pages();
    b = min(b, (unsigned)__shfl_xor((int)b, 1)); b = min(b, (unsigned)__shfl_xor((int)b, 4));
    const unsigned fd = __builtin_amdgcn_readfirstlane(*found);
    const unsigned half = fd ? fd & 2u : __builtin_amdgcn_readlane(b, 2) < __builtin_amdgcn_readlane(b, 0) ? 2u : 0u;
    if (w == 3 && l < 8) flag_put(cx, flag_ptr(cx.ws, F_GO + l), 0x80000000u | half);
    unsigned xc = 0;
    if (w < 3) {
#pragma unroll
      for (int k = 0; k < PROBE_N; ++k) sample(page(w + k) | half, 5 + (k >> 2));
    } else {
      for (unsigned spins = 0; __any((xc = flag_get(cx, flag_ptr(cx.ws, F_INITW) + min(l, 35))) == 0u) && !spin_fail(spins, cx);) __builtin_amdgcn_s_sleep(2);
    }
    b = (pages() << 3) | (l & 7);
    b = min(b, (unsigned)__shfl_xor((int)b, 1)); b = min(b, (unsigned)__shfl_xor((int)b, 4));
    if (w == 3) {
      const unsigned v = l ? __shfl((int)xc, min(l, 3) * 9 - 1) - 1u : xcc_id();
      const unsigned p0 = (__builtin_amdgcn_readlane(b, half) & 7u) ^ xcc_page(xcc_id());
      unsigned seen = 0, go = 0xC0020000u;
#pragma unroll
      for (int x = 0; x < 4; ++x) seen |= 1u << (__builtin_amdgcn_readlane(v, x) & 7u);
#pragma unroll
      for (int x = 0; x < 4; ++x)
        go |= (seen == 15u || seen == 240u ? (xcc_page(__builtin_amdgcn_readlane(v, x)) ^ p0) & 7u : page(x) | half) << (4 * x);
      if (l < 8) flag_put(cx, flag_ptr(cx.ws, F_GO + l), go);
    }
    return;
  }
  if (all) {
    for (int j = (rid - 1) * 256 + tid; j < 78 * 2048; j += 35 * 256) {
      const int wi = j >> 11, win = wi < 2 ? 25 + wi : wi < 28 ? 31 + wi : wi < 52 ? 37 + wi : 45 + wi;
      __builtin_amdgcn_raw_buffer_store_b128((u32x4){~0u, ~0u, ~0u, ~0u}, rs, win * 32768 + (j & 2047) * 16, 0, 16);
    }
  }
  for (int j = (rid - 1) * 256 + tid; j < 34 * 2048; j += 35 * 256) {
    const int wi = 33 - (j >> 11), ch = j & 2047;
    const int win = wi < 24 ? wi + 1 : wi < 27 ? wi + 65 : wi == 27 ? 59 : wi == 28 ? 123 : wi - 2;
    const unsigned f = wi < 30 ? 0xFFFFFFFFu : 0u;
    __builtin_amdgcn_raw_buffer_store_b128((u32x4){f, f, f, f}, rs, win * 32768 + ch * 16, 0, 16);
  }
  cx.tag = __builtin_amdgcn_readfirstlane(gen) + 1u;
  WAIT_VM(0);
  BARRIER();
  if (tid == 0) flag_put(cx, flag_ptr(cx.ws, F_INITW) + rid - 1, xcc_id() + 1u);
}

__device__ __forceinline__ void keep_role(const Params& p) {
  char* const ws = p.ws;
  const int l = lane_id();
  const Ctx cx = {ws, __builtin_amdgcn_readfirstlane(gen_load(ws)) + 1u};
  if (l == 0) flag_put(cx, flag_ptr(ws, F_INITW) + 35, 1u);
  const rsrc_t crs = mk_rsrc(comm_base(ws), COMM_BYTES);
  Place pl;
  pl.load(cx, 0);
  int dp[2] = {4 * p.dur0, 4 * p.rd0}, boost = 0;
  for (unsigned spins = 0; p.t_first && __any(min(flag_get(cx, flag_ptr(ws, F_U + (l & 31)) + (l >> 5)), flag_get(cx, flag_ptr(ws, F_U + (l & 31)) + 2 + (l >> 5))) == 0u) && !spin_fail(spins, cx);)
    __builtin_amdgcn_s_sleep(1);
  unsigned t = rt_now() + p.t_first - 2 * (p.dur0 + p.t_a + p.xtra) - (p.rd0 + p.t_d + p.xg0), ta = 0, shift = 0;
  auto plan = [&](int b) {
    u32x4 wd = {0u, 0u, 0u, 0u};
#pragma unroll
    for (int k = 0; k < 4; ++k) {
      const int s = 4 * b + k;
      const unsigned t1 = t + ((dp[0] + k) >> 2) + p.t_a + boost + (s < p.sa ? p.xtra : s < p.sa2 ? p.x2 : s > p.xl0 ? p.xlast : 0);
      t = t1 + ((dp[1] + 3 - k) >> 2) + p.t_d + boost + (s < p.sa ? p.xg0 : s < p.sa2 ? p.g2 : s >= p.sb ? p.xg1 : 0);
      if ((l & 7) == k) wd = (u32x4){(unsigned)s, t1, t, 0u};
      if (k == 2) ta = t;
    }
    st128(wd, crs, l < 32 ? (16384u ^ pl.dyn(l >> 3)) + (l & 7) * 16 : 0x7F000000u, TW_OFF + 1024 + b * 128);
  };
  plan(0);
  for (int b = 1; b < NSEG / 4; ++b) {
    u32x2 dw;
    const unsigned before = shift;
    for (unsigned spins = 0; !spin_fail(spins, cx); shift += p.dret) {
      const int d = (int)(ta + shift + p.keep_at - rt_now());
      if (d < -20 && d > -30000) continue;
      if (d > 60 && d < 2000) sleep_n((d - 40) >> 2);
      while ((unsigned)(ta + shift + p.keep_at - rt_now() - 1u) < 2000u) __builtin_amdgcn_s_sleep(2);
      const unsigned f = flag_get(cx, (GAS u64*)(comm_base(ws) + TW_OFF + (16384u ^ pl.dyn(0)) + 512));
      dw = __builtin_amdgcn_raw_buffer_load_b64(crs, pl.near((l < 32 ? N_DUR : N_RD - 32) + l), 0, 16);
      if ((__builtin_amdgcn_readfirstlane(f) & 255u) >= (unsigned)(4 * b - 2)) { shift = __builtin_amdgcn_readfirstlane(f) >> 8; break; }
    }
    if (b > 2) boost = shift - before > (unsigned)p.dret ? min(boost + p.boost, 12) : shift == before ? max(boost - 1, 0) : boost;
    unsigned dur = dw[1] == cx.tag && (dw[0] & 1023u) ? (unsigned)((float)(dw[0] >> 10) * 64.f * __builtin_amdgcn_rcpf((float)(dw[0] & 1023u))) : 0u;
    const unsigned long long none = __ballot(dur == 0u);
#pragma unroll
    for (int m = 1; m < 32; m <<= 1) dur = max(dur, (unsigned)__shfl_xor((int)dur, m));
#pragma unroll
    for (int k = 0; k < 2; ++k) {
      const int dm = __builtin_amdgcn_readlane(dur, 32 * k), d = dp[k];
      if ((unsigned)(none >> (32 * k)) == 0u) dp[k] = max(min(dm, d + p.dur_inc), d - p.dur_dec);
    }
    plan(b);
  }
}

#define X_ASM_TEXT \
  "s_cmp_gt_u32 %28, 1\n" \
  "s_cbranch_scc1 .LX_other_%=\n" \
  "s_mov_b64 s[40:41], %24\n" \
  "s_and_b32 s41, s41, 0xffff\n" \
  "s_mov_b32 s42, 255852544\n" \
  "s_mov_b32 s43, 0x00020000\n" \
  "s_mov_b32 s46, 4718592\n" \
  "s_mov_b32 s47, 0x00020000\n" \
  "s_lshl_b32 s72, %28, 11\n" \
  "buffer_load_dword v196, off, s[40:43], %38 sc1\n" \
  "s_mov_b64 s[44:45], %25\n" \
  "s_and_b32 s45, s45, 0xffff\n" \
  "s_add_u32 s60, %29, 0\n" \
  "s_lshl_b32 s61, s72, 1\n" \
  "s_add_u32 m0, s61, 16384\n" \
  "s_nop 0\n" \
  "buffer_load_dwordx4 v200, s[44:47], s60 offen lds\n" \
  "buffer_load_dwordx4 v201, s[44:47], s60 offen offset:1024 lds\n" \
  "buffer_load_dwordx4 v202, s[44:47], s60 offen offset:2048 lds\n" \
  "buffer_load_dwordx4 v203, s[44:47], s60 offen offset:3072 lds\n" \
  "s_add_u32 s60, %29, 524288\n" \
  "s_lshl_b32 s61, s72, 1\n" \
  "s_add_u32 m0, s61, 32768\n" \
  "s_nop 0\n" \
  "buffer_load_dwordx4 v200, s[44:47], s60 offen lds\n" \
  "buffer_load_dwordx4 v201, s[44:47], s60 offen offset:1024 lds\n" \
  "buffer_load_dwordx4 v202, s[44:47], s60 offen offset:2048 lds\n" \
  "buffer_load_dwordx4 v203, s[44:47], s60 offen offset:3072 lds\n" \
  "s_add_u32 s60, %29, 1048576\n" \
  "s_lshl_b32 s61, s72, 1\n" \
  "s_add_u32 m0, s61, 49152\n" \
  "s_nop 0\n" \
  "buffer_load_dwordx4 v200, s[44:47], s60 offen lds\n" \
  "buffer_load_dwordx4 v201, s[44:47], s60 offen offset:1024 lds\n" \
  "buffer_load_dwordx4 v202, s[44:47], s60 offen offset:2048 lds\n" \
  "buffer_load_dwordx4 v203, s[44:47], s60 offen offset:3072 lds\n" \
  "s_add_u32 s60, %29, 1572864\n" \
  "s_lshl_b32 s61, s72, 1\n" \
  "s_add_u32 m0, s61, 65536\n" \
  "s_nop 0\n" \
  "buffer_load_dwordx4 v200, s[44:47], s60 offen lds\n" \
  "buffer_load_dwordx4 v201, s[44:47], s60 offen offset:1024 lds\n" \
  "buffer_load_dwordx4 v202, s[44:47], s60 offen offset:2048 lds\n" \
  "buffer_load_dwordx4 v203, s[44:47], s60 offen offset:3072 lds\n" \
  "s_add_u32 s60, %29, 2097152\n" \
  "s_lshl_b32 s61, s72, 1\n" \
  "s_add_u32 m0, s61, 81920\n" \
  "s_nop 0\n" \
  "buffer_load_dwordx4 v200, s[44:47], s60 offen lds\n" \
  "buffer_load_dwordx4 v201, s[44:47], s60 offen offset:1024 lds\n" \
  "buffer_load_dwordx4 v202, s[44:47], s60 offen offset:2048 lds\n" \
  "buffer_load_dwordx4 v203, s[44:47], s60 offen offset:3072 lds\n" \
  "s_add_u32 s60, %29, 2621440\n" \
  "s_lshl_b32 s61, s72, 1\n" \
  "s_add_u32 m0, s61, 98304\n" \
  "s_nop 0\n" \
  "buffer_load_dwordx4 v200, s[44:47], s60 offen lds\n" \
  "buffer_load_dwordx4 v201, s[44:47], s60 offen offset:1024 lds\n" \
  "buffer_load_dwordx4 v202, s[44:47], s60 offen offset:2048 lds\n" \
  "buffer_load_dwordx4 v203, s[44:47], s60 offen offset:3072 lds\n" \
  "s_add_u32 s60, %29, 3145728\n" \
  "s_lshl_b32 s61, s72, 1\n" \
  "s_add_u32 m0, s61, 114688\n" \
  "s_nop 0\n" \
  "buffer_load_dwordx4 v200, s[44:47], s60 offen lds\n" \
  "buffer_load_dwordx4 v201, s[44:47], s60 offen offset:1024 lds\n" \
  "buffer_load_dwordx4 v202, s[44:47], s60 offen offset:2048 lds\n" \
  "buffer_load_dwordx4 v203, s[44:47], s60 offen offset:3072 lds\n" \
  "s_add_u32 s60, %29, 3670016\n" \
  "s_lshl_b32 s61, s72, 1\n" \
  "s_add_u32 m0, s61, 131072\n" \
  "s_nop 0\n" \
  "buffer_load_dwordx4 v200, s[44:47], s60 offen lds\n" \
  "buffer_load_dwordx4 v201, s[44:47], s60 offen offset:1024 lds\n" \
  "buffer_load_dwordx4 v202, s[44:47], s60 offen offset:2048 lds\n" \
  "buffer_load_dwordx4 v203, s[44:47], s60 offen offset:3072 lds\n" \
  "s_add_u32 s60, %29, 4194304\n" \
  "s_lshl_b32 s61, s72, 1\n" \
  "s_add_u32 m0, s61, 147456\n" \
  "s_nop 0\n" \
  "buffer_load_dwordx4 v200, s[44:47], s60 offen lds\n" \
  "buffer_load_dwordx4 v201, s[44:47], s60 offen offset:1024 lds\n" \
  "buffer_load_dwordx4 v202, s[44:47], s60 offen offset:2048 lds\n" \
  "buffer_load_dwordx4 v203, s[44:47], s60 offen offset:3072 lds\n" \
  "s_mov_b64 s[44:45], %27\n" \
  "s_and_b32 s45, s45, 0xffff\n" \
  "s_lshr_b32 s60, s72, 3\n" \
  "s_mov_b32 m0, s72\n" \
  "s_nop 0\n" \
  "buffer_load_dwordx4 v206, s[44:47], s60 offen lds\n" \
  "s_add_u32 s60, s60, 128\n" \
  "s_add_u32 m0, s72, 1024\n" \
  "s_nop 0\n" \
  "buffer_load_dwordx4 v206, s[44:47], s60 offen lds\n" \
  "s_movk_i32 s60, 1024\n" \
  "s_movk_i32 m0, 8192\n" \
  "s_nop 0\n" \
  "buffer_load_dwordx4 v206, s[44:47], s60 offen lds\n" \
  "s_waitcnt vmcnt(27)\n" \
  "s_barrier\n" \
  "v_add_u32 v194, 16384, v204\n" \
  "ds_read_b64_tr_b16 a[0:1], v194\n" \
  "ds_read_b64_tr_b16 a[2:3], v194 offset:512\n" \
  "ds_read_b64_tr_b16 a[4:5], v194 offset:8192\n" \
  "ds_read_b64_tr_b16 a[6:7], v194 offset:8704\n" \
  "v_add_u32 v195, 16384, v205\n" \
  "ds_read_b64_tr_b16 a[72:73], v195\n" \
  "ds_read_b64_tr_b16 a[74:75], v195 offset:512\n" \
  "ds_read_b64_tr_b16 a[76:77], v195 offset:8192\n" \
  "ds_read_b64_tr_b16 a[78:79], v195 offset:8704\n" \
  "v_add_u32 v194, 32768, v204\n" \
  "ds_read_b64_tr_b16 a[8:9], v194\n" \
  "ds_read_b64_tr_b16 a[10:11], v194 offset:512\n" \
  "ds_read_b64_tr_b16 a[12:13], v194 offset:8192\n" \
  "ds_read_b64_tr_b16 a[14:15], v194 offset:8704\n" \
  "v_add_u32 v195, 32768, v205\n" \
  "ds_read_b64_tr_b16 a[80:81], v195\n" \
  "ds_read_b64_tr_b16 a[82:83], v195 offset:512\n" \
  "ds_read_b64_tr_b16 a[84:85], v195 offset:8192\n" \
  "ds_read_b64_tr_b16 a[86:87], v195 offset:8704\n" \
  "v_add_u32 v194, 49152, v204\n" \
  "ds_read_b64_tr_b16 a[16:17], v194\n" \
  "ds_read_b64_tr_b16 a[18:19], v194 offset:512\n" \
  "ds_read_b64_tr_b16 a[20:21], v194 offset:8192\n" \
  "ds_read_b64_tr_b16 a[22:23], v194 offset:8704\n" \
  "v_add_u32 v195, 49152, v205\n" \
  "ds_read_b64_tr_b16 a[88:89], v195\n" \
  "ds_read_b64_tr_b16 a[90:91], v195 offset:512\n" \
  "ds_read_b64_tr_b16 a[92:93], v195 offset:8192\n" \
  "ds_read_b64_tr_b16 a[94:95], v195 offset:8704\n" \
  "s_waitcnt lgkmcnt(0)\n" \
  "s_barrier\n" \
  "s_mov_b64 s[44:45], %26\n" \
  "s_and_b32 s45, s45, 0xffff\n" \
  "s_add_u32 s60, %29, 0\n" \
  "s_lshl_b32 s61, s72, 1\n" \
  "s_add_u32 m0, s61, 16384\n" \
  "s_nop 0\n" \
  "buffer_load_dwordx4 v200, s[44:47], s60 offen lds\n" \
  "buffer_load_dwordx4 v201, s[44:47], s60 offen offset:1024 lds\n" \
  "buffer_load_dwordx4 v202, s[44:47], s60 offen offset:2048 lds\n" \
  "buffer_load_dwordx4 v203, s[44:47], s60 offen offset:3072 lds\n" \
  "s_add_u32 s60, %29, 524288\n" \
  "s_lshl_b32 s61, s72, 1\n" \
  "s_add_u32 m0, s61, 32768\n" \
  "s_nop 0\n" \
  "buffer_load_dwordx4 v200, s[44:47], s60 offen lds\n" \
  "buffer_load_dwordx4 v201, s[44:47], s60 offen offset:1024 lds\n" \
  "buffer_load_dwordx4 v202, s[44:47], s60 offen offset:2048 lds\n" \
  "buffer_load_dwordx4 v203, s[44:47], s60 offen offset:3072 lds\n" \
  "s_add_u32 s60, %29, 1048576\n" \
  "s_lshl_b32 s61, s72, 1\n" \
  "s_add_u32 m0, s61, 49152\n" \
  "s_nop 0\n" \
  "buffer_load_dwordx4 v200, s[44:47], s60 offen lds\n" \
  "buffer_load_dwordx4 v201, s[44:47], s60 offen offset:1024 lds\n" \
  "buffer_load_dwordx4 v202, s[44:47], s60 offen offset:2048 lds\n" \
  "buffer_load_dwordx4 v203, s[44:47], s60 offen offset:3072 lds\n" \
  "v_readfirstlane_b32 s55, v196\n" \
  "s_add_u32 s55, s55, 1\n" \
  "s_waitcnt vmcnt(27)\n" \
  "s_barrier\n" \
  "v_add_u32 v194, 65536, v204\n" \
  "ds_read_b64_tr_b16 a[24:25], v194\n" \
  "ds_read_b64_tr_b16 a[26:27], v194 offset:512\n" \
  "ds_read_b64_tr_b16 a[28:29], v194 offset:8192\n" \
  "ds_read_b64_tr_b16 a[30:31], v194 offset:8704\n" \
  "v_add_u32 v195, 65536, v205\n" \
  "ds_read_b64_tr_b16 a[96:97], v195\n" \
  "ds_read_b64_tr_b16 a[98:99], v195 offset:512\n" \
  "ds_read_b64_tr_b16 a[100:101], v195 offset:8192\n" \
  "ds_read_b64_tr_b16 a[102:103], v195 offset:8704\n" \
  "v_add_u32 v194, 81920, v204\n" \
  "ds_read_b64_tr_b16 a[32:33], v194\n" \
  "ds_read_b64_tr_b16 a[34:35], v194 offset:512\n" \
  "ds_read_b64_tr_b16 a[36:37], v194 offset:8192\n" \
  "ds_read_b64_tr_b16 a[38:39], v194 offset:8704\n" \
  "v_add_u32 v195, 81920, v205\n" \
  "ds_read_b64_tr_b16 a[104:105], v195\n" \
  "ds_read_b64_tr_b16 a[106:107], v195 offset:512\n" \
  "ds_read_b64_tr_b16 a[108:109], v195 offset:8192\n" \
  "ds_read_b64_tr_b16 a[110:111], v195 offset:8704\n" \
  "v_add_u32 v194, 98304, v204\n" \
  "ds_read_b64_tr_b16 a[40:41], v194\n" \
  "ds_read_b64_tr_b16 a[42:43], v194 offset:512\n" \
  "ds_read_b64_tr_b16 a[44:45], v194 offset:8192\n" \
  "ds_read_b64_tr_b16 a[46:47], v194 offset:8704\n" \
  "v_add_u32 v195, 98304, v205\n" \
  "ds_read_b64_tr_b16 a[112:113], v195\n" \
  "ds_read_b64_tr_b16 a[114:115], v195 offset:512\n" \
  "ds_read_b64_tr_b16 a[116:117], v195 offset:8192\n" \
  "ds_read_b64_tr_b16 a[118:119], v195 offset:8704\n" \
  "s_waitcnt lgkmcnt(0)\n" \
  "s_barrier\n" \
  "s_add_u32 s60, %29, 1572864\n" \
  "s_lshl_b32 s61, s72, 1\n" \
  "s_add_u32 m0, s61, 65536\n" \
  "s_nop 0\n" \
  "buffer_load_dwordx4 v200, s[44:47], s60 offen lds\n" \
  "buffer_load_dwordx4 v201, s[44:47], s60 offen offset:1024 lds\n" \
  "buffer_load_dwordx4 v202, s[44:47], s60 offen offset:2048 lds\n" \
  "buffer_load_dwordx4 v203, s[44:47], s60 offen offset:3072 lds\n" \
  "s_add_u32 s60, %29, 2097152\n" \
  "s_lshl_b32 s61, s72, 1\n" \
  "s_add_u32 m0, s61, 81920\n" \
  "s_nop 0\n" \
  "buffer_load_dwordx4 v200, s[44:47], s60 offen lds\n" \
  "buffer_load_dwordx4 v201, s[44:47], s60 offen offset:1024 lds\n" \
  "buffer_load_dwordx4 v202, s[44:47], s60 offen offset:2048 lds\n" \
  "buffer_load_dwordx4 v203, s[44:47], s60 offen offset:3072 lds\n" \
  "s_add_u32 s60, %29, 2621440\n" \
  "s_lshl_b32 s61, s72, 1\n" \
  "s_add_u32 m0, s61, 98304\n" \
  "s_nop 0\n" \
  "buffer_load_dwordx4 v200, s[44:47], s60 offen lds\n" \
  "buffer_load_dwordx4 v201, s[44:47], s60 offen offset:1024 lds\n" \
  "buffer_load_dwordx4 v202, s[44:47], s60 offen offset:2048 lds\n" \
  "buffer_load_dwordx4 v203, s[44:47], s60 offen offset:3072 lds\n" \
  "s_waitcnt vmcnt(27)\n" \
  "s_barrier\n" \
  "v_add_u32 v194, 114688, v204\n" \
  "ds_read_b64_tr_b16 a[48:49], v194\n" \
  "ds_read_b64_tr_b16 a[50:51], v194 offset:512\n" \
  "ds_read_b64_tr_b16 a[52:53], v194 offset:8192\n" \
  "ds_read_b64_tr_b16 a[54:55], v194 offset:8704\n" \
  "v_add_u32 v195, 114688, v205\n" \
  "ds_read_b64_tr_b16 a[120:121], v195\n" \
  "ds_read_b64_tr_b16 a[122:123], v195 offset:512\n" \
  "ds_read_b64_tr_b16 a[124:125], v195 offset:8192\n" \
  "ds_read_b64_tr_b16 a[126:127], v195 offset:8704\n" \
  "v_add_u32 v194, 131072, v204\n" \
  "ds_read_b64_tr_b16 a[56:57], v194\n" \
  "ds_read_b64_tr_b16 a[58:59], v194 offset:512\n" \
  "ds_read_b64_tr_b16 a[60:61], v194 offset:8192\n" \
  "ds_read_b64_tr_b16 a[62:63], v194 offset:8704\n" \
  "v_add_u32 v195, 131072, v205\n" \
  "ds_read_b64_tr_b16 a[128:129], v195\n" \
  "ds_read_b64_tr_b16 a[130:131], v195 offset:512\n" \
  "ds_read_b64_tr_b16 a[132:133], v195 offset:8192\n" \
  "ds_read_b64_tr_b16 a[134:135], v195 offset:8704\n" \
  "v_add_u32 v194, 147456, v204\n" \
  "ds_read_b64_tr_b16 a[64:65], v194\n" \
  "ds_read_b64_tr_b16 a[66:67], v194 offset:512\n" \
  "ds_read_b64_tr_b16 a[68:69], v194 offset:8192\n" \
  "ds_read_b64_tr_b16 a[70:71], v194 offset:8704\n" \
  "v_add_u32 v195, 147456, v205\n" \
  "ds_read_b64_tr_b16 a[136:137], v195\n" \
  "ds_read_b64_tr_b16 a[138:139], v195 offset:512\n" \
  "ds_read_b64_tr_b16 a[140:141], v195 offset:8192\n" \
  "ds_read_b64_tr_b16 a[142:143], v195 offset:8704\n" \
  "s_waitcnt lgkmcnt(0)\n" \
  "s_barrier\n" \
  "s_add_u32 s60, %29, 3145728\n" \
  "s_lshl_b32 s61, s72, 1\n" \
  "s_add_u32 m0, s61, 114688\n" \
  "s_nop 0\n" \
  "buffer_load_dwordx4 v200, s[44:47], s60 offen lds\n" \
  "buffer_load_dwordx4 v201, s[44:47], s60 offen offset:1024 lds\n" \
  "buffer_load_dwordx4 v202, s[44:47], s60 offen offset:2048 lds\n" \
  "buffer_load_dwordx4 v203, s[44:47], s60 offen offset:3072 lds\n" \
  "s_add_u32 s60, %29, 3670016\n" \
  "s_lshl_b32 s61, s72, 1\n" \
  "s_add_u32 m0, s61, 131072\n" \
  "s_nop 0\n" \
  "buffer_load_dwordx4 v200, s[44:47], s60 offen lds\n" \
  "buffer_load_dwordx4 v201, s[44:47], s60 offen offset:1024 lds\n" \
  "buffer_load_dwordx4 v202, s[44:47], s60 offen offset:2048 lds\n" \
  "buffer_load_dwordx4 v203, s[44:47], s60 offen offset:3072 lds\n" \
  "s_add_u32 s60, %29, 4194304\n" \
  "s_lshl_b32 s61, s72, 1\n" \
  "s_add_u32 m0, s61, 147456\n" \
  "s_nop 0\n" \
  "buffer_load_dwordx4 v200, s[44:47], s60 offen lds\n" \
  "buffer_load_dwordx4 v201, s[44:47], s60 offen offset:1024 lds\n" \
  "buffer_load_dwordx4 v202, s[44:47], s60 offen offset:2048 lds\n" \
  "buffer_load_dwordx4 v203, s[44:47], s60 offen offset:3072 lds\n" \
  "buffer_load_dwordx2 v[198:199], off, s[40:43], %37 sc1\n" \
  "s_waitcnt vmcnt(25)\n" \
  "s_barrier\n" \
  "v_add_u32 v195, 16384, v205\n" \
  "ds_read_b64_tr_b16 a[216:217], v195\n" \
  "ds_read_b64_tr_b16 a[218:219], v195 offset:512\n" \
  "ds_read_b64_tr_b16 a[220:221], v195 offset:8192\n" \
  "ds_read_b64_tr_b16 a[222:223], v195 offset:8704\n" \
  "v_add_u32 v195, 32768, v205\n" \
  "ds_read_b64_tr_b16 a[224:225], v195\n" \
  "ds_read_b64_tr_b16 a[226:227], v195 offset:512\n" \
  "ds_read_b64_tr_b16 a[228:229], v195 offset:8192\n" \
  "ds_read_b64_tr_b16 a[230:231], v195 offset:8704\n" \
  "v_add_u32 v195, 49152, v205\n" \
  "ds_read_b64_tr_b16 a[232:233], v195\n" \
  "ds_read_b64_tr_b16 a[234:235], v195 offset:512\n" \
  "ds_read_b64_tr_b16 a[236:237], v195 offset:8192\n" \
  "ds_read_b64_tr_b16 a[238:239], v195 offset:8704\n" \
  "s_waitcnt lgkmcnt(0)\n" \
  "s_waitcnt vmcnt(13)\n" \
  "s_barrier\n" \
  "v_add_u32 v195, 65536, v205\n" \
  "ds_read_b64_tr_b16 a[240:241], v195\n" \
  "ds_read_b64_tr_b16 a[242:243], v195 offset:512\n" \
  "ds_read_b64_tr_b16 a[244:245], v195 offset:8192\n" \
  "ds_read_b64_tr_b16 a[246:247], v195 offset:8704\n" \
  "v_add_u32 v194, 81920, v204\n" \
  "ds_read_b64_tr_b16 a[176:177], v194\n" \
  "ds_read_b64_tr_b16 a[178:179], v194 offset:512\n" \
  "ds_read_b64_tr_b16 a[180:181], v194 offset:8192\n" \
  "ds_read_b64_tr_b16 a[182:183], v194 offset:8704\n" \
  "v_add_u32 v195, 81920, v205\n" \
  "ds_read_b64_tr_b16 a[248:249], v195\n" \
  "ds_read_b64_tr_b16 a[250:251], v195 offset:512\n" \
  "ds_read_b64_tr_b16 a[252:253], v195 offset:8192\n" \
  "ds_read_b64_tr_b16 a[254:255], v195 offset:8704\n" \
  "v_add_u32 v194, 98304, v204\n" \
  "ds_read_b64_tr_b16 a[184:185], v194\n" \
  "ds_read_b64_tr_b16 a[186:187], v194 offset:512\n" \
  "ds_read_b64_tr_b16 a[188:189], v194 offset:8192\n" \
  "ds_read_b64_tr_b16 a[190:191], v194 offset:8704\n" \
  "v_add_u32 v195, 98304, v205\n" \
  "ds_read_b64_tr_b16 v[0:1], v195\n" \
  "ds_read_b64_tr_b16 v[2:3], v195 offset:512\n" \
  "ds_read_b64_tr_b16 v[4:5], v195 offset:8192\n" \
  "ds_read_b64_tr_b16 v[6:7], v195 offset:8704\n" \
  "s_waitcnt lgkmcnt(0)\n" \
  "s_waitcnt vmcnt(1)\n" \
  "s_barrier\n" \
  "v_add_u32 v194, 114688, v204\n" \
  "ds_read_b64_tr_b16 a[192:193], v194\n" \
  "ds_read_b64_tr_b16 a[194:195], v194 offset:512\n" \
  "ds_read_b64_tr_b16 a[196:197], v194 offset:8192\n" \
  "ds_read_b64_tr_b16 a[198:199], v194 offset:8704\n" \
  "v_add_u32 v195, 114688, v205\n" \
  "ds_read_b64_tr_b16 v[8:9], v195\n" \
  "ds_read_b64_tr_b16 v[10:11], v195 offset:512\n" \
  "ds_read_b64_tr_b16 v[12:13], v195 offset:8192\n" \
  "ds_read_b64_tr_b16 v[14:15], v195 offset:8704\n" \
  "v_add_u32 v194, 131072, v204\n" \
  "ds_read_b64_tr_b16 a[200:201], v194\n" \
  "ds_read_b64_tr_b16 a[202:203], v194 offset:512\n" \
  "ds_read_b64_tr_b16 a[204:205], v194 offset:8192\n" \
  "ds_read_b64_tr_b16 a[206:207], v194 offset:8704\n" \
  "v_add_u32 v195, 131072, v205\n" \
  "ds_read_b64_tr_b16 v[16:17], v195\n" \
  "ds_read_b64_tr_b16 v[18:19], v195 offset:512\n" \
  "ds_read_b64_tr_b16 v[20:21], v195 offset:8192\n" \
  "ds_read_b64_tr_b16 v[22:23], v195 offset:8704\n" \
  "v_add_u32 v194, 147456, v204\n" \
  "ds_read_b64_tr_b16 a[208:209], v194\n" \
  "ds_read_b64_tr_b16 a[210:211], v194 offset:512\n" \
  "ds_read_b64_tr_b16 a[212:213], v194 offset:8192\n" \
  "ds_read_b64_tr_b16 a[214:215], v194 offset:8704\n" \
  "v_add_u32 v195, 147456, v205\n" \
  "ds_read_b64_tr_b16 v[24:25], v195\n" \
  "ds_read_b64_tr_b16 v[26:27], v195 offset:512\n" \
  "ds_read_b64_tr_b16 v[28:29], v195 offset:8192\n" \
  "ds_read_b64_tr_b16 v[30:31], v195 offset:8704\n" \
  "s_waitcnt lgkmcnt(0)\n" \
  "s_waitcnt vmcnt(0)\n" \
  "v_readfirstlane_b32 s96, v198\n" \
  "v_readfirstlane_b32 s63, v199\n" \
  "s_cmp_eq_u32 s63, s55\n" \
  "s_cselect_b32 s96, s96, 0\n" \
  "s_cmp_ge_u32 s96, 0xc0000000\n" \
  "s_cbranch_scc1 .LX_go_x_%=\n" \
  "s_mov_b32 s69, 0\n" \
  ".LX_go_%=:\n" \
  "buffer_load_dwordx2 v[196:197], off, s[40:43], %37 sc1\n" \
  "s_waitcnt vmcnt(0)\n" \
  "v_readfirstlane_b32 s96, v196\n" \
  "v_readfirstlane_b32 s63, v197\n" \
  "s_cmp_eq_u32 s63, s55\n" \
  "s_cselect_b32 s96, s96, 0\n" \
  "s_cmp_ge_u32 s96, 0xc0000000\n" \
  "s_cbranch_scc1 .LX_go_x_%=\n" \
  "s_sleep 8\n" \
  "s_add_u32 s69, s69, 1\n" \
  "s_and_b32 s63, s69, 1023\n" \
  "s_cbranch_scc1 .LX_go_%=\n" \
  "s_cmp_gt_u32 s69, 2097152\n" \
  "s_cbranch_scc1 .LX_go_set_%=\n" \
  "buffer_load_dwordx2 v[196:197], off, s[40:43], %38 offset:128 sc1\n" \
  "s_waitcnt vmcnt(0)\n" \
  "v_readfirstlane_b32 s63, v197\n" \
  "s_cmp_eq_u32 s63, s55\n" \
  "s_cbranch_scc0 .LX_go_%=\n" \
  "s_branch .LX_go_x_%=\n" \
  ".LX_go_set_%=:\n" \
  "v_mov_b32 v196, 1\n" \
  "v_mov_b32 v197, s55\n" \
  "buffer_store_dwordx2 v[196:197], off, s[40:43], %38 offset:128 sc1\n" \
  "s_waitcnt vmcnt(0)\n" \
  ".LX_go_x_%=:\n" \
  "s_and_b32 s60, s96, 2\n" \
  "s_lshl_b32 s60, s60, 12\n" \
  "s_xor_b32 s74, %34, s60\n" \
  "s_xor_b32 s75, %36, s60\n" \
  "s_bfe_u32 s56, s96, 0x30010\n" \
  "s_lshl_b32 s56, s56, 12\n" \
  "s_or_b32 s56, s56, 0x200000\n" \
  "s_mov_b32 s57, 1052672\n" \
  "s_lshl_b32 s61, %33, 2\n" \
  "s_lshr_b32 s51, s96, s61\n" \
  "s_and_b32 s51, s51, 7\n" \
  "s_lshl_b32 s51, s51, 12\n" \
  "s_xor_b32 s51, s51, 4210688\n" \
  "s_mov_b32 s71, 0\n" \
  "s_add_u32 s45, s51, 886784\n" \
  "s_mov_b32 s46, -1\n" \
  "v_mov_b64 v[32:33], -1\n" \
  "v_mov_b64 v[34:35], -1\n" \
  "v_and_b32 v194, 3, v246\n" \
  "v_lshlrev_b32 v194, 2, v194\n" \
  "v_lshrrev_b32 v194, v194, s96\n" \
  "v_and_b32 v194, 7, v194\n" \
  "v_lshlrev_b32 v194, 12, v194\n" \
  "v_and_b32 v246, -4, v246\n" \
  "v_xor_b32 v246, v246, v194\n" \
  "v_and_b32 v194, 3, v247\n" \
  "v_lshlrev_b32 v194, 2, v194\n" \
  "v_lshrrev_b32 v194, v194, s96\n" \
  "v_and_b32 v194, 7, v194\n" \
  "v_lshlrev_b32 v194, 12, v194\n" \
  "v_and_b32 v247, -4, v247\n" \
  "v_xor_b32 v247, v247, v194\n" \
  "s_xor_b32 s62, s56, s57\n" \
  "s_nop 1\n" \
  "s_andn2_b32 s60, 2, s96\n" \
  "s_lshl_b32 s60, s60, 12\n" \
  "s_xor_b32 s60, s60, 8192\n" \
  "s_xor_b32 s58, %31, s60\n" \
  "s_mov_b32 s49, 0\n" \
  "s_add_u32 s47, %41, s60\n" \
  "s_add_u32 s97, s60, 8388608\n" \
  "s_xor_b32 s50, %32, s60\n" \
  "s_mov_b32 s48, 0\n" \
  "s_mov_b32 s54, 0\n" \
  "s_cmp_eq_u32 %28, 0\n" \
  "s_cselect_b64 s[88:89], -1, 0\n" \
  "s_mov_b32 s68, 0\n" \
  "s_mov_b32 s70, 1\n" \
  "s_mov_b32 s73, 0\n" \
  "s_mov_b32 s44, 0\n" \
  "s_mov_b32 s53, 0\n" \
  "s_memrealtime s[86:87]\n" \
  "s_memtime s[76:77]\n" \
  "s_waitcnt lgkmcnt(0)\n" \
  "s_mov_b32 s90, s76\n" \
  "s_mov_b32 s87, 340\n" \
  "s_mov_b32 s64, 0x3e0293ee\n" \
  "s_mov_b32 s65, 0xbe0293ee\n" \
  "v_mov_b64 v[44:45], 0\n" \
  "v_mov_b64 v[46:47], 0\n" \
  "v_mov_b64 v[48:49], 0\n" \
  "v_mov_b64 v[50:51], 0\n" \
  "v_mov_b64 v[52:53], 0\n" \
  "v_mov_b64 v[54:55], 0\n" \
  "v_mov_b64 v[56:57], 0\n" \
  "v_mov_b64 v[58:59], 0\n" \
  "v_mov_b32 v40, 0\n" \
  "v_mov_b32 v41, s55\n" \
  "v_mov_b64 v[42:43], v[40:41]\n" \
  "s_mov_b64 s[92:93], 0\n" \
  "s_mov_b32 s92, 0xffff0000\n" \
  "s_lshr_b32 s60, s72, 1\n" \
  "s_add_u32 s60, s60, 32768\n" \
  "v_add_u32 v178, s60, v240\n" \
  "v_xor_b32 v246, 1073741824, v246\n" \
  "v_xor_b32 v247, 1073741824, v247\n" \
  "v_mov_b32 v182, 1065369472\n" \
  "v_mov_b32 v183, 1065369472\n" \
  "s_and_b32 s60, s72, 2048\n" \
  "s_add_u32 s50, s50, s60\n" \
  "s_lshr_b32 s60, s60, 1\n" \
  "v_add_u32 v179, s60, v244\n" \
  "s_lshr_b32 s60, s72, 9\n" \
  "s_add_u32 s60, s60, 17408\n" \
  "v_mov_b32 v184, s60\n" \
  "s_mov_b32 s52, 0\n" \
  "s_barrier\n" \
  "ds_write_b32 v184, v40\n" \
  "s_branch .LX_xr_%=\n" \
  ".LX_dry_b_%=:\n" \
  "s_waitcnt vmcnt(0)\n" \
  "s_barrier\n" \
  "ds_read_b64 v[64:65], v178\n" \
  "ds_read_b64 v[66:67], v178 offset:2048\n" \
  "ds_read_b64 v[68:69], v178 offset:4096\n" \
  "ds_read_b64 v[70:71], v178 offset:6144\n" \
  "ds_read_b64 v[72:73], v178 offset:8\n" \
  "ds_read_b64 v[74:75], v178 offset:2056\n" \
  "ds_read_b64 v[76:77], v178 offset:4104\n" \
  "ds_read_b64 v[78:79], v178 offset:6152\n" \
  "v_xor_b32 v178, 8192, v178\n" \
  "s_mov_b32 s67, 2130706432\n" \
  "s_waitcnt vmcnt(0) lgkmcnt(0)\n" \
  "s_barrier\n" \
  "s_branch .LX_p_%=\n" \
  "s_nop 7\n" \
  ".p2align 6\n" \
  ".fill (((40 - (.LX_p_%= - .LX_rot_%=)) & 63) >> 2), 4, 0xBF800000\n" \
  ".LX_rot_%=:\n" \
  "s_xor_b32 s51, s51, s56\n" \
  "s_xor_b32 s71, s71, s56\n" \
  "v_xor_b32 v246, s56, v246\n" \
  "v_xor_b32 v247, s56, v247\n" \
  "s_mov_b32 s60, s56\n" \
  "s_mov_b32 s56, s57\n" \
  "s_mov_b32 s57, s60\n" \
  "s_add_u32 s59, s51, 819200\n" \
  "s_mov_b32 m0, 0\n" \
  "s_mov_b32 s70, 0\n" \
  "s_mov_b32 s68, 0\n" \
  "s_waitcnt lgkmcnt(0)\n" \
  "s_cmp_lg_u32 %28, 0\n" \
  "s_cbranch_scc1 .LX_g3_%=\n" \
  "s_mov_b32 s69, 0\n" \
  ".LX_tw_%=:\n" \
  "v_readfirstlane_b32 s63, v36\n" \
  "s_cmp_lg_u32 s63, s48\n" \
  "s_cbranch_scc1 .LX_tw_re_%=\n" \
  "v_readfirstlane_b32 s53, v38\n" \
  ".LX_rt_%=:\n" \
  "s_memrealtime s[60:61]\n" \
  "s_add_u32 s73, s53, s44\n" \
  "s_waitcnt lgkmcnt(0)\n" \
  "s_sub_u32 s62, s73, s60\n" \
  "s_cmp_ge_u32 s62, 2000\n" \
  "s_cbranch_scc1 .LX_lat_%=\n" \
  "s_sub_u32 s62, s62, 30\n" \
  ".LX_slp_%=:\n" \
  "s_cmp_le_i32 s62, 0\n" \
  "s_cbranch_scc1 .LX_clk_%=\n" \
  "s_sleep 1\n" \
  "s_sub_u32 s62, s62, 3\n" \
  "s_branch .LX_slp_%=\n" \
  ".LX_clk_%=:\n" \
  "s_memrealtime s[60:61]\n" \
  "s_waitcnt lgkmcnt(0)\n" \
  "s_sub_u32 s62, s73, s60\n" \
  "s_cmp_gt_i32 s62, 21\n" \
  "s_cbranch_scc1 .LX_clk_%=\n" \
  "s_cmp_le_i32 s62, 11\n" \
  "s_cbranch_scc1 .LX_clk2_%=\n" \
  "s_memrealtime s[76:77]\n" \
  "s_nop 4\n" \
  "s_memrealtime s[78:79]\n" \
  "s_nop 4\n" \
  "s_memrealtime s[80:81]\n" \
  "s_nop 4\n" \
  "s_memrealtime s[82:83]\n" \
  "s_waitcnt lgkmcnt(0)\n" \
  "s_mov_b32 s77, 1\n" \
  "s_cmp_eq_u32 s78, s76\n" \
  "s_addc_u32 s77, s77, 0\n" \
  "s_cmp_eq_u32 s80, s76\n" \
  "s_addc_u32 s77, s77, 0\n" \
  "s_cmp_eq_u32 s82, s76\n" \
  "s_addc_u32 s77, s77, 0\n" \
  "s_mul_i32 s77, s77, 24\n" \
  "s_lshr_b32 s78, s87, 2\n" \
  "s_sub_u32 s77, s78, s77\n" \
  "s_max_i32 s77, s77, 0\n" \
  "s_sub_u32 s76, s73, s76\n" \
  "s_mul_i32 s76, s76, s87\n" \
  "s_ashr_i32 s76, s76, 4\n" \
  "s_sub_u32 s76, s76, s77\n" \
  "s_sub_u32 s76, s76, 200\n" \
  "s_max_i32 s76, s76, 0\n" \
  "s_min_i32 s76, s76, 320\n" \
  "s_lshr_b32 s76, s76, 3\n" \
  "s_lshl_b32 s76, s76, 2\n" \
  "s_mov_b32 s68, 1\n" \
  "s_getpc_b64 s[78:79]\n" \
  "s_sub_u32 s76, 180, s76\n" \
  "s_add_u32 s78, s78, s76\n" \
  "s_addc_u32 s79, s79, 0\n" \
  "s_setpc_b64 s[78:79]\n" \
  "s_nop 1\n" \
  "s_nop 1\n" \
  "s_nop 1\n" \
  "s_nop 1\n" \
  "s_nop 1\n" \
  "s_nop 1\n" \
  "s_nop 1\n" \
  "s_nop 1\n" \
  "s_nop 1\n" \
  "s_nop 1\n" \
  "s_nop 1\n" \
  "s_nop 1\n" \
  "s_nop 1\n" \
  "s_nop 1\n" \
  "s_nop 1\n" \
  "s_nop 1\n" \
  "s_nop 1\n" \
  "s_nop 1\n" \
  "s_nop 1\n" \
  "s_nop 1\n" \
  "s_nop 1\n" \
  "s_nop 1\n" \
  "s_nop 1\n" \
  "s_nop 1\n" \
  "s_nop 1\n" \
  "s_nop 1\n" \
  "s_nop 1\n" \
  "s_nop 1\n" \
  "s_nop 1\n" \
  "s_nop 1\n" \
  "s_nop 1\n" \
  "s_nop 1\n" \
  "s_nop 1\n" \
  "s_nop 1\n" \
  "s_nop 1\n" \
  "s_nop 1\n" \
  "s_nop 1\n" \
  "s_nop 1\n" \
  "s_nop 1\n" \
  "s_nop 1\n" \
  ".LX_noclk_%=:\n" \
  "buffer_load_dwordx4 v240, s[40:43], s59 offen sc1 lds\n" \
  "s_memtime s[66:67]\n" \
  "buffer_load_dwordx4 v240, s[40:43], s59 offen offset:1024 sc1 lds\n" \
  "buffer_load_dwordx4 v240, s[40:43], s59 offen offset:2048 sc1 lds\n" \
  "buffer_load_dwordx4 v240, s[40:43], s59 offen offset:3072 sc1 lds\n" \
  "s_add_u32 s60, s59, 32768\n" \
  "s_movk_i32 m0, 4096\n" \
  "s_nop 0\n" \
  "buffer_load_dwordx4 v240, s[40:43], s60 offen sc1 lds\n" \
  "buffer_load_dwordx4 v240, s[40:43], s60 offen offset:1024 sc1 lds\n" \
  "buffer_load_dwordx4 v240, s[40:43], s60 offen offset:2048 sc1 lds\n" \
  "buffer_load_dwordx4 v240, s[40:43], s60 offen offset:3072 sc1 lds\n" \
  "s_add_u32 s60, s59, 65536\n" \
  "s_movk_i32 m0, 8192\n" \
  "s_nop 0\n" \
  "buffer_load_dwordx4 v240, s[40:43], s60 offen sc1 lds\n" \
  ".LX_g3_%=:\n" \
  "s_waitcnt vmcnt(6)\n" \
  "s_barrier\n" \
  ".LX_p_%=:\n" \
  "ds_read_b128 v[100:103], v240\n" \
  "ds_read_b128 v[104:107], v240 offset:1024\n" \
  "ds_read_b128 v[108:111], v240 offset:2048\n" \
  "s_waitcnt lgkmcnt(2)\n" \
  "v_smfmac_f32_16x16x64_bf16 v[44:47], v[100:103], a[0:7], v239\n" \
  "s_waitcnt vmcnt(0)\n" \
  "s_barrier\n" \
  "s_waitcnt lgkmcnt(1)\n" \
  "v_smfmac_f32_16x16x64_bf16 v[44:47], v[104:107], a[8:15], v239\n" \
  "ds_read_b128 v[112:115], v240 offset:3072\n" \
  "v_smfmac_f32_16x16x64_bf16 v[48:51], v[100:103], a[72:79], v239\n" \
  "ds_read_b128 v[116:119], v240 offset:6144\n" \
  "s_waitcnt lgkmcnt(2)\n" \
  "v_smfmac_f32_16x16x64_bf16 v[44:47], v[108:111], a[16:23], v239\n" \
  "ds_read_b128 v[120:123], v240 offset:4096\n" \
  "v_smfmac_f32_16x16x64_bf16 v[48:51], v[104:107], a[80:87], v239\n" \
  "ds_read_b128 v[124:127], v240 offset:5120\n" \
  "v_smfmac_f32_16x16x64_bf16 v[48:51], v[108:111], a[88:95], v239\n" \
  "ds_read_b128 v[128:131], v240 offset:7168\n" \
  "s_waitcnt lgkmcnt(4)\n" \
  "v_smfmac_f32_16x16x64_bf16 v[44:47], v[112:115], a[24:31], v239\n" \
  "ds_read_b128 v[132:135], v240 offset:8192\n" \
  "v_smfmac_f32_16x16x64_bf16 v[48:51], v[112:115], a[96:103], v239\n" \
  "s_waitcnt lgkmcnt(3)\n" \
  "v_smfmac_f32_16x16x64_bf16 v[44:47], v[120:123], a[32:39], v239\n" \
  "s_waitcnt lgkmcnt(2)\n" \
  "v_smfmac_f32_16x16x64_bf16 v[44:47], v[124:127], a[40:47], v239\n" \
  "v_smfmac_f32_16x16x64_bf16 v[44:47], v[116:119], a[48:55], v239\n" \
  "s_waitcnt lgkmcnt(1)\n" \
  "v_smfmac_f32_16x16x64_bf16 v[44:47], v[128:131], a[56:63], v239\n" \
  "s_waitcnt lgkmcnt(0)\n" \
  "v_smfmac_f32_16x16x64_bf16 v[44:47], v[132:135], a[64:71], v239\n" \
  "s_nop 2\n" \
  "v_smfmac_f32_16x16x64_bf16 v[48:51], v[120:123], a[104:111], v239\n" \
  "s_nop 2\n" \
  "v_smfmac_f32_16x16x64_bf16 v[48:51], v[124:127], a[112:119], v239\n" \
  "v_add_f32 v45, v45, v47\n" \
  "v_smfmac_f32_16x16x64_bf16 v[48:51], v[116:119], a[120:127], v239\n" \
  "v_add_f32 v44, v44, v46\n" \
  "v_cmp_u_f32 vcc, v44, v45\n" \
  "v_smfmac_f32_16x16x64_bf16 v[48:51], v[128:131], a[128:135], v239\n" \
  "v_mul_f32 v45, s64, v45\n" \
  "v_smfmac_f32_16x16x64_bf16 v[48:51], v[132:135], a[136:143], v239\n" \
  "s_cbranch_vccnz .LX_slow_%=\n" \
  ".LX_back_%=:\n" \
  "s_cmp_eq_u32 s48, 63\n" \
  "v_mul_f32 v44, s64, v44\n" \
  "v_cvt_pk_bf16_f32 v44, v44, v45\n" \
  "v_smfmac_f32_16x16x64_bf16 v[52:55], v[120:123], a[176:183], v239\n" \
  "ds_write_b32 v241, v44\n" \
  "s_nop 1\n" \
  "v_add_f32 v48, v48, v50\n" \
  "v_add_f32 v49, v49, v51\n" \
  "v_mul_f32 v49, s64, v49\n" \
  "v_mul_f32 v48, s64, v48\n" \
  "v_cvt_pk_bf16_f32 v48, v48, v49\n" \
  "v_smfmac_f32_16x16x64_bf16 v[52:55], v[124:127], a[184:191], v239\n" \
  "ds_write_b32 v241, v48 offset:256\n" \
  "v_smfmac_f32_16x16x64_bf16 v[52:55], v[116:119], a[192:199], v239\n" \
  "v_smfmac_f32_16x16x64_bf16 v[56:59], v[100:103], a[216:223], v239\n" \
  "s_waitcnt lgkmcnt(1)\n" \
  "s_barrier\n" \
  "v_smfmac_f32_16x16x64_bf16 v[52:55], v[128:131], a[200:207], v239\n" \
  "ds_read_b64_tr_b16 v[102:103], v243 offset:1536\n" \
  "ds_read_b64_tr_b16 v[100:101], v243 offset:1024\n" \
  "v_smfmac_f32_16x16x64_bf16 v[52:55], v[132:135], a[208:215], v239\n" \
  "ds_read_b64_tr_b16 v[138:139], v243 offset:512\n" \
  "ds_read_b64_tr_b16 v[136:137], v243\n" \
  "v_smfmac_f32_16x16x64_bf16 v[56:59], v[104:107], a[224:231], v239\n" \
  "s_waitcnt lgkmcnt(4)\n" \
  "s_barrier\n" \
  "v_smfmac_f32_16x16x64_bf16 v[56:59], v[108:111], a[232:239], v239\n" \
  "ds_read_b64_tr_b16 v[104:105], v243 offset:256\n" \
  "ds_read_b64_tr_b16 v[106:107], v243 offset:768\n" \
  "ds_read_b64_tr_b16 v[110:111], v243 offset:1792\n" \
  "v_smfmac_f32_16x16x64_bf16 v[56:59], v[112:115], a[240:247], v239\n" \
  "ds_read_b64_tr_b16 v[108:109], v243 offset:1280\n" \
  "s_waitcnt lgkmcnt(4)\n" \
  "v_mfma_f32_16x16x32_bf16 v[112:115], v[64:67], v[136:139], 0\n" \
  "v_add_f32 v52, v52, v54\n" \
  "v_mfma_f32_16x16x32_bf16 v[112:115], v[68:71], v[100:103], v[112:115]\n" \
  "s_waitcnt lgkmcnt(2)\n" \
  "v_mfma_f32_16x16x32_bf16 v[112:115], v[72:75], v[104:107], v[112:115]\n" \
  "s_waitcnt lgkmcnt(0)\n" \
  "v_mfma_f32_16x16x32_bf16 v[112:115], v[76:79], v[108:111], v[112:115]\n" \
  "s_nop 1\n" \
  "v_add_f32 v53, v53, v55\n" \
  "v_smfmac_f32_16x16x64_bf16 v[56:59], v[120:123], a[248:255], v239\n" \
  "s_barrier\n" \
  "ds_read_b64 v[188:189], v255\n" \
  "s_nop 1\n" \
  "v_exp_f32 v100, v112\n" \
  "v_exp_f32 v101, v113\n" \
  "v_exp_f32 v103, v115\n" \
  "v_exp_f32 v102, v114\n" \
  "v_cvt_pk_bf16_f32 v186, v100, v101\n" \
  "v_cvt_pk_bf16_f32 v187, v102, v103\n" \
  "v_smfmac_f32_16x16x64_bf16 v[56:59], v[124:127], v[0:7], v239\n" \
  "buffer_store_dwordx2 v[186:187], v246, s[40:43], 0 offen sc1\n" \
  "buffer_store_dwordx2 v[186:187], v247, s[40:43], 0 offen sc1\n" \
  "v_mfma_f32_16x16x16_bf16 v[100:103], v[182:183], v[186:187], 0\n" \
  "s_nop 0\n" \
  "s_waitcnt lgkmcnt(0)\n" \
  "v_add_f32 v53, v53, v189\n" \
  "v_smfmac_f32_16x16x64_bf16 v[56:59], v[116:119], v[8:15], v239\n" \
  "v_add_f32 v52, v52, v188\n" \
  "v_smfmac_f32_16x16x64_bf16 v[56:59], v[128:131], v[16:23], v239\n" \
  "v_cvt_pk_bf16_f32 v52, v52, v53\n" \
  "v_smfmac_f32_16x16x64_bf16 v[56:59], v[132:135], v[24:31], v239\n" \
  "ds_write_b32 v248, v100\n" \
  "ds_write_b32 v242, v52\n" \
  "s_nop 5\n" \
  "v_add_f32 v57, v57, v59\n" \
  "v_add_f32 v56, v56, v58\n" \
  "v_cvt_pk_bf16_f32 v56, v56, v57\n" \
  "ds_write_b32 v242, v56 offset:256\n" \
  "s_waitcnt lgkmcnt(1)\n" \
  "s_barrier\n" \
  "s_waitcnt lgkmcnt(0)\n" \
  "s_barrier\n" \
  "s_cbranch_scc1 .LX_xr_%=\n" \
  "s_memrealtime s[98:99]\n" \
  "s_memtime s[84:85]\n" \
  ".LX_loop_%=:\n" \
  "s_cmp_ge_u32 s48, 55\n" \
  "s_cbranch_scc1 .LX_xl_%=\n" \
  ".LX_xr_b_%=:\n" \
  "s_add_u32 s95, s48, 1\n" \
  "s_and_b32 s63, s95, 3\n" \
  "s_lshl_b32 s95, s95, 5\n" \
  "s_lshl_b32 s63, s63, 4\n" \
  "s_sub_u32 s95, s95, s63\n" \
  "s_add_u32 s95, s95, s45\n" \
  "s_mov_b64 exec, s[88:89]\n" \
  "buffer_load_dwordx4 v[36:39], off, s[40:43], s95 sc1\n" \
  "s_mov_b64 exec, -1\n" \
  "v_and_b32 v246, 3221225471, v246\n" \
  "v_and_b32 v247, 3221225471, v247\n" \
  "s_waitcnt lgkmcnt(0)\n" \
  "s_lshl_b32 s60, s66, 10\n" \
  "s_or_b32 s60, s60, s87\n" \
  "s_cmp_lg_u32 s68, 0\n" \
  "s_cselect_b32 s60, s60, 0\n" \
  "v_mov_b32 v187, s60\n" \
  "s_sub_u32 s76, s84, s90\n" \
  "s_sub_u32 s77, s98, s86\n" \
  "v_cvt_f32_u32 v188, s76\n" \
  "v_cvt_f32_u32 v189, s77\n" \
  "v_rcp_f32 v189, v189\n" \
  "v_mul_f32 v188, 16.0, v188\n" \
  "v_fma_f32 v188, v188, v189, 0.5\n" \
  "v_cvt_u32_f32 v188, v188\n" \
  "s_nop 0\n" \
  "v_readfirstlane_b32 s76, v188\n" \
  "s_max_u32 s76, s76, 160\n" \
  "s_min_u32 s76, s76, 400\n" \
  "s_add_u32 s77, s48, 0\n" \
  "s_and_b32 s77, s77, 3\n" \
  "s_cselect_b32 s87, s87, s76\n" \
  "s_cselect_b32 s86, s86, s98\n" \
  "s_cselect_b32 s90, s90, s84\n" \
  "s_add_u32 s48, s48, 1\n" \
  "ds_read_b64 v[64:65], v178\n" \
  "ds_read_b64 v[66:67], v178 offset:2048\n" \
  "ds_read_b64 v[68:69], v178 offset:4096\n" \
  "ds_read_b64 v[70:71], v178 offset:6144\n" \
  "ds_read_b64 v[72:73], v178 offset:8\n" \
  "ds_read_b64 v[74:75], v178 offset:2056\n" \
  "ds_read_b64 v[76:77], v178 offset:4104\n" \
  "ds_read_b64 v[78:79], v178 offset:6152\n" \
  "v_xor_b32 v178, 8192, v178\n" \
  "v_mov_b64 v[44:45], 0\n" \
  "v_mov_b64 v[46:47], 0\n" \
  "v_mov_b64 v[48:49], 0\n" \
  "v_mov_b64 v[50:51], 0\n" \
  "v_mov_b64 v[52:53], 0\n" \
  "v_mov_b64 v[54:55], 0\n" \
  "v_mov_b64 v[56:57], 0\n" \
  "v_mov_b64 v[58:59], 0\n" \
  "v_mov_b32 v40, s48\n" \
  "s_waitcnt vmcnt(0)\n" \
  "ds_write2_b32 v184, v40, v187 offset1:2\n" \
  "s_cmp_eq_u32 s48, 58\n" \
  "s_cbranch_scc1 .LX_ef_%=\n" \
  ".LX_ef_b_%=:\n" \
  "s_branch .LX_rot_%=\n" \
  "s_nop 7\n" \
  ".LX_slow_%=:\n" \
  "s_bitcmp1_b32 s70, 0\n" \
  "s_cbranch_scc1 .LX_back_%=\n" \
  "s_waitcnt lgkmcnt(0)\n" \
  "s_cmp_lt_u32 s70, 10\n" \
  "s_nop 3\n" \
  "s_cbranch_scc1 .LX_nol_%=\n" \
  "v_max_u32 v189, v100, v101\n" \
  "v_max3_u32 v189, v189, v102, v103\n" \
  "v_max3_u32 v189, v189, v104, v105\n" \
  "v_max3_u32 v189, v189, v106, v107\n" \
  "v_max3_u32 v189, v189, v108, v109\n" \
  "v_max3_u32 v189, v189, v110, v111\n" \
  "v_max3_u32 v189, v189, v112, v113\n" \
  "v_max3_u32 v189, v189, v114, v115\n" \
  "v_max3_u32 v189, v189, v120, v121\n" \
  "v_max3_u32 v189, v189, v122, v123\n" \
  "v_max3_u32 v189, v189, v124, v125\n" \
  "v_max3_u32 v189, v189, v126, v127\n" \
  "v_max3_u32 v189, v189, v116, v117\n" \
  "v_max3_u32 v189, v189, v118, v119\n" \
  "v_max3_u32 v189, v189, v128, v129\n" \
  "v_max3_u32 v189, v189, v130, v131\n" \
  "v_max3_u32 v189, v189, v132, v133\n" \
  "v_max3_u32 v189, v189, v134, v135\n" \
  "v_cmp_eq_u32 vcc, -1, v189\n" \
  "s_cbranch_vccz .LX_back_%=\n" \
  ".LX_nol_%=:\n" \
  "s_add_u32 s70, s70, 2\n" \
  "s_cmp_gt_u32 s70, 65536\n" \
  "s_cbranch_scc1 .LX_dset_%=\n" \
  "s_barrier\n" \
  "v_mov_b64 v[44:45], 0\n" \
  "v_mov_b64 v[46:47], 0\n" \
  "v_mov_b64 v[48:49], 0\n" \
  "v_mov_b64 v[50:51], 0\n" \
  "v_mov_b64 v[52:53], 0\n" \
  "v_mov_b64 v[54:55], 0\n" \
  "v_mov_b64 v[56:57], 0\n" \
  "v_mov_b64 v[58:59], 0\n" \
  "s_cmp_lg_u32 %28, 0\n" \
  "s_cbranch_scc1 .LX_g3_%=\n" \
  "s_and_b32 s62, %40, 0xffff\n" \
  "s_add_u32 s44, s44, s62\n" \
  "s_xor_b32 s59, s59, 0\n" \
  "s_mov_b32 s68, 0\n" \
  "s_mov_b32 m0, 0\n" \
  "s_branch .LX_rt_%=\n" \
  ".LX_dset_%=:\n" \
  "v_mov_b32 v124, 1\n" \
  "v_mov_b32 v125, s55\n" \
  "buffer_store_dwordx2 v[124:125], off, s[40:43], %38 offset:128 sc1\n" \
  "s_mov_b32 s70, 1\n" \
  "v_mov_b64 v[44:45], 0\n" \
  "v_mov_b64 v[46:47], 0\n" \
  "v_mov_b64 v[48:49], 0\n" \
  "v_mov_b64 v[50:51], 0\n" \
  "v_mov_b64 v[52:53], 0\n" \
  "v_mov_b64 v[54:55], 0\n" \
  "v_mov_b64 v[56:57], 0\n" \
  "v_mov_b64 v[58:59], 0\n" \
  "s_waitcnt vmcnt(0) lgkmcnt(0)\n" \
  "s_barrier\n" \
  "s_branch .LX_p_%=\n" \
  ".LX_xr_%=:\n" \
  "s_waitcnt lgkmcnt(0)\n" \
  "s_nop 7\n" \
  "ds_read_b64_tr_b16 v[132:133], v244\n" \
  "ds_read_b64_tr_b16 v[134:135], v244 offset:256\n" \
  "ds_read_b64_tr_b16 v[136:137], v244 offset:512\n" \
  "ds_read_b64_tr_b16 v[138:139], v244 offset:768\n" \
  "ds_read_b64_tr_b16 v[140:141], v244 offset:1024\n" \
  "ds_read_b64_tr_b16 v[142:143], v244 offset:1280\n" \
  "ds_read_b64_tr_b16 v[144:145], v244 offset:1536\n" \
  "ds_read_b64_tr_b16 v[146:147], v244 offset:1792\n" \
  "s_waitcnt lgkmcnt(6)\n" \
  "v_mfma_f32_16x16x32_bf16 v[148:151], v[132:135], v[84:87], 0\n" \
  "s_waitcnt lgkmcnt(4)\n" \
  "v_mfma_f32_16x16x32_bf16 v[148:151], v[136:139], v[88:91], v[148:151]\n" \
  "s_waitcnt lgkmcnt(2)\n" \
  "v_mfma_f32_16x16x32_bf16 v[148:151], v[140:143], v[92:95], v[148:151]\n" \
  "s_waitcnt lgkmcnt(0)\n" \
  "v_mfma_f32_16x16x32_bf16 v[148:151], v[144:147], v[96:99], v[148:151]\n" \
  "s_sub_u32 s60, s48, 58\n" \
  "s_lshl_b32 s60, s60, 16\n" \
  "s_add_u32 s60, s60, s97\n" \
  "s_cmp_eq_u32 s48, 0\n" \
  "s_cselect_b32 s60, 2130706432, s60\n" \
  "s_nop 2\n" \
  "v_max_f32 v158, v148, v149\n" \
  "v_max3_f32 v158, v150, v151, v158\n" \
  "v_mov_b32 v159, v158\n" \
  "s_nop 1\n" \
  "v_permlane32_swap_b32 v158, v159\n" \
  "v_max_f32 v158, v158, v159\n" \
  "v_mul_f32 v152, s65, v158\n" \
  "v_min_f32 v152, v152, v180\n" \
  "v_fma_f32 v148, v148, s64, v152\n" \
  "v_fma_f32 v149, v149, s64, v152\n" \
  "v_fma_f32 v150, v150, s64, v152\n" \
  "v_fma_f32 v151, v151, s64, v152\n" \
  "v_sub_f32 v160, v152, v180\n" \
  "v_exp_f32 v148, v148\n" \
  "v_exp_f32 v149, v149\n" \
  "v_exp_f32 v150, v150\n" \
  "v_exp_f32 v151, v151\n" \
  "v_exp_f32 v160, v160\n" \
  "v_add_f32 v153, v148, v149\n" \
  "v_add_f32 v158, v150, v151\n" \
  "v_add_f32 v153, v153, v158\n" \
  "v_mov_b32 v159, v153\n" \
  "s_nop 1\n" \
  "v_permlane32_swap_b32 v153, v159\n" \
  "v_add_f32 v153, v153, v159\n" \
  "v_fmac_f32 v153, v160, v181\n" \
  "v_rcp_f32 v154, v153\n" \
  "s_nop 0\n" \
  "v_mul_f32 v160, v160, v154\n" \
  "v_mul_f32 v154, v154, v252\n" \
  "v_mul_f32 v148, v148, v154\n" \
  "v_mul_f32 v149, v149, v154\n" \
  "v_mul_f32 v150, v150, v154\n" \
  "v_mul_f32 v151, v151, v154\n" \
  "v_fmac_f32 v148, v160, v253\n" \
  "v_fmac_f32 v150, v160, v254\n" \
  "v_cvt_pk_bf16_f32 v156, v148, v149\n" \
  "v_cvt_pk_bf16_f32 v157, v150, v151\n" \
  "buffer_store_dwordx2 v[156:157], v251, s[40:43], s60 offen sc1\n" \
  "s_cmp_eq_u32 s48, 63\n" \
  "s_cbranch_scc1 .LX_end_%=\n" \
  ".LX_xq_%=:\n" \
  "s_add_u32 s61, s48, 1\n" \
  "s_lshl_b32 s60, s61, 19\n" \
  "s_add_u32 s60, s60, s47\n" \
  "s_lshr_b32 s62, s60, 8\n" \
  "s_and_b32 s62, s62, 8192\n" \
  "s_xor_b32 s62, s62, s60\n" \
  "s_cmp_eq_u32 s48, 0\n" \
  "s_cselect_b32 s62, 2130706432, s62\n" \
  "s_add_u32 s63, s62, 4096\n" \
  "buffer_load_dwordx4 v[84:87], v249, s[40:43], s62 offen sc1\n" \
  "buffer_load_dwordx4 v[88:91], v249, s[40:43], s62 offen offset:2048 sc1\n" \
  "buffer_load_dwordx4 v[92:95], v249, s[40:43], s63 offen sc1\n" \
  "buffer_load_dwordx4 v[96:99], v249, s[40:43], s63 offen offset:2048 sc1\n" \
  "s_sub_u32 s60, s61, 58\n" \
  "s_lshl_b32 s60, s60, 16\n" \
  "s_add_u32 s60, s60, s97\n" \
  "s_cmp_eq_u32 s48, 0\n" \
  "s_cselect_b32 s60, 2130706432, s60\n" \
  "buffer_load_dwordx2 v[180:181], v250, s[40:43], s60 offen sc1\n" \
  "s_cbranch_scc1 .LX_dry_b_%=\n" \
  "s_branch .LX_xr_b_%=\n" \
  ".LX_xl_%=:\n" \
  "s_cmp_eq_u32 s48, 57\n" \
  "s_cbranch_scc1 .LX_xq_%=\n" \
  "s_cmp_eq_u32 s48, 56\n" \
  "s_cbranch_scc1 .LX_xc_%=\n" \
  "s_cmp_lg_u32 s48, 55\n" \
  "s_cbranch_scc1 .LX_xr_%=\n" \
  "buffer_load_dwordx2 v[180:181], off, s[40:43], %42 sc1\n" \
  "s_branch .LX_xr_b_%=\n" \
  ".LX_xc_%=:\n" \
  "v_readfirstlane_b32 s63, v181\n" \
  "s_cmp_eq_u32 s63, s55\n" \
  "s_cbranch_scc1 .LX_xr_b_%=\n" \
  "s_mov_b32 s69, 0\n" \
  ".LX_xs_%=:\n" \
  "buffer_load_dwordx2 v[180:181], off, s[40:43], %42 sc1\n" \
  "s_waitcnt vmcnt(0)\n" \
  "v_readfirstlane_b32 s62, v180\n" \
  "v_readfirstlane_b32 s63, v181\n" \
  "s_cmp_eq_u32 s63, s55\n" \
  "s_cselect_b32 s62, s62, 0\n" \
  "s_cmp_lg_u32 s62, 0\n" \
  "s_cbranch_scc1 .LX_xs_x_%=\n" \
  "s_sleep 2\n" \
  "s_add_u32 s69, s69, 1\n" \
  "s_and_b32 s63, s69, 1023\n" \
  "s_cbranch_scc1 .LX_xs_%=\n" \
  "s_cmp_gt_u32 s69, 2097152\n" \
  "s_cbranch_scc1 .LX_xs_set_%=\n" \
  "buffer_load_dwordx2 v[180:181], off, s[40:43], %38 offset:128 sc1\n" \
  "s_waitcnt vmcnt(0)\n" \
  "v_readfirstlane_b32 s63, v181\n" \
  "s_cmp_eq_u32 s63, s55\n" \
  "s_cbranch_scc0 .LX_xs_%=\n" \
  "s_branch .LX_xs_x_%=\n" \
  ".LX_xs_set_%=:\n" \
  "v_mov_b32 v180, 1\n" \
  "v_mov_b32 v181, s55\n" \
  "buffer_store_dwordx2 v[180:181], off, s[40:43], %38 offset:128 sc1\n" \
  "s_waitcnt vmcnt(0)\n" \
  ".LX_xs_x_%=:\n" \
  "s_branch .LX_xr_b_%=\n" \
  ".LX_ef_%=:\n" \
  "s_waitcnt vmcnt(0) lgkmcnt(0)\n" \
  "s_branch .LX_ef_b_%=\n" \
  ".LX_tw_re_%=:\n" \
  "s_add_u32 s69, s69, 1\n" \
  "s_cmp_gt_u32 s69, 200\n" \
  "s_cbranch_scc1 .LX_noclk_%=\n" \
  "s_sleep 2\n" \
  "buffer_load_dwordx4 v[36:39], off, s[40:43], s95 sc1\n" \
  "s_waitcnt vmcnt(0)\n" \
  "s_branch .LX_tw_%=\n" \
  ".LX_lat_%=:\n" \
  "s_lshr_b32 s63, %40, 16\n" \
  "s_add_u32 s62, s62, s63\n" \
  "s_cmp_lt_u32 s62, 4294937296\n" \
  "s_cbranch_scc1 .LX_noclk_%=\n" \
  "s_and_b32 s63, %40, 0xffff\n" \
  ".LX_lat1_%=:\n" \
  "s_add_u32 s44, s44, s63\n" \
  "s_add_u32 s62, s62, s63\n" \
  "s_cbranch_scc0 .LX_lat1_%=\n" \
  "s_branch .LX_rt_%=\n" \
  ".LX_clk2_%=:\n" \
  "s_cmp_le_i32 s62, 3\n" \
  "s_cbranch_scc1 .LX_clk_done_%=\n" \
  "s_memrealtime s[60:61]\n" \
  "s_waitcnt lgkmcnt(0)\n" \
  "s_sub_u32 s62, s73, s60\n" \
  "s_branch .LX_clk2_%=\n" \
  ".LX_clk_done_%=:\n" \
  "s_branch .LX_noclk_%=\n" \
  ".LX_end_%=:\n" \
  "s_waitcnt vmcnt(0)\n" \
  "s_waitcnt vmcnt(0)\n" \
  "s_nop 7\n" \
  "s_endpgm\n" \
  ".LX_other_%=:\n"
#define X_ASM_VOPS "+{v239}"(ci.sidx), "+{v240}"(ci.l16), "+{v241}"(ci.qw), "+{v242}"(ci.kw), "+{v243}"(ci.fq), "+{v244}"(ci.fk), "+{v245}"(ci.kxo), "+{v246}"(ci.pst), "+{v247}"(ci.ps2), "+{v248}"(ci.lx), "+{v249}"(ci.qxo), "+{v250}"(ci.sto), "+{v251}"(ci.plx), "+{v252}"(ci.mk), "+{v253}"(ci.ck0), "+{v254}"(ci.ck2), "+{v255}"(ci.hx), "+{v200}"(ci.vq0), "+{v201}"(ci.vq1), "+{v202}"(ci.vq2), "+{v203}"(ci.vq3), "+{v204}"(ci.tro0), "+{v205}"(ci.tro1), "+{v206}"(ci.m0o)
#define X_ASM_SOPS "s"(ci.ws), "s"(ci.Wa), "s"(ci.Wb), "s"(ci.mem0), "s"(ci.w), "s"(ci.o_a), "s"(ci.o_b), "s"(ci.o_nx), "s"(ci.o_exp), "s"(ci.lab), "s"(ci.f_p), "s"(ci.f_m), "s"(ci.f_dur), "s"(ci.f_go), "s"(ci.o_ctl), "s"(ci.o_dbg), "s"(ci.dret), "s"(ci.o_qx), "s"(ci.f_s), "s"(ci.f_r)
#define X_ASM_CLOB "v0", "v1", "v2", "v3", "v4", "v5", "v6", "v7", "v8", "v9", "v10", "v11", "v12", "v13", "v14", "v15", "v16", "v17", "v18", "v19", "v20", "v21", "v22", "v23", "v24", "v25", "v26", "v27", "v28", "v29", "v30", "v31", "v32", "v33", "v34", "v35", "v36", "v37", "v38", "v39", "v40", "v41", "v42", "v43", "v44", "v45", "v46", "v47", "v48", "v49", "v50", "v51", "v52", "v53", "v54", "v55", "v56", "v57", "v58", "v59", "v60", "v61", "v62", "v63", "v64", "v65", "v66", "v67", "v68", "v69", "v70", "v71", "v72", "v73", "v74", "v75", "v76", "v77", "v78", "v79", "v80", "v81", "v82", "v83", "v84", "v85", "v86", "v87", "v88", "v89", "v90", "v91", "v92", "v93", "v94", "v95", "v96", "v97", "v98", "v99", "v100", "v101", "v102", "v103", "v104", "v105", "v106", "v107", "v108", "v109", "v110", "v111", "v112", "v113", "v114", "v115", "v116", "v117", "v118", "v119", "v120", "v121", "v122", "v123", "v124", "v125", "v126", "v127", "v128", "v129", "v130", "v131", "v132", "v133", "v134", "v135", "v136", "v137", "v138", "v139", "v140", "v141", "v142", "v143", "v144", "v145", "v146", "v147", "v148", "v149", "v150", "v151", "v152", "v153", "v154", "v155", "v156", "v157", "v158", "v159", "v160", "v161", "v162", "v163", "v164", "v165", "v166", "v167", "v168", "v169", "v170", "v171", "v172", "v173", "v174", "v175", "v176", "v177", "v178", "v179", "v180", "v181", "v182", "v183", "v184", "v185", "v186", "v187", "v188", "v189", "v190", "v191", "v192", "v193", "v194", "v195", "v196", "v197", "v198", "v199", "a0", "a1", "a2", "a3", "a4", "a5", "a6", "a7", "a8", "a9", "a10", "a11", "a12", "a13", "a14", "a15", "a16", "a17", "a18", "a19", "a20", "a21", "a22", "a23", "a24", "a25", "a26", "a27", "a28", "a29", "a30", "a31", "a32", "a33", "a34", "a35", "a36", "a37", "a38", "a39", "a40", "a41", "a42", "a43", "a44", "a45", "a46", "a47", "a48", "a49", "a50", "a51", "a52", "a53", "a54", "a55", "a56", "a57", "a58", "a59", "a60", "a61", "a62", "a63", "a64", "a65", "a66", "a67", "a68", "a69", "a70", "a71", "a72", "a73", "a74", "a75", "a76", "a77", "a78", "a79", "a80", "a81", "a82", "a83", "a84", "a85", "a86", "a87", "a88", "a89", "a90", "a91", "a92", "a93", "a94", "a95", "a96", "a97", "a98", "a99", "a100", "a101", "a102", "a103", "a104", "a105", "a106", "a107", "a108", "a109", "a110", "a111", "a112", "a113", "a114", "a115", "a116", "a117", "a118", "a119", "a120", "a121", "a122", "a123", "a124", "a125", "a126", "a127", "a128", "a129", "a130", "a131", "a132", "a133", "a134", "a135", "a136", "a137", "a138", "a139", "a140", "a141", "a142", "a143", "a144", "a145", "a146", "a147", "a148", "a149", "a150", "a151", "a152", "a153", "a154", "a155", "a156", "a157", "a158", "a159", "a160", "a161", "a162", "a163", "a164", "a165", "a166", "a167", "a168", "a169", "a170", "a171", "a172", "a173", "a174", "a175", "a176", "a177", "a178", "a179", "a180", "a181", "a182", "a183", "a184", "a185", "a186", "a187", "a188", "a189", "a190", "a191", "a192", "a193", "a194", "a195", "a196", "a197", "a198", "a199", "a200", "a201", "a202", "a203", "a204", "a205", "a206", "a207", "a208", "a209", "a210", "a211", "a212", "a213", "a214", "a215", "a216", "a217", "a218", "a219", "a220", "a221", "a222", "a223", "a224", "a225", "a226", "a227", "a228", "a229", "a230", "a231", "a232", "a233", "a234", "a235", "a236", "a237", "a238", "a239", "a240", "a241", "a242", "a243", "a244", "a245", "a246", "a247", "a248", "a249", "a250", "a251", "a252", "a253", "a254", "a255", "s40", "s41", "s42", "s43", "s44", "s45", "s46", "s47", "s48", "s49", "s50", "s51", "s52", "s53", "s54", "s55", "s56", "s57", "s58", "s59", "s60", "s61", "s62", "s63", "s64", "s65", "s66", "s67", "s68", "s69", "s70", "s71", "s72", "s73", "s74", "s75", "s76", "s77", "s78", "s79", "s80", "s81", "s82", "s83", "s84", "s85", "s86", "s87", "s88", "s89", "s90", "s91", "s92", "s93", "s94", "s95", "s96", "s97", "s98", "s99", "vcc", "scc", "memory"
#define M_ASM_TEXT \
  "s_mov_b64 s[40:41], %24\n" \
  "s_and_b32 s41, s41, 0xffff\n" \
  "s_mov_b32 s42, 255852544\n" \
  "s_mov_b32 s43, 0x00020000\n" \
  "s_mov_b32 s46, 4718592\n" \
  "s_mov_b32 s47, 0x00020000\n" \
  "s_lshl_b32 s72, %28, 11\n" \
  "buffer_load_dword v196, off, s[40:43], %38 sc1\n" \
  "s_mov_b64 s[44:45], %25\n" \
  "s_and_b32 s45, s45, 0xffff\n" \
  "s_add_u32 s60, %29, 0\n" \
  "s_lshl_b32 s61, s72, 1\n" \
  "s_add_u32 m0, s61, 16384\n" \
  "s_nop 0\n" \
  "buffer_load_dwordx4 v200, s[44:47], s60 offen lds\n" \
  "buffer_load_dwordx4 v201, s[44:47], s60 offen offset:1024 lds\n" \
  "buffer_load_dwordx4 v202, s[44:47], s60 offen offset:2048 lds\n" \
  "buffer_load_dwordx4 v203, s[44:47], s60 offen offset:3072 lds\n" \
  "s_add_u32 s60, %29, 524288\n" \
  "s_lshl_b32 s61, s72, 1\n" \
  "s_add_u32 m0, s61, 32768\n" \
  "s_nop 0\n" \
  "buffer_load_dwordx4 v200, s[44:47], s60 offen lds\n" \
  "buffer_load_dwordx4 v201, s[44:47], s60 offen offset:1024 lds\n" \
  "buffer_load_dwordx4 v202, s[44:47], s60 offen offset:2048 lds\n" \
  "buffer_load_dwordx4 v203, s[44:47], s60 offen offset:3072 lds\n" \
  "s_add_u32 s60, %29, 1048576\n" \
  "s_lshl_b32 s61, s72, 1\n" \
  "s_add_u32 m0, s61, 49152\n" \
  "s_nop 0\n" \
  "buffer_load_dwordx4 v200, s[44:47], s60 offen lds\n" \
  "buffer_load_dwordx4 v201, s[44:47], s60 offen offset:1024 lds\n" \
  "buffer_load_dwordx4 v202, s[44:47], s60 offen offset:2048 lds\n" \
  "buffer_load_dwordx4 v203, s[44:47], s60 offen offset:3072 lds\n" \
  "s_add_u32 s60, %29, 1572864\n" \
  "s_lshl_b32 s61, s72, 1\n" \
  "s_add_u32 m0, s61, 65536\n" \
  "s_nop 0\n" \
  "buffer_load_dwordx4 v200, s[44:47], s60 offen lds\n" \
  "buffer_load_dwordx4 v201, s[44:47], s60 offen offset:1024 lds\n" \
  "buffer_load_dwordx4 v202, s[44:47], s60 offen offset:2048 lds\n" \
  "buffer_load_dwordx4 v203, s[44:47], s60 offen offset:3072 lds\n" \
  "s_add_u32 s60, %29, 2097152\n" \
  "s_lshl_b32 s61, s72, 1\n" \
  "s_add_u32 m0, s61, 81920\n" \
  "s_nop 0\n" \
  "buffer_load_dwordx4 v200, s[44:47], s60 offen lds\n" \
  "buffer_load_dwordx4 v201, s[44:47], s60 offen offset:1024 lds\n" \
  "buffer_load_dwordx4 v202, s[44:47], s60 offen offset:2048 lds\n" \
  "buffer_load_dwordx4 v203, s[44:47], s60 offen offset:3072 lds\n" \
  "s_add_u32 s60, %29, 2621440\n" \
  "s_lshl_b32 s61, s72, 1\n" \
  "s_add_u32 m0, s61, 98304\n" \
  "s_nop 0\n" \
  "buffer_load_dwordx4 v200, s[44:47], s60 offen lds\n" \
  "buffer_load_dwordx4 v201, s[44:47], s60 offen offset:1024 lds\n" \
  "buffer_load_dwordx4 v202, s[44:47], s60 offen offset:2048 lds\n" \
  "buffer_load_dwordx4 v203, s[44:47], s60 offen offset:3072 lds\n" \
  "s_add_u32 s60, %29, 3145728\n" \
  "s_lshl_b32 s61, s72, 1\n" \
  "s_add_u32 m0, s61, 114688\n" \
  "s_nop 0\n" \
  "buffer_load_dwordx4 v200, s[44:47], s60 offen lds\n" \
  "buffer_load_dwordx4 v201, s[44:47], s60 offen offset:1024 lds\n" \
  "buffer_load_dwordx4 v202, s[44:47], s60 offen offset:2048 lds\n" \
  "buffer_load_dwordx4 v203, s[44:47], s60 offen offset:3072 lds\n" \
  "s_add_u32 s60, %29, 3670016\n" \
  "s_lshl_b32 s61, s72, 1\n" \
  "s_add_u32 m0, s61, 131072\n" \
  "s_nop 0\n" \
  "buffer_load_dwordx4 v200, s[44:47], s60 offen lds\n" \
  "buffer_load_dwordx4 v201, s[44:47], s60 offen offset:1024 lds\n" \
  "buffer_load_dwordx4 v202, s[44:47], s60 offen offset:2048 lds\n" \
  "buffer_load_dwordx4 v203, s[44:47], s60 offen offset:3072 lds\n" \
  "s_add_u32 s60, %29, 4194304\n" \
  "s_lshl_b32 s61, s72, 1\n" \
  "s_add_u32 m0, s61, 147456\n" \
  "s_nop 0\n" \
  "buffer_load_dwordx4 v200, s[44:47], s60 offen lds\n" \
  "buffer_load_dwordx4 v201, s[44:47], s60 offen offset:1024 lds\n" \
  "buffer_load_dwordx4 v202, s[44:47], s60 offen offset:2048 lds\n" \
  "buffer_load_dwordx4 v203, s[44:47], s60 offen offset:3072 lds\n" \
  "s_mov_b64 s[44:45], %27\n" \
  "s_and_b32 s45, s45, 0xffff\n" \
  "s_lshr_b32 s60, s72, 3\n" \
  "s_mov_b32 m0, s72\n" \
  "s_nop 0\n" \
  "buffer_load_dwordx4 v206, s[44:47], s60 offen lds\n" \
  "s_add_u32 s60, s60, 128\n" \
  "s_add_u32 m0, s72, 1024\n" \
  "s_nop 0\n" \
  "buffer_load_dwordx4 v206, s[44:47], s60 offen lds\n" \
  "s_movk_i32 s60, 1024\n" \
  "s_movk_i32 m0, 8192\n" \
  "s_nop 0\n" \
  "buffer_load_dwordx4 v206, s[44:47], s60 offen lds\n" \
  "s_waitcnt vmcnt(27)\n" \
  "s_barrier\n" \
  "v_add_u32 v194, 16384, v204\n" \
  "ds_read_b64_tr_b16 a[0:1], v194\n" \
  "ds_read_b64_tr_b16 a[2:3], v194 offset:512\n" \
  "ds_read_b64_tr_b16 a[4:5], v194 offset:8192\n" \
  "ds_read_b64_tr_b16 a[6:7], v194 offset:8704\n" \
  "v_add_u32 v195, 16384, v205\n" \
  "ds_read_b64_tr_b16 a[72:73], v195\n" \
  "ds_read_b64_tr_b16 a[74:75], v195 offset:512\n" \
  "ds_read_b64_tr_b16 a[76:77], v195 offset:8192\n" \
  "ds_read_b64_tr_b16 a[78:79], v195 offset:8704\n" \
  "v_add_u32 v194, 32768, v204\n" \
  "ds_read_b64_tr_b16 a[8:9], v194\n" \
  "ds_read_b64_tr_b16 a[10:11], v194 offset:512\n" \
  "ds_read_b64_tr_b16 a[12:13], v194 offset:8192\n" \
  "ds_read_b64_tr_b16 a[14:15], v194 offset:8704\n" \
  "v_add_u32 v195, 32768, v205\n" \
  "ds_read_b64_tr_b16 a[80:81], v195\n" \
  "ds_read_b64_tr_b16 a[82:83], v195 offset:512\n" \
  "ds_read_b64_tr_b16 a[84:85], v195 offset:8192\n" \
  "ds_read_b64_tr_b16 a[86:87], v195 offset:8704\n" \
  "v_add_u32 v194, 49152, v204\n" \
  "ds_read_b64_tr_b16 a[16:17], v194\n" \
  "ds_read_b64_tr_b16 a[18:19], v194 offset:512\n" \
  "ds_read_b64_tr_b16 a[20:21], v194 offset:8192\n" \
  "ds_read_b64_tr_b16 a[22:23], v194 offset:8704\n" \
  "v_add_u32 v195, 49152, v205\n" \
  "ds_read_b64_tr_b16 a[88:89], v195\n" \
  "ds_read_b64_tr_b16 a[90:91], v195 offset:512\n" \
  "ds_read_b64_tr_b16 a[92:93], v195 offset:8192\n" \
  "ds_read_b64_tr_b16 a[94:95], v195 offset:8704\n" \
  "s_waitcnt lgkmcnt(0)\n" \
  "s_barrier\n" \
  "s_mov_b64 s[44:45], %26\n" \
  "s_and_b32 s45, s45, 0xffff\n" \
  "s_add_u32 s60, %29, 0\n" \
  "s_lshl_b32 s61, s72, 1\n" \
  "s_add_u32 m0, s61, 16384\n" \
  "s_nop 0\n" \
  "buffer_load_dwordx4 v200, s[44:47], s60 offen lds\n" \
  "buffer_load_dwordx4 v201, s[44:47], s60 offen offset:1024 lds\n" \
  "buffer_load_dwordx4 v202, s[44:47], s60 offen offset:2048 lds\n" \
  "buffer_load_dwordx4 v203, s[44:47], s60 offen offset:3072 lds\n" \
  "s_add_u32 s60, %29, 524288\n" \
  "s_lshl_b32 s61, s72, 1\n" \
  "s_add_u32 m0, s61, 32768\n" \
  "s_nop 0\n" \
  "buffer_load_dwordx4 v200, s[44:47], s60 offen lds\n" \
  "buffer_load_dwordx4 v201, s[44:47], s60 offen offset:1024 lds\n" \
  "buffer_load_dwordx4 v202, s[44:47], s60 offen offset:2048 lds\n" \
  "buffer_load_dwordx4 v203, s[44:47], s60 offen offset:3072 lds\n" \
  "s_add_u32 s60, %29, 1048576\n" \
  "s_lshl_b32 s61, s72, 1\n" \
  "s_add_u32 m0, s61, 49152\n" \
  "s_nop 0\n" \
  "buffer_load_dwordx4 v200, s[44:47], s60 offen lds\n" \
  "buffer_load_dwordx4 v201, s[44:47], s60 offen offset:1024 lds\n" \
  "buffer_load_dwordx4 v202, s[44:47], s60 offen offset:2048 lds\n" \
  "buffer_load_dwordx4 v203, s[44:47], s60 offen offset:3072 lds\n" \
  "v_readfirstlane_b32 s55, v196\n" \
  "s_add_u32 s55, s55, 1\n" \
  "s_waitcnt vmcnt(27)\n" \
  "s_barrier\n" \
  "v_add_u32 v194, 65536, v204\n" \
  "ds_read_b64_tr_b16 a[24:25], v194\n" \
  "ds_read_b64_tr_b16 a[26:27], v194 offset:512\n" \
  "ds_read_b64_tr_b16 a[28:29], v194 offset:8192\n" \
  "ds_read_b64_tr_b16 a[30:31], v194 offset:8704\n" \
  "v_add_u32 v195, 65536, v205\n" \
  "ds_read_b64_tr_b16 a[96:97], v195\n" \
  "ds_read_b64_tr_b16 a[98:99], v195 offset:512\n" \
  "ds_read_b64_tr_b16 a[100:101], v195 offset:8192\n" \
  "ds_read_b64_tr_b16 a[102:103], v195 offset:8704\n" \
  "v_add_u32 v194, 81920, v204\n" \
  "ds_read_b64_tr_b16 a[32:33], v194\n" \
  "ds_read_b64_tr_b16 a[34:35], v194 offset:512\n" \
  "ds_read_b64_tr_b16 a[36:37], v194 offset:8192\n" \
  "ds_read_b64_tr_b16 a[38:39], v194 offset:8704\n" \
  "v_add_u32 v195, 81920, v205\n" \
  "ds_read_b64_tr_b16 a[104:105], v195\n" \
  "ds_read_b64_tr_b16 a[106:107], v195 offset:512\n" \
  "ds_read_b64_tr_b16 a[108:109], v195 offset:8192\n" \
  "ds_read_b64_tr_b16 a[110:111], v195 offset:8704\n" \
  "v_add_u32 v194, 98304, v204\n" \
  "ds_read_b64_tr_b16 a[40:41], v194\n" \
  "ds_read_b64_tr_b16 a[42:43], v194 offset:512\n" \
  "ds_read_b64_tr_b16 a[44:45], v194 offset:8192\n" \
  "ds_read_b64_tr_b16 a[46:47], v194 offset:8704\n" \
  "v_add_u32 v195, 98304, v205\n" \
  "ds_read_b64_tr_b16 a[112:113], v195\n" \
  "ds_read_b64_tr_b16 a[114:115], v195 offset:512\n" \
  "ds_read_b64_tr_b16 a[116:117], v195 offset:8192\n" \
  "ds_read_b64_tr_b16 a[118:119], v195 offset:8704\n" \
  "s_waitcnt lgkmcnt(0)\n" \
  "s_barrier\n" \
  "s_add_u32 s60, %29, 1572864\n" \
  "s_lshl_b32 s61, s72, 1\n" \
  "s_add_u32 m0, s61, 65536\n" \
  "s_nop 0\n" \
  "buffer_load_dwordx4 v200, s[44:47], s60 offen lds\n" \
  "buffer_load_dwordx4 v201, s[44:47], s60 offen offset:1024 lds\n" \
  "buffer_load_dwordx4 v202, s[44:47], s60 offen offset:2048 lds\n" \
  "buffer_load_dwordx4 v203, s[44:47], s60 offen offset:3072 lds\n" \
  "s_add_u32 s60, %29, 2097152\n" \
  "s_lshl_b32 s61, s72, 1\n" \
  "s_add_u32 m0, s61, 81920\n" \
  "s_nop 0\n" \
  "buffer_load_dwordx4 v200, s[44:47], s60 offen lds\n" \
  "buffer_load_dwordx4 v201, s[44:47], s60 offen offset:1024 lds\n" \
  "buffer_load_dwordx4 v202, s[44:47], s60 offen offset:2048 lds\n" \
  "buffer_load_dwordx4 v203, s[44:47], s60 offen offset:3072 lds\n" \
  "s_add_u32 s60, %29, 2621440\n" \
  "s_lshl_b32 s61, s72, 1\n" \
  "s_add_u32 m0, s61, 98304\n" \
  "s_nop 0\n" \
  "buffer_load_dwordx4 v200, s[44:47], s60 offen lds\n" \
  "buffer_load_dwordx4 v201, s[44:47], s60 offen offset:1024 lds\n" \
  "buffer_load_dwordx4 v202, s[44:47], s60 offen offset:2048 lds\n" \
  "buffer_load_dwordx4 v203, s[44:47], s60 offen offset:3072 lds\n" \
  "s_waitcnt vmcnt(27)\n" \
  "s_barrier\n" \
  "v_add_u32 v194, 114688, v204\n" \
  "ds_read_b64_tr_b16 a[48:49], v194\n" \
  "ds_read_b64_tr_b16 a[50:51], v194 offset:512\n" \
  "ds_read_b64_tr_b16 a[52:53], v194 offset:8192\n" \
  "ds_read_b64_tr_b16 a[54:55], v194 offset:8704\n" \
  "v_add_u32 v195, 114688, v205\n" \
  "ds_read_b64_tr_b16 a[120:121], v195\n" \
  "ds_read_b64_tr_b16 a[122:123], v195 offset:512\n" \
  "ds_read_b64_tr_b16 a[124:125], v195 offset:8192\n" \
  "ds_read_b64_tr_b16 a[126:127], v195 offset:8704\n" \
  "v_add_u32 v194, 131072, v204\n" \
  "ds_read_b64_tr_b16 a[56:57], v194\n" \
  "ds_read_b64_tr_b16 a[58:59], v194 offset:512\n" \
  "ds_read_b64_tr_b16 a[60:61], v194 offset:8192\n" \
  "ds_read_b64_tr_b16 a[62:63], v194 offset:8704\n" \
  "v_add_u32 v195, 131072, v205\n" \
  "ds_read_b64_tr_b16 a[128:129], v195\n" \
  "ds_read_b64_tr_b16 a[130:131], v195 offset:512\n" \
  "ds_read_b64_tr_b16 a[132:133], v195 offset:8192\n" \
  "ds_read_b64_tr_b16 a[134:135], v195 offset:8704\n" \
  "v_add_u32 v194, 147456, v204\n" \
  "ds_read_b64_tr_b16 a[64:65], v194\n" \
  "ds_read_b64_tr_b16 a[66:67], v194 offset:512\n" \
  "ds_read_b64_tr_b16 a[68:69], v194 offset:8192\n" \
  "ds_read_b64_tr_b16 a[70:71], v194 offset:8704\n" \
  "v_add_u32 v195, 147456, v205\n" \
  "ds_read_b64_tr_b16 a[136:137], v195\n" \
  "ds_read_b64_tr_b16 a[138:139], v195 offset:512\n" \
  "ds_read_b64_tr_b16 a[140:141], v195 offset:8192\n" \
  "ds_read_b64_tr_b16 a[142:143], v195 offset:8704\n" \
  "s_waitcnt lgkmcnt(0)\n" \
  "s_barrier\n" \
  "s_add_u32 s60, %29, 3145728\n" \
  "s_lshl_b32 s61, s72, 1\n" \
  "s_add_u32 m0, s61, 114688\n" \
  "s_nop 0\n" \
  "buffer_load_dwordx4 v200, s[44:47], s60 offen lds\n" \
  "buffer_load_dwordx4 v201, s[44:47], s60 offen offset:1024 lds\n" \
  "buffer_load_dwordx4 v202, s[44:47], s60 offen offset:2048 lds\n" \
  "buffer_load_dwordx4 v203, s[44:47], s60 offen offset:3072 lds\n" \
  "s_add_u32 s60, %29, 3670016\n" \
  "s_lshl_b32 s61, s72, 1\n" \
  "s_add_u32 m0, s61, 131072\n" \
  "s_nop 0\n" \
  "buffer_load_dwordx4 v200, s[44:47], s60 offen lds\n" \
  "buffer_load_dwordx4 v201, s[44:47], s60 offen offset:1024 lds\n" \
  "buffer_load_dwordx4 v202, s[44:47], s60 offen offset:2048 lds\n" \
  "buffer_load_dwordx4 v203, s[44:47], s60 offen offset:3072 lds\n" \
  "s_add_u32 s60, %29, 4194304\n" \
  "s_lshl_b32 s61, s72, 1\n" \
  "s_add_u32 m0, s61, 147456\n" \
  "s_nop 0\n" \
  "buffer_load_dwordx4 v200, s[44:47], s60 offen lds\n" \
  "buffer_load_dwordx4 v201, s[44:47], s60 offen offset:1024 lds\n" \
  "buffer_load_dwordx4 v202, s[44:47], s60 offen offset:2048 lds\n" \
  "buffer_load_dwordx4 v203, s[44:47], s60 offen offset:3072 lds\n" \
  "buffer_load_dwordx2 v[198:199], off, s[40:43], %37 sc1\n" \
  "s_waitcnt vmcnt(25)\n" \
  "s_barrier\n" \
  "v_add_u32 v194, 16384, v204\n" \
  "ds_read_b64_tr_b16 a[144:145], v194\n" \
  "ds_read_b64_tr_b16 a[146:147], v194 offset:512\n" \
  "ds_read_b64_tr_b16 a[148:149], v194 offset:8192\n" \
  "ds_read_b64_tr_b16 a[150:151], v194 offset:8704\n" \
  "v_add_u32 v195, 16384, v205\n" \
  "ds_read_b64_tr_b16 a[216:217], v195\n" \
  "ds_read_b64_tr_b16 a[218:219], v195 offset:512\n" \
  "ds_read_b64_tr_b16 a[220:221], v195 offset:8192\n" \
  "ds_read_b64_tr_b16 a[222:223], v195 offset:8704\n" \
  "v_xor_b32 v195, 128, v194\n" \
  "ds_read_b64_tr_b16 v[32:33], v195\n" \
  "ds_read_b64_tr_b16 v[34:35], v195 offset:512\n" \
  "ds_read_b64_tr_b16 v[36:37], v195 offset:8192\n" \
  "ds_read_b64_tr_b16 v[38:39], v195 offset:8704\n" \
  "v_add_u32 v194, 32768, v204\n" \
  "ds_read_b64_tr_b16 a[152:153], v194\n" \
  "ds_read_b64_tr_b16 a[154:155], v194 offset:512\n" \
  "ds_read_b64_tr_b16 a[156:157], v194 offset:8192\n" \
  "ds_read_b64_tr_b16 a[158:159], v194 offset:8704\n" \
  "v_add_u32 v195, 32768, v205\n" \
  "ds_read_b64_tr_b16 a[224:225], v195\n" \
  "ds_read_b64_tr_b16 a[226:227], v195 offset:512\n" \
  "ds_read_b64_tr_b16 a[228:229], v195 offset:8192\n" \
  "ds_read_b64_tr_b16 a[230:231], v195 offset:8704\n" \
  "v_xor_b32 v195, 128, v194\n" \
  "ds_read_b64_tr_b16 v[40:41], v195\n" \
  "ds_read_b64_tr_b16 v[42:43], v195 offset:512\n" \
  "ds_read_b64_tr_b16 v[44:45], v195 offset:8192\n" \
  "ds_read_b64_tr_b16 v[46:47], v195 offset:8704\n" \
  "v_add_u32 v194, 49152, v204\n" \
  "ds_read_b64_tr_b16 a[160:161], v194\n" \
  "ds_read_b64_tr_b16 a[162:163], v194 offset:512\n" \
  "ds_read_b64_tr_b16 a[164:165], v194 offset:8192\n" \
  "ds_read_b64_tr_b16 a[166:167], v194 offset:8704\n" \
  "v_add_u32 v195, 49152, v205\n" \
  "ds_read_b64_tr_b16 a[232:233], v195\n" \
  "ds_read_b64_tr_b16 a[234:235], v195 offset:512\n" \
  "ds_read_b64_tr_b16 a[236:237], v195 offset:8192\n" \
  "ds_read_b64_tr_b16 a[238:239], v195 offset:8704\n" \
  "v_xor_b32 v195, 128, v194\n" \
  "ds_read_b64_tr_b16 v[48:49], v195\n" \
  "ds_read_b64_tr_b16 v[50:51], v195 offset:512\n" \
  "ds_read_b64_tr_b16 v[52:53], v195 offset:8192\n" \
  "ds_read_b64_tr_b16 v[54:55], v195 offset:8704\n" \
  "s_waitcnt lgkmcnt(0)\n" \
  "s_waitcnt vmcnt(13)\n" \
  "s_barrier\n" \
  "v_add_u32 v194, 65536, v204\n" \
  "ds_read_b64_tr_b16 a[168:169], v194\n" \
  "ds_read_b64_tr_b16 a[170:171], v194 offset:512\n" \
  "ds_read_b64_tr_b16 a[172:173], v194 offset:8192\n" \
  "ds_read_b64_tr_b16 a[174:175], v194 offset:8704\n" \
  "v_add_u32 v195, 65536, v205\n" \
  "ds_read_b64_tr_b16 a[240:241], v195\n" \
  "ds_read_b64_tr_b16 a[242:243], v195 offset:512\n" \
  "ds_read_b64_tr_b16 a[244:245], v195 offset:8192\n" \
  "ds_read_b64_tr_b16 a[246:247], v195 offset:8704\n" \
  "v_xor_b32 v195, 128, v194\n" \
  "ds_read_b64_tr_b16 v[56:57], v195\n" \
  "ds_read_b64_tr_b16 v[58:59], v195 offset:512\n" \
  "ds_read_b64_tr_b16 v[60:61], v195 offset:8192\n" \
  "ds_read_b64_tr_b16 v[62:63], v195 offset:8704\n" \
  "v_add_u32 v194, 81920, v204\n" \
  "ds_read_b64_tr_b16 a[176:177], v194\n" \
  "ds_read_b64_tr_b16 a[178:179], v194 offset:512\n" \
  "ds_read_b64_tr_b16 a[180:181], v194 offset:8192\n" \
  "ds_read_b64_tr_b16 a[182:183], v194 offset:8704\n" \
  "v_add_u32 v195, 81920, v205\n" \
  "ds_read_b64_tr_b16 a[248:249], v195\n" \
  "ds_read_b64_tr_b16 a[250:251], v195 offset:512\n" \
  "ds_read_b64_tr_b16 a[252:253], v195 offset:8192\n" \
  "ds_read_b64_tr_b16 a[254:255], v195 offset:8704\n" \
  "v_add_u32 v194, 98304, v204\n" \
  "ds_read_b64_tr_b16 a[184:185], v194\n" \
  "ds_read_b64_tr_b16 a[186:187], v194 offset:512\n" \
  "ds_read_b64_tr_b16 a[188:189], v194 offset:8192\n" \
  "ds_read_b64_tr_b16 a[190:191], v194 offset:8704\n" \
  "v_add_u32 v195, 98304, v205\n" \
  "ds_read_b64_tr_b16 v[0:1], v195\n" \
  "ds_read_b64_tr_b16 v[2:3], v195 offset:512\n" \
  "ds_read_b64_tr_b16 v[4:5], v195 offset:8192\n" \
  "ds_read_b64_tr_b16 v[6:7], v195 offset:8704\n" \
  "s_waitcnt lgkmcnt(0)\n" \
  "s_waitcnt vmcnt(1)\n" \
  "s_barrier\n" \
  "v_add_u32 v194, 114688, v204\n" \
  "ds_read_b64_tr_b16 a[192:193], v194\n" \
  "ds_read_b64_tr_b16 a[194:195], v194 offset:512\n" \
  "ds_read_b64_tr_b16 a[196:197], v194 offset:8192\n" \
  "ds_read_b64_tr_b16 a[198:199], v194 offset:8704\n" \
  "v_add_u32 v195, 114688, v205\n" \
  "ds_read_b64_tr_b16 v[8:9], v195\n" \
  "ds_read_b64_tr_b16 v[10:11], v195 offset:512\n" \
  "ds_read_b64_tr_b16 v[12:13], v195 offset:8192\n" \
  "ds_read_b64_tr_b16 v[14:15], v195 offset:8704\n" \
  "v_add_u32 v194, 131072, v204\n" \
  "ds_read_b64_tr_b16 a[200:201], v194\n" \
  "ds_read_b64_tr_b16 a[202:203], v194 offset:512\n" \
  "ds_read_b64_tr_b16 a[204:205], v194 offset:8192\n" \
  "ds_read_b64_tr_b16 a[206:207], v194 offset:8704\n" \
  "v_add_u32 v195, 131072, v205\n" \
  "ds_read_b64_tr_b16 v[16:17], v195\n" \
  "ds_read_b64_tr_b16 v[18:19], v195 offset:512\n" \
  "ds_read_b64_tr_b16 v[20:21], v195 offset:8192\n" \
  "ds_read_b64_tr_b16 v[22:23], v195 offset:8704\n" \
  "v_add_u32 v194, 147456, v204\n" \
  "ds_read_b64_tr_b16 a[208:209], v194\n" \
  "ds_read_b64_tr_b16 a[210:211], v194 offset:512\n" \
  "ds_read_b64_tr_b16 a[212:213], v194 offset:8192\n" \
  "ds_read_b64_tr_b16 a[214:215], v194 offset:8704\n" \
  "v_add_u32 v195, 147456, v205\n" \
  "ds_read_b64_tr_b16 v[24:25], v195\n" \
  "ds_read_b64_tr_b16 v[26:27], v195 offset:512\n" \
  "ds_read_b64_tr_b16 v[28:29], v195 offset:8192\n" \
  "ds_read_b64_tr_b16 v[30:31], v195 offset:8704\n" \
  "s_waitcnt lgkmcnt(0)\n" \
  "s_waitcnt vmcnt(0)\n" \
  "v_readfirstlane_b32 s96, v198\n" \
  "v_readfirstlane_b32 s63, v199\n" \
  "s_cmp_eq_u32 s63, s55\n" \
  "s_cselect_b32 s96, s96, 0\n" \
  "s_cmp_ge_u32 s96, 0xc0000000\n" \
  "s_cbranch_scc1 .LM_go_x_%=\n" \
  "s_mov_b32 s69, 0\n" \
  ".LM_go_%=:\n" \
  "buffer_load_dwordx2 v[196:197], off, s[40:43], %37 sc1\n" \
  "s_waitcnt vmcnt(0)\n" \
  "v_readfirstlane_b32 s96, v196\n" \
  "v_readfirstlane_b32 s63, v197\n" \
  "s_cmp_eq_u32 s63, s55\n" \
  "s_cselect_b32 s96, s96, 0\n" \
  "s_cmp_ge_u32 s96, 0xc0000000\n" \
  "s_cbranch_scc1 .LM_go_x_%=\n" \
  "s_sleep 8\n" \
  "s_add_u32 s69, s69, 1\n" \
  "s_and_b32 s63, s69, 1023\n" \
  "s_cbranch_scc1 .LM_go_%=\n" \
  "s_cmp_gt_u32 s69, 2097152\n" \
  "s_cbranch_scc1 .LM_go_set_%=\n" \
  "buffer_load_dwordx2 v[196:197], off, s[40:43], %38 offset:128 sc1\n" \
  "s_waitcnt vmcnt(0)\n" \
  "v_readfirstlane_b32 s63, v197\n" \
  "s_cmp_eq_u32 s63, s55\n" \
  "s_cbranch_scc0 .LM_go_%=\n" \
  "s_branch .LM_go_x_%=\n" \
  ".LM_go_set_%=:\n" \
  "v_mov_b32 v196, 1\n" \
  "v_mov_b32 v197, s55\n" \
  "buffer_store_dwordx2 v[196:197], off, s[40:43], %38 offset:128 sc1\n" \
  "s_waitcnt vmcnt(0)\n" \
  ".LM_go_x_%=:\n" \
  "s_and_b32 s60, s96, 2\n" \
  "s_lshl_b32 s60, s60, 12\n" \
  "s_xor_b32 s74, %34, s60\n" \
  "s_xor_b32 s75, %36, s60\n" \
  "s_bfe_u32 s56, s96, 0x30010\n" \
  "s_lshl_b32 s56, s56, 12\n" \
  "s_or_b32 s56, s56, 0x200000\n" \
  "s_mov_b32 s57, 1052672\n" \
  "s_lshl_b32 s61, %33, 2\n" \
  "s_lshr_b32 s51, s96, s61\n" \
  "s_and_b32 s51, s51, 7\n" \
  "s_lshl_b32 s51, s51, 12\n" \
  "s_xor_b32 s51, s51, 4210688\n" \
  "s_mov_b32 s71, 0\n" \
  "s_add_u32 s45, s51, 886784\n" \
  "s_mov_b32 s46, -1\n" \
  "v_mov_b64 v[64:65], -1\n" \
  "v_mov_b64 v[66:67], -1\n" \
  "v_and_b32 v194, 3, v246\n" \
  "v_lshlrev_b32 v194, 2, v194\n" \
  "v_lshrrev_b32 v194, v194, s96\n" \
  "v_and_b32 v194, 7, v194\n" \
  "v_lshlrev_b32 v194, 12, v194\n" \
  "v_and_b32 v246, -4, v246\n" \
  "v_xor_b32 v246, v246, v194\n" \
  "v_and_b32 v194, 3, v247\n" \
  "v_lshlrev_b32 v194, 2, v194\n" \
  "v_lshrrev_b32 v194, v194, s96\n" \
  "v_and_b32 v194, 7, v194\n" \
  "v_lshlrev_b32 v194, 12, v194\n" \
  "v_and_b32 v247, -4, v247\n" \
  "v_xor_b32 v247, v247, v194\n" \
  "v_xor_b32 v190, s56, v246\n" \
  "buffer_store_dwordx2 v[64:65], v190, s[40:43], 0 offen sc1\n" \
  "v_xor_b32 v191, s56, v247\n" \
  "buffer_store_dwordx2 v[64:65], v191, s[40:43], 0 offen sc1\n" \
  "buffer_store_dwordx2 v[64:65], v191, s[40:43], 0 offen offset:256 sc1\n" \
  "s_xor_b32 s62, s56, s57\n" \
  "s_nop 1\n" \
  "v_xor_b32 v190, s62, v246\n" \
  "buffer_store_dwordx2 v[64:65], v190, s[40:43], 0 offen sc1\n" \
  "v_xor_b32 v191, s62, v247\n" \
  "buffer_store_dwordx2 v[64:65], v191, s[40:43], 0 offen sc1\n" \
  "buffer_store_dwordx2 v[64:65], v191, s[40:43], 0 offen offset:256 sc1\n" \
  "s_andn2_b32 s60, 2, s96\n" \
  "s_lshl_b32 s60, s60, 12\n" \
  "s_xor_b32 s60, s60, 8192\n" \
  "s_xor_b32 s58, %31, s60\n" \
  "s_mov_b32 s49, 0\n" \
  "s_add_u32 s47, %41, s60\n" \
  "s_add_u32 s97, s60, 8388608\n" \
  "s_xor_b32 s50, %32, s60\n" \
  "s_mov_b32 s48, 0\n" \
  "s_mov_b32 s54, 0\n" \
  "s_cmp_eq_u32 %28, 0\n" \
  "s_cselect_b64 s[88:89], -1, 0\n" \
  "s_mov_b32 s68, 0\n" \
  "s_mov_b32 s70, 1\n" \
  "s_mov_b32 s73, 0\n" \
  "s_mov_b32 s44, 0\n" \
  "s_mov_b32 s53, 0\n" \
  "s_memrealtime s[86:87]\n" \
  "s_memtime s[76:77]\n" \
  "s_waitcnt lgkmcnt(0)\n" \
  "s_mov_b32 s90, s76\n" \
  "s_mov_b32 s87, 340\n" \
  "s_mov_b32 s64, 0x3e0293ee\n" \
  "s_mov_b32 s65, 0xbe0293ee\n" \
  "v_mov_b64 v[76:77], 0\n" \
  "v_mov_b64 v[78:79], 0\n" \
  "v_mov_b64 v[80:81], 0\n" \
  "v_mov_b64 v[82:83], 0\n" \
  "v_mov_b64 v[84:85], 0\n" \
  "v_mov_b64 v[86:87], 0\n" \
  "v_mov_b64 v[88:89], 0\n" \
  "v_mov_b64 v[90:91], 0\n" \
  "v_mov_b64 v[92:93], 0\n" \
  "v_mov_b64 v[94:95], 0\n" \
  "v_mov_b32 v72, 0\n" \
  "v_mov_b32 v73, s55\n" \
  "v_mov_b64 v[74:75], v[72:73]\n" \
  "s_mov_b64 s[92:93], 0\n" \
  "s_mov_b32 s92, 0xffff0000\n" \
  "s_mov_b32 s60, 8192\n" \
  "s_add_u32 s60, s60, 32768\n" \
  "v_add_u32 v176, s60, v240\n" \
  "v_mov_b32 v180, v252\n" \
  "v_mov_b64 v[96:97], 0\n" \
  "v_mov_b64 v[98:99], 0\n" \
  "v_mov_b32 v96, v253\n" \
  "v_mov_b32 v98, v254\n" \
  "s_and_b32 s60, s72, 2048\n" \
  "s_add_u32 s50, s50, s60\n" \
  "s_lshr_b32 s60, s60, 1\n" \
  "v_add_u32 v177, s60, v244\n" \
  "s_lshr_b32 s60, s72, 9\n" \
  "s_add_u32 s60, s60, 17408\n" \
  "v_mov_b32 v184, s60\n" \
  "s_mov_b32 s52, 0\n" \
  "s_barrier\n" \
  "ds_write_b32 v184, v72\n" \
  "s_branch .LM_xr_%=\n" \
  ".LM_dry_b_%=:\n" \
  "s_and_b32 s60, s72, 2048\n" \
  "s_lshl_b32 s91, s60, 1\n" \
  "s_add_u32 s91, s91, 40960\n" \
  "v_mov_b64 v[182:183], 0\n" \
  "s_mov_b32 s69, 0\n" \
  ".LM_k0_%=:\n" \
  "buffer_load_dwordx2 v[196:197], off, s[40:43], s74 sc1\n" \
  "s_waitcnt vmcnt(0)\n" \
  "v_readfirstlane_b32 s54, v196\n" \
  "v_readfirstlane_b32 s63, v197\n" \
  "s_cmp_eq_u32 s63, s55\n" \
  "s_cselect_b32 s54, s54, 0\n" \
  "s_cmp_gt_i32 s54, 1\n" \
  "s_cbranch_scc1 .LM_k0_x_%=\n" \
  "s_sleep 2\n" \
  "s_add_u32 s69, s69, 1\n" \
  "s_and_b32 s63, s69, 1023\n" \
  "s_cbranch_scc1 .LM_k0_%=\n" \
  "s_cmp_gt_u32 s69, 2097152\n" \
  "s_cbranch_scc1 .LM_k0_set_%=\n" \
  "buffer_load_dwordx2 v[196:197], off, s[40:43], %38 offset:128 sc1\n" \
  "s_waitcnt vmcnt(0)\n" \
  "v_readfirstlane_b32 s63, v197\n" \
  "s_cmp_eq_u32 s63, s55\n" \
  "s_cbranch_scc0 .LM_k0_%=\n" \
  "s_branch .LM_k0_x_%=\n" \
  ".LM_k0_set_%=:\n" \
  "v_mov_b32 v196, 1\n" \
  "v_mov_b32 v197, s55\n" \
  "buffer_store_dwordx2 v[196:197], off, s[40:43], %38 offset:128 sc1\n" \
  "s_waitcnt vmcnt(0)\n" \
  ".LM_k0_x_%=:\n" \
  "s_xor_b32 s60, s49, s58\n" \
  "s_add_u32 s49, s49, 16384\n" \
  "s_xor_b32 s91, s91, 8192\n" \
  "s_mov_b32 m0, s91\n" \
  "s_nop 0\n" \
  "buffer_load_dwordx4 v245, s[40:43], s60 offen sc1 lds\n" \
  "buffer_load_dwordx4 v245, s[40:43], s60 offen offset:1024 sc1 lds\n" \
  "buffer_load_dwordx4 v245, s[40:43], s60 offen offset:2048 sc1 lds\n" \
  "buffer_load_dwordx4 v245, s[40:43], s60 offen offset:3072 sc1 lds\n" \
  "s_xor_b32 s60, s49, s58\n" \
  "s_add_u32 s49, s49, 16384\n" \
  "s_xor_b32 s91, s91, 8192\n" \
  "s_mov_b32 m0, s91\n" \
  "s_nop 0\n" \
  "buffer_load_dwordx4 v245, s[40:43], s60 offen sc1 lds\n" \
  "buffer_load_dwordx4 v245, s[40:43], s60 offen offset:1024 sc1 lds\n" \
  "buffer_load_dwordx4 v245, s[40:43], s60 offen offset:2048 sc1 lds\n" \
  "buffer_load_dwordx4 v245, s[40:43], s60 offen offset:3072 sc1 lds\n" \
  "s_waitcnt vmcnt(0)\n" \
  "s_barrier\n" \
  "v_xor_b32 v176, 8192, v176\n" \
  "s_mov_b32 s67, 2130706432\n" \
  "s_waitcnt vmcnt(0) lgkmcnt(0)\n" \
  "s_barrier\n" \
  "s_branch .LM_p_%=\n" \
  "s_nop 7\n" \
  ".p2align 6\n" \
  ".fill (((24 - (.LM_p_%= - .LM_rot_%=)) & 63) >> 2), 4, 0xBF800000\n" \
  ".LM_rot_%=:\n" \
  "s_xor_b32 s51, s51, s56\n" \
  "s_xor_b32 s71, s71, s56\n" \
  "v_xor_b32 v246, s56, v246\n" \
  "v_xor_b32 v247, s56, v247\n" \
  "s_mov_b32 s60, s56\n" \
  "s_mov_b32 s56, s57\n" \
  "s_mov_b32 s57, s60\n" \
  "s_add_u32 s59, s51, 819200\n" \
  "s_mov_b32 m0, 0\n" \
  "s_mov_b32 s70, 0\n" \
  "s_waitcnt lgkmcnt(0)\n" \
  ".LM_rt_%=:\n" \
  "s_barrier\n" \
  ".LM_p_%=:\n" \
  "ds_read_b128 v[116:119], v240\n" \
  "ds_read_b128 v[120:123], v240 offset:1024\n" \
  "ds_read_b128 v[124:127], v240 offset:2048\n" \
  "s_waitcnt lgkmcnt(2)\n" \
  "v_smfmac_f32_16x16x64_bf16 v[80:83], v[116:119], a[72:79], v239\n" \
  "s_barrier\n" \
  "v_smfmac_f32_16x16x64_bf16 v[76:79], v[116:119], a[0:7], v239\n" \
  "ds_read_b128 v[128:131], v240 offset:3072\n" \
  "s_waitcnt lgkmcnt(2)\n" \
  "v_smfmac_f32_16x16x64_bf16 v[80:83], v[120:123], a[80:87], v239\n" \
  "ds_read_b128 v[132:135], v240 offset:4096\n" \
  "v_smfmac_f32_16x16x64_bf16 v[76:79], v[120:123], a[8:15], v239\n" \
  "ds_read_b128 v[136:139], v240 offset:5120\n" \
  "s_waitcnt lgkmcnt(3)\n" \
  "v_smfmac_f32_16x16x64_bf16 v[76:79], v[124:127], a[16:23], v239\n" \
  "ds_read_b128 v[140:143], v240 offset:6144\n" \
  "ds_read_b128 v[144:147], v240 offset:7168\n" \
  "v_smfmac_f32_16x16x64_bf16 v[80:83], v[124:127], a[88:95], v239\n" \
  "s_waitcnt lgkmcnt(4)\n" \
  "v_smfmac_f32_16x16x64_bf16 v[76:79], v[128:131], a[24:31], v239\n" \
  "ds_read_b128 v[148:151], v240 offset:8192\n" \
  "s_waitcnt lgkmcnt(4)\n" \
  "v_smfmac_f32_16x16x64_bf16 v[76:79], v[132:135], a[32:39], v239\n" \
  "s_waitcnt lgkmcnt(3)\n" \
  "v_smfmac_f32_16x16x64_bf16 v[76:79], v[136:139], a[40:47], v239\n" \
  "s_waitcnt lgkmcnt(2)\n" \
  "v_smfmac_f32_16x16x64_bf16 v[76:79], v[140:143], a[48:55], v239\n" \
  "s_waitcnt lgkmcnt(1)\n" \
  "v_smfmac_f32_16x16x64_bf16 v[76:79], v[144:147], a[56:63], v239\n" \
  "s_waitcnt lgkmcnt(0)\n" \
  "v_smfmac_f32_16x16x64_bf16 v[76:79], v[148:151], a[64:71], v239\n" \
  "s_nop 2\n" \
  "v_smfmac_f32_16x16x64_bf16 v[80:83], v[128:131], a[96:103], v239\n" \
  "s_nop 2\n" \
  "v_smfmac_f32_16x16x64_bf16 v[80:83], v[132:135], a[104:111], v239\n" \
  "v_add_f32 v76, v76, v78\n" \
  "v_add_f32 v77, v77, v79\n" \
  "v_smfmac_f32_16x16x64_bf16 v[80:83], v[136:139], a[112:119], v239\n" \
  "v_cmp_u_f32 vcc, v76, v77\n" \
  "v_smfmac_f32_16x16x64_bf16 v[80:83], v[140:143], a[120:127], v239\n" \
  "v_mul_f32 v77, s64, v77\n" \
  "v_smfmac_f32_16x16x64_bf16 v[80:83], v[144:147], a[128:135], v239\n" \
  "v_mul_f32 v76, s64, v76\n" \
  "v_smfmac_f32_16x16x64_bf16 v[80:83], v[148:151], a[136:143], v239\n" \
  "s_cbranch_vccnz .LM_slow_%=\n" \
  ".LM_back_%=:\n" \
  "s_cmp_eq_u32 s48, 63\n" \
  "v_cvt_pk_bf16_f32 v76, v76, v77\n" \
  "v_smfmac_f32_16x16x64_bf16 v[92:95], v[116:119], v[32:39], v239\n" \
  "ds_write_b32 v241, v76\n" \
  "s_nop 2\n" \
  "v_add_f32 v80, v80, v82\n" \
  "v_mul_f32 v80, s64, v80\n" \
  "v_add_f32 v81, v81, v83\n" \
  "v_mul_f32 v81, s64, v81\n" \
  "v_cvt_pk_bf16_f32 v80, v80, v81\n" \
  "v_smfmac_f32_16x16x64_bf16 v[92:95], v[120:123], v[40:47], v239\n" \
  "ds_write_b32 v241, v80 offset:256\n" \
  "v_smfmac_f32_16x16x64_bf16 v[92:95], v[124:127], v[48:55], v239\n" \
  "v_smfmac_f32_16x16x64_bf16 v[92:95], v[128:131], v[56:63], v239\n" \
  "s_waitcnt lgkmcnt(1)\n" \
  "s_barrier\n" \
  "v_smfmac_f32_16x16x64_bf16 v[84:87], v[116:119], a[144:151], v239\n" \
  "s_nop 0\n" \
  "ds_read_b64_tr_b16 v[152:153], v243 offset:1024\n" \
  "v_smfmac_f32_16x16x64_bf16 v[88:91], v[116:119], a[216:223], v239\n" \
  "s_nop 1\n" \
  "v_add_f32 v93, v93, v95\n" \
  "s_waitcnt lgkmcnt(1)\n" \
  "s_barrier\n" \
  "v_add_f32 v92, v92, v94\n" \
  "v_smfmac_f32_16x16x64_bf16 v[84:87], v[120:123], a[152:159], v239\n" \
  "ds_read_b64_tr_b16 v[116:117], v243\n" \
  "v_smfmac_f32_16x16x64_bf16 v[84:87], v[124:127], a[160:167], v239\n" \
  "ds_write_b64 v255, v[92:93]\n" \
  "v_smfmac_f32_16x16x64_bf16 v[84:87], v[128:131], a[168:175], v239\n" \
  "v_smfmac_f32_16x16x64_bf16 v[84:87], v[132:135], a[176:183], v239\n" \
  "s_waitcnt vmcnt(0) \n" \
  "buffer_store_dwordx2 v[72:73], off, s[40:43], %35 sc1\n" \
  "v_smfmac_f32_16x16x64_bf16 v[84:87], v[136:139], a[184:191], v239\n" \
  "v_smfmac_f32_16x16x64_bf16 v[84:87], v[140:143], a[192:199], v239\n" \
  "s_waitcnt lgkmcnt(0)\n" \
  "s_barrier\n" \
  "v_smfmac_f32_16x16x64_bf16 v[84:87], v[144:147], a[200:207], v239\n" \
  "v_smfmac_f32_16x16x64_bf16 v[84:87], v[148:151], a[208:215], v239\n" \
  "ds_read_b64_tr_b16 v[154:155], v243 offset:1536\n" \
  "ds_read_b64_tr_b16 v[158:159], v243 offset:1792\n" \
  "v_smfmac_f32_16x16x64_bf16 v[88:91], v[120:123], a[224:231], v239\n" \
  "s_nop 2\n" \
  "v_smfmac_f32_16x16x64_bf16 v[88:91], v[124:127], a[232:239], v239\n" \
  "s_nop 0\n" \
  "v_add_f32 v84, v84, v86\n" \
  "v_smfmac_f32_16x16x64_bf16 v[88:91], v[128:131], a[240:247], v239\n" \
  "ds_read_b64_tr_b16 v[120:121], v243 offset:256\n" \
  "v_add_f32 v85, v85, v87\n" \
  "v_smfmac_f32_16x16x64_bf16 v[88:91], v[132:135], a[248:255], v239\n" \
  "ds_read_b64_tr_b16 v[122:123], v243 offset:768\n" \
  "v_cvt_pk_bf16_f32 v84, v84, v85\n" \
  "v_smfmac_f32_16x16x64_bf16 v[88:91], v[136:139], v[0:7], v239\n" \
  "v_smfmac_f32_16x16x64_bf16 v[88:91], v[140:143], v[8:15], v239\n" \
  "ds_write_b32 v242, v84\n" \
  "v_smfmac_f32_16x16x64_bf16 v[88:91], v[144:147], v[16:23], v239\n" \
  "v_smfmac_f32_16x16x64_bf16 v[88:91], v[148:151], v[24:31], v239\n" \
  "ds_read_b64_tr_b16 v[118:119], v243 offset:512\n" \
  "s_nop 6\n" \
  "v_add_f32 v88, v88, v90\n" \
  "v_add_f32 v89, v89, v91\n" \
  "v_cvt_pk_bf16_f32 v88, v88, v89\n" \
  "ds_write_b32 v242, v88 offset:256\n" \
  "s_waitcnt lgkmcnt(2)\n" \
  "s_barrier\n" \
  "ds_read_b64_tr_b16 v[124:125], v244 offset:1024\n" \
  "ds_read_b64_tr_b16 v[126:127], v244 offset:1536\n" \
  "ds_read_b64_tr_b16 v[130:131], v244 offset:512\n" \
  "ds_read_b64_tr_b16 v[128:129], v244\n" \
  "s_waitcnt lgkmcnt(4)\n" \
  "s_barrier\n" \
  "s_cbranch_scc1 .LM_xr_%=\n" \
  "ds_read_b64_tr_b16 v[134:135], v244 offset:768\n" \
  "ds_read_b64_tr_b16 v[132:133], v244 offset:256\n" \
  "ds_read_b64_tr_b16 v[156:157], v243 offset:1280\n" \
  "ds_read_b64_tr_b16 v[136:137], v244 offset:1280\n" \
  "ds_read_b64 v[188:189], v248\n" \
  "ds_read_b64_tr_b16 v[138:139], v244 offset:1792\n" \
  "s_waitcnt lgkmcnt(6)\n" \
  "v_mfma_f32_16x16x32_bf16 v[140:143], v[128:131], v[116:119], 0\n" \
  "v_mfma_f32_16x16x32_bf16 v[140:143], v[124:127], v[152:155], v[140:143]\n" \
  "s_waitcnt lgkmcnt(4)\n" \
  "v_mfma_f32_16x16x32_bf16 v[140:143], v[132:135], v[120:123], v[140:143]\n" \
  "s_waitcnt lgkmcnt(0)\n" \
  "v_mfma_f32_16x16x32_bf16 v[140:143], v[136:139], v[156:159], v[140:143]\n" \
  "s_nop 7\n" \
  "v_exp_f32 v119, v143\n" \
  "v_exp_f32 v117, v141\n" \
  "v_exp_f32 v118, v142\n" \
  "v_exp_f32 v116, v140\n" \
  "v_add_f32 v188, v188, v189\n" \
  "v_pk_add_f32 v[186:187], v[116:117], v[118:119]\n" \
  "s_nop 0\n" \
  "v_pk_add_f32 v[174:175], v[186:187], v[186:187] op_sel:[0,1] op_sel_hi:[1,0]\n" \
  "v_pk_fma_f32 v[116:117], v[116:117], v[180:181], v[96:97] op_sel_hi:[1,0,1]\n" \
  "v_pk_fma_f32 v[118:119], v[118:119], v[180:181], v[98:99] op_sel_hi:[1,0,1]\n" \
  "v_permlane32_swap_b32 v174, v175\n" \
  "v_add_f32 v174, v174, v175\n" \
  "v_add_f32 v174, v174, v188\n" \
  "v_rcp_f32 v188, v174\n" \
  "v_add_u32 v187, 3766484992, v174\n" \
  "v_pk_mul_f32 v[116:117], v[116:117], v[188:189] op_sel_hi:[1,0]\n" \
  "v_pk_mul_f32 v[118:119], v[118:119], v[188:189] op_sel_hi:[1,0]\n" \
  "v_cvt_pk_bf16_f32 v188, v116, v117\n" \
  "v_cvt_pk_bf16_f32 v189, v118, v119\n" \
  "v_cmp_le_u32 vcc, s52, v187\n" \
  "s_cbranch_vccnz .LM_ex_%=\n" \
  ".LM_ex_b_%=:\n" \
  "buffer_store_dwordx2 v[188:189], v246, s[40:43], 0 offen sc1\n" \
  "s_memrealtime s[98:99]\n" \
  "s_memtime s[84:85]\n" \
  ".LM_loop_%=:\n" \
  "s_cmp_ge_u32 s48, 55\n" \
  "s_cbranch_scc1 .LM_xl_%=\n" \
  ".LM_xr_b_%=:\n" \
  "ds_read_b64_tr_b16 v[116:117], v177\n" \
  "ds_read_b64_tr_b16 v[118:119], v177 offset:256\n" \
  "ds_read_b64_tr_b16 v[120:121], v177 offset:512\n" \
  "ds_read_b64_tr_b16 v[122:123], v177 offset:768\n" \
  "ds_read_b32 v188, v184\n" \
  "v_readfirstlane_b32 s63, v183\n" \
  "v_readfirstlane_b32 s62, v182\n" \
  "s_cmp_eq_u32 s63, s55\n" \
  "s_cselect_b32 s62, s62, 0\n" \
  "s_max_i32 s54, s54, s62\n" \
  "s_add_u32 s48, s48, 1\n" \
  "s_add_u32 s62, s48, 1\n" \
  "s_min_u32 s62, s62, 63\n" \
  "s_cmp_le_i32 s54, s62\n" \
  "s_cbranch_scc1 .LM_pk_go_%=\n" \
  ".LM_pk_ok_%=:\n" \
  "s_xor_b32 s60, s49, s58\n" \
  "s_add_u32 s49, s49, 16384\n" \
  "s_xor_b32 s91, s91, 8192\n" \
  "s_mov_b32 m0, s91\n" \
  "s_nop 0\n" \
  "buffer_load_dwordx4 v245, s[40:43], s60 offen sc1 lds\n" \
  "buffer_load_dwordx4 v245, s[40:43], s60 offen offset:1024 sc1 lds\n" \
  "buffer_load_dwordx4 v245, s[40:43], s60 offen offset:2048 sc1 lds\n" \
  "buffer_load_dwordx4 v245, s[40:43], s60 offen offset:3072 sc1 lds\n" \
  "s_add_u32 s62, s48, 2\n" \
  "s_cmp_le_i32 s54, s62\n" \
  "s_cselect_b32 s62, s74, 2130706432\n" \
  "buffer_load_dwordx2 v[182:183], off, s[40:43], s62 sc1\n" \
  "v_xor_b32 v176, 8192, v176\n" \
  "v_mov_b64 v[76:77], 0\n" \
  "v_mov_b64 v[78:79], 0\n" \
  "v_mov_b64 v[80:81], 0\n" \
  "v_mov_b64 v[82:83], 0\n" \
  "v_mov_b64 v[84:85], 0\n" \
  "v_mov_b64 v[86:87], 0\n" \
  "v_mov_b64 v[88:89], 0\n" \
  "v_mov_b64 v[90:91], 0\n" \
  "v_mov_b64 v[92:93], 0\n" \
  "v_mov_b64 v[94:95], 0\n" \
  "v_mov_b32 v72, s48\n" \
  "s_sub_u32 s60, s48, 2\n" \
  "s_cselect_b32 s60, 8192, s60\n" \
  "s_lshl_b32 s60, s60, 14\n" \
  "s_xor_b32 s60, s60, s50\n" \
  "s_cmp_le_u32 s48, 58\n" \
  "s_cselect_b32 s60, s60, 2130706432\n" \
  "s_waitcnt lgkmcnt(3)\n" \
  "buffer_store_dwordx4 v[116:119], v240, s[40:43], s60 offen sc1\n" \
  "s_waitcnt lgkmcnt(1)\n" \
  "buffer_store_dwordx4 v[120:123], v240, s[40:43], s60 offen offset:1024 sc1\n" \
  "s_waitcnt lgkmcnt(0)\n" \
  "s_waitcnt lgkmcnt(0)\n" \
  "v_readfirstlane_b32 s60, v188\n" \
  "v_mov_b32 v189, s55\n" \
  "s_lshl_b32 s66, s66, 10\n" \
  "s_and_b32 s61, s60, 1023\n" \
  "s_cselect_b32 s62, 4096000, 0\n" \
  "s_andn2_b32 s60, s60, 1023\n" \
  "s_sub_u32 s60, s66, s60\n" \
  "s_mov_b32 s66, s84\n" \
  "s_or_b32 s60, s60, s61\n" \
  "s_cmp_lt_u32 s60, s62\n" \
  "s_cselect_b32 s60, s60, -1\n" \
  "s_min_u32 s46, s46, s60\n" \
  "v_mov_b32 v188, s46\n" \
  "s_cmp_lg_u32 s46, -1\n" \
  "s_cselect_b32 s61, s75, 2130706432\n" \
  "s_and_b32 s60, s48, 3\n" \
  "s_cmp_eq_u32 s60, 2\n" \
  "s_cselect_b32 s61, s61, 2130706432\n" \
  "s_cselect_b32 s46, -1, s46\n" \
  "buffer_store_dwordx2 v[188:189], off, s[40:43], s61 sc1\n" \
  "s_cmp_eq_u32 s48, 58\n" \
  "s_cbranch_scc1 .LM_ef_%=\n" \
  ".LM_ef_b_%=:\n" \
  "s_branch .LM_rot_%=\n" \
  "s_nop 7\n" \
  ".LM_slow_%=:\n" \
  "s_bitcmp1_b32 s70, 0\n" \
  "s_cbranch_scc1 .LM_back_%=\n" \
  "s_waitcnt lgkmcnt(0)\n" \
  "s_cmp_lt_u32 s70, 10\n" \
  "s_nop 3\n" \
  "s_cbranch_scc1 .LM_nol_%=\n" \
  "v_max_u32 v189, v116, v117\n" \
  "v_max3_u32 v189, v189, v118, v119\n" \
  "v_max3_u32 v189, v189, v120, v121\n" \
  "v_max3_u32 v189, v189, v122, v123\n" \
  "v_max3_u32 v189, v189, v124, v125\n" \
  "v_max3_u32 v189, v189, v126, v127\n" \
  "v_max3_u32 v189, v189, v128, v129\n" \
  "v_max3_u32 v189, v189, v130, v131\n" \
  "v_max3_u32 v189, v189, v132, v133\n" \
  "v_max3_u32 v189, v189, v134, v135\n" \
  "v_max3_u32 v189, v189, v136, v137\n" \
  "v_max3_u32 v189, v189, v138, v139\n" \
  "v_max3_u32 v189, v189, v140, v141\n" \
  "v_max3_u32 v189, v189, v142, v143\n" \
  "v_max3_u32 v189, v189, v144, v145\n" \
  "v_max3_u32 v189, v189, v146, v147\n" \
  "v_max3_u32 v189, v189, v148, v149\n" \
  "v_max3_u32 v189, v189, v150, v151\n" \
  "v_cmp_eq_u32 vcc, -1, v189\n" \
  "s_cbranch_vccz .LM_back_%=\n" \
  ".LM_nol_%=:\n" \
  "s_add_u32 s70, s70, 2\n" \
  "s_cmp_gt_u32 s70, 65536\n" \
  "s_cbranch_scc1 .LM_dset_%=\n" \
  "s_barrier\n" \
  "v_mov_b64 v[76:77], 0\n" \
  "v_mov_b64 v[78:79], 0\n" \
  "v_mov_b64 v[80:81], 0\n" \
  "v_mov_b64 v[82:83], 0\n" \
  "v_mov_b64 v[84:85], 0\n" \
  "v_mov_b64 v[86:87], 0\n" \
  "v_mov_b64 v[88:89], 0\n" \
  "v_mov_b64 v[90:91], 0\n" \
  "v_mov_b64 v[92:93], 0\n" \
  "v_mov_b64 v[94:95], 0\n" \
  "s_branch .LM_rt_%=\n" \
  ".LM_dset_%=:\n" \
  "v_mov_b32 v136, 1\n" \
  "v_mov_b32 v137, s55\n" \
  "buffer_store_dwordx2 v[136:137], off, s[40:43], %38 offset:128 sc1\n" \
  "s_mov_b32 s70, 1\n" \
  "v_mov_b64 v[76:77], 0\n" \
  "v_mov_b64 v[78:79], 0\n" \
  "v_mov_b64 v[80:81], 0\n" \
  "v_mov_b64 v[82:83], 0\n" \
  "v_mov_b64 v[84:85], 0\n" \
  "v_mov_b64 v[86:87], 0\n" \
  "v_mov_b64 v[88:89], 0\n" \
  "v_mov_b64 v[90:91], 0\n" \
  "v_mov_b64 v[92:93], 0\n" \
  "v_mov_b64 v[94:95], 0\n" \
  "s_waitcnt vmcnt(0) lgkmcnt(0)\n" \
  "s_barrier\n" \
  "s_branch .LM_p_%=\n" \
  ".LM_xr_%=:\n" \
  "s_waitcnt lgkmcnt(0)\n" \
  "s_nop 7\n" \
  "ds_read_b64_tr_b16 v[132:133], v244\n" \
  "ds_read_b64_tr_b16 v[134:135], v244 offset:256\n" \
  "ds_read_b64_tr_b16 v[136:137], v244 offset:512\n" \
  "ds_read_b64_tr_b16 v[138:139], v244 offset:768\n" \
  "ds_read_b64_tr_b16 v[140:141], v244 offset:1024\n" \
  "ds_read_b64_tr_b16 v[142:143], v244 offset:1280\n" \
  "ds_read_b64_tr_b16 v[144:145], v244 offset:1536\n" \
  "ds_read_b64_tr_b16 v[146:147], v244 offset:1792\n" \
  "s_waitcnt lgkmcnt(6)\n" \
  "v_mfma_f32_16x16x32_bf16 v[148:151], v[132:135], v[100:103], 0\n" \
  "s_waitcnt lgkmcnt(4)\n" \
  "v_mfma_f32_16x16x32_bf16 v[148:151], v[136:139], v[104:107], v[148:151]\n" \
  "s_waitcnt lgkmcnt(2)\n" \
  "v_mfma_f32_16x16x32_bf16 v[148:151], v[140:143], v[108:111], v[148:151]\n" \
  "s_waitcnt lgkmcnt(0)\n" \
  "v_mfma_f32_16x16x32_bf16 v[148:151], v[144:147], v[112:115], v[148:151]\n" \
  "s_sub_u32 s60, s48, 58\n" \
  "s_lshl_b32 s60, s60, 16\n" \
  "s_add_u32 s60, s60, s97\n" \
  "s_cmp_eq_u32 s48, 0\n" \
  "s_cselect_b32 s60, 2130706432, s60\n" \
  "s_nop 2\n" \
  "v_max_f32 v158, v148, v149\n" \
  "v_max3_f32 v158, v150, v151, v158\n" \
  "v_mov_b32 v159, v158\n" \
  "s_nop 1\n" \
  "v_permlane32_swap_b32 v158, v159\n" \
  "v_max_f32 v158, v158, v159\n" \
  "v_mul_f32 v152, s65, v158\n" \
  "v_min_f32 v152, v152, v178\n" \
  "v_fma_f32 v148, v148, s64, v152\n" \
  "v_fma_f32 v149, v149, s64, v152\n" \
  "v_fma_f32 v150, v150, s64, v152\n" \
  "v_fma_f32 v151, v151, s64, v152\n" \
  "v_sub_f32 v160, v152, v178\n" \
  "v_exp_f32 v148, v148\n" \
  "v_exp_f32 v149, v149\n" \
  "v_exp_f32 v150, v150\n" \
  "v_exp_f32 v151, v151\n" \
  "v_exp_f32 v160, v160\n" \
  "v_add_f32 v153, v148, v149\n" \
  "v_add_f32 v158, v150, v151\n" \
  "v_add_f32 v153, v153, v158\n" \
  "v_mov_b32 v159, v153\n" \
  "s_nop 1\n" \
  "v_permlane32_swap_b32 v153, v159\n" \
  "v_add_f32 v153, v153, v159\n" \
  "v_fmac_f32 v153, v160, v179\n" \
  "v_rcp_f32 v154, v153\n" \
  "s_nop 0\n" \
  "v_mul_f32 v160, v160, v154\n" \
  "v_mul_f32 v154, v154, v252\n" \
  "v_mul_f32 v148, v148, v154\n" \
  "v_mul_f32 v149, v149, v154\n" \
  "v_mul_f32 v150, v150, v154\n" \
  "v_mul_f32 v151, v151, v154\n" \
  "v_fmac_f32 v148, v160, v253\n" \
  "v_fmac_f32 v150, v160, v254\n" \
  "v_cvt_pk_bf16_f32 v156, v148, v149\n" \
  "v_cvt_pk_bf16_f32 v157, v150, v151\n" \
  "buffer_store_dwordx2 v[156:157], v251, s[40:43], s60 offen sc1\n" \
  "s_cmp_eq_u32 s48, 63\n" \
  "s_cbranch_scc1 .LM_end_%=\n" \
  ".LM_xq_%=:\n" \
  "s_add_u32 s61, s48, 1\n" \
  "s_lshl_b32 s60, s61, 19\n" \
  "s_add_u32 s60, s60, s47\n" \
  "s_lshr_b32 s62, s60, 8\n" \
  "s_and_b32 s62, s62, 8192\n" \
  "s_xor_b32 s62, s62, s60\n" \
  "s_cmp_eq_u32 s48, 0\n" \
  "s_cselect_b32 s62, 2130706432, s62\n" \
  "s_add_u32 s63, s62, 4096\n" \
  "buffer_load_dwordx4 v[100:103], v249, s[40:43], s62 offen sc1\n" \
  "buffer_load_dwordx4 v[104:107], v249, s[40:43], s62 offen offset:2048 sc1\n" \
  "buffer_load_dwordx4 v[108:111], v249, s[40:43], s63 offen sc1\n" \
  "buffer_load_dwordx4 v[112:115], v249, s[40:43], s63 offen offset:2048 sc1\n" \
  "s_sub_u32 s60, s61, 58\n" \
  "s_lshl_b32 s60, s60, 16\n" \
  "s_add_u32 s60, s60, s97\n" \
  "s_cmp_eq_u32 s48, 0\n" \
  "s_cselect_b32 s60, 2130706432, s60\n" \
  "buffer_load_dwordx2 v[178:179], v250, s[40:43], s60 offen sc1\n" \
  "s_cbranch_scc1 .LM_dry_b_%=\n" \
  "s_branch .LM_xr_b_%=\n" \
  ".LM_xl_%=:\n" \
  "s_cmp_eq_u32 s48, 57\n" \
  "s_cbranch_scc1 .LM_xq_%=\n" \
  "s_cmp_eq_u32 s48, 56\n" \
  "s_cbranch_scc1 .LM_xc_%=\n" \
  "s_cmp_lg_u32 s48, 55\n" \
  "s_cbranch_scc1 .LM_xr_%=\n" \
  "buffer_load_dwordx2 v[178:179], off, s[40:43], %42 sc1\n" \
  "s_branch .LM_xr_b_%=\n" \
  ".LM_xc_%=:\n" \
  "v_readfirstlane_b32 s63, v179\n" \
  "s_cmp_eq_u32 s63, s55\n" \
  "s_cbranch_scc1 .LM_xr_b_%=\n" \
  "s_mov_b32 s69, 0\n" \
  ".LM_xs_%=:\n" \
  "buffer_load_dwordx2 v[178:179], off, s[40:43], %42 sc1\n" \
  "s_waitcnt vmcnt(0)\n" \
  "v_readfirstlane_b32 s62, v178\n" \
  "v_readfirstlane_b32 s63, v179\n" \
  "s_cmp_eq_u32 s63, s55\n" \
  "s_cselect_b32 s62, s62, 0\n" \
  "s_cmp_lg_u32 s62, 0\n" \
  "s_cbranch_scc1 .LM_xs_x_%=\n" \
  "s_sleep 2\n" \
  "s_add_u32 s69, s69, 1\n" \
  "s_and_b32 s63, s69, 1023\n" \
  "s_cbranch_scc1 .LM_xs_%=\n" \
  "s_cmp_gt_u32 s69, 2097152\n" \
  "s_cbranch_scc1 .LM_xs_set_%=\n" \
  "buffer_load_dwordx2 v[178:179], off, s[40:43], %38 offset:128 sc1\n" \
  "s_waitcnt vmcnt(0)\n" \
  "v_readfirstlane_b32 s63, v179\n" \
  "s_cmp_eq_u32 s63, s55\n" \
  "s_cbranch_scc0 .LM_xs_%=\n" \
  "s_branch .LM_xs_x_%=\n" \
  ".LM_xs_set_%=:\n" \
  "v_mov_b32 v178, 1\n" \
  "v_mov_b32 v179, s55\n" \
  "buffer_store_dwordx2 v[178:179], off, s[40:43], %38 offset:128 sc1\n" \
  "s_waitcnt vmcnt(0)\n" \
  ".LM_xs_x_%=:\n" \
  "s_branch .LM_xr_b_%=\n" \
  ".LM_pk_go_%=:\n" \
  "s_mov_b32 s94, s62\n" \
  "s_mov_b32 s69, 0\n" \
  ".LM_pk_%=:\n" \
  "buffer_load_dwordx2 v[186:187], off, s[40:43], s74 sc1\n" \
  "s_waitcnt vmcnt(0)\n" \
  "v_readfirstlane_b32 s54, v186\n" \
  "v_readfirstlane_b32 s63, v187\n" \
  "s_cmp_eq_u32 s63, s55\n" \
  "s_cselect_b32 s54, s54, 0\n" \
  "s_cmp_gt_i32 s54, s94\n" \
  "s_cbranch_scc1 .LM_pk_x_%=\n" \
  "s_sleep 2\n" \
  "s_add_u32 s69, s69, 1\n" \
  "s_and_b32 s63, s69, 1023\n" \
  "s_cbranch_scc1 .LM_pk_%=\n" \
  "s_cmp_gt_u32 s69, 2097152\n" \
  "s_cbranch_scc1 .LM_pk_set_%=\n" \
  "buffer_load_dwordx2 v[186:187], off, s[40:43], %38 offset:128 sc1\n" \
  "s_waitcnt vmcnt(0)\n" \
  "v_readfirstlane_b32 s63, v187\n" \
  "s_cmp_eq_u32 s63, s55\n" \
  "s_cbranch_scc0 .LM_pk_%=\n" \
  "s_branch .LM_pk_x_%=\n" \
  ".LM_pk_set_%=:\n" \
  "v_mov_b32 v186, 1\n" \
  "v_mov_b32 v187, s55\n" \
  "buffer_store_dwordx2 v[186:187], off, s[40:43], %38 offset:128 sc1\n" \
  "s_waitcnt vmcnt(0)\n" \
  ".LM_pk_x_%=:\n" \
  "s_branch .LM_pk_ok_%=\n" \
  ".LM_ef_%=:\n" \
  "s_waitcnt vmcnt(0) lgkmcnt(0)\n" \
  "buffer_store_dwordx2 v[72:73], off, s[40:43], %35 sc1\n" \
  "s_branch .LM_ef_b_%=\n" \
  ".LM_ex_%=:\n" \
  "s_waitcnt vmcnt(0) lgkmcnt(0)\n" \
  "s_nop 7\n" \
  "ds_read_b64_tr_b16 v[120:121], v243\n" \
  "ds_read_b64_tr_b16 v[122:123], v243 offset:256\n" \
  "ds_read_b64_tr_b16 v[124:125], v243 offset:512\n" \
  "ds_read_b64_tr_b16 v[126:127], v243 offset:768\n" \
  "ds_read_b64_tr_b16 v[128:129], v243 offset:1024\n" \
  "ds_read_b64_tr_b16 v[130:131], v243 offset:1280\n" \
  "ds_read_b64_tr_b16 v[132:133], v243 offset:1536\n" \
  "ds_read_b64_tr_b16 v[134:135], v243 offset:1792\n" \
  "ds_read_b128 v[136:139], v176\n" \
  "ds_read_b128 v[144:147], v176 offset:2048\n" \
  "ds_read_b128 v[148:151], v176 offset:4096\n" \
  "ds_read_b128 v[152:155], v176 offset:6144\n" \
  "s_waitcnt lgkmcnt(3)\n" \
  "v_mfma_f32_16x16x32_bf16 v[156:159], v[136:139], v[120:123], 0\n" \
  "s_waitcnt lgkmcnt(2)\n" \
  "v_mfma_f32_16x16x32_bf16 v[156:159], v[144:147], v[124:127], v[156:159]\n" \
  "s_waitcnt lgkmcnt(1)\n" \
  "v_mfma_f32_16x16x32_bf16 v[156:159], v[148:151], v[128:131], v[156:159]\n" \
  "s_waitcnt lgkmcnt(0)\n" \
  "v_mfma_f32_16x16x32_bf16 v[156:159], v[152:155], v[132:135], v[156:159]\n" \
  "s_nop 7\n" \
  "ds_read_b128 v[136:139], v176 offset:1024\n" \
  "ds_read_b128 v[144:147], v176 offset:3072\n" \
  "ds_read_b128 v[148:151], v176 offset:5120\n" \
  "ds_read_b128 v[152:155], v176 offset:7168\n" \
  "s_waitcnt lgkmcnt(3)\n" \
  "v_mfma_f32_16x16x32_bf16 v[160:163], v[136:139], v[120:123], 0\n" \
  "s_waitcnt lgkmcnt(2)\n" \
  "v_mfma_f32_16x16x32_bf16 v[160:163], v[144:147], v[124:127], v[160:163]\n" \
  "s_waitcnt lgkmcnt(1)\n" \
  "v_mfma_f32_16x16x32_bf16 v[160:163], v[148:151], v[128:131], v[160:163]\n" \
  "s_waitcnt lgkmcnt(0)\n" \
  "v_mfma_f32_16x16x32_bf16 v[160:163], v[152:155], v[132:135], v[160:163]\n" \
  "s_nop 7\n" \
  "v_max_f32 v164, v140, v141\n" \
  "v_max3_f32 v164, v164, v142, v143\n" \
  "v_max3_f32 v164, v164, v156, v157\n" \
  "v_max3_f32 v164, v164, v158, v159\n" \
  "s_nop 2\n" \
  "v_max3_f32 v164, v164, v160, v161\n" \
  "v_max3_f32 v164, v164, v162, v163\n" \
  "v_mov_b32 v165, v164\n" \
  "s_nop 1\n" \
  "v_permlane32_swap_b32 v164, v165\n" \
  "v_max_f32 v164, v164, v165\n" \
  "v_mov_b32 v165, v164\n" \
  "s_nop 1\n" \
  "v_permlane16_swap_b32 v164, v165\n" \
  "v_max_f32 v164, v164, v165\n" \
  "v_sub_f32 v116, v140, v164\n" \
  "v_sub_f32 v117, v141, v164\n" \
  "v_sub_f32 v118, v142, v164\n" \
  "v_sub_f32 v119, v143, v164\n" \
  "v_sub_f32 v156, v156, v164\n" \
  "v_sub_f32 v157, v157, v164\n" \
  "v_sub_f32 v158, v158, v164\n" \
  "v_sub_f32 v159, v159, v164\n" \
  "v_sub_f32 v160, v160, v164\n" \
  "v_sub_f32 v161, v161, v164\n" \
  "v_sub_f32 v162, v162, v164\n" \
  "v_sub_f32 v163, v163, v164\n" \
  "v_exp_f32 v116, v116\n" \
  "v_exp_f32 v117, v117\n" \
  "v_exp_f32 v118, v118\n" \
  "v_exp_f32 v119, v119\n" \
  "v_exp_f32 v156, v156\n" \
  "v_exp_f32 v157, v157\n" \
  "v_exp_f32 v158, v158\n" \
  "v_exp_f32 v159, v159\n" \
  "v_exp_f32 v160, v160\n" \
  "v_exp_f32 v161, v161\n" \
  "v_exp_f32 v162, v162\n" \
  "v_exp_f32 v163, v163\n" \
  "v_add_f32 v164, v116, v117\n" \
  "v_add_f32 v164, v164, v118\n" \
  "v_add_f32 v164, v164, v119\n" \
  "v_mul_f32 v164, 0.5, v164\n" \
  "v_add_f32 v164, v164, v156\n" \
  "v_add_f32 v164, v164, v157\n" \
  "v_add_f32 v164, v164, v158\n" \
  "v_add_f32 v164, v164, v159\n" \
  "v_add_f32 v164, v164, v160\n" \
  "v_add_f32 v164, v164, v161\n" \
  "v_add_f32 v164, v164, v162\n" \
  "v_add_f32 v164, v164, v163\n" \
  "v_mov_b32 v165, v164\n" \
  "s_nop 1\n" \
  "v_permlane32_swap_b32 v164, v165\n" \
  "v_add_f32 v164, v164, v165\n" \
  "v_mov_b32 v165, v164\n" \
  "s_nop 1\n" \
  "v_permlane16_swap_b32 v164, v165\n" \
  "v_add_f32 v164, v164, v165\n" \
  "v_rcp_f32 v188, v164\n" \
  "v_cvt_pk_bf16_f32 v156, v156, v157\n" \
  "v_cvt_pk_bf16_f32 v157, v158, v159\n" \
  "v_cvt_pk_bf16_f32 v160, v160, v161\n" \
  "v_cvt_pk_bf16_f32 v161, v162, v163\n" \
  "v_mov_b32 v165, 17408\n" \
  "s_cmp_eq_u32 s48, 0\n" \
  "s_cbranch_scc1 .LM_ex2_%=\n" \
  ".LM_ex1_%=:\n" \
  "ds_read_b64 v[166:167], v165\n" \
  "s_waitcnt lgkmcnt(0)\n" \
  "v_min_u32 v166, v166, v167\n" \
  "v_cmp_ge_u32 vcc, s48, v166\n" \
  "s_cbranch_vccnz .LM_ex1_%=\n" \
  ".LM_ex2_%=:\n" \
  "buffer_store_dwordx2 v[156:157], v247, s[40:43], 0 offen sc1\n" \
  "buffer_store_dwordx2 v[160:161], v247, s[40:43], 0 offen offset:256 sc1\n" \
  "v_pk_fma_f32 v[116:117], v[116:117], v[180:181], v[96:97] op_sel_hi:[1,0,1]\n" \
  "v_pk_fma_f32 v[118:119], v[118:119], v[180:181], v[98:99] op_sel_hi:[1,0,1]\n" \
  "v_pk_mul_f32 v[116:117], v[116:117], v[188:189] op_sel_hi:[1,0]\n" \
  "v_pk_mul_f32 v[118:119], v[118:119], v[188:189] op_sel_hi:[1,0]\n" \
  "v_cvt_pk_bf16_f32 v188, v116, v117\n" \
  "v_cvt_pk_bf16_f32 v189, v118, v119\n" \
  "s_waitcnt vmcnt(0)\n" \
  "s_mov_b32 s52, 2.0\n" \
  "s_cmp_eq_u32 s48, 0\n" \
  "s_cbranch_scc1 .LM_ex_b_%=\n" \
  "v_lshrrev_b32 v165, 1, v240\n" \
  "v_min_u32 v165, 280, v165\n" \
  "buffer_load_dwordx2 v[166:167], v165, s[40:43], %43 offen sc1\n" \
  "s_waitcnt vmcnt(0)\n" \
  "v_cmp_eq_u32 vcc, s55, v167\n" \
  "s_nop 1\n" \
  "v_cndmask_b32 v166, 0, v166, vcc\n" \
  "v_add_u32 v164, 2, v166\n" \
  "s_mov_b32 s69, 0\n" \
  ".LM_ex3_%=:\n" \
  "s_sleep 8\n" \
  "buffer_load_dwordx2 v[166:167], v165, s[40:43], %43 offen sc1\n" \
  "s_waitcnt vmcnt(0)\n" \
  "v_cmp_eq_u32 vcc, s55, v167\n" \
  "s_nop 1\n" \
  "v_cndmask_b32 v166, 0, v166, vcc\n" \
  "v_cmp_gt_u32 vcc, v164, v166\n" \
  "s_cbranch_vccz .LM_ex_b_%=\n" \
  "s_add_u32 s69, s69, 1\n" \
  "s_and_b32 s63, s69, 1023\n" \
  "s_cbranch_scc1 .LM_ex3_%=\n" \
  "s_cmp_gt_u32 s69, 2097152\n" \
  "s_cbranch_scc1 .LM_ex3_set_%=\n" \
  "buffer_load_dwordx2 v[166:167], off, s[40:43], %38 offset:128 sc1\n" \
  "s_waitcnt vmcnt(0)\n" \
  "v_readfirstlane_b32 s63, v167\n" \
  "s_cmp_eq_u32 s63, s55\n" \
  "s_cbranch_scc0 .LM_ex3_%=\n" \
  "s_branch .LM_ex_b_%=\n" \
  ".LM_ex3_set_%=:\n" \
  "v_mov_b32 v166, 1\n" \
  "v_mov_b32 v167, s55\n" \
  "buffer_store_dwordx2 v[166:167], off, s[40:43], %38 offset:128 sc1\n" \
  "s_waitcnt vmcnt(0)\n" \
  "s_branch .LM_ex_b_%=\n" \
  ".LM_end_%=:\n" \
  "s_waitcnt vmcnt(0)\n" \
  "s_waitcnt vmcnt(0)\n" \
  "s_nop 7\n" \
  "s_endpgm\n"
#define M_ASM_VOPS "+{v239}"(ci.sidx), "+{v240}"(ci.l16), "+{v241}"(ci.qw), "+{v242}"(ci.kw), "+{v243}"(ci.fq), "+{v244}"(ci.fk), "+{v245}"(ci.kxo), "+{v246}"(ci.pst), "+{v247}"(ci.ps2), "+{v248}"(ci.lx), "+{v249}"(ci.qxo), "+{v250}"(ci.sto), "+{v251}"(ci.plx), "+{v252}"(ci.mk), "+{v253}"(ci.ck0), "+{v254}"(ci.ck2), "+{v255}"(ci.hx), "+{v200}"(ci.vq0), "+{v201}"(ci.vq1), "+{v202}"(ci.vq2), "+{v203}"(ci.vq3), "+{v204}"(ci.tro0), "+{v205}"(ci.tro1), "+{v206}"(ci.m0o)
#define M_ASM_SOPS "s"(ci.ws), "s"(ci.Wa), "s"(ci.Wb), "s"(ci.mem0), "s"(ci.w), "s"(ci.o_a), "s"(ci.o_b), "s"(ci.o_nx), "s"(ci.o_exp), "s"(ci.lab), "s"(ci.f_p), "s"(ci.f_m), "s"(ci.f_dur), "s"(ci.f_go), "s"(ci.o_ctl), "s"(ci.o_dbg), "s"(ci.dret), "s"(ci.o_qx), "s"(ci.f_s), "s"(ci.f_r)
#define M_ASM_CLOB "v0", "v1", "v2", "v3", "v4", "v5", "v6", "v7", "v8", "v9", "v10", "v11", "v12", "v13", "v14", "v15", "v16", "v17", "v18", "v19", "v20", "v21", "v22", "v23", "v24", "v25", "v26", "v27", "v28", "v29", "v30", "v31", "v32", "v33", "v34", "v35", "v36", "v37", "v38", "v39", "v40", "v41", "v42", "v43", "v44", "v45", "v46", "v47", "v48", "v49", "v50", "v51", "v52", "v53", "v54", "v55", "v56", "v57", "v58", "v59", "v60", "v61", "v62", "v63", "v64", "v65", "v66", "v67", "v68", "v69", "v70", "v71", "v72", "v73", "v74", "v75", "v76", "v77", "v78", "v79", "v80", "v81", "v82", "v83", "v84", "v85", "v86", "v87", "v88", "v89", "v90", "v91", "v92", "v93", "v94", "v95", "v96", "v97", "v98", "v99", "v100", "v101", "v102", "v103", "v104", "v105", "v106", "v107", "v108", "v109", "v110", "v111", "v112", "v113", "v114", "v115", "v116", "v117", "v118", "v119", "v120", "v121", "v122", "v123", "v124", "v125", "v126", "v127", "v128", "v129", "v130", "v131", "v132", "v133", "v134", "v135", "v136", "v137", "v138", "v139", "v140", "v141", "v142", "v143", "v144", "v145", "v146", "v147", "v148", "v149", "v150", "v151", "v152", "v153", "v154", "v155", "v156", "v157", "v158", "v159", "v160", "v161", "v162", "v163", "v164", "v165", "v166", "v167", "v168", "v169", "v170", "v171", "v172", "v173", "v174", "v175", "v176", "v177", "v178", "v179", "v180", "v181", "v182", "v183", "v184", "v185", "v186", "v187", "v188", "v189", "v190", "v191", "v192", "v193", "v194", "v195", "v196", "v197", "v198", "v199", "a0", "a1", "a2", "a3", "a4", "a5", "a6", "a7", "a8", "a9", "a10", "a11", "a12", "a13", "a14", "a15", "a16", "a17", "a18", "a19", "a20", "a21", "a22", "a23", "a24", "a25", "a26", "a27", "a28", "a29", "a30", "a31", "a32", "a33", "a34", "a35", "a36", "a37", "a38", "a39", "a40", "a41", "a42", "a43", "a44", "a45", "a46", "a47", "a48", "a49", "a50", "a51", "a52", "a53", "a54", "a55", "a56", "a57", "a58", "a59", "a60", "a61", "a62", "a63", "a64", "a65", "a66", "a67", "a68", "a69", "a70", "a71", "a72", "a73", "a74", "a75", "a76", "a77", "a78", "a79", "a80", "a81", "a82", "a83", "a84", "a85", "a86", "a87", "a88", "a89", "a90", "a91", "a92", "a93", "a94", "a95", "a96", "a97", "a98", "a99", "a100", "a101", "a102", "a103", "a104", "a105", "a106", "a107", "a108", "a109", "a110", "a111", "a112", "a113", "a114", "a115", "a116", "a117", "a118", "a119", "a120", "a121", "a122", "a123", "a124", "a125", "a126", "a127", "a128", "a129", "a130", "a131", "a132", "a133", "a134", "a135", "a136", "a137", "a138", "a139", "a140", "a141", "a142", "a143", "a144", "a145", "a146", "a147", "a148", "a149", "a150", "a151", "a152", "a153", "a154", "a155", "a156", "a157", "a158", "a159", "a160", "a161", "a162", "a163", "a164", "a165", "a166", "a167", "a168", "a169", "a170", "a171", "a172", "a173", "a174", "a175", "a176", "a177", "a178", "a179", "a180", "a181", "a182", "a183", "a184", "a185", "a186", "a187", "a188", "a189", "a190", "a191", "a192", "a193", "a194", "a195", "a196", "a197", "a198", "a199", "a200", "a201", "a202", "a203", "a204", "a205", "a206", "a207", "a208", "a209", "a210", "a211", "a212", "a213", "a214", "a215", "a216", "a217", "a218", "a219", "a220", "a221", "a222", "a223", "a224", "a225", "a226", "a227", "a228", "a229", "a230", "a231", "a232", "a233", "a234", "a235", "a236", "a237", "a238", "a239", "a240", "a241", "a242", "a243", "a244", "a245", "a246", "a247", "a248", "a249", "a250", "a251", "a252", "a253", "a254", "a255", "s40", "s41", "s42", "s43", "s44", "s45", "s46", "s47", "s48", "s49", "s50", "s51", "s52", "s53", "s54", "s55", "s56", "s57", "s58", "s59", "s60", "s61", "s62", "s63", "s64", "s65", "s66", "s67", "s68", "s69", "s70", "s71", "s72", "s73", "s74", "s75", "s76", "s77", "s78", "s79", "s80", "s81", "s82", "s83", "s84", "s85", "s86", "s87", "s88", "s89", "s90", "s91", "s92", "s93", "s94", "s95", "s96", "s97", "s98", "s99", "vcc", "scc", "memory"
#define V_ASM_TEXT \
  "s_mov_b64 s[40:41], %32\n" \
  "s_and_b32 s41, s41, 0xffff\n" \
  "s_mov_b32 s42, 255852544\n" \
  "s_mov_b32 s43, 0x00020000\n" \
  "s_mov_b32 s46, 4718592\n" \
  "s_mov_b32 s47, 0x00020000\n" \
  "s_lshl_b32 s72, %36, 11\n" \
  "buffer_load_dword v196, off, s[40:43], %46 sc1\n" \
  "s_mov_b64 s[44:45], %34\n" \
  "s_and_b32 s45, s45, 0xffff\n" \
  "s_add_u32 s60, %38, 0\n" \
  "s_lshl_b32 s61, s72, 1\n" \
  "s_add_u32 m0, s61, 16384\n" \
  "s_nop 0\n" \
  "buffer_load_dwordx4 v204, s[44:47], s60 offen lds\n" \
  "buffer_load_dwordx4 v205, s[44:47], s60 offen offset:1024 lds\n" \
  "buffer_load_dwordx4 v206, s[44:47], s60 offen offset:2048 lds\n" \
  "buffer_load_dwordx4 v207, s[44:47], s60 offen offset:3072 lds\n" \
  "s_add_u32 s60, %38, 18432\n" \
  "s_lshl_b32 s61, s72, 1\n" \
  "s_add_u32 m0, s61, 32768\n" \
  "s_nop 0\n" \
  "buffer_load_dwordx4 v204, s[44:47], s60 offen lds\n" \
  "buffer_load_dwordx4 v205, s[44:47], s60 offen offset:1024 lds\n" \
  "buffer_load_dwordx4 v206, s[44:47], s60 offen offset:2048 lds\n" \
  "buffer_load_dwordx4 v207, s[44:47], s60 offen offset:3072 lds\n" \
  "s_add_u32 s60, %38, 256\n" \
  "s_lshl_b32 s61, s72, 1\n" \
  "s_add_u32 m0, s61, 49152\n" \
  "s_nop 0\n" \
  "buffer_load_dwordx4 v204, s[44:47], s60 offen lds\n" \
  "buffer_load_dwordx4 v205, s[44:47], s60 offen offset:1024 lds\n" \
  "buffer_load_dwordx4 v206, s[44:47], s60 offen offset:2048 lds\n" \
  "buffer_load_dwordx4 v207, s[44:47], s60 offen offset:3072 lds\n" \
  "s_add_u32 s60, %38, 18688\n" \
  "s_lshl_b32 s61, s72, 1\n" \
  "s_add_u32 m0, s61, 65536\n" \
  "s_nop 0\n" \
  "buffer_load_dwordx4 v204, s[44:47], s60 offen lds\n" \
  "buffer_load_dwordx4 v205, s[44:47], s60 offen offset:1024 lds\n" \
  "buffer_load_dwordx4 v206, s[44:47], s60 offen offset:2048 lds\n" \
  "buffer_load_dwordx4 v207, s[44:47], s60 offen offset:3072 lds\n" \
  "s_add_u32 s60, %38, 512\n" \
  "s_lshl_b32 s61, s72, 1\n" \
  "s_add_u32 m0, s61, 81920\n" \
  "s_nop 0\n" \
  "buffer_load_dwordx4 v204, s[44:47], s60 offen lds\n" \
  "buffer_load_dwordx4 v205, s[44:47], s60 offen offset:1024 lds\n" \
  "buffer_load_dwordx4 v206, s[44:47], s60 offen offset:2048 lds\n" \
  "buffer_load_dwordx4 v207, s[44:47], s60 offen offset:3072 lds\n" \
  "s_add_u32 s60, %38, 18944\n" \
  "s_lshl_b32 s61, s72, 1\n" \
  "s_add_u32 m0, s61, 98304\n" \
  "s_nop 0\n" \
  "buffer_load_dwordx4 v204, s[44:47], s60 offen lds\n" \
  "buffer_load_dwordx4 v205, s[44:47], s60 offen offset:1024 lds\n" \
  "buffer_load_dwordx4 v206, s[44:47], s60 offen offset:2048 lds\n" \
  "buffer_load_dwordx4 v207, s[44:47], s60 offen offset:3072 lds\n" \
  "s_add_u32 s60, %38, 768\n" \
  "s_lshl_b32 s61, s72, 1\n" \
  "s_add_u32 m0, s61, 114688\n" \
  "s_nop 0\n" \
  "buffer_load_dwordx4 v204, s[44:47], s60 offen lds\n" \
  "buffer_load_dwordx4 v205, s[44:47], s60 offen offset:1024 lds\n" \
  "buffer_load_dwordx4 v206, s[44:47], s60 offen offset:2048 lds\n" \
  "buffer_load_dwordx4 v207, s[44:47], s60 offen offset:3072 lds\n" \
  "s_add_u32 s60, %38, 19200\n" \
  "s_lshl_b32 s61, s72, 1\n" \
  "s_add_u32 m0, s61, 131072\n" \
  "s_nop 0\n" \
  "buffer_load_dwordx4 v204, s[44:47], s60 offen lds\n" \
  "buffer_load_dwordx4 v205, s[44:47], s60 offen offset:1024 lds\n" \
  "buffer_load_dwordx4 v206, s[44:47], s60 offen offset:2048 lds\n" \
  "buffer_load_dwordx4 v207, s[44:47], s60 offen offset:3072 lds\n" \
  "s_mov_b32 s60, %38\n" \
  "s_lshl_b32 s61, s72, 1\n" \
  "s_add_u32 m0, s61, 147456\n" \
  "s_nop 0\n" \
  "buffer_load_dwordx4 v208, s[44:47], s60 offen lds\n" \
  "buffer_load_dwordx4 v209, s[44:47], s60 offen offset:1024 lds\n" \
  "buffer_load_dwordx4 v210, s[44:47], s60 offen offset:2048 lds\n" \
  "buffer_load_dwordx4 v211, s[44:47], s60 offen offset:3072 lds\n" \
  "s_mov_b64 s[44:45], %35\n" \
  "s_and_b32 s45, s45, 0xffff\n" \
  "s_lshr_b32 s60, s72, 3\n" \
  "s_mov_b32 m0, s72\n" \
  "s_nop 0\n" \
  "buffer_load_dwordx4 v214, s[44:47], s60 offen lds\n" \
  "s_add_u32 s60, s60, 128\n" \
  "s_add_u32 m0, s72, 1024\n" \
  "s_nop 0\n" \
  "buffer_load_dwordx4 v214, s[44:47], s60 offen lds\n" \
  "s_movk_i32 s60, 1024\n" \
  "s_movk_i32 m0, 8192\n" \
  "s_nop 0\n" \
  "buffer_load_dwordx4 v214, s[44:47], s60 offen lds\n" \
  "s_waitcnt vmcnt(27)\n" \
  "s_barrier\n" \
  "v_add_u32 v194, 16384, v215\n" \
  "v_xor_b32 v195, 2176, v194\n" \
  "ds_read_b64_tr_b16 a[144:145], v194\n" \
  "ds_read_b64_tr_b16 a[146:147], v194 offset:512\n" \
  "ds_read_b64_tr_b16 a[148:149], v195\n" \
  "ds_read_b64_tr_b16 a[150:151], v195 offset:512\n" \
  "ds_read_b64_tr_b16 a[152:153], v194 offset:8192\n" \
  "ds_read_b64_tr_b16 a[154:155], v194 offset:8704\n" \
  "ds_read_b64_tr_b16 a[156:157], v195 offset:8192\n" \
  "ds_read_b64_tr_b16 a[158:159], v195 offset:8704\n" \
  "v_add_u32 v194, 32768, v215\n" \
  "v_xor_b32 v195, 2176, v194\n" \
  "ds_read_b64_tr_b16 a[160:161], v194\n" \
  "ds_read_b64_tr_b16 a[162:163], v194 offset:512\n" \
  "ds_read_b64_tr_b16 a[164:165], v195\n" \
  "ds_read_b64_tr_b16 a[166:167], v195 offset:512\n" \
  "ds_read_b64_tr_b16 a[168:169], v194 offset:8192\n" \
  "ds_read_b64_tr_b16 a[170:171], v194 offset:8704\n" \
  "ds_read_b64_tr_b16 a[172:173], v195 offset:8192\n" \
  "ds_read_b64_tr_b16 a[174:175], v195 offset:8704\n" \
  "v_add_u32 v194, 49152, v215\n" \
  "v_xor_b32 v195, 2176, v194\n" \
  "ds_read_b64_tr_b16 a[176:177], v194\n" \
  "ds_read_b64_tr_b16 a[178:179], v194 offset:512\n" \
  "ds_read_b64_tr_b16 a[180:181], v195\n" \
  "ds_read_b64_tr_b16 a[182:183], v195 offset:512\n" \
  "ds_read_b64_tr_b16 a[184:185], v194 offset:8192\n" \
  "ds_read_b64_tr_b16 a[186:187], v194 offset:8704\n" \
  "ds_read_b64_tr_b16 a[188:189], v195 offset:8192\n" \
  "ds_read_b64_tr_b16 a[190:191], v195 offset:8704\n" \
  "s_waitcnt lgkmcnt(0)\n" \
  "s_barrier\n" \
  "s_mov_b64 s[44:45], %33\n" \
  "s_and_b32 s45, s45, 0xffff\n" \
  "s_add_u32 s60, %37, 0\n" \
  "s_lshl_b32 s61, s72, 1\n" \
  "s_add_u32 m0, s61, 16384\n" \
  "s_nop 0\n" \
  "buffer_load_dwordx4 v200, s[44:47], s60 offen lds\n" \
  "buffer_load_dwordx4 v201, s[44:47], s60 offen offset:1024 lds\n" \
  "buffer_load_dwordx4 v202, s[44:47], s60 offen offset:2048 lds\n" \
  "buffer_load_dwordx4 v203, s[44:47], s60 offen offset:3072 lds\n" \
  "s_add_u32 s60, %37, 524288\n" \
  "s_lshl_b32 s61, s72, 1\n" \
  "s_add_u32 m0, s61, 32768\n" \
  "s_nop 0\n" \
  "buffer_load_dwordx4 v200, s[44:47], s60 offen lds\n" \
  "buffer_load_dwordx4 v201, s[44:47], s60 offen offset:1024 lds\n" \
  "buffer_load_dwordx4 v202, s[44:47], s60 offen offset:2048 lds\n" \
  "buffer_load_dwordx4 v203, s[44:47], s60 offen offset:3072 lds\n" \
  "s_add_u32 s60, %37, 1048576\n" \
  "s_lshl_b32 s61, s72, 1\n" \
  "s_add_u32 m0, s61, 49152\n" \
  "s_nop 0\n" \
  "buffer_load_dwordx4 v200, s[44:47], s60 offen lds\n" \
  "buffer_load_dwordx4 v201, s[44:47], s60 offen offset:1024 lds\n" \
  "buffer_load_dwordx4 v202, s[44:47], s60 offen offset:2048 lds\n" \
  "buffer_load_dwordx4 v203, s[44:47], s60 offen offset:3072 lds\n" \
  "v_readfirstlane_b32 s55, v196\n" \
  "s_add_u32 s55, s55, 1\n" \
  "s_waitcnt vmcnt(27)\n" \
  "s_barrier\n" \
  "v_add_u32 v194, 65536, v215\n" \
  "v_xor_b32 v195, 2176, v194\n" \
  "ds_read_b64_tr_b16 a[192:193], v194\n" \
  "ds_read_b64_tr_b16 a[194:195], v194 offset:512\n" \
  "ds_read_b64_tr_b16 a[196:197], v195\n" \
  "ds_read_b64_tr_b16 a[198:199], v195 offset:512\n" \
  "ds_read_b64_tr_b16 a[200:201], v194 offset:8192\n" \
  "ds_read_b64_tr_b16 a[202:203], v194 offset:8704\n" \
  "ds_read_b64_tr_b16 a[204:205], v195 offset:8192\n" \
  "ds_read_b64_tr_b16 a[206:207], v195 offset:8704\n" \
  "v_add_u32 v194, 81920, v215\n" \
  "v_xor_b32 v195, 2176, v194\n" \
  "ds_read_b64_tr_b16 a[208:209], v194\n" \
  "ds_read_b64_tr_b16 a[210:211], v194 offset:512\n" \
  "ds_read_b64_tr_b16 a[212:213], v195\n" \
  "ds_read_b64_tr_b16 a[214:215], v195 offset:512\n" \
  "ds_read_b64_tr_b16 a[216:217], v194 offset:8192\n" \
  "ds_read_b64_tr_b16 a[218:219], v194 offset:8704\n" \
  "ds_read_b64_tr_b16 a[220:221], v195 offset:8192\n" \
  "ds_read_b64_tr_b16 a[222:223], v195 offset:8704\n" \
  "v_add_u32 v194, 98304, v215\n" \
  "v_xor_b32 v195, 2176, v194\n" \
  "ds_read_b64_tr_b16 a[224:225], v194\n" \
  "ds_read_b64_tr_b16 a[226:227], v194 offset:512\n" \
  "ds_read_b64_tr_b16 a[228:229], v195\n" \
  "ds_read_b64_tr_b16 a[230:231], v195 offset:512\n" \
  "ds_read_b64_tr_b16 a[232:233], v194 offset:8192\n" \
  "ds_read_b64_tr_b16 a[234:235], v194 offset:8704\n" \
  "ds_read_b64_tr_b16 a[236:237], v195 offset:8192\n" \
  "ds_read_b64_tr_b16 a[238:239], v195 offset:8704\n" \
  "s_waitcnt lgkmcnt(0)\n" \
  "s_barrier\n" \
  "s_add_u32 s60, %37, 1572864\n" \
  "s_lshl_b32 s61, s72, 1\n" \
  "s_add_u32 m0, s61, 65536\n" \
  "s_nop 0\n" \
  "buffer_load_dwordx4 v200, s[44:47], s60 offen lds\n" \
  "buffer_load_dwordx4 v201, s[44:47], s60 offen offset:1024 lds\n" \
  "buffer_load_dwordx4 v202, s[44:47], s60 offen offset:2048 lds\n" \
  "buffer_load_dwordx4 v203, s[44:47], s60 offen offset:3072 lds\n" \
  "s_add_u32 s60, %37, 2097152\n" \
  "s_lshl_b32 s61, s72, 1\n" \
  "s_add_u32 m0, s61, 81920\n" \
  "s_nop 0\n" \
  "buffer_load_dwordx4 v200, s[44:47], s60 offen lds\n" \
  "buffer_load_dwordx4 v201, s[44:47], s60 offen offset:1024 lds\n" \
  "buffer_load_dwordx4 v202, s[44:47], s60 offen offset:2048 lds\n" \
  "buffer_load_dwordx4 v203, s[44:47], s60 offen offset:3072 lds\n" \
  "s_add_u32 s60, %37, 2621440\n" \
  "s_lshl_b32 s61, s72, 1\n" \
  "s_add_u32 m0, s61, 98304\n" \
  "s_nop 0\n" \
  "buffer_load_dwordx4 v200, s[44:47], s60 offen lds\n" \
  "buffer_load_dwordx4 v201, s[44:47], s60 offen offset:1024 lds\n" \
  "buffer_load_dwordx4 v202, s[44:47], s60 offen offset:2048 lds\n" \
  "buffer_load_dwordx4 v203, s[44:47], s60 offen offset:3072 lds\n" \
  "s_waitcnt vmcnt(27)\n" \
  "s_barrier\n" \
  "v_add_u32 v194, 114688, v215\n" \
  "v_xor_b32 v195, 2176, v194\n" \
  "ds_read_b64_tr_b16 a[240:241], v194\n" \
  "ds_read_b64_tr_b16 a[242:243], v194 offset:512\n" \
  "ds_read_b64_tr_b16 a[244:245], v195\n" \
  "ds_read_b64_tr_b16 a[246:247], v195 offset:512\n" \
  "ds_read_b64_tr_b16 a[248:249], v194 offset:8192\n" \
  "ds_read_b64_tr_b16 a[250:251], v194 offset:8704\n" \
  "ds_read_b64_tr_b16 a[252:253], v195 offset:8192\n" \
  "ds_read_b64_tr_b16 a[254:255], v195 offset:8704\n" \
  "v_add_u32 v194, 131072, v215\n" \
  "v_xor_b32 v195, 2176, v194\n" \
  "ds_read_b64_tr_b16 v[0:1], v194\n" \
  "ds_read_b64_tr_b16 v[2:3], v194 offset:512\n" \
  "ds_read_b64_tr_b16 v[4:5], v195\n" \
  "ds_read_b64_tr_b16 v[6:7], v195 offset:512\n" \
  "ds_read_b64_tr_b16 v[8:9], v194 offset:8192\n" \
  "ds_read_b64_tr_b16 v[10:11], v194 offset:8704\n" \
  "ds_read_b64_tr_b16 v[12:13], v195 offset:8192\n" \
  "ds_read_b64_tr_b16 v[14:15], v195 offset:8704\n" \
  "v_mov_b32 v194, v216\n" \
  "v_xor_b32 v195, 2176, v194\n" \
  "ds_read_b64_tr_b16 v[16:17], v194\n" \
  "ds_read_b64_tr_b16 v[18:19], v194 offset:512\n" \
  "ds_read_b64_tr_b16 v[20:21], v195\n" \
  "ds_read_b64_tr_b16 v[22:23], v195 offset:512\n" \
  "ds_read_b64_tr_b16 v[24:25], v194 offset:8192\n" \
  "ds_read_b64_tr_b16 v[26:27], v194 offset:8704\n" \
  "ds_read_b64_tr_b16 v[28:29], v195 offset:8192\n" \
  "ds_read_b64_tr_b16 v[30:31], v195 offset:8704\n" \
  "v_mov_b32 v194, v217\n" \
  "v_xor_b32 v195, 2176, v194\n" \
  "ds_read_b64_tr_b16 v[32:33], v194\n" \
  "ds_read_b64_tr_b16 v[34:35], v194 offset:512\n" \
  "ds_read_b64_tr_b16 v[36:37], v195\n" \
  "ds_read_b64_tr_b16 v[38:39], v195 offset:512\n" \
  "ds_read_b64_tr_b16 v[40:41], v194 offset:8192\n" \
  "ds_read_b64_tr_b16 v[42:43], v194 offset:8704\n" \
  "ds_read_b64_tr_b16 v[44:45], v195 offset:8192\n" \
  "ds_read_b64_tr_b16 v[46:47], v195 offset:8704\n" \
  "s_waitcnt lgkmcnt(0)\n" \
  "s_barrier\n" \
  "s_add_u32 s60, %37, 3145728\n" \
  "s_lshl_b32 s61, s72, 1\n" \
  "s_add_u32 m0, s61, 114688\n" \
  "s_nop 0\n" \
  "buffer_load_dwordx4 v200, s[44:47], s60 offen lds\n" \
  "buffer_load_dwordx4 v201, s[44:47], s60 offen offset:1024 lds\n" \
  "buffer_load_dwordx4 v202, s[44:47], s60 offen offset:2048 lds\n" \
  "buffer_load_dwordx4 v203, s[44:47], s60 offen offset:3072 lds\n" \
  "s_add_u32 s60, %37, 3670016\n" \
  "s_lshl_b32 s61, s72, 1\n" \
  "s_add_u32 m0, s61, 131072\n" \
  "s_nop 0\n" \
  "buffer_load_dwordx4 v200, s[44:47], s60 offen lds\n" \
  "buffer_load_dwordx4 v201, s[44:47], s60 offen offset:1024 lds\n" \
  "buffer_load_dwordx4 v202, s[44:47], s60 offen offset:2048 lds\n" \
  "buffer_load_dwordx4 v203, s[44:47], s60 offen offset:3072 lds\n" \
  "s_add_u32 s60, %37, 4194304\n" \
  "s_lshl_b32 s61, s72, 1\n" \
  "s_add_u32 m0, s61, 147456\n" \
  "s_nop 0\n" \
  "buffer_load_dwordx4 v200, s[44:47], s60 offen lds\n" \
  "buffer_load_dwordx4 v201, s[44:47], s60 offen offset:1024 lds\n" \
  "buffer_load_dwordx4 v202, s[44:47], s60 offen offset:2048 lds\n" \
  "buffer_load_dwordx4 v203, s[44:47], s60 offen offset:3072 lds\n" \
  "buffer_load_dwordx2 v[198:199], off, s[40:43], %45 sc1\n" \
  "s_waitcnt vmcnt(25)\n" \
  "s_barrier\n" \
  "v_add_u32 v194, 16384, v212\n" \
  "ds_read_b64_tr_b16 a[0:1], v194\n" \
  "ds_read_b64_tr_b16 a[2:3], v194 offset:512\n" \
  "ds_read_b64_tr_b16 a[4:5], v194 offset:8192\n" \
  "ds_read_b64_tr_b16 a[6:7], v194 offset:8704\n" \
  "v_add_u32 v195, 16384, v213\n" \
  "ds_read_b64_tr_b16 a[72:73], v195\n" \
  "ds_read_b64_tr_b16 a[74:75], v195 offset:512\n" \
  "ds_read_b64_tr_b16 a[76:77], v195 offset:8192\n" \
  "ds_read_b64_tr_b16 a[78:79], v195 offset:8704\n" \
  "v_add_u32 v194, 32768, v212\n" \
  "ds_read_b64_tr_b16 a[8:9], v194\n" \
  "ds_read_b64_tr_b16 a[10:11], v194 offset:512\n" \
  "ds_read_b64_tr_b16 a[12:13], v194 offset:8192\n" \
  "ds_read_b64_tr_b16 a[14:15], v194 offset:8704\n" \
  "v_add_u32 v195, 32768, v213\n" \
  "ds_read_b64_tr_b16 a[80:81], v195\n" \
  "ds_read_b64_tr_b16 a[82:83], v195 offset:512\n" \
  "ds_read_b64_tr_b16 a[84:85], v195 offset:8192\n" \
  "ds_read_b64_tr_b16 a[86:87], v195 offset:8704\n" \
  "v_add_u32 v194, 49152, v212\n" \
  "ds_read_b64_tr_b16 a[16:17], v194\n" \
  "ds_read_b64_tr_b16 a[18:19], v194 offset:512\n" \
  "ds_read_b64_tr_b16 a[20:21], v194 offset:8192\n" \
  "ds_read_b64_tr_b16 a[22:23], v194 offset:8704\n" \
  "v_add_u32 v195, 49152, v213\n" \
  "ds_read_b64_tr_b16 a[88:89], v195\n" \
  "ds_read_b64_tr_b16 a[90:91], v195 offset:512\n" \
  "ds_read_b64_tr_b16 a[92:93], v195 offset:8192\n" \
  "ds_read_b64_tr_b16 a[94:95], v195 offset:8704\n" \
  "s_waitcnt lgkmcnt(0)\n" \
  "s_waitcnt vmcnt(13)\n" \
  "s_barrier\n" \
  "v_add_u32 v194, 65536, v212\n" \
  "ds_read_b64_tr_b16 a[24:25], v194\n" \
  "ds_read_b64_tr_b16 a[26:27], v194 offset:512\n" \
  "ds_read_b64_tr_b16 a[28:29], v194 offset:8192\n" \
  "ds_read_b64_tr_b16 a[30:31], v194 offset:8704\n" \
  "v_add_u32 v195, 65536, v213\n" \
  "ds_read_b64_tr_b16 a[96:97], v195\n" \
  "ds_read_b64_tr_b16 a[98:99], v195 offset:512\n" \
  "ds_read_b64_tr_b16 a[100:101], v195 offset:8192\n" \
  "ds_read_b64_tr_b16 a[102:103], v195 offset:8704\n" \
  "v_add_u32 v194, 81920, v212\n" \
  "ds_read_b64_tr_b16 a[32:33], v194\n" \
  "ds_read_b64_tr_b16 a[34:35], v194 offset:512\n" \
  "ds_read_b64_tr_b16 a[36:37], v194 offset:8192\n" \
  "ds_read_b64_tr_b16 a[38:39], v194 offset:8704\n" \
  "v_add_u32 v195, 81920, v213\n" \
  "ds_read_b64_tr_b16 a[104:105], v195\n" \
  "ds_read_b64_tr_b16 a[106:107], v195 offset:512\n" \
  "ds_read_b64_tr_b16 a[108:109], v195 offset:8192\n" \
  "ds_read_b64_tr_b16 a[110:111], v195 offset:8704\n" \
  "v_add_u32 v194, 98304, v212\n" \
  "ds_read_b64_tr_b16 a[40:41], v194\n" \
  "ds_read_b64_tr_b16 a[42:43], v194 offset:512\n" \
  "ds_read_b64_tr_b16 a[44:45], v194 offset:8192\n" \
  "ds_read_b64_tr_b16 a[46:47], v194 offset:8704\n" \
  "v_add_u32 v195, 98304, v213\n" \
  "ds_read_b64_tr_b16 a[112:113], v195\n" \
  "ds_read_b64_tr_b16 a[114:115], v195 offset:512\n" \
  "ds_read_b64_tr_b16 a[116:117], v195 offset:8192\n" \
  "ds_read_b64_tr_b16 a[118:119], v195 offset:8704\n" \
  "s_waitcnt lgkmcnt(0)\n" \
  "s_waitcnt vmcnt(1)\n" \
  "s_barrier\n" \
  "v_add_u32 v194, 114688, v212\n" \
  "ds_read_b64_tr_b16 a[48:49], v194\n" \
  "ds_read_b64_tr_b16 a[50:51], v194 offset:512\n" \
  "ds_read_b64_tr_b16 a[52:53], v194 offset:8192\n" \
  "ds_read_b64_tr_b16 a[54:55], v194 offset:8704\n" \
  "v_add_u32 v195, 114688, v213\n" \
  "ds_read_b64_tr_b16 a[120:121], v195\n" \
  "ds_read_b64_tr_b16 a[122:123], v195 offset:512\n" \
  "ds_read_b64_tr_b16 a[124:125], v195 offset:8192\n" \
  "ds_read_b64_tr_b16 a[126:127], v195 offset:8704\n" \
  "v_add_u32 v194, 131072, v212\n" \
  "ds_read_b64_tr_b16 a[56:57], v194\n" \
  "ds_read_b64_tr_b16 a[58:59], v194 offset:512\n" \
  "ds_read_b64_tr_b16 a[60:61], v194 offset:8192\n" \
  "ds_read_b64_tr_b16 a[62:63], v194 offset:8704\n" \
  "v_add_u32 v195, 131072, v213\n" \
  "ds_read_b64_tr_b16 a[128:129], v195\n" \
  "ds_read_b64_tr_b16 a[130:131], v195 offset:512\n" \
  "ds_read_b64_tr_b16 a[132:133], v195 offset:8192\n" \
  "ds_read_b64_tr_b16 a[134:135], v195 offset:8704\n" \
  "v_add_u32 v194, 147456, v212\n" \
  "ds_read_b64_tr_b16 a[64:65], v194\n" \
  "ds_read_b64_tr_b16 a[66:67], v194 offset:512\n" \
  "ds_read_b64_tr_b16 a[68:69], v194 offset:8192\n" \
  "ds_read_b64_tr_b16 a[70:71], v194 offset:8704\n" \
  "v_add_u32 v195, 147456, v213\n" \
  "ds_read_b64_tr_b16 a[136:137], v195\n" \
  "ds_read_b64_tr_b16 a[138:139], v195 offset:512\n" \
  "ds_read_b64_tr_b16 a[140:141], v195 offset:8192\n" \
  "ds_read_b64_tr_b16 a[142:143], v195 offset:8704\n" \
  "s_waitcnt lgkmcnt(0)\n" \
  "s_waitcnt vmcnt(0)\n" \
  "v_readfirstlane_b32 s96, v198\n" \
  "v_readfirstlane_b32 s63, v199\n" \
  "s_cmp_eq_u32 s63, s55\n" \
  "s_cselect_b32 s96, s96, 0\n" \
  "s_cmp_ge_u32 s96, 0xc0000000\n" \
  "s_cbranch_scc1 .LV_go_x_%=\n" \
  "s_mov_b32 s69, 0\n" \
  ".LV_go_%=:\n" \
  "buffer_load_dwordx2 v[196:197], off, s[40:43], %45 sc1\n" \
  "s_waitcnt vmcnt(0)\n" \
  "v_readfirstlane_b32 s96, v196\n" \
  "v_readfirstlane_b32 s63, v197\n" \
  "s_cmp_eq_u32 s63, s55\n" \
  "s_cselect_b32 s96, s96, 0\n" \
  "s_cmp_ge_u32 s96, 0xc0000000\n" \
  "s_cbranch_scc1 .LV_go_x_%=\n" \
  "s_sleep 8\n" \
  "s_add_u32 s69, s69, 1\n" \
  "s_and_b32 s63, s69, 1023\n" \
  "s_cbranch_scc1 .LV_go_%=\n" \
  "s_cmp_gt_u32 s69, 2097152\n" \
  "s_cbranch_scc1 .LV_go_set_%=\n" \
  "buffer_load_dwordx2 v[196:197], off, s[40:43], %46 offset:128 sc1\n" \
  "s_waitcnt vmcnt(0)\n" \
  "v_readfirstlane_b32 s63, v197\n" \
  "s_cmp_eq_u32 s63, s55\n" \
  "s_cbranch_scc0 .LV_go_%=\n" \
  "s_branch .LV_go_x_%=\n" \
  ".LV_go_set_%=:\n" \
  "v_mov_b32 v196, 1\n" \
  "v_mov_b32 v197, s55\n" \
  "buffer_store_dwordx2 v[196:197], off, s[40:43], %46 offset:128 sc1\n" \
  "s_waitcnt vmcnt(0)\n" \
  ".LV_go_x_%=:\n" \
  "s_and_b32 s60, s96, 2\n" \
  "s_lshl_b32 s60, s60, 12\n" \
  "s_xor_b32 s74, %42, s60\n" \
  "s_xor_b32 s75, %44, s60\n" \
  "s_bfe_u32 s56, s96, 0x30010\n" \
  "s_lshl_b32 s56, s56, 12\n" \
  "s_or_b32 s56, s56, 0x200000\n" \
  "s_mov_b32 s57, 1052672\n" \
  "s_lshl_b32 s61, %41, 2\n" \
  "s_lshr_b32 s51, s96, s61\n" \
  "s_and_b32 s51, s51, 7\n" \
  "s_lshl_b32 s51, s51, 12\n" \
  "s_xor_b32 s51, s51, 4210688\n" \
  "s_mov_b32 s71, 0\n" \
  "s_add_u32 s45, s51, 886784\n" \
  "s_mov_b32 s46, -1\n" \
  "v_mov_b64 v[48:49], -1\n" \
  "v_mov_b64 v[50:51], -1\n" \
  "v_and_b32 v194, 3, v250\n" \
  "v_lshlrev_b32 v194, 2, v194\n" \
  "v_lshrrev_b32 v194, v194, s96\n" \
  "v_and_b32 v194, 7, v194\n" \
  "v_lshlrev_b32 v194, 12, v194\n" \
  "v_and_b32 v250, -4, v250\n" \
  "v_xor_b32 v250, v250, v194\n" \
  "v_and_b32 v194, 3, v251\n" \
  "v_lshlrev_b32 v194, 2, v194\n" \
  "v_lshrrev_b32 v194, v194, s96\n" \
  "v_and_b32 v194, 7, v194\n" \
  "v_lshlrev_b32 v194, 12, v194\n" \
  "v_and_b32 v251, -4, v251\n" \
  "v_xor_b32 v251, v251, v194\n" \
  "v_and_b32 v194, 3, v252\n" \
  "v_lshlrev_b32 v194, 2, v194\n" \
  "v_lshrrev_b32 v194, v194, s96\n" \
  "v_and_b32 v194, 7, v194\n" \
  "v_lshlrev_b32 v194, 12, v194\n" \
  "v_and_b32 v252, -4, v252\n" \
  "v_xor_b32 v252, v252, v194\n" \
  "v_xor_b32 v185, s56, v250\n" \
  "buffer_store_dwordx4 v[48:51], v185, s[40:43], 0 offen sc1\n" \
  "v_xor_b32 v186, s56, v251\n" \
  "buffer_store_dwordx4 v[48:51], v186, s[40:43], 0 offen sc1\n" \
  "v_xor_b32 v187, s56, v252\n" \
  "buffer_store_dwordx2 v[48:49], v187, s[40:43], 0 offen sc1\n" \
  "s_xor_b32 s62, s56, s57\n" \
  "s_nop 1\n" \
  "v_xor_b32 v185, s62, v250\n" \
  "buffer_store_dwordx4 v[48:51], v185, s[40:43], 0 offen sc1\n" \
  "v_xor_b32 v186, s62, v251\n" \
  "buffer_store_dwordx4 v[48:51], v186, s[40:43], 0 offen sc1\n" \
  "v_xor_b32 v187, s62, v252\n" \
  "buffer_store_dwordx2 v[48:49], v187, s[40:43], 0 offen sc1\n" \
  "s_andn2_b32 s60, 2, s96\n" \
  "s_lshl_b32 s60, s60, 12\n" \
  "s_xor_b32 s60, s60, 8192\n" \
  "s_xor_b32 s58, %39, s60\n" \
  "s_mov_b32 s49, 0\n" \
  "s_add_u32 s47, %49, s60\n" \
  "s_add_u32 s97, s60, 8388608\n" \
  "s_xor_b32 s50, %40, s60\n" \
  "s_mov_b32 s48, 0\n" \
  "s_mov_b32 s54, 0\n" \
  "s_cmp_eq_u32 %36, 0\n" \
  "s_cselect_b64 s[88:89], -1, 0\n" \
  "s_mov_b32 s68, 0\n" \
  "s_mov_b32 s70, 1\n" \
  "s_mov_b32 s73, 0\n" \
  "s_mov_b32 s44, 0\n" \
  "s_mov_b32 s53, 0\n" \
  "s_memrealtime s[86:87]\n" \
  "s_memtime s[76:77]\n" \
  "s_waitcnt lgkmcnt(0)\n" \
  "s_mov_b32 s90, s76\n" \
  "s_mov_b32 s87, 340\n" \
  "s_mov_b32 s64, 0x3e0293ee\n" \
  "s_mov_b32 s65, 0xbe0293ee\n" \
  "v_mov_b64 v[60:61], 0\n" \
  "v_mov_b64 v[62:63], 0\n" \
  "v_mov_b64 v[64:65], 0\n" \
  "v_mov_b64 v[66:67], 0\n" \
  "v_mov_b64 v[68:69], 0\n" \
  "v_mov_b64 v[70:71], 0\n" \
  "v_mov_b64 v[72:73], 0\n" \
  "v_mov_b64 v[74:75], 0\n" \
  "v_mov_b64 v[76:77], 0\n" \
  "v_mov_b64 v[78:79], 0\n" \
  "v_mov_b64 v[80:81], 0\n" \
  "v_mov_b64 v[82:83], 0\n" \
  "v_mov_b64 v[84:85], 0\n" \
  "v_mov_b64 v[86:87], 0\n" \
  "v_mov_b32 v56, 0\n" \
  "v_mov_b32 v57, s55\n" \
  "v_mov_b64 v[58:59], v[56:57]\n" \
  "v_mov_b64 v[90:91], 0\n" \
  "s_mov_b32 s67, 2130706432\n" \
  "s_waitcnt vmcnt(0) lgkmcnt(0)\n" \
  "s_barrier\n" \
  "s_branch .LV_p_%=\n" \
  "s_nop 7\n" \
  ".p2align 6\n" \
  ".fill (((20 - (.LV_p_%= - .LV_rot_%=)) & 63) >> 2), 4, 0xBF800000\n" \
  ".LV_rot_%=:\n" \
  "s_xor_b32 s51, s51, s56\n" \
  "s_xor_b32 s71, s71, s56\n" \
  "v_xor_b32 v250, s56, v250\n" \
  "v_xor_b32 v251, s56, v251\n" \
  "v_xor_b32 v252, s56, v252\n" \
  "s_mov_b32 s60, s56\n" \
  "s_mov_b32 s56, s57\n" \
  "s_mov_b32 s57, s60\n" \
  "s_add_u32 s59, s51, 819200\n" \
  "s_mov_b32 m0, 0\n" \
  "s_mov_b32 s70, 0\n" \
  "s_waitcnt lgkmcnt(0)\n" \
  ".LV_rt_%=:\n" \
  "s_barrier\n" \
  ".LV_p_%=:\n" \
  "ds_read_b128 v[92:95], v243\n" \
  "ds_read_b128 v[96:99], v243 offset:3072\n" \
  "ds_read_b128 v[100:103], v243 offset:1024\n" \
  "ds_read_b128 v[104:107], v243 offset:2048\n" \
  "s_waitcnt lgkmcnt(3)\n" \
  "v_smfmac_f32_16x16x64_bf16 v[60:63], v[92:95], a[0:7], v242\n" \
  "s_barrier\n" \
  "ds_read_b128 v[108:111], v243 offset:4096\n" \
  "v_smfmac_f32_16x16x64_bf16 v[64:67], v[92:95], a[72:79], v242\n" \
  "ds_read_b128 v[112:115], v243 offset:5120\n" \
  "s_waitcnt lgkmcnt(3)\n" \
  "v_smfmac_f32_16x16x64_bf16 v[60:63], v[100:103], a[8:15], v242\n" \
  "ds_read_b128 v[116:119], v243 offset:6144\n" \
  "s_waitcnt lgkmcnt(3)\n" \
  "v_smfmac_f32_16x16x64_bf16 v[60:63], v[104:107], a[16:23], v242\n" \
  "ds_read_b128 v[120:123], v243 offset:7168\n" \
  "v_smfmac_f32_16x16x64_bf16 v[60:63], v[96:99], a[24:31], v242\n" \
  "ds_read_b128 v[124:127], v243 offset:8192\n" \
  "s_waitcnt lgkmcnt(4)\n" \
  "v_smfmac_f32_16x16x64_bf16 v[60:63], v[108:111], a[32:39], v242\n" \
  "s_waitcnt lgkmcnt(3)\n" \
  "v_smfmac_f32_16x16x64_bf16 v[60:63], v[112:115], a[40:47], v242\n" \
  "s_waitcnt lgkmcnt(2)\n" \
  "v_smfmac_f32_16x16x64_bf16 v[60:63], v[116:119], a[48:55], v242\n" \
  "s_waitcnt lgkmcnt(1)\n" \
  "v_smfmac_f32_16x16x64_bf16 v[60:63], v[120:123], a[56:63], v242\n" \
  "s_waitcnt lgkmcnt(0)\n" \
  "v_smfmac_f32_16x16x64_bf16 v[60:63], v[124:127], a[64:71], v242\n" \
  "s_nop 2\n" \
  "v_smfmac_f32_16x16x64_bf16 v[64:67], v[100:103], a[80:87], v242\n" \
  "s_nop 2\n" \
  "v_smfmac_f32_16x16x64_bf16 v[64:67], v[104:107], a[88:95], v242\n" \
  "v_add_f32 v60, v60, v62\n" \
  "v_add_f32 v61, v61, v63\n" \
  "v_cvt_pk_bf16_f32 v88, v60, v61\n" \
  "v_cmp_u_f32 vcc, v60, v61\n" \
  "v_smfmac_f32_16x16x64_bf16 v[64:67], v[96:99], a[96:103], v242\n" \
  "s_cbranch_vccnz .LV_slow_%=\n" \
  ".LV_back_%=:\n" \
  "ds_write_b32 v244, v88\n" \
  "v_smfmac_f32_16x16x64_bf16 v[64:67], v[108:111], a[104:111], v242\n" \
  "v_smfmac_f32_16x16x64_bf16 v[64:67], v[112:115], a[112:119], v242\n" \
  "v_smfmac_f32_16x16x64_bf16 v[64:67], v[116:119], a[120:127], v242\n" \
  "v_smfmac_f32_16x16x64_bf16 v[64:67], v[120:123], a[128:135], v242\n" \
  "s_waitcnt lgkmcnt(0)\n" \
  "s_waitcnt vmcnt(0) \n" \
  "s_barrier\n" \
  "ds_read_b64_tr_b16 v[92:93], v245\n" \
  "ds_read_b64_tr_b16 v[94:95], v245 offset:512\n" \
  "v_smfmac_f32_16x16x64_bf16 v[64:67], v[124:127], a[136:143], v242\n" \
  "ds_read_b64_tr_b16 v[96:97], v245 offset:1024\n" \
  "ds_read_b64_tr_b16 v[98:99], v245 offset:1536\n" \
  "s_nop 5\n" \
  "v_add_f32 v64, v64, v66\n" \
  "v_add_f32 v65, v65, v67\n" \
  "v_cvt_pk_bf16_f32 v89, v64, v65\n" \
  "s_waitcnt lgkmcnt(2)\n" \
  "v_smfmac_f32_16x16x64_bf16 v[76:79], v[92:95], a[208:215], v248\n" \
  "ds_write_b32 v244, v89 offset:256\n" \
  "s_waitcnt lgkmcnt(1)\n" \
  "v_smfmac_f32_16x16x64_bf16 v[76:79], v[96:99], a[216:223], v248\n" \
  "v_smfmac_f32_16x16x64_bf16 v[68:71], v[92:95], a[144:151], v248\n" \
  "v_smfmac_f32_16x16x64_bf16 v[72:75], v[92:95], a[176:183], v248\n" \
  "buffer_store_dwordx2 v[56:57], off, s[40:43], %43 sc1\n" \
  "v_smfmac_f32_16x16x64_bf16 v[68:71], v[96:99], a[152:159], v248\n" \
  "s_waitcnt lgkmcnt(0)\n" \
  "s_barrier\n" \
  "v_smfmac_f32_16x16x64_bf16 v[80:83], v[92:95], a[240:247], v248\n" \
  "ds_read_b64_tr_b16 v[102:103], v245 offset:768\n" \
  "ds_read_b64_tr_b16 v[100:101], v245 offset:256\n" \
  "v_smfmac_f32_16x16x64_bf16 v[84:87], v[92:95], v[16:23], v248\n" \
  "ds_read_b64_tr_b16 v[94:95], v245 offset:1792\n" \
  "ds_read_b64_tr_b16 v[92:93], v245 offset:1280\n" \
  "v_smfmac_f32_16x16x64_bf16 v[80:83], v[96:99], a[248:255], v248\n" \
  "v_smfmac_f32_16x16x64_bf16 v[72:75], v[96:99], a[184:191], v248\n" \
  "s_waitcnt lgkmcnt(2)\n" \
  "v_smfmac_f32_16x16x64_bf16 v[68:71], v[100:103], a[160:167], v248\n" \
  "s_waitcnt lgkmcnt(0)\n" \
  "v_smfmac_f32_16x16x64_bf16 v[68:71], v[92:95], a[168:175], v248\n" \
  "s_nop 2\n" \
  "v_smfmac_f32_16x16x64_bf16 v[72:75], v[100:103], a[192:199], v248\n" \
  "s_nop 2\n" \
  "v_smfmac_f32_16x16x64_bf16 v[72:75], v[92:95], a[200:207], v248\n" \
  "s_nop 1\n" \
  "v_cvt_pk_bf16_f32 v68, v68, v69\n" \
  "v_smfmac_f32_16x16x64_bf16 v[84:87], v[96:99], v[24:31], v248\n" \
  "s_nop 0\n" \
  "v_cvt_pk_bf16_f32 v69, v70, v71\n" \
  "v_smfmac_f32_16x16x64_bf16 v[76:79], v[100:103], a[224:231], v248\n" \
  "v_smfmac_f32_16x16x64_bf16 v[80:83], v[100:103], v[0:7], v248\n" \
  "v_cvt_pk_bf16_f32 v71, v74, v75\n" \
  "v_smfmac_f32_16x16x64_bf16 v[84:87], v[100:103], v[32:39], v248\n" \
  "v_cvt_pk_bf16_f32 v70, v72, v73\n" \
  "v_smfmac_f32_16x16x64_bf16 v[76:79], v[92:95], a[232:239], v248\n" \
  "s_nop 1\n" \
  "v_permlane16_swap_b32 v69, v71\n" \
  "v_smfmac_f32_16x16x64_bf16 v[80:83], v[92:95], v[8:15], v248\n" \
  "s_nop 0\n" \
  "s_nop 0\n" \
  "v_permlane16_swap_b32 v68, v70\n" \
  "v_smfmac_f32_16x16x64_bf16 v[84:87], v[92:95], v[40:47], v248\n" \
  "buffer_store_dwordx4 v[68:71], v250, s[40:43], 0 offen sc1\n" \
  "v_cvt_pk_bf16_f32 v76, v76, v77\n" \
  "v_cvt_pk_bf16_f32 v77, v78, v79\n" \
  "s_nop 0\n" \
  "v_cvt_pk_bf16_f32 v78, v80, v81\n" \
  "v_cvt_pk_bf16_f32 v79, v82, v83\n" \
  "s_nop 0\n" \
  "v_permlane16_swap_b32 v76, v78\n" \
  "v_cvt_pk_bf16_f32 v84, v84, v85\n" \
  "v_cvt_pk_bf16_f32 v85, v86, v87\n" \
  "v_permlane16_swap_b32 v77, v79\n" \
  "buffer_store_dwordx4 v[76:79], v251, s[40:43], 0 offen sc1\n" \
  "buffer_store_dwordx2 v[84:85], v252, s[40:43], 0 offen sc1\n" \
  ".LV_loop_%=:\n" \
  "s_cmp_ge_u32 s48, 58\n" \
  "s_cbranch_scc1 .LV_cp_%=\n" \
  ".LV_cp_b_%=:\n" \
  "v_mov_b64 v[60:61], 0\n" \
  "v_mov_b64 v[62:63], 0\n" \
  "v_mov_b64 v[64:65], 0\n" \
  "v_mov_b64 v[66:67], 0\n" \
  "v_mov_b64 v[68:69], 0\n" \
  "v_mov_b64 v[70:71], 0\n" \
  "v_mov_b64 v[72:73], 0\n" \
  "v_mov_b64 v[74:75], 0\n" \
  "v_mov_b64 v[76:77], 0\n" \
  "v_mov_b64 v[78:79], 0\n" \
  "v_mov_b64 v[80:81], 0\n" \
  "v_mov_b64 v[82:83], 0\n" \
  "v_mov_b64 v[84:85], 0\n" \
  "v_mov_b64 v[86:87], 0\n" \
  "ds_read_b32 v92, v246\n" \
  "ds_read_b32 v93, v246 offset:256\n" \
  "v_mov_b64 v[94:95], 0\n" \
  "s_sub_u32 s60, s48, 1\n" \
  "s_cselect_b32 s60, 8192, s60\n" \
  "s_lshl_b32 s60, s60, 14\n" \
  "s_xor_b32 s60, s60, s50\n" \
  "buffer_store_dwordx4 v[88:91], v247, s[40:43], s60 offen sc1\n" \
  "s_waitcnt lgkmcnt(0)\n" \
  "buffer_store_dwordx4 v[92:95], v249, s[40:43], s60 offen sc1\n" \
  "s_add_u32 s48, s48, 1\n" \
  "v_mov_b32 v56, s48\n" \
  "s_waitcnt lgkmcnt(0)\n" \
  "s_cmp_gt_u32 s48, 57\n" \
  "s_cbranch_scc1 .LV_ef_%=\n" \
  ".LV_ef_b_%=:\n" \
  "s_branch .LV_rot_%=\n" \
  "s_nop 7\n" \
  ".LV_slow_%=:\n" \
  "s_bitcmp1_b32 s70, 0\n" \
  "s_cbranch_scc1 .LV_back_%=\n" \
  "s_waitcnt lgkmcnt(0)\n" \
  "s_cmp_lt_u32 s70, 10\n" \
  "s_nop 3\n" \
  "s_cbranch_scc1 .LV_nol_%=\n" \
  "v_max_u32 v217, v92, v93\n" \
  "v_max3_u32 v217, v217, v94, v95\n" \
  "v_max3_u32 v217, v217, v100, v101\n" \
  "v_max3_u32 v217, v217, v102, v103\n" \
  "v_max3_u32 v217, v217, v104, v105\n" \
  "v_max3_u32 v217, v217, v106, v107\n" \
  "v_max3_u32 v217, v217, v96, v97\n" \
  "v_max3_u32 v217, v217, v98, v99\n" \
  "v_max3_u32 v217, v217, v108, v109\n" \
  "v_max3_u32 v217, v217, v110, v111\n" \
  "v_max3_u32 v217, v217, v112, v113\n" \
  "v_max3_u32 v217, v217, v114, v115\n" \
  "v_max3_u32 v217, v217, v116, v117\n" \
  "v_max3_u32 v217, v217, v118, v119\n" \
  "v_max3_u32 v217, v217, v120, v121\n" \
  "v_max3_u32 v217, v217, v122, v123\n" \
  "v_max3_u32 v217, v217, v124, v125\n" \
  "v_max3_u32 v217, v217, v126, v127\n" \
  "v_cmp_eq_u32 vcc, -1, v217\n" \
  "s_cbranch_vccz .LV_back_%=\n" \
  ".LV_nol_%=:\n" \
  "s_add_u32 s70, s70, 2\n" \
  "s_cmp_gt_u32 s70, 65536\n" \
  "s_cbranch_scc1 .LV_dset_%=\n" \
  "s_barrier\n" \
  "v_mov_b64 v[60:61], 0\n" \
  "v_mov_b64 v[62:63], 0\n" \
  "v_mov_b64 v[64:65], 0\n" \
  "v_mov_b64 v[66:67], 0\n" \
  "v_mov_b64 v[68:69], 0\n" \
  "v_mov_b64 v[70:71], 0\n" \
  "v_mov_b64 v[72:73], 0\n" \
  "v_mov_b64 v[74:75], 0\n" \
  "v_mov_b64 v[76:77], 0\n" \
  "v_mov_b64 v[78:79], 0\n" \
  "v_mov_b64 v[80:81], 0\n" \
  "v_mov_b64 v[82:83], 0\n" \
  "v_mov_b64 v[84:85], 0\n" \
  "v_mov_b64 v[86:87], 0\n" \
  "s_branch .LV_rt_%=\n" \
  ".LV_dset_%=:\n" \
  "v_mov_b32 v112, 1\n" \
  "v_mov_b32 v113, s55\n" \
  "buffer_store_dwordx2 v[112:113], off, s[40:43], %46 offset:128 sc1\n" \
  "s_mov_b32 s70, 1\n" \
  "v_mov_b64 v[60:61], 0\n" \
  "v_mov_b64 v[62:63], 0\n" \
  "v_mov_b64 v[64:65], 0\n" \
  "v_mov_b64 v[66:67], 0\n" \
  "v_mov_b64 v[68:69], 0\n" \
  "v_mov_b64 v[70:71], 0\n" \
  "v_mov_b64 v[72:73], 0\n" \
  "v_mov_b64 v[74:75], 0\n" \
  "v_mov_b64 v[76:77], 0\n" \
  "v_mov_b64 v[78:79], 0\n" \
  "v_mov_b64 v[80:81], 0\n" \
  "v_mov_b64 v[82:83], 0\n" \
  "v_mov_b64 v[84:85], 0\n" \
  "v_mov_b64 v[86:87], 0\n" \
  "s_waitcnt vmcnt(0) lgkmcnt(0)\n" \
  "s_barrier\n" \
  "s_branch .LV_p_%=\n" \
  ".LV_cp_%=:\n" \
  "s_sub_u32 s60, s48, 58\n" \
  "s_lshl_b32 s60, s60, 20\n" \
  "s_add_u32 s60, s60, s47\n" \
  "s_lshr_b32 s62, s60, 8\n" \
  "s_and_b32 s62, s62, 8192\n" \
  "s_xor_b32 s62, s62, s60\n" \
  "buffer_store_dwordx4 v[68:71], v253, s[40:43], s62 offen sc1\n" \
  "buffer_store_dwordx4 v[76:79], v254, s[40:43], s62 offen sc1\n" \
  "buffer_store_dwordx2 v[84:85], v255, s[40:43], s62 offen sc1\n" \
  "s_nop 1\n" \
  "s_branch .LV_cp_b_%=\n" \
  ".LV_ef_%=:\n" \
  "s_cmp_eq_u32 s48, 64\n" \
  "s_cbranch_scc1 .LV_end_%=\n" \
  "s_waitcnt vmcnt(0)\n" \
  "s_barrier\n" \
  "buffer_store_dwordx2 v[56:57], off, s[40:43], %43 sc1\n" \
  "s_branch .LV_ef_b_%=\n" \
  ".LV_end_%=:\n" \
  "s_waitcnt vmcnt(0)\n" \
  "s_waitcnt vmcnt(0)\n" \
  "s_nop 7\n" \
  "s_endpgm\n"
#define V_ASM_VOPS "+{v242}"(ci.sidx), "+{v243}"(ci.l16), "+{v244}"(ci.viw), "+{v245}"(ci.fva), "+{v246}"(ci.pk3), "+{v247}"(ci.fvs), "+{v248}"(ci.sid2), "+{v249}"(ci.uo), "+{v250}"(ci.po0), "+{v251}"(ci.po1), "+{v252}"(ci.po2), "+{v253}"(ci.co0), "+{v254}"(ci.co1), "+{v255}"(ci.co2), "+{v200}"(ci.vq0), "+{v201}"(ci.vq1), "+{v202}"(ci.vq2), "+{v203}"(ci.vq3), "+{v204}"(ci.vo0), "+{v205}"(ci.vo1), "+{v206}"(ci.vo2), "+{v207}"(ci.vo3), "+{v208}"(ci.v50), "+{v209}"(ci.v51), "+{v210}"(ci.v52), "+{v211}"(ci.v53), "+{v212}"(ci.tro0), "+{v213}"(ci.tro1), "+{v214}"(ci.m0o), "+{v215}"(ci.tro2), "+{v216}"(ci.tro3), "+{v217}"(ci.tro4)
#define V_ASM_SOPS "s"(ci.ws), "s"(ci.Wa), "s"(ci.Wb), "s"(ci.mem0), "s"(ci.w), "s"(ci.o_a), "s"(ci.o_b), "s"(ci.o_nx), "s"(ci.o_exp), "s"(ci.lab), "s"(ci.f_p), "s"(ci.f_m), "s"(ci.f_dur), "s"(ci.f_go), "s"(ci.o_ctl), "s"(ci.o_dbg), "s"(ci.dret), "s"(ci.o_qx), "s"(ci.f_s), "s"(ci.f_r)
#define V_ASM_CLOB "v0", "v1", "v2", "v3", "v4", "v5", "v6", "v7", "v8", "v9", "v10", "v11", "v12", "v13", "v14", "v15", "v16", "v17", "v18", "v19", "v20", "v21", "v22", "v23", "v24", "v25", "v26", "v27", "v28", "v29", "v30", "v31", "v32", "v33", "v34", "v35", "v36", "v37", "v38", "v39", "v40", "v41", "v42", "v43", "v44", "v45", "v46", "v47", "v48", "v49", "v50", "v51", "v52", "v53", "v54", "v55", "v56", "v57", "v58", "v59", "v60", "v61", "v62", "v63", "v64", "v65", "v66", "v67", "v68", "v69", "v70", "v71", "v72", "v73", "v74", "v75", "v76", "v77", "v78", "v79", "v80", "v81", "v82", "v83", "v84", "v85", "v86", "v87", "v88", "v89", "v90", "v91", "v92", "v93", "v94", "v95", "v96", "v97", "v98", "v99", "v100", "v101", "v102", "v103", "v104", "v105", "v106", "v107", "v108", "v109", "v110", "v111", "v112", "v113", "v114", "v115", "v116", "v117", "v118", "v119", "v120", "v121", "v122", "v123", "v124", "v125", "v126", "v127", "v128", "v129", "v130", "v131", "v132", "v133", "v134", "v135", "v136", "v137", "v138", "v139", "v140", "v141", "v142", "v143", "v144", "v145", "v146", "v147", "v148", "v149", "v150", "v151", "v152", "v153", "v154", "v155", "v156", "v157", "v158", "v159", "v160", "v161", "v162", "v163", "v164", "v165", "v166", "v167", "v168", "v169", "v170", "v171", "v172", "v173", "v174", "v175", "v176", "v177", "v178", "v179", "v180", "v181", "v182", "v183", "v184", "v185", "v186", "v187", "v188", "v189", "v190", "v191", "v192", "v193", "v194", "v195", "v196", "v197", "v198", "v199", "a0", "a1", "a2", "a3", "a4", "a5", "a6", "a7", "a8", "a9", "a10", "a11", "a12", "a13", "a14", "a15", "a16", "a17", "a18", "a19", "a20", "a21", "a22", "a23", "a24", "a25", "a26", "a27", "a28", "a29", "a30", "a31", "a32", "a33", "a34", "a35", "a36", "a37", "a38", "a39", "a40", "a41", "a42", "a43", "a44", "a45", "a46", "a47", "a48", "a49", "a50", "a51", "a52", "a53", "a54", "a55", "a56", "a57", "a58", "a59", "a60", "a61", "a62", "a63", "a64", "a65", "a66", "a67", "a68", "a69", "a70", "a71", "a72", "a73", "a74", "a75", "a76", "a77", "a78", "a79", "a80", "a81", "a82", "a83", "a84", "a85", "a86", "a87", "a88", "a89", "a90", "a91", "a92", "a93", "a94", "a95", "a96", "a97", "a98", "a99", "a100", "a101", "a102", "a103", "a104", "a105", "a106", "a107", "a108", "a109", "a110", "a111", "a112", "a113", "a114", "a115", "a116", "a117", "a118", "a119", "a120", "a121", "a122", "a123", "a124", "a125", "a126", "a127", "a128", "a129", "a130", "a131", "a132", "a133", "a134", "a135", "a136", "a137", "a138", "a139", "a140", "a141", "a142", "a143", "a144", "a145", "a146", "a147", "a148", "a149", "a150", "a151", "a152", "a153", "a154", "a155", "a156", "a157", "a158", "a159", "a160", "a161", "a162", "a163", "a164", "a165", "a166", "a167", "a168", "a169", "a170", "a171", "a172", "a173", "a174", "a175", "a176", "a177", "a178", "a179", "a180", "a181", "a182", "a183", "a184", "a185", "a186", "a187", "a188", "a189", "a190", "a191", "a192", "a193", "a194", "a195", "a196", "a197", "a198", "a199", "a200", "a201", "a202", "a203", "a204", "a205", "a206", "a207", "a208", "a209", "a210", "a211", "a212", "a213", "a214", "a215", "a216", "a217", "a218", "a219", "a220", "a221", "a222", "a223", "a224", "a225", "a226", "a227", "a228", "a229", "a230", "a231", "a232", "a233", "a234", "a235", "a236", "a237", "a238", "a239", "a240", "a241", "a242", "a243", "a244", "a245", "a246", "a247", "a248", "a249", "a250", "a251", "a252", "a253", "a254", "a255", "s40", "s41", "s42", "s43", "s44", "s45", "s46", "s47", "s48", "s49", "s50", "s51", "s52", "s53", "s54", "s55", "s56", "s57", "s58", "s59", "s60", "s61", "s62", "s63", "s64", "s65", "s66", "s67", "s68", "s69", "s70", "s71", "s72", "s73", "s74", "s75", "s76", "s77", "s78", "s79", "s80", "s81", "s82", "s83", "s84", "s85", "s86", "s87", "s88", "s89", "s90", "s91", "s92", "s93", "s94", "s95", "s96", "s97", "s98", "s99", "vcc", "scc", "memory"
#define W_ASM_TEXT \
  "s_cmp_gt_u32 %36, 0\n" \
  "s_cbranch_scc1 .LW_other_%=\n" \
  "s_mov_b64 s[40:41], %32\n" \
  "s_and_b32 s41, s41, 0xffff\n" \
  "s_mov_b32 s42, 255852544\n" \
  "s_mov_b32 s43, 0x00020000\n" \
  "s_mov_b32 s46, 4718592\n" \
  "s_mov_b32 s47, 0x00020000\n" \
  "s_lshl_b32 s72, %36, 11\n" \
  "buffer_load_dword v196, off, s[40:43], %46 sc1\n" \
  "s_mov_b64 s[44:45], %34\n" \
  "s_and_b32 s45, s45, 0xffff\n" \
  "s_add_u32 s60, %38, 0\n" \
  "s_lshl_b32 s61, s72, 1\n" \
  "s_add_u32 m0, s61, 16384\n" \
  "s_nop 0\n" \
  "buffer_load_dwordx4 v204, s[44:47], s60 offen lds\n" \
  "buffer_load_dwordx4 v205, s[44:47], s60 offen offset:1024 lds\n" \
  "buffer_load_dwordx4 v206, s[44:47], s60 offen offset:2048 lds\n" \
  "buffer_load_dwordx4 v207, s[44:47], s60 offen offset:3072 lds\n" \
  "s_add_u32 s60, %38, 18432\n" \
  "s_lshl_b32 s61, s72, 1\n" \
  "s_add_u32 m0, s61, 32768\n" \
  "s_nop 0\n" \
  "buffer_load_dwordx4 v204, s[44:47], s60 offen lds\n" \
  "buffer_load_dwordx4 v205, s[44:47], s60 offen offset:1024 lds\n" \
  "buffer_load_dwordx4 v206, s[44:47], s60 offen offset:2048 lds\n" \
  "buffer_load_dwordx4 v207, s[44:47], s60 offen offset:3072 lds\n" \
  "s_add_u32 s60, %38, 256\n" \
  "s_lshl_b32 s61, s72, 1\n" \
  "s_add_u32 m0, s61, 49152\n" \
  "s_nop 0\n" \
  "buffer_load_dwordx4 v204, s[44:47], s60 offen lds\n" \
  "buffer_load_dwordx4 v205, s[44:47], s60 offen offset:1024 lds\n" \
  "buffer_load_dwordx4 v206, s[44:47], s60 offen offset:2048 lds\n" \
  "buffer_load_dwordx4 v207, s[44:47], s60 offen offset:3072 lds\n" \
  "s_add_u32 s60, %38, 18688\n" \
  "s_lshl_b32 s61, s72, 1\n" \
  "s_add_u32 m0, s61, 65536\n" \
  "s_nop 0\n" \
  "buffer_load_dwordx4 v204, s[44:47], s60 offen lds\n" \
  "buffer_load_dwordx4 v205, s[44:47], s60 offen offset:1024 lds\n" \
  "buffer_load_dwordx4 v206, s[44:47], s60 offen offset:2048 lds\n" \
  "buffer_load_dwordx4 v207, s[44:47], s60 offen offset:3072 lds\n" \
  "s_add_u32 s60, %38, 512\n" \
  "s_lshl_b32 s61, s72, 1\n" \
  "s_add_u32 m0, s61, 81920\n" \
  "s_nop 0\n" \
  "buffer_load_dwordx4 v204, s[44:47], s60 offen lds\n" \
  "buffer_load_dwordx4 v205, s[44:47], s60 offen offset:1024 lds\n" \
  "buffer_load_dwordx4 v206, s[44:47], s60 offen offset:2048 lds\n" \
  "buffer_load_dwordx4 v207, s[44:47], s60 offen offset:3072 lds\n" \
  "s_add_u32 s60, %38, 18944\n" \
  "s_lshl_b32 s61, s72, 1\n" \
  "s_add_u32 m0, s61, 98304\n" \
  "s_nop 0\n" \
  "buffer_load_dwordx4 v204, s[44:47], s60 offen lds\n" \
  "buffer_load_dwordx4 v205, s[44:47], s60 offen offset:1024 lds\n" \
  "buffer_load_dwordx4 v206, s[44:47], s60 offen offset:2048 lds\n" \
  "buffer_load_dwordx4 v207, s[44:47], s60 offen offset:3072 lds\n" \
  "s_add_u32 s60, %38, 768\n" \
  "s_lshl_b32 s61, s72, 1\n" \
  "s_add_u32 m0, s61, 114688\n" \
  "s_nop 0\n" \
  "buffer_load_dwordx4 v204, s[44:47], s60 offen lds\n" \
  "buffer_load_dwordx4 v205, s[44:47], s60 offen offset:1024 lds\n" \
  "buffer_load_dwordx4 v206, s[44:47], s60 offen offset:2048 lds\n" \
  "buffer_load_dwordx4 v207, s[44:47], s60 offen offset:3072 lds\n" \
  "s_add_u32 s60, %38, 19200\n" \
  "s_lshl_b32 s61, s72, 1\n" \
  "s_add_u32 m0, s61, 131072\n" \
  "s_nop 0\n" \
  "buffer_load_dwordx4 v204, s[44:47], s60 offen lds\n" \
  "buffer_load_dwordx4 v205, s[44:47], s60 offen offset:1024 lds\n" \
  "buffer_load_dwordx4 v206, s[44:47], s60 offen offset:2048 lds\n" \
  "buffer_load_dwordx4 v207, s[44:47], s60 offen offset:3072 lds\n" \
  "s_mov_b32 s60, %38\n" \
  "s_lshl_b32 s61, s72, 1\n" \
  "s_add_u32 m0, s61, 147456\n" \
  "s_nop 0\n" \
  "buffer_load_dwordx4 v208, s[44:47], s60 offen lds\n" \
  "buffer_load_dwordx4 v209, s[44:47], s60 offen offset:1024 lds\n" \
  "buffer_load_dwordx4 v210, s[44:47], s60 offen offset:2048 lds\n" \
  "buffer_load_dwordx4 v211, s[44:47], s60 offen offset:3072 lds\n" \
  "s_mov_b64 s[44:45], %35\n" \
  "s_and_b32 s45, s45, 0xffff\n" \
  "s_lshr_b32 s60, s72, 3\n" \
  "s_mov_b32 m0, s72\n" \
  "s_nop 0\n" \
  "buffer_load_dwordx4 v214, s[44:47], s60 offen lds\n" \
  "s_add_u32 s60, s60, 128\n" \
  "s_add_u32 m0, s72, 1024\n" \
  "s_nop 0\n" \
  "buffer_load_dwordx4 v214, s[44:47], s60 offen lds\n" \
  "s_movk_i32 s60, 1024\n" \
  "s_movk_i32 m0, 8192\n" \
  "s_nop 0\n" \
  "buffer_load_dwordx4 v214, s[44:47], s60 offen lds\n" \
  "s_waitcnt vmcnt(27)\n" \
  "s_barrier\n" \
  "v_add_u32 v194, 16384, v215\n" \
  "v_xor_b32 v195, 2176, v194\n" \
  "ds_read_b64_tr_b16 a[144:145], v194\n" \
  "ds_read_b64_tr_b16 a[146:147], v194 offset:512\n" \
  "ds_read_b64_tr_b16 a[148:149], v195\n" \
  "ds_read_b64_tr_b16 a[150:151], v195 offset:512\n" \
  "ds_read_b64_tr_b16 a[152:153], v194 offset:8192\n" \
  "ds_read_b64_tr_b16 a[154:155], v194 offset:8704\n" \
  "ds_read_b64_tr_b16 a[156:157], v195 offset:8192\n" \
  "ds_read_b64_tr_b16 a[158:159], v195 offset:8704\n" \
  "v_add_u32 v194, 32768, v215\n" \
  "v_xor_b32 v195, 2176, v194\n" \
  "ds_read_b64_tr_b16 a[160:161], v194\n" \
  "ds_read_b64_tr_b16 a[162:163], v194 offset:512\n" \
  "ds_read_b64_tr_b16 a[164:165], v195\n" \
  "ds_read_b64_tr_b16 a[166:167], v195 offset:512\n" \
  "ds_read_b64_tr_b16 a[168:169], v194 offset:8192\n" \
  "ds_read_b64_tr_b16 a[170:171], v194 offset:8704\n" \
  "ds_read_b64_tr_b16 a[172:173], v195 offset:8192\n" \
  "ds_read_b64_tr_b16 a[174:175], v195 offset:8704\n" \
  "v_add_u32 v194, 49152, v215\n" \
  "v_xor_b32 v195, 2176, v194\n" \
  "ds_read_b64_tr_b16 a[176:177], v194\n" \
  "ds_read_b64_tr_b16 a[178:179], v194 offset:512\n" \
  "ds_read_b64_tr_b16 a[180:181], v195\n" \
  "ds_read_b64_tr_b16 a[182:183], v195 offset:512\n" \
  "ds_read_b64_tr_b16 a[184:185], v194 offset:8192\n" \
  "ds_read_b64_tr_b16 a[186:187], v194 offset:8704\n" \
  "ds_read_b64_tr_b16 a[188:189], v195 offset:8192\n" \
  "ds_read_b64_tr_b16 a[190:191], v195 offset:8704\n" \
  "s_waitcnt lgkmcnt(0)\n" \
  "s_barrier\n" \
  "s_mov_b64 s[44:45], %33\n" \
  "s_and_b32 s45, s45, 0xffff\n" \
  "s_add_u32 s60, %37, 0\n" \
  "s_lshl_b32 s61, s72, 1\n" \
  "s_add_u32 m0, s61, 16384\n" \
  "s_nop 0\n" \
  "buffer_load_dwordx4 v200, s[44:47], s60 offen lds\n" \
  "buffer_load_dwordx4 v201, s[44:47], s60 offen offset:1024 lds\n" \
  "buffer_load_dwordx4 v202, s[44:47], s60 offen offset:2048 lds\n" \
  "buffer_load_dwordx4 v203, s[44:47], s60 offen offset:3072 lds\n" \
  "s_add_u32 s60, %37, 524288\n" \
  "s_lshl_b32 s61, s72, 1\n" \
  "s_add_u32 m0, s61, 32768\n" \
  "s_nop 0\n" \
  "buffer_load_dwordx4 v200, s[44:47], s60 offen lds\n" \
  "buffer_load_dwordx4 v201, s[44:47], s60 offen offset:1024 lds\n" \
  "buffer_load_dwordx4 v202, s[44:47], s60 offen offset:2048 lds\n" \
  "buffer_load_dwordx4 v203, s[44:47], s60 offen offset:3072 lds\n" \
  "s_add_u32 s60, %37, 1048576\n" \
  "s_lshl_b32 s61, s72, 1\n" \
  "s_add_u32 m0, s61, 49152\n" \
  "s_nop 0\n" \
  "buffer_load_dwordx4 v200, s[44:47], s60 offen lds\n" \
  "buffer_load_dwordx4 v201, s[44:47], s60 offen offset:1024 lds\n" \
  "buffer_load_dwordx4 v202, s[44:47], s60 offen offset:2048 lds\n" \
  "buffer_load_dwordx4 v203, s[44:47], s60 offen offset:3072 lds\n" \
  "v_readfirstlane_b32 s55, v196\n" \
  "s_add_u32 s55, s55, 1\n" \
  "s_waitcnt vmcnt(27)\n" \
  "s_barrier\n" \
  "v_add_u32 v194, 65536, v215\n" \
  "v_xor_b32 v195, 2176, v194\n" \
  "ds_read_b64_tr_b16 a[192:193], v194\n" \
  "ds_read_b64_tr_b16 a[194:195], v194 offset:512\n" \
  "ds_read_b64_tr_b16 a[196:197], v195\n" \
  "ds_read_b64_tr_b16 a[198:199], v195 offset:512\n" \
  "ds_read_b64_tr_b16 a[200:201], v194 offset:8192\n" \
  "ds_read_b64_tr_b16 a[202:203], v194 offset:8704\n" \
  "ds_read_b64_tr_b16 a[204:205], v195 offset:8192\n" \
  "ds_read_b64_tr_b16 a[206:207], v195 offset:8704\n" \
  "v_add_u32 v194, 81920, v215\n" \
  "v_xor_b32 v195, 2176, v194\n" \
  "ds_read_b64_tr_b16 a[208:209], v194\n" \
  "ds_read_b64_tr_b16 a[210:211], v194 offset:512\n" \
  "ds_read_b64_tr_b16 a[212:213], v195\n" \
  "ds_read_b64_tr_b16 a[214:215], v195 offset:512\n" \
  "ds_read_b64_tr_b16 a[216:217], v194 offset:8192\n" \
  "ds_read_b64_tr_b16 a[218:219], v194 offset:8704\n" \
  "ds_read_b64_tr_b16 a[220:221], v195 offset:8192\n" \
  "ds_read_b64_tr_b16 a[222:223], v195 offset:8704\n" \
  "v_add_u32 v194, 98304, v215\n" \
  "v_xor_b32 v195, 2176, v194\n" \
  "ds_read_b64_tr_b16 a[224:225], v194\n" \
  "ds_read_b64_tr_b16 a[226:227], v194 offset:512\n" \
  "ds_read_b64_tr_b16 a[228:229], v195\n" \
  "ds_read_b64_tr_b16 a[230:231], v195 offset:512\n" \
  "ds_read_b64_tr_b16 a[232:233], v194 offset:8192\n" \
  "ds_read_b64_tr_b16 a[234:235], v194 offset:8704\n" \
  "ds_read_b64_tr_b16 a[236:237], v195 offset:8192\n" \
  "ds_read_b64_tr_b16 a[238:239], v195 offset:8704\n" \
  "s_waitcnt lgkmcnt(0)\n" \
  "s_barrier\n" \
  "s_add_u32 s60, %37, 1572864\n" \
  "s_lshl_b32 s61, s72, 1\n" \
  "s_add_u32 m0, s61, 65536\n" \
  "s_nop 0\n" \
  "buffer_load_dwordx4 v200, s[44:47], s60 offen lds\n" \
  "buffer_load_dwordx4 v201, s[44:47], s60 offen offset:1024 lds\n" \
  "buffer_load_dwordx4 v202, s[44:47], s60 offen offset:2048 lds\n" \
  "buffer_load_dwordx4 v203, s[44:47], s60 offen offset:3072 lds\n" \
  "s_add_u32 s60, %37, 2097152\n" \
  "s_lshl_b32 s61, s72, 1\n" \
  "s_add_u32 m0, s61, 81920\n" \
  "s_nop 0\n" \
  "buffer_load_dwordx4 v200, s[44:47], s60 offen lds\n" \
  "buffer_load_dwordx4 v201, s[44:47], s60 offen offset:1024 lds\n" \
  "buffer_load_dwordx4 v202, s[44:47], s60 offen offset:2048 lds\n" \
  "buffer_load_dwordx4 v203, s[44:47], s60 offen offset:3072 lds\n" \
  "s_add_u32 s60, %37, 2621440\n" \
  "s_lshl_b32 s61, s72, 1\n" \
  "s_add_u32 m0, s61, 98304\n" \
  "s_nop 0\n" \
  "buffer_load_dwordx4 v200, s[44:47], s60 offen lds\n" \
  "buffer_load_dwordx4 v201, s[44:47], s60 offen offset:1024 lds\n" \
  "buffer_load_dwordx4 v202, s[44:47], s60 offen offset:2048 lds\n" \
  "buffer_load_dwordx4 v203, s[44:47], s60 offen offset:3072 lds\n" \
  "s_waitcnt vmcnt(27)\n" \
  "s_barrier\n" \
  "s_waitcnt lgkmcnt(0)\n" \
  "s_barrier\n" \
  "s_add_u32 s60, %37, 3145728\n" \
  "s_lshl_b32 s61, s72, 1\n" \
  "s_add_u32 m0, s61, 114688\n" \
  "s_nop 0\n" \
  "buffer_load_dwordx4 v200, s[44:47], s60 offen lds\n" \
  "buffer_load_dwordx4 v201, s[44:47], s60 offen offset:1024 lds\n" \
  "buffer_load_dwordx4 v202, s[44:47], s60 offen offset:2048 lds\n" \
  "buffer_load_dwordx4 v203, s[44:47], s60 offen offset:3072 lds\n" \
  "s_add_u32 s60, %37, 3670016\n" \
  "s_lshl_b32 s61, s72, 1\n" \
  "s_add_u32 m0, s61, 131072\n" \
  "s_nop 0\n" \
  "buffer_load_dwordx4 v200, s[44:47], s60 offen lds\n" \
  "buffer_load_dwordx4 v201, s[44:47], s60 offen offset:1024 lds\n" \
  "buffer_load_dwordx4 v202, s[44:47], s60 offen offset:2048 lds\n" \
  "buffer_load_dwordx4 v203, s[44:47], s60 offen offset:3072 lds\n" \
  "s_add_u32 s60, %37, 4194304\n" \
  "s_lshl_b32 s61, s72, 1\n" \
  "s_add_u32 m0, s61, 147456\n" \
  "s_nop 0\n" \
  "buffer_load_dwordx4 v200, s[44:47], s60 offen lds\n" \
  "buffer_load_dwordx4 v201, s[44:47], s60 offen offset:1024 lds\n" \
  "buffer_load_dwordx4 v202, s[44:47], s60 offen offset:2048 lds\n" \
  "buffer_load_dwordx4 v203, s[44:47], s60 offen offset:3072 lds\n" \
  "buffer_load_dwordx2 v[198:199], off, s[40:43], %45 sc1\n" \
  "s_waitcnt vmcnt(25)\n" \
  "s_barrier\n" \
  "v_add_u32 v194, 16384, v212\n" \
  "ds_read_b64_tr_b16 a[0:1], v194\n" \
  "ds_read_b64_tr_b16 a[2:3], v194 offset:512\n" \
  "ds_read_b64_tr_b16 a[4:5], v194 offset:8192\n" \
  "ds_read_b64_tr_b16 a[6:7], v194 offset:8704\n" \
  "v_add_u32 v195, 16384, v213\n" \
  "ds_read_b64_tr_b16 a[72:73], v195\n" \
  "ds_read_b64_tr_b16 a[74:75], v195 offset:512\n" \
  "ds_read_b64_tr_b16 a[76:77], v195 offset:8192\n" \
  "ds_read_b64_tr_b16 a[78:79], v195 offset:8704\n" \
  "v_add_u32 v194, 32768, v212\n" \
  "ds_read_b64_tr_b16 a[8:9], v194\n" \
  "ds_read_b64_tr_b16 a[10:11], v194 offset:512\n" \
  "ds_read_b64_tr_b16 a[12:13], v194 offset:8192\n" \
  "ds_read_b64_tr_b16 a[14:15], v194 offset:8704\n" \
  "v_add_u32 v195, 32768, v213\n" \
  "ds_read_b64_tr_b16 a[80:81], v195\n" \
  "ds_read_b64_tr_b16 a[82:83], v195 offset:512\n" \
  "ds_read_b64_tr_b16 a[84:85], v195 offset:8192\n" \
  "ds_read_b64_tr_b16 a[86:87], v195 offset:8704\n" \
  "v_add_u32 v194, 49152, v212\n" \
  "ds_read_b64_tr_b16 a[16:17], v194\n" \
  "ds_read_b64_tr_b16 a[18:19], v194 offset:512\n" \
  "ds_read_b64_tr_b16 a[20:21], v194 offset:8192\n" \
  "ds_read_b64_tr_b16 a[22:23], v194 offset:8704\n" \
  "v_add_u32 v195, 49152, v213\n" \
  "ds_read_b64_tr_b16 a[88:89], v195\n" \
  "ds_read_b64_tr_b16 a[90:91], v195 offset:512\n" \
  "ds_read_b64_tr_b16 a[92:93], v195 offset:8192\n" \
  "ds_read_b64_tr_b16 a[94:95], v195 offset:8704\n" \
  "s_waitcnt lgkmcnt(0)\n" \
  "s_waitcnt vmcnt(13)\n" \
  "s_barrier\n" \
  "v_add_u32 v194, 65536, v212\n" \
  "ds_read_b64_tr_b16 a[24:25], v194\n" \
  "ds_read_b64_tr_b16 a[26:27], v194 offset:512\n" \
  "ds_read_b64_tr_b16 a[28:29], v194 offset:8192\n" \
  "ds_read_b64_tr_b16 a[30:31], v194 offset:8704\n" \
  "v_add_u32 v195, 65536, v213\n" \
  "ds_read_b64_tr_b16 a[96:97], v195\n" \
  "ds_read_b64_tr_b16 a[98:99], v195 offset:512\n" \
  "ds_read_b64_tr_b16 a[100:101], v195 offset:8192\n" \
  "ds_read_b64_tr_b16 a[102:103], v195 offset:8704\n" \
  "v_add_u32 v194, 81920, v212\n" \
  "ds_read_b64_tr_b16 a[32:33], v194\n" \
  "ds_read_b64_tr_b16 a[34:35], v194 offset:512\n" \
  "ds_read_b64_tr_b16 a[36:37], v194 offset:8192\n" \
  "ds_read_b64_tr_b16 a[38:39], v194 offset:8704\n" \
  "v_add_u32 v195, 81920, v213\n" \
  "ds_read_b64_tr_b16 a[104:105], v195\n" \
  "ds_read_b64_tr_b16 a[106:107], v195 offset:512\n" \
  "ds_read_b64_tr_b16 a[108:109], v195 offset:8192\n" \
  "ds_read_b64_tr_b16 a[110:111], v195 offset:8704\n" \
  "v_add_u32 v194, 98304, v212\n" \
  "ds_read_b64_tr_b16 a[40:41], v194\n" \
  "ds_read_b64_tr_b16 a[42:43], v194 offset:512\n" \
  "ds_read_b64_tr_b16 a[44:45], v194 offset:8192\n" \
  "ds_read_b64_tr_b16 a[46:47], v194 offset:8704\n" \
  "v_add_u32 v195, 98304, v213\n" \
  "ds_read_b64_tr_b16 a[112:113], v195\n" \
  "ds_read_b64_tr_b16 a[114:115], v195 offset:512\n" \
  "ds_read_b64_tr_b16 a[116:117], v195 offset:8192\n" \
  "ds_read_b64_tr_b16 a[118:119], v195 offset:8704\n" \
  "s_waitcnt lgkmcnt(0)\n" \
  "s_waitcnt vmcnt(1)\n" \
  "s_barrier\n" \
  "v_add_u32 v194, 114688, v212\n" \
  "ds_read_b64_tr_b16 a[48:49], v194\n" \
  "ds_read_b64_tr_b16 a[50:51], v194 offset:512\n" \
  "ds_read_b64_tr_b16 a[52:53], v194 offset:8192\n" \
  "ds_read_b64_tr_b16 a[54:55], v194 offset:8704\n" \
  "v_add_u32 v195, 114688, v213\n" \
  "ds_read_b64_tr_b16 a[120:121], v195\n" \
  "ds_read_b64_tr_b16 a[122:123], v195 offset:512\n" \
  "ds_read_b64_tr_b16 a[124:125], v195 offset:8192\n" \
  "ds_read_b64_tr_b16 a[126:127], v195 offset:8704\n" \
  "v_add_u32 v194, 131072, v212\n" \
  "ds_read_b64_tr_b16 a[56:57], v194\n" \
  "ds_read_b64_tr_b16 a[58:59], v194 offset:512\n" \
  "ds_read_b64_tr_b16 a[60:61], v194 offset:8192\n" \
  "ds_read_b64_tr_b16 a[62:63], v194 offset:8704\n" \
  "v_add_u32 v195, 131072, v213\n" \
  "ds_read_b64_tr_b16 a[128:129], v195\n" \
  "ds_read_b64_tr_b16 a[130:131], v195 offset:512\n" \
  "ds_read_b64_tr_b16 a[132:133], v195 offset:8192\n" \
  "ds_read_b64_tr_b16 a[134:135], v195 offset:8704\n" \
  "v_add_u32 v194, 147456, v212\n" \
  "ds_read_b64_tr_b16 a[64:65], v194\n" \
  "ds_read_b64_tr_b16 a[66:67], v194 offset:512\n" \
  "ds_read_b64_tr_b16 a[68:69], v194 offset:8192\n" \
  "ds_read_b64_tr_b16 a[70:71], v194 offset:8704\n" \
  "v_add_u32 v195, 147456, v213\n" \
  "ds_read_b64_tr_b16 a[136:137], v195\n" \
  "ds_read_b64_tr_b16 a[138:139], v195 offset:512\n" \
  "ds_read_b64_tr_b16 a[140:141], v195 offset:8192\n" \
  "ds_read_b64_tr_b16 a[142:143], v195 offset:8704\n" \
  "s_waitcnt lgkmcnt(0)\n" \
  "s_waitcnt vmcnt(0)\n" \
  "v_readfirstlane_b32 s96, v198\n" \
  "v_readfirstlane_b32 s63, v199\n" \
  "s_cmp_eq_u32 s63, s55\n" \
  "s_cselect_b32 s96, s96, 0\n" \
  "s_cmp_ge_u32 s96, 0xc0000000\n" \
  "s_cbranch_scc1 .LW_go_x_%=\n" \
  "s_mov_b32 s69, 0\n" \
  ".LW_go_%=:\n" \
  "buffer_load_dwordx2 v[196:197], off, s[40:43], %45 sc1\n" \
  "s_waitcnt vmcnt(0)\n" \
  "v_readfirstlane_b32 s96, v196\n" \
  "v_readfirstlane_b32 s63, v197\n" \
  "s_cmp_eq_u32 s63, s55\n" \
  "s_cselect_b32 s96, s96, 0\n" \
  "s_cmp_ge_u32 s96, 0xc0000000\n" \
  "s_cbranch_scc1 .LW_go_x_%=\n" \
  "s_sleep 8\n" \
  "s_add_u32 s69, s69, 1\n" \
  "s_and_b32 s63, s69, 1023\n" \
  "s_cbranch_scc1 .LW_go_%=\n" \
  "s_cmp_gt_u32 s69, 2097152\n" \
  "s_cbranch_scc1 .LW_go_set_%=\n" \
  "buffer_load_dwordx2 v[196:197], off, s[40:43], %46 offset:128 sc1\n" \
  "s_waitcnt vmcnt(0)\n" \
  "v_readfirstlane_b32 s63, v197\n" \
  "s_cmp_eq_u32 s63, s55\n" \
  "s_cbranch_scc0 .LW_go_%=\n" \
  "s_branch .LW_go_x_%=\n" \
  ".LW_go_set_%=:\n" \
  "v_mov_b32 v196, 1\n" \
  "v_mov_b32 v197, s55\n" \
  "buffer_store_dwordx2 v[196:197], off, s[40:43], %46 offset:128 sc1\n" \
  "s_waitcnt vmcnt(0)\n" \
  ".LW_go_x_%=:\n" \
  "s_and_b32 s60, s96, 2\n" \
  "s_lshl_b32 s60, s60, 12\n" \
  "s_xor_b32 s74, %42, s60\n" \
  "s_xor_b32 s75, %44, s60\n" \
  "s_bfe_u32 s56, s96, 0x30010\n" \
  "s_lshl_b32 s56, s56, 12\n" \
  "s_or_b32 s56, s56, 0x200000\n" \
  "s_mov_b32 s57, 1052672\n" \
  "s_lshl_b32 s61, %41, 2\n" \
  "s_lshr_b32 s51, s96, s61\n" \
  "s_and_b32 s51, s51, 7\n" \
  "s_lshl_b32 s51, s51, 12\n" \
  "s_xor_b32 s51, s51, 4210688\n" \
  "s_mov_b32 s71, 0\n" \
  "s_add_u32 s45, s51, 886784\n" \
  "s_mov_b32 s46, -1\n" \
  "v_mov_b64 v[48:49], -1\n" \
  "v_mov_b64 v[50:51], -1\n" \
  "v_and_b32 v194, 3, v250\n" \
  "v_lshlrev_b32 v194, 2, v194\n" \
  "v_lshrrev_b32 v194, v194, s96\n" \
  "v_and_b32 v194, 7, v194\n" \
  "v_lshlrev_b32 v194, 12, v194\n" \
  "v_and_b32 v250, -4, v250\n" \
  "v_xor_b32 v250, v250, v194\n" \
  "v_and_b32 v194, 3, v252\n" \
  "v_lshlrev_b32 v194, 2, v194\n" \
  "v_lshrrev_b32 v194, v194, s96\n" \
  "v_and_b32 v194, 7, v194\n" \
  "v_lshlrev_b32 v194, 12, v194\n" \
  "v_and_b32 v252, -4, v252\n" \
  "v_xor_b32 v252, v252, v194\n" \
  "v_and_b32 v194, 3, v251\n" \
  "v_lshlrev_b32 v194, 2, v194\n" \
  "v_lshrrev_b32 v194, v194, s96\n" \
  "v_and_b32 v194, 7, v194\n" \
  "v_lshlrev_b32 v194, 12, v194\n" \
  "v_and_b32 v251, -4, v251\n" \
  "v_xor_b32 v251, v251, v194\n" \
  "v_xor_b32 v185, s56, v250\n" \
  "buffer_store_dwordx4 v[48:51], v185, s[40:43], 0 offen sc1\n" \
  "v_xor_b32 v186, s56, v252\n" \
  "buffer_store_dwordx2 v[48:49], v186, s[40:43], 0 offen sc1\n" \
  "s_xor_b32 s62, s56, s57\n" \
  "s_nop 1\n" \
  "v_xor_b32 v185, s62, v250\n" \
  "buffer_store_dwordx4 v[48:51], v185, s[40:43], 0 offen sc1\n" \
  "v_xor_b32 v186, s62, v252\n" \
  "buffer_store_dwordx2 v[48:49], v186, s[40:43], 0 offen sc1\n" \
  "s_andn2_b32 s60, 2, s96\n" \
  "s_lshl_b32 s60, s60, 12\n" \
  "s_xor_b32 s60, s60, 8192\n" \
  "s_xor_b32 s58, %39, s60\n" \
  "s_mov_b32 s49, 0\n" \
  "s_add_u32 s47, %49, s60\n" \
  "s_add_u32 s97, s60, 8388608\n" \
  "s_xor_b32 s50, %40, s60\n" \
  "s_mov_b32 s48, 0\n" \
  "s_mov_b32 s54, 0\n" \
  "s_cmp_eq_u32 %36, 0\n" \
  "s_cselect_b64 s[88:89], -1, 0\n" \
  "s_mov_b32 s68, 0\n" \
  "s_mov_b32 s70, 1\n" \
  "s_mov_b32 s73, 0\n" \
  "s_mov_b32 s44, 0\n" \
  "s_mov_b32 s53, 0\n" \
  "s_memrealtime s[86:87]\n" \
  "s_memtime s[76:77]\n" \
  "s_waitcnt lgkmcnt(0)\n" \
  "s_mov_b32 s90, s76\n" \
  "s_mov_b32 s87, 340\n" \
  "s_mov_b32 s64, 0x3e0293ee\n" \
  "s_mov_b32 s65, 0xbe0293ee\n" \
  "v_mov_b64 v[60:61], 0\n" \
  "v_mov_b64 v[62:63], 0\n" \
  "v_mov_b64 v[64:65], 0\n" \
  "v_mov_b64 v[66:67], 0\n" \
  "v_mov_b64 v[68:69], 0\n" \
  "v_mov_b64 v[70:71], 0\n" \
  "v_mov_b64 v[72:73], 0\n" \
  "v_mov_b64 v[74:75], 0\n" \
  "v_mov_b64 v[76:77], 0\n" \
  "v_mov_b64 v[78:79], 0\n" \
  "v_mov_b32 v56, 0\n" \
  "v_mov_b32 v57, s55\n" \
  "v_mov_b64 v[58:59], v[56:57]\n" \
  "v_mov_b64 v[82:83], 0\n" \
  "s_mov_b32 s67, 2130706432\n" \
  "s_waitcnt vmcnt(0) lgkmcnt(0)\n" \
  "s_barrier\n" \
  "s_branch .LW_p_%=\n" \
  "s_nop 7\n" \
  ".p2align 6\n" \
  ".fill (((40 - (.LW_p_%= - .LW_rot_%=)) & 63) >> 2), 4, 0xBF800000\n" \
  ".LW_rot_%=:\n" \
  "s_xor_b32 s51, s51, s56\n" \
  "s_xor_b32 s71, s71, s56\n" \
  "v_xor_b32 v250, s56, v250\n" \
  "v_xor_b32 v252, s56, v252\n" \
  "s_mov_b32 s60, s56\n" \
  "s_mov_b32 s56, s57\n" \
  "s_mov_b32 s57, s60\n" \
  "s_add_u32 s59, s51, 819200\n" \
  "s_mov_b32 m0, 0\n" \
  "s_mov_b32 s70, 0\n" \
  "s_mov_b32 s68, 0\n" \
  "s_waitcnt lgkmcnt(0)\n" \
  "s_mov_b32 s69, 0\n" \
  ".LW_tw_%=:\n" \
  "v_readfirstlane_b32 s63, v52\n" \
  "s_cmp_lg_u32 s63, s48\n" \
  "s_cbranch_scc1 .LW_tw_re_%=\n" \
  "v_readfirstlane_b32 s53, v54\n" \
  ".LW_rt_%=:\n" \
  "s_memrealtime s[60:61]\n" \
  "s_add_u32 s73, s53, s44\n" \
  "s_waitcnt lgkmcnt(0)\n" \
  "s_sub_u32 s62, s73, s60\n" \
  "s_cmp_ge_u32 s62, 2000\n" \
  "s_cbranch_scc1 .LW_lat_%=\n" \
  "s_sub_u32 s62, s62, 30\n" \
  ".LW_slp_%=:\n" \
  "s_cmp_le_i32 s62, 0\n" \
  "s_cbranch_scc1 .LW_clk_%=\n" \
  "s_sleep 1\n" \
  "s_sub_u32 s62, s62, 3\n" \
  "s_branch .LW_slp_%=\n" \
  ".LW_clk_%=:\n" \
  "s_memrealtime s[60:61]\n" \
  "s_waitcnt lgkmcnt(0)\n" \
  "s_sub_u32 s62, s73, s60\n" \
  "s_cmp_gt_i32 s62, 21\n" \
  "s_cbranch_scc1 .LW_clk_%=\n" \
  "s_cmp_le_i32 s62, 11\n" \
  "s_cbranch_scc1 .LW_clk2_%=\n" \
  "s_memrealtime s[76:77]\n" \
  "s_nop 4\n" \
  "s_memrealtime s[78:79]\n" \
  "s_nop 4\n" \
  "s_memrealtime s[80:81]\n" \
  "s_nop 4\n" \
  "s_memrealtime s[82:83]\n" \
  "s_waitcnt lgkmcnt(0)\n" \
  "s_mov_b32 s77, 1\n" \
  "s_cmp_eq_u32 s78, s76\n" \
  "s_addc_u32 s77, s77, 0\n" \
  "s_cmp_eq_u32 s80, s76\n" \
  "s_addc_u32 s77, s77, 0\n" \
  "s_cmp_eq_u32 s82, s76\n" \
  "s_addc_u32 s77, s77, 0\n" \
  "s_mul_i32 s77, s77, 24\n" \
  "s_lshr_b32 s78, s87, 2\n" \
  "s_sub_u32 s77, s78, s77\n" \
  "s_max_i32 s77, s77, 0\n" \
  "s_sub_u32 s76, s73, s76\n" \
  "s_mul_i32 s76, s76, s87\n" \
  "s_ashr_i32 s76, s76, 4\n" \
  "s_sub_u32 s76, s76, s77\n" \
  "s_sub_u32 s76, s76, 200\n" \
  "s_max_i32 s76, s76, 0\n" \
  "s_min_i32 s76, s76, 320\n" \
  "s_lshr_b32 s76, s76, 3\n" \
  "s_lshl_b32 s76, s76, 2\n" \
  "s_mov_b32 s68, 1\n" \
  "s_getpc_b64 s[78:79]\n" \
  "s_sub_u32 s76, 180, s76\n" \
  "s_add_u32 s78, s78, s76\n" \
  "s_addc_u32 s79, s79, 0\n" \
  "s_setpc_b64 s[78:79]\n" \
  "s_nop 1\n" \
  "s_nop 1\n" \
  "s_nop 1\n" \
  "s_nop 1\n" \
  "s_nop 1\n" \
  "s_nop 1\n" \
  "s_nop 1\n" \
  "s_nop 1\n" \
  "s_nop 1\n" \
  "s_nop 1\n" \
  "s_nop 1\n" \
  "s_nop 1\n" \
  "s_nop 1\n" \
  "s_nop 1\n" \
  "s_nop 1\n" \
  "s_nop 1\n" \
  "s_nop 1\n" \
  "s_nop 1\n" \
  "s_nop 1\n" \
  "s_nop 1\n" \
  "s_nop 1\n" \
  "s_nop 1\n" \
  "s_nop 1\n" \
  "s_nop 1\n" \
  "s_nop 1\n" \
  "s_nop 1\n" \
  "s_nop 1\n" \
  "s_nop 1\n" \
  "s_nop 1\n" \
  "s_nop 1\n" \
  "s_nop 1\n" \
  "s_nop 1\n" \
  "s_nop 1\n" \
  "s_nop 1\n" \
  "s_nop 1\n" \
  "s_nop 1\n" \
  "s_nop 1\n" \
  "s_nop 1\n" \
  "s_nop 1\n" \
  "s_nop 1\n" \
  ".LW_noclk_%=:\n" \
  "buffer_load_dwordx4 v243, s[40:43], s59 offen sc1 lds\n" \
  "buffer_load_dwordx4 v243, s[40:43], s59 offen offset:1024 sc1 lds\n" \
  "buffer_load_dwordx4 v243, s[40:43], s59 offen offset:2048 sc1 lds\n" \
  "buffer_load_dwordx4 v243, s[40:43], s59 offen offset:3072 sc1 lds\n" \
  "s_add_u32 s60, s59, 32768\n" \
  "s_movk_i32 m0, 4096\n" \
  "s_nop 0\n" \
  "buffer_load_dwordx4 v243, s[40:43], s60 offen sc1 lds\n" \
  "buffer_load_dwordx4 v243, s[40:43], s60 offen offset:1024 sc1 lds\n" \
  "buffer_load_dwordx4 v243, s[40:43], s60 offen offset:2048 sc1 lds\n" \
  "buffer_load_dwordx4 v243, s[40:43], s60 offen offset:3072 sc1 lds\n" \
  "s_add_u32 s60, s59, 65536\n" \
  "s_movk_i32 m0, 8192\n" \
  "s_nop 0\n" \
  "buffer_load_dwordx4 v243, s[40:43], s60 offen sc1 lds\n" \
  ".LW_g3_%=:\n" \
  "s_waitcnt vmcnt(5)\n" \
  "s_barrier\n" \
  ".LW_p_%=:\n" \
  "ds_read_b128 v[84:87], v243\n" \
  "ds_read_b128 v[88:91], v243 offset:1024\n" \
  "ds_read_b128 v[92:95], v243 offset:2048\n" \
  "ds_read_b128 v[96:99], v243 offset:3072\n" \
  "s_waitcnt lgkmcnt(3)\n" \
  "v_smfmac_f32_16x16x64_bf16 v[60:63], v[84:87], a[0:7], v242\n" \
  "s_waitcnt vmcnt(0)\n" \
  "s_barrier\n" \
  "ds_read_b128 v[100:103], v243 offset:5120\n" \
  "ds_read_b128 v[104:107], v243 offset:4096\n" \
  "s_waitcnt lgkmcnt(4)\n" \
  "v_smfmac_f32_16x16x64_bf16 v[60:63], v[88:91], a[8:15], v242\n" \
  "ds_read_b128 v[108:111], v243 offset:7168\n" \
  "s_waitcnt lgkmcnt(4)\n" \
  "v_smfmac_f32_16x16x64_bf16 v[60:63], v[92:95], a[16:23], v242\n" \
  "ds_read_b128 v[112:115], v243 offset:6144\n" \
  "v_smfmac_f32_16x16x64_bf16 v[64:67], v[84:87], a[72:79], v242\n" \
  "ds_read_b128 v[116:119], v243 offset:8192\n" \
  "s_waitcnt lgkmcnt(5)\n" \
  "v_smfmac_f32_16x16x64_bf16 v[60:63], v[96:99], a[24:31], v242\n" \
  "s_waitcnt lgkmcnt(3)\n" \
  "v_smfmac_f32_16x16x64_bf16 v[60:63], v[104:107], a[32:39], v242\n" \
  "v_smfmac_f32_16x16x64_bf16 v[60:63], v[100:103], a[40:47], v242\n" \
  "s_waitcnt lgkmcnt(1)\n" \
  "v_smfmac_f32_16x16x64_bf16 v[60:63], v[112:115], a[48:55], v242\n" \
  "v_smfmac_f32_16x16x64_bf16 v[60:63], v[108:111], a[56:63], v242\n" \
  "s_waitcnt lgkmcnt(0)\n" \
  "v_smfmac_f32_16x16x64_bf16 v[60:63], v[116:119], a[64:71], v242\n" \
  "s_nop 2\n" \
  "v_smfmac_f32_16x16x64_bf16 v[64:67], v[88:91], a[80:87], v242\n" \
  "s_nop 3\n" \
  "v_add_f32 v60, v60, v62\n" \
  "v_add_f32 v61, v61, v63\n" \
  "v_cmp_u_f32 vcc, v60, v61\n" \
  "v_cvt_pk_bf16_f32 v80, v60, v61\n" \
  "v_smfmac_f32_16x16x64_bf16 v[64:67], v[92:95], a[88:95], v242\n" \
  "s_cbranch_vccnz .LW_slow_%=\n" \
  ".LW_back_%=:\n" \
  "ds_write_b32 v244, v80\n" \
  "v_smfmac_f32_16x16x64_bf16 v[64:67], v[96:99], a[96:103], v242\n" \
  "v_smfmac_f32_16x16x64_bf16 v[64:67], v[104:107], a[104:111], v242\n" \
  "v_smfmac_f32_16x16x64_bf16 v[64:67], v[100:103], a[112:119], v242\n" \
  "v_smfmac_f32_16x16x64_bf16 v[64:67], v[112:115], a[120:127], v242\n" \
  "s_waitcnt lgkmcnt(0)\n" \
  "s_barrier\n" \
  "v_smfmac_f32_16x16x64_bf16 v[64:67], v[108:111], a[128:135], v242\n" \
  "ds_read_b64_tr_b16 v[86:87], v245 offset:1536\n" \
  "ds_read_b64_tr_b16 v[90:91], v245 offset:512\n" \
  "v_smfmac_f32_16x16x64_bf16 v[64:67], v[116:119], a[136:143], v242\n" \
  "ds_read_b64_tr_b16 v[88:89], v245\n" \
  "s_nop 6\n" \
  "v_add_f32 v65, v65, v67\n" \
  "v_add_f32 v64, v64, v66\n" \
  "v_cvt_pk_bf16_f32 v81, v64, v65\n" \
  "ds_write_b32 v244, v81 offset:256\n" \
  "s_waitcnt lgkmcnt(1)\n" \
  "v_smfmac_f32_16x16x64_bf16 v[76:79], v[88:91], a[208:215], v248\n" \
  "v_smfmac_f32_16x16x64_bf16 v[72:75], v[88:91], a[176:183], v248\n" \
  "ds_read_b64_tr_b16 v[84:85], v245 offset:1024\n" \
  "v_smfmac_f32_16x16x64_bf16 v[68:71], v[88:91], a[144:151], v248\n" \
  "s_waitcnt lgkmcnt(1)\n" \
  "s_barrier\n" \
  "ds_read_b64_tr_b16 v[88:89], v245 offset:256\n" \
  "ds_read_b64_tr_b16 v[90:91], v245 offset:768\n" \
  "ds_read_b64_tr_b16 v[94:95], v245 offset:1792\n" \
  "s_waitcnt lgkmcnt(3)\n" \
  "v_smfmac_f32_16x16x64_bf16 v[68:71], v[84:87], a[152:159], v248\n" \
  "ds_read_b64_tr_b16 v[92:93], v245 offset:1280\n" \
  "v_smfmac_f32_16x16x64_bf16 v[72:75], v[84:87], a[184:191], v248\n" \
  "v_smfmac_f32_16x16x64_bf16 v[76:79], v[84:87], a[216:223], v248\n" \
  "s_waitcnt lgkmcnt(2)\n" \
  "v_smfmac_f32_16x16x64_bf16 v[68:71], v[88:91], a[160:167], v248\n" \
  "s_waitcnt lgkmcnt(0)\n" \
  "v_smfmac_f32_16x16x64_bf16 v[68:71], v[92:95], a[168:175], v248\n" \
  "s_nop 2\n" \
  "v_smfmac_f32_16x16x64_bf16 v[72:75], v[88:91], a[192:199], v248\n" \
  "s_nop 0\n" \
  "v_smfmac_f32_16x16x64_bf16 v[72:75], v[92:95], a[200:207], v248\n" \
  "s_nop 2\n" \
  "v_smfmac_f32_16x16x64_bf16 v[76:79], v[88:91], a[224:231], v248\n" \
  "s_nop 0\n" \
  "v_smfmac_f32_16x16x64_bf16 v[76:79], v[92:95], a[232:239], v248\n" \
  "v_cvt_pk_bf16_f32 v68, v68, v69\n" \
  "v_cvt_pk_bf16_f32 v69, v70, v71\n" \
  "v_cvt_pk_bf16_f32 v71, v74, v75\n" \
  "v_cvt_pk_bf16_f32 v70, v72, v73\n" \
  "s_nop 1\n" \
  "v_permlane16_swap_b32 v68, v70\n" \
  "v_permlane16_swap_b32 v69, v71\n" \
  "v_cvt_pk_bf16_f32 v76, v76, v77\n" \
  "v_cvt_pk_bf16_f32 v77, v78, v79\n" \
  "buffer_store_dwordx4 v[68:71], v250, s[40:43], 0 offen sc1\n" \
  "buffer_store_dwordx2 v[76:77], v252, s[40:43], 0 offen sc1\n" \
  ".LW_loop_%=:\n" \
  "s_memrealtime s[98:99]\n" \
  "s_memtime s[84:85]\n" \
  "s_cmp_ge_u32 s48, 58\n" \
  "s_cbranch_scc1 .LW_cp_%=\n" \
  ".LW_cp_b_%=:\n" \
  "v_mov_b64 v[60:61], 0\n" \
  "v_mov_b64 v[62:63], 0\n" \
  "v_mov_b64 v[64:65], 0\n" \
  "v_mov_b64 v[66:67], 0\n" \
  "v_mov_b64 v[68:69], 0\n" \
  "v_mov_b64 v[70:71], 0\n" \
  "v_mov_b64 v[72:73], 0\n" \
  "v_mov_b64 v[74:75], 0\n" \
  "v_mov_b64 v[76:77], 0\n" \
  "v_mov_b64 v[78:79], 0\n" \
  "s_add_u32 s95, s48, 1\n" \
  "s_and_b32 s63, s95, 3\n" \
  "s_lshl_b32 s95, s95, 5\n" \
  "s_lshl_b32 s63, s63, 4\n" \
  "s_sub_u32 s95, s95, s63\n" \
  "s_add_u32 s95, s95, s45\n" \
  "buffer_load_dwordx4 v[52:55], off, s[40:43], s95 sc1\n" \
  "s_add_u32 s48, s48, 1\n" \
  "v_mov_b32 v56, s48\n" \
  "s_waitcnt lgkmcnt(0)\n" \
  "s_cmp_gt_u32 s48, 57\n" \
  "s_cbranch_scc1 .LW_ef_%=\n" \
  ".LW_ef_b_%=:\n" \
  "s_sub_u32 s76, s84, s90\n" \
  "s_sub_u32 s77, s98, s86\n" \
  "v_cvt_f32_u32 v216, s76\n" \
  "v_cvt_f32_u32 v217, s77\n" \
  "v_rcp_f32 v217, v217\n" \
  "v_mul_f32 v216, 16.0, v216\n" \
  "v_fma_f32 v216, v216, v217, 0.5\n" \
  "v_cvt_u32_f32 v216, v216\n" \
  "s_nop 0\n" \
  "v_readfirstlane_b32 s76, v216\n" \
  "s_max_u32 s76, s76, 160\n" \
  "s_min_u32 s76, s76, 400\n" \
  "s_add_u32 s77, s48, 3\n" \
  "s_and_b32 s77, s77, 3\n" \
  "s_cselect_b32 s87, s87, s76\n" \
  "s_cselect_b32 s86, s86, s98\n" \
  "s_cselect_b32 s90, s90, s84\n" \
  "s_waitcnt vmcnt(0)\n" \
  "s_branch .LW_rot_%=\n" \
  "s_nop 7\n" \
  ".LW_slow_%=:\n" \
  "s_bitcmp1_b32 s70, 0\n" \
  "s_cbranch_scc1 .LW_back_%=\n" \
  "s_waitcnt lgkmcnt(0)\n" \
  "s_cmp_lt_u32 s70, 10\n" \
  "s_nop 3\n" \
  "s_cbranch_scc1 .LW_nol_%=\n" \
  "v_max_u32 v217, v84, v85\n" \
  "v_max3_u32 v217, v217, v86, v87\n" \
  "v_max3_u32 v217, v217, v88, v89\n" \
  "v_max3_u32 v217, v217, v90, v91\n" \
  "v_max3_u32 v217, v217, v92, v93\n" \
  "v_max3_u32 v217, v217, v94, v95\n" \
  "v_max3_u32 v217, v217, v96, v97\n" \
  "v_max3_u32 v217, v217, v98, v99\n" \
  "v_max3_u32 v217, v217, v104, v105\n" \
  "v_max3_u32 v217, v217, v106, v107\n" \
  "v_max3_u32 v217, v217, v100, v101\n" \
  "v_max3_u32 v217, v217, v102, v103\n" \
  "v_max3_u32 v217, v217, v112, v113\n" \
  "v_max3_u32 v217, v217, v114, v115\n" \
  "v_max3_u32 v217, v217, v108, v109\n" \
  "v_max3_u32 v217, v217, v110, v111\n" \
  "v_max3_u32 v217, v217, v116, v117\n" \
  "v_max3_u32 v217, v217, v118, v119\n" \
  "v_cmp_eq_u32 vcc, -1, v217\n" \
  "s_cbranch_vccz .LW_back_%=\n" \
  ".LW_nol_%=:\n" \
  "s_add_u32 s70, s70, 2\n" \
  "s_cmp_gt_u32 s70, 65536\n" \
  "s_cbranch_scc1 .LW_dset_%=\n" \
  "s_barrier\n" \
  "v_mov_b64 v[60:61], 0\n" \
  "v_mov_b64 v[62:63], 0\n" \
  "v_mov_b64 v[64:65], 0\n" \
  "v_mov_b64 v[66:67], 0\n" \
  "v_mov_b64 v[68:69], 0\n" \
  "v_mov_b64 v[70:71], 0\n" \
  "v_mov_b64 v[72:73], 0\n" \
  "v_mov_b64 v[74:75], 0\n" \
  "v_mov_b64 v[76:77], 0\n" \
  "v_mov_b64 v[78:79], 0\n" \
  "s_and_b32 s62, %48, 0xffff\n" \
  "s_add_u32 s44, s44, s62\n" \
  "s_xor_b32 s59, s59, 0\n" \
  "s_mov_b32 s68, 0\n" \
  "s_mov_b32 m0, 0\n" \
  "s_branch .LW_rt_%=\n" \
  ".LW_dset_%=:\n" \
  "v_mov_b32 v100, 1\n" \
  "v_mov_b32 v101, s55\n" \
  "buffer_store_dwordx2 v[100:101], off, s[40:43], %46 offset:128 sc1\n" \
  "s_mov_b32 s70, 1\n" \
  "v_mov_b64 v[60:61], 0\n" \
  "v_mov_b64 v[62:63], 0\n" \
  "v_mov_b64 v[64:65], 0\n" \
  "v_mov_b64 v[66:67], 0\n" \
  "v_mov_b64 v[68:69], 0\n" \
  "v_mov_b64 v[70:71], 0\n" \
  "v_mov_b64 v[72:73], 0\n" \
  "v_mov_b64 v[74:75], 0\n" \
  "v_mov_b64 v[76:77], 0\n" \
  "v_mov_b64 v[78:79], 0\n" \
  "s_waitcnt vmcnt(0) lgkmcnt(0)\n" \
  "s_barrier\n" \
  "s_branch .LW_p_%=\n" \
  ".LW_cp_%=:\n" \
  "s_sub_u32 s60, s48, 58\n" \
  "s_lshl_b32 s60, s60, 20\n" \
  "s_add_u32 s60, s60, s47\n" \
  "s_lshr_b32 s62, s60, 8\n" \
  "s_and_b32 s62, s62, 8192\n" \
  "s_xor_b32 s62, s62, s60\n" \
  "buffer_store_dwordx4 v[68:71], v253, s[40:43], s62 offen sc1\n" \
  "buffer_store_dwordx2 v[76:77], v255, s[40:43], s62 offen sc1\n" \
  "s_nop 1\n" \
  "s_branch .LW_cp_b_%=\n" \
  ".LW_ef_%=:\n" \
  "s_cmp_eq_u32 s48, 64\n" \
  "s_cbranch_scc1 .LW_end_%=\n" \
  "s_waitcnt vmcnt(0)\n" \
  "s_barrier\n" \
  "s_branch .LW_ef_b_%=\n" \
  ".LW_tw_re_%=:\n" \
  "s_add_u32 s69, s69, 1\n" \
  "s_cmp_gt_u32 s69, 200\n" \
  "s_cbranch_scc1 .LW_noclk_%=\n" \
  "s_sleep 2\n" \
  "buffer_load_dwordx4 v[52:55], off, s[40:43], s95 sc1\n" \
  "s_waitcnt vmcnt(0)\n" \
  "s_branch .LW_tw_%=\n" \
  ".LW_lat_%=:\n" \
  "s_lshr_b32 s63, %48, 16\n" \
  "s_add_u32 s62, s62, s63\n" \
  "s_cmp_lt_u32 s62, 4294937296\n" \
  "s_cbranch_scc1 .LW_noclk_%=\n" \
  "s_and_b32 s63, %48, 0xffff\n" \
  ".LW_lat1_%=:\n" \
  "s_add_u32 s44, s44, s63\n" \
  "s_add_u32 s62, s62, s63\n" \
  "s_cbranch_scc0 .LW_lat1_%=\n" \
  "s_branch .LW_rt_%=\n" \
  ".LW_clk2_%=:\n" \
  "s_cmp_le_i32 s62, 3\n" \
  "s_cbranch_scc1 .LW_clk_done_%=\n" \
  "s_memrealtime s[60:61]\n" \
  "s_waitcnt lgkmcnt(0)\n" \
  "s_sub_u32 s62, s73, s60\n" \
  "s_branch .LW_clk2_%=\n" \
  ".LW_clk_done_%=:\n" \
  "s_branch .LW_noclk_%=\n" \
  ".LW_end_%=:\n" \
  "s_waitcnt vmcnt(0)\n" \
  "s_waitcnt vmcnt(0)\n" \
  "s_nop 7\n" \
  "s_endpgm\n" \
  ".LW_other_%=:\n"
#define W_ASM_VOPS "+{v242}"(ci.sidx), "+{v243}"(ci.l16), "+{v244}"(ci.viw), "+{v245}"(ci.fva), "+{v246}"(ci.pk3), "+{v247}"(ci.fvs), "+{v248}"(ci.sid2), "+{v249}"(ci.uo), "+{v250}"(ci.po0), "+{v251}"(ci.po1), "+{v252}"(ci.po2), "+{v253}"(ci.co0), "+{v254}"(ci.co1), "+{v255}"(ci.co2), "+{v200}"(ci.vq0), "+{v201}"(ci.vq1), "+{v202}"(ci.vq2), "+{v203}"(ci.vq3), "+{v204}"(ci.vo0), "+{v205}"(ci.vo1), "+{v206}"(ci.vo2), "+{v207}"(ci.vo3), "+{v208}"(ci.v50), "+{v209}"(ci.v51), "+{v210}"(ci.v52), "+{v211}"(ci.v53), "+{v212}"(ci.tro0), "+{v213}"(ci.tro1), "+{v214}"(ci.m0o), "+{v215}"(ci.tro2), "+{v216}"(ci.tro3), "+{v217}"(ci.tro4)
#define W_ASM_SOPS "s"(ci.ws), "s"(ci.Wa), "s"(ci.Wb), "s"(ci.mem0), "s"(ci.w), "s"(ci.o_a), "s"(ci.o_b), "s"(ci.o_nx), "s"(ci.o_exp), "s"(ci.lab), "s"(ci.f_p), "s"(ci.f_m), "s"(ci.f_dur), "s"(ci.f_go), "s"(ci.o_ctl), "s"(ci.o_dbg), "s"(ci.dret), "s"(ci.o_qx), "s"(ci.f_s), "s"(ci.f_r)
#define W_ASM_CLOB "v0", "v1", "v2", "v3", "v4", "v5", "v6", "v7", "v8", "v9", "v10", "v11", "v12", "v13", "v14", "v15", "v16", "v17", "v18", "v19", "v20", "v21", "v22", "v23", "v24", "v25", "v26", "v27", "v28", "v29", "v30", "v31", "v32", "v33", "v34", "v35", "v36", "v37", "v38", "v39", "v40", "v41", "v42", "v43", "v44", "v45", "v46", "v47", "v48", "v49", "v50", "v51", "v52", "v53", "v54", "v55", "v56", "v57", "v58", "v59", "v60", "v61", "v62", "v63", "v64", "v65", "v66", "v67", "v68", "v69", "v70", "v71", "v72", "v73", "v74", "v75", "v76", "v77", "v78", "v79", "v80", "v81", "v82", "v83", "v84", "v85", "v86", "v87", "v88", "v89", "v90", "v91", "v92", "v93", "v94", "v95", "v96", "v97", "v98", "v99", "v100", "v101", "v102", "v103", "v104", "v105", "v106", "v107", "v108", "v109", "v110", "v111", "v112", "v113", "v114", "v115", "v116", "v117", "v118", "v119", "v120", "v121", "v122", "v123", "v124", "v125", "v126", "v127", "v128", "v129", "v130", "v131", "v132", "v133", "v134", "v135", "v136", "v137", "v138", "v139", "v140", "v141", "v142", "v143", "v144", "v145", "v146", "v147", "v148", "v149", "v150", "v151", "v152", "v153", "v154", "v155", "v156", "v157", "v158", "v159", "v160", "v161", "v162", "v163", "v164", "v165", "v166", "v167", "v168", "v169", "v170", "v171", "v172", "v173", "v174", "v175", "v176", "v177", "v178", "v179", "v180", "v181", "v182", "v183", "v184", "v185", "v186", "v187", "v188", "v189", "v190", "v191", "v192", "v193", "v194", "v195", "v196", "v197", "v198", "v199", "a0", "a1", "a2", "a3", "a4", "a5", "a6", "a7", "a8", "a9", "a10", "a11", "a12", "a13", "a14", "a15", "a16", "a17", "a18", "a19", "a20", "a21", "a22", "a23", "a24", "a25", "a26", "a27", "a28", "a29", "a30", "a31", "a32", "a33", "a34", "a35", "a36", "a37", "a38", "a39", "a40", "a41", "a42", "a43", "a44", "a45", "a46", "a47", "a48", "a49", "a50", "a51", "a52", "a53", "a54", "a55", "a56", "a57", "a58", "a59", "a60", "a61", "a62", "a63", "a64", "a65", "a66", "a67", "a68", "a69", "a70", "a71", "a72", "a73", "a74", "a75", "a76", "a77", "a78", "a79", "a80", "a81", "a82", "a83", "a84", "a85", "a86", "a87", "a88", "a89", "a90", "a91", "a92", "a93", "a94", "a95", "a96", "a97", "a98", "a99", "a100", "a101", "a102", "a103", "a104", "a105", "a106", "a107", "a108", "a109", "a110", "a111", "a112", "a113", "a114", "a115", "a116", "a117", "a118", "a119", "a120", "a121", "a122", "a123", "a124", "a125", "a126", "a127", "a128", "a129", "a130", "a131", "a132", "a133", "a134", "a135", "a136", "a137", "a138", "a139", "a140", "a141", "a142", "a143", "a144", "a145", "a146", "a147", "a148", "a149", "a150", "a151", "a152", "a153", "a154", "a155", "a156", "a157", "a158", "a159", "a160", "a161", "a162", "a163", "a164", "a165", "a166", "a167", "a168", "a169", "a170", "a171", "a172", "a173", "a174", "a175", "a176", "a177", "a178", "a179", "a180", "a181", "a182", "a183", "a184", "a185", "a186", "a187", "a188", "a189", "a190", "a191", "a192", "a193", "a194", "a195", "a196", "a197", "a198", "a199", "a200", "a201", "a202", "a203", "a204", "a205", "a206", "a207", "a208", "a209", "a210", "a211", "a212", "a213", "a214", "a215", "a216", "a217", "a218", "a219", "a220", "a221", "a222", "a223", "a224", "a225", "a226", "a227", "a228", "a229", "a230", "a231", "a232", "a233", "a234", "a235", "a236", "a237", "a238", "a239", "a240", "a241", "a242", "a243", "a244", "a245", "a246", "a247", "a248", "a249", "a250", "a251", "a252", "a253", "a254", "a255", "s40", "s41", "s42", "s43", "s44", "s45", "s46", "s47", "s48", "s49", "s50", "s51", "s52", "s53", "s54", "s55", "s56", "s57", "s58", "s59", "s60", "s61", "s62", "s63", "s64", "s65", "s66", "s67", "s68", "s69", "s70", "s71", "s72", "s73", "s74", "s75", "s76", "s77", "s78", "s79", "s80", "s81", "s82", "s83", "s84", "s85", "s86", "s87", "s88", "s89", "s90", "s91", "s92", "s93", "s94", "s95", "s96", "s97", "s98", "s99", "vcc", "scc", "memory"
#define R_ASM_TEXT \
  "s_mov_b64 s[40:41], %18\n" \
  "s_and_b32 s41, s41, 0xffff\n" \
  "s_mov_b32 s42, 255852544\n" \
  "s_mov_b32 s43, 0x00020000\n" \
  "s_bfe_u32 s56, %20, 0x30010\n" \
  "s_lshl_b32 s56, s56, 12\n" \
  "s_or_b32 s56, s56, 0x200000\n" \
  "s_mov_b32 s57, 1052672\n" \
  "s_mov_b32 s48, 0\n" \
  "s_cmp_lg_u32 %19, 0\n" \
  "s_cbranch_scc1 .LR_help_%=\n" \
  "s_lshl_b32 s61, %24, 2\n" \
  "s_lshr_b32 s51, %20, s61\n" \
  "s_and_b32 s51, s51, 7\n" \
  "s_lshl_b32 s51, s51, 12\n" \
  "s_xor_b32 s51, s51, 4210688\n" \
  "s_mov_b32 s71, s56\n" \
  "s_add_u32 s47, s51, 886784\n" \
  "s_lshr_b32 s52, %20, 0\n" \
  "s_and_b32 s52, s52, 7\n" \
  "s_lshl_b32 s52, s52, 12\n" \
  "s_xor_b32 s52, s52, 4210688\n" \
  "s_xor_b32 s52, s52, s56\n" \
  "s_lshr_b32 s64, %20, 4\n" \
  "s_and_b32 s64, s64, 7\n" \
  "s_lshl_b32 s64, s64, 12\n" \
  "s_xor_b32 s64, s64, 4210688\n" \
  "s_xor_b32 s64, s64, s56\n" \
  "s_lshr_b32 s65, %20, 8\n" \
  "s_and_b32 s65, s65, 7\n" \
  "s_lshl_b32 s65, s65, 12\n" \
  "s_xor_b32 s65, s65, 4210688\n" \
  "s_xor_b32 s65, s65, s56\n" \
  "s_lshr_b32 s67, %20, 12\n" \
  "s_and_b32 s67, s67, 7\n" \
  "s_lshl_b32 s67, s67, 12\n" \
  "s_xor_b32 s67, s67, 4210688\n" \
  "s_xor_b32 s67, s67, s56\n" \
  "v_add_u32 v208, 32768, v215\n" \
  "v_add_u32 v209, 65536, v215\n" \
  "v_add_u32 v210, 98304, v215\n" \
  "v_add_u32 v211, 40960, v214\n" \
  "s_mov_b32 s49, %22\n" \
  "s_mov_b32 s58, %23\n" \
  "s_memrealtime s[86:87]\n" \
  "s_memtime s[90:91]\n" \
  "s_waitcnt lgkmcnt(0)\n" \
  "s_mov_b32 s87, 900\n" \
  "s_mov_b32 s50, 0\n" \
  "s_mov_b32 s44, 0\n" \
  "s_mov_b32 s88, 0\n" \
  "s_mov_b32 s45, 0\n" \
  "s_mov_b32 s72, 2130706432\n" \
  "s_mov_b32 s46, 4096\n" \
  "v_mov_b32 v199, %21\n" \
  "s_mov_b32 s89, 2130706432\n" \
  "s_xor_b32 s51, s51, 0x40000000\n" \
  "s_xor_b32 s49, s49, 0x40000000\n" \
  "s_xor_b32 s58, s58, 0x40000000\n" \
  "s_xor_b32 s47, s47, 0x40000000\n" \
  "s_xor_b32 s52, s52, 0x40000000\n" \
  "s_xor_b32 s64, s64, 0x40000000\n" \
  "s_xor_b32 s65, s65, 0x40000000\n" \
  "s_xor_b32 s67, s67, 0x40000000\n" \
  ".LR_loop_%=:\n" \
  "s_add_u32 s95, s48, 0\n" \
  "s_and_b32 s63, s95, 3\n" \
  "s_lshl_b32 s95, s95, 5\n" \
  "s_lshl_b32 s63, s63, 4\n" \
  "s_sub_u32 s95, s95, s63\n" \
  "s_add_u32 s95, s95, s47\n" \
  "s_mov_b32 s54, s51\n" \
  "s_add_u32 s94, s51, 32768\n" \
  "s_mov_b32 s70, 0\n" \
  "s_mov_b32 s96, 0\n" \
  "buffer_load_dwordx4 v[200:203], off, s[40:43], s95 sc1\n" \
  "s_sub_u32 s76, s84, s90\n" \
  "s_sub_u32 s77, s98, s86\n" \
  "v_cvt_f32_u32 v196, s76\n" \
  "v_cvt_f32_u32 v197, s77\n" \
  "v_rcp_f32 v197, v197\n" \
  "v_mul_f32 v196, 16.0, v196\n" \
  "v_fma_f32 v196, v196, v197, 0.5\n" \
  "v_cvt_u32_f32 v196, v196\n" \
  "s_nop 0\n" \
  "v_readfirstlane_b32 s76, v196\n" \
  "s_max_u32 s76, s76, 160\n" \
  "s_min_u32 s76, s76, 400\n" \
  "s_add_u32 s77, s48, 2\n" \
  "s_and_b32 s77, s77, 3\n" \
  "s_cselect_b32 s87, s87, s76\n" \
  "s_cselect_b32 s86, s86, s98\n" \
  "s_cselect_b32 s90, s90, s84\n" \
  "v_mov_b64 v[64:65], 0\n" \
  "v_mov_b64 v[66:67], 0\n" \
  "v_mov_b64 v[68:69], 0\n" \
  "v_mov_b64 v[70:71], 0\n" \
  "v_mov_b64 v[72:73], 0\n" \
  "v_mov_b64 v[74:75], 0\n" \
  "v_mov_b64 v[76:77], 0\n" \
  "v_mov_b64 v[78:79], 0\n" \
  "v_mov_b64 v[80:81], 0\n" \
  "v_mov_b64 v[82:83], 0\n" \
  "v_mov_b64 v[84:85], 0\n" \
  "v_mov_b64 v[86:87], 0\n" \
  "v_mov_b64 v[88:89], 0\n" \
  "v_mov_b64 v[90:91], 0\n" \
  "v_mov_b64 v[92:93], 0\n" \
  "v_mov_b64 v[94:95], 0\n" \
  "v_mov_b64 v[96:97], 0\n" \
  "v_mov_b64 v[98:99], 0\n" \
  "v_mov_b64 v[100:101], 0\n" \
  "v_mov_b64 v[102:103], 0\n" \
  "v_mov_b64 v[104:105], 0\n" \
  "v_mov_b64 v[106:107], 0\n" \
  "v_mov_b64 v[108:109], 0\n" \
  "v_mov_b64 v[110:111], 0\n" \
  "v_mov_b64 v[112:113], 0\n" \
  "v_mov_b64 v[114:115], 0\n" \
  "v_mov_b64 v[116:117], 0\n" \
  "v_mov_b64 v[118:119], 0\n" \
  "v_mov_b64 v[120:121], 0\n" \
  "v_mov_b64 v[122:123], 0\n" \
  "v_mov_b64 v[124:125], 0\n" \
  "v_mov_b64 v[126:127], 0\n" \
  "v_mov_b64 v[192:193], 0\n" \
  "v_mov_b64 v[194:195], 0\n" \
  "buffer_store_dwordx2 v[198:199], off, s[40:43], s89 sc1\n" \
  "s_barrier\n" \
  "ds_read_b128 a[0:3], v211 offset:0\n" \
  "ds_read_b128 a[4:7], v211 offset:1024\n" \
  "ds_read_b128 a[8:11], v211 offset:2048\n" \
  "ds_read_b128 a[12:15], v211 offset:3072\n" \
  "ds_read_b128 a[16:19], v211 offset:4096\n" \
  "ds_read_b128 a[20:23], v211 offset:5120\n" \
  "ds_read_b128 a[24:27], v211 offset:6144\n" \
  "ds_read_b128 a[28:31], v211 offset:7168\n" \
  "ds_read_b128 a[32:35], v211 offset:8192\n" \
  "ds_read_b128 a[36:39], v211 offset:9216\n" \
  "ds_read_b128 a[40:43], v211 offset:10240\n" \
  "ds_read_b128 a[44:47], v211 offset:11264\n" \
  "ds_read_b128 a[48:51], v211 offset:12288\n" \
  "ds_read_b128 a[52:55], v211 offset:13312\n" \
  "ds_read_b128 a[56:59], v211 offset:14336\n" \
  "ds_read_b128 a[60:63], v211 offset:15360\n" \
  "ds_read_b128 a[64:67], v211 offset:16384\n" \
  "ds_read_b128 a[68:71], v211 offset:17408\n" \
  "ds_read_b128 a[72:75], v211 offset:18432\n" \
  "ds_read_b128 a[76:79], v211 offset:19456\n" \
  "ds_read_b128 a[80:83], v211 offset:20480\n" \
  "ds_read_b128 a[84:87], v211 offset:21504\n" \
  "ds_read_b128 a[88:91], v211 offset:22528\n" \
  "ds_read_b128 a[92:95], v211 offset:23552\n" \
  "ds_read_b128 a[96:99], v211 offset:24576\n" \
  "ds_read_b128 a[100:103], v211 offset:25600\n" \
  "ds_read_b128 a[104:107], v211 offset:26624\n" \
  "ds_read_b128 a[108:111], v211 offset:27648\n" \
  "ds_read_b128 a[112:115], v211 offset:28672\n" \
  "ds_read_b128 a[116:119], v211 offset:29696\n" \
  "ds_read_b128 a[120:123], v211 offset:30720\n" \
  "ds_read_b128 a[124:127], v211 offset:31744\n" \
  "v_xor_b32 v211, 32768, v211\n" \
  "s_mov_b32 s69, 0\n" \
  "s_waitcnt vmcnt(1)\n" \
  ".LR_tw_%=:\n" \
  "v_readfirstlane_b32 s60, v200\n" \
  "s_cmp_lg_u32 s60, s48\n" \
  "s_cbranch_scc1 .LR_tw_re_%=\n" \
  "v_readfirstlane_b32 s50, v201\n" \
  "s_cmp_eq_u32 s48, 0\n" \
  "s_cselect_b32 s50, s86, s50\n" \
  "s_cmp_lg_u32 s44, s45\n" \
  "s_cselect_b32 s60, s46, 0\n" \
  "s_mov_b32 s45, s44\n" \
  "s_xor_b32 s54, s54, s60\n" \
  "s_xor_b32 s94, s94, s60\n" \
  ".LR_again_%=:\n" \
  "s_memrealtime s[60:61]\n" \
  "s_add_u32 s53, s50, s44\n" \
  "s_bfe_u32 s63, %28, 0x80010\n" \
  "s_waitcnt lgkmcnt(0)\n" \
  "s_sub_u32 s62, s53, s60\n" \
  "s_sub_u32 s61, s62, 14\n" \
  "s_cmp_ge_u32 s61, 2000\n" \
  "s_cbranch_scc1 .LR_near_%=\n" \
  "s_sub_u32 s62, s62, s63\n" \
  "s_cmp_le_i32 s62, 0\n" \
  "s_cbranch_scc1 .LR_fin_%=\n" \
  "s_sub_u32 s62, s62, 9\n" \
  ".LR_slp_%=:\n" \
  "s_cmp_le_i32 s62, 0\n" \
  "s_cbranch_scc1 .LR_clk_%=\n" \
  "s_sleep 1\n" \
  "s_sub_u32 s62, s62, 3\n" \
  "s_branch .LR_slp_%=\n" \
  ".LR_clk_%=:\n" \
  "s_memrealtime s[60:61]\n" \
  "s_waitcnt lgkmcnt(0)\n" \
  "s_sub_u32 s62, s53, s60\n" \
  "s_cmp_gt_i32 s62, s63\n" \
  "s_cbranch_scc1 .LR_clk_%=\n" \
  ".LR_fin_%=:\n" \
  "s_memrealtime s[76:77]\n" \
  "s_nop 4\n" \
  "s_memrealtime s[78:79]\n" \
  "s_nop 4\n" \
  "s_memrealtime s[80:81]\n" \
  "s_nop 4\n" \
  "s_memrealtime s[82:83]\n" \
  "s_waitcnt lgkmcnt(0)\n" \
  "s_mov_b32 s77, 1\n" \
  "s_cmp_eq_u32 s78, s76\n" \
  "s_addc_u32 s77, s77, 0\n" \
  "s_cmp_eq_u32 s80, s76\n" \
  "s_addc_u32 s77, s77, 0\n" \
  "s_cmp_eq_u32 s82, s76\n" \
  "s_addc_u32 s77, s77, 0\n" \
  "s_mul_i32 s77, s77, 24\n" \
  "s_lshr_b32 s78, s87, 2\n" \
  "s_sub_u32 s77, s78, s77\n" \
  "s_max_i32 s77, s77, 0\n" \
  "s_sub_u32 s76, s53, s76\n" \
  "s_mul_i32 s76, s76, s87\n" \
  "s_ashr_i32 s76, s76, 4\n" \
  "s_sub_u32 s76, s76, s77\n" \
  "s_sub_u32 s76, s76, 220\n" \
  "s_max_i32 s76, s76, 0\n" \
  "s_min_i32 s76, s76, 400\n" \
  "s_lshr_b32 s76, s76, 3\n" \
  "s_lshl_b32 s76, s76, 2\n" \
  "s_getpc_b64 s[78:79]\n" \
  "s_sub_u32 s76, 220, s76\n" \
  "s_add_u32 s78, s78, s76\n" \
  "s_addc_u32 s79, s79, 0\n" \
  "s_setpc_b64 s[78:79]\n" \
  "s_nop 1\n" \
  "s_nop 1\n" \
  "s_nop 1\n" \
  "s_nop 1\n" \
  "s_nop 1\n" \
  "s_nop 1\n" \
  "s_nop 1\n" \
  "s_nop 1\n" \
  "s_nop 1\n" \
  "s_nop 1\n" \
  "s_nop 1\n" \
  "s_nop 1\n" \
  "s_nop 1\n" \
  "s_nop 1\n" \
  "s_nop 1\n" \
  "s_nop 1\n" \
  "s_nop 1\n" \
  "s_nop 1\n" \
  "s_nop 1\n" \
  "s_nop 1\n" \
  "s_nop 1\n" \
  "s_nop 1\n" \
  "s_nop 1\n" \
  "s_nop 1\n" \
  "s_nop 1\n" \
  "s_nop 1\n" \
  "s_nop 1\n" \
  "s_nop 1\n" \
  "s_nop 1\n" \
  "s_nop 1\n" \
  "s_nop 1\n" \
  "s_nop 1\n" \
  "s_nop 1\n" \
  "s_nop 1\n" \
  "s_nop 1\n" \
  "s_nop 1\n" \
  "s_nop 1\n" \
  "s_nop 1\n" \
  "s_nop 1\n" \
  "s_nop 1\n" \
  "s_nop 1\n" \
  "s_nop 1\n" \
  "s_nop 1\n" \
  "s_nop 1\n" \
  "s_nop 1\n" \
  "s_nop 1\n" \
  "s_nop 1\n" \
  "s_nop 1\n" \
  "s_nop 1\n" \
  "s_nop 1\n" \
  ".LR_b2_%=:\n" \
  "buffer_load_dwordx4 v[0:3], v215, s[40:43], s54 offen sc1\n" \
  "s_memtime s[92:93]\n" \
  "s_cmp_lg_u32 s96, 0\n" \
  "buffer_load_dwordx4 v[4:7], v215, s[40:43], s54 offen offset:1024 sc1\n" \
  "buffer_load_dwordx4 v[8:11], v215, s[40:43], s54 offen offset:2048 sc1\n" \
  "buffer_load_dwordx4 v[12:15], v215, s[40:43], s54 offen offset:3072 sc1\n" \
  "buffer_load_dwordx4 v[16:19], v208, s[40:43], s54 offen sc1\n" \
  "buffer_load_dwordx4 v[20:23], v208, s[40:43], s54 offen offset:1024 sc1\n" \
  "buffer_load_dwordx4 v[24:27], v208, s[40:43], s54 offen offset:2048 sc1\n" \
  "buffer_load_dwordx4 v[28:31], v208, s[40:43], s54 offen offset:3072 sc1\n" \
  "buffer_load_dwordx4 v[32:35], v209, s[40:43], s54 offen sc1\n" \
  "buffer_load_dwordx4 v[36:39], v209, s[40:43], s54 offen offset:1024 sc1\n" \
  "buffer_load_dwordx4 v[40:43], v209, s[40:43], s54 offen offset:2048 sc1\n" \
  "buffer_load_dwordx4 v[44:47], v209, s[40:43], s54 offen offset:3072 sc1\n" \
  "buffer_load_dwordx4 v[48:51], v210, s[40:43], s54 offen sc1\n" \
  "buffer_load_dwordx4 v[52:55], v210, s[40:43], s54 offen offset:1024 sc1\n" \
  "buffer_load_dwordx4 v[56:59], v210, s[40:43], s54 offen offset:2048 sc1\n" \
  "buffer_load_dwordx4 v[60:63], v210, s[40:43], s54 offen offset:3072 sc1\n" \
  "buffer_load_dwordx4 v[128:131], v214, s[40:43], s49 offen sc1\n" \
  "buffer_load_dwordx4 v[132:135], v214, s[40:43], s49 offen offset:1024 sc1\n" \
  "buffer_load_dwordx4 v[136:139], v214, s[40:43], s49 offen offset:2048 sc1\n" \
  "buffer_load_dwordx4 v[140:143], v214, s[40:43], s49 offen offset:3072 sc1\n" \
  "buffer_load_dwordx4 v[144:147], v214, s[40:43], s58 offen sc1\n" \
  "buffer_load_dwordx4 v[148:151], v214, s[40:43], s58 offen offset:1024 sc1\n" \
  "buffer_load_dwordx4 v[152:155], v214, s[40:43], s58 offen offset:2048 sc1\n" \
  "buffer_load_dwordx4 v[156:159], v214, s[40:43], s58 offen offset:3072 sc1\n" \
  "buffer_load_dwordx4 v[204:207], v215, s[40:43], s54 offen sc1\n" \
  "buffer_load_dwordx4 v[204:207], v215, s[40:43], s54 offen sc1\n" \
  "buffer_load_dwordx4 v[204:207], v215, s[40:43], s54 offen sc1\n" \
  "buffer_load_dwordx4 v[160:163], v216, s[40:43], s54 offen sc1\n" \
  "buffer_load_dwordx4 v[164:167], v216, s[40:43], s54 offen offset:1024 sc1\n" \
  "buffer_load_dwordx4 v[168:171], v216, s[40:43], s54 offen offset:2048 sc1\n" \
  "buffer_load_dwordx4 v[172:175], v216, s[40:43], s54 offen offset:3072 sc1\n" \
  "buffer_load_dwordx4 v[176:179], v216, s[40:43], s94 offen sc1\n" \
  "buffer_load_dwordx4 v[180:183], v216, s[40:43], s94 offen offset:1024 sc1\n" \
  "buffer_load_dwordx4 v[184:187], v216, s[40:43], s94 offen offset:2048 sc1\n" \
  "buffer_load_dwordx4 v[188:191], v216, s[40:43], s94 offen offset:3072 sc1\n" \
  "s_cbranch_scc1 .LR_nb_%=\n" \
  "s_barrier\n" \
  ".LR_nb_%=:\n" \
  "s_waitcnt vmcnt(34)\n" \
  "v_smfmac_f32_16x16x64_bf16 v[64:67], v[0:3], a[0:7], v212\n" \
  "s_waitcnt vmcnt(33)\n" \
  "s_nop 1\n" \
  "v_smfmac_f32_16x16x64_bf16 v[68:71], v[4:7], a[8:15], v212\n" \
  "s_waitcnt vmcnt(32)\n" \
  "s_nop 1\n" \
  "v_smfmac_f32_16x16x64_bf16 v[72:75], v[8:11], a[16:23], v212\n" \
  "s_waitcnt vmcnt(31)\n" \
  "v_cvt_pk_bf16_f32 v64, v64, v65\n" \
  "v_cvt_pk_bf16_f32 v65, v66, v67\n" \
  "v_smfmac_f32_16x16x64_bf16 v[76:79], v[12:15], a[24:31], v212\n" \
  "s_waitcnt vmcnt(30)\n" \
  "v_cvt_pk_bf16_f32 v66, v68, v69\n" \
  "v_cvt_pk_bf16_f32 v67, v70, v71\n" \
  "v_smfmac_f32_16x16x64_bf16 v[80:83], v[16:19], a[32:39], v212\n" \
  "s_waitcnt vmcnt(29)\n" \
  "v_cvt_pk_bf16_f32 v68, v72, v73\n" \
  "v_cvt_pk_bf16_f32 v69, v74, v75\n" \
  "v_smfmac_f32_16x16x64_bf16 v[84:87], v[20:23], a[40:47], v212\n" \
  "s_waitcnt vmcnt(28)\n" \
  "v_cvt_pk_bf16_f32 v70, v76, v77\n" \
  "v_cvt_pk_bf16_f32 v71, v78, v79\n" \
  "v_smfmac_f32_16x16x64_bf16 v[88:91], v[24:27], a[48:55], v212\n" \
  "s_waitcnt vmcnt(27)\n" \
  "v_cvt_pk_bf16_f32 v80, v80, v81\n" \
  "v_cvt_pk_bf16_f32 v81, v82, v83\n" \
  "v_smfmac_f32_16x16x64_bf16 v[92:95], v[28:31], a[56:63], v212\n" \
  "s_waitcnt vmcnt(26)\n" \
  "v_cvt_pk_bf16_f32 v82, v84, v85\n" \
  "v_cvt_pk_bf16_f32 v83, v86, v87\n" \
  "v_smfmac_f32_16x16x64_bf16 v[96:99], v[32:35], a[64:71], v212\n" \
  "s_waitcnt vmcnt(25)\n" \
  "v_cvt_pk_bf16_f32 v84, v88, v89\n" \
  "v_cvt_pk_bf16_f32 v85, v90, v91\n" \
  "v_smfmac_f32_16x16x64_bf16 v[100:103], v[36:39], a[72:79], v212\n" \
  "s_waitcnt vmcnt(24)\n" \
  "v_cvt_pk_bf16_f32 v86, v92, v93\n" \
  "v_cvt_pk_bf16_f32 v87, v94, v95\n" \
  "v_smfmac_f32_16x16x64_bf16 v[104:107], v[40:43], a[80:87], v212\n" \
  "s_waitcnt vmcnt(23)\n" \
  "v_cvt_pk_bf16_f32 v96, v96, v97\n" \
  "v_cvt_pk_bf16_f32 v97, v98, v99\n" \
  "v_smfmac_f32_16x16x64_bf16 v[108:111], v[44:47], a[88:95], v212\n" \
  "s_waitcnt vmcnt(22)\n" \
  "v_cvt_pk_bf16_f32 v98, v100, v101\n" \
  "v_cvt_pk_bf16_f32 v99, v102, v103\n" \
  "v_smfmac_f32_16x16x64_bf16 v[112:115], v[48:51], a[96:103], v212\n" \
  "s_waitcnt vmcnt(21)\n" \
  "v_cvt_pk_bf16_f32 v100, v104, v105\n" \
  "v_cvt_pk_bf16_f32 v101, v106, v107\n" \
  "v_smfmac_f32_16x16x64_bf16 v[116:119], v[52:55], a[104:111], v212\n" \
  "s_waitcnt vmcnt(20)\n" \
  "v_cvt_pk_bf16_f32 v102, v108, v109\n" \
  "v_cvt_pk_bf16_f32 v103, v110, v111\n" \
  "v_smfmac_f32_16x16x64_bf16 v[120:123], v[56:59], a[112:119], v212\n" \
  "s_waitcnt vmcnt(19)\n" \
  "v_cvt_pk_bf16_f32 v112, v112, v113\n" \
  "v_cvt_pk_bf16_f32 v113, v114, v115\n" \
  "v_smfmac_f32_16x16x64_bf16 v[124:127], v[60:63], a[120:127], v212\n" \
  "s_mov_b32 s96, 1\n" \
  "v_cvt_pk_bf16_f32 v114, v116, v117\n" \
  "v_cvt_pk_bf16_f32 v115, v118, v119\n" \
  "s_nop 0\n" \
  "v_cvt_pk_bf16_f32 v116, v120, v121\n" \
  "v_cvt_pk_bf16_f32 v117, v122, v123\n" \
  "s_nop 1\n" \
  "v_cvt_pk_bf16_f32 v118, v124, v125\n" \
  "v_cvt_pk_bf16_f32 v119, v126, v127\n" \
  "s_waitcnt vmcnt(18)\n" \
  "v_permlane16_swap_b32 v128, v129\n" \
  "v_permlane16_swap_b32 v130, v131\n" \
  "s_waitcnt vmcnt(17)\n" \
  "v_permlane16_swap_b32 v132, v133\n" \
  "v_permlane16_swap_b32 v134, v135\n" \
  "s_waitcnt vmcnt(16)\n" \
  "v_permlane16_swap_b32 v136, v137\n" \
  "v_permlane16_swap_b32 v138, v139\n" \
  "s_waitcnt vmcnt(15)\n" \
  "v_permlane16_swap_b32 v140, v141\n" \
  "v_permlane16_swap_b32 v142, v143\n" \
  "s_waitcnt vmcnt(14)\n" \
  "v_permlane16_swap_b32 v144, v145\n" \
  "v_permlane16_swap_b32 v146, v147\n" \
  "s_waitcnt vmcnt(13)\n" \
  "v_permlane16_swap_b32 v148, v149\n" \
  "v_permlane16_swap_b32 v150, v151\n" \
  "s_waitcnt vmcnt(12)\n" \
  "v_permlane16_swap_b32 v152, v153\n" \
  "v_permlane16_swap_b32 v154, v155\n" \
  "s_waitcnt vmcnt(11)\n" \
  "v_permlane16_swap_b32 v156, v157\n" \
  "v_permlane16_swap_b32 v158, v159\n" \
  "s_nop 0\n" \
  "s_waitcnt vmcnt(7)\n" \
  "v_smfmac_f32_16x16x64_bf16 v[192:195], v[160:163], v[128:135], v212\n" \
  "s_waitcnt vmcnt(6)\n" \
  "v_smfmac_f32_16x16x64_bf16 v[192:195], v[164:167], v[136:143], v212\n" \
  "s_waitcnt vmcnt(5)\n" \
  "v_smfmac_f32_16x16x64_bf16 v[192:195], v[168:171], v[144:151], v212\n" \
  "s_waitcnt vmcnt(4)\n" \
  "v_smfmac_f32_16x16x64_bf16 v[192:195], v[172:175], v[152:159], v212\n" \
  "s_waitcnt vmcnt(3)\n" \
  "v_smfmac_f32_16x16x64_bf16 v[192:195], v[176:179], v[64:71], v213\n" \
  "s_waitcnt vmcnt(2)\n" \
  "v_smfmac_f32_16x16x64_bf16 v[192:195], v[180:183], v[80:87], v213\n" \
  "s_waitcnt vmcnt(1)\n" \
  "v_smfmac_f32_16x16x64_bf16 v[192:195], v[184:187], v[96:103], v213\n" \
  "s_waitcnt vmcnt(0)\n" \
  "v_smfmac_f32_16x16x64_bf16 v[192:195], v[188:191], v[112:119], v213\n" \
  "s_nop 7\n" \
  "v_permlane32_swap_b32 v192, v194\n" \
  "v_permlane32_swap_b32 v193, v195\n" \
  "v_add_f32 v192, v192, v194\n" \
  "v_add_f32 v193, v193, v195\n" \
  "v_cvt_pk_bf16_f32 v194, v192, v193\n" \
  "v_cmp_u_f32 vcc, v192, v193\n" \
  "s_nop 0\n" \
  "v_mov_b32_dpp v195, v194 quad_perm:[1,0,3,2] row_mask:0xf bank_mask:0xf\n" \
  "v_perm_b32 v194, v195, v194, v218\n" \
  "s_cbranch_vccnz .LR_bad_%=\n" \
  ".LR_good_%=:\n" \
  "buffer_store_dword v194, v217, s[40:43], s52 offen sc1\n" \
  "buffer_store_dword v194, v217, s[40:43], s64 offen sc1\n" \
  "buffer_store_dword v194, v217, s[40:43], s65 offen sc1\n" \
  "buffer_store_dword v194, v217, s[40:43], s67 offen sc1\n" \
  "s_memrealtime s[98:99]\n" \
  "s_memtime s[84:85]\n" \
  "s_waitcnt lgkmcnt(0)\n" \
  "s_sub_u32 s60, s84, s92\n" \
  "s_cmp_lt_u32 s60, 4000\n" \
  "s_cselect_b32 s60, s60, 4194304\n" \
  "s_cmp_eq_u32 s70, 0\n" \
  "s_cselect_b32 s60, s60, 4194304\n" \
  "s_min_u32 s66, s66, s60\n" \
  "s_lshl_b32 s60, s66, 10\n" \
  "s_cselect_b32 s89, %30, 2130706432\n" \
  "s_or_b32 s60, s60, s87\n" \
  "v_mov_b32 v198, s60\n" \
  "s_and_b32 s61, s48, 3\n" \
  "s_cmp_eq_u32 s61, 1\n" \
  "s_cselect_b32 s89, s89, 2130706432\n" \
  "s_cselect_b32 s66, 4194304, s66\n" \
  "s_cmp_eq_u32 s48, 0\n" \
  "s_cbranch_scc1 .LR_go0_%=\n" \
  "s_cmp_lg_u32 %26, 0\n" \
  "s_cbranch_scc1 .LR_extra_%=\n" \
  ".LR_extra_b_%=:\n" \
  "s_xor_b32 s51, s51, s56\n" \
  "s_xor_b32 s49, s49, s56\n" \
  "s_xor_b32 s58, s58, s56\n" \
  "s_xor_b32 s52, s52, s57\n" \
  "s_xor_b32 s64, s64, s57\n" \
  "s_xor_b32 s65, s65, s57\n" \
  "s_xor_b32 s67, s67, s57\n" \
  "s_xor_b32 s71, s71, s57\n" \
  "s_mov_b32 s60, s56\n" \
  "s_mov_b32 s56, s57\n" \
  "s_mov_b32 s57, s60\n" \
  ".LR_go_%=:\n" \
  "s_add_u32 s48, s48, 1\n" \
  "s_cmp_lt_u32 s48, 64\n" \
  "s_cbranch_scc1 .LR_loop_%=\n" \
  "s_branch .LR_end_%=\n" \
  ".LR_tw_re_%=:\n" \
  "s_add_u32 s69, s69, 1\n" \
  "s_cmp_gt_u32 s69, 64\n" \
  "s_cbranch_scc1 .LR_now_%=\n" \
  "buffer_load_dwordx4 v[200:203], off, s[40:43], s95 sc1\n" \
  "s_waitcnt vmcnt(0)\n" \
  "s_branch .LR_tw_%=\n" \
  ".LR_now_%=:\n" \
  "s_waitcnt lgkmcnt(0)\n" \
  "s_branch .LR_b2_%=\n" \
  ".LR_near_%=:\n" \
  "s_cmp_ge_u32 s62, 14\n" \
  "s_cbranch_scc1 .LR_lat_%=\n" \
  "s_cmp_le_u32 s62, 4\n" \
  "s_cbranch_scc1 .LR_b2_%=\n" \
  "s_memrealtime s[60:61]\n" \
  "s_waitcnt lgkmcnt(0)\n" \
  "s_sub_u32 s62, s53, s60\n" \
  "s_branch .LR_near_%=\n" \
  ".LR_lat_%=:\n" \
  "s_lshr_b32 s63, %29, 16\n" \
  "s_add_u32 s62, s62, s63\n" \
  "s_cmp_lt_u32 s62, 4294937296\n" \
  "s_cbranch_scc1 .LR_b2_%=\n" \
  "s_and_b32 s63, %29, 0xffff\n" \
  ".LR_lat1_%=:\n" \
  "s_add_u32 s44, s44, s63\n" \
  "s_add_u32 s62, s62, s63\n" \
  "s_cbranch_scc0 .LR_lat1_%=\n" \
  "s_branch .LR_again_%=\n" \
  ".LR_mv_%=:\n" \
  "s_sub_u32 s76, s77, s44\n" \
  "s_cmp_gt_u32 s76, 2000\n" \
  "s_cbranch_scc1 .LR_b2_%=\n" \
  "s_mov_b32 s44, s77\n" \
  "s_branch .LR_again_%=\n" \
  ".LR_go0_%=:\n" \
  "s_xor_b32 s51, s51, 0x40000000\n" \
  "s_xor_b32 s49, s49, 0x40000000\n" \
  "s_xor_b32 s58, s58, 0x40000000\n" \
  "s_xor_b32 s47, s47, 0x40000000\n" \
  "s_xor_b32 s52, s52, 0x40000000\n" \
  "s_xor_b32 s64, s64, 0x40000000\n" \
  "s_xor_b32 s65, s65, 0x40000000\n" \
  "s_xor_b32 s67, s67, 0x40000000\n" \
  "s_mov_b32 s44, 0\n" \
  "s_mov_b32 s89, 2130706432\n" \
  "s_mov_b32 s87, 340\n" \
  "s_mov_b32 s66, 4194304\n" \
  "s_add_u32 s95, s47, 16\n" \
  "s_mov_b32 s69, 0\n" \
  ".LR_go1_%=:\n" \
  "buffer_load_dwordx4 v[200:203], off, s[40:43], s95 sc1\n" \
  "s_add_u32 s69, s69, 1\n" \
  "s_waitcnt vmcnt(0)\n" \
  "v_readfirstlane_b32 s60, v200\n" \
  "s_sleep 1\n" \
  "s_cmp_gt_u32 s69, 200\n" \
  "s_cselect_b32 s60, 1, s60\n" \
  "s_cmp_lg_u32 s60, 1\n" \
  "s_cbranch_scc1 .LR_go1_%=\n" \
  "s_branch .LR_go_%=\n" \
  ".LR_extra_%=:\n" \
  "s_sub_u32 s62, s48, 42\n" \
  "s_cmp_lt_u32 s62, 21\n" \
  "s_cselect_b32 s62, s71, 2130706432\n" \
  "s_cmp_eq_u32 s61, 2\n" \
  "s_cselect_b32 s61, %26, 2130706432\n" \
  "v_readfirstlane_b32 s63, v202\n" \
  "s_sub_u32 s60, s63, s68\n" \
  "s_mov_b32 s68, s63\n" \
  "s_add_u32 s63, s63, s60\n" \
  "s_add_u32 s63, s63, s44\n" \
  "s_lshl_b32 s60, s44, 8\n" \
  "s_or_b32 s60, s60, s48\n" \
  "v_add_u32_e64 v196, 1, s48\n" \
  "v_mov_b32 v197, s63\n" \
  "v_xor_b32 v204, s62, v219\n" \
  "v_or_b32 v204, v204, v222\n" \
  "v_writelane_b32 v196, s60, 4\n" \
  "v_writelane_b32 v197, %21, 4\n" \
  "v_writelane_b32 v204, s61, 4\n" \
  "s_nop 1\n" \
  "buffer_store_dwordx2 v[196:197], v204, s[40:43], 0 offen sc1\n" \
  "s_branch .LR_extra_b_%=\n" \
  ".LR_bad_%=:\n" \
  "s_cmp_eq_u32 s48, 0\n" \
  "s_cbranch_scc1 .LR_good_%=\n" \
  "s_add_u32 s70, s70, 1\n" \
  "s_cmp_gt_u32 s70, 5\n" \
  "s_cbranch_scc1 .LR_bad1_%=\n" \
  ".LR_redo_%=:\n" \
  "s_and_b32 s62, %29, 0xffff\n" \
  "s_add_u32 s44, s44, s62\n" \
  "v_mov_b32 v196, s44\n" \
  "v_xor_b32 v197, s71, v221\n" \
  "s_xor_b32 s54, s54, s46\n" \
  "s_xor_b32 s94, s94, s46\n" \
  "v_mov_b64 v[64:65], 0\n" \
  "v_mov_b64 v[66:67], 0\n" \
  "v_mov_b64 v[68:69], 0\n" \
  "v_mov_b64 v[70:71], 0\n" \
  "v_mov_b64 v[72:73], 0\n" \
  "v_mov_b64 v[74:75], 0\n" \
  "v_mov_b64 v[76:77], 0\n" \
  "v_mov_b64 v[78:79], 0\n" \
  "v_mov_b64 v[80:81], 0\n" \
  "v_mov_b64 v[82:83], 0\n" \
  "v_mov_b64 v[84:85], 0\n" \
  "v_mov_b64 v[86:87], 0\n" \
  "v_mov_b64 v[88:89], 0\n" \
  "v_mov_b64 v[90:91], 0\n" \
  "v_mov_b64 v[92:93], 0\n" \
  "v_mov_b64 v[94:95], 0\n" \
  "v_mov_b64 v[96:97], 0\n" \
  "v_mov_b64 v[98:99], 0\n" \
  "v_mov_b64 v[100:101], 0\n" \
  "v_mov_b64 v[102:103], 0\n" \
  "v_mov_b64 v[104:105], 0\n" \
  "v_mov_b64 v[106:107], 0\n" \
  "v_mov_b64 v[108:109], 0\n" \
  "v_mov_b64 v[110:111], 0\n" \
  "v_mov_b64 v[112:113], 0\n" \
  "v_mov_b64 v[114:115], 0\n" \
  "v_mov_b64 v[116:117], 0\n" \
  "v_mov_b64 v[118:119], 0\n" \
  "v_mov_b64 v[120:121], 0\n" \
  "v_mov_b64 v[122:123], 0\n" \
  "v_mov_b64 v[124:125], 0\n" \
  "v_mov_b64 v[126:127], 0\n" \
  "v_mov_b64 v[192:193], 0\n" \
  "v_mov_b64 v[194:195], 0\n" \
  "s_branch .LR_again_%=\n" \
  ".LR_bad1_%=:\n" \
  "v_max_u32 v196, v0, v1\n" \
  "v_max3_u32 v196, v196, v2, v3\n" \
  "v_max3_u32 v196, v196, v4, v5\n" \
  "v_max3_u32 v196, v196, v6, v7\n" \
  "v_max3_u32 v196, v196, v8, v9\n" \
  "v_max3_u32 v196, v196, v10, v11\n" \
  "v_max3_u32 v196, v196, v12, v13\n" \
  "v_max3_u32 v196, v196, v14, v15\n" \
  "v_max3_u32 v196, v196, v16, v17\n" \
  "v_max3_u32 v196, v196, v18, v19\n" \
  "v_max3_u32 v196, v196, v20, v21\n" \
  "v_max3_u32 v196, v196, v22, v23\n" \
  "v_max3_u32 v196, v196, v24, v25\n" \
  "v_max3_u32 v196, v196, v26, v27\n" \
  "v_max3_u32 v196, v196, v28, v29\n" \
  "v_max3_u32 v196, v196, v30, v31\n" \
  "v_max3_u32 v196, v196, v32, v33\n" \
  "v_max3_u32 v196, v196, v34, v35\n" \
  "v_max3_u32 v196, v196, v36, v37\n" \
  "v_max3_u32 v196, v196, v38, v39\n" \
  "v_max3_u32 v196, v196, v40, v41\n" \
  "v_max3_u32 v196, v196, v42, v43\n" \
  "v_max3_u32 v196, v196, v44, v45\n" \
  "v_max3_u32 v196, v196, v46, v47\n" \
  "v_max3_u32 v196, v196, v48, v49\n" \
  "v_max3_u32 v196, v196, v50, v51\n" \
  "v_max3_u32 v196, v196, v52, v53\n" \
  "v_max3_u32 v196, v196, v54, v55\n" \
  "v_max3_u32 v196, v196, v56, v57\n" \
  "v_max3_u32 v196, v196, v58, v59\n" \
  "v_max3_u32 v196, v196, v60, v61\n" \
  "v_max3_u32 v196, v196, v62, v63\n" \
  "v_max_u32 v197, v128, v129\n" \
  "v_max3_u32 v197, v197, v130, v131\n" \
  "v_max3_u32 v197, v197, v132, v133\n" \
  "v_max3_u32 v197, v197, v134, v135\n" \
  "v_max3_u32 v197, v197, v136, v137\n" \
  "v_max3_u32 v197, v197, v138, v139\n" \
  "v_max3_u32 v197, v197, v140, v141\n" \
  "v_max3_u32 v197, v197, v142, v143\n" \
  "v_max3_u32 v197, v197, v144, v145\n" \
  "v_max3_u32 v197, v197, v146, v147\n" \
  "v_max3_u32 v197, v197, v148, v149\n" \
  "v_max3_u32 v197, v197, v150, v151\n" \
  "v_max3_u32 v197, v197, v152, v153\n" \
  "v_max3_u32 v197, v197, v154, v155\n" \
  "v_max3_u32 v197, v197, v156, v157\n" \
  "v_max3_u32 v197, v197, v158, v159\n" \
  "v_max_u32 v204, v160, v161\n" \
  "v_max3_u32 v204, v204, v162, v163\n" \
  "v_max3_u32 v204, v204, v164, v165\n" \
  "v_max3_u32 v204, v204, v166, v167\n" \
  "v_max3_u32 v204, v204, v168, v169\n" \
  "v_max3_u32 v204, v204, v170, v171\n" \
  "v_max3_u32 v204, v204, v172, v173\n" \
  "v_max3_u32 v204, v204, v174, v175\n" \
  "v_max3_u32 v204, v204, v176, v177\n" \
  "v_max3_u32 v204, v204, v178, v179\n" \
  "v_max3_u32 v204, v204, v180, v181\n" \
  "v_max3_u32 v204, v204, v182, v183\n" \
  "v_max3_u32 v204, v204, v184, v185\n" \
  "v_max3_u32 v204, v204, v186, v187\n" \
  "v_max3_u32 v204, v204, v188, v189\n" \
  "v_max3_u32 v204, v204, v190, v191\n" \
  "v_max3_u32 v196, v196, v197, v204\n" \
  "v_cmp_eq_u32 vcc, -1, v196\n" \
  "s_cbranch_vccz .LR_nan_%=\n" \
  "s_add_u32 s88, s88, 1\n" \
  "v_mov_b32 v196, s88\n" \
  "v_mov_b32 v197, %21\n" \
  "buffer_store_dwordx2 v[196:197], off, s[40:43], %31 sc1\n" \
  "s_waitcnt vmcnt(0)\n" \
  "s_and_b32 s63, s70, 255\n" \
  "s_cbranch_scc1 .LR_redo_%=\n" \
  "buffer_load_dwordx2 v[196:197], off, s[40:43], %27 offset:128 sc1\n" \
  "s_waitcnt vmcnt(0)\n" \
  "v_readfirstlane_b32 s63, v197\n" \
  "s_cmp_eq_u32 s63, %21\n" \
  "s_cbranch_scc1 .LR_nan_%=\n" \
  "s_cmp_lt_u32 s70, 262144\n" \
  "s_cbranch_scc1 .LR_redo_%=\n" \
  "v_mov_b32 v196, 1\n" \
  "v_mov_b32 v197, %21\n" \
  "buffer_store_dwordx2 v[196:197], off, s[40:43], %27 offset:128 sc1\n" \
  ".LR_nan_%=:\n" \
  "v_min_u32 v194, -2, v194\n" \
  "s_branch .LR_good_%=\n" \
  ".LR_fu_%=:\n" \
  "s_add_u32 s60, s48, 0\n" \
  "s_lshl_b32 s62, s60, 7\n" \
  "s_add_u32 s62, s62, %33\n" \
  "v_mov_b32 v146, s62\n" \
  "s_sub_u32 s60, s60, 0\n" \
  "s_cmp_lt_u32 s60, 10\n" \
  "s_cselect_b64 s[76:77], -1, 0\n" \
  "v_cndmask_b32 v146, v220, v146, s[76:77]\n" \
  "buffer_load_dwordx2 v[140:141], v146, s[40:43], 0 offen sc1\n" \
  "s_waitcnt vmcnt(0)\n" \
  "v_cmp_ne_u32 vcc, %21, v141\n" \
  "s_nop 1\n" \
  "v_cndmask_b32 v140, v140, 0, vcc\n" \
  "v_max_i32 v140, v140, v218\n" \
  "v_cmp_ge_i32 vcc, s48, v140\n" \
  "s_cbranch_vccnz .LR_fu0_%=\n" \
  "s_cmp_lg_u32 %19, 3\n" \
  "s_cbranch_scc1 .LR_h8_%=\n" \
  "s_xor_b32 s60, s61, s75\n" \
  "buffer_load_dwordx4 v[40:43], v214, s[40:43], s60 offen sc1\n" \
  "s_xor_b32 s60, s60, 1024\n" \
  "buffer_load_dwordx4 v[44:47], v214, s[40:43], s60 offen sc1\n" \
  ".LR_h8_%=:\n" \
  "s_xor_b32 s60, s61, s64\n" \
  "buffer_load_dwordx4 v[0:3], v214, s[40:43], s60 offen sc1\n" \
  "s_xor_b32 s60, s60, 1024\n" \
  "buffer_load_dwordx4 v[4:7], v214, s[40:43], s60 offen sc1\n" \
  "s_xor_b32 s60, s61, s65\n" \
  "buffer_load_dwordx4 v[8:11], v214, s[40:43], s60 offen sc1\n" \
  "s_xor_b32 s60, s60, 1024\n" \
  "buffer_load_dwordx4 v[12:15], v214, s[40:43], s60 offen sc1\n" \
  "s_xor_b32 s60, s61, s67\n" \
  "buffer_load_dwordx4 v[16:19], v214, s[40:43], s60 offen sc1\n" \
  "s_xor_b32 s60, s60, 1024\n" \
  "buffer_load_dwordx4 v[20:23], v214, s[40:43], s60 offen sc1\n" \
  "s_cmp_lt_u32 s48, 62\n" \
  "s_cselect_b32 s60, s71, 2130706432\n" \
  "v_xor_b32 v142, s60, v221\n" \
  "v_xor_b32 v143, s60, v222\n" \
  "v_xor_b32 v144, s60, v223\n" \
  "buffer_store_dwordx4 v[136:139], v142, s[40:43], 0 offen sc1\n" \
  "buffer_store_dwordx4 v[136:139], v143, s[40:43], 0 offen sc1\n" \
  "buffer_store_dwordx4 v[136:139], v144, s[40:43], 0 offen sc1\n" \
  "s_barrier\n" \
  "s_xor_b32 s60, s61, s68\n" \
  "buffer_load_dwordx4 v[24:27], v214, s[40:43], s60 offen sc1\n" \
  "s_xor_b32 s60, s60, 1024\n" \
  "buffer_load_dwordx4 v[28:31], v214, s[40:43], s60 offen sc1\n" \
  "s_xor_b32 s60, s61, s74\n" \
  "buffer_load_dwordx4 v[32:35], v214, s[40:43], s60 offen sc1\n" \
  "s_xor_b32 s60, s60, 1024\n" \
  "buffer_load_dwordx4 v[36:39], v214, s[40:43], s60 offen sc1\n" \
  "s_add_u32 s60, s48, 1\n" \
  "s_lshl_b32 s62, s60, 7\n" \
  "s_add_u32 s62, s62, %33\n" \
  "v_mov_b32 v146, s62\n" \
  "s_sub_u32 s60, s60, 0\n" \
  "s_cmp_lt_u32 s60, 10\n" \
  "s_cselect_b64 s[76:77], -1, 0\n" \
  "v_cndmask_b32 v146, v220, v146, s[76:77]\n" \
  "buffer_load_dwordx2 v[140:141], v146, s[40:43], 0 offen sc1\n" \
  "s_waitcnt vmcnt(0)\n" \
  "v_mov_b32 v72, v24\n" \
  "v_mov_b32 v73, v28\n" \
  "v_mov_b32 v74, v25\n" \
  "v_mov_b32 v75, v29\n" \
  "v_mov_b32 v76, v26\n" \
  "v_mov_b32 v77, v30\n" \
  "v_mov_b32 v78, v27\n" \
  "v_mov_b32 v79, v31\n" \
  "ds_write_b128 v211, v[72:75] offset:6144\n" \
  "ds_write_b128 v211, v[76:79] offset:7168\n" \
  "v_mov_b32 v80, v32\n" \
  "v_mov_b32 v81, v36\n" \
  "v_mov_b32 v82, v33\n" \
  "v_mov_b32 v83, v37\n" \
  "v_mov_b32 v84, v34\n" \
  "v_mov_b32 v85, v38\n" \
  "v_mov_b32 v86, v35\n" \
  "v_mov_b32 v87, v39\n" \
  "ds_write_b128 v211, v[80:83] offset:8192\n" \
  "ds_write_b128 v211, v[84:87] offset:9216\n" \
  "s_branch .LR_fu_b_%=\n" \
  ".LR_fu0_%=:\n" \
  "s_cmp_lt_u32 s48, 62\n" \
  "s_cselect_b32 s60, s71, 2130706432\n" \
  "v_xor_b32 v142, s60, v221\n" \
  "v_xor_b32 v143, s60, v222\n" \
  "v_xor_b32 v144, s60, v223\n" \
  "buffer_store_dwordx4 v[136:139], v142, s[40:43], 0 offen sc1\n" \
  "buffer_store_dwordx4 v[136:139], v143, s[40:43], 0 offen sc1\n" \
  "buffer_store_dwordx4 v[136:139], v144, s[40:43], 0 offen sc1\n" \
  "s_barrier\n" \
  "s_sleep 0\n" \
  "s_mov_b32 s69, 0\n" \
  ".LR_fu1_%=:\n" \
  "s_add_u32 s60, s48, 0\n" \
  "s_lshl_b32 s62, s60, 7\n" \
  "s_add_u32 s62, s62, %33\n" \
  "v_mov_b32 v146, s62\n" \
  "s_sub_u32 s60, s60, 0\n" \
  "s_cmp_lt_u32 s60, 10\n" \
  "s_cselect_b64 s[76:77], -1, 0\n" \
  "v_cndmask_b32 v146, v220, v146, s[76:77]\n" \
  "buffer_load_dwordx2 v[140:141], v146, s[40:43], 0 offen sc1\n" \
  "s_waitcnt vmcnt(0)\n" \
  "v_cmp_ne_u32 vcc, %21, v141\n" \
  "s_nop 1\n" \
  "v_cndmask_b32 v140, v140, 0, vcc\n" \
  "v_max_i32 v140, v140, v218\n" \
  "v_cmp_ge_i32 vcc, s48, v140\n" \
  "s_cbranch_vccz .LR_fu_x_%=\n" \
  "s_sleep 2\n" \
  "s_add_u32 s69, s69, 1\n" \
  "s_and_b32 s63, s69, 1023\n" \
  "s_cbranch_scc1 .LR_fu1_%=\n" \
  "s_cmp_gt_u32 s69, 2097152\n" \
  "s_cbranch_scc1 .LR_fu1_set_%=\n" \
  "buffer_load_dwordx2 v[196:197], off, s[40:43], %27 offset:128 sc1\n" \
  "s_waitcnt vmcnt(0)\n" \
  "v_readfirstlane_b32 s63, v197\n" \
  "s_cmp_eq_u32 s63, %21\n" \
  "s_cbranch_scc0 .LR_fu1_%=\n" \
  "s_branch .LR_fu_x_%=\n" \
  ".LR_fu1_set_%=:\n" \
  "v_mov_b32 v196, 1\n" \
  "v_mov_b32 v197, %21\n" \
  "buffer_store_dwordx2 v[196:197], off, s[40:43], %27 offset:128 sc1\n" \
  "s_waitcnt vmcnt(0)\n" \
  "s_branch .LR_fu_x_%=\n" \
  ".LR_fu_x_%=:\n" \
  "s_xor_b32 s60, s61, s68\n" \
  "buffer_load_dwordx4 v[24:27], v214, s[40:43], s60 offen sc1\n" \
  "s_xor_b32 s60, s60, 1024\n" \
  "buffer_load_dwordx4 v[28:31], v214, s[40:43], s60 offen sc1\n" \
  "s_xor_b32 s60, s61, s74\n" \
  "buffer_load_dwordx4 v[32:35], v214, s[40:43], s60 offen sc1\n" \
  "s_xor_b32 s60, s60, 1024\n" \
  "buffer_load_dwordx4 v[36:39], v214, s[40:43], s60 offen sc1\n" \
  "s_cmp_lg_u32 %19, 3\n" \
  "s_cbranch_scc1 .LR_h7_%=\n" \
  "s_xor_b32 s60, s61, s75\n" \
  "buffer_load_dwordx4 v[40:43], v214, s[40:43], s60 offen sc1\n" \
  "s_xor_b32 s60, s60, 1024\n" \
  "buffer_load_dwordx4 v[44:47], v214, s[40:43], s60 offen sc1\n" \
  ".LR_h7_%=:\n" \
  "s_xor_b32 s60, s61, s64\n" \
  "buffer_load_dwordx4 v[0:3], v214, s[40:43], s60 offen sc1\n" \
  "s_xor_b32 s60, s60, 1024\n" \
  "buffer_load_dwordx4 v[4:7], v214, s[40:43], s60 offen sc1\n" \
  "s_xor_b32 s60, s61, s65\n" \
  "buffer_load_dwordx4 v[8:11], v214, s[40:43], s60 offen sc1\n" \
  "s_xor_b32 s60, s60, 1024\n" \
  "buffer_load_dwordx4 v[12:15], v214, s[40:43], s60 offen sc1\n" \
  "s_xor_b32 s60, s61, s67\n" \
  "buffer_load_dwordx4 v[16:19], v214, s[40:43], s60 offen sc1\n" \
  "s_xor_b32 s60, s60, 1024\n" \
  "buffer_load_dwordx4 v[20:23], v214, s[40:43], s60 offen sc1\n" \
  "s_add_u32 s60, s48, 1\n" \
  "s_lshl_b32 s62, s60, 7\n" \
  "s_add_u32 s62, s62, %33\n" \
  "v_mov_b32 v146, s62\n" \
  "s_sub_u32 s60, s60, 0\n" \
  "s_cmp_lt_u32 s60, 10\n" \
  "s_cselect_b64 s[76:77], -1, 0\n" \
  "v_cndmask_b32 v146, v220, v146, s[76:77]\n" \
  "buffer_load_dwordx2 v[140:141], v146, s[40:43], 0 offen sc1\n" \
  "s_waitcnt vmcnt(7)\n" \
  "v_mov_b32 v72, v24\n" \
  "v_mov_b32 v73, v28\n" \
  "v_mov_b32 v74, v25\n" \
  "v_mov_b32 v75, v29\n" \
  "v_mov_b32 v76, v26\n" \
  "v_mov_b32 v77, v30\n" \
  "v_mov_b32 v78, v27\n" \
  "v_mov_b32 v79, v31\n" \
  "ds_write_b128 v211, v[72:75] offset:6144\n" \
  "ds_write_b128 v211, v[76:79] offset:7168\n" \
  "v_mov_b32 v80, v32\n" \
  "v_mov_b32 v81, v36\n" \
  "v_mov_b32 v82, v33\n" \
  "v_mov_b32 v83, v37\n" \
  "v_mov_b32 v84, v34\n" \
  "v_mov_b32 v85, v38\n" \
  "v_mov_b32 v86, v35\n" \
  "v_mov_b32 v87, v39\n" \
  "ds_write_b128 v211, v[80:83] offset:8192\n" \
  "ds_write_b128 v211, v[84:87] offset:9216\n" \
  "s_waitcnt vmcnt(0)\n" \
  "s_branch .LR_fu_b_%=\n" \
  ".LR_nb0_%=:\n" \
  "s_waitcnt vmcnt(0)\n" \
  "s_branch .LR_nb1_%=\n" \
  ".LR_help_%=:\n" \
  "s_mul_i32 s60, %19, 5\n" \
  "s_sub_u32 s60, s60, 5\n" \
  "s_lshl_b32 s61, s60, 11\n" \
  "s_add_u32 s61, s61, 8192\n" \
  "v_add_u32 v211, s61, v214\n" \
  "s_lshr_b32 s61, s60, 2\n" \
  "s_lshl_b32 s61, s61, 14\n" \
  "s_and_b32 s62, s60, 3\n" \
  "s_lshl_b32 s62, s62, 11\n" \
  "s_xor_b32 s61, s61, s62\n" \
  "s_xor_b32 s64, s61, %25\n" \
  "s_add_u32 s60, s60, 1\n" \
  "s_lshr_b32 s61, s60, 2\n" \
  "s_lshl_b32 s61, s61, 14\n" \
  "s_and_b32 s62, s60, 3\n" \
  "s_lshl_b32 s62, s62, 11\n" \
  "s_xor_b32 s61, s61, s62\n" \
  "s_xor_b32 s65, s61, %25\n" \
  "s_add_u32 s60, s60, 1\n" \
  "s_lshr_b32 s61, s60, 2\n" \
  "s_lshl_b32 s61, s61, 14\n" \
  "s_and_b32 s62, s60, 3\n" \
  "s_lshl_b32 s62, s62, 11\n" \
  "s_xor_b32 s61, s61, s62\n" \
  "s_xor_b32 s67, s61, %25\n" \
  "s_add_u32 s60, s60, 1\n" \
  "s_lshr_b32 s61, s60, 2\n" \
  "s_lshl_b32 s61, s61, 14\n" \
  "s_and_b32 s62, s60, 3\n" \
  "s_lshl_b32 s62, s62, 11\n" \
  "s_xor_b32 s61, s61, s62\n" \
  "s_xor_b32 s68, s61, %25\n" \
  "s_add_u32 s60, s60, 1\n" \
  "s_lshr_b32 s61, s60, 2\n" \
  "s_lshl_b32 s61, s61, 14\n" \
  "s_and_b32 s62, s60, 3\n" \
  "s_lshl_b32 s62, s62, 11\n" \
  "s_xor_b32 s61, s61, s62\n" \
  "s_xor_b32 s74, s61, %25\n" \
  "s_add_u32 s60, s60, 1\n" \
  "s_lshr_b32 s61, s60, 2\n" \
  "s_lshl_b32 s61, s61, 14\n" \
  "s_and_b32 s62, s60, 3\n" \
  "s_lshl_b32 s62, s62, 11\n" \
  "s_xor_b32 s61, s61, s62\n" \
  "s_xor_b32 s75, s61, %25\n" \
  "s_add_u32 s60, s60, 1\n" \
  "s_xor_b32 s71, s56, s57\n" \
  "s_movk_i32 s88, -32768\n" \
  "v_mov_b64 v[136:137], -1\n" \
  "v_mov_b64 v[138:139], -1\n" \
  "v_mov_b64 v[140:141], 0\n" \
  "s_mov_b64 vcc, -1\n" \
  "s_barrier\n" \
  ".LR_hl_%=:\n" \
  "s_and_b32 s61, s48, 15\n" \
  "s_lshl_b32 s61, s61, 16\n" \
  "s_bfe_u32 s60, %32, 524288\n" \
  ".LR_hs_%=:\n" \
  "s_cmp_eq_u32 s60, 0\n" \
  "s_cbranch_scc1 .LR_hs_x_%=\n" \
  "s_sleep 1\n" \
  "s_sub_u32 s60, s60, 1\n" \
  "s_branch .LR_hs_%=\n" \
  ".LR_hs_x_%=:\n" \
  "s_xor_b32 s60, s71, s57\n" \
  "v_xor_b32 v145, s60, v224\n" \
  "buffer_store_dwordx4 v[136:139], v145, s[40:43], 0 offen sc1\n" \
  "s_cbranch_vccnz .LR_fu_%=\n" \
  "s_cmp_lg_u32 %19, 3\n" \
  "s_cbranch_scc1 .LR_h5_%=\n" \
  "s_xor_b32 s60, s61, s75\n" \
  "buffer_load_dwordx4 v[40:43], v214, s[40:43], s60 offen sc1\n" \
  "s_xor_b32 s60, s60, 1024\n" \
  "buffer_load_dwordx4 v[44:47], v214, s[40:43], s60 offen sc1\n" \
  ".LR_h5_%=:\n" \
  "s_xor_b32 s60, s61, s64\n" \
  "buffer_load_dwordx4 v[0:3], v214, s[40:43], s60 offen sc1\n" \
  "s_xor_b32 s60, s60, 1024\n" \
  "buffer_load_dwordx4 v[4:7], v214, s[40:43], s60 offen sc1\n" \
  "s_xor_b32 s60, s61, s65\n" \
  "buffer_load_dwordx4 v[8:11], v214, s[40:43], s60 offen sc1\n" \
  "s_xor_b32 s60, s60, 1024\n" \
  "buffer_load_dwordx4 v[12:15], v214, s[40:43], s60 offen sc1\n" \
  "s_xor_b32 s60, s61, s67\n" \
  "buffer_load_dwordx4 v[16:19], v214, s[40:43], s60 offen sc1\n" \
  "s_xor_b32 s60, s60, 1024\n" \
  "buffer_load_dwordx4 v[20:23], v214, s[40:43], s60 offen sc1\n" \
  "s_cmp_lt_u32 s48, 62\n" \
  "s_cselect_b32 s60, s71, 2130706432\n" \
  "v_xor_b32 v142, s60, v221\n" \
  "v_xor_b32 v143, s60, v222\n" \
  "v_xor_b32 v144, s60, v223\n" \
  "buffer_store_dwordx4 v[136:139], v142, s[40:43], 0 offen sc1\n" \
  "buffer_store_dwordx4 v[136:139], v143, s[40:43], 0 offen sc1\n" \
  "buffer_store_dwordx4 v[136:139], v144, s[40:43], 0 offen sc1\n" \
  "s_waitcnt vmcnt(9)\n" \
  "v_mov_b32 v72, v24\n" \
  "v_mov_b32 v73, v28\n" \
  "v_mov_b32 v74, v25\n" \
  "v_mov_b32 v75, v29\n" \
  "v_mov_b32 v76, v26\n" \
  "v_mov_b32 v77, v30\n" \
  "v_mov_b32 v78, v27\n" \
  "v_mov_b32 v79, v31\n" \
  "ds_write_b128 v211, v[72:75] offset:6144\n" \
  "ds_write_b128 v211, v[76:79] offset:7168\n" \
  "v_mov_b32 v80, v32\n" \
  "v_mov_b32 v81, v36\n" \
  "v_mov_b32 v82, v33\n" \
  "v_mov_b32 v83, v37\n" \
  "v_mov_b32 v84, v34\n" \
  "v_mov_b32 v85, v38\n" \
  "v_mov_b32 v86, v35\n" \
  "v_mov_b32 v87, v39\n" \
  "ds_write_b128 v211, v[80:83] offset:8192\n" \
  "ds_write_b128 v211, v[84:87] offset:9216\n" \
  "s_barrier\n" \
  ".LR_fu_b_%=:\n" \
  "s_add_u32 s48, s48, 1\n" \
  "v_cmp_ne_u32 vcc, %21, v141\n" \
  "s_nop 1\n" \
  "v_cndmask_b32 v140, v140, 0, vcc\n" \
  "v_max_i32 v140, v140, v218\n" \
  "v_cmp_ge_i32 vcc, s48, v140\n" \
  "s_and_b32 s61, s48, 15\n" \
  "s_lshl_b32 s61, s61, 16\n" \
  "s_cbranch_vccnz .LR_nb0_%=\n" \
  "s_bfe_u32 s60, %32, 524296\n" \
  ".LR_ht_%=:\n" \
  "s_cmp_eq_u32 s60, 0\n" \
  "s_cbranch_scc1 .LR_ht_x_%=\n" \
  "s_sleep 1\n" \
  "s_sub_u32 s60, s60, 1\n" \
  "s_branch .LR_ht_%=\n" \
  ".LR_ht_x_%=:\n" \
  "s_xor_b32 s60, s61, s68\n" \
  "buffer_load_dwordx4 v[24:27], v214, s[40:43], s60 offen sc1\n" \
  "s_xor_b32 s60, s60, 1024\n" \
  "buffer_load_dwordx4 v[28:31], v214, s[40:43], s60 offen sc1\n" \
  "s_xor_b32 s60, s61, s74\n" \
  "buffer_load_dwordx4 v[32:35], v214, s[40:43], s60 offen sc1\n" \
  "s_xor_b32 s60, s60, 1024\n" \
  "buffer_load_dwordx4 v[36:39], v214, s[40:43], s60 offen sc1\n" \
  "s_waitcnt vmcnt(4)\n" \
  ".LR_nb1_%=:\n" \
  "s_add_u32 s60, s48, 3\n" \
  "s_min_u32 s60, s60, 63\n" \
  "v_cmp_ge_i32 s[76:77], s60, v140\n" \
  "s_nop 1\n" \
  "s_cmp_eq_u64 s[76:77], 0\n" \
  "s_cbranch_scc1 .LR_nf_%=\n" \
  "s_add_u32 s60, s48, 1\n" \
  "s_lshl_b32 s62, s60, 7\n" \
  "s_add_u32 s62, s62, %33\n" \
  "v_mov_b32 v146, s62\n" \
  "s_sub_u32 s60, s60, 0\n" \
  "s_cmp_lt_u32 s60, 10\n" \
  "s_cselect_b64 s[76:77], -1, 0\n" \
  "v_cndmask_b32 v146, v220, v146, s[76:77]\n" \
  "buffer_load_dwordx2 v[140:141], v146, s[40:43], 0 offen sc1\n" \
  ".LR_nf_%=:\n" \
  "v_mov_b32 v48, v0\n" \
  "v_mov_b32 v49, v4\n" \
  "v_mov_b32 v50, v1\n" \
  "v_mov_b32 v51, v5\n" \
  "v_mov_b32 v52, v2\n" \
  "v_mov_b32 v53, v6\n" \
  "v_mov_b32 v54, v3\n" \
  "v_mov_b32 v55, v7\n" \
  "ds_write_b128 v211, v[48:51] offset:0\n" \
  "ds_write_b128 v211, v[52:55] offset:1024\n" \
  "v_mov_b32 v56, v8\n" \
  "v_mov_b32 v57, v12\n" \
  "v_mov_b32 v58, v9\n" \
  "v_mov_b32 v59, v13\n" \
  "v_mov_b32 v60, v10\n" \
  "v_mov_b32 v61, v14\n" \
  "v_mov_b32 v62, v11\n" \
  "v_mov_b32 v63, v15\n" \
  "ds_write_b128 v211, v[56:59] offset:2048\n" \
  "ds_write_b128 v211, v[60:63] offset:3072\n" \
  "v_mov_b32 v64, v16\n" \
  "v_mov_b32 v65, v20\n" \
  "v_mov_b32 v66, v17\n" \
  "v_mov_b32 v67, v21\n" \
  "v_mov_b32 v68, v18\n" \
  "v_mov_b32 v69, v22\n" \
  "v_mov_b32 v70, v19\n" \
  "v_mov_b32 v71, v23\n" \
  "ds_write_b128 v211, v[64:67] offset:4096\n" \
  "ds_write_b128 v211, v[68:71] offset:5120\n" \
  "s_cmp_lg_u32 %19, 3\n" \
  "s_cbranch_scc1 .LR_h6_%=\n" \
  "v_mov_b32 v88, v40\n" \
  "v_mov_b32 v89, v44\n" \
  "v_mov_b32 v90, v41\n" \
  "v_mov_b32 v91, v45\n" \
  "v_mov_b32 v92, v42\n" \
  "v_mov_b32 v93, v46\n" \
  "v_mov_b32 v94, v43\n" \
  "v_mov_b32 v95, v47\n" \
  "ds_write_b128 v211, v[88:91] offset:10240\n" \
  "ds_write_b128 v211, v[92:95] offset:11264\n" \
  ".LR_h6_%=:\n" \
  "s_sub_u32 s88, 0, s88\n" \
  "s_xor_b32 s71, s71, s56\n" \
  "s_mov_b32 s61, s56\n" \
  "s_mov_b32 s56, s57\n" \
  "s_mov_b32 s57, s61\n" \
  "s_waitcnt lgkmcnt(0)\n" \
  "v_add_u32 v211, s88, v211\n" \
  "s_barrier\n" \
  "s_cmp_lt_u32 s48, 63\n" \
  "s_cbranch_scc1 .LR_hl_%=\n" \
  "s_barrier\n" \
  ".LR_end_%=:\n" \
  "s_waitcnt vmcnt(0)\n"
#define R_ASM_VOPS "+{v212}"(ri.sidx), "+{v213}"(ri.cidx), "+{v214}"(ri.l16), "+{v215}"(ri.ax), "+{v216}"(ri.am), "+{v217}"(ri.mo), "+{v218}"(ri.sel), "+{v219}"(ri.to), "+{v220}"(ri.fu), "+{v221}"(ri.p0), "+{v222}"(ri.p1), "+{v223}"(ri.p2), "+{v224}"(ri.p3), "+{v225}"(ri.d0), "+{v226}"(ri.d1), "+{v227}"(ri.d2), "+{v228}"(ri.d3), "+{v229}"(ri.d4)
#define R_ASM_SOPS "s"(ri.ws), "s"(ri.w), "s"(ri.go), "s"(ri.tag), "s"(ri.o_va), "s"(ri.o_vb), "s"(ri.lab), "s"(ri.o_u), "s"(ri.kind), "s"(ri.o_ctl), "s"(ri.pa), "s"(ri.pb), "s"(ri.f_d), "s"(ri.f_r), "s"(ri.ph), "s"(ri.f_l)
#define R_ASM_CLOB "v0", "v1", "v2", "v3", "v4", "v5", "v6", "v7", "v8", "v9", "v10", "v11", "v12", "v13", "v14", "v15", "v16", "v17", "v18", "v19", "v20", "v21", "v22", "v23", "v24", "v25", "v26", "v27", "v28", "v29", "v30", "v31", "v32", "v33", "v34", "v35", "v36", "v37", "v38", "v39", "v40", "v41", "v42", "v43", "v44", "v45", "v46", "v47", "v48", "v49", "v50", "v51", "v52", "v53", "v54", "v55", "v56", "v57", "v58", "v59", "v60", "v61", "v62", "v63", "v64", "v65", "v66", "v67", "v68", "v69", "v70", "v71", "v72", "v73", "v74", "v75", "v76", "v77", "v78", "v79", "v80", "v81", "v82", "v83", "v84", "v85", "v86", "v87", "v88", "v89", "v90", "v91", "v92", "v93", "v94", "v95", "v96", "v97", "v98", "v99", "v100", "v101", "v102", "v103", "v104", "v105", "v106", "v107", "v108", "v109", "v110", "v111", "v112", "v113", "v114", "v115", "v116", "v117", "v118", "v119", "v120", "v121", "v122", "v123", "v124", "v125", "v126", "v127", "v128", "v129", "v130", "v131", "v132", "v133", "v134", "v135", "v136", "v137", "v138", "v139", "v140", "v141", "v142", "v143", "v144", "v145", "v146", "v147", "v148", "v149", "v150", "v151", "v152", "v153", "v154", "v155", "v156", "v157", "v158", "v159", "v160", "v161", "v162", "v163", "v164", "v165", "v166", "v167", "v168", "v169", "v170", "v171", "v172", "v173", "v174", "v175", "v176", "v177", "v178", "v179", "v180", "v181", "v182", "v183", "v184", "v185", "v186", "v187", "v188", "v189", "v190", "v191", "v192", "v193", "v194", "v195", "v196", "v197", "v198", "v199", "v200", "v201", "v202", "v203", "v204", "v205", "v206", "v207", "v208", "v209", "v210", "v211", "a0", "a1", "a2", "a3", "a4", "a5", "a6", "a7", "a8", "a9", "a10", "a11", "a12", "a13", "a14", "a15", "a16", "a17", "a18", "a19", "a20", "a21", "a22", "a23", "a24", "a25", "a26", "a27", "a28", "a29", "a30", "a31", "a32", "a33", "a34", "a35", "a36", "a37", "a38", "a39", "a40", "a41", "a42", "a43", "a44", "a45", "a46", "a47", "a48", "a49", "a50", "a51", "a52", "a53", "a54", "a55", "a56", "a57", "a58", "a59", "a60", "a61", "a62", "a63", "a64", "a65", "a66", "a67", "a68", "a69", "a70", "a71", "a72", "a73", "a74", "a75", "a76", "a77", "a78", "a79", "a80", "a81", "a82", "a83", "a84", "a85", "a86", "a87", "a88", "a89", "a90", "a91", "a92", "a93", "a94", "a95", "a96", "a97", "a98", "a99", "a100", "a101", "a102", "a103", "a104", "a105", "a106", "a107", "a108", "a109", "a110", "a111", "a112", "a113", "a114", "a115", "a116", "a117", "a118", "a119", "a120", "a121", "a122", "a123", "a124", "a125", "a126", "a127", "s40", "s41", "s42", "s43", "s44", "s45", "s46", "s47", "s48", "s49", "s50", "s51", "s52", "s53", "s54", "s55", "s56", "s57", "s58", "s59", "s60", "s61", "s62", "s63", "s64", "s65", "s66", "s67", "s68", "s69", "s70", "s71", "s72", "s73", "s74", "s75", "s76", "s77", "s78", "s79", "s80", "s81", "s82", "s83", "s84", "s85", "s86", "s87", "s88", "s89", "s90", "s91", "s92", "s93", "s94", "s95", "s96", "s97", "s98", "s99", "vcc", "scc", "memory"
#define K_ASM_TEXT \
  "s_mov_b64 s[40:41], %30\n" \
  "s_and_b32 s41, s41, 0xffff\n" \
  "s_mov_b32 s42, 255852544\n" \
  "s_mov_b32 s43, 0x00020000\n" \
  "s_mov_b32 s47, 0x00020000\n" \
  "s_lshl_b32 s72, %34, 12\n" \
  "buffer_load_dword v184, off, s[40:43], %42 sc1\n" \
  "s_mov_b64 s[44:45], %31\n" \
  "s_and_b32 s45, s45, 0xffff\n" \
  "s_mov_b32 s46, 4718592\n" \
  "s_add_u32 s60, %35, 0\n" \
  "s_add_u32 m0, s72, 16384\n" \
  "s_nop 0\n" \
  "buffer_load_dwordx4 v170, s[44:47], s60 offen lds\n" \
  "buffer_load_dwordx4 v171, s[44:47], s60 offen offset:1024 lds\n" \
  "buffer_load_dwordx4 v172, s[44:47], s60 offen offset:2048 lds\n" \
  "buffer_load_dwordx4 v173, s[44:47], s60 offen offset:3072 lds\n" \
  "s_add_u32 s60, %35, 524288\n" \
  "s_add_u32 m0, s72, 32768\n" \
  "s_nop 0\n" \
  "buffer_load_dwordx4 v170, s[44:47], s60 offen lds\n" \
  "buffer_load_dwordx4 v171, s[44:47], s60 offen offset:1024 lds\n" \
  "buffer_load_dwordx4 v172, s[44:47], s60 offen offset:2048 lds\n" \
  "buffer_load_dwordx4 v173, s[44:47], s60 offen offset:3072 lds\n" \
  "s_add_u32 s60, %35, 1048576\n" \
  "s_add_u32 m0, s72, 49152\n" \
  "s_nop 0\n" \
  "buffer_load_dwordx4 v170, s[44:47], s60 offen lds\n" \
  "buffer_load_dwordx4 v171, s[44:47], s60 offen offset:1024 lds\n" \
  "buffer_load_dwordx4 v172, s[44:47], s60 offen offset:2048 lds\n" \
  "buffer_load_dwordx4 v173, s[44:47], s60 offen offset:3072 lds\n" \
  "s_add_u32 s60, %35, 1572864\n" \
  "s_add_u32 m0, s72, 65536\n" \
  "s_nop 0\n" \
  "buffer_load_dwordx4 v170, s[44:47], s60 offen lds\n" \
  "buffer_load_dwordx4 v171, s[44:47], s60 offen offset:1024 lds\n" \
  "buffer_load_dwordx4 v172, s[44:47], s60 offen offset:2048 lds\n" \
  "buffer_load_dwordx4 v173, s[44:47], s60 offen offset:3072 lds\n" \
  "s_add_u32 s60, %35, 2097152\n" \
  "s_add_u32 m0, s72, 81920\n" \
  "s_nop 0\n" \
  "buffer_load_dwordx4 v170, s[44:47], s60 offen lds\n" \
  "buffer_load_dwordx4 v171, s[44:47], s60 offen offset:1024 lds\n" \
  "buffer_load_dwordx4 v172, s[44:47], s60 offen offset:2048 lds\n" \
  "buffer_load_dwordx4 v173, s[44:47], s60 offen offset:3072 lds\n" \
  "s_add_u32 s60, %35, 2621440\n" \
  "s_add_u32 m0, s72, 98304\n" \
  "s_nop 0\n" \
  "buffer_load_dwordx4 v170, s[44:47], s60 offen lds\n" \
  "buffer_load_dwordx4 v171, s[44:47], s60 offen offset:1024 lds\n" \
  "buffer_load_dwordx4 v172, s[44:47], s60 offen offset:2048 lds\n" \
  "buffer_load_dwordx4 v173, s[44:47], s60 offen offset:3072 lds\n" \
  "s_add_u32 s60, %35, 3145728\n" \
  "s_add_u32 m0, s72, 114688\n" \
  "s_nop 0\n" \
  "buffer_load_dwordx4 v170, s[44:47], s60 offen lds\n" \
  "buffer_load_dwordx4 v171, s[44:47], s60 offen offset:1024 lds\n" \
  "buffer_load_dwordx4 v172, s[44:47], s60 offen offset:2048 lds\n" \
  "buffer_load_dwordx4 v173, s[44:47], s60 offen offset:3072 lds\n" \
  "s_add_u32 s60, %35, 3670016\n" \
  "s_add_u32 m0, s72, 131072\n" \
  "s_nop 0\n" \
  "buffer_load_dwordx4 v170, s[44:47], s60 offen lds\n" \
  "buffer_load_dwordx4 v171, s[44:47], s60 offen offset:1024 lds\n" \
  "buffer_load_dwordx4 v172, s[44:47], s60 offen offset:2048 lds\n" \
  "buffer_load_dwordx4 v173, s[44:47], s60 offen offset:3072 lds\n" \
  "s_add_u32 s60, %35, 4194304\n" \
  "s_add_u32 m0, s72, 147456\n" \
  "s_nop 0\n" \
  "buffer_load_dwordx4 v170, s[44:47], s60 offen lds\n" \
  "buffer_load_dwordx4 v171, s[44:47], s60 offen offset:1024 lds\n" \
  "buffer_load_dwordx4 v172, s[44:47], s60 offen offset:2048 lds\n" \
  "buffer_load_dwordx4 v173, s[44:47], s60 offen offset:3072 lds\n" \
  "s_waitcnt vmcnt(24)\n" \
  "s_barrier\n" \
  "v_add_u32 v186, 16384, v182\n" \
  "ds_read_b64_tr_b16 a[0:1], v186\n" \
  "ds_read_b64_tr_b16 a[2:3], v186 offset:1024\n" \
  "ds_read_b64_tr_b16 a[4:5], v186 offset:8192\n" \
  "ds_read_b64_tr_b16 a[6:7], v186 offset:9216\n" \
  "v_add_u32 v187, 16384, v183\n" \
  "ds_read_b64_tr_b16 a[72:73], v187\n" \
  "ds_read_b64_tr_b16 a[74:75], v187 offset:1024\n" \
  "ds_read_b64_tr_b16 a[76:77], v187 offset:8192\n" \
  "ds_read_b64_tr_b16 a[78:79], v187 offset:9216\n" \
  "v_add_u32 v186, 32768, v182\n" \
  "ds_read_b64_tr_b16 a[8:9], v186\n" \
  "ds_read_b64_tr_b16 a[10:11], v186 offset:1024\n" \
  "ds_read_b64_tr_b16 a[12:13], v186 offset:8192\n" \
  "ds_read_b64_tr_b16 a[14:15], v186 offset:9216\n" \
  "v_add_u32 v187, 32768, v183\n" \
  "ds_read_b64_tr_b16 a[80:81], v187\n" \
  "ds_read_b64_tr_b16 a[82:83], v187 offset:1024\n" \
  "ds_read_b64_tr_b16 a[84:85], v187 offset:8192\n" \
  "ds_read_b64_tr_b16 a[86:87], v187 offset:9216\n" \
  "v_add_u32 v186, 49152, v182\n" \
  "ds_read_b64_tr_b16 a[16:17], v186\n" \
  "ds_read_b64_tr_b16 a[18:19], v186 offset:1024\n" \
  "ds_read_b64_tr_b16 a[20:21], v186 offset:8192\n" \
  "ds_read_b64_tr_b16 a[22:23], v186 offset:9216\n" \
  "v_add_u32 v187, 49152, v183\n" \
  "ds_read_b64_tr_b16 a[88:89], v187\n" \
  "ds_read_b64_tr_b16 a[90:91], v187 offset:1024\n" \
  "ds_read_b64_tr_b16 a[92:93], v187 offset:8192\n" \
  "ds_read_b64_tr_b16 a[94:95], v187 offset:9216\n" \
  "s_waitcnt lgkmcnt(0)\n" \
  "s_barrier\n" \
  "v_readfirstlane_b32 s55, v184\n" \
  "s_add_u32 s55, s55, 1\n" \
  "s_mov_b64 s[44:45], %33\n" \
  "s_and_b32 s45, s45, 0xffff\n" \
  "s_mov_b32 s46, 2359296\n" \
  "s_mov_b32 s51, 0\n" \
  "s_mul_i32 s52, %34, 9216\n" \
  "s_add_u32 s52, s52, 16384\n" \
  "s_add_u32 m0, s52, 0\n" \
  "s_nop 0\n" \
  "buffer_load_dwordx4 v196, s[44:47], s51 offen lds\n" \
  "s_add_u32 m0, s52, 1024\n" \
  "s_nop 0\n" \
  "buffer_load_dwordx4 v197, s[44:47], s51 offen lds\n" \
  "s_add_u32 m0, s52, 2048\n" \
  "s_nop 0\n" \
  "buffer_load_dwordx4 v198, s[44:47], s51 offen lds\n" \
  "s_add_u32 m0, s52, 3072\n" \
  "s_nop 0\n" \
  "buffer_load_dwordx4 v199, s[44:47], s51 offen lds\n" \
  "s_add_u32 m0, s52, 4096\n" \
  "s_nop 0\n" \
  "buffer_load_dwordx4 v200, s[44:47], s51 offen lds\n" \
  "s_add_u32 m0, s52, 5120\n" \
  "s_nop 0\n" \
  "buffer_load_dwordx4 v201, s[44:47], s51 offen lds\n" \
  "s_add_u32 m0, s52, 6144\n" \
  "s_nop 0\n" \
  "buffer_load_dwordx4 v202, s[44:47], s51 offen lds\n" \
  "s_add_u32 m0, s52, 7168\n" \
  "s_nop 0\n" \
  "buffer_load_dwordx4 v203, s[44:47], s51 offen lds\n" \
  "s_add_u32 m0, s52, 8192\n" \
  "s_nop 0\n" \
  "buffer_load_dwordx4 v204, s[44:47], s51 offen lds\n" \
  "s_add_u32 s51, s51, 36864\n" \
  "s_min_u32 s51, s51, 2322432\n" \
  "s_add_u32 s52, s52, 36864\n" \
  "s_cmp_ge_u32 s52, 163840\n" \
  "s_cselect_b32 s60, 147456, 0\n" \
  "s_sub_u32 s52, s52, s60\n" \
  "s_waitcnt vmcnt(21)\n" \
  "s_barrier\n" \
  "v_add_u32 v186, 65536, v182\n" \
  "ds_read_b64_tr_b16 a[24:25], v186\n" \
  "ds_read_b64_tr_b16 a[26:27], v186 offset:1024\n" \
  "ds_read_b64_tr_b16 a[28:29], v186 offset:8192\n" \
  "ds_read_b64_tr_b16 a[30:31], v186 offset:9216\n" \
  "v_add_u32 v187, 65536, v183\n" \
  "ds_read_b64_tr_b16 a[96:97], v187\n" \
  "ds_read_b64_tr_b16 a[98:99], v187 offset:1024\n" \
  "ds_read_b64_tr_b16 a[100:101], v187 offset:8192\n" \
  "ds_read_b64_tr_b16 a[102:103], v187 offset:9216\n" \
  "v_add_u32 v186, 81920, v182\n" \
  "ds_read_b64_tr_b16 a[32:33], v186\n" \
  "ds_read_b64_tr_b16 a[34:35], v186 offset:1024\n" \
  "ds_read_b64_tr_b16 a[36:37], v186 offset:8192\n" \
  "ds_read_b64_tr_b16 a[38:39], v186 offset:9216\n" \
  "v_add_u32 v187, 81920, v183\n" \
  "ds_read_b64_tr_b16 a[104:105], v187\n" \
  "ds_read_b64_tr_b16 a[106:107], v187 offset:1024\n" \
  "ds_read_b64_tr_b16 a[108:109], v187 offset:8192\n" \
  "ds_read_b64_tr_b16 a[110:111], v187 offset:9216\n" \
  "v_add_u32 v186, 98304, v182\n" \
  "ds_read_b64_tr_b16 a[40:41], v186\n" \
  "ds_read_b64_tr_b16 a[42:43], v186 offset:1024\n" \
  "ds_read_b64_tr_b16 a[44:45], v186 offset:8192\n" \
  "ds_read_b64_tr_b16 a[46:47], v186 offset:9216\n" \
  "v_add_u32 v187, 98304, v183\n" \
  "ds_read_b64_tr_b16 a[112:113], v187\n" \
  "ds_read_b64_tr_b16 a[114:115], v187 offset:1024\n" \
  "ds_read_b64_tr_b16 a[116:117], v187 offset:8192\n" \
  "ds_read_b64_tr_b16 a[118:119], v187 offset:9216\n" \
  "s_waitcnt lgkmcnt(0)\n" \
  "s_barrier\n" \
  "buffer_load_dwordx2 v[184:185], off, s[40:43], %41 sc1\n" \
  "s_add_u32 m0, s52, 0\n" \
  "s_nop 0\n" \
  "buffer_load_dwordx4 v196, s[44:47], s51 offen lds\n" \
  "s_add_u32 m0, s52, 1024\n" \
  "s_nop 0\n" \
  "buffer_load_dwordx4 v197, s[44:47], s51 offen lds\n" \
  "s_add_u32 m0, s52, 2048\n" \
  "s_nop 0\n" \
  "buffer_load_dwordx4 v198, s[44:47], s51 offen lds\n" \
  "s_add_u32 m0, s52, 3072\n" \
  "s_nop 0\n" \
  "buffer_load_dwordx4 v199, s[44:47], s51 offen lds\n" \
  "s_add_u32 m0, s52, 4096\n" \
  "s_nop 0\n" \
  "buffer_load_dwordx4 v200, s[44:47], s51 offen lds\n" \
  "s_add_u32 m0, s52, 5120\n" \
  "s_nop 0\n" \
  "buffer_load_dwordx4 v201, s[44:47], s51 offen lds\n" \
  "s_add_u32 m0, s52, 6144\n" \
  "s_nop 0\n" \
  "buffer_load_dwordx4 v202, s[44:47], s51 offen lds\n" \
  "s_add_u32 m0, s52, 7168\n" \
  "s_nop 0\n" \
  "buffer_load_dwordx4 v203, s[44:47], s51 offen lds\n" \
  "s_add_u32 m0, s52, 8192\n" \
  "s_nop 0\n" \
  "buffer_load_dwordx4 v204, s[44:47], s51 offen lds\n" \
  "s_add_u32 s51, s51, 36864\n" \
  "s_min_u32 s51, s51, 2322432\n" \
  "s_add_u32 s52, s52, 36864\n" \
  "s_cmp_ge_u32 s52, 163840\n" \
  "s_cselect_b32 s60, 147456, 0\n" \
  "s_sub_u32 s52, s52, s60\n" \
  "s_waitcnt vmcnt(19)\n" \
  "s_barrier\n" \
  "v_add_u32 v186, 114688, v182\n" \
  "ds_read_b64_tr_b16 a[48:49], v186\n" \
  "ds_read_b64_tr_b16 a[50:51], v186 offset:1024\n" \
  "ds_read_b64_tr_b16 a[52:53], v186 offset:8192\n" \
  "ds_read_b64_tr_b16 a[54:55], v186 offset:9216\n" \
  "v_add_u32 v187, 114688, v183\n" \
  "ds_read_b64_tr_b16 a[120:121], v187\n" \
  "ds_read_b64_tr_b16 a[122:123], v187 offset:1024\n" \
  "ds_read_b64_tr_b16 a[124:125], v187 offset:8192\n" \
  "ds_read_b64_tr_b16 a[126:127], v187 offset:9216\n" \
  "v_add_u32 v186, 131072, v182\n" \
  "ds_read_b64_tr_b16 a[56:57], v186\n" \
  "ds_read_b64_tr_b16 a[58:59], v186 offset:1024\n" \
  "ds_read_b64_tr_b16 a[60:61], v186 offset:8192\n" \
  "ds_read_b64_tr_b16 a[62:63], v186 offset:9216\n" \
  "v_add_u32 v187, 131072, v183\n" \
  "ds_read_b64_tr_b16 a[128:129], v187\n" \
  "ds_read_b64_tr_b16 a[130:131], v187 offset:1024\n" \
  "ds_read_b64_tr_b16 a[132:133], v187 offset:8192\n" \
  "ds_read_b64_tr_b16 a[134:135], v187 offset:9216\n" \
  "v_add_u32 v186, 147456, v182\n" \
  "ds_read_b64_tr_b16 a[64:65], v186\n" \
  "ds_read_b64_tr_b16 a[66:67], v186 offset:1024\n" \
  "ds_read_b64_tr_b16 a[68:69], v186 offset:8192\n" \
  "ds_read_b64_tr_b16 a[70:71], v186 offset:9216\n" \
  "v_add_u32 v187, 147456, v183\n" \
  "ds_read_b64_tr_b16 a[136:137], v187\n" \
  "ds_read_b64_tr_b16 a[138:139], v187 offset:1024\n" \
  "ds_read_b64_tr_b16 a[140:141], v187 offset:8192\n" \
  "ds_read_b64_tr_b16 a[142:143], v187 offset:9216\n" \
  "s_waitcnt lgkmcnt(0)\n" \
  "s_mov_b32 s69, 0\n" \
  "s_waitcnt vmcnt(9)\n" \
  "s_branch .LK_go1_%=\n" \
  ".LK_go_%=:\n" \
  "buffer_load_dwordx2 v[184:185], off, s[40:43], %41 sc1\n" \
  "s_waitcnt vmcnt(0)\n" \
  ".LK_go1_%=:\n" \
  "v_readfirstlane_b32 s96, v184\n" \
  "v_readfirstlane_b32 s63, v185\n" \
  "s_cmp_eq_u32 s63, s55\n" \
  "s_cselect_b32 s96, s96, 0\n" \
  "s_cmp_ge_u32 s96, 0x80000000\n" \
  "s_cbranch_scc1 .LK_go_x_%=\n" \
  "s_sleep 4\n" \
  "s_add_u32 s69, s69, 1\n" \
  "s_cmp_lt_u32 s69, 2097152\n" \
  "s_cbranch_scc1 .LK_go_%=\n" \
  "v_mov_b32 v184, 1\n" \
  "v_mov_b32 v185, s55\n" \
  "buffer_store_dwordx2 v[184:185], off, s[40:43], %42 offset:128 sc1\n" \
  ".LK_go_x_%=:\n" \
  "s_and_b32 s60, s96, 2\n" \
  "s_lshl_b32 s60, s60, 12\n" \
  "s_xor_b32 s53, %39, s60\n" \
  "s_mov_b32 s54, %40\n" \
  "s_andn2_b32 s60, 2, s96\n" \
  "s_lshl_b32 s60, s60, 12\n" \
  "s_add_u32 s49, %37, s60\n" \
  "s_add_u32 s50, %38, s60\n" \
  "s_xor_b32 s66, %38, s60\n" \
  "s_xor_b32 s59, %44, s60\n" \
  "s_or_b32 s50, s50, 0x40000000\n" \
  "s_mov_b32 s57, 2130706432\n" \
  "s_mov_b32 s48, 0\n" \
  "v_mov_b32 v169, s55\n" \
  "v_mov_b32 v167, 2130706432\n" \
  "v_mov_b32 v163, 2130706432\n" \
  "v_mov_b32 v166, -1\n" \
  "s_mov_b64 exec, 3\n" \
  "v_mov_b32 v167, s54\n" \
  "s_mov_b64 exec, 1\n" \
  "v_mov_b32 v167, s53\n" \
  "v_mov_b32 v163, s53\n" \
  "v_mov_b32 v166, 0\n" \
  "s_mov_b64 exec, -1\n" \
  "s_barrier\n" \
  "ds_read_b128 v[32:35], v191\n" \
  "ds_read_b128 v[36:39], v191 offset:18432\n" \
  "ds_read_b128 v[40:43], v192\n" \
  "ds_read_b128 v[44:47], v192 offset:18432\n" \
  "ds_read_b128 v[48:51], v191 offset:128\n" \
  "ds_read_b128 v[52:55], v191 offset:18560\n" \
  "s_waitcnt lgkmcnt(0)\n" \
  "s_sub_u32 s49, s49, 524288\n" \
  "s_or_b32 s49, s49, 0x40000000\n" \
  "s_mov_b32 s56, -1\n" \
  "s_mov_b32 s57, 0x10000\n" \
  "s_mov_b32 s71, 36864\n" \
  ".LK_ma_%=:\n" \
  "s_waitcnt lgkmcnt(5)\n" \
  "v_mfma_f32_16x16x32_bf16 v[64:67], a[0:3], v[32:35], 0\n" \
  "ds_read_b128 v[56:59], v192 offset:128\n" \
  "s_waitcnt lgkmcnt(5)\n" \
  "v_mfma_f32_16x16x32_bf16 v[72:75], a[0:3], v[36:39], 0\n" \
  "ds_read_b128 v[60:63], v192 offset:18560\n" \
  "s_lshr_b32 s62, s49, 8\n" \
  "v_mfma_f32_16x16x32_bf16 v[68:71], a[72:75], v[32:35], 0\n" \
  "s_add_u32 m0, s52, 0\n" \
  "s_and_b32 s62, s62, 8192\n" \
  "s_xor_b32 s62, s62, s49\n" \
  "v_mfma_f32_16x16x32_bf16 v[76:79], a[72:75], v[36:39], 0\n" \
  "s_waitcnt lgkmcnt(5)\n" \
  "buffer_load_dwordx4 v196, s[44:47], s51 offen lds\n" \
  "v_mfma_f32_16x16x32_bf16 v[64:67], a[4:7], v[40:43], v[64:67]\n" \
  "ds_read_b128 v[32:35], v191 offset:256\n" \
  "s_waitcnt lgkmcnt(5)\n" \
  "v_cvt_pk_bf16_f32 v96, v80, v81\n" \
  "v_mfma_f32_16x16x32_bf16 v[72:75], a[4:7], v[44:47], v[72:75]\n" \
  "ds_read_b128 v[36:39], v191 offset:18688\n" \
  "v_cvt_pk_bf16_f32 v97, v82, v83\n" \
  "v_mfma_f32_16x16x32_bf16 v[68:71], a[76:79], v[40:43], v[68:71]\n" \
  "s_lshl_b32 s63, s57, 14\n" \
  "v_cvt_pk_bf16_f32 v98, v84, v85\n" \
  "v_cvt_pk_bf16_f32 v99, v86, v87\n" \
  "v_mfma_f32_16x16x32_bf16 v[76:79], a[76:79], v[44:47], v[76:79]\n" \
  "s_xor_b32 s63, s63, s59\n" \
  "s_waitcnt lgkmcnt(5)\n" \
  "v_mfma_f32_16x16x32_bf16 v[64:67], a[8:11], v[48:51], v[64:67]\n" \
  "ds_read_b128 v[40:43], v192 offset:256\n" \
  "s_waitcnt lgkmcnt(5)\n" \
  "v_mfma_f32_16x16x32_bf16 v[72:75], a[8:11], v[52:55], v[72:75]\n" \
  "ds_read_b128 v[44:47], v192 offset:18688\n" \
  "v_mfma_f32_16x16x32_bf16 v[68:71], a[80:83], v[48:51], v[68:71]\n" \
  "s_add_u32 m0, s52, 1024\n" \
  "s_waitcnt lgkmcnt(5)\n" \
  "v_mfma_f32_16x16x32_bf16 v[76:79], a[80:83], v[52:55], v[76:79]\n" \
  "buffer_load_dwordx4 v197, s[44:47], s51 offen lds\n" \
  "v_mfma_f32_16x16x32_bf16 v[64:67], a[12:15], v[56:59], v[64:67]\n" \
  "ds_read_b128 v[48:51], v191 offset:384\n" \
  "s_waitcnt lgkmcnt(5)\n" \
  "v_mfma_f32_16x16x32_bf16 v[72:75], a[12:15], v[60:63], v[72:75]\n" \
  "ds_read_b128 v[52:55], v191 offset:18816\n" \
  "v_mfma_f32_16x16x32_bf16 v[68:71], a[84:87], v[56:59], v[68:71]\n" \
  "buffer_store_dwordx4 v[96:99], v190, s[40:43], s62 offen sc1\n" \
  "v_mfma_f32_16x16x32_bf16 v[76:79], a[84:87], v[60:63], v[76:79]\n" \
  "s_waitcnt lgkmcnt(5)\n" \
  "buffer_store_dwordx4 v[96:99], v190, s[40:43], s63 offen sc1\n" \
  "v_mfma_f32_16x16x32_bf16 v[64:67], a[16:19], v[32:35], v[64:67]\n" \
  "ds_read_b128 v[56:59], v192 offset:384\n" \
  "s_waitcnt lgkmcnt(5)\n" \
  "v_cvt_pk_bf16_f32 v100, v88, v89\n" \
  "v_mfma_f32_16x16x32_bf16 v[72:75], a[16:19], v[36:39], v[72:75]\n" \
  "ds_read_b128 v[60:63], v192 offset:18816\n" \
  "v_cvt_pk_bf16_f32 v101, v90, v91\n" \
  "v_mfma_f32_16x16x32_bf16 v[68:71], a[88:91], v[32:35], v[68:71]\n" \
  "s_add_u32 m0, s52, 2048\n" \
  "v_cvt_pk_bf16_f32 v102, v92, v93\n" \
  "v_cvt_pk_bf16_f32 v103, v94, v95\n" \
  "v_mfma_f32_16x16x32_bf16 v[76:79], a[88:91], v[36:39], v[76:79]\n" \
  "s_waitcnt lgkmcnt(5)\n" \
  "buffer_load_dwordx4 v198, s[44:47], s51 offen lds\n" \
  "v_mfma_f32_16x16x32_bf16 v[64:67], a[20:23], v[40:43], v[64:67]\n" \
  "ds_read_b128 v[32:35], v191 offset:512\n" \
  "s_waitcnt lgkmcnt(5)\n" \
  "v_mfma_f32_16x16x32_bf16 v[72:75], a[20:23], v[44:47], v[72:75]\n" \
  "ds_read_b128 v[36:39], v191 offset:18944\n" \
  "v_mfma_f32_16x16x32_bf16 v[68:71], a[92:95], v[40:43], v[68:71]\n" \
  "buffer_store_dwordx4 v[100:103], v190, s[40:43], s62 offen offset:1024 sc1\n" \
  "v_mfma_f32_16x16x32_bf16 v[76:79], a[92:95], v[44:47], v[76:79]\n" \
  "s_waitcnt lgkmcnt(5)\n" \
  "buffer_store_dwordx4 v[100:103], v190, s[40:43], s63 offen offset:1024 sc1\n" \
  "v_mfma_f32_16x16x32_bf16 v[64:67], a[24:27], v[48:51], v[64:67]\n" \
  "ds_read_b128 v[40:43], v192 offset:512\n" \
  "s_add_u32 s49, s49, 524288\n" \
  "s_waitcnt lgkmcnt(5)\n" \
  "v_mfma_f32_16x16x32_bf16 v[72:75], a[24:27], v[52:55], v[72:75]\n" \
  "ds_read_b128 v[44:47], v192 offset:18944\n" \
  "s_bitset0_b32 s49, 30\n" \
  "v_mfma_f32_16x16x32_bf16 v[68:71], a[96:99], v[48:51], v[68:71]\n" \
  "s_add_u32 m0, s52, 3072\n" \
  "s_waitcnt lgkmcnt(5)\n" \
  "v_mfma_f32_16x16x32_bf16 v[76:79], a[96:99], v[52:55], v[76:79]\n" \
  "buffer_load_dwordx4 v199, s[44:47], s51 offen lds\n" \
  "v_mfma_f32_16x16x32_bf16 v[64:67], a[28:31], v[56:59], v[64:67]\n" \
  "ds_read_b128 v[48:51], v191 offset:640\n" \
  "s_waitcnt lgkmcnt(5)\n" \
  "v_mfma_f32_16x16x32_bf16 v[72:75], a[28:31], v[60:63], v[72:75]\n" \
  "ds_read_b128 v[52:55], v191 offset:19072\n" \
  "v_mfma_f32_16x16x32_bf16 v[68:71], a[100:103], v[56:59], v[68:71]\n" \
  "s_cmp_eq_u32 s48, 20\n" \
  "s_cselect_b32 s60, 19922944, 0\n" \
  "s_add_u32 s49, s49, s60\n" \
  "v_mfma_f32_16x16x32_bf16 v[76:79], a[100:103], v[60:63], v[76:79]\n" \
  "s_add_u32 s58, s48, 1\n" \
  "s_and_b32 s58, s58, 3\n" \
  "s_cselect_b32 s58, 0, 147456\n" \
  "s_waitcnt lgkmcnt(5)\n" \
  "v_mfma_f32_16x16x32_bf16 v[64:67], a[32:35], v[32:35], v[64:67]\n" \
  "ds_read_b128 v[56:59], v192 offset:640\n" \
  "s_sub_u32 s58, 36864, s58\n" \
  "s_waitcnt lgkmcnt(5)\n" \
  "v_mfma_f32_16x16x32_bf16 v[72:75], a[32:35], v[36:39], v[72:75]\n" \
  "ds_read_b128 v[60:63], v192 offset:19072\n" \
  "v_add_u32 v165, s58, v191\n" \
  "v_mfma_f32_16x16x32_bf16 v[68:71], a[104:107], v[32:35], v[68:71]\n" \
  "s_add_u32 m0, s52, 4096\n" \
  "v_add_u32 v164, s58, v192\n" \
  "v_mfma_f32_16x16x32_bf16 v[76:79], a[104:107], v[36:39], v[76:79]\n" \
  "s_waitcnt lgkmcnt(5)\n" \
  "buffer_load_dwordx4 v200, s[44:47], s51 offen lds\n" \
  "v_mfma_f32_16x16x32_bf16 v[64:67], a[36:39], v[40:43], v[64:67]\n" \
  "ds_read_b128 v[32:35], v191 offset:768\n" \
  "s_waitcnt lgkmcnt(5)\n" \
  "v_mfma_f32_16x16x32_bf16 v[72:75], a[36:39], v[44:47], v[72:75]\n" \
  "ds_read_b128 v[36:39], v191 offset:19200\n" \
  "v_mfma_f32_16x16x32_bf16 v[68:71], a[108:111], v[40:43], v[68:71]\n" \
  "v_mfma_f32_16x16x32_bf16 v[76:79], a[108:111], v[44:47], v[76:79]\n" \
  "s_waitcnt lgkmcnt(5)\n" \
  "v_mfma_f32_16x16x32_bf16 v[64:67], a[40:43], v[48:51], v[64:67]\n" \
  "ds_read_b128 v[40:43], v192 offset:768\n" \
  "s_waitcnt lgkmcnt(5)\n" \
  "v_mfma_f32_16x16x32_bf16 v[72:75], a[40:43], v[52:55], v[72:75]\n" \
  "ds_read_b128 v[44:47], v192 offset:19200\n" \
  "v_mfma_f32_16x16x32_bf16 v[68:71], a[112:115], v[48:51], v[68:71]\n" \
  "s_add_u32 m0, s52, 5120\n" \
  "s_waitcnt lgkmcnt(5)\n" \
  "v_mfma_f32_16x16x32_bf16 v[76:79], a[112:115], v[52:55], v[76:79]\n" \
  "buffer_load_dwordx4 v201, s[44:47], s51 offen lds\n" \
  "v_mfma_f32_16x16x32_bf16 v[64:67], a[44:47], v[56:59], v[64:67]\n" \
  "ds_read_b128 v[48:51], v191 offset:896\n" \
  "s_waitcnt lgkmcnt(5)\n" \
  "v_mfma_f32_16x16x32_bf16 v[72:75], a[44:47], v[60:63], v[72:75]\n" \
  "ds_read_b128 v[52:55], v191 offset:19328\n" \
  "v_mfma_f32_16x16x32_bf16 v[68:71], a[116:119], v[56:59], v[68:71]\n" \
  "v_mfma_f32_16x16x32_bf16 v[76:79], a[116:119], v[60:63], v[76:79]\n" \
  "s_waitcnt lgkmcnt(5)\n" \
  "v_mfma_f32_16x16x32_bf16 v[64:67], a[48:51], v[32:35], v[64:67]\n" \
  "ds_read_b128 v[56:59], v192 offset:896\n" \
  "s_waitcnt lgkmcnt(5)\n" \
  "v_mfma_f32_16x16x32_bf16 v[72:75], a[48:51], v[36:39], v[72:75]\n" \
  "ds_read_b128 v[60:63], v192 offset:19328\n" \
  "v_mfma_f32_16x16x32_bf16 v[68:71], a[120:123], v[32:35], v[68:71]\n" \
  "s_add_u32 m0, s52, 6144\n" \
  "v_mov_b32 v168, s56\n" \
  "v_mfma_f32_16x16x32_bf16 v[76:79], a[120:123], v[36:39], v[76:79]\n" \
  "s_waitcnt lgkmcnt(5)\n" \
  "buffer_load_dwordx4 v202, s[44:47], s51 offen lds\n" \
  "v_mfma_f32_16x16x32_bf16 v[64:67], a[52:55], v[40:43], v[64:67]\n" \
  "ds_read_b128 v[32:35], v191 offset:1024\n" \
  "s_waitcnt lgkmcnt(5)\n" \
  "v_mfma_f32_16x16x32_bf16 v[72:75], a[52:55], v[44:47], v[72:75]\n" \
  "ds_read_b128 v[36:39], v191 offset:19456\n" \
  "v_mfma_f32_16x16x32_bf16 v[68:71], a[124:127], v[40:43], v[68:71]\n" \
  "s_waitcnt vmcnt(11)\n" \
  "s_barrier\n" \
  "v_mfma_f32_16x16x32_bf16 v[76:79], a[124:127], v[44:47], v[76:79]\n" \
  "s_waitcnt lgkmcnt(5)\n" \
  "buffer_store_dwordx2 v[168:169], v163, s[40:43], 0 offen sc1\n" \
  "v_mfma_f32_16x16x32_bf16 v[64:67], a[56:59], v[48:51], v[64:67]\n" \
  "ds_read_b128 v[40:43], v192 offset:1024\n" \
  "s_waitcnt lgkmcnt(5)\n" \
  "v_mfma_f32_16x16x32_bf16 v[72:75], a[56:59], v[52:55], v[72:75]\n" \
  "ds_read_b128 v[44:47], v192 offset:19456\n" \
  "v_mfma_f32_16x16x32_bf16 v[68:71], a[128:131], v[48:51], v[68:71]\n" \
  "s_add_u32 m0, s52, 7168\n" \
  "s_waitcnt lgkmcnt(5)\n" \
  "v_mfma_f32_16x16x32_bf16 v[76:79], a[128:131], v[52:55], v[76:79]\n" \
  "buffer_load_dwordx4 v203, s[44:47], s51 offen lds\n" \
  "v_mfma_f32_16x16x32_bf16 v[64:67], a[60:63], v[56:59], v[64:67]\n" \
  "ds_read_b128 v[48:51], v165\n" \
  "s_waitcnt lgkmcnt(5)\n" \
  "v_mfma_f32_16x16x32_bf16 v[72:75], a[60:63], v[60:63], v[72:75]\n" \
  "ds_read_b128 v[52:55], v165 offset:18432\n" \
  "v_mfma_f32_16x16x32_bf16 v[68:71], a[132:135], v[56:59], v[68:71]\n" \
  "s_min_u32 s56, s48, 20\n" \
  "s_cselect_b32 s57, 0, 38\n" \
  "s_add_u32 s57, s57, s48\n" \
  "v_mfma_f32_16x16x32_bf16 v[76:79], a[132:135], v[60:63], v[76:79]\n" \
  "s_add_u32 s48, s48, 1\n" \
  "v_mov_b32 v191, v165\n" \
  "v_mov_b32 v192, v164\n" \
  "s_waitcnt lgkmcnt(5)\n" \
  "v_mfma_f32_16x16x32_bf16 v[64:67], a[64:67], v[32:35], v[64:67]\n" \
  "ds_read_b128 v[56:59], v164\n" \
  "s_waitcnt lgkmcnt(5)\n" \
  "v_mfma_f32_16x16x32_bf16 v[72:75], a[64:67], v[36:39], v[72:75]\n" \
  "ds_read_b128 v[60:63], v164 offset:18432\n" \
  "v_mfma_f32_16x16x32_bf16 v[68:71], a[136:139], v[32:35], v[68:71]\n" \
  "s_add_u32 m0, s52, 8192\n" \
  "s_cmp_eq_u32 s51, 700416\n" \
  "s_cselect_b32 s70, 1437696, s71\n" \
  "v_mfma_f32_16x16x32_bf16 v[76:79], a[136:139], v[36:39], v[76:79]\n" \
  "s_waitcnt lgkmcnt(5)\n" \
  "buffer_load_dwordx4 v204, s[44:47], s51 offen lds\n" \
  "v_mfma_f32_16x16x32_bf16 v[64:67], a[68:71], v[40:43], v[64:67]\n" \
  "ds_read_b128 v[32:35], v165 offset:128\n" \
  "s_add_u32 s51, s51, s70\n" \
  "s_waitcnt lgkmcnt(5)\n" \
  "v_mfma_f32_16x16x32_bf16 v[72:75], a[68:71], v[44:47], v[72:75]\n" \
  "ds_read_b128 v[36:39], v165 offset:18560\n" \
  "s_min_u32 s51, s51, 2322432\n" \
  "v_mfma_f32_16x16x32_bf16 v[68:71], a[140:143], v[40:43], v[68:71]\n" \
  "s_add_u32 s52, s52, 36864\n" \
  "v_mfma_f32_16x16x32_bf16 v[76:79], a[140:143], v[44:47], v[76:79]\n" \
  "s_cmp_ge_u32 s52, 163840\n" \
  "s_cselect_b32 s60, 147456, 0\n" \
  "s_sub_u32 s52, s52, s60\n" \
  "s_cmp_le_u32 s48, 4\n" \
  "s_cbranch_scc1 .LK_mea_%=\n" \
  ".LK_mea_b_%=:\n" \
  "s_waitcnt lgkmcnt(5)\n" \
  "v_mfma_f32_16x16x32_bf16 v[80:83], a[0:3], v[48:51], 0\n" \
  "ds_read_b128 v[40:43], v192 offset:128\n" \
  "s_waitcnt lgkmcnt(5)\n" \
  "v_mfma_f32_16x16x32_bf16 v[88:91], a[0:3], v[52:55], 0\n" \
  "ds_read_b128 v[44:47], v192 offset:18560\n" \
  "s_lshr_b32 s62, s49, 8\n" \
  "v_mfma_f32_16x16x32_bf16 v[84:87], a[72:75], v[48:51], 0\n" \
  "s_add_u32 m0, s52, 0\n" \
  "s_and_b32 s62, s62, 8192\n" \
  "s_xor_b32 s62, s62, s49\n" \
  "v_mfma_f32_16x16x32_bf16 v[92:95], a[72:75], v[52:55], 0\n" \
  "s_waitcnt lgkmcnt(5)\n" \
  "buffer_load_dwordx4 v196, s[44:47], s51 offen lds\n" \
  "v_mfma_f32_16x16x32_bf16 v[80:83], a[4:7], v[56:59], v[80:83]\n" \
  "ds_read_b128 v[48:51], v191 offset:256\n" \
  "s_waitcnt lgkmcnt(5)\n" \
  "v_cvt_pk_bf16_f32 v96, v64, v65\n" \
  "v_mfma_f32_16x16x32_bf16 v[88:91], a[4:7], v[60:63], v[88:91]\n" \
  "ds_read_b128 v[52:55], v191 offset:18688\n" \
  "v_cvt_pk_bf16_f32 v97, v66, v67\n" \
  "v_mfma_f32_16x16x32_bf16 v[84:87], a[76:79], v[56:59], v[84:87]\n" \
  "s_lshl_b32 s63, s57, 14\n" \
  "v_cvt_pk_bf16_f32 v98, v68, v69\n" \
  "v_cvt_pk_bf16_f32 v99, v70, v71\n" \
  "v_mfma_f32_16x16x32_bf16 v[92:95], a[76:79], v[60:63], v[92:95]\n" \
  "s_xor_b32 s63, s63, s59\n" \
  "s_waitcnt lgkmcnt(5)\n" \
  "v_mfma_f32_16x16x32_bf16 v[80:83], a[8:11], v[32:35], v[80:83]\n" \
  "ds_read_b128 v[56:59], v192 offset:256\n" \
  "s_waitcnt lgkmcnt(5)\n" \
  "v_mfma_f32_16x16x32_bf16 v[88:91], a[8:11], v[36:39], v[88:91]\n" \
  "ds_read_b128 v[60:63], v192 offset:18688\n" \
  "v_mfma_f32_16x16x32_bf16 v[84:87], a[80:83], v[32:35], v[84:87]\n" \
  "s_add_u32 m0, s52, 1024\n" \
  "s_waitcnt lgkmcnt(5)\n" \
  "v_mfma_f32_16x16x32_bf16 v[92:95], a[80:83], v[36:39], v[92:95]\n" \
  "buffer_load_dwordx4 v197, s[44:47], s51 offen lds\n" \
  "v_mfma_f32_16x16x32_bf16 v[80:83], a[12:15], v[40:43], v[80:83]\n" \
  "ds_read_b128 v[32:35], v191 offset:384\n" \
  "s_waitcnt lgkmcnt(5)\n" \
  "v_mfma_f32_16x16x32_bf16 v[88:91], a[12:15], v[44:47], v[88:91]\n" \
  "ds_read_b128 v[36:39], v191 offset:18816\n" \
  "v_mfma_f32_16x16x32_bf16 v[84:87], a[84:87], v[40:43], v[84:87]\n" \
  "buffer_store_dwordx4 v[96:99], v190, s[40:43], s62 offen sc1\n" \
  "v_mfma_f32_16x16x32_bf16 v[92:95], a[84:87], v[44:47], v[92:95]\n" \
  "s_waitcnt lgkmcnt(5)\n" \
  "buffer_store_dwordx4 v[96:99], v190, s[40:43], s63 offen sc1\n" \
  "v_mfma_f32_16x16x32_bf16 v[80:83], a[16:19], v[48:51], v[80:83]\n" \
  "ds_read_b128 v[40:43], v192 offset:384\n" \
  "s_waitcnt lgkmcnt(5)\n" \
  "v_cvt_pk_bf16_f32 v100, v72, v73\n" \
  "v_mfma_f32_16x16x32_bf16 v[88:91], a[16:19], v[52:55], v[88:91]\n" \
  "ds_read_b128 v[44:47], v192 offset:18816\n" \
  "v_cvt_pk_bf16_f32 v101, v74, v75\n" \
  "v_mfma_f32_16x16x32_bf16 v[84:87], a[88:91], v[48:51], v[84:87]\n" \
  "s_add_u32 m0, s52, 2048\n" \
  "v_cvt_pk_bf16_f32 v102, v76, v77\n" \
  "v_cvt_pk_bf16_f32 v103, v78, v79\n" \
  "v_mfma_f32_16x16x32_bf16 v[92:95], a[88:91], v[52:55], v[92:95]\n" \
  "s_waitcnt lgkmcnt(5)\n" \
  "buffer_load_dwordx4 v198, s[44:47], s51 offen lds\n" \
  "v_mfma_f32_16x16x32_bf16 v[80:83], a[20:23], v[56:59], v[80:83]\n" \
  "ds_read_b128 v[48:51], v191 offset:512\n" \
  "s_waitcnt lgkmcnt(5)\n" \
  "v_mfma_f32_16x16x32_bf16 v[88:91], a[20:23], v[60:63], v[88:91]\n" \
  "ds_read_b128 v[52:55], v191 offset:18944\n" \
  "v_mfma_f32_16x16x32_bf16 v[84:87], a[92:95], v[56:59], v[84:87]\n" \
  "buffer_store_dwordx4 v[100:103], v190, s[40:43], s62 offen offset:1024 sc1\n" \
  "v_mfma_f32_16x16x32_bf16 v[92:95], a[92:95], v[60:63], v[92:95]\n" \
  "s_waitcnt lgkmcnt(5)\n" \
  "buffer_store_dwordx4 v[100:103], v190, s[40:43], s63 offen offset:1024 sc1\n" \
  "v_mfma_f32_16x16x32_bf16 v[80:83], a[24:27], v[32:35], v[80:83]\n" \
  "ds_read_b128 v[56:59], v192 offset:512\n" \
  "s_add_u32 s49, s49, 524288\n" \
  "s_waitcnt lgkmcnt(5)\n" \
  "v_mfma_f32_16x16x32_bf16 v[88:91], a[24:27], v[36:39], v[88:91]\n" \
  "ds_read_b128 v[60:63], v192 offset:18944\n" \
  "s_bitset0_b32 s49, 30\n" \
  "v_mfma_f32_16x16x32_bf16 v[84:87], a[96:99], v[32:35], v[84:87]\n" \
  "s_add_u32 m0, s52, 3072\n" \
  "s_waitcnt lgkmcnt(5)\n" \
  "v_mfma_f32_16x16x32_bf16 v[92:95], a[96:99], v[36:39], v[92:95]\n" \
  "buffer_load_dwordx4 v199, s[44:47], s51 offen lds\n" \
  "v_mfma_f32_16x16x32_bf16 v[80:83], a[28:31], v[40:43], v[80:83]\n" \
  "ds_read_b128 v[32:35], v191 offset:640\n" \
  "s_waitcnt lgkmcnt(5)\n" \
  "v_mfma_f32_16x16x32_bf16 v[88:91], a[28:31], v[44:47], v[88:91]\n" \
  "ds_read_b128 v[36:39], v191 offset:19072\n" \
  "v_mfma_f32_16x16x32_bf16 v[84:87], a[100:103], v[40:43], v[84:87]\n" \
  "s_cmp_eq_u32 s48, 20\n" \
  "s_cselect_b32 s60, 19922944, 0\n" \
  "s_add_u32 s49, s49, s60\n" \
  "v_mfma_f32_16x16x32_bf16 v[92:95], a[100:103], v[44:47], v[92:95]\n" \
  "s_add_u32 s58, s48, 1\n" \
  "s_and_b32 s58, s58, 3\n" \
  "s_cselect_b32 s58, 0, 147456\n" \
  "s_waitcnt lgkmcnt(5)\n" \
  "v_mfma_f32_16x16x32_bf16 v[80:83], a[32:35], v[48:51], v[80:83]\n" \
  "ds_read_b128 v[40:43], v192 offset:640\n" \
  "s_sub_u32 s58, 36864, s58\n" \
  "s_waitcnt lgkmcnt(5)\n" \
  "v_mfma_f32_16x16x32_bf16 v[88:91], a[32:35], v[52:55], v[88:91]\n" \
  "ds_read_b128 v[44:47], v192 offset:19072\n" \
  "v_add_u32 v165, s58, v191\n" \
  "v_mfma_f32_16x16x32_bf16 v[84:87], a[104:107], v[48:51], v[84:87]\n" \
  "s_add_u32 m0, s52, 4096\n" \
  "v_add_u32 v164, s58, v192\n" \
  "v_mfma_f32_16x16x32_bf16 v[92:95], a[104:107], v[52:55], v[92:95]\n" \
  "s_waitcnt lgkmcnt(5)\n" \
  "buffer_load_dwordx4 v200, s[44:47], s51 offen lds\n" \
  "v_mfma_f32_16x16x32_bf16 v[80:83], a[36:39], v[56:59], v[80:83]\n" \
  "ds_read_b128 v[48:51], v191 offset:768\n" \
  "s_waitcnt lgkmcnt(5)\n" \
  "v_mfma_f32_16x16x32_bf16 v[88:91], a[36:39], v[60:63], v[88:91]\n" \
  "ds_read_b128 v[52:55], v191 offset:19200\n" \
  "v_mfma_f32_16x16x32_bf16 v[84:87], a[108:111], v[56:59], v[84:87]\n" \
  "v_mfma_f32_16x16x32_bf16 v[92:95], a[108:111], v[60:63], v[92:95]\n" \
  "s_waitcnt lgkmcnt(5)\n" \
  "v_mfma_f32_16x16x32_bf16 v[80:83], a[40:43], v[32:35], v[80:83]\n" \
  "ds_read_b128 v[56:59], v192 offset:768\n" \
  "s_waitcnt lgkmcnt(5)\n" \
  "v_mfma_f32_16x16x32_bf16 v[88:91], a[40:43], v[36:39], v[88:91]\n" \
  "ds_read_b128 v[60:63], v192 offset:19200\n" \
  "v_mfma_f32_16x16x32_bf16 v[84:87], a[112:115], v[32:35], v[84:87]\n" \
  "s_add_u32 m0, s52, 5120\n" \
  "s_waitcnt lgkmcnt(5)\n" \
  "v_mfma_f32_16x16x32_bf16 v[92:95], a[112:115], v[36:39], v[92:95]\n" \
  "buffer_load_dwordx4 v201, s[44:47], s51 offen lds\n" \
  "v_mfma_f32_16x16x32_bf16 v[80:83], a[44:47], v[40:43], v[80:83]\n" \
  "ds_read_b128 v[32:35], v191 offset:896\n" \
  "s_waitcnt lgkmcnt(5)\n" \
  "v_mfma_f32_16x16x32_bf16 v[88:91], a[44:47], v[44:47], v[88:91]\n" \
  "ds_read_b128 v[36:39], v191 offset:19328\n" \
  "v_mfma_f32_16x16x32_bf16 v[84:87], a[116:119], v[40:43], v[84:87]\n" \
  "v_mfma_f32_16x16x32_bf16 v[92:95], a[116:119], v[44:47], v[92:95]\n" \
  "s_waitcnt lgkmcnt(5)\n" \
  "v_mfma_f32_16x16x32_bf16 v[80:83], a[48:51], v[48:51], v[80:83]\n" \
  "ds_read_b128 v[40:43], v192 offset:896\n" \
  "s_waitcnt lgkmcnt(5)\n" \
  "v_mfma_f32_16x16x32_bf16 v[88:91], a[48:51], v[52:55], v[88:91]\n" \
  "ds_read_b128 v[44:47], v192 offset:19328\n" \
  "v_mfma_f32_16x16x32_bf16 v[84:87], a[120:123], v[48:51], v[84:87]\n" \
  "s_add_u32 m0, s52, 6144\n" \
  "v_mov_b32 v168, s56\n" \
  "v_mfma_f32_16x16x32_bf16 v[92:95], a[120:123], v[52:55], v[92:95]\n" \
  "s_waitcnt lgkmcnt(5)\n" \
  "buffer_load_dwordx4 v202, s[44:47], s51 offen lds\n" \
  "v_mfma_f32_16x16x32_bf16 v[80:83], a[52:55], v[56:59], v[80:83]\n" \
  "ds_read_b128 v[48:51], v191 offset:1024\n" \
  "s_waitcnt lgkmcnt(5)\n" \
  "v_mfma_f32_16x16x32_bf16 v[88:91], a[52:55], v[60:63], v[88:91]\n" \
  "ds_read_b128 v[52:55], v191 offset:19456\n" \
  "v_mfma_f32_16x16x32_bf16 v[84:87], a[124:127], v[56:59], v[84:87]\n" \
  "s_waitcnt vmcnt(11)\n" \
  "s_barrier\n" \
  "v_mfma_f32_16x16x32_bf16 v[92:95], a[124:127], v[60:63], v[92:95]\n" \
  "s_waitcnt lgkmcnt(5)\n" \
  "buffer_store_dwordx2 v[168:169], v163, s[40:43], 0 offen sc1\n" \
  "v_mfma_f32_16x16x32_bf16 v[80:83], a[56:59], v[32:35], v[80:83]\n" \
  "ds_read_b128 v[56:59], v192 offset:1024\n" \
  "s_waitcnt lgkmcnt(5)\n" \
  "v_mfma_f32_16x16x32_bf16 v[88:91], a[56:59], v[36:39], v[88:91]\n" \
  "ds_read_b128 v[60:63], v192 offset:19456\n" \
  "v_mfma_f32_16x16x32_bf16 v[84:87], a[128:131], v[32:35], v[84:87]\n" \
  "s_add_u32 m0, s52, 7168\n" \
  "s_waitcnt lgkmcnt(5)\n" \
  "v_mfma_f32_16x16x32_bf16 v[92:95], a[128:131], v[36:39], v[92:95]\n" \
  "buffer_load_dwordx4 v203, s[44:47], s51 offen lds\n" \
  "v_mfma_f32_16x16x32_bf16 v[80:83], a[60:63], v[40:43], v[80:83]\n" \
  "ds_read_b128 v[32:35], v165\n" \
  "s_waitcnt lgkmcnt(5)\n" \
  "v_mfma_f32_16x16x32_bf16 v[88:91], a[60:63], v[44:47], v[88:91]\n" \
  "ds_read_b128 v[36:39], v165 offset:18432\n" \
  "v_mfma_f32_16x16x32_bf16 v[84:87], a[132:135], v[40:43], v[84:87]\n" \
  "s_min_u32 s56, s48, 20\n" \
  "s_cselect_b32 s57, 0, 38\n" \
  "s_add_u32 s57, s57, s48\n" \
  "v_mfma_f32_16x16x32_bf16 v[92:95], a[132:135], v[44:47], v[92:95]\n" \
  "s_add_u32 s48, s48, 1\n" \
  "v_mov_b32 v191, v165\n" \
  "v_mov_b32 v192, v164\n" \
  "s_waitcnt lgkmcnt(5)\n" \
  "v_mfma_f32_16x16x32_bf16 v[80:83], a[64:67], v[48:51], v[80:83]\n" \
  "ds_read_b128 v[40:43], v164\n" \
  "s_waitcnt lgkmcnt(5)\n" \
  "v_mfma_f32_16x16x32_bf16 v[88:91], a[64:67], v[52:55], v[88:91]\n" \
  "ds_read_b128 v[44:47], v164 offset:18432\n" \
  "v_mfma_f32_16x16x32_bf16 v[84:87], a[136:139], v[48:51], v[84:87]\n" \
  "s_add_u32 m0, s52, 8192\n" \
  "s_cmp_eq_u32 s51, 700416\n" \
  "s_cselect_b32 s70, 1437696, s71\n" \
  "v_mfma_f32_16x16x32_bf16 v[92:95], a[136:139], v[52:55], v[92:95]\n" \
  "s_waitcnt lgkmcnt(5)\n" \
  "buffer_load_dwordx4 v204, s[44:47], s51 offen lds\n" \
  "v_mfma_f32_16x16x32_bf16 v[80:83], a[68:71], v[56:59], v[80:83]\n" \
  "ds_read_b128 v[48:51], v165 offset:128\n" \
  "s_add_u32 s51, s51, s70\n" \
  "s_waitcnt lgkmcnt(5)\n" \
  "v_mfma_f32_16x16x32_bf16 v[88:91], a[68:71], v[60:63], v[88:91]\n" \
  "ds_read_b128 v[52:55], v165 offset:18560\n" \
  "s_min_u32 s51, s51, 2322432\n" \
  "v_mfma_f32_16x16x32_bf16 v[84:87], a[140:143], v[56:59], v[84:87]\n" \
  "s_add_u32 s52, s52, 36864\n" \
  "v_mfma_f32_16x16x32_bf16 v[92:95], a[140:143], v[60:63], v[92:95]\n" \
  "s_cmp_ge_u32 s52, 163840\n" \
  "s_cselect_b32 s60, 147456, 0\n" \
  "s_sub_u32 s52, s52, s60\n" \
  "s_cmp_le_u32 s48, 4\n" \
  "s_cbranch_scc1 .LK_meb_%=\n" \
  ".LK_meb_b_%=:\n" \
  "s_cmp_lt_u32 s48, 26\n" \
  "s_cbranch_scc1 .LK_ma_%=\n" \
  "s_waitcnt lgkmcnt(0)\n" \
  "s_nop 7\n" \
  "s_lshr_b32 s62, s49, 8\n" \
  "s_and_b32 s62, s62, 8192\n" \
  "s_xor_b32 s62, s62, s49\n" \
  "s_lshl_b32 s63, s57, 14\n" \
  "s_xor_b32 s63, s63, s59\n" \
  "v_cvt_pk_bf16_f32 v96, v80, v81\n" \
  "v_cvt_pk_bf16_f32 v97, v82, v83\n" \
  "v_cvt_pk_bf16_f32 v98, v84, v85\n" \
  "v_cvt_pk_bf16_f32 v99, v86, v87\n" \
  "buffer_store_dwordx4 v[96:99], v190, s[40:43], s62 offen sc1\n" \
  "buffer_store_dwordx4 v[96:99], v190, s[40:43], s63 offen sc1\n" \
  "v_cvt_pk_bf16_f32 v100, v88, v89\n" \
  "v_cvt_pk_bf16_f32 v101, v90, v91\n" \
  "v_cvt_pk_bf16_f32 v102, v92, v93\n" \
  "v_cvt_pk_bf16_f32 v103, v94, v95\n" \
  "buffer_store_dwordx4 v[100:103], v190, s[40:43], s62 offen offset:1024 sc1\n" \
  "buffer_store_dwordx4 v[100:103], v190, s[40:43], s63 offen offset:1024 sc1\n" \
  "s_sub_u32 s49, s49, 22544384\n" \
  "s_mov_b32 s48, 20\n" \
  "s_waitcnt vmcnt(0)\n" \
  "s_barrier\n" \
  "v_mov_b32 v168, 1\n" \
  "s_mov_b64 exec, 2\n" \
  "buffer_store_dwordx2 v[168:169], v167, s[40:43], 0 offen sc1\n" \
  "s_mov_b64 exec, -1\n" \
  "v_subrev_u32 v191, 73728, v191\n" \
  "v_subrev_u32 v192, 73728, v192\n" \
  "s_waitcnt vmcnt(0)\n" \
  "s_cmp_eq_u32 %43, 77\n" \
  "s_cbranch_scc1 .LK_end_%=\n" \
  "s_mov_b64 s[44:45], %32\n" \
  "s_and_b32 s45, s45, 0xffff\n" \
  "s_mov_b32 s46, 4718592\n" \
  "s_add_u32 s60, %35, 0\n" \
  "s_add_u32 m0, s72, 16384\n" \
  "s_nop 0\n" \
  "buffer_load_dwordx4 v170, s[44:47], s60 offen lds\n" \
  "buffer_load_dwordx4 v171, s[44:47], s60 offen offset:1024 lds\n" \
  "buffer_load_dwordx4 v172, s[44:47], s60 offen offset:2048 lds\n" \
  "buffer_load_dwordx4 v173, s[44:47], s60 offen offset:3072 lds\n" \
  "s_add_u32 s60, %35, 524288\n" \
  "s_add_u32 m0, s72, 32768\n" \
  "s_nop 0\n" \
  "buffer_load_dwordx4 v170, s[44:47], s60 offen lds\n" \
  "buffer_load_dwordx4 v171, s[44:47], s60 offen offset:1024 lds\n" \
  "buffer_load_dwordx4 v172, s[44:47], s60 offen offset:2048 lds\n" \
  "buffer_load_dwordx4 v173, s[44:47], s60 offen offset:3072 lds\n" \
  "s_add_u32 s60, %35, 1048576\n" \
  "s_add_u32 m0, s72, 49152\n" \
  "s_nop 0\n" \
  "buffer_load_dwordx4 v170, s[44:47], s60 offen lds\n" \
  "buffer_load_dwordx4 v171, s[44:47], s60 offen offset:1024 lds\n" \
  "buffer_load_dwordx4 v172, s[44:47], s60 offen offset:2048 lds\n" \
  "buffer_load_dwordx4 v173, s[44:47], s60 offen offset:3072 lds\n" \
  "s_add_u32 s60, %35, 1572864\n" \
  "s_add_u32 m0, s72, 65536\n" \
  "s_nop 0\n" \
  "buffer_load_dwordx4 v170, s[44:47], s60 offen lds\n" \
  "buffer_load_dwordx4 v171, s[44:47], s60 offen offset:1024 lds\n" \
  "buffer_load_dwordx4 v172, s[44:47], s60 offen offset:2048 lds\n" \
  "buffer_load_dwordx4 v173, s[44:47], s60 offen offset:3072 lds\n" \
  "s_add_u32 s60, %35, 2097152\n" \
  "s_add_u32 m0, s72, 81920\n" \
  "s_nop 0\n" \
  "buffer_load_dwordx4 v170, s[44:47], s60 offen lds\n" \
  "buffer_load_dwordx4 v171, s[44:47], s60 offen offset:1024 lds\n" \
  "buffer_load_dwordx4 v172, s[44:47], s60 offen offset:2048 lds\n" \
  "buffer_load_dwordx4 v173, s[44:47], s60 offen offset:3072 lds\n" \
  "s_add_u32 s60, %35, 2621440\n" \
  "s_add_u32 m0, s72, 98304\n" \
  "s_nop 0\n" \
  "buffer_load_dwordx4 v170, s[44:47], s60 offen lds\n" \
  "buffer_load_dwordx4 v171, s[44:47], s60 offen offset:1024 lds\n" \
  "buffer_load_dwordx4 v172, s[44:47], s60 offen offset:2048 lds\n" \
  "buffer_load_dwordx4 v173, s[44:47], s60 offen offset:3072 lds\n" \
  "s_add_u32 s60, %35, 3145728\n" \
  "s_add_u32 m0, s72, 114688\n" \
  "s_nop 0\n" \
  "buffer_load_dwordx4 v170, s[44:47], s60 offen lds\n" \
  "buffer_load_dwordx4 v171, s[44:47], s60 offen offset:1024 lds\n" \
  "buffer_load_dwordx4 v172, s[44:47], s60 offen offset:2048 lds\n" \
  "buffer_load_dwordx4 v173, s[44:47], s60 offen offset:3072 lds\n" \
  "s_add_u32 s60, %35, 3670016\n" \
  "s_add_u32 m0, s72, 131072\n" \
  "s_nop 0\n" \
  "buffer_load_dwordx4 v170, s[44:47], s60 offen lds\n" \
  "buffer_load_dwordx4 v171, s[44:47], s60 offen offset:1024 lds\n" \
  "buffer_load_dwordx4 v172, s[44:47], s60 offen offset:2048 lds\n" \
  "buffer_load_dwordx4 v173, s[44:47], s60 offen offset:3072 lds\n" \
  "s_add_u32 s60, %35, 4194304\n" \
  "s_add_u32 m0, s72, 147456\n" \
  "s_nop 0\n" \
  "buffer_load_dwordx4 v170, s[44:47], s60 offen lds\n" \
  "buffer_load_dwordx4 v171, s[44:47], s60 offen offset:1024 lds\n" \
  "buffer_load_dwordx4 v172, s[44:47], s60 offen offset:2048 lds\n" \
  "buffer_load_dwordx4 v173, s[44:47], s60 offen offset:3072 lds\n" \
  "s_waitcnt vmcnt(24)\n" \
  "s_barrier\n" \
  "v_add_u32 v186, 16384, v182\n" \
  "ds_read_b64_tr_b16 a[144:145], v186\n" \
  "ds_read_b64_tr_b16 a[146:147], v186 offset:1024\n" \
  "ds_read_b64_tr_b16 a[148:149], v186 offset:8192\n" \
  "ds_read_b64_tr_b16 a[150:151], v186 offset:9216\n" \
  "v_add_u32 v187, 16384, v183\n" \
  "ds_read_b64_tr_b16 a[216:217], v187\n" \
  "ds_read_b64_tr_b16 a[218:219], v187 offset:1024\n" \
  "ds_read_b64_tr_b16 a[220:221], v187 offset:8192\n" \
  "ds_read_b64_tr_b16 a[222:223], v187 offset:9216\n" \
  "v_add_u32 v186, 32768, v182\n" \
  "ds_read_b64_tr_b16 a[152:153], v186\n" \
  "ds_read_b64_tr_b16 a[154:155], v186 offset:1024\n" \
  "ds_read_b64_tr_b16 a[156:157], v186 offset:8192\n" \
  "ds_read_b64_tr_b16 a[158:159], v186 offset:9216\n" \
  "v_add_u32 v187, 32768, v183\n" \
  "ds_read_b64_tr_b16 a[224:225], v187\n" \
  "ds_read_b64_tr_b16 a[226:227], v187 offset:1024\n" \
  "ds_read_b64_tr_b16 a[228:229], v187 offset:8192\n" \
  "ds_read_b64_tr_b16 a[230:231], v187 offset:9216\n" \
  "v_add_u32 v186, 49152, v182\n" \
  "ds_read_b64_tr_b16 a[160:161], v186\n" \
  "ds_read_b64_tr_b16 a[162:163], v186 offset:1024\n" \
  "ds_read_b64_tr_b16 a[164:165], v186 offset:8192\n" \
  "ds_read_b64_tr_b16 a[166:167], v186 offset:9216\n" \
  "v_add_u32 v187, 49152, v183\n" \
  "ds_read_b64_tr_b16 a[232:233], v187\n" \
  "ds_read_b64_tr_b16 a[234:235], v187 offset:1024\n" \
  "ds_read_b64_tr_b16 a[236:237], v187 offset:8192\n" \
  "ds_read_b64_tr_b16 a[238:239], v187 offset:9216\n" \
  "s_waitcnt lgkmcnt(0)\n" \
  "s_barrier\n" \
  "s_mov_b64 s[44:45], %33\n" \
  "s_and_b32 s45, s45, 0xffff\n" \
  "s_mov_b32 s46, 2359296\n" \
  "s_mov_b32 s51, 737280\n" \
  "s_mul_i32 s52, %34, 9216\n" \
  "s_add_u32 s52, s52, 16384\n" \
  "s_add_u32 m0, s52, 0\n" \
  "s_nop 0\n" \
  "buffer_load_dwordx4 v196, s[44:47], s51 offen lds\n" \
  "s_add_u32 m0, s52, 1024\n" \
  "s_nop 0\n" \
  "buffer_load_dwordx4 v197, s[44:47], s51 offen lds\n" \
  "s_add_u32 m0, s52, 2048\n" \
  "s_nop 0\n" \
  "buffer_load_dwordx4 v198, s[44:47], s51 offen lds\n" \
  "s_add_u32 m0, s52, 3072\n" \
  "s_nop 0\n" \
  "buffer_load_dwordx4 v199, s[44:47], s51 offen lds\n" \
  "s_add_u32 m0, s52, 4096\n" \
  "s_nop 0\n" \
  "buffer_load_dwordx4 v200, s[44:47], s51 offen lds\n" \
  "s_add_u32 m0, s52, 5120\n" \
  "s_nop 0\n" \
  "buffer_load_dwordx4 v201, s[44:47], s51 offen lds\n" \
  "s_add_u32 m0, s52, 6144\n" \
  "s_nop 0\n" \
  "buffer_load_dwordx4 v202, s[44:47], s51 offen lds\n" \
  "s_add_u32 m0, s52, 7168\n" \
  "s_nop 0\n" \
  "buffer_load_dwordx4 v203, s[44:47], s51 offen lds\n" \
  "s_add_u32 m0, s52, 8192\n" \
  "s_nop 0\n" \
  "buffer_load_dwordx4 v204, s[44:47], s51 offen lds\n" \
  "s_add_u32 s51, s51, 36864\n" \
  "s_min_u32 s51, s51, 2322432\n" \
  "s_add_u32 s52, s52, 36864\n" \
  "s_cmp_ge_u32 s52, 163840\n" \
  "s_cselect_b32 s60, 147456, 0\n" \
  "s_sub_u32 s52, s52, s60\n" \
  "s_waitcnt vmcnt(21)\n" \
  "s_barrier\n" \
  "v_add_u32 v186, 65536, v182\n" \
  "ds_read_b64_tr_b16 a[168:169], v186\n" \
  "ds_read_b64_tr_b16 a[170:171], v186 offset:1024\n" \
  "ds_read_b64_tr_b16 a[172:173], v186 offset:8192\n" \
  "ds_read_b64_tr_b16 a[174:175], v186 offset:9216\n" \
  "v_add_u32 v187, 65536, v183\n" \
  "ds_read_b64_tr_b16 a[240:241], v187\n" \
  "ds_read_b64_tr_b16 a[242:243], v187 offset:1024\n" \
  "ds_read_b64_tr_b16 a[244:245], v187 offset:8192\n" \
  "ds_read_b64_tr_b16 a[246:247], v187 offset:9216\n" \
  "v_add_u32 v186, 81920, v182\n" \
  "ds_read_b64_tr_b16 a[176:177], v186\n" \
  "ds_read_b64_tr_b16 a[178:179], v186 offset:1024\n" \
  "ds_read_b64_tr_b16 a[180:181], v186 offset:8192\n" \
  "ds_read_b64_tr_b16 a[182:183], v186 offset:9216\n" \
  "v_add_u32 v187, 81920, v183\n" \
  "ds_read_b64_tr_b16 a[248:249], v187\n" \
  "ds_read_b64_tr_b16 a[250:251], v187 offset:1024\n" \
  "ds_read_b64_tr_b16 a[252:253], v187 offset:8192\n" \
  "ds_read_b64_tr_b16 a[254:255], v187 offset:9216\n" \
  "v_add_u32 v186, 98304, v182\n" \
  "ds_read_b64_tr_b16 a[184:185], v186\n" \
  "ds_read_b64_tr_b16 a[186:187], v186 offset:1024\n" \
  "ds_read_b64_tr_b16 a[188:189], v186 offset:8192\n" \
  "ds_read_b64_tr_b16 a[190:191], v186 offset:9216\n" \
  "v_add_u32 v187, 98304, v183\n" \
  "ds_read_b64_tr_b16 v[0:1], v187\n" \
  "ds_read_b64_tr_b16 v[2:3], v187 offset:1024\n" \
  "ds_read_b64_tr_b16 v[4:5], v187 offset:8192\n" \
  "ds_read_b64_tr_b16 v[6:7], v187 offset:9216\n" \
  "s_waitcnt lgkmcnt(0)\n" \
  "s_barrier\n" \
  "s_add_u32 m0, s52, 0\n" \
  "s_nop 0\n" \
  "buffer_load_dwordx4 v196, s[44:47], s51 offen lds\n" \
  "s_add_u32 m0, s52, 1024\n" \
  "s_nop 0\n" \
  "buffer_load_dwordx4 v197, s[44:47], s51 offen lds\n" \
  "s_add_u32 m0, s52, 2048\n" \
  "s_nop 0\n" \
  "buffer_load_dwordx4 v198, s[44:47], s51 offen lds\n" \
  "s_add_u32 m0, s52, 3072\n" \
  "s_nop 0\n" \
  "buffer_load_dwordx4 v199, s[44:47], s51 offen lds\n" \
  "s_add_u32 m0, s52, 4096\n" \
  "s_nop 0\n" \
  "buffer_load_dwordx4 v200, s[44:47], s51 offen lds\n" \
  "s_add_u32 m0, s52, 5120\n" \
  "s_nop 0\n" \
  "buffer_load_dwordx4 v201, s[44:47], s51 offen lds\n" \
  "s_add_u32 m0, s52, 6144\n" \
  "s_nop 0\n" \
  "buffer_load_dwordx4 v202, s[44:47], s51 offen lds\n" \
  "s_add_u32 m0, s52, 7168\n" \
  "s_nop 0\n" \
  "buffer_load_dwordx4 v203, s[44:47], s51 offen lds\n" \
  "s_add_u32 m0, s52, 8192\n" \
  "s_nop 0\n" \
  "buffer_load_dwordx4 v204, s[44:47], s51 offen lds\n" \
  "s_add_u32 s51, s51, 36864\n" \
  "s_min_u32 s51, s51, 2322432\n" \
  "s_add_u32 s52, s52, 36864\n" \
  "s_cmp_ge_u32 s52, 163840\n" \
  "s_cselect_b32 s60, 147456, 0\n" \
  "s_sub_u32 s52, s52, s60\n" \
  "s_waitcnt vmcnt(18)\n" \
  "s_barrier\n" \
  "v_add_u32 v186, 114688, v182\n" \
  "ds_read_b64_tr_b16 a[192:193], v186\n" \
  "ds_read_b64_tr_b16 a[194:195], v186 offset:1024\n" \
  "ds_read_b64_tr_b16 a[196:197], v186 offset:8192\n" \
  "ds_read_b64_tr_b16 a[198:199], v186 offset:9216\n" \
  "v_add_u32 v187, 114688, v183\n" \
  "ds_read_b64_tr_b16 v[8:9], v187\n" \
  "ds_read_b64_tr_b16 v[10:11], v187 offset:1024\n" \
  "ds_read_b64_tr_b16 v[12:13], v187 offset:8192\n" \
  "ds_read_b64_tr_b16 v[14:15], v187 offset:9216\n" \
  "v_add_u32 v186, 131072, v182\n" \
  "ds_read_b64_tr_b16 a[200:201], v186\n" \
  "ds_read_b64_tr_b16 a[202:203], v186 offset:1024\n" \
  "ds_read_b64_tr_b16 a[204:205], v186 offset:8192\n" \
  "ds_read_b64_tr_b16 a[206:207], v186 offset:9216\n" \
  "v_add_u32 v187, 131072, v183\n" \
  "ds_read_b64_tr_b16 v[16:17], v187\n" \
  "ds_read_b64_tr_b16 v[18:19], v187 offset:1024\n" \
  "ds_read_b64_tr_b16 v[20:21], v187 offset:8192\n" \
  "ds_read_b64_tr_b16 v[22:23], v187 offset:9216\n" \
  "v_add_u32 v186, 147456, v182\n" \
  "ds_read_b64_tr_b16 a[208:209], v186\n" \
  "ds_read_b64_tr_b16 a[210:211], v186 offset:1024\n" \
  "ds_read_b64_tr_b16 a[212:213], v186 offset:8192\n" \
  "ds_read_b64_tr_b16 a[214:215], v186 offset:9216\n" \
  "v_add_u32 v187, 147456, v183\n" \
  "ds_read_b64_tr_b16 v[24:25], v187\n" \
  "ds_read_b64_tr_b16 v[26:27], v187 offset:1024\n" \
  "ds_read_b64_tr_b16 v[28:29], v187 offset:8192\n" \
  "ds_read_b64_tr_b16 v[30:31], v187 offset:9216\n" \
  "s_waitcnt lgkmcnt(0)\n" \
  "s_waitcnt vmcnt(9)\n" \
  "s_barrier\n" \
  "ds_read_b128 v[32:35], v191\n" \
  "ds_read_b128 v[36:39], v191 offset:18432\n" \
  "ds_read_b128 v[40:43], v192\n" \
  "ds_read_b128 v[44:47], v192 offset:18432\n" \
  "ds_read_b128 v[48:51], v191 offset:128\n" \
  "ds_read_b128 v[52:55], v191 offset:18560\n" \
  "s_waitcnt lgkmcnt(0)\n" \
  ".LK_loop_%=:\n" \
  "s_waitcnt lgkmcnt(5)\n" \
  "v_mfma_f32_16x16x32_bf16 v[64:67], a[0:3], v[32:35], 0\n" \
  "ds_read_b128 v[56:59], v192 offset:128\n" \
  "s_waitcnt lgkmcnt(5)\n" \
  "v_mfma_f32_16x16x32_bf16 v[72:75], a[0:3], v[36:39], 0\n" \
  "ds_read_b128 v[60:63], v192 offset:18560\n" \
  "s_lshr_b32 s62, s50, 8\n" \
  "v_mfma_f32_16x16x32_bf16 v[68:71], a[72:75], v[32:35], 0\n" \
  "s_and_b32 s62, s62, 8192\n" \
  "s_xor_b32 s62, s62, s50\n" \
  "v_cvt_pk_bf16_f32 v96, v80, v81\n" \
  "v_mfma_f32_16x16x32_bf16 v[76:79], a[72:75], v[36:39], 0\n" \
  "s_waitcnt lgkmcnt(5)\n" \
  "v_cvt_pk_bf16_f32 v97, v82, v83\n" \
  "v_cvt_pk_bf16_f32 v98, v84, v85\n" \
  "v_mfma_f32_16x16x32_bf16 v[64:67], a[4:7], v[40:43], v[64:67]\n" \
  "ds_read_b128 v[32:35], v191 offset:256\n" \
  "s_waitcnt lgkmcnt(5)\n" \
  "v_cvt_pk_bf16_f32 v99, v86, v87\n" \
  "v_mfma_f32_16x16x32_bf16 v[72:75], a[4:7], v[44:47], v[72:75]\n" \
  "ds_read_b128 v[36:39], v191 offset:18688\n" \
  "v_mfma_f32_16x16x32_bf16 v[68:71], a[76:79], v[40:43], v[68:71]\n" \
  "buffer_store_dwordx4 v[96:99], v190, s[40:43], s62 offen sc1\n" \
  "v_mfma_f32_16x16x32_bf16 v[76:79], a[76:79], v[44:47], v[76:79]\n" \
  "s_waitcnt lgkmcnt(5)\n" \
  "v_cvt_pk_bf16_f32 v100, v88, v89\n" \
  "v_cvt_pk_bf16_f32 v101, v90, v91\n" \
  "v_mfma_f32_16x16x32_bf16 v[64:67], a[8:11], v[48:51], v[64:67]\n" \
  "ds_read_b128 v[40:43], v192 offset:256\n" \
  "s_waitcnt lgkmcnt(5)\n" \
  "v_cvt_pk_bf16_f32 v102, v92, v93\n" \
  "v_mfma_f32_16x16x32_bf16 v[72:75], a[8:11], v[52:55], v[72:75]\n" \
  "ds_read_b128 v[44:47], v192 offset:18688\n" \
  "v_cvt_pk_bf16_f32 v103, v94, v95\n" \
  "v_mfma_f32_16x16x32_bf16 v[68:71], a[80:83], v[48:51], v[68:71]\n" \
  "buffer_store_dwordx4 v[100:103], v190, s[40:43], s62 offen offset:1024 sc1\n" \
  "v_mfma_f32_16x16x32_bf16 v[76:79], a[80:83], v[52:55], v[76:79]\n" \
  "s_add_u32 s50, s50, 524288\n" \
  "s_bitset0_b32 s50, 30\n" \
  "s_waitcnt lgkmcnt(5)\n" \
  "v_mfma_f32_16x16x32_bf16 v[64:67], a[12:15], v[56:59], v[64:67]\n" \
  "ds_read_b128 v[48:51], v191 offset:384\n" \
  "s_waitcnt lgkmcnt(5)\n" \
  "v_mfma_f32_16x16x32_bf16 v[72:75], a[12:15], v[60:63], v[72:75]\n" \
  "ds_read_b128 v[52:55], v191 offset:18816\n" \
  "v_mfma_f32_16x16x32_bf16 v[68:71], a[84:87], v[56:59], v[68:71]\n" \
  "v_mfma_f32_16x16x32_bf16 v[76:79], a[84:87], v[60:63], v[76:79]\n" \
  "s_waitcnt lgkmcnt(5)\n" \
  "v_mfma_f32_16x16x32_bf16 v[64:67], a[16:19], v[32:35], v[64:67]\n" \
  "ds_read_b128 v[56:59], v192 offset:384\n" \
  "s_waitcnt lgkmcnt(5)\n" \
  "v_mfma_f32_16x16x32_bf16 v[72:75], a[16:19], v[36:39], v[72:75]\n" \
  "ds_read_b128 v[60:63], v192 offset:18816\n" \
  "v_mfma_f32_16x16x32_bf16 v[68:71], a[88:91], v[32:35], v[68:71]\n" \
  "v_mfma_f32_16x16x32_bf16 v[76:79], a[88:91], v[36:39], v[76:79]\n" \
  "s_waitcnt lgkmcnt(5)\n" \
  "v_mfma_f32_16x16x32_bf16 v[64:67], a[20:23], v[40:43], v[64:67]\n" \
  "ds_read_b128 v[32:35], v191 offset:512\n" \
  "s_waitcnt lgkmcnt(5)\n" \
  "v_mfma_f32_16x16x32_bf16 v[72:75], a[20:23], v[44:47], v[72:75]\n" \
  "ds_read_b128 v[36:39], v191 offset:18944\n" \
  "v_mfma_f32_16x16x32_bf16 v[68:71], a[92:95], v[40:43], v[68:71]\n" \
  "v_mfma_f32_16x16x32_bf16 v[76:79], a[92:95], v[44:47], v[76:79]\n" \
  "s_waitcnt lgkmcnt(5)\n" \
  "v_mfma_f32_16x16x32_bf16 v[64:67], a[24:27], v[48:51], v[64:67]\n" \
  "ds_read_b128 v[40:43], v192 offset:512\n" \
  "s_waitcnt lgkmcnt(5)\n" \
  "v_mfma_f32_16x16x32_bf16 v[72:75], a[24:27], v[52:55], v[72:75]\n" \
  "ds_read_b128 v[44:47], v192 offset:18944\n" \
  "v_mfma_f32_16x16x32_bf16 v[68:71], a[96:99], v[48:51], v[68:71]\n" \
  "v_mfma_f32_16x16x32_bf16 v[76:79], a[96:99], v[52:55], v[76:79]\n" \
  "s_waitcnt lgkmcnt(5)\n" \
  "v_mfma_f32_16x16x32_bf16 v[64:67], a[28:31], v[56:59], v[64:67]\n" \
  "ds_read_b128 v[48:51], v191 offset:640\n" \
  "s_waitcnt lgkmcnt(5)\n" \
  "v_mfma_f32_16x16x32_bf16 v[72:75], a[28:31], v[60:63], v[72:75]\n" \
  "ds_read_b128 v[52:55], v191 offset:19072\n" \
  "v_mfma_f32_16x16x32_bf16 v[68:71], a[100:103], v[56:59], v[68:71]\n" \
  "s_add_u32 m0, s52, 0\n" \
  "s_waitcnt lgkmcnt(5)\n" \
  "v_mfma_f32_16x16x32_bf16 v[76:79], a[100:103], v[60:63], v[76:79]\n" \
  "buffer_load_dwordx4 v196, s[44:47], s51 offen lds\n" \
  "v_mfma_f32_16x16x32_bf16 v[64:67], a[32:35], v[32:35], v[64:67]\n" \
  "ds_read_b128 v[56:59], v192 offset:640\n" \
  "s_waitcnt lgkmcnt(5)\n" \
  "v_mfma_f32_16x16x32_bf16 v[72:75], a[32:35], v[36:39], v[72:75]\n" \
  "ds_read_b128 v[60:63], v192 offset:19072\n" \
  "v_mfma_f32_16x16x32_bf16 v[68:71], a[104:107], v[32:35], v[68:71]\n" \
  "v_mfma_f32_16x16x32_bf16 v[76:79], a[104:107], v[36:39], v[76:79]\n" \
  "s_waitcnt lgkmcnt(5)\n" \
  "v_mfma_f32_16x16x32_bf16 v[64:67], a[36:39], v[40:43], v[64:67]\n" \
  "ds_read_b128 v[32:35], v191 offset:768\n" \
  "s_waitcnt lgkmcnt(5)\n" \
  "v_mfma_f32_16x16x32_bf16 v[72:75], a[36:39], v[44:47], v[72:75]\n" \
  "ds_read_b128 v[36:39], v191 offset:19200\n" \
  "v_mfma_f32_16x16x32_bf16 v[68:71], a[108:111], v[40:43], v[68:71]\n" \
  "s_add_u32 m0, s52, 1024\n" \
  "s_waitcnt lgkmcnt(5)\n" \
  "v_mfma_f32_16x16x32_bf16 v[76:79], a[108:111], v[44:47], v[76:79]\n" \
  "buffer_load_dwordx4 v197, s[44:47], s51 offen lds\n" \
  "v_mfma_f32_16x16x32_bf16 v[64:67], a[40:43], v[48:51], v[64:67]\n" \
  "ds_read_b128 v[40:43], v192 offset:768\n" \
  "s_waitcnt lgkmcnt(5)\n" \
  "v_mfma_f32_16x16x32_bf16 v[72:75], a[40:43], v[52:55], v[72:75]\n" \
  "ds_read_b128 v[44:47], v192 offset:19200\n" \
  "v_mfma_f32_16x16x32_bf16 v[68:71], a[112:115], v[48:51], v[68:71]\n" \
  "v_mfma_f32_16x16x32_bf16 v[76:79], a[112:115], v[52:55], v[76:79]\n" \
  "s_waitcnt lgkmcnt(5)\n" \
  "v_mfma_f32_16x16x32_bf16 v[64:67], a[44:47], v[56:59], v[64:67]\n" \
  "ds_read_b128 v[48:51], v191 offset:896\n" \
  "s_waitcnt lgkmcnt(5)\n" \
  "v_mfma_f32_16x16x32_bf16 v[72:75], a[44:47], v[60:63], v[72:75]\n" \
  "ds_read_b128 v[52:55], v191 offset:19328\n" \
  "v_mfma_f32_16x16x32_bf16 v[68:71], a[116:119], v[56:59], v[68:71]\n" \
  "s_add_u32 m0, s52, 2048\n" \
  "v_add_u32 v168, s48, v166\n" \
  "v_mfma_f32_16x16x32_bf16 v[76:79], a[116:119], v[60:63], v[76:79]\n" \
  "s_waitcnt lgkmcnt(5)\n" \
  "buffer_load_dwordx4 v198, s[44:47], s51 offen lds\n" \
  "v_mfma_f32_16x16x32_bf16 v[64:67], a[48:51], v[32:35], v[64:67]\n" \
  "ds_read_b128 v[56:59], v192 offset:896\n" \
  "s_waitcnt lgkmcnt(5)\n" \
  "v_mfma_f32_16x16x32_bf16 v[72:75], a[48:51], v[36:39], v[72:75]\n" \
  "ds_read_b128 v[60:63], v192 offset:19328\n" \
  "v_mfma_f32_16x16x32_bf16 v[68:71], a[120:123], v[32:35], v[68:71]\n" \
  "s_waitcnt vmcnt(5)\n" \
  "s_barrier\n" \
  "v_mfma_f32_16x16x32_bf16 v[76:79], a[120:123], v[36:39], v[76:79]\n" \
  "s_waitcnt lgkmcnt(5)\n" \
  "buffer_store_dwordx2 v[168:169], v167, s[40:43], 0 offen sc1\n" \
  "v_mfma_f32_16x16x32_bf16 v[64:67], a[52:55], v[40:43], v[64:67]\n" \
  "ds_read_b128 v[32:35], v191 offset:1024\n" \
  "s_waitcnt lgkmcnt(5)\n" \
  "v_mfma_f32_16x16x32_bf16 v[72:75], a[52:55], v[44:47], v[72:75]\n" \
  "ds_read_b128 v[36:39], v191 offset:19456\n" \
  "v_mfma_f32_16x16x32_bf16 v[68:71], a[124:127], v[40:43], v[68:71]\n" \
  "s_add_u32 m0, s52, 3072\n" \
  "s_waitcnt lgkmcnt(5)\n" \
  "v_mfma_f32_16x16x32_bf16 v[76:79], a[124:127], v[44:47], v[76:79]\n" \
  "buffer_load_dwordx4 v199, s[44:47], s51 offen lds\n" \
  "v_mfma_f32_16x16x32_bf16 v[64:67], a[56:59], v[48:51], v[64:67]\n" \
  "ds_read_b128 v[40:43], v192 offset:1024\n" \
  "s_waitcnt lgkmcnt(5)\n" \
  "v_mfma_f32_16x16x32_bf16 v[72:75], a[56:59], v[52:55], v[72:75]\n" \
  "ds_read_b128 v[44:47], v192 offset:19456\n" \
  "v_mfma_f32_16x16x32_bf16 v[68:71], a[128:131], v[48:51], v[68:71]\n" \
  "v_mfma_f32_16x16x32_bf16 v[76:79], a[128:131], v[52:55], v[76:79]\n" \
  "s_waitcnt lgkmcnt(5)\n" \
  "v_mfma_f32_16x16x32_bf16 v[64:67], a[60:63], v[56:59], v[64:67]\n" \
  "ds_read_b128 v[48:51], v191\n" \
  "s_waitcnt lgkmcnt(5)\n" \
  "v_mfma_f32_16x16x32_bf16 v[72:75], a[60:63], v[60:63], v[72:75]\n" \
  "ds_read_b128 v[52:55], v191 offset:18432\n" \
  "v_mfma_f32_16x16x32_bf16 v[68:71], a[132:135], v[56:59], v[68:71]\n" \
  "s_add_u32 m0, s52, 4096\n" \
  "s_waitcnt lgkmcnt(5)\n" \
  "v_mfma_f32_16x16x32_bf16 v[76:79], a[132:135], v[60:63], v[76:79]\n" \
  "buffer_load_dwordx4 v200, s[44:47], s51 offen lds\n" \
  "v_mfma_f32_16x16x32_bf16 v[64:67], a[64:67], v[32:35], v[64:67]\n" \
  "ds_read_b128 v[56:59], v192\n" \
  "s_waitcnt lgkmcnt(5)\n" \
  "v_mfma_f32_16x16x32_bf16 v[72:75], a[64:67], v[36:39], v[72:75]\n" \
  "ds_read_b128 v[60:63], v192 offset:18432\n" \
  "v_mfma_f32_16x16x32_bf16 v[68:71], a[136:139], v[32:35], v[68:71]\n" \
  "v_mfma_f32_16x16x32_bf16 v[76:79], a[136:139], v[36:39], v[76:79]\n" \
  "s_waitcnt lgkmcnt(5)\n" \
  "v_mfma_f32_16x16x32_bf16 v[64:67], a[68:71], v[40:43], v[64:67]\n" \
  "ds_read_b128 v[32:35], v191 offset:128\n" \
  "s_waitcnt lgkmcnt(5)\n" \
  "v_mfma_f32_16x16x32_bf16 v[72:75], a[68:71], v[44:47], v[72:75]\n" \
  "ds_read_b128 v[36:39], v191 offset:18560\n" \
  "v_mfma_f32_16x16x32_bf16 v[68:71], a[140:143], v[40:43], v[68:71]\n" \
  "v_mfma_f32_16x16x32_bf16 v[76:79], a[140:143], v[44:47], v[76:79]\n" \
  "s_waitcnt lgkmcnt(5)\n" \
  "v_mfma_f32_16x16x32_bf16 v[80:83], a[144:147], v[48:51], 0\n" \
  "ds_read_b128 v[40:43], v192 offset:128\n" \
  "s_waitcnt lgkmcnt(5)\n" \
  "v_mfma_f32_16x16x32_bf16 v[88:91], a[144:147], v[52:55], 0\n" \
  "ds_read_b128 v[44:47], v192 offset:18560\n" \
  "s_lshr_b32 s62, s49, 8\n" \
  "v_mfma_f32_16x16x32_bf16 v[84:87], a[216:219], v[48:51], 0\n" \
  "s_and_b32 s62, s62, 8192\n" \
  "s_xor_b32 s62, s62, s49\n" \
  "v_cvt_pk_bf16_f32 v96, v64, v65\n" \
  "v_mfma_f32_16x16x32_bf16 v[92:95], a[216:219], v[52:55], 0\n" \
  "s_waitcnt lgkmcnt(5)\n" \
  "v_cvt_pk_bf16_f32 v97, v66, v67\n" \
  "v_cvt_pk_bf16_f32 v98, v68, v69\n" \
  "v_mfma_f32_16x16x32_bf16 v[80:83], a[148:151], v[56:59], v[80:83]\n" \
  "ds_read_b128 v[48:51], v191 offset:256\n" \
  "s_waitcnt lgkmcnt(5)\n" \
  "v_cvt_pk_bf16_f32 v99, v70, v71\n" \
  "v_mfma_f32_16x16x32_bf16 v[88:91], a[148:151], v[60:63], v[88:91]\n" \
  "ds_read_b128 v[52:55], v191 offset:18688\n" \
  "s_lshl_b32 s63, s48, 14\n" \
  "v_mfma_f32_16x16x32_bf16 v[84:87], a[220:223], v[56:59], v[84:87]\n" \
  "s_xor_b32 s63, s63, s59\n" \
  "s_waitcnt lgkmcnt(5)\n" \
  "v_mfma_f32_16x16x32_bf16 v[92:95], a[220:223], v[60:63], v[92:95]\n" \
  "buffer_store_dwordx4 v[96:99], v190, s[40:43], s62 offen sc1\n" \
  "v_mfma_f32_16x16x32_bf16 v[80:83], a[152:155], v[32:35], v[80:83]\n" \
  "ds_read_b128 v[56:59], v192 offset:256\n" \
  "s_waitcnt lgkmcnt(5)\n" \
  "v_mfma_f32_16x16x32_bf16 v[88:91], a[152:155], v[36:39], v[88:91]\n" \
  "ds_read_b128 v[60:63], v192 offset:18688\n" \
  "v_mfma_f32_16x16x32_bf16 v[84:87], a[224:227], v[32:35], v[84:87]\n" \
  "buffer_store_dwordx4 v[96:99], v190, s[40:43], s63 offen sc1\n" \
  "v_mfma_f32_16x16x32_bf16 v[92:95], a[224:227], v[36:39], v[92:95]\n" \
  "s_waitcnt lgkmcnt(5)\n" \
  "v_cvt_pk_bf16_f32 v100, v72, v73\n" \
  "v_cvt_pk_bf16_f32 v101, v74, v75\n" \
  "v_mfma_f32_16x16x32_bf16 v[80:83], a[156:159], v[40:43], v[80:83]\n" \
  "ds_read_b128 v[32:35], v191 offset:384\n" \
  "s_waitcnt lgkmcnt(5)\n" \
  "v_cvt_pk_bf16_f32 v102, v76, v77\n" \
  "v_mfma_f32_16x16x32_bf16 v[88:91], a[156:159], v[44:47], v[88:91]\n" \
  "ds_read_b128 v[36:39], v191 offset:18816\n" \
  "v_cvt_pk_bf16_f32 v103, v78, v79\n" \
  "v_mfma_f32_16x16x32_bf16 v[84:87], a[228:231], v[40:43], v[84:87]\n" \
  "buffer_store_dwordx4 v[100:103], v190, s[40:43], s62 offen offset:1024 sc1\n" \
  "v_mfma_f32_16x16x32_bf16 v[92:95], a[228:231], v[44:47], v[92:95]\n" \
  "s_waitcnt lgkmcnt(5)\n" \
  "buffer_store_dwordx4 v[100:103], v190, s[40:43], s63 offen offset:1024 sc1\n" \
  "v_mfma_f32_16x16x32_bf16 v[80:83], a[160:163], v[48:51], v[80:83]\n" \
  "ds_read_b128 v[40:43], v192 offset:384\n" \
  "s_add_u32 s49, s49, 524288\n" \
  "s_waitcnt lgkmcnt(5)\n" \
  "v_mfma_f32_16x16x32_bf16 v[88:91], a[160:163], v[52:55], v[88:91]\n" \
  "ds_read_b128 v[44:47], v192 offset:18816\n" \
  "s_add_u32 s58, s48, 1\n" \
  "v_mfma_f32_16x16x32_bf16 v[84:87], a[232:235], v[48:51], v[84:87]\n" \
  "s_and_b32 s58, s58, 3\n" \
  "s_cselect_b32 s58, 0, 147456\n" \
  "s_sub_u32 s58, 36864, s58\n" \
  "v_mfma_f32_16x16x32_bf16 v[92:95], a[232:235], v[52:55], v[92:95]\n" \
  "s_waitcnt lgkmcnt(5)\n" \
  "v_add_u32 v165, s58, v191\n" \
  "v_add_u32 v164, s58, v192\n" \
  "v_mfma_f32_16x16x32_bf16 v[80:83], a[164:167], v[56:59], v[80:83]\n" \
  "ds_read_b128 v[48:51], v191 offset:512\n" \
  "s_waitcnt lgkmcnt(5)\n" \
  "v_mfma_f32_16x16x32_bf16 v[88:91], a[164:167], v[60:63], v[88:91]\n" \
  "ds_read_b128 v[52:55], v191 offset:18944\n" \
  "v_mfma_f32_16x16x32_bf16 v[84:87], a[236:239], v[56:59], v[84:87]\n" \
  "v_mfma_f32_16x16x32_bf16 v[92:95], a[236:239], v[60:63], v[92:95]\n" \
  "s_waitcnt lgkmcnt(5)\n" \
  "v_mfma_f32_16x16x32_bf16 v[80:83], a[168:171], v[32:35], v[80:83]\n" \
  "ds_read_b128 v[56:59], v192 offset:512\n" \
  "s_waitcnt lgkmcnt(5)\n" \
  "v_mfma_f32_16x16x32_bf16 v[88:91], a[168:171], v[36:39], v[88:91]\n" \
  "ds_read_b128 v[60:63], v192 offset:18944\n" \
  "v_mfma_f32_16x16x32_bf16 v[84:87], a[240:243], v[32:35], v[84:87]\n" \
  "v_mfma_f32_16x16x32_bf16 v[92:95], a[240:243], v[36:39], v[92:95]\n" \
  "s_waitcnt lgkmcnt(5)\n" \
  "v_mfma_f32_16x16x32_bf16 v[80:83], a[172:175], v[40:43], v[80:83]\n" \
  "ds_read_b128 v[32:35], v191 offset:640\n" \
  "s_waitcnt lgkmcnt(5)\n" \
  "v_mfma_f32_16x16x32_bf16 v[88:91], a[172:175], v[44:47], v[88:91]\n" \
  "ds_read_b128 v[36:39], v191 offset:19072\n" \
  "v_mfma_f32_16x16x32_bf16 v[84:87], a[244:247], v[40:43], v[84:87]\n" \
  "v_mfma_f32_16x16x32_bf16 v[92:95], a[244:247], v[44:47], v[92:95]\n" \
  "s_waitcnt lgkmcnt(5)\n" \
  "v_mfma_f32_16x16x32_bf16 v[80:83], a[176:179], v[48:51], v[80:83]\n" \
  "ds_read_b128 v[40:43], v192 offset:640\n" \
  "s_waitcnt lgkmcnt(5)\n" \
  "v_mfma_f32_16x16x32_bf16 v[88:91], a[176:179], v[52:55], v[88:91]\n" \
  "ds_read_b128 v[44:47], v192 offset:19072\n" \
  "v_mfma_f32_16x16x32_bf16 v[84:87], a[248:251], v[48:51], v[84:87]\n" \
  "s_add_u32 m0, s52, 5120\n" \
  "s_waitcnt lgkmcnt(5)\n" \
  "v_mfma_f32_16x16x32_bf16 v[92:95], a[248:251], v[52:55], v[92:95]\n" \
  "buffer_load_dwordx4 v201, s[44:47], s51 offen lds\n" \
  "v_mfma_f32_16x16x32_bf16 v[80:83], a[180:183], v[56:59], v[80:83]\n" \
  "ds_read_b128 v[48:51], v191 offset:768\n" \
  "s_waitcnt lgkmcnt(5)\n" \
  "v_mfma_f32_16x16x32_bf16 v[88:91], a[180:183], v[60:63], v[88:91]\n" \
  "ds_read_b128 v[52:55], v191 offset:19200\n" \
  "v_mfma_f32_16x16x32_bf16 v[84:87], a[252:255], v[56:59], v[84:87]\n" \
  "v_mfma_f32_16x16x32_bf16 v[92:95], a[252:255], v[60:63], v[92:95]\n" \
  "s_waitcnt lgkmcnt(5)\n" \
  "v_mfma_f32_16x16x32_bf16 v[80:83], a[184:187], v[32:35], v[80:83]\n" \
  "ds_read_b128 v[56:59], v192 offset:768\n" \
  "s_waitcnt lgkmcnt(5)\n" \
  "v_mfma_f32_16x16x32_bf16 v[88:91], a[184:187], v[36:39], v[88:91]\n" \
  "ds_read_b128 v[60:63], v192 offset:19200\n" \
  "v_mfma_f32_16x16x32_bf16 v[84:87], v[0:3], v[32:35], v[84:87]\n" \
  "s_add_u32 m0, s52, 6144\n" \
  "s_waitcnt lgkmcnt(5)\n" \
  "v_mfma_f32_16x16x32_bf16 v[92:95], v[0:3], v[36:39], v[92:95]\n" \
  "buffer_load_dwordx4 v202, s[44:47], s51 offen lds\n" \
  "v_mfma_f32_16x16x32_bf16 v[80:83], a[188:191], v[40:43], v[80:83]\n" \
  "ds_read_b128 v[32:35], v191 offset:896\n" \
  "s_waitcnt lgkmcnt(5)\n" \
  "v_mfma_f32_16x16x32_bf16 v[88:91], a[188:191], v[44:47], v[88:91]\n" \
  "ds_read_b128 v[36:39], v191 offset:19328\n" \
  "v_mfma_f32_16x16x32_bf16 v[84:87], v[4:7], v[40:43], v[84:87]\n" \
  "v_mfma_f32_16x16x32_bf16 v[92:95], v[4:7], v[44:47], v[92:95]\n" \
  "s_waitcnt lgkmcnt(5)\n" \
  "v_mfma_f32_16x16x32_bf16 v[80:83], a[192:195], v[48:51], v[80:83]\n" \
  "ds_read_b128 v[40:43], v192 offset:896\n" \
  "s_waitcnt lgkmcnt(5)\n" \
  "v_mfma_f32_16x16x32_bf16 v[88:91], a[192:195], v[52:55], v[88:91]\n" \
  "ds_read_b128 v[44:47], v192 offset:19328\n" \
  "v_mfma_f32_16x16x32_bf16 v[84:87], v[8:11], v[48:51], v[84:87]\n" \
  "s_add_u32 m0, s52, 7168\n" \
  "s_waitcnt lgkmcnt(5)\n" \
  "v_mfma_f32_16x16x32_bf16 v[92:95], v[8:11], v[52:55], v[92:95]\n" \
  "buffer_load_dwordx4 v203, s[44:47], s51 offen lds\n" \
  "v_mfma_f32_16x16x32_bf16 v[80:83], a[196:199], v[56:59], v[80:83]\n" \
  "ds_read_b128 v[48:51], v191 offset:1024\n" \
  "s_waitcnt lgkmcnt(5)\n" \
  "v_mfma_f32_16x16x32_bf16 v[88:91], a[196:199], v[60:63], v[88:91]\n" \
  "ds_read_b128 v[52:55], v191 offset:19456\n" \
  "v_mfma_f32_16x16x32_bf16 v[84:87], v[12:15], v[56:59], v[84:87]\n" \
  "v_mfma_f32_16x16x32_bf16 v[92:95], v[12:15], v[60:63], v[92:95]\n" \
  "s_waitcnt lgkmcnt(5)\n" \
  "v_mfma_f32_16x16x32_bf16 v[80:83], a[200:203], v[32:35], v[80:83]\n" \
  "ds_read_b128 v[56:59], v192 offset:1024\n" \
  "s_waitcnt lgkmcnt(5)\n" \
  "v_mfma_f32_16x16x32_bf16 v[88:91], a[200:203], v[36:39], v[88:91]\n" \
  "ds_read_b128 v[60:63], v192 offset:19456\n" \
  "v_mfma_f32_16x16x32_bf16 v[84:87], v[16:19], v[32:35], v[84:87]\n" \
  "s_add_u32 m0, s52, 8192\n" \
  "s_waitcnt lgkmcnt(5)\n" \
  "v_mfma_f32_16x16x32_bf16 v[92:95], v[16:19], v[36:39], v[92:95]\n" \
  "buffer_load_dwordx4 v204, s[44:47], s51 offen lds\n" \
  "v_mfma_f32_16x16x32_bf16 v[80:83], a[204:207], v[40:43], v[80:83]\n" \
  "ds_read_b128 v[32:35], v165\n" \
  "s_add_u32 s51, s51, 36864\n" \
  "s_waitcnt lgkmcnt(5)\n" \
  "v_mfma_f32_16x16x32_bf16 v[88:91], a[204:207], v[44:47], v[88:91]\n" \
  "ds_read_b128 v[36:39], v165 offset:18432\n" \
  "s_min_u32 s51, s51, 2322432\n" \
  "v_mfma_f32_16x16x32_bf16 v[84:87], v[20:23], v[40:43], v[84:87]\n" \
  "s_add_u32 s52, s52, 36864\n" \
  "v_mfma_f32_16x16x32_bf16 v[92:95], v[20:23], v[44:47], v[92:95]\n" \
  "s_cmp_ge_u32 s52, 163840\n" \
  "s_cselect_b32 s60, 147456, 0\n" \
  "s_sub_u32 s52, s52, s60\n" \
  "s_waitcnt lgkmcnt(5)\n" \
  "v_mfma_f32_16x16x32_bf16 v[80:83], a[208:211], v[48:51], v[80:83]\n" \
  "ds_read_b128 v[40:43], v164\n" \
  "s_waitcnt lgkmcnt(5)\n" \
  "v_mfma_f32_16x16x32_bf16 v[88:91], a[208:211], v[52:55], v[88:91]\n" \
  "ds_read_b128 v[44:47], v164 offset:18432\n" \
  "v_mfma_f32_16x16x32_bf16 v[84:87], v[24:27], v[48:51], v[84:87]\n" \
  "v_mov_b32 v191, v165\n" \
  "v_mov_b32 v192, v164\n" \
  "v_mfma_f32_16x16x32_bf16 v[92:95], v[24:27], v[52:55], v[92:95]\n" \
  "s_waitcnt lgkmcnt(5)\n" \
  "v_mfma_f32_16x16x32_bf16 v[80:83], a[212:215], v[56:59], v[80:83]\n" \
  "ds_read_b128 v[48:51], v165 offset:128\n" \
  "s_waitcnt lgkmcnt(5)\n" \
  "v_mfma_f32_16x16x32_bf16 v[88:91], a[212:215], v[60:63], v[88:91]\n" \
  "ds_read_b128 v[52:55], v165 offset:18560\n" \
  "v_mfma_f32_16x16x32_bf16 v[84:87], v[28:31], v[56:59], v[84:87]\n" \
  "s_add_u32 s48, s48, 1\n" \
  "s_cmp_lt_u32 s48, 58\n" \
  "v_mfma_f32_16x16x32_bf16 v[92:95], v[28:31], v[60:63], v[92:95]\n" \
  "s_cbranch_scc1 .LK_loop_%=\n" \
  "s_waitcnt lgkmcnt(0)\n" \
  "s_nop 7\n" \
  "s_lshr_b32 s62, s50, 8\n" \
  "s_and_b32 s62, s62, 8192\n" \
  "s_xor_b32 s62, s62, s50\n" \
  "v_cvt_pk_bf16_f32 v96, v80, v81\n" \
  "v_cvt_pk_bf16_f32 v97, v82, v83\n" \
  "v_cvt_pk_bf16_f32 v98, v84, v85\n" \
  "v_cvt_pk_bf16_f32 v99, v86, v87\n" \
  "buffer_store_dwordx4 v[96:99], v190, s[40:43], s62 offen sc1\n" \
  "v_cvt_pk_bf16_f32 v100, v88, v89\n" \
  "v_cvt_pk_bf16_f32 v101, v90, v91\n" \
  "v_cvt_pk_bf16_f32 v102, v92, v93\n" \
  "v_cvt_pk_bf16_f32 v103, v94, v95\n" \
  "buffer_store_dwordx4 v[100:103], v190, s[40:43], s62 offen offset:1024 sc1\n" \
  "s_waitcnt vmcnt(0)\n" \
  "s_barrier\n" \
  "s_mov_b32 s48, 64\n" \
  "s_mov_b32 s69, 0\n" \
  ".LK_ie_%=:\n" \
  "s_cmp_ge_u32 s96, 0xc0000000\n" \
  "s_cbranch_scc1 .LK_ie_x_%=\n" \
  "buffer_load_dwordx2 v[184:185], off, s[40:43], %41 sc1\n" \
  "s_waitcnt vmcnt(0)\n" \
  "s_add_u32 s69, s69, 1\n" \
  "s_cmp_lt_u32 s69, 2097152\n" \
  "v_readfirstlane_b32 s96, v184\n" \
  "s_cbranch_scc1 .LK_ie_%=\n" \
  ".LK_ie_x_%=:\n" \
  "v_mov_b32 v168, s48\n" \
  "buffer_store_dwordx2 v[168:169], v167, s[40:43], 0 offen sc1\n" \
  "s_branch .LK_end_%=\n" \
  ".LK_mea_%=:\n" \
  "s_waitcnt vmcnt(7)\n" \
  "s_barrier\n" \
  "v_mov_b32 v168, s56\n" \
  "s_mov_b64 exec, 1\n" \
  "buffer_store_dwordx2 v[168:169], v167, s[40:43], 0 offen sc1\n" \
  "s_mov_b64 exec, -1\n" \
  "s_branch .LK_mea_b_%=\n" \
  ".LK_meb_%=:\n" \
  "s_waitcnt vmcnt(7)\n" \
  "s_barrier\n" \
  "v_mov_b32 v168, s56\n" \
  "s_mov_b64 exec, 1\n" \
  "buffer_store_dwordx2 v[168:169], v167, s[40:43], 0 offen sc1\n" \
  "s_mov_b64 exec, -1\n" \
  "s_cmp_lg_u32 s48, 4\n" \
  "s_cbranch_scc1 .LK_in_x_%=\n" \
  "s_mov_b32 s69, 0\n" \
  ".LK_in_%=:\n" \
  "s_cmp_ge_u32 s96, 0xc0000000\n" \
  "s_cbranch_scc1 .LK_in_x_%=\n" \
  "buffer_load_dwordx2 v[184:185], off, s[40:43], %41 sc1\n" \
  "s_waitcnt vmcnt(0)\n" \
  "s_add_u32 s69, s69, 1\n" \
  "s_cmp_lt_u32 s69, 2097152\n" \
  "v_readfirstlane_b32 s96, v184\n" \
  "s_cbranch_scc1 .LK_in_%=\n" \
  ".LK_in_x_%=:\n" \
  "s_branch .LK_meb_b_%=\n" \
  ".LK_end_%=:\n" \
  "s_mov_b32 %0, s55\n"
#define K_FIRST 20
#define U_HELP 14
#define U_THIRD 0
#define U_WATCH 10
#define HAND_B 8
#define U_LEAD 14
#define VO_SWAP 1
static_assert(SDIR == 58, "generator");
#define U_ASM_TEXT \
  "s_mov_b64 s[40:41], %30\n" \
  "s_and_b32 s41, s41, 0xffff\n" \
  "s_mov_b32 s42, 255852544\n" \
  "s_mov_b32 s43, 0x00020000\n" \
  "s_mov_b32 s47, 0x00020000\n" \
  "s_lshl_b32 s72, %34, 12\n" \
  "buffer_load_dword v184, off, s[40:43], %42 sc1\n" \
  "s_and_b32 s73, %45, 255\n" \
  "s_bfe_u32 s74, %45, 0x80008\n" \
  "s_lshr_b32 s68, %45, 16\n" \
  "s_mov_b32 s75, s73\n" \
  "s_mov_b32 s70, 2\n" \
  "s_mov_b64 s[44:45], %31\n" \
  "s_and_b32 s45, s45, 0xffff\n" \
  "s_mov_b32 s46, 4718592\n" \
  "s_add_u32 s60, %35, 0\n" \
  "s_add_u32 m0, s72, 16384\n" \
  "s_nop 0\n" \
  "buffer_load_dwordx4 v170, s[44:47], s60 offen lds\n" \
  "buffer_load_dwordx4 v171, s[44:47], s60 offen offset:1024 lds\n" \
  "buffer_load_dwordx4 v172, s[44:47], s60 offen offset:2048 lds\n" \
  "buffer_load_dwordx4 v173, s[44:47], s60 offen offset:3072 lds\n" \
  "s_add_u32 s60, %35, 524288\n" \
  "s_add_u32 m0, s72, 32768\n" \
  "s_nop 0\n" \
  "buffer_load_dwordx4 v170, s[44:47], s60 offen lds\n" \
  "buffer_load_dwordx4 v171, s[44:47], s60 offen offset:1024 lds\n" \
  "buffer_load_dwordx4 v172, s[44:47], s60 offen offset:2048 lds\n" \
  "buffer_load_dwordx4 v173, s[44:47], s60 offen offset:3072 lds\n" \
  "s_add_u32 s60, %35, 1048576\n" \
  "s_add_u32 m0, s72, 49152\n" \
  "s_nop 0\n" \
  "buffer_load_dwordx4 v170, s[44:47], s60 offen lds\n" \
  "buffer_load_dwordx4 v171, s[44:47], s60 offen offset:1024 lds\n" \
  "buffer_load_dwordx4 v172, s[44:47], s60 offen offset:2048 lds\n" \
  "buffer_load_dwordx4 v173, s[44:47], s60 offen offset:3072 lds\n" \
  "s_add_u32 s60, %35, 1572864\n" \
  "s_add_u32 m0, s72, 65536\n" \
  "s_nop 0\n" \
  "buffer_load_dwordx4 v170, s[44:47], s60 offen lds\n" \
  "buffer_load_dwordx4 v171, s[44:47], s60 offen offset:1024 lds\n" \
  "buffer_load_dwordx4 v172, s[44:47], s60 offen offset:2048 lds\n" \
  "buffer_load_dwordx4 v173, s[44:47], s60 offen offset:3072 lds\n" \
  "s_add_u32 s60, %35, 2097152\n" \
  "s_add_u32 m0, s72, 81920\n" \
  "s_nop 0\n" \
  "buffer_load_dwordx4 v170, s[44:47], s60 offen lds\n" \
  "buffer_load_dwordx4 v171, s[44:47], s60 offen offset:1024 lds\n" \
  "buffer_load_dwordx4 v172, s[44:47], s60 offen offset:2048 lds\n" \
  "buffer_load_dwordx4 v173, s[44:47], s60 offen offset:3072 lds\n" \
  "s_add_u32 s60, %35, 2621440\n" \
  "s_add_u32 m0, s72, 98304\n" \
  "s_nop 0\n" \
  "buffer_load_dwordx4 v170, s[44:47], s60 offen lds\n" \
  "buffer_load_dwordx4 v171, s[44:47], s60 offen offset:1024 lds\n" \
  "buffer_load_dwordx4 v172, s[44:47], s60 offen offset:2048 lds\n" \
  "buffer_load_dwordx4 v173, s[44:47], s60 offen offset:3072 lds\n" \
  "s_add_u32 s60, %35, 3145728\n" \
  "s_add_u32 m0, s72, 114688\n" \
  "s_nop 0\n" \
  "buffer_load_dwordx4 v170, s[44:47], s60 offen lds\n" \
  "buffer_load_dwordx4 v171, s[44:47], s60 offen offset:1024 lds\n" \
  "buffer_load_dwordx4 v172, s[44:47], s60 offen offset:2048 lds\n" \
  "buffer_load_dwordx4 v173, s[44:47], s60 offen offset:3072 lds\n" \
  "s_add_u32 s60, %35, 3670016\n" \
  "s_add_u32 m0, s72, 131072\n" \
  "s_nop 0\n" \
  "buffer_load_dwordx4 v170, s[44:47], s60 offen lds\n" \
  "buffer_load_dwordx4 v171, s[44:47], s60 offen offset:1024 lds\n" \
  "buffer_load_dwordx4 v172, s[44:47], s60 offen offset:2048 lds\n" \
  "buffer_load_dwordx4 v173, s[44:47], s60 offen offset:3072 lds\n" \
  "s_add_u32 s60, %35, 4194304\n" \
  "s_add_u32 m0, s72, 147456\n" \
  "s_nop 0\n" \
  "buffer_load_dwordx4 v170, s[44:47], s60 offen lds\n" \
  "buffer_load_dwordx4 v171, s[44:47], s60 offen offset:1024 lds\n" \
  "buffer_load_dwordx4 v172, s[44:47], s60 offen offset:2048 lds\n" \
  "buffer_load_dwordx4 v173, s[44:47], s60 offen offset:3072 lds\n" \
  "s_waitcnt vmcnt(24)\n" \
  "s_barrier\n" \
  "v_add_u32 v186, 16384, v182\n" \
  "ds_read_b64_tr_b16 a[0:1], v186\n" \
  "ds_read_b64_tr_b16 a[2:3], v186 offset:1024\n" \
  "ds_read_b64_tr_b16 a[4:5], v186 offset:8192\n" \
  "ds_read_b64_tr_b16 a[6:7], v186 offset:9216\n" \
  "v_add_u32 v187, 16384, v183\n" \
  "ds_read_b64_tr_b16 a[72:73], v187\n" \
  "ds_read_b64_tr_b16 a[74:75], v187 offset:1024\n" \
  "ds_read_b64_tr_b16 a[76:77], v187 offset:8192\n" \
  "ds_read_b64_tr_b16 a[78:79], v187 offset:9216\n" \
  "v_add_u32 v186, 32768, v182\n" \
  "ds_read_b64_tr_b16 a[8:9], v186\n" \
  "ds_read_b64_tr_b16 a[10:11], v186 offset:1024\n" \
  "ds_read_b64_tr_b16 a[12:13], v186 offset:8192\n" \
  "ds_read_b64_tr_b16 a[14:15], v186 offset:9216\n" \
  "v_add_u32 v187, 32768, v183\n" \
  "ds_read_b64_tr_b16 a[80:81], v187\n" \
  "ds_read_b64_tr_b16 a[82:83], v187 offset:1024\n" \
  "ds_read_b64_tr_b16 a[84:85], v187 offset:8192\n" \
  "ds_read_b64_tr_b16 a[86:87], v187 offset:9216\n" \
  "v_add_u32 v186, 49152, v182\n" \
  "ds_read_b64_tr_b16 a[16:17], v186\n" \
  "ds_read_b64_tr_b16 a[18:19], v186 offset:1024\n" \
  "ds_read_b64_tr_b16 a[20:21], v186 offset:8192\n" \
  "ds_read_b64_tr_b16 a[22:23], v186 offset:9216\n" \
  "v_add_u32 v187, 49152, v183\n" \
  "ds_read_b64_tr_b16 a[88:89], v187\n" \
  "ds_read_b64_tr_b16 a[90:91], v187 offset:1024\n" \
  "ds_read_b64_tr_b16 a[92:93], v187 offset:8192\n" \
  "ds_read_b64_tr_b16 a[94:95], v187 offset:9216\n" \
  "s_waitcnt lgkmcnt(0)\n" \
  "s_barrier\n" \
  "s_mov_b64 s[44:45], %32\n" \
  "s_and_b32 s45, s45, 0xffff\n" \
  "s_mov_b32 s46, 4718592\n" \
  "s_add_u32 s60, %36, 0\n" \
  "s_add_u32 m0, s72, 16384\n" \
  "s_nop 0\n" \
  "buffer_load_dwordx4 v174, s[44:47], s60 offen lds\n" \
  "buffer_load_dwordx4 v175, s[44:47], s60 offen offset:1024 lds\n" \
  "buffer_load_dwordx4 v176, s[44:47], s60 offen offset:2048 lds\n" \
  "buffer_load_dwordx4 v177, s[44:47], s60 offen offset:3072 lds\n" \
  "s_add_u32 s60, %36, 73728\n" \
  "s_add_u32 m0, s72, 32768\n" \
  "s_nop 0\n" \
  "buffer_load_dwordx4 v174, s[44:47], s60 offen lds\n" \
  "buffer_load_dwordx4 v175, s[44:47], s60 offen offset:1024 lds\n" \
  "buffer_load_dwordx4 v176, s[44:47], s60 offen offset:2048 lds\n" \
  "buffer_load_dwordx4 v177, s[44:47], s60 offen offset:3072 lds\n" \
  "s_add_u32 s60, %36, 256\n" \
  "s_add_u32 m0, s72, 49152\n" \
  "s_nop 0\n" \
  "buffer_load_dwordx4 v174, s[44:47], s60 offen lds\n" \
  "buffer_load_dwordx4 v175, s[44:47], s60 offen offset:1024 lds\n" \
  "buffer_load_dwordx4 v176, s[44:47], s60 offen offset:2048 lds\n" \
  "buffer_load_dwordx4 v177, s[44:47], s60 offen offset:3072 lds\n" \
  "v_readfirstlane_b32 s55, v184\n" \
  "s_add_u32 s55, s55, 1\n" \
  "s_waitcnt vmcnt(24)\n" \
  "s_barrier\n" \
  "v_add_u32 v186, 65536, v182\n" \
  "ds_read_b64_tr_b16 a[24:25], v186\n" \
  "ds_read_b64_tr_b16 a[26:27], v186 offset:1024\n" \
  "ds_read_b64_tr_b16 a[28:29], v186 offset:8192\n" \
  "ds_read_b64_tr_b16 a[30:31], v186 offset:9216\n" \
  "v_add_u32 v187, 65536, v183\n" \
  "ds_read_b64_tr_b16 a[96:97], v187\n" \
  "ds_read_b64_tr_b16 a[98:99], v187 offset:1024\n" \
  "ds_read_b64_tr_b16 a[100:101], v187 offset:8192\n" \
  "ds_read_b64_tr_b16 a[102:103], v187 offset:9216\n" \
  "v_add_u32 v186, 81920, v182\n" \
  "ds_read_b64_tr_b16 a[32:33], v186\n" \
  "ds_read_b64_tr_b16 a[34:35], v186 offset:1024\n" \
  "ds_read_b64_tr_b16 a[36:37], v186 offset:8192\n" \
  "ds_read_b64_tr_b16 a[38:39], v186 offset:9216\n" \
  "v_add_u32 v187, 81920, v183\n" \
  "ds_read_b64_tr_b16 a[104:105], v187\n" \
  "ds_read_b64_tr_b16 a[106:107], v187 offset:1024\n" \
  "ds_read_b64_tr_b16 a[108:109], v187 offset:8192\n" \
  "ds_read_b64_tr_b16 a[110:111], v187 offset:9216\n" \
  "v_add_u32 v186, 98304, v182\n" \
  "ds_read_b64_tr_b16 a[40:41], v186\n" \
  "ds_read_b64_tr_b16 a[42:43], v186 offset:1024\n" \
  "ds_read_b64_tr_b16 a[44:45], v186 offset:8192\n" \
  "ds_read_b64_tr_b16 a[46:47], v186 offset:9216\n" \
  "v_add_u32 v187, 98304, v183\n" \
  "ds_read_b64_tr_b16 a[112:113], v187\n" \
  "ds_read_b64_tr_b16 a[114:115], v187 offset:1024\n" \
  "ds_read_b64_tr_b16 a[116:117], v187 offset:8192\n" \
  "ds_read_b64_tr_b16 a[118:119], v187 offset:9216\n" \
  "s_waitcnt lgkmcnt(0)\n" \
  "s_barrier\n" \
  "s_add_u32 s60, %36, 73984\n" \
  "s_add_u32 m0, s72, 65536\n" \
  "s_nop 0\n" \
  "buffer_load_dwordx4 v174, s[44:47], s60 offen lds\n" \
  "buffer_load_dwordx4 v175, s[44:47], s60 offen offset:1024 lds\n" \
  "buffer_load_dwordx4 v176, s[44:47], s60 offen offset:2048 lds\n" \
  "buffer_load_dwordx4 v177, s[44:47], s60 offen offset:3072 lds\n" \
  "s_add_u32 s60, %36, 512\n" \
  "s_add_u32 m0, s72, 81920\n" \
  "s_nop 0\n" \
  "buffer_load_dwordx4 v174, s[44:47], s60 offen lds\n" \
  "buffer_load_dwordx4 v175, s[44:47], s60 offen offset:1024 lds\n" \
  "buffer_load_dwordx4 v176, s[44:47], s60 offen offset:2048 lds\n" \
  "buffer_load_dwordx4 v177, s[44:47], s60 offen offset:3072 lds\n" \
  "s_add_u32 s60, %36, 74240\n" \
  "s_add_u32 m0, s72, 98304\n" \
  "s_nop 0\n" \
  "buffer_load_dwordx4 v174, s[44:47], s60 offen lds\n" \
  "buffer_load_dwordx4 v175, s[44:47], s60 offen offset:1024 lds\n" \
  "buffer_load_dwordx4 v176, s[44:47], s60 offen offset:2048 lds\n" \
  "buffer_load_dwordx4 v177, s[44:47], s60 offen offset:3072 lds\n" \
  "s_waitcnt vmcnt(24)\n" \
  "s_barrier\n" \
  "v_add_u32 v186, 114688, v182\n" \
  "ds_read_b64_tr_b16 a[48:49], v186\n" \
  "ds_read_b64_tr_b16 a[50:51], v186 offset:1024\n" \
  "ds_read_b64_tr_b16 a[52:53], v186 offset:8192\n" \
  "ds_read_b64_tr_b16 a[54:55], v186 offset:9216\n" \
  "v_add_u32 v187, 114688, v183\n" \
  "ds_read_b64_tr_b16 a[120:121], v187\n" \
  "ds_read_b64_tr_b16 a[122:123], v187 offset:1024\n" \
  "ds_read_b64_tr_b16 a[124:125], v187 offset:8192\n" \
  "ds_read_b64_tr_b16 a[126:127], v187 offset:9216\n" \
  "v_add_u32 v186, 131072, v182\n" \
  "ds_read_b64_tr_b16 a[56:57], v186\n" \
  "ds_read_b64_tr_b16 a[58:59], v186 offset:1024\n" \
  "ds_read_b64_tr_b16 a[60:61], v186 offset:8192\n" \
  "ds_read_b64_tr_b16 a[62:63], v186 offset:9216\n" \
  "v_add_u32 v187, 131072, v183\n" \
  "ds_read_b64_tr_b16 a[128:129], v187\n" \
  "ds_read_b64_tr_b16 a[130:131], v187 offset:1024\n" \
  "ds_read_b64_tr_b16 a[132:133], v187 offset:8192\n" \
  "ds_read_b64_tr_b16 a[134:135], v187 offset:9216\n" \
  "v_add_u32 v186, 147456, v182\n" \
  "ds_read_b64_tr_b16 a[64:65], v186\n" \
  "ds_read_b64_tr_b16 a[66:67], v186 offset:1024\n" \
  "ds_read_b64_tr_b16 a[68:69], v186 offset:8192\n" \
  "ds_read_b64_tr_b16 a[70:71], v186 offset:9216\n" \
  "v_add_u32 v187, 147456, v183\n" \
  "ds_read_b64_tr_b16 a[136:137], v187\n" \
  "ds_read_b64_tr_b16 a[138:139], v187 offset:1024\n" \
  "ds_read_b64_tr_b16 a[140:141], v187 offset:8192\n" \
  "ds_read_b64_tr_b16 a[142:143], v187 offset:9216\n" \
  "s_waitcnt lgkmcnt(0)\n" \
  "s_barrier\n" \
  "s_add_u32 s60, %36, 768\n" \
  "s_add_u32 m0, s72, 114688\n" \
  "s_nop 0\n" \
  "buffer_load_dwordx4 v174, s[44:47], s60 offen lds\n" \
  "buffer_load_dwordx4 v175, s[44:47], s60 offen offset:1024 lds\n" \
  "buffer_load_dwordx4 v176, s[44:47], s60 offen offset:2048 lds\n" \
  "buffer_load_dwordx4 v177, s[44:47], s60 offen offset:3072 lds\n" \
  "s_add_u32 s60, %36, 74496\n" \
  "s_add_u32 m0, s72, 131072\n" \
  "s_nop 0\n" \
  "buffer_load_dwordx4 v174, s[44:47], s60 offen lds\n" \
  "buffer_load_dwordx4 v175, s[44:47], s60 offen offset:1024 lds\n" \
  "buffer_load_dwordx4 v176, s[44:47], s60 offen offset:2048 lds\n" \
  "buffer_load_dwordx4 v177, s[44:47], s60 offen offset:3072 lds\n" \
  "s_add_u32 s60, %36, 0\n" \
  "s_add_u32 m0, s72, 147456\n" \
  "s_nop 0\n" \
  "buffer_load_dwordx4 v178, s[44:47], s60 offen lds\n" \
  "buffer_load_dwordx4 v179, s[44:47], s60 offen offset:1024 lds\n" \
  "buffer_load_dwordx4 v180, s[44:47], s60 offen offset:2048 lds\n" \
  "buffer_load_dwordx4 v181, s[44:47], s60 offen offset:3072 lds\n" \
  "s_waitcnt vmcnt(24)\n" \
  "s_barrier\n" \
  "v_add_u32 v186, 16384, v182\n" \
  "ds_read_b64_tr_b16 a[144:145], v186\n" \
  "ds_read_b64_tr_b16 a[146:147], v186 offset:1024\n" \
  "ds_read_b64_tr_b16 a[148:149], v186 offset:8192\n" \
  "ds_read_b64_tr_b16 a[150:151], v186 offset:9216\n" \
  "v_add_u32 v187, 16384, v183\n" \
  "ds_read_b64_tr_b16 a[160:161], v187\n" \
  "ds_read_b64_tr_b16 a[162:163], v187 offset:1024\n" \
  "ds_read_b64_tr_b16 a[164:165], v187 offset:8192\n" \
  "ds_read_b64_tr_b16 a[166:167], v187 offset:9216\n" \
  "v_add_u32 v186, 32768, v182\n" \
  "ds_read_b64_tr_b16 a[152:153], v186\n" \
  "ds_read_b64_tr_b16 a[154:155], v186 offset:1024\n" \
  "ds_read_b64_tr_b16 a[156:157], v186 offset:8192\n" \
  "ds_read_b64_tr_b16 a[158:159], v186 offset:9216\n" \
  "v_add_u32 v187, 32768, v183\n" \
  "ds_read_b64_tr_b16 a[168:169], v187\n" \
  "ds_read_b64_tr_b16 a[170:171], v187 offset:1024\n" \
  "ds_read_b64_tr_b16 a[172:173], v187 offset:8192\n" \
  "ds_read_b64_tr_b16 a[174:175], v187 offset:9216\n" \
  "v_add_u32 v186, 49152, v182\n" \
  "ds_read_b64_tr_b16 a[176:177], v186\n" \
  "ds_read_b64_tr_b16 a[178:179], v186 offset:1024\n" \
  "ds_read_b64_tr_b16 a[180:181], v186 offset:8192\n" \
  "ds_read_b64_tr_b16 a[182:183], v186 offset:9216\n" \
  "v_add_u32 v187, 49152, v183\n" \
  "ds_read_b64_tr_b16 a[192:193], v187\n" \
  "ds_read_b64_tr_b16 a[194:195], v187 offset:1024\n" \
  "ds_read_b64_tr_b16 a[196:197], v187 offset:8192\n" \
  "ds_read_b64_tr_b16 a[198:199], v187 offset:9216\n" \
  "s_waitcnt lgkmcnt(0)\n" \
  "s_barrier\n" \
  "s_mov_b64 s[44:45], %33\n" \
  "s_and_b32 s45, s45, 0xffff\n" \
  "s_mov_b32 s46, 2359296\n" \
  "s_mul_i32 s51, s73, 36864\n" \
  "s_mul_i32 s52, %34, 9216\n" \
  "s_add_u32 s52, s52, 16384\n" \
  "s_add_u32 m0, s52, 0\n" \
  "s_nop 0\n" \
  "buffer_load_dwordx4 v196, s[44:47], s51 offen lds\n" \
  "s_add_u32 m0, s52, 1024\n" \
  "s_nop 0\n" \
  "buffer_load_dwordx4 v197, s[44:47], s51 offen lds\n" \
  "s_add_u32 m0, s52, 2048\n" \
  "s_nop 0\n" \
  "buffer_load_dwordx4 v198, s[44:47], s51 offen lds\n" \
  "s_add_u32 m0, s52, 3072\n" \
  "s_nop 0\n" \
  "buffer_load_dwordx4 v199, s[44:47], s51 offen lds\n" \
  "s_add_u32 m0, s52, 4096\n" \
  "s_nop 0\n" \
  "buffer_load_dwordx4 v200, s[44:47], s51 offen lds\n" \
  "s_add_u32 m0, s52, 5120\n" \
  "s_nop 0\n" \
  "buffer_load_dwordx4 v201, s[44:47], s51 offen lds\n" \
  "s_add_u32 m0, s52, 6144\n" \
  "s_nop 0\n" \
  "buffer_load_dwordx4 v202, s[44:47], s51 offen lds\n" \
  "s_add_u32 m0, s52, 7168\n" \
  "s_nop 0\n" \
  "buffer_load_dwordx4 v203, s[44:47], s51 offen lds\n" \
  "s_add_u32 m0, s52, 8192\n" \
  "s_nop 0\n" \
  "buffer_load_dwordx4 v204, s[44:47], s51 offen lds\n" \
  "s_cmp_lt_u32 s51, 1032192\n" \
  "s_cselect_b32 s60, 36864, 0\n" \
  "s_add_u32 s51, s51, s60\n" \
  "s_add_u32 s51, s51, 36864\n" \
  "s_min_u32 s51, s51, 2322432\n" \
  "s_add_u32 s52, s52, 36864\n" \
  "s_cmp_ge_u32 s52, 163840\n" \
  "s_cselect_b32 s60, 147456, 0\n" \
  "s_sub_u32 s52, s52, s60\n" \
  "s_cmp_eq_u32 s73, 0\n" \
  "s_cselect_b32 s51, 73728, s51\n" \
  "s_waitcnt vmcnt(21)\n" \
  "s_barrier\n" \
  "v_add_u32 v186, 65536, v182\n" \
  "ds_read_b64_tr_b16 a[184:185], v186\n" \
  "ds_read_b64_tr_b16 a[186:187], v186 offset:1024\n" \
  "ds_read_b64_tr_b16 a[188:189], v186 offset:8192\n" \
  "ds_read_b64_tr_b16 a[190:191], v186 offset:9216\n" \
  "v_add_u32 v187, 65536, v183\n" \
  "ds_read_b64_tr_b16 a[200:201], v187\n" \
  "ds_read_b64_tr_b16 a[202:203], v187 offset:1024\n" \
  "ds_read_b64_tr_b16 a[204:205], v187 offset:8192\n" \
  "ds_read_b64_tr_b16 a[206:207], v187 offset:9216\n" \
  "v_add_u32 v186, 81920, v182\n" \
  "ds_read_b64_tr_b16 a[208:209], v186\n" \
  "ds_read_b64_tr_b16 a[210:211], v186 offset:1024\n" \
  "ds_read_b64_tr_b16 a[212:213], v186 offset:8192\n" \
  "ds_read_b64_tr_b16 a[214:215], v186 offset:9216\n" \
  "v_add_u32 v187, 81920, v183\n" \
  "ds_read_b64_tr_b16 a[224:225], v187\n" \
  "ds_read_b64_tr_b16 a[226:227], v187 offset:1024\n" \
  "ds_read_b64_tr_b16 a[228:229], v187 offset:8192\n" \
  "ds_read_b64_tr_b16 a[230:231], v187 offset:9216\n" \
  "v_add_u32 v186, 98304, v182\n" \
  "ds_read_b64_tr_b16 a[216:217], v186\n" \
  "ds_read_b64_tr_b16 a[218:219], v186 offset:1024\n" \
  "ds_read_b64_tr_b16 a[220:221], v186 offset:8192\n" \
  "ds_read_b64_tr_b16 a[222:223], v186 offset:9216\n" \
  "v_add_u32 v187, 98304, v183\n" \
  "ds_read_b64_tr_b16 a[232:233], v187\n" \
  "ds_read_b64_tr_b16 a[234:235], v187 offset:1024\n" \
  "ds_read_b64_tr_b16 a[236:237], v187 offset:8192\n" \
  "ds_read_b64_tr_b16 a[238:239], v187 offset:9216\n" \
  "s_waitcnt lgkmcnt(0)\n" \
  "s_barrier\n" \
  "buffer_load_dwordx2 v[184:185], off, s[40:43], %41 sc1\n" \
  "s_add_u32 m0, s52, 0\n" \
  "s_nop 0\n" \
  "buffer_load_dwordx4 v196, s[44:47], s51 offen lds\n" \
  "s_add_u32 m0, s52, 1024\n" \
  "s_nop 0\n" \
  "buffer_load_dwordx4 v197, s[44:47], s51 offen lds\n" \
  "s_add_u32 m0, s52, 2048\n" \
  "s_nop 0\n" \
  "buffer_load_dwordx4 v198, s[44:47], s51 offen lds\n" \
  "s_add_u32 m0, s52, 3072\n" \
  "s_nop 0\n" \
  "buffer_load_dwordx4 v199, s[44:47], s51 offen lds\n" \
  "s_add_u32 m0, s52, 4096\n" \
  "s_nop 0\n" \
  "buffer_load_dwordx4 v200, s[44:47], s51 offen lds\n" \
  "s_add_u32 m0, s52, 5120\n" \
  "s_nop 0\n" \
  "buffer_load_dwordx4 v201, s[44:47], s51 offen lds\n" \
  "s_add_u32 m0, s52, 6144\n" \
  "s_nop 0\n" \
  "buffer_load_dwordx4 v202, s[44:47], s51 offen lds\n" \
  "s_add_u32 m0, s52, 7168\n" \
  "s_nop 0\n" \
  "buffer_load_dwordx4 v203, s[44:47], s51 offen lds\n" \
  "s_add_u32 m0, s52, 8192\n" \
  "s_nop 0\n" \
  "buffer_load_dwordx4 v204, s[44:47], s51 offen lds\n" \
  "s_cmp_lt_u32 s51, 1032192\n" \
  "s_cselect_b32 s60, 36864, 0\n" \
  "s_add_u32 s51, s51, s60\n" \
  "s_add_u32 s51, s51, 36864\n" \
  "s_min_u32 s51, s51, 2322432\n" \
  "s_add_u32 s52, s52, 36864\n" \
  "s_cmp_ge_u32 s52, 163840\n" \
  "s_cselect_b32 s60, 147456, 0\n" \
  "s_sub_u32 s52, s52, s60\n" \
  "s_waitcnt vmcnt(19)\n" \
  "s_barrier\n" \
  "v_add_u32 v186, 114688, v182\n" \
  "ds_read_b64_tr_b16 a[240:241], v186\n" \
  "ds_read_b64_tr_b16 a[242:243], v186 offset:1024\n" \
  "ds_read_b64_tr_b16 a[244:245], v186 offset:8192\n" \
  "ds_read_b64_tr_b16 a[246:247], v186 offset:9216\n" \
  "v_add_u32 v187, 114688, v183\n" \
  "ds_read_b64_tr_b16 v[0:1], v187\n" \
  "ds_read_b64_tr_b16 v[2:3], v187 offset:1024\n" \
  "ds_read_b64_tr_b16 v[4:5], v187 offset:8192\n" \
  "ds_read_b64_tr_b16 v[6:7], v187 offset:9216\n" \
  "v_add_u32 v186, 131072, v182\n" \
  "ds_read_b64_tr_b16 a[248:249], v186\n" \
  "ds_read_b64_tr_b16 a[250:251], v186 offset:1024\n" \
  "ds_read_b64_tr_b16 a[252:253], v186 offset:8192\n" \
  "ds_read_b64_tr_b16 a[254:255], v186 offset:9216\n" \
  "v_add_u32 v187, 131072, v183\n" \
  "ds_read_b64_tr_b16 v[8:9], v187\n" \
  "ds_read_b64_tr_b16 v[10:11], v187 offset:1024\n" \
  "ds_read_b64_tr_b16 v[12:13], v187 offset:8192\n" \
  "ds_read_b64_tr_b16 v[14:15], v187 offset:9216\n" \
  "v_add_u32 v186, 147456, v182\n" \
  "ds_read_b64_tr_b16 v[16:17], v186\n" \
  "ds_read_b64_tr_b16 v[18:19], v186 offset:1024\n" \
  "ds_read_b64_tr_b16 v[20:21], v186 offset:8192\n" \
  "ds_read_b64_tr_b16 v[22:23], v186 offset:9216\n" \
  "v_add_u32 v187, 147456, v183\n" \
  "ds_read_b64_tr_b16 v[24:25], v187\n" \
  "ds_read_b64_tr_b16 v[26:27], v187 offset:1024\n" \
  "ds_read_b64_tr_b16 v[28:29], v187 offset:8192\n" \
  "ds_read_b64_tr_b16 v[30:31], v187 offset:9216\n" \
  "s_waitcnt lgkmcnt(0)\n" \
  "s_mov_b32 s69, 0\n" \
  "s_waitcnt vmcnt(9)\n" \
  "s_branch .LU_go1_%=\n" \
  ".LU_go_%=:\n" \
  "buffer_load_dwordx2 v[184:185], off, s[40:43], %41 sc1\n" \
  "s_waitcnt vmcnt(0)\n" \
  ".LU_go1_%=:\n" \
  "v_readfirstlane_b32 s96, v184\n" \
  "v_readfirstlane_b32 s63, v185\n" \
  "s_cmp_eq_u32 s63, s55\n" \
  "s_cselect_b32 s96, s96, 0\n" \
  "s_cmp_ge_u32 s96, 0x80000000\n" \
  "s_cbranch_scc1 .LU_go_x_%=\n" \
  "s_sleep 4\n" \
  "s_add_u32 s69, s69, 1\n" \
  "s_cmp_lt_u32 s69, 2097152\n" \
  "s_cbranch_scc1 .LU_go_%=\n" \
  "v_mov_b32 v184, 1\n" \
  "v_mov_b32 v185, s55\n" \
  "buffer_store_dwordx2 v[184:185], off, s[40:43], %42 offset:128 sc1\n" \
  ".LU_go_x_%=:\n" \
  "s_and_b32 s60, s96, 2\n" \
  "s_lshl_b32 s60, s60, 12\n" \
  "s_xor_b32 s53, %39, s60\n" \
  "s_xor_b32 s54, %40, s60\n" \
  "s_andn2_b32 s60, 2, s96\n" \
  "s_lshl_b32 s60, s60, 12\n" \
  "s_add_u32 s49, %37, s60\n" \
  "s_add_u32 s50, %38, s60\n" \
  "s_xor_b32 s66, %38, s60\n" \
  "s_xor_b32 s59, %44, s60\n" \
  "s_or_b32 s50, s50, 0x40000000\n" \
  "s_mov_b32 s57, 2130706432\n" \
  "s_mov_b32 s48, 0\n" \
  "v_mov_b32 v169, s55\n" \
  "v_mov_b32 v167, 2130706432\n" \
  "v_mov_b32 v161, 2130706432\n" \
  "v_mov_b32 v166, 0\n" \
  "s_mov_b64 exec, 7\n" \
  "v_mov_b32 v167, %46\n" \
  "s_mov_b64 exec, 3\n" \
  "v_mov_b32 v167, s54\n" \
  "s_mov_b64 exec, 1\n" \
  "v_mov_b32 v167, s53\n" \
  "v_mov_b32 v161, s53\n" \
  "v_mov_b32 v166, 0\n" \
  "s_mov_b64 exec, -1\n" \
  "s_barrier\n" \
  "ds_read_b128 v[32:35], v191\n" \
  "ds_read_b128 v[36:39], v191 offset:18432\n" \
  "ds_read_b128 v[40:43], v192\n" \
  "ds_read_b128 v[44:47], v192 offset:18432\n" \
  "ds_read_b128 v[48:51], v191 offset:128\n" \
  "ds_read_b128 v[52:55], v191 offset:18560\n" \
  "s_waitcnt lgkmcnt(0)\n" \
  "v_mov_b64 v[164:165], 0\n" \
  ".LU_loop_%=:\n" \
  "v_mfma_f32_16x16x32_bf16 v[64:67], v[32:35], a[0:3], 0\n" \
  "ds_read_b128 v[56:59], v192 offset:128\n" \
  "v_mfma_f32_16x16x32_bf16 v[72:75], v[36:39], a[0:3], 0\n" \
  "ds_read_b128 v[60:63], v192 offset:18560\n" \
  "v_mfma_f32_16x16x32_bf16 v[68:71], v[32:35], a[72:75], 0\n" \
  "s_mov_b32 s63, s57\n" \
  "v_cvt_pk_bf16_f32 v104, v88, v89\n" \
  "v_cvt_pk_bf16_f32 v105, v90, v91\n" \
  "v_mfma_f32_16x16x32_bf16 v[76:79], v[36:39], a[72:75], 0\n" \
  "v_cvt_pk_bf16_f32 v106, v92, v93\n" \
  "v_cvt_pk_bf16_f32 v107, v94, v95\n" \
  "v_mfma_f32_16x16x32_bf16 v[64:67], v[40:43], a[4:7], v[64:67]\n" \
  "ds_read_b128 v[32:35], v191 offset:256\n" \
  "v_mfma_f32_16x16x32_bf16 v[72:75], v[44:47], a[4:7], v[72:75]\n" \
  "ds_read_b128 v[36:39], v191 offset:18688\n" \
  "v_mfma_f32_16x16x32_bf16 v[68:71], v[40:43], a[76:79], v[68:71]\n" \
  "buffer_store_dwordx4 v[104:107], v190, s[40:43], s63 offen sc1\n" \
  "v_mfma_f32_16x16x32_bf16 v[76:79], v[44:47], a[76:79], v[76:79]\n" \
  "v_mfma_f32_16x16x32_bf16 v[64:67], v[48:51], a[8:11], v[64:67]\n" \
  "ds_read_b128 v[40:43], v192 offset:256\n" \
  "v_mfma_f32_16x16x32_bf16 v[72:75], v[52:55], a[8:11], v[72:75]\n" \
  "ds_read_b128 v[44:47], v192 offset:18688\n" \
  "v_mfma_f32_16x16x32_bf16 v[68:71], v[48:51], a[80:83], v[68:71]\n" \
  "s_add_u32 m0, s52, 0\n" \
  "s_waitcnt lgkmcnt(5)\n" \
  "v_mfma_f32_16x16x32_bf16 v[76:79], v[52:55], a[80:83], v[76:79]\n" \
  "buffer_load_dwordx4 v196, s[44:47], s51 offen lds\n" \
  "v_mfma_f32_16x16x32_bf16 v[64:67], v[56:59], a[12:15], v[64:67]\n" \
  "ds_read_b128 v[48:51], v191 offset:384\n" \
  "s_mov_b32 s58, s75\n" \
  "s_waitcnt lgkmcnt(5)\n" \
  "v_mfma_f32_16x16x32_bf16 v[72:75], v[60:63], a[12:15], v[72:75]\n" \
  "ds_read_b128 v[52:55], v191 offset:18816\n" \
  "s_and_b32 s58, s58, 15\n" \
  "v_mfma_f32_16x16x32_bf16 v[68:71], v[56:59], a[84:87], v[68:71]\n" \
  "s_lshl_b32 s58, s58, 16\n" \
  "s_xor_b32 s50, s58, s66\n" \
  "s_xor_b32 s57, s58, s59\n" \
  "v_mfma_f32_16x16x32_bf16 v[76:79], v[60:63], a[84:87], v[76:79]\n" \
  "s_cmp_lt_u32 s73, 28\n" \
  "v_mul_i32_i24 v161, s70, v166\n" \
  "s_cselect_b32 s70, 2, 1\n" \
  "s_waitcnt lgkmcnt(5)\n" \
  "v_mfma_f32_16x16x32_bf16 v[64:67], v[32:35], a[16:19], v[64:67]\n" \
  "ds_read_b128 v[56:59], v192 offset:384\n" \
  "s_lshl_b32 s71, s70, 19\n" \
  "s_waitcnt lgkmcnt(5)\n" \
  "v_mfma_f32_16x16x32_bf16 v[72:75], v[36:39], a[16:19], v[72:75]\n" \
  "ds_read_b128 v[60:63], v192 offset:18816\n" \
  "s_mov_b32 s75, s73\n" \
  "v_mfma_f32_16x16x32_bf16 v[68:71], v[32:35], a[88:91], v[68:71]\n" \
  "s_add_u32 m0, s52, 1024\n" \
  "s_waitcnt lgkmcnt(5)\n" \
  "v_mfma_f32_16x16x32_bf16 v[76:79], v[36:39], a[88:91], v[76:79]\n" \
  "buffer_load_dwordx4 v197, s[44:47], s51 offen lds\n" \
  "v_mfma_f32_16x16x32_bf16 v[64:67], v[40:43], a[20:23], v[64:67]\n" \
  "ds_read_b128 v[32:35], v191 offset:512\n" \
  "s_waitcnt lgkmcnt(5)\n" \
  "v_mfma_f32_16x16x32_bf16 v[72:75], v[44:47], a[20:23], v[72:75]\n" \
  "ds_read_b128 v[36:39], v191 offset:18944\n" \
  "v_mfma_f32_16x16x32_bf16 v[68:71], v[40:43], a[92:95], v[68:71]\n" \
  "v_mfma_f32_16x16x32_bf16 v[76:79], v[44:47], a[92:95], v[76:79]\n" \
  "s_waitcnt lgkmcnt(5)\n" \
  "v_mfma_f32_16x16x32_bf16 v[64:67], v[48:51], a[24:27], v[64:67]\n" \
  "ds_read_b128 v[40:43], v192 offset:512\n" \
  "s_waitcnt lgkmcnt(5)\n" \
  "v_xor_b32 v160, 8, v194\n" \
  "v_mfma_f32_16x16x32_bf16 v[72:75], v[52:55], a[24:27], v[72:75]\n" \
  "ds_read_b128 v[44:47], v192 offset:18944\n" \
  "v_mfma_f32_16x16x32_bf16 v[68:71], v[48:51], a[96:99], v[68:71]\n" \
  "s_add_u32 m0, s52, 2048\n" \
  "v_add_u32 v168, s73, v161\n" \
  "v_mfma_f32_16x16x32_bf16 v[76:79], v[52:55], a[96:99], v[76:79]\n" \
  "s_waitcnt lgkmcnt(5)\n" \
  "buffer_load_dwordx4 v198, s[44:47], s51 offen lds\n" \
  "v_mfma_f32_16x16x32_bf16 v[64:67], v[56:59], a[28:31], v[64:67]\n" \
  "ds_read_b128 v[48:51], v191 offset:640\n" \
  "s_waitcnt lgkmcnt(5)\n" \
  "v_mfma_f32_16x16x32_bf16 v[72:75], v[60:63], a[28:31], v[72:75]\n" \
  "ds_read_b128 v[52:55], v191 offset:19072\n" \
  "v_mfma_f32_16x16x32_bf16 v[68:71], v[56:59], a[100:103], v[68:71]\n" \
  "s_waitcnt vmcnt(14)\n" \
  "s_barrier\n" \
  "v_mfma_f32_16x16x32_bf16 v[76:79], v[60:63], a[100:103], v[76:79]\n" \
  "ds_read_b64_tr_b16 v[104:105], v194\n" \
  "s_waitcnt lgkmcnt(6)\n" \
  "v_mfma_f32_16x16x32_bf16 v[64:67], v[32:35], a[32:35], v[64:67]\n" \
  "ds_read_b128 v[56:59], v192 offset:640\n" \
  "s_waitcnt lgkmcnt(6)\n" \
  "v_mfma_f32_16x16x32_bf16 v[72:75], v[36:39], a[32:35], v[72:75]\n" \
  "ds_read_b128 v[60:63], v192 offset:19072\n" \
  "v_mfma_f32_16x16x32_bf16 v[68:71], v[32:35], a[104:107], v[68:71]\n" \
  "s_add_u32 m0, s52, 3072\n" \
  "ds_read_b64_tr_b16 v[106:107], v194 offset:256\n" \
  "s_waitcnt lgkmcnt(7)\n" \
  "v_mfma_f32_16x16x32_bf16 v[76:79], v[36:39], a[104:107], v[76:79]\n" \
  "buffer_load_dwordx4 v199, s[44:47], s51 offen lds\n" \
  "v_mfma_f32_16x16x32_bf16 v[64:67], v[40:43], a[36:39], v[64:67]\n" \
  "ds_read_b128 v[32:35], v191 offset:768\n" \
  "s_waitcnt lgkmcnt(7)\n" \
  "v_mfma_f32_16x16x32_bf16 v[72:75], v[44:47], a[36:39], v[72:75]\n" \
  "ds_read_b128 v[36:39], v191 offset:19200\n" \
  "v_mfma_f32_16x16x32_bf16 v[68:71], v[40:43], a[108:111], v[68:71]\n" \
  "s_cmp_le_u32 s48, 7\n" \
  "s_cbranch_scc1 .LU_e2_%=\n" \
  ".LU_e2_b_%=:\n" \
  "v_mfma_f32_16x16x32_bf16 v[76:79], v[44:47], a[108:111], v[76:79]\n" \
  "ds_read_b64_tr_b16 v[108:109], v160\n" \
  "s_waitcnt lgkmcnt(8)\n" \
  "v_mfma_f32_16x16x32_bf16 v[64:67], v[48:51], a[40:43], v[64:67]\n" \
  "ds_read_b128 v[40:43], v192 offset:768\n" \
  "s_waitcnt lgkmcnt(8)\n" \
  "v_mfma_f32_16x16x32_bf16 v[72:75], v[52:55], a[40:43], v[72:75]\n" \
  "ds_read_b128 v[44:47], v192 offset:19200\n" \
  "v_mfma_f32_16x16x32_bf16 v[68:71], v[48:51], a[112:115], v[68:71]\n" \
  "s_add_u32 m0, s52, 4096\n" \
  "ds_read_b64_tr_b16 v[110:111], v160 offset:256\n" \
  "s_waitcnt lgkmcnt(8)\n" \
  "v_mfma_f32_16x16x32_bf16 v[76:79], v[52:55], a[112:115], v[76:79]\n" \
  "buffer_load_dwordx4 v200, s[44:47], s51 offen lds\n" \
  "v_mfma_f32_16x16x32_bf16 v[64:67], v[56:59], a[44:47], v[64:67]\n" \
  "ds_read_b128 v[48:51], v191 offset:896\n" \
  "s_waitcnt lgkmcnt(8)\n" \
  "v_mfma_f32_16x16x32_bf16 v[72:75], v[60:63], a[44:47], v[72:75]\n" \
  "ds_read_b128 v[52:55], v191 offset:19328\n" \
  "v_mfma_f32_16x16x32_bf16 v[68:71], v[56:59], a[116:119], v[68:71]\n" \
  "ds_read_b64_tr_b16 v[112:113], v194 offset:2048\n" \
  "v_mfma_f32_16x16x32_bf16 v[76:79], v[60:63], a[116:119], v[76:79]\n" \
  "ds_read_b64_tr_b16 v[114:115], v194 offset:2304\n" \
  "s_waitcnt lgkmcnt(9)\n" \
  "v_mfma_f32_16x16x32_bf16 v[64:67], v[32:35], a[48:51], v[64:67]\n" \
  "ds_read_b128 v[56:59], v192 offset:896\n" \
  "s_waitcnt lgkmcnt(9)\n" \
  "v_mfma_f32_16x16x32_bf16 v[72:75], v[36:39], a[48:51], v[72:75]\n" \
  "ds_read_b128 v[60:63], v192 offset:19328\n" \
  "v_mfma_f32_16x16x32_bf16 v[68:71], v[32:35], a[120:123], v[68:71]\n" \
  "s_add_u32 m0, s52, 5120\n" \
  "ds_read_b64_tr_b16 v[116:117], v160 offset:2048\n" \
  "s_waitcnt lgkmcnt(9)\n" \
  "v_mfma_f32_16x16x32_bf16 v[76:79], v[36:39], a[120:123], v[76:79]\n" \
  "buffer_load_dwordx4 v201, s[44:47], s51 offen lds\n" \
  "v_mfma_f32_16x16x32_bf16 v[64:67], v[40:43], a[52:55], v[64:67]\n" \
  "ds_read_b128 v[32:35], v191 offset:1024\n" \
  "s_waitcnt lgkmcnt(9)\n" \
  "v_mfma_f32_16x16x32_bf16 v[72:75], v[44:47], a[52:55], v[72:75]\n" \
  "ds_read_b128 v[36:39], v191 offset:19456\n" \
  "v_mfma_f32_16x16x32_bf16 v[68:71], v[40:43], a[124:127], v[68:71]\n" \
  "ds_read_b64_tr_b16 v[118:119], v160 offset:2304\n" \
  "v_mfma_f32_16x16x32_bf16 v[76:79], v[44:47], a[124:127], v[76:79]\n" \
  "ds_read_b64_tr_b16 v[120:121], v194 offset:4096\n" \
  "s_waitcnt lgkmcnt(10)\n" \
  "v_mfma_f32_16x16x32_bf16 v[64:67], v[48:51], a[56:59], v[64:67]\n" \
  "ds_read_b128 v[40:43], v192 offset:1024\n" \
  "s_waitcnt lgkmcnt(10)\n" \
  "v_mfma_f32_16x16x32_bf16 v[72:75], v[52:55], a[56:59], v[72:75]\n" \
  "ds_read_b128 v[44:47], v192 offset:19456\n" \
  "v_mfma_f32_16x16x32_bf16 v[68:71], v[48:51], a[128:131], v[68:71]\n" \
  "s_add_u32 m0, s52, 6144\n" \
  "ds_read_b64_tr_b16 v[122:123], v194 offset:4352\n" \
  "s_waitcnt lgkmcnt(9)\n" \
  "v_mfma_f32_16x16x32_bf16 v[76:79], v[52:55], a[128:131], v[76:79]\n" \
  "buffer_load_dwordx4 v202, s[44:47], s51 offen lds\n" \
  "v_mfma_f32_16x16x32_bf16 v[64:67], v[56:59], a[60:63], v[64:67]\n" \
  "ds_read_b64_tr_b16 v[124:125], v160 offset:4096\n" \
  "s_waitcnt lgkmcnt(9)\n" \
  "v_mfma_f32_16x16x32_bf16 v[72:75], v[60:63], a[60:63], v[72:75]\n" \
  "ds_read_b64_tr_b16 v[126:127], v160 offset:4352\n" \
  "v_mfma_f32_16x16x32_bf16 v[68:71], v[56:59], a[132:135], v[68:71]\n" \
  "s_add_u32 m0, s52, 7168\n" \
  "ds_read_b64_tr_b16 v[128:129], v194 offset:6144\n" \
  "s_waitcnt lgkmcnt(9)\n" \
  "v_mfma_f32_16x16x32_bf16 v[76:79], v[60:63], a[132:135], v[76:79]\n" \
  "buffer_load_dwordx4 v203, s[44:47], s51 offen lds\n" \
  "v_mfma_f32_16x16x32_bf16 v[64:67], v[32:35], a[64:67], v[64:67]\n" \
  "ds_read_b64_tr_b16 v[130:131], v194 offset:6400\n" \
  "s_waitcnt lgkmcnt(9)\n" \
  "v_mfma_f32_16x16x32_bf16 v[72:75], v[36:39], a[64:67], v[72:75]\n" \
  "ds_read_b64_tr_b16 v[132:133], v160 offset:6144\n" \
  "v_mfma_f32_16x16x32_bf16 v[68:71], v[32:35], a[136:139], v[68:71]\n" \
  "ds_read_b64_tr_b16 v[134:135], v160 offset:6400\n" \
  "v_mfma_f32_16x16x32_bf16 v[76:79], v[36:39], a[136:139], v[76:79]\n" \
  "s_waitcnt lgkmcnt(8)\n" \
  "v_mfma_f32_16x16x32_bf16 v[64:67], v[40:43], a[68:71], v[64:67]\n" \
  "s_waitcnt lgkmcnt(7)\n" \
  "v_mfma_f32_16x16x32_bf16 v[72:75], v[44:47], a[68:71], v[72:75]\n" \
  "v_mfma_f32_16x16x32_bf16 v[68:71], v[40:43], a[140:143], v[68:71]\n" \
  "s_add_u32 m0, s52, 8192\n" \
  "v_mfma_f32_16x16x32_bf16 v[76:79], v[44:47], a[140:143], v[76:79]\n" \
  "buffer_load_dwordx4 v204, s[44:47], s51 offen lds\n" \
  "s_cmp_le_u32 s48, 5\n" \
  "s_cbranch_scc1 .LU_first_%=\n" \
  ".LU_phb_%=:\n" \
  "v_mfma_f32_16x16x32_bf16 v[88:91], v[104:107], a[144:147], 0\n" \
  "v_mfma_f32_16x16x32_bf16 v[92:95], v[108:111], a[144:147], 0\n" \
  "s_lshr_b32 s62, s49, 8\n" \
  "s_and_b32 s62, s62, 8192\n" \
  "s_xor_b32 s62, s62, s49\n" \
  "v_mfma_f32_16x16x32_bf16 v[88:91], v[112:115], a[148:151], v[88:91]\n" \
  "v_cvt_pk_bf16_f32 v80, v64, v65\n" \
  "v_cvt_pk_bf16_f32 v81, v66, v67\n" \
  "v_mfma_f32_16x16x32_bf16 v[92:95], v[116:119], a[148:151], v[92:95]\n" \
  "s_waitcnt lgkmcnt(6)\n" \
  "v_cvt_pk_bf16_f32 v82, v72, v73\n" \
  "v_cvt_pk_bf16_f32 v83, v74, v75\n" \
  "v_mfma_f32_16x16x32_bf16 v[88:91], v[120:123], a[152:155], v[88:91]\n" \
  "s_waitcnt lgkmcnt(4)\n" \
  "buffer_store_dwordx4 v[80:83], v190, s[40:43], s62 offen sc1\n" \
  "v_mfma_f32_16x16x32_bf16 v[92:95], v[124:127], a[152:155], v[92:95]\n" \
  "ds_write_b128 v193, v[80:83]\n" \
  "s_waitcnt lgkmcnt(3)\n" \
  "v_mfma_f32_16x16x32_bf16 v[88:91], v[128:131], a[156:159], v[88:91]\n" \
  "s_waitcnt lgkmcnt(1)\n" \
  "v_cvt_pk_bf16_f32 v84, v76, v77\n" \
  "v_cvt_pk_bf16_f32 v85, v78, v79\n" \
  "v_mfma_f32_16x16x32_bf16 v[92:95], v[132:135], a[156:159], v[92:95]\n" \
  "v_cvt_pk_bf16_f32 v86, v68, v69\n" \
  "v_cvt_pk_bf16_f32 v87, v70, v71\n" \
  "v_mfma_f32_16x16x32_bf16 v[96:99], v[104:107], a[160:163], 0\n" \
  "buffer_store_dwordx4 v[84:87], v190, s[40:43], s62 offen offset:1024 sc1\n" \
  "v_mfma_f32_16x16x32_bf16 v[100:103], v[108:111], a[160:163], 0\n" \
  "ds_write_b128 v193, v[84:87] offset:1024\n" \
  "v_mfma_f32_16x16x32_bf16 v[96:99], v[112:115], a[164:167], v[96:99]\n" \
  "s_xor_b32 s63, s50, 0\n" \
  "v_cvt_pk_bf16_f32 v136, v88, v89\n" \
  "v_cvt_pk_bf16_f32 v137, v90, v91\n" \
  "v_mfma_f32_16x16x32_bf16 v[100:103], v[116:119], a[164:167], v[100:103]\n" \
  "s_add_u32 s63, s63, 0\n" \
  "v_cvt_pk_bf16_f32 v138, v92, v93\n" \
  "v_cvt_pk_bf16_f32 v139, v94, v95\n" \
  "v_mfma_f32_16x16x32_bf16 v[96:99], v[120:123], a[168:171], v[96:99]\n" \
  "buffer_store_dwordx4 v[136:139], v190, s[40:43], s63 offen sc1\n" \
  "v_mfma_f32_16x16x32_bf16 v[100:103], v[124:127], a[168:171], v[100:103]\n" \
  "s_add_u32 s58, s48, 1\n" \
  "s_and_b32 s58, s58, 3\n" \
  "s_cselect_b32 s58, 0, 147456\n" \
  "v_mfma_f32_16x16x32_bf16 v[96:99], v[128:131], a[172:175], v[96:99]\n" \
  "s_sub_u32 s58, 36864, s58\n" \
  "v_add_u32 v163, s58, v191\n" \
  "v_add_u32 v162, s58, v192\n" \
  "v_mfma_f32_16x16x32_bf16 v[100:103], v[132:135], a[172:175], v[100:103]\n" \
  "s_cmp_lt_u32 s51, 1032192\n" \
  "s_cselect_b32 s60, 36864, 0\n" \
  "s_add_u32 s51, s51, s60\n" \
  "v_mfma_f32_16x16x32_bf16 v[88:91], v[104:107], a[176:179], 0\n" \
  "s_add_u32 s51, s51, 36864\n" \
  "s_min_u32 s51, s51, 2322432\n" \
  "s_add_u32 s52, s52, 36864\n" \
  "v_mfma_f32_16x16x32_bf16 v[92:95], v[108:111], a[176:179], 0\n" \
  "s_cmp_ge_u32 s52, 163840\n" \
  "s_cselect_b32 s60, 147456, 0\n" \
  "s_sub_u32 s52, s52, s60\n" \
  "v_mfma_f32_16x16x32_bf16 v[88:91], v[112:115], a[180:183], v[88:91]\n" \
  "s_xor_b32 s63, s50, 1024\n" \
  "v_cvt_pk_bf16_f32 v136, v96, v97\n" \
  "v_cvt_pk_bf16_f32 v137, v98, v99\n" \
  "v_mfma_f32_16x16x32_bf16 v[92:95], v[116:119], a[180:183], v[92:95]\n" \
  "s_add_u32 s63, s63, 1048576\n" \
  "v_cvt_pk_bf16_f32 v138, v100, v101\n" \
  "v_cvt_pk_bf16_f32 v139, v102, v103\n" \
  "v_mfma_f32_16x16x32_bf16 v[88:91], v[120:123], a[184:187], v[88:91]\n" \
  "buffer_store_dwordx4 v[136:139], v190, s[40:43], s63 offen sc1\n" \
  "v_mfma_f32_16x16x32_bf16 v[92:95], v[124:127], a[184:187], v[92:95]\n" \
  "v_mfma_f32_16x16x32_bf16 v[88:91], v[128:131], a[188:191], v[88:91]\n" \
  "s_nop 2\n" \
  "v_mfma_f32_16x16x32_bf16 v[92:95], v[132:135], a[188:191], v[92:95]\n" \
  "v_mfma_f32_16x16x32_bf16 v[96:99], v[104:107], a[192:195], 0\n" \
  "v_mfma_f32_16x16x32_bf16 v[100:103], v[108:111], a[192:195], 0\n" \
  "v_mfma_f32_16x16x32_bf16 v[96:99], v[112:115], a[196:199], v[96:99]\n" \
  "s_xor_b32 s63, s50, 278528\n" \
  "v_cvt_pk_bf16_f32 v136, v88, v89\n" \
  "v_cvt_pk_bf16_f32 v137, v90, v91\n" \
  "v_mfma_f32_16x16x32_bf16 v[100:103], v[116:119], a[196:199], v[100:103]\n" \
  "s_add_u32 s63, s63, 8388608\n" \
  "v_cvt_pk_bf16_f32 v138, v92, v93\n" \
  "v_cvt_pk_bf16_f32 v139, v94, v95\n" \
  "v_mfma_f32_16x16x32_bf16 v[96:99], v[120:123], a[200:203], v[96:99]\n" \
  "buffer_store_dwordx4 v[136:139], v190, s[40:43], s63 offen sc1\n" \
  "v_mfma_f32_16x16x32_bf16 v[100:103], v[124:127], a[200:203], v[100:103]\n" \
  "v_mfma_f32_16x16x32_bf16 v[96:99], v[128:131], a[204:207], v[96:99]\n" \
  "v_readfirstlane_b32 s58, v164\n" \
  "v_readfirstlane_b32 s61, v165\n" \
  "v_mfma_f32_16x16x32_bf16 v[100:103], v[132:135], a[204:207], v[100:103]\n" \
  "s_cmp_eq_u32 s61, s55\n" \
  "s_cselect_b32 s58, s58, 0\n" \
  "s_add_u32 s58, s58, s68\n" \
  "v_mfma_f32_16x16x32_bf16 v[88:91], v[104:107], a[208:211], 0\n" \
  "s_cmp_lt_u32 s58, s73\n" \
  "s_cbranch_scc1 .LU_fc_%=\n" \
  ".LU_fc_b_%=:\n" \
  "v_mfma_f32_16x16x32_bf16 v[92:95], v[108:111], a[208:211], 0\n" \
  "buffer_load_dwordx2 v[164:165], off, s[40:43], %43 sc1\n" \
  "v_mfma_f32_16x16x32_bf16 v[88:91], v[112:115], a[212:215], v[88:91]\n" \
  "s_xor_b32 s63, s50, 279552\n" \
  "v_cvt_pk_bf16_f32 v136, v96, v97\n" \
  "v_cvt_pk_bf16_f32 v137, v98, v99\n" \
  "v_mfma_f32_16x16x32_bf16 v[92:95], v[116:119], a[212:215], v[92:95]\n" \
  "s_add_u32 s63, s63, 9437184\n" \
  "v_cvt_pk_bf16_f32 v138, v100, v101\n" \
  "v_cvt_pk_bf16_f32 v139, v102, v103\n" \
  "v_mfma_f32_16x16x32_bf16 v[88:91], v[120:123], a[216:219], v[88:91]\n" \
  "buffer_store_dwordx4 v[136:139], v190, s[40:43], s63 offen sc1\n" \
  "v_mfma_f32_16x16x32_bf16 v[92:95], v[124:127], a[216:219], v[92:95]\n" \
  "v_mfma_f32_16x16x32_bf16 v[88:91], v[128:131], a[220:223], v[88:91]\n" \
  "s_nop 0\n" \
  "s_waitcnt vmcnt(16)\n" \
  "buffer_store_dwordx2 v[168:169], v167, s[40:43], 0 offen sc1\n" \
  "v_mfma_f32_16x16x32_bf16 v[92:95], v[132:135], a[220:223], v[92:95]\n" \
  "v_mfma_f32_16x16x32_bf16 v[96:99], v[104:107], a[224:227], 0\n" \
  "v_mfma_f32_16x16x32_bf16 v[100:103], v[108:111], a[224:227], 0\n" \
  "v_mfma_f32_16x16x32_bf16 v[96:99], v[112:115], a[228:231], v[96:99]\n" \
  "s_xor_b32 s63, s50, 557056\n" \
  "v_cvt_pk_bf16_f32 v136, v88, v89\n" \
  "v_cvt_pk_bf16_f32 v137, v90, v91\n" \
  "v_mfma_f32_16x16x32_bf16 v[100:103], v[116:119], a[228:231], v[100:103]\n" \
  "s_add_u32 s63, s63, 16777216\n" \
  "v_cvt_pk_bf16_f32 v138, v92, v93\n" \
  "v_cvt_pk_bf16_f32 v139, v94, v95\n" \
  "v_mfma_f32_16x16x32_bf16 v[96:99], v[120:123], a[232:235], v[96:99]\n" \
  "buffer_store_dwordx4 v[136:139], v190, s[40:43], s63 offen sc1\n" \
  "v_mfma_f32_16x16x32_bf16 v[100:103], v[124:127], a[232:235], v[100:103]\n" \
  "v_mfma_f32_16x16x32_bf16 v[96:99], v[128:131], a[236:239], v[96:99]\n" \
  "ds_read_b128 v[32:35], v163\n" \
  "v_mfma_f32_16x16x32_bf16 v[100:103], v[132:135], a[236:239], v[100:103]\n" \
  "ds_read_b128 v[36:39], v163 offset:18432\n" \
  "v_mfma_f32_16x16x32_bf16 v[88:91], v[104:107], a[240:243], 0\n" \
  "ds_read_b128 v[40:43], v162\n" \
  "v_mfma_f32_16x16x32_bf16 v[92:95], v[108:111], a[240:243], 0\n" \
  "ds_read_b128 v[44:47], v162 offset:18432\n" \
  "v_mfma_f32_16x16x32_bf16 v[88:91], v[112:115], a[244:247], v[88:91]\n" \
  "s_xor_b32 s63, s50, 558080\n" \
  "v_cvt_pk_bf16_f32 v136, v96, v97\n" \
  "v_cvt_pk_bf16_f32 v137, v98, v99\n" \
  "v_mfma_f32_16x16x32_bf16 v[92:95], v[116:119], a[244:247], v[92:95]\n" \
  "s_add_u32 s63, s63, 17825792\n" \
  "v_cvt_pk_bf16_f32 v138, v100, v101\n" \
  "v_cvt_pk_bf16_f32 v139, v102, v103\n" \
  "v_mfma_f32_16x16x32_bf16 v[88:91], v[120:123], a[248:251], v[88:91]\n" \
  "buffer_store_dwordx4 v[136:139], v190, s[40:43], s63 offen sc1\n" \
  "v_mfma_f32_16x16x32_bf16 v[92:95], v[124:127], a[248:251], v[92:95]\n" \
  "v_mfma_f32_16x16x32_bf16 v[88:91], v[128:131], a[252:255], v[88:91]\n" \
  "ds_read_b128 v[48:51], v163 offset:128\n" \
  "v_mfma_f32_16x16x32_bf16 v[92:95], v[132:135], a[252:255], v[92:95]\n" \
  "ds_read_b128 v[52:55], v163 offset:18560\n" \
  "v_mfma_f32_16x16x32_bf16 v[96:99], v[104:107], v[0:3], 0\n" \
  "v_mov_b32 v191, v163\n" \
  "v_mov_b32 v192, v162\n" \
  "v_mfma_f32_16x16x32_bf16 v[100:103], v[108:111], v[0:3], 0\n" \
  "s_add_u32 s49, s49, s71\n" \
  "v_xor_b32 v193, 8192, v193\n" \
  "v_xor_b32 v194, 8192, v194\n" \
  "v_mfma_f32_16x16x32_bf16 v[96:99], v[112:115], v[4:7], v[96:99]\n" \
  "s_xor_b32 s63, s50, 835584\n" \
  "v_cvt_pk_bf16_f32 v136, v88, v89\n" \
  "v_cvt_pk_bf16_f32 v137, v90, v91\n" \
  "v_mfma_f32_16x16x32_bf16 v[100:103], v[116:119], v[4:7], v[100:103]\n" \
  "s_add_u32 s63, s63, 25165824\n" \
  "v_cvt_pk_bf16_f32 v138, v92, v93\n" \
  "v_cvt_pk_bf16_f32 v139, v94, v95\n" \
  "v_mfma_f32_16x16x32_bf16 v[96:99], v[120:123], v[8:11], v[96:99]\n" \
  "buffer_store_dwordx4 v[136:139], v190, s[40:43], s63 offen sc1\n" \
  "v_mfma_f32_16x16x32_bf16 v[100:103], v[124:127], v[8:11], v[100:103]\n" \
  "s_add_u32 s48, s48, 1\n" \
  "s_add_u32 s73, s73, s70\n" \
  "v_mfma_f32_16x16x32_bf16 v[96:99], v[128:131], v[12:15], v[96:99]\n" \
  "s_cmp_eq_u32 s73, s74\n" \
  "s_cselect_b32 s49, 2130706432, s49\n" \
  "v_mfma_f32_16x16x32_bf16 v[100:103], v[132:135], v[12:15], v[100:103]\n" \
  "s_cmp_le_u32 s48, 5\n" \
  "s_addc_u32 s67, s73, 0\n" \
  "v_mfma_f32_16x16x32_bf16 v[88:91], v[104:107], v[16:19], 0\n" \
  "s_cmp_gt_u32 s67, s74\n" \
  "s_cselect_b32 s67, 2, 0\n" \
  "v_mfma_f32_16x16x32_bf16 v[92:95], v[108:111], v[16:19], 0\n" \
  "s_cmp_le_u32 s48, 1\n" \
  "s_addc_u32 s67, s67, 0\n" \
  "v_mfma_f32_16x16x32_bf16 v[88:91], v[112:115], v[20:23], v[88:91]\n" \
  "s_xor_b32 s63, s50, 836608\n" \
  "v_cvt_pk_bf16_f32 v104, v96, v97\n" \
  "v_cvt_pk_bf16_f32 v105, v98, v99\n" \
  "v_mfma_f32_16x16x32_bf16 v[92:95], v[116:119], v[20:23], v[92:95]\n" \
  "s_add_u32 s63, s63, 26214400\n" \
  "v_cvt_pk_bf16_f32 v106, v100, v101\n" \
  "v_cvt_pk_bf16_f32 v107, v102, v103\n" \
  "v_mfma_f32_16x16x32_bf16 v[88:91], v[120:123], v[24:27], v[88:91]\n" \
  "buffer_store_dwordx4 v[104:107], v190, s[40:43], s63 offen sc1\n" \
  "v_mfma_f32_16x16x32_bf16 v[92:95], v[124:127], v[24:27], v[92:95]\n" \
  "v_mfma_f32_16x16x32_bf16 v[88:91], v[128:131], v[28:31], v[88:91]\n" \
  "v_mfma_f32_16x16x32_bf16 v[92:95], v[132:135], v[28:31], v[92:95]\n" \
  "s_cmp_eq_u32 s67, 0\n" \
  "s_waitcnt lgkmcnt(0)\n" \
  ".LU_first_b_%=:\n" \
  "s_cbranch_scc1 .LU_loop_%=\n" \
  "s_cmp_eq_u32 s67, 1\n" \
  "s_cbranch_scc1 .LU_now_%=\n" \
  "s_waitcnt lgkmcnt(0)\n" \
  "s_nop 7\n" \
  "s_mov_b32 s63, s57\n" \
  "v_cvt_pk_bf16_f32 v104, v88, v89\n" \
  "v_cvt_pk_bf16_f32 v105, v90, v91\n" \
  "v_cvt_pk_bf16_f32 v106, v92, v93\n" \
  "v_cvt_pk_bf16_f32 v107, v94, v95\n" \
  "buffer_store_dwordx4 v[104:107], v190, s[40:43], s63 offen sc1\n" \
  "s_waitcnt vmcnt(0)\n" \
  "s_barrier\n" \
  "s_mov_b32 s48, 64\n" \
  "s_mov_b32 s69, 0\n" \
  ".LU_ie_%=:\n" \
  "s_cmp_ge_u32 s96, 0xc0000000\n" \
  "s_cbranch_scc1 .LU_ie_x_%=\n" \
  "buffer_load_dwordx2 v[184:185], off, s[40:43], %41 sc1\n" \
  "s_waitcnt vmcnt(0)\n" \
  "s_add_u32 s69, s69, 1\n" \
  "s_cmp_lt_u32 s69, 2097152\n" \
  "v_readfirstlane_b32 s96, v184\n" \
  "s_cbranch_scc1 .LU_ie_%=\n" \
  ".LU_ie_x_%=:\n" \
  "v_mov_b32 v168, s48\n" \
  "buffer_store_dwordx2 v[168:169], v167, s[40:43], 0 offen sc1\n" \
  "s_branch .LU_end_%=\n" \
  ".LU_e2_%=:\n" \
  "s_waitcnt vmcnt(4)\n" \
  "v_add_u32 v168, s73, v161\n" \
  "s_mov_b64 exec, 2\n" \
  "buffer_store_dwordx2 v[168:169], v167, s[40:43], 0 offen sc1\n" \
  "s_mov_b64 exec, -1\n" \
  "s_cmp_lg_u32 s48, 7\n" \
  "v_readfirstlane_b32 s96, v184\n" \
  "s_cbranch_scc1 .LU_in_x_%=\n" \
  "s_mov_b32 s69, 0\n" \
  ".LU_in_%=:\n" \
  "s_cmp_ge_u32 s96, 0xc0000000\n" \
  "s_cbranch_scc1 .LU_in_x_%=\n" \
  "buffer_load_dwordx2 v[184:185], off, s[40:43], %41 sc1\n" \
  "s_waitcnt vmcnt(0)\n" \
  "s_add_u32 s69, s69, 1\n" \
  "s_cmp_lt_u32 s69, 2097152\n" \
  "v_readfirstlane_b32 s96, v184\n" \
  "s_cbranch_scc1 .LU_in_%=\n" \
  ".LU_in_x_%=:\n" \
  "buffer_load_dwordx2 v[184:185], off, s[40:43], %41 sc1\n" \
  "s_branch .LU_e2_b_%=\n" \
  ".LU_fc_%=:\n" \
  "s_mov_b32 s69, 0\n" \
  ".LU_fc1_%=:\n" \
  "s_sleep 8\n" \
  "buffer_load_dwordx2 v[164:165], off, s[40:43], %43 sc1\n" \
  "s_waitcnt vmcnt(0)\n" \
  "v_readfirstlane_b32 s58, v164\n" \
  "v_readfirstlane_b32 s61, v165\n" \
  "s_cmp_eq_u32 s61, s55\n" \
  "s_cselect_b32 s58, s58, 0\n" \
  "s_add_u32 s58, s58, s68\n" \
  "s_cmp_lt_u32 s58, s73\n" \
  "s_cbranch_scc0 .LU_fc_b_%=\n" \
  "s_add_u32 s69, s69, 1\n" \
  "s_cmp_lt_u32 s69, 524288\n" \
  "s_cbranch_scc1 .LU_fc1_%=\n" \
  "v_mov_b32 v164, 1\n" \
  "v_mov_b32 v165, s55\n" \
  "buffer_store_dwordx2 v[164:165], off, s[40:43], %42 offset:128 sc1\n" \
  "s_waitcnt vmcnt(0)\n" \
  "v_mov_b32 v164, 100\n" \
  "s_branch .LU_fc_b_%=\n" \
  ".LU_first_%=:\n" \
  "s_waitcnt lgkmcnt(0)\n" \
  "s_cmp_eq_u32 s73, 0\n" \
  "s_cselect_b32 s70, 2, s70\n" \
  "s_lshl_b32 s71, s70, 19\n" \
  "s_nop 4\n" \
  "s_lshr_b32 s62, s49, 8\n" \
  "s_and_b32 s62, s62, 8192\n" \
  "s_xor_b32 s62, s62, s49\n" \
  "v_cvt_pk_bf16_f32 v80, v64, v65\n" \
  "v_cvt_pk_bf16_f32 v81, v66, v67\n" \
  "v_cvt_pk_bf16_f32 v82, v72, v73\n" \
  "v_cvt_pk_bf16_f32 v83, v74, v75\n" \
  "buffer_store_dwordx4 v[80:83], v190, s[40:43], s62 offen sc1\n" \
  "ds_write_b128 v193, v[80:83]\n" \
  "v_cvt_pk_bf16_f32 v84, v76, v77\n" \
  "v_cvt_pk_bf16_f32 v85, v78, v79\n" \
  "v_cvt_pk_bf16_f32 v86, v68, v69\n" \
  "v_cvt_pk_bf16_f32 v87, v70, v71\n" \
  "buffer_store_dwordx4 v[84:87], v190, s[40:43], s62 offen offset:1024 sc1\n" \
  "ds_write_b128 v193, v[84:87] offset:1024\n" \
  "s_cmp_eq_u32 s48, 5\n" \
  "s_cbranch_scc1 .LU_skip_%=\n" \
  "v_xor_b32 v160, 8, v194\n" \
  "s_waitcnt lgkmcnt(0)\n" \
  "s_barrier\n" \
  "v_xor_b32 v194, 8192, v194\n" \
  "v_xor_b32 v160, 8192, v160\n" \
  "ds_read_b64_tr_b16 v[104:105], v194\n" \
  "ds_read_b64_tr_b16 v[106:107], v194 offset:256\n" \
  "ds_read_b64_tr_b16 v[108:109], v160\n" \
  "ds_read_b64_tr_b16 v[110:111], v160 offset:256\n" \
  "ds_read_b64_tr_b16 v[112:113], v194 offset:2048\n" \
  "ds_read_b64_tr_b16 v[114:115], v194 offset:2304\n" \
  "ds_read_b64_tr_b16 v[116:117], v160 offset:2048\n" \
  "ds_read_b64_tr_b16 v[118:119], v160 offset:2304\n" \
  "ds_read_b64_tr_b16 v[120:121], v194 offset:4096\n" \
  "ds_read_b64_tr_b16 v[122:123], v194 offset:4352\n" \
  "ds_read_b64_tr_b16 v[124:125], v160 offset:4096\n" \
  "ds_read_b64_tr_b16 v[126:127], v160 offset:4352\n" \
  "ds_read_b64_tr_b16 v[128:129], v194 offset:6144\n" \
  "ds_read_b64_tr_b16 v[130:131], v194 offset:6400\n" \
  "ds_read_b64_tr_b16 v[132:133], v160 offset:6144\n" \
  "ds_read_b64_tr_b16 v[134:135], v160 offset:6400\n" \
  "s_mov_b32 s58, s73\n" \
  "s_and_b32 s58, s58, 15\n" \
  "s_lshl_b32 s58, s58, 16\n" \
  "s_xor_b32 s50, s58, s66\n" \
  "s_xor_b32 s57, s58, s59\n" \
  "v_xor_b32 v194, 8192, v194\n" \
  "s_waitcnt lgkmcnt(0)\n" \
  "s_branch .LU_phb_%=\n" \
  ".LU_skip_%=:\n" \
  "s_mov_b64 exec, 6\n" \
  "v_mov_b32 v166, -1\n" \
  "s_mov_b64 exec, -1\n" \
  "s_add_u32 s58, s48, 1\n" \
  "s_and_b32 s58, s58, 3\n" \
  "s_cselect_b32 s58, 0, 147456\n" \
  "s_sub_u32 s58, 36864, s58\n" \
  "s_cmp_lt_u32 s51, 1032192\n" \
  "s_cselect_b32 s60, 36864, 0\n" \
  "s_add_u32 s51, s51, s60\n" \
  "s_add_u32 s51, s51, 36864\n" \
  "s_min_u32 s51, s51, 2322432\n" \
  "s_add_u32 s52, s52, 36864\n" \
  "s_cmp_ge_u32 s52, 163840\n" \
  "s_cselect_b32 s60, 147456, 0\n" \
  "s_sub_u32 s52, s52, s60\n" \
  "v_add_u32 v163, s58, v191\n" \
  "v_add_u32 v162, s58, v192\n" \
  "ds_read_b128 v[32:35], v163\n" \
  "ds_read_b128 v[36:39], v163 offset:18432\n" \
  "ds_read_b128 v[40:43], v162\n" \
  "ds_read_b128 v[44:47], v162 offset:18432\n" \
  "ds_read_b128 v[48:51], v163 offset:128\n" \
  "ds_read_b128 v[52:55], v163 offset:18560\n" \
  "s_add_u32 s49, s49, s71\n" \
  "s_mov_b32 s57, 2130706432\n" \
  "s_mov_b32 s69, 0\n" \
  "v_mov_b32 v191, v163\n" \
  "v_mov_b32 v192, v162\n" \
  "v_xor_b32 v193, 8192, v193\n" \
  "v_xor_b32 v194, 8192, v194\n" \
  ".LU_sk1_%=:\n" \
  "buffer_load_dwordx2 v[164:165], off, s[40:43], %43 sc1\n" \
  "s_waitcnt vmcnt(0)\n" \
  "v_readfirstlane_b32 s58, v164\n" \
  "v_readfirstlane_b32 s61, v165\n" \
  "s_cmp_eq_u32 s61, s55\n" \
  "s_cselect_b32 s58, s58, 0\n" \
  "s_add_u32 s58, s58, s68\n" \
  "s_cmp_lt_u32 s58, s73\n" \
  "s_cbranch_scc0 .LU_sk2_%=\n" \
  "v_mov_b32 v168, s73\n" \
  "s_mov_b64 exec, 2\n" \
  "buffer_store_dwordx2 v[168:169], v167, s[40:43], 0 offen sc1\n" \
  "s_mov_b64 exec, -1\n" \
  "s_sleep 16\n" \
  "s_add_u32 s69, s69, 1\n" \
  "s_cmp_lt_u32 s69, 524288\n" \
  "s_cbranch_scc1 .LU_sk1_%=\n" \
  "v_mov_b32 v164, 1\n" \
  "v_mov_b32 v165, s55\n" \
  "buffer_store_dwordx2 v[164:165], off, s[40:43], %42 offset:128 sc1\n" \
  "s_waitcnt vmcnt(0)\n" \
  ".LU_sk2_%=:\n" \
  "s_add_u32 s48, s48, 1\n" \
  "s_add_u32 s73, s73, s70\n" \
  "s_cmp_eq_u32 s73, s74\n" \
  "s_cselect_b32 s49, 2130706432, s49\n" \
  "s_cmp_le_u32 s48, 5\n" \
  "s_addc_u32 s67, s73, 0\n" \
  "s_cmp_gt_u32 s67, s74\n" \
  "s_cselect_b32 s67, 2, 0\n" \
  "s_cmp_le_u32 s48, 1\n" \
  "s_addc_u32 s67, s67, 0\n" \
  "s_cmp_eq_u32 s67, 0\n" \
  "s_waitcnt lgkmcnt(0)\n" \
  "s_branch .LU_first_b_%=\n" \
  ".LU_now_%=:\n" \
  "s_cmp_gt_u32 s48, 1\n" \
  "s_cbranch_scc1 .LU_loop_%=\n" \
  "s_nop 7\n" \
  "s_mov_b32 s63, s57\n" \
  "v_cvt_pk_bf16_f32 v104, v88, v89\n" \
  "v_cvt_pk_bf16_f32 v105, v90, v91\n" \
  "v_cvt_pk_bf16_f32 v106, v92, v93\n" \
  "v_cvt_pk_bf16_f32 v107, v94, v95\n" \
  "buffer_store_dwordx4 v[104:107], v190, s[40:43], s63 offen sc1\n" \
  "v_mov_b32 v168, s73\n" \
  "s_mov_b64 exec, 6\n" \
  "s_waitcnt vmcnt(0)\n" \
  "buffer_store_dwordx2 v[168:169], v167, s[40:43], 0 offen sc1\n" \
  "s_mov_b64 exec, -1\n" \
  "s_branch .LU_loop_%=\n" \
  ".LU_end_%=:\n" \
  "s_mov_b32 %0, s55\n"
#define P_ASM_VOPS "=&s"(tag), "+{v190}"(pi.l16), "+{v191}"(pi.xr0), "+{v192}"(pi.xr1), "+{v193}"(pi.ew), "+{v194}"(pi.er), "+{v195}"(pi.uo), "+{v196}"(pi.xs0), "+{v197}"(pi.xs1), "+{v198}"(pi.xs2), "+{v199}"(pi.xs3), "+{v200}"(pi.xs4), "+{v201}"(pi.xs5), "+{v202}"(pi.xs6), "+{v203}"(pi.xs7), "+{v204}"(pi.xs8), "+{v170}"(pi.vq0), "+{v171}"(pi.vq1), "+{v172}"(pi.vq2), "+{v173}"(pi.vq3), "+{v174}"(pi.vo0), "+{v175}"(pi.vo1), "+{v176}"(pi.vo2), "+{v177}"(pi.vo3), "+{v178}"(pi.v50), "+{v179}"(pi.v51), "+{v180}"(pi.v52), "+{v181}"(pi.v53), "+{v182}"(pi.tro0), "+{v183}"(pi.tro1)
#define P_ASM_SOPS "s"(pi.ws), "s"(pi.Wa), "s"(pi.Wb), "s"(pi.x), "s"(pi.w), "s"(pi.o_a), "s"(pi.o_b), "s"(pi.o_p), "s"(pi.o_q), "s"(pi.f_a), "s"(pi.f_b), "s"(pi.f_go), "s"(pi.o_ctl), "s"(pi.f_c), "s"(pi.u8), "s"(pi.rng), "s"(pi.f_d)
#define P_ASM_CLOB "v0", "v1", "v2", "v3", "v4", "v5", "v6", "v7", "v8", "v9", "v10", "v11", "v12", "v13", "v14", "v15", "v16", "v17", "v18", "v19", "v20", "v21", "v22", "v23", "v24", "v25", "v26", "v27", "v28", "v29", "v30", "v31", "v32", "v33", "v34", "v35", "v36", "v37", "v38", "v39", "v40", "v41", "v42", "v43", "v44", "v45", "v46", "v47", "v48", "v49", "v50", "v51", "v52", "v53", "v54", "v55", "v56", "v57", "v58", "v59", "v60", "v61", "v62", "v63", "v64", "v65", "v66", "v67", "v68", "v69", "v70", "v71", "v72", "v73", "v74", "v75", "v76", "v77", "v78", "v79", "v80", "v81", "v82", "v83", "v84", "v85", "v86", "v87", "v88", "v89", "v90", "v91", "v92", "v93", "v94", "v95", "v96", "v97", "v98", "v99", "v100", "v101", "v102", "v103", "v104", "v105", "v106", "v107", "v108", "v109", "v110", "v111", "v112", "v113", "v114", "v115", "v116", "v117", "v118", "v119", "v120", "v121", "v122", "v123", "v124", "v125", "v126", "v127", "v128", "v129", "v130", "v131", "v132", "v133", "v134", "v135", "v136", "v137", "v138", "v139", "v140", "v141", "v142", "v143", "v144", "v145", "v146", "v147", "v148", "v149", "v150", "v151", "v152", "v153", "v154", "v155", "v156", "v157", "v158", "v159", "v160", "v161", "v162", "v163", "v164", "v165", "v166", "v167", "v168", "v169", "v184", "v185", "v186", "v187", "v188", "v189", "a0", "a1", "a2", "a3", "a4", "a5", "a6", "a7", "a8", "a9", "a10", "a11", "a12", "a13", "a14", "a15", "a16", "a17", "a18", "a19", "a20", "a21", "a22", "a23", "a24", "a25", "a26", "a27", "a28", "a29", "a30", "a31", "a32", "a33", "a34", "a35", "a36", "a37", "a38", "a39", "a40", "a41", "a42", "a43", "a44", "a45", "a46", "a47", "a48", "a49", "a50", "a51", "a52", "a53", "a54", "a55", "a56", "a57", "a58", "a59", "a60", "a61", "a62", "a63", "a64", "a65", "a66", "a67", "a68", "a69", "a70", "a71", "a72", "a73", "a74", "a75", "a76", "a77", "a78", "a79", "a80", "a81", "a82", "a83", "a84", "a85", "a86", "a87", "a88", "a89", "a90", "a91", "a92", "a93", "a94", "a95", "a96", "a97", "a98", "a99", "a100", "a101", "a102", "a103", "a104", "a105", "a106", "a107", "a108", "a109", "a110", "a111", "a112", "a113", "a114", "a115", "a116", "a117", "a118", "a119", "a120", "a121", "a122", "a123", "a124", "a125", "a126", "a127", "a128", "a129", "a130", "a131", "a132", "a133", "a134", "a135", "a136", "a137", "a138", "a139", "a140", "a141", "a142", "a143", "a144", "a145", "a146", "a147", "a148", "a149", "a150", "a151", "a152", "a153", "a154", "a155", "a156", "a157", "a158", "a159", "a160", "a161", "a162", "a163", "a164", "a165", "a166", "a167", "a168", "a169", "a170", "a171", "a172", "a173", "a174", "a175", "a176", "a177", "a178", "a179", "a180", "a181", "a182", "a183", "a184", "a185", "a186", "a187", "a188", "a189", "a190", "a191", "a192", "a193", "a194", "a195", "a196", "a197", "a198", "a199", "a200", "a201", "a202", "a203", "a204", "a205", "a206", "a207", "a208", "a209", "a210", "a211", "a212", "a213", "a214", "a215", "a216", "a217", "a218", "a219", "a220", "a221", "a222", "a223", "a224", "a225", "a226", "a227", "a228", "a229", "a230", "a231", "a232", "a233", "a234", "a235", "a236", "a237", "a238", "a239", "a240", "a241", "a242", "a243", "a244", "a245", "a246", "a247", "a248", "a249", "a250", "a251", "a252", "a253", "a254", "a255", "s40", "s41", "s42", "s43", "s44", "s45", "s46", "s47", "s48", "s49", "s50", "s51", "s52", "s53", "s54", "s55", "s56", "s57", "s58", "s59", "s60", "s61", "s62", "s63", "s64", "s65", "s66", "s67", "s68", "s69", "s70", "s71", "s72", "s73", "s74", "s75", "s76", "s77", "s78", "s79", "s80", "s81", "s82", "s83", "s84", "s85", "s86", "s87", "s88", "s89", "s90", "s91", "s92", "s93", "s94", "s95", "s96", "s97", "s98", "s99", "vcc", "scc", "memory"
#define O_ASM_TEXT \
  "s_mov_b64 s[40:41], %11\n" \
  "s_and_b32 s41, s41, 0xffff\n" \
  "s_mov_b32 s42, 255852544\n" \
  "s_mov_b32 s43, 0x00020000\n" \
  "s_mov_b64 s[44:45], %12\n" \
  "s_and_b32 s45, s45, 0xffff\n" \
  "s_mov_b32 s46, 4718592\n" \
  "s_mov_b32 s47, 0x00020000\n" \
  "s_mul_i32 s62, %13, 24576\n" \
  "s_add_u32 s60, %15, 0\n" \
  "s_add_u32 m0, s62, 0\n" \
  "s_nop 0\n" \
  "buffer_load_dwordx4 v194, s[44:47], s60 offen lds\n" \
  "s_add_u32 m0, s62, 1024\n" \
  "s_nop 0\n" \
  "buffer_load_dwordx4 v195, s[44:47], s60 offen lds\n" \
  "s_add_u32 m0, s62, 2048\n" \
  "s_nop 0\n" \
  "buffer_load_dwordx4 v196, s[44:47], s60 offen lds\n" \
  "s_add_u32 s60, %15, 18432\n" \
  "s_add_u32 m0, s62, 3072\n" \
  "s_nop 0\n" \
  "buffer_load_dwordx4 v194, s[44:47], s60 offen lds\n" \
  "s_add_u32 m0, s62, 4096\n" \
  "s_nop 0\n" \
  "buffer_load_dwordx4 v195, s[44:47], s60 offen lds\n" \
  "s_add_u32 m0, s62, 5120\n" \
  "s_nop 0\n" \
  "buffer_load_dwordx4 v196, s[44:47], s60 offen lds\n" \
  "s_add_u32 s60, %15, 36864\n" \
  "s_add_u32 m0, s62, 6144\n" \
  "s_nop 0\n" \
  "buffer_load_dwordx4 v194, s[44:47], s60 offen lds\n" \
  "s_add_u32 m0, s62, 7168\n" \
  "s_nop 0\n" \
  "buffer_load_dwordx4 v195, s[44:47], s60 offen lds\n" \
  "s_add_u32 m0, s62, 8192\n" \
  "s_nop 0\n" \
  "buffer_load_dwordx4 v196, s[44:47], s60 offen lds\n" \
  "s_add_u32 s60, %15, 55296\n" \
  "s_add_u32 m0, s62, 9216\n" \
  "s_nop 0\n" \
  "buffer_load_dwordx4 v194, s[44:47], s60 offen lds\n" \
  "s_add_u32 m0, s62, 10240\n" \
  "s_nop 0\n" \
  "buffer_load_dwordx4 v195, s[44:47], s60 offen lds\n" \
  "s_add_u32 m0, s62, 11264\n" \
  "s_nop 0\n" \
  "buffer_load_dwordx4 v196, s[44:47], s60 offen lds\n" \
  "s_add_u32 s60, %15, 73728\n" \
  "s_add_u32 m0, s62, 12288\n" \
  "s_nop 0\n" \
  "buffer_load_dwordx4 v194, s[44:47], s60 offen lds\n" \
  "s_add_u32 m0, s62, 13312\n" \
  "s_nop 0\n" \
  "buffer_load_dwordx4 v195, s[44:47], s60 offen lds\n" \
  "s_add_u32 m0, s62, 14336\n" \
  "s_nop 0\n" \
  "buffer_load_dwordx4 v196, s[44:47], s60 offen lds\n" \
  "s_add_u32 s60, %15, 92160\n" \
  "s_add_u32 m0, s62, 15360\n" \
  "s_nop 0\n" \
  "buffer_load_dwordx4 v194, s[44:47], s60 offen lds\n" \
  "s_add_u32 m0, s62, 16384\n" \
  "s_nop 0\n" \
  "buffer_load_dwordx4 v195, s[44:47], s60 offen lds\n" \
  "s_add_u32 m0, s62, 17408\n" \
  "s_nop 0\n" \
  "buffer_load_dwordx4 v196, s[44:47], s60 offen lds\n" \
  "s_add_u32 s60, %15, 110592\n" \
  "s_add_u32 m0, s62, 18432\n" \
  "s_nop 0\n" \
  "buffer_load_dwordx4 v194, s[44:47], s60 offen lds\n" \
  "s_add_u32 m0, s62, 19456\n" \
  "s_nop 0\n" \
  "buffer_load_dwordx4 v195, s[44:47], s60 offen lds\n" \
  "s_add_u32 m0, s62, 20480\n" \
  "s_nop 0\n" \
  "buffer_load_dwordx4 v196, s[44:47], s60 offen lds\n" \
  "s_add_u32 s60, %15, 129024\n" \
  "s_add_u32 m0, s62, 21504\n" \
  "s_nop 0\n" \
  "buffer_load_dwordx4 v194, s[44:47], s60 offen lds\n" \
  "s_add_u32 m0, s62, 22528\n" \
  "s_nop 0\n" \
  "buffer_load_dwordx4 v195, s[44:47], s60 offen lds\n" \
  "s_add_u32 m0, s62, 23552\n" \
  "s_nop 0\n" \
  "buffer_load_dwordx4 v196, s[44:47], s60 offen lds\n" \
  "s_waitcnt vmcnt(0)\n" \
  "ds_read_b64_tr_b16 a[0:1], v197\n" \
  "ds_read_b64_tr_b16 a[2:3], v197 offset:3072\n" \
  "ds_read_b64_tr_b16 a[4:5], v197 offset:32\n" \
  "ds_read_b64_tr_b16 a[6:7], v197 offset:3104\n" \
  "ds_read_b64_tr_b16 a[8:9], v197 offset:64\n" \
  "ds_read_b64_tr_b16 a[10:11], v197 offset:3136\n" \
  "ds_read_b64_tr_b16 a[12:13], v197 offset:96\n" \
  "ds_read_b64_tr_b16 a[14:15], v197 offset:3168\n" \
  "ds_read_b64_tr_b16 a[16:17], v197 offset:128\n" \
  "ds_read_b64_tr_b16 a[18:19], v197 offset:3200\n" \
  "ds_read_b64_tr_b16 a[20:21], v197 offset:160\n" \
  "ds_read_b64_tr_b16 a[22:23], v197 offset:3232\n" \
  "ds_read_b64_tr_b16 a[24:25], v197 offset:6144\n" \
  "ds_read_b64_tr_b16 a[26:27], v197 offset:9216\n" \
  "ds_read_b64_tr_b16 a[28:29], v197 offset:6176\n" \
  "ds_read_b64_tr_b16 a[30:31], v197 offset:9248\n" \
  "ds_read_b64_tr_b16 a[32:33], v197 offset:6208\n" \
  "ds_read_b64_tr_b16 a[34:35], v197 offset:9280\n" \
  "ds_read_b64_tr_b16 a[36:37], v197 offset:6240\n" \
  "ds_read_b64_tr_b16 a[38:39], v197 offset:9312\n" \
  "ds_read_b64_tr_b16 a[40:41], v197 offset:6272\n" \
  "ds_read_b64_tr_b16 a[42:43], v197 offset:9344\n" \
  "ds_read_b64_tr_b16 a[44:45], v197 offset:6304\n" \
  "ds_read_b64_tr_b16 a[46:47], v197 offset:9376\n" \
  "ds_read_b64_tr_b16 a[48:49], v197 offset:12288\n" \
  "ds_read_b64_tr_b16 a[50:51], v197 offset:15360\n" \
  "ds_read_b64_tr_b16 a[52:53], v197 offset:12320\n" \
  "ds_read_b64_tr_b16 a[54:55], v197 offset:15392\n" \
  "ds_read_b64_tr_b16 a[56:57], v197 offset:12352\n" \
  "ds_read_b64_tr_b16 a[58:59], v197 offset:15424\n" \
  "ds_read_b64_tr_b16 a[60:61], v197 offset:12384\n" \
  "ds_read_b64_tr_b16 a[62:63], v197 offset:15456\n" \
  "ds_read_b64_tr_b16 a[64:65], v197 offset:12416\n" \
  "ds_read_b64_tr_b16 a[66:67], v197 offset:15488\n" \
  "ds_read_b64_tr_b16 a[68:69], v197 offset:12448\n" \
  "ds_read_b64_tr_b16 a[70:71], v197 offset:15520\n" \
  "ds_read_b64_tr_b16 a[72:73], v197 offset:18432\n" \
  "ds_read_b64_tr_b16 a[74:75], v197 offset:21504\n" \
  "ds_read_b64_tr_b16 a[76:77], v197 offset:18464\n" \
  "ds_read_b64_tr_b16 a[78:79], v197 offset:21536\n" \
  "ds_read_b64_tr_b16 a[80:81], v197 offset:18496\n" \
  "ds_read_b64_tr_b16 a[82:83], v197 offset:21568\n" \
  "ds_read_b64_tr_b16 a[84:85], v197 offset:18528\n" \
  "ds_read_b64_tr_b16 a[86:87], v197 offset:21600\n" \
  "ds_read_b64_tr_b16 a[88:89], v197 offset:18560\n" \
  "ds_read_b64_tr_b16 a[90:91], v197 offset:21632\n" \
  "ds_read_b64_tr_b16 a[92:93], v197 offset:18592\n" \
  "ds_read_b64_tr_b16 a[94:95], v197 offset:21664\n" \
  "s_waitcnt lgkmcnt(0)\n" \
  "s_add_u32 s60, %15, 147456\n" \
  "s_add_u32 m0, s62, 0\n" \
  "s_nop 0\n" \
  "buffer_load_dwordx4 v194, s[44:47], s60 offen lds\n" \
  "s_add_u32 m0, s62, 1024\n" \
  "s_nop 0\n" \
  "buffer_load_dwordx4 v195, s[44:47], s60 offen lds\n" \
  "s_add_u32 m0, s62, 2048\n" \
  "s_nop 0\n" \
  "buffer_load_dwordx4 v196, s[44:47], s60 offen lds\n" \
  "s_add_u32 s60, %15, 165888\n" \
  "s_add_u32 m0, s62, 3072\n" \
  "s_nop 0\n" \
  "buffer_load_dwordx4 v194, s[44:47], s60 offen lds\n" \
  "s_add_u32 m0, s62, 4096\n" \
  "s_nop 0\n" \
  "buffer_load_dwordx4 v195, s[44:47], s60 offen lds\n" \
  "s_add_u32 m0, s62, 5120\n" \
  "s_nop 0\n" \
  "buffer_load_dwordx4 v196, s[44:47], s60 offen lds\n" \
  "s_add_u32 s60, %15, 184320\n" \
  "s_add_u32 m0, s62, 6144\n" \
  "s_nop 0\n" \
  "buffer_load_dwordx4 v194, s[44:47], s60 offen lds\n" \
  "s_add_u32 m0, s62, 7168\n" \
  "s_nop 0\n" \
  "buffer_load_dwordx4 v195, s[44:47], s60 offen lds\n" \
  "s_add_u32 m0, s62, 8192\n" \
  "s_nop 0\n" \
  "buffer_load_dwordx4 v196, s[44:47], s60 offen lds\n" \
  "s_add_u32 s60, %15, 202752\n" \
  "s_add_u32 m0, s62, 9216\n" \
  "s_nop 0\n" \
  "buffer_load_dwordx4 v194, s[44:47], s60 offen lds\n" \
  "s_add_u32 m0, s62, 10240\n" \
  "s_nop 0\n" \
  "buffer_load_dwordx4 v195, s[44:47], s60 offen lds\n" \
  "s_add_u32 m0, s62, 11264\n" \
  "s_nop 0\n" \
  "buffer_load_dwordx4 v196, s[44:47], s60 offen lds\n" \
  "s_add_u32 s60, %15, 221184\n" \
  "s_add_u32 m0, s62, 12288\n" \
  "s_nop 0\n" \
  "buffer_load_dwordx4 v194, s[44:47], s60 offen lds\n" \
  "s_add_u32 m0, s62, 13312\n" \
  "s_nop 0\n" \
  "buffer_load_dwordx4 v195, s[44:47], s60 offen lds\n" \
  "s_add_u32 m0, s62, 14336\n" \
  "s_nop 0\n" \
  "buffer_load_dwordx4 v196, s[44:47], s60 offen lds\n" \
  "s_add_u32 s60, %15, 239616\n" \
  "s_add_u32 m0, s62, 15360\n" \
  "s_nop 0\n" \
  "buffer_load_dwordx4 v194, s[44:47], s60 offen lds\n" \
  "s_add_u32 m0, s62, 16384\n" \
  "s_nop 0\n" \
  "buffer_load_dwordx4 v195, s[44:47], s60 offen lds\n" \
  "s_add_u32 m0, s62, 17408\n" \
  "s_nop 0\n" \
  "buffer_load_dwordx4 v196, s[44:47], s60 offen lds\n" \
  "s_add_u32 s60, %15, 258048\n" \
  "s_add_u32 m0, s62, 18432\n" \
  "s_nop 0\n" \
  "buffer_load_dwordx4 v194, s[44:47], s60 offen lds\n" \
  "s_add_u32 m0, s62, 19456\n" \
  "s_nop 0\n" \
  "buffer_load_dwordx4 v195, s[44:47], s60 offen lds\n" \
  "s_add_u32 m0, s62, 20480\n" \
  "s_nop 0\n" \
  "buffer_load_dwordx4 v196, s[44:47], s60 offen lds\n" \
  "s_add_u32 s60, %15, 276480\n" \
  "s_add_u32 m0, s62, 21504\n" \
  "s_nop 0\n" \
  "buffer_load_dwordx4 v194, s[44:47], s60 offen lds\n" \
  "s_add_u32 m0, s62, 22528\n" \
  "s_nop 0\n" \
  "buffer_load_dwordx4 v195, s[44:47], s60 offen lds\n" \
  "s_add_u32 m0, s62, 23552\n" \
  "s_nop 0\n" \
  "buffer_load_dwordx4 v196, s[44:47], s60 offen lds\n" \
  "s_waitcnt vmcnt(0)\n" \
  "ds_read_b64_tr_b16 a[96:97], v197\n" \
  "ds_read_b64_tr_b16 a[98:99], v197 offset:3072\n" \
  "ds_read_b64_tr_b16 a[100:101], v197 offset:32\n" \
  "ds_read_b64_tr_b16 a[102:103], v197 offset:3104\n" \
  "ds_read_b64_tr_b16 a[104:105], v197 offset:64\n" \
  "ds_read_b64_tr_b16 a[106:107], v197 offset:3136\n" \
  "ds_read_b64_tr_b16 a[108:109], v197 offset:96\n" \
  "ds_read_b64_tr_b16 a[110:111], v197 offset:3168\n" \
  "ds_read_b64_tr_b16 a[112:113], v197 offset:128\n" \
  "ds_read_b64_tr_b16 a[114:115], v197 offset:3200\n" \
  "ds_read_b64_tr_b16 a[116:117], v197 offset:160\n" \
  "ds_read_b64_tr_b16 a[118:119], v197 offset:3232\n" \
  "ds_read_b64_tr_b16 a[120:121], v197 offset:6144\n" \
  "ds_read_b64_tr_b16 a[122:123], v197 offset:9216\n" \
  "ds_read_b64_tr_b16 a[124:125], v197 offset:6176\n" \
  "ds_read_b64_tr_b16 a[126:127], v197 offset:9248\n" \
  "ds_read_b64_tr_b16 a[128:129], v197 offset:6208\n" \
  "ds_read_b64_tr_b16 a[130:131], v197 offset:9280\n" \
  "ds_read_b64_tr_b16 a[132:133], v197 offset:6240\n" \
  "ds_read_b64_tr_b16 a[134:135], v197 offset:9312\n" \
  "ds_read_b64_tr_b16 a[136:137], v197 offset:6272\n" \
  "ds_read_b64_tr_b16 a[138:139], v197 offset:9344\n" \
  "ds_read_b64_tr_b16 a[140:141], v197 offset:6304\n" \
  "ds_read_b64_tr_b16 a[142:143], v197 offset:9376\n" \
  "ds_read_b64_tr_b16 a[144:145], v197 offset:12288\n" \
  "ds_read_b64_tr_b16 a[146:147], v197 offset:15360\n" \
  "ds_read_b64_tr_b16 a[148:149], v197 offset:12320\n" \
  "ds_read_b64_tr_b16 a[150:151], v197 offset:15392\n" \
  "ds_read_b64_tr_b16 a[152:153], v197 offset:12352\n" \
  "ds_read_b64_tr_b16 a[154:155], v197 offset:15424\n" \
  "ds_read_b64_tr_b16 a[156:157], v197 offset:12384\n" \
  "ds_read_b64_tr_b16 a[158:159], v197 offset:15456\n" \
  "ds_read_b64_tr_b16 a[160:161], v197 offset:12416\n" \
  "ds_read_b64_tr_b16 a[162:163], v197 offset:15488\n" \
  "ds_read_b64_tr_b16 a[164:165], v197 offset:12448\n" \
  "ds_read_b64_tr_b16 a[166:167], v197 offset:15520\n" \
  "ds_read_b64_tr_b16 a[168:169], v197 offset:18432\n" \
  "ds_read_b64_tr_b16 a[170:171], v197 offset:21504\n" \
  "ds_read_b64_tr_b16 a[172:173], v197 offset:18464\n" \
  "ds_read_b64_tr_b16 a[174:175], v197 offset:21536\n" \
  "ds_read_b64_tr_b16 a[176:177], v197 offset:18496\n" \
  "ds_read_b64_tr_b16 a[178:179], v197 offset:21568\n" \
  "ds_read_b64_tr_b16 a[180:181], v197 offset:18528\n" \
  "ds_read_b64_tr_b16 a[182:183], v197 offset:21600\n" \
  "ds_read_b64_tr_b16 a[184:185], v197 offset:18560\n" \
  "ds_read_b64_tr_b16 a[186:187], v197 offset:21632\n" \
  "ds_read_b64_tr_b16 a[188:189], v197 offset:18592\n" \
  "ds_read_b64_tr_b16 a[190:191], v197 offset:21664\n" \
  "s_waitcnt lgkmcnt(0)\n" \
  "s_barrier\n" \
  "s_mov_b32 s56, %20\n" \
  "s_mov_b32 s48, 0\n" \
  "s_mov_b32 s53, 2130706432\n" \
  "v_mov_b32 v187, %14\n" \
  "s_branch .LO_fl_%=\n" \
  ".LO_fl_b_%=:\n" \
  "s_and_b32 s48, s56, 255\n" \
  "s_bfe_u32 s55, s56, 0x80008\n" \
  "s_lshr_b32 s56, s56, 16\n" \
  "s_lshl_b32 s49, s48, 19\n" \
  "s_add_u32 s49, s49, %16\n" \
  "s_mul_i32 s50, s48, 393216\n" \
  "s_add_u32 s50, s50, %17\n" \
  "s_mov_b32 s53, 2130706432\n" \
  "s_mov_b32 s94, s48\n" \
  "v_mov_b32 v191, v199\n" \
  "v_mov_b32 v192, v200\n" \
  "s_mov_b32 s69, 0\n" \
  ".LO_p0_%=:\n" \
  "buffer_load_dwordx2 v[188:189], v193, s[40:43], 0 offen sc1\n" \
  "s_waitcnt vmcnt(0)\n" \
  "v_cmp_ne_u32 vcc, %14, v189\n" \
  "s_nop 1\n" \
  "v_cndmask_b32 v188, v188, 0, vcc\n" \
  "v_cmp_ge_i32 vcc, s94, v188\n" \
  "s_cbranch_vccz .LO_p0_x_%=\n" \
  "s_sleep 2\n" \
  "s_add_u32 s69, s69, 1\n" \
  "s_and_b32 s63, s69, 1023\n" \
  "s_cbranch_scc1 .LO_p0_%=\n" \
  "buffer_load_dwordx2 v[188:189], off, s[40:43], %19 offset:128 sc1\n" \
  "s_waitcnt vmcnt(0)\n" \
  "v_readfirstlane_b32 s63, v189\n" \
  "s_cmp_eq_u32 s63, %14\n" \
  "s_cbranch_scc1 .LO_p0_x_%=\n" \
  "s_cmp_le_u32 s69, 2097152\n" \
  "s_cbranch_scc1 .LO_p0_%=\n" \
  "v_mov_b32 v188, 1\n" \
  "v_mov_b32 v189, %14\n" \
  "buffer_store_dwordx2 v[188:189], off, s[40:43], %19 offset:128 sc1\n" \
  "s_waitcnt vmcnt(0)\n" \
  ".LO_p0_x_%=:\n" \
  "s_lshr_b32 s76, s49, 8\n" \
  "s_and_b32 s76, s76, 8192\n" \
  "s_xor_b32 s76, s76, s49\n" \
  "s_add_u32 s77, s76, 4096\n" \
  "s_add_u32 s78, s76, 16384\n" \
  "s_add_u32 s79, s76, 20480\n" \
  "s_add_u32 s49, s49, 524288\n" \
  "buffer_load_dwordx4 v[0:3], v190, s[40:43], s76 offen sc1\n" \
  "buffer_load_dwordx4 v[8:11], v190, s[40:43], s76 offen offset:2048 sc1\n" \
  "buffer_load_dwordx4 v[16:19], v190, s[40:43], s77 offen sc1\n" \
  "buffer_load_dwordx4 v[24:27], v190, s[40:43], s77 offen offset:2048 sc1\n" \
  "buffer_load_dwordx4 v[32:35], v190, s[40:43], s78 offen sc1\n" \
  "buffer_load_dwordx4 v[40:43], v190, s[40:43], s78 offen offset:2048 sc1\n" \
  "buffer_load_dwordx4 v[48:51], v190, s[40:43], s79 offen sc1\n" \
  "buffer_load_dwordx4 v[56:59], v190, s[40:43], s79 offen offset:2048 sc1\n" \
  "buffer_load_dwordx4 v[4:7], v190, s[40:43], s76 offen offset:1024 sc1\n" \
  "buffer_load_dwordx4 v[12:15], v190, s[40:43], s76 offen offset:3072 sc1\n" \
  "buffer_load_dwordx4 v[20:23], v190, s[40:43], s77 offen offset:1024 sc1\n" \
  "buffer_load_dwordx4 v[28:31], v190, s[40:43], s77 offen offset:3072 sc1\n" \
  "buffer_load_dwordx4 v[36:39], v190, s[40:43], s78 offen offset:1024 sc1\n" \
  "buffer_load_dwordx4 v[44:47], v190, s[40:43], s78 offen offset:3072 sc1\n" \
  "buffer_load_dwordx4 v[52:55], v190, s[40:43], s79 offen offset:1024 sc1\n" \
  "buffer_load_dwordx4 v[60:63], v190, s[40:43], s79 offen offset:3072 sc1\n" \
  "s_lshr_b32 s76, s49, 8\n" \
  "s_and_b32 s76, s76, 8192\n" \
  "s_xor_b32 s76, s76, s49\n" \
  "s_add_u32 s77, s76, 4096\n" \
  "s_add_u32 s78, s76, 16384\n" \
  "s_add_u32 s79, s76, 20480\n" \
  "s_add_u32 s49, s49, 524288\n" \
  "s_add_u32 s94, s94, 1\n" \
  "s_mov_b32 s54, 0\n" \
  "s_waitcnt vmcnt(0)\n" \
  "v_cmp_ne_u32 vcc, %14, v189\n" \
  "s_nop 1\n" \
  "v_cndmask_b32 v188, v188, 0, vcc\n" \
  "v_cmp_ge_i32 vcc, s94, v188\n" \
  ".LO_loop_%=:\n" \
  "s_cbranch_vccnz .LO_pl_%=\n" \
  ".LO_pl_b_%=:\n" \
  "s_waitcnt vmcnt(18)\n" \
  "v_mfma_f32_16x16x32_bf16 v[64:67], a[0:3], v[0:3], 0\n" \
  "s_waitcnt lgkmcnt(4)\n" \
  "buffer_load_dwordx2 v[188:189], v193, s[40:43], 0 offen sc1\n" \
  "v_mfma_f32_16x16x32_bf16 v[68:71], a[4:7], v[0:3], 0\n" \
  "s_waitcnt lgkmcnt(3)\n" \
  "v_mfma_f32_16x16x32_bf16 v[72:75], a[8:11], v[0:3], 0\n" \
  "ds_write_b128 v191, v[88:91] offset:6144\n" \
  "s_waitcnt lgkmcnt(3)\n" \
  "v_mfma_f32_16x16x32_bf16 v[76:79], a[12:15], v[0:3], 0\n" \
  "s_mov_b32 s81, s79\n" \
  "s_waitcnt lgkmcnt(2)\n" \
  "v_mfma_f32_16x16x32_bf16 v[80:83], a[16:19], v[0:3], 0\n" \
  "s_waitcnt lgkmcnt(1)\n" \
  "v_mfma_f32_16x16x32_bf16 v[84:87], a[20:23], v[0:3], 0\n" \
  "buffer_load_dwordx4 v[0:3], v190, s[40:43], s76 offen sc1\n" \
  "s_waitcnt vmcnt(19)\n" \
  "v_mfma_f32_16x16x32_bf16 v[64:67], a[24:27], v[8:11], v[64:67]\n" \
  "v_mfma_f32_16x16x32_bf16 v[68:71], a[28:31], v[8:11], v[68:71]\n" \
  "v_mfma_f32_16x16x32_bf16 v[72:75], a[32:35], v[8:11], v[72:75]\n" \
  "ds_write_b128 v191, v[92:95] offset:7168\n" \
  "v_mfma_f32_16x16x32_bf16 v[76:79], a[36:39], v[8:11], v[76:79]\n" \
  "v_mfma_f32_16x16x32_bf16 v[80:83], a[40:43], v[8:11], v[80:83]\n" \
  "s_waitcnt vmcnt(18)\n" \
  "v_mfma_f32_16x16x32_bf16 v[84:87], a[44:47], v[8:11], v[84:87]\n" \
  "buffer_load_dwordx4 v[8:11], v190, s[40:43], s76 offen offset:2048 sc1\n" \
  "v_mfma_f32_16x16x32_bf16 v[64:67], a[48:51], v[16:19], v[64:67]\n" \
  "v_mfma_f32_16x16x32_bf16 v[68:71], a[52:55], v[16:19], v[68:71]\n" \
  "v_mfma_f32_16x16x32_bf16 v[72:75], a[56:59], v[16:19], v[72:75]\n" \
  "ds_write_b128 v191, v[96:99] offset:8192\n" \
  "v_mfma_f32_16x16x32_bf16 v[76:79], a[60:63], v[16:19], v[76:79]\n" \
  "v_mfma_f32_16x16x32_bf16 v[80:83], a[64:67], v[16:19], v[80:83]\n" \
  "s_waitcnt vmcnt(18)\n" \
  "v_mfma_f32_16x16x32_bf16 v[84:87], a[68:71], v[16:19], v[84:87]\n" \
  "buffer_load_dwordx4 v[16:19], v190, s[40:43], s77 offen sc1\n" \
  "v_mfma_f32_16x16x32_bf16 v[64:67], a[72:75], v[24:27], v[64:67]\n" \
  "v_mfma_f32_16x16x32_bf16 v[68:71], a[76:79], v[24:27], v[68:71]\n" \
  "v_mfma_f32_16x16x32_bf16 v[72:75], a[80:83], v[24:27], v[72:75]\n" \
  "ds_write_b128 v191, v[100:103] offset:9216\n" \
  "v_mfma_f32_16x16x32_bf16 v[76:79], a[84:87], v[24:27], v[76:79]\n" \
  "v_mfma_f32_16x16x32_bf16 v[80:83], a[88:91], v[24:27], v[80:83]\n" \
  "s_waitcnt vmcnt(18)\n" \
  "v_mfma_f32_16x16x32_bf16 v[84:87], a[92:95], v[24:27], v[84:87]\n" \
  "buffer_load_dwordx4 v[24:27], v190, s[40:43], s77 offen offset:2048 sc1\n" \
  "v_mfma_f32_16x16x32_bf16 v[64:67], a[96:99], v[32:35], v[64:67]\n" \
  "v_mfma_f32_16x16x32_bf16 v[68:71], a[100:103], v[32:35], v[68:71]\n" \
  "v_mfma_f32_16x16x32_bf16 v[72:75], a[104:107], v[32:35], v[72:75]\n" \
  "ds_write_b128 v191, v[104:107] offset:10240\n" \
  "v_mfma_f32_16x16x32_bf16 v[76:79], a[108:111], v[32:35], v[76:79]\n" \
  "v_mfma_f32_16x16x32_bf16 v[80:83], a[112:115], v[32:35], v[80:83]\n" \
  "s_waitcnt vmcnt(18)\n" \
  "v_mfma_f32_16x16x32_bf16 v[84:87], a[116:119], v[32:35], v[84:87]\n" \
  "buffer_load_dwordx4 v[32:35], v190, s[40:43], s78 offen sc1\n" \
  "v_mfma_f32_16x16x32_bf16 v[64:67], a[120:123], v[40:43], v[64:67]\n" \
  "v_mfma_f32_16x16x32_bf16 v[68:71], a[124:127], v[40:43], v[68:71]\n" \
  "v_mfma_f32_16x16x32_bf16 v[72:75], a[128:131], v[40:43], v[72:75]\n" \
  "ds_write_b128 v191, v[108:111] offset:11264\n" \
  "v_mfma_f32_16x16x32_bf16 v[76:79], a[132:135], v[40:43], v[76:79]\n" \
  "v_xor_b32 v191, 65536, v191\n" \
  "v_mfma_f32_16x16x32_bf16 v[80:83], a[136:139], v[40:43], v[80:83]\n" \
  "s_waitcnt vmcnt(18)\n" \
  "v_mfma_f32_16x16x32_bf16 v[84:87], a[140:143], v[40:43], v[84:87]\n" \
  "buffer_load_dwordx4 v[40:43], v190, s[40:43], s78 offen offset:2048 sc1\n" \
  "v_mfma_f32_16x16x32_bf16 v[64:67], a[144:147], v[48:51], v[64:67]\n" \
  "v_mfma_f32_16x16x32_bf16 v[68:71], a[148:151], v[48:51], v[68:71]\n" \
  "s_add_i32 s60, s48, -1\n" \
  "v_mov_b32 v186, s60\n" \
  "v_mfma_f32_16x16x32_bf16 v[72:75], a[152:155], v[48:51], v[72:75]\n" \
  "v_mfma_f32_16x16x32_bf16 v[76:79], a[156:159], v[48:51], v[76:79]\n" \
  "s_waitcnt lgkmcnt(0)\n" \
  "s_waitcnt vmcnt(8)\n" \
  "s_barrier\n" \
  "v_mfma_f32_16x16x32_bf16 v[80:83], a[160:163], v[48:51], v[80:83]\n" \
  "ds_read_b128 v[120:123], v192\n" \
  "v_mfma_f32_16x16x32_bf16 v[84:87], a[164:167], v[48:51], v[84:87]\n" \
  "buffer_load_dwordx4 v[48:51], v190, s[40:43], s79 offen sc1\n" \
  "v_mfma_f32_16x16x32_bf16 v[64:67], a[168:171], v[56:59], v[64:67]\n" \
  "ds_read_b128 v[112:115], v192 offset:12288\n" \
  "v_mfma_f32_16x16x32_bf16 v[68:71], a[172:175], v[56:59], v[68:71]\n" \
  "buffer_store_dwordx2 v[186:187], off, s[40:43], %18 sc1\n" \
  "v_mfma_f32_16x16x32_bf16 v[72:75], a[176:179], v[56:59], v[72:75]\n" \
  "ds_read_b128 v[116:119], v192 offset:24576\n" \
  "v_mfma_f32_16x16x32_bf16 v[76:79], a[180:183], v[56:59], v[76:79]\n" \
  "ds_read_b128 v[124:127], v192 offset:36864\n" \
  "v_mfma_f32_16x16x32_bf16 v[80:83], a[184:187], v[56:59], v[80:83]\n" \
  "ds_read_b128 v[128:131], v192 offset:1024\n" \
  "v_mfma_f32_16x16x32_bf16 v[84:87], a[188:191], v[56:59], v[84:87]\n" \
  "buffer_load_dwordx4 v[56:59], v190, s[40:43], s79 offen offset:2048 sc1\n" \
  "v_mfma_f32_16x16x32_bf16 v[88:91], a[0:3], v[4:7], 0\n" \
  "ds_read_b128 v[132:135], v192 offset:13312\n" \
  "ds_read_b128 v[136:139], v192 offset:25600\n" \
  "v_mfma_f32_16x16x32_bf16 v[92:95], a[4:7], v[4:7], 0\n" \
  "ds_read_b128 v[140:143], v192 offset:37888\n" \
  "ds_read_b128 v[144:147], v192 offset:2048\n" \
  "v_mfma_f32_16x16x32_bf16 v[96:99], a[8:11], v[4:7], 0\n" \
  "ds_write_b128 v191, v[64:67]\n" \
  "v_mfma_f32_16x16x32_bf16 v[100:103], a[12:15], v[4:7], 0\n" \
  "s_lshr_b32 s80, s49, 8\n" \
  "s_and_b32 s80, s80, 8192\n" \
  "v_mfma_f32_16x16x32_bf16 v[104:107], a[16:19], v[4:7], 0\n" \
  "s_xor_b32 s80, s80, s49\n" \
  "s_add_u32 s49, s49, 524288\n" \
  "v_mfma_f32_16x16x32_bf16 v[108:111], a[20:23], v[4:7], 0\n" \
  "buffer_load_dwordx4 v[4:7], v190, s[40:43], s76 offen offset:1024 sc1\n" \
  "v_mfma_f32_16x16x32_bf16 v[88:91], a[24:27], v[12:15], v[88:91]\n" \
  "ds_read_b128 v[148:151], v192 offset:14336\n" \
  "v_mfma_f32_16x16x32_bf16 v[92:95], a[28:31], v[12:15], v[92:95]\n" \
  "ds_read_b128 v[152:155], v192 offset:26624\n" \
  "ds_read_b128 v[156:159], v192 offset:38912\n" \
  "v_mfma_f32_16x16x32_bf16 v[96:99], a[32:35], v[12:15], v[96:99]\n" \
  "ds_write_b128 v191, v[68:71] offset:1024\n" \
  "v_mfma_f32_16x16x32_bf16 v[100:103], a[36:39], v[12:15], v[100:103]\n" \
  "s_add_u32 s79, s80, 20480\n" \
  "s_add_u32 s48, s48, 1\n" \
  "v_mfma_f32_16x16x32_bf16 v[104:107], a[40:43], v[12:15], v[104:107]\n" \
  "s_waitcnt lgkmcnt(12)\n" \
  "v_add_f32 v120, v120, v112\n" \
  "v_add_f32 v121, v121, v113\n" \
  "v_mfma_f32_16x16x32_bf16 v[108:111], a[44:47], v[12:15], v[108:111]\n" \
  "buffer_load_dwordx4 v[12:15], v190, s[40:43], s76 offen offset:3072 sc1\n" \
  "v_mfma_f32_16x16x32_bf16 v[88:91], a[48:51], v[20:23], v[88:91]\n" \
  "s_mov_b32 s76, s80\n" \
  "v_add_f32 v122, v122, v114\n" \
  "v_add_f32 v123, v123, v115\n" \
  "v_mfma_f32_16x16x32_bf16 v[92:95], a[52:55], v[20:23], v[92:95]\n" \
  "s_waitcnt lgkmcnt(10)\n" \
  "s_add_u32 s94, s48, 1\n" \
  "v_add_f32 v116, v116, v124\n" \
  "v_add_f32 v117, v117, v125\n" \
  "v_mfma_f32_16x16x32_bf16 v[96:99], a[56:59], v[20:23], v[96:99]\n" \
  "ds_write_b128 v191, v[72:75] offset:2048\n" \
  "v_mfma_f32_16x16x32_bf16 v[100:103], a[60:63], v[20:23], v[100:103]\n" \
  "v_add_f32 v118, v118, v126\n" \
  "v_add_f32 v119, v119, v127\n" \
  "v_mfma_f32_16x16x32_bf16 v[104:107], a[64:67], v[20:23], v[104:107]\n" \
  "v_add_f32 v120, v120, v116\n" \
  "v_add_f32 v121, v121, v117\n" \
  "v_mfma_f32_16x16x32_bf16 v[108:111], a[68:71], v[20:23], v[108:111]\n" \
  "buffer_load_dwordx4 v[20:23], v190, s[40:43], s77 offen offset:1024 sc1\n" \
  "v_mfma_f32_16x16x32_bf16 v[88:91], a[72:75], v[28:31], v[88:91]\n" \
  "v_add_f32 v122, v122, v118\n" \
  "v_add_f32 v123, v123, v119\n" \
  "v_mfma_f32_16x16x32_bf16 v[92:95], a[76:79], v[28:31], v[92:95]\n" \
  "v_cvt_pk_bf16_f32 v112, v120, v121\n" \
  "v_cvt_pk_bf16_f32 v113, v122, v123\n" \
  "v_mfma_f32_16x16x32_bf16 v[96:99], a[80:83], v[28:31], v[96:99]\n" \
  "ds_write_b128 v191, v[76:79] offset:3072\n" \
  "v_mfma_f32_16x16x32_bf16 v[100:103], a[84:87], v[28:31], v[100:103]\n" \
  "s_waitcnt lgkmcnt(10)\n" \
  "v_add_f32 v128, v128, v132\n" \
  "v_add_f32 v129, v129, v133\n" \
  "v_mfma_f32_16x16x32_bf16 v[104:107], a[88:91], v[28:31], v[104:107]\n" \
  "v_add_f32 v130, v130, v134\n" \
  "v_add_f32 v131, v131, v135\n" \
  "v_mfma_f32_16x16x32_bf16 v[108:111], a[92:95], v[28:31], v[108:111]\n" \
  "buffer_load_dwordx4 v[28:31], v190, s[40:43], s77 offen offset:3072 sc1\n" \
  "v_mfma_f32_16x16x32_bf16 v[88:91], a[96:99], v[36:39], v[88:91]\n" \
  "s_waitcnt lgkmcnt(8)\n" \
  "s_add_u32 s77, s80, 4096\n" \
  "v_add_f32 v136, v136, v140\n" \
  "v_add_f32 v137, v137, v141\n" \
  "v_mfma_f32_16x16x32_bf16 v[92:95], a[100:103], v[36:39], v[92:95]\n" \
  "v_add_f32 v138, v138, v142\n" \
  "v_add_f32 v139, v139, v143\n" \
  "v_mfma_f32_16x16x32_bf16 v[96:99], a[104:107], v[36:39], v[96:99]\n" \
  "ds_write_b128 v191, v[80:83] offset:4096\n" \
  "v_mfma_f32_16x16x32_bf16 v[100:103], a[108:111], v[36:39], v[100:103]\n" \
  "v_add_f32 v128, v128, v136\n" \
  "v_add_f32 v129, v129, v137\n" \
  "v_mfma_f32_16x16x32_bf16 v[104:107], a[112:115], v[36:39], v[104:107]\n" \
  "v_add_f32 v130, v130, v138\n" \
  "v_add_f32 v131, v131, v139\n" \
  "v_mfma_f32_16x16x32_bf16 v[108:111], a[116:119], v[36:39], v[108:111]\n" \
  "buffer_load_dwordx4 v[36:39], v190, s[40:43], s78 offen offset:1024 sc1\n" \
  "v_mfma_f32_16x16x32_bf16 v[88:91], a[120:123], v[44:47], v[88:91]\n" \
  "v_cvt_pk_bf16_f32 v114, v128, v129\n" \
  "v_cvt_pk_bf16_f32 v115, v130, v131\n" \
  "v_mfma_f32_16x16x32_bf16 v[92:95], a[124:127], v[44:47], v[92:95]\n" \
  "buffer_store_dwordx4 v[112:115], v190, s[40:43], s53 offen sc1\n" \
  "v_mfma_f32_16x16x32_bf16 v[96:99], a[128:131], v[44:47], v[96:99]\n" \
  "ds_write_b128 v191, v[84:87] offset:5120\n" \
  "v_mfma_f32_16x16x32_bf16 v[100:103], a[132:135], v[44:47], v[100:103]\n" \
  "s_waitcnt lgkmcnt(7)\n" \
  "v_add_f32 v144, v144, v148\n" \
  "v_add_f32 v145, v145, v149\n" \
  "v_mfma_f32_16x16x32_bf16 v[104:107], a[136:139], v[44:47], v[104:107]\n" \
  "v_add_f32 v146, v146, v150\n" \
  "v_add_f32 v147, v147, v151\n" \
  "v_mfma_f32_16x16x32_bf16 v[108:111], a[140:143], v[44:47], v[108:111]\n" \
  "buffer_load_dwordx4 v[44:47], v190, s[40:43], s78 offen offset:3072 sc1\n" \
  "v_mfma_f32_16x16x32_bf16 v[88:91], a[144:147], v[52:55], v[88:91]\n" \
  "s_waitcnt lgkmcnt(5)\n" \
  "s_add_u32 s78, s80, 16384\n" \
  "v_add_f32 v152, v152, v156\n" \
  "v_add_f32 v153, v153, v157\n" \
  "v_mfma_f32_16x16x32_bf16 v[92:95], a[148:151], v[52:55], v[92:95]\n" \
  "v_add_f32 v154, v154, v158\n" \
  "v_add_f32 v155, v155, v159\n" \
  "v_mfma_f32_16x16x32_bf16 v[96:99], a[152:155], v[52:55], v[96:99]\n" \
  "s_cmp_lt_u32 s94, s55\n" \
  "s_cselect_b32 s94, s94, -1\n" \
  "s_waitcnt vmcnt(16)\n" \
  "v_cmp_ne_u32 vcc, %14, v189\n" \
  "v_mfma_f32_16x16x32_bf16 v[100:103], a[156:159], v[52:55], v[100:103]\n" \
  "v_add_f32 v144, v144, v152\n" \
  "v_add_f32 v145, v145, v153\n" \
  "v_mfma_f32_16x16x32_bf16 v[104:107], a[160:163], v[52:55], v[104:107]\n" \
  "v_add_f32 v146, v146, v154\n" \
  "v_add_f32 v147, v147, v155\n" \
  "v_mfma_f32_16x16x32_bf16 v[108:111], a[164:167], v[52:55], v[108:111]\n" \
  "buffer_load_dwordx4 v[52:55], v190, s[40:43], s81 offen offset:1024 sc1\n" \
  "v_mfma_f32_16x16x32_bf16 v[88:91], a[168:171], v[60:63], v[88:91]\n" \
  "v_cvt_pk_bf16_f32 v116, v144, v145\n" \
  "v_cvt_pk_bf16_f32 v117, v146, v147\n" \
  "v_mfma_f32_16x16x32_bf16 v[92:95], a[172:175], v[60:63], v[92:95]\n" \
  "buffer_store_dwordx2 v[116:117], v198, s[40:43], s53 offen offset:1024 sc1\n" \
  "v_mfma_f32_16x16x32_bf16 v[96:99], a[176:179], v[60:63], v[96:99]\n" \
  "s_lshr_b32 s53, s50, 8\n" \
  "s_and_b32 s53, s53, 8192\n" \
  "v_cndmask_b32 v188, v188, 0, vcc\n" \
  "v_mfma_f32_16x16x32_bf16 v[100:103], a[180:183], v[60:63], v[100:103]\n" \
  "s_xor_b32 s53, s53, s50\n" \
  "s_add_u32 s50, s50, 393216\n" \
  "v_mfma_f32_16x16x32_bf16 v[104:107], a[184:187], v[60:63], v[104:107]\n" \
  "s_cmp_eq_u32 s54, 0\n" \
  "s_cselect_b32 s60, s48, 99\n" \
  "s_cmp_lt_u32 s60, s55\n" \
  "v_xor_b32 v192, 65536, v192\n" \
  "v_cmp_ge_i32 vcc, s94, v188\n" \
  "v_mfma_f32_16x16x32_bf16 v[108:111], a[188:191], v[60:63], v[108:111]\n" \
  "buffer_load_dwordx4 v[60:63], v190, s[40:43], s81 offen offset:3072 sc1\n" \
  "s_cbranch_scc1 .LO_loop_%=\n" \
  "s_cmp_lg_u32 s54, 0\n" \
  "s_cbranch_scc1 .LO_ft_%=\n" \
  ".LO_fl_%=:\n" \
  "s_nop 7\n" \
  "ds_write_b128 v191, v[88:91] offset:6144\n" \
  "ds_write_b128 v191, v[92:95] offset:7168\n" \
  "ds_write_b128 v191, v[96:99] offset:8192\n" \
  "ds_write_b128 v191, v[100:103] offset:9216\n" \
  "ds_write_b128 v191, v[104:107] offset:10240\n" \
  "ds_write_b128 v191, v[108:111] offset:11264\n" \
  "s_waitcnt lgkmcnt(0)\n" \
  "s_barrier\n" \
  "ds_read_b128 v[120:123], v192\n" \
  "ds_read_b128 v[112:115], v192 offset:12288\n" \
  "ds_read_b128 v[116:119], v192 offset:24576\n" \
  "ds_read_b128 v[124:127], v192 offset:36864\n" \
  "ds_read_b128 v[128:131], v192 offset:1024\n" \
  "ds_read_b128 v[132:135], v192 offset:13312\n" \
  "ds_read_b128 v[136:139], v192 offset:25600\n" \
  "ds_read_b128 v[140:143], v192 offset:37888\n" \
  "ds_read_b128 v[144:147], v192 offset:2048\n" \
  "ds_read_b128 v[148:151], v192 offset:14336\n" \
  "ds_read_b128 v[152:155], v192 offset:26624\n" \
  "ds_read_b128 v[156:159], v192 offset:38912\n" \
  "s_waitcnt lgkmcnt(10)\n" \
  "v_add_f32 v120, v120, v112\n" \
  "v_add_f32 v121, v121, v113\n" \
  "v_add_f32 v122, v122, v114\n" \
  "v_add_f32 v123, v123, v115\n" \
  "s_waitcnt lgkmcnt(8)\n" \
  "v_add_f32 v116, v116, v124\n" \
  "v_add_f32 v117, v117, v125\n" \
  "v_add_f32 v118, v118, v126\n" \
  "v_add_f32 v119, v119, v127\n" \
  "v_add_f32 v120, v120, v116\n" \
  "v_add_f32 v121, v121, v117\n" \
  "v_add_f32 v122, v122, v118\n" \
  "v_add_f32 v123, v123, v119\n" \
  "v_cvt_pk_bf16_f32 v112, v120, v121\n" \
  "v_cvt_pk_bf16_f32 v113, v122, v123\n" \
  "s_waitcnt lgkmcnt(6)\n" \
  "v_add_f32 v128, v128, v132\n" \
  "v_add_f32 v129, v129, v133\n" \
  "v_add_f32 v130, v130, v134\n" \
  "v_add_f32 v131, v131, v135\n" \
  "s_waitcnt lgkmcnt(4)\n" \
  "v_add_f32 v136, v136, v140\n" \
  "v_add_f32 v137, v137, v141\n" \
  "v_add_f32 v138, v138, v142\n" \
  "v_add_f32 v139, v139, v143\n" \
  "v_add_f32 v128, v128, v136\n" \
  "v_add_f32 v129, v129, v137\n" \
  "v_add_f32 v130, v130, v138\n" \
  "v_add_f32 v131, v131, v139\n" \
  "v_cvt_pk_bf16_f32 v114, v128, v129\n" \
  "v_cvt_pk_bf16_f32 v115, v130, v131\n" \
  "buffer_store_dwordx4 v[112:115], v190, s[40:43], s53 offen sc1\n" \
  "s_waitcnt lgkmcnt(2)\n" \
  "v_add_f32 v144, v144, v148\n" \
  "v_add_f32 v145, v145, v149\n" \
  "v_add_f32 v146, v146, v150\n" \
  "v_add_f32 v147, v147, v151\n" \
  "s_waitcnt lgkmcnt(0)\n" \
  "v_add_f32 v152, v152, v156\n" \
  "v_add_f32 v153, v153, v157\n" \
  "v_add_f32 v154, v154, v158\n" \
  "v_add_f32 v155, v155, v159\n" \
  "v_add_f32 v144, v144, v152\n" \
  "v_add_f32 v145, v145, v153\n" \
  "v_add_f32 v146, v146, v154\n" \
  "v_add_f32 v147, v147, v155\n" \
  "v_cvt_pk_bf16_f32 v116, v144, v145\n" \
  "v_cvt_pk_bf16_f32 v117, v146, v147\n" \
  "buffer_store_dwordx2 v[116:117], v198, s[40:43], s53 offen offset:1024 sc1\n" \
  "s_waitcnt vmcnt(0)\n" \
  "s_barrier\n" \
  "s_add_i32 s60, s48, 0\n" \
  "v_mov_b32 v186, s60\n" \
  "buffer_store_dwordx2 v[186:187], off, s[40:43], %18 sc1\n" \
  "s_cmp_lg_u32 s56, 0\n" \
  "s_cbranch_scc1 .LO_fl_b_%=\n" \
  "s_branch .LO_end_%=\n" \
  ".LO_pl_%=:\n" \
  "s_add_u32 s60, s48, 2\n" \
  "s_cmp_lg_u32 s60, s55\n" \
  "s_cbranch_scc1 .LO_pw_%=\n" \
  "s_mov_b32 s54, s76\n" \
  "s_mov_b32 s76, 2130706432\n" \
  "s_mov_b32 s77, 2130706432\n" \
  "s_mov_b32 s78, 2130706432\n" \
  "s_mov_b32 s79, 2130706432\n" \
  "s_branch .LO_pl_b_%=\n" \
  ".LO_ft_%=:\n" \
  "s_mov_b32 s94, s48\n" \
  ".LO_pw_%=:\n" \
  "s_mov_b32 s69, 0\n" \
  ".LO_p1_%=:\n" \
  "buffer_load_dwordx2 v[188:189], v193, s[40:43], 0 offen sc1\n" \
  "s_waitcnt vmcnt(0)\n" \
  "v_cmp_ne_u32 vcc, %14, v189\n" \
  "s_nop 1\n" \
  "v_cndmask_b32 v188, v188, 0, vcc\n" \
  "v_cmp_ge_i32 vcc, s94, v188\n" \
  "s_cbranch_vccz .LO_p1_x_%=\n" \
  "s_sleep 2\n" \
  "s_add_u32 s69, s69, 1\n" \
  "s_and_b32 s63, s69, 1023\n" \
  "s_cbranch_scc1 .LO_p1_%=\n" \
  "buffer_load_dwordx2 v[188:189], off, s[40:43], %19 offset:128 sc1\n" \
  "s_waitcnt vmcnt(0)\n" \
  "v_readfirstlane_b32 s63, v189\n" \
  "s_cmp_eq_u32 s63, %14\n" \
  "s_cbranch_scc1 .LO_p1_x_%=\n" \
  "s_cmp_le_u32 s69, 2097152\n" \
  "s_cbranch_scc1 .LO_p1_%=\n" \
  "v_mov_b32 v188, 1\n" \
  "v_mov_b32 v189, %14\n" \
  "buffer_store_dwordx2 v[188:189], off, s[40:43], %19 offset:128 sc1\n" \
  "s_waitcnt vmcnt(0)\n" \
  ".LO_p1_x_%=:\n" \
  "s_cmp_eq_u32 s54, 0\n" \
  "s_cbranch_scc1 .LO_pl_b_%=\n" \
  "s_mov_b32 s76, s54\n" \
  "s_mov_b32 s54, 0\n" \
  "s_add_u32 s77, s76, 4096\n" \
  "s_add_u32 s78, s76, 16384\n" \
  "s_add_u32 s79, s76, 20480\n" \
  "buffer_load_dwordx4 v[0:3], v190, s[40:43], s76 offen sc1\n" \
  "buffer_load_dwordx4 v[8:11], v190, s[40:43], s76 offen offset:2048 sc1\n" \
  "buffer_load_dwordx4 v[16:19], v190, s[40:43], s77 offen sc1\n" \
  "buffer_load_dwordx4 v[24:27], v190, s[40:43], s77 offen offset:2048 sc1\n" \
  "buffer_load_dwordx4 v[32:35], v190, s[40:43], s78 offen sc1\n" \
  "buffer_load_dwordx4 v[40:43], v190, s[40:43], s78 offen offset:2048 sc1\n" \
  "buffer_load_dwordx4 v[48:51], v190, s[40:43], s79 offen sc1\n" \
  "buffer_load_dwordx4 v[56:59], v190, s[40:43], s79 offen offset:2048 sc1\n" \
  "buffer_load_dwordx4 v[4:7], v190, s[40:43], s76 offen offset:1024 sc1\n" \
  "buffer_load_dwordx4 v[12:15], v190, s[40:43], s76 offen offset:3072 sc1\n" \
  "buffer_load_dwordx4 v[20:23], v190, s[40:43], s77 offen offset:1024 sc1\n" \
  "buffer_load_dwordx4 v[28:31], v190, s[40:43], s77 offen offset:3072 sc1\n" \
  "buffer_load_dwordx4 v[36:39], v190, s[40:43], s78 offen offset:1024 sc1\n" \
  "buffer_load_dwordx4 v[44:47], v190, s[40:43], s78 offen offset:3072 sc1\n" \
  "buffer_load_dwordx4 v[52:55], v190, s[40:43], s79 offen offset:1024 sc1\n" \
  "buffer_load_dwordx4 v[60:63], v190, s[40:43], s79 offen offset:3072 sc1\n" \
  "s_waitcnt vmcnt(8)\n" \
  "s_branch .LO_pl_b_%=\n" \
  ".LO_end_%=:\n"
#define O_ASM_VOPS "+{v190}"(oi.l16), "+{v191}"(oi.rw), "+{v192}"(oi.rr), "+{v193}"(oi.fo), "+{v194}"(oi.wv0), "+{v195}"(oi.wv1), "+{v196}"(oi.wv2), "+{v197}"(oi.tro), "+{v198}"(oi.l8), "+{v199}"(oi.rw0), "+{v200}"(oi.rr0)
#define O_ASM_SOPS "s"(oi.ws), "s"(oi.Wo), "s"(oi.w), "s"(oi.tag), "s"(oi.o_w), "s"(oi.o_o), "s"(oi.o_a), "s"(oi.f_a), "s"(oi.o_ctl), "s"(oi.ns)
#define O_ASM_CLOB "v0", "v1", "v2", "v3", "v4", "v5", "v6", "v7", "v8", "v9", "v10", "v11", "v12", "v13", "v14", "v15", "v16", "v17", "v18", "v19", "v20", "v21", "v22", "v23", "v24", "v25", "v26", "v27", "v28", "v29", "v30", "v31", "v32", "v33", "v34", "v35", "v36", "v37", "v38", "v39", "v40", "v41", "v42", "v43", "v44", "v45", "v46", "v47", "v48", "v49", "v50", "v51", "v52", "v53", "v54", "v55", "v56", "v57", "v58", "v59", "v60", "v61", "v62", "v63", "v64", "v65", "v66", "v67", "v68", "v69", "v70", "v71", "v72", "v73", "v74", "v75", "v76", "v77", "v78", "v79", "v80", "v81", "v82", "v83", "v84", "v85", "v86", "v87", "v88", "v89", "v90", "v91", "v92", "v93", "v94", "v95", "v96", "v97", "v98", "v99", "v100", "v101", "v102", "v103", "v104", "v105", "v106", "v107", "v108", "v109", "v110", "v111", "v112", "v113", "v114", "v115", "v116", "v117", "v118", "v119", "v120", "v121", "v122", "v123", "v124", "v125", "v126", "v127", "v128", "v129", "v130", "v131", "v132", "v133", "v134", "v135", "v136", "v137", "v138", "v139", "v140", "v141", "v142", "v143", "v144", "v145", "v146", "v147", "v148", "v149", "v150", "v151", "v152", "v153", "v154", "v155", "v156", "v157", "v158", "v159", "v160", "v161", "v162", "v163", "v164", "v165", "v166", "v167", "v168", "v169", "v170", "v171", "v172", "v173", "v174", "v175", "v176", "v177", "v178", "v179", "v180", "v181", "v182", "v183", "v184", "v185", "v186", "v187", "v188", "v189", "a0", "a1", "a2", "a3", "a4", "a5", "a6", "a7", "a8", "a9", "a10", "a11", "a12", "a13", "a14", "a15", "a16", "a17", "a18", "a19", "a20", "a21", "a22", "a23", "a24", "a25", "a26", "a27", "a28", "a29", "a30", "a31", "a32", "a33", "a34", "a35", "a36", "a37", "a38", "a39", "a40", "a41", "a42", "a43", "a44", "a45", "a46", "a47", "a48", "a49", "a50", "a51", "a52", "a53", "a54", "a55", "a56", "a57", "a58", "a59", "a60", "a61", "a62", "a63", "a64", "a65", "a66", "a67", "a68", "a69", "a70", "a71", "a72", "a73", "a74", "a75", "a76", "a77", "a78", "a79", "a80", "a81", "a82", "a83", "a84", "a85", "a86", "a87", "a88", "a89", "a90", "a91", "a92", "a93", "a94", "a95", "a96", "a97", "a98", "a99", "a100", "a101", "a102", "a103", "a104", "a105", "a106", "a107", "a108", "a109", "a110", "a111", "a112", "a113", "a114", "a115", "a116", "a117", "a118", "a119", "a120", "a121", "a122", "a123", "a124", "a125", "a126", "a127", "a128", "a129", "a130", "a131", "a132", "a133", "a134", "a135", "a136", "a137", "a138", "a139", "a140", "a141", "a142", "a143", "a144", "a145", "a146", "a147", "a148", "a149", "a150", "a151", "a152", "a153", "a154", "a155", "a156", "a157", "a158", "a159", "a160", "a161", "a162", "a163", "a164", "a165", "a166", "a167", "a168", "a169", "a170", "a171", "a172", "a173", "a174", "a175", "a176", "a177", "a178", "a179", "a180", "a181", "a182", "a183", "a184", "a185", "a186", "a187", "a188", "a189", "a190", "a191", "s40", "s41", "s42", "s43", "s44", "s45", "s46", "s47", "s48", "s49", "s50", "s51", "s52", "s53", "s54", "s55", "s56", "s57", "s58", "s59", "s60", "s61", "s62", "s63", "s64", "s65", "s66", "s67", "s68", "s69", "s70", "s71", "s72", "s73", "s74", "s75", "s76", "s77", "s78", "s79", "s80", "s81", "s82", "s83", "s84", "s85", "s86", "s87", "s88", "s89", "s90", "s91", "s92", "s93", "s94", "s95", "s96", "s97", "s98", "s99", "vcc", "scc", "memory"
#define F_ASM_TEXT \
  "s_mov_b64 s[40:41], %31\n" \
  "s_and_b32 s41, s41, 0xffff\n" \
  "s_mov_b32 s42, 255852544\n" \
  "s_mov_b32 s43, 0x00020000\n" \
  "s_mov_b64 s[44:45], %32\n" \
  "s_and_b32 s45, s45, 0xffff\n" \
  "s_mov_b32 s46, 2359296\n" \
  "s_mov_b32 s47, 0x00020000\n" \
  "s_mov_b32 s48, 0\n" \
  "s_mov_b32 s58, 0\n" \
  "s_mov_b32 s59, 0\n" \
  "s_mov_b32 s49, %37\n" \
  "s_mov_b32 s50, %38\n" \
  "s_mov_b32 s51, %36\n" \
  "s_mov_b32 s56, %39\n" \
  "s_mov_b32 s57, %40\n" \
  "s_mov_b32 s64, %44\n" \
  "v_not_b32 v197, %33\n" \
  "v_mov_b32 v62, -1\n" \
  ".LF_pre_%=:\n" \
  "v_readfirstlane_b32 s60, v197\n" \
  "s_cmp_eq_u32 s60, %33\n" \
  "s_cbranch_scc1 .LF_fu_x_%=\n" \
  ".LF_fu_%=:\n" \
  "buffer_load_dwordx2 v[196:197], off, s[40:43], s64 sc1\n" \
  "buffer_load_dwordx2 v[62:63], off, s[40:43], s50 offset:256 sc1\n" \
  "s_memrealtime s[76:77]\n" \
  "s_waitcnt vmcnt(0) lgkmcnt(0)\n" \
  "v_readfirstlane_b32 s60, v197\n" \
  "s_cmp_eq_u32 s60, %33\n" \
  "s_cbranch_scc1 .LF_fu_x_%=\n" \
  "v_readfirstlane_b32 s60, v62\n" \
  "s_sub_u32 s60, s60, 58\n" \
  "s_sub_u32 s60, s60, s48\n" \
  "s_cmp_lt_u32 s48, s58\n" \
  "s_cselect_b32 s60, s60, -1\n" \
  "v_readfirstlane_b32 s61, v63\n" \
  "s_sub_u32 s61, s61, s76\n" \
  "s_cmp_lt_i32 s61, -60\n" \
  "s_cselect_b32 s60, s60, -1\n" \
  "s_cmp_lt_u32 s60, 8\n" \
  "s_cbranch_scc1 .LF_loop_%=\n" \
  "s_sleep 6\n" \
  "s_add_u32 s59, s59, 1\n" \
  "s_cmp_lt_u32 s59, 131072\n" \
  "s_cbranch_scc1 .LF_fu_%=\n" \
  "v_mov_b32 v32, 1\n" \
  "v_mov_b32 v33, %33\n" \
  "buffer_store_dwordx2 v[32:33], off, s[40:43], %41 offset:128 sc1\n" \
  "s_waitcnt vmcnt(0)\n" \
  ".LF_fu_x_%=:\n" \
  "v_readfirstlane_b32 s60, v212\n" \
  "s_cmp_eq_u32 s60, 2130706432\n" \
  "s_cbranch_scc1 .LF_fl_%=\n" \
  "s_add_u32 s61, s58, 58\n" \
  "s_and_b32 s61, s61, 15\n" \
  "s_lshl_b32 s61, s61, 16\n" \
  "s_xor_b32 s61, s61, %34\n" \
  "s_lshl_b32 s62, s58, 17\n" \
  "s_add_u32 s62, s62, %35\n" \
  "s_xor_b32 s60, s61, 0\n" \
  "buffer_load_dwordx4 v[32:35], v202, s[40:43], s60 offen sc1\n" \
  "s_sleep 1\n" \
  "buffer_load_dwordx4 v[64:67], v203, s[40:43], s62 offen sc1\n" \
  "s_sleep 1\n" \
  "buffer_load_dwordx4 v[68:71], v203, s[40:43], s62 offen offset:1024 sc1\n" \
  "s_sleep 1\n" \
  "s_xor_b32 s60, s61, 1024\n" \
  "buffer_load_dwordx4 v[36:39], v202, s[40:43], s60 offen sc1\n" \
  "s_sleep 1\n" \
  "buffer_load_dwordx4 v[72:75], v203, s[40:43], s62 offen offset:2048 sc1\n" \
  "s_sleep 1\n" \
  "buffer_load_dwordx4 v[76:79], v203, s[40:43], s62 offen offset:3072 sc1\n" \
  "s_sleep 1\n" \
  "s_xor_b32 s60, s61, 2048\n" \
  "buffer_load_dwordx4 v[40:43], v202, s[40:43], s60 offen sc1\n" \
  "s_sleep 1\n" \
  "buffer_load_dwordx4 v[80:83], v204, s[40:43], s62 offen sc1\n" \
  "s_sleep 1\n" \
  "buffer_load_dwordx4 v[84:87], v204, s[40:43], s62 offen offset:1024 sc1\n" \
  "s_sleep 1\n" \
  "s_xor_b32 s60, s61, 3072\n" \
  "buffer_load_dwordx4 v[44:47], v202, s[40:43], s60 offen sc1\n" \
  "s_sleep 1\n" \
  "buffer_load_dwordx4 v[88:91], v204, s[40:43], s62 offen offset:2048 sc1\n" \
  "s_sleep 1\n" \
  "buffer_load_dwordx4 v[92:95], v204, s[40:43], s62 offen offset:3072 sc1\n" \
  "s_sleep 1\n" \
  "s_xor_b32 s60, s61, 4096\n" \
  "buffer_load_dwordx4 v[48:51], v202, s[40:43], s60 offen sc1\n" \
  "s_sleep 1\n" \
  "buffer_load_dwordx4 v[96:99], v205, s[40:43], s62 offen sc1\n" \
  "s_sleep 1\n" \
  "buffer_load_dwordx4 v[100:103], v205, s[40:43], s62 offen offset:1024 sc1\n" \
  "s_sleep 1\n" \
  "s_xor_b32 s60, s61, 5120\n" \
  "buffer_load_dwordx4 v[52:55], v202, s[40:43], s60 offen sc1\n" \
  "s_sleep 1\n" \
  "buffer_load_dwordx4 v[104:107], v205, s[40:43], s62 offen offset:2048 sc1\n" \
  "s_sleep 1\n" \
  "buffer_load_dwordx4 v[108:111], v205, s[40:43], s62 offen offset:3072 sc1\n" \
  "s_sleep 1\n" \
  "s_xor_b32 s60, s61, 6144\n" \
  "buffer_load_dwordx4 v[56:59], v202, s[40:43], s60 offen sc1\n" \
  "s_sleep 1\n" \
  "buffer_load_dwordx4 v[112:115], v206, s[40:43], s62 offen sc1\n" \
  "s_sleep 1\n" \
  "buffer_load_dwordx4 v[116:119], v206, s[40:43], s62 offen offset:1024 sc1\n" \
  "s_sleep 1\n" \
  "s_xor_b32 s60, s61, 7168\n" \
  "buffer_load_dwordx4 v[60:63], v202, s[40:43], s60 offen sc1\n" \
  "s_sleep 1\n" \
  "buffer_load_dwordx4 v[120:123], v206, s[40:43], s62 offen offset:2048 sc1\n" \
  "s_sleep 1\n" \
  "buffer_load_dwordx4 v[124:127], v206, s[40:43], s62 offen offset:3072 sc1\n" \
  "s_sleep 1\n" \
  "s_add_u32 s64, s64, 128\n" \
  ".LF_fl_b_%=:\n" \
  "v_mov_b64 v[128:129], 0\n" \
  "v_mov_b64 v[130:131], 0\n" \
  "v_mov_b64 v[132:133], 0\n" \
  "v_mov_b64 v[134:135], 0\n" \
  "v_mov_b64 v[136:137], 0\n" \
  "v_mov_b64 v[138:139], 0\n" \
  "v_mov_b64 v[140:141], 0\n" \
  "v_mov_b64 v[142:143], 0\n" \
  "v_mov_b64 v[144:145], 0\n" \
  "v_mov_b64 v[146:147], 0\n" \
  "v_mov_b64 v[148:149], 0\n" \
  "v_mov_b64 v[150:151], 0\n" \
  "v_mov_b64 v[152:153], 0\n" \
  "v_mov_b64 v[154:155], 0\n" \
  "v_mov_b64 v[156:157], 0\n" \
  "v_mov_b64 v[158:159], 0\n" \
  "v_mov_b64 v[160:161], 0\n" \
  "v_mov_b64 v[162:163], 0\n" \
  "v_mov_b64 v[164:165], 0\n" \
  "v_mov_b64 v[166:167], 0\n" \
  "v_mov_b64 v[168:169], 0\n" \
  "v_mov_b64 v[170:171], 0\n" \
  "v_mov_b64 v[172:173], 0\n" \
  "v_mov_b64 v[174:175], 0\n" \
  "v_mov_b64 v[176:177], 0\n" \
  "v_mov_b64 v[178:179], 0\n" \
  "v_mov_b64 v[180:181], 0\n" \
  "v_mov_b64 v[182:183], 0\n" \
  "v_mov_b64 v[184:185], 0\n" \
  "v_mov_b64 v[186:187], 0\n" \
  "v_mov_b64 v[188:189], 0\n" \
  "v_mov_b64 v[190:191], 0\n" \
  "s_waitcnt vmcnt(0)\n" \
  "v_mov_b32 v0, v32\n" \
  "v_mov_b32 v1, v36\n" \
  "v_mov_b32 v2, v33\n" \
  "v_mov_b32 v3, v37\n" \
  "v_mov_b32 v4, v34\n" \
  "v_mov_b32 v5, v38\n" \
  "v_mov_b32 v6, v35\n" \
  "v_mov_b32 v7, v39\n" \
  "v_mov_b32 v8, v40\n" \
  "v_mov_b32 v9, v44\n" \
  "v_mov_b32 v10, v41\n" \
  "v_mov_b32 v11, v45\n" \
  "v_mov_b32 v12, v42\n" \
  "v_mov_b32 v13, v46\n" \
  "v_mov_b32 v14, v43\n" \
  "v_mov_b32 v15, v47\n" \
  "v_mov_b32 v16, v48\n" \
  "v_mov_b32 v17, v52\n" \
  "v_mov_b32 v18, v49\n" \
  "v_mov_b32 v19, v53\n" \
  "v_mov_b32 v20, v50\n" \
  "v_mov_b32 v21, v54\n" \
  "v_mov_b32 v22, v51\n" \
  "v_mov_b32 v23, v55\n" \
  "v_mov_b32 v24, v56\n" \
  "v_mov_b32 v25, v60\n" \
  "v_mov_b32 v26, v57\n" \
  "v_mov_b32 v27, v61\n" \
  "v_mov_b32 v28, v58\n" \
  "v_mov_b32 v29, v62\n" \
  "v_mov_b32 v30, v59\n" \
  "v_mov_b32 v31, v63\n" \
  "s_nop 1\n" \
  "v_smfmac_f32_16x16x64_bf16 v[128:131], v[64:67], v[0:7], v200\n" \
  "v_smfmac_f32_16x16x64_bf16 v[132:135], v[68:71], v[8:15], v200\n" \
  "v_smfmac_f32_16x16x64_bf16 v[136:139], v[72:75], v[16:23], v200\n" \
  "v_smfmac_f32_16x16x64_bf16 v[140:143], v[76:79], v[24:31], v200\n" \
  "v_smfmac_f32_16x16x64_bf16 v[144:147], v[80:83], v[0:7], v200\n" \
  "v_smfmac_f32_16x16x64_bf16 v[148:151], v[84:87], v[8:15], v200\n" \
  "v_smfmac_f32_16x16x64_bf16 v[152:155], v[88:91], v[16:23], v200\n" \
  "v_smfmac_f32_16x16x64_bf16 v[156:159], v[92:95], v[24:31], v200\n" \
  "v_smfmac_f32_16x16x64_bf16 v[160:163], v[96:99], v[0:7], v200\n" \
  "v_smfmac_f32_16x16x64_bf16 v[164:167], v[100:103], v[8:15], v200\n" \
  "v_smfmac_f32_16x16x64_bf16 v[168:171], v[104:107], v[16:23], v200\n" \
  "v_smfmac_f32_16x16x64_bf16 v[172:175], v[108:111], v[24:31], v200\n" \
  "v_smfmac_f32_16x16x64_bf16 v[176:179], v[112:115], v[0:7], v200\n" \
  "v_smfmac_f32_16x16x64_bf16 v[180:183], v[116:119], v[8:15], v200\n" \
  "v_smfmac_f32_16x16x64_bf16 v[184:187], v[120:123], v[16:23], v200\n" \
  "v_smfmac_f32_16x16x64_bf16 v[188:191], v[124:127], v[24:31], v200\n" \
  "s_add_u32 s58, s58, 1\n" \
  "buffer_load_dwordx2 v[62:63], off, s[40:43], s50 offset:256 sc1\n" \
  "buffer_load_dwordx2 v[196:197], off, s[40:43], s64 sc1\n" \
  "s_cmp_eq_u32 s58, 6\n" \
  "s_cbranch_scc1 .LF_ne_%=\n" \
  ".LF_ne_%=:\n" \
  "s_nop 7\n" \
  "v_cvt_pk_bf16_f32 v128, v128, v129\n" \
  "v_cvt_pk_bf16_f32 v129, v130, v131\n" \
  "v_cvt_pk_bf16_f32 v130, v132, v133\n" \
  "v_cvt_pk_bf16_f32 v131, v134, v135\n" \
  "v_cvt_pk_bf16_f32 v132, v136, v137\n" \
  "v_cvt_pk_bf16_f32 v133, v138, v139\n" \
  "v_cvt_pk_bf16_f32 v134, v140, v141\n" \
  "v_cvt_pk_bf16_f32 v135, v142, v143\n" \
  "v_cvt_pk_bf16_f32 v136, v144, v145\n" \
  "v_cvt_pk_bf16_f32 v137, v146, v147\n" \
  "v_cvt_pk_bf16_f32 v138, v148, v149\n" \
  "v_cvt_pk_bf16_f32 v139, v150, v151\n" \
  "v_cvt_pk_bf16_f32 v140, v152, v153\n" \
  "v_cvt_pk_bf16_f32 v141, v154, v155\n" \
  "v_cvt_pk_bf16_f32 v142, v156, v157\n" \
  "v_cvt_pk_bf16_f32 v143, v158, v159\n" \
  "v_cvt_pk_bf16_f32 v144, v160, v161\n" \
  "v_cvt_pk_bf16_f32 v145, v162, v163\n" \
  "v_cvt_pk_bf16_f32 v146, v164, v165\n" \
  "v_cvt_pk_bf16_f32 v147, v166, v167\n" \
  "v_cvt_pk_bf16_f32 v148, v168, v169\n" \
  "v_cvt_pk_bf16_f32 v149, v170, v171\n" \
  "v_cvt_pk_bf16_f32 v150, v172, v173\n" \
  "v_cvt_pk_bf16_f32 v151, v174, v175\n" \
  "v_cvt_pk_bf16_f32 v152, v176, v177\n" \
  "v_cvt_pk_bf16_f32 v153, v178, v179\n" \
  "v_cvt_pk_bf16_f32 v154, v180, v181\n" \
  "v_cvt_pk_bf16_f32 v155, v182, v183\n" \
  "v_cvt_pk_bf16_f32 v156, v184, v185\n" \
  "v_cvt_pk_bf16_f32 v157, v186, v187\n" \
  "v_cvt_pk_bf16_f32 v158, v188, v189\n" \
  "v_cvt_pk_bf16_f32 v159, v190, v191\n" \
  "s_cmp_eq_u32 s58, 1\n" \
  "s_cbranch_scc1 .LF_w1_%=\n" \
  "s_cmp_eq_u32 s58, 2\n" \
  "s_cbranch_scc1 .LF_w2_%=\n" \
  "s_cmp_eq_u32 s58, 3\n" \
  "s_cbranch_scc1 .LF_w3_%=\n" \
  "s_cmp_eq_u32 s58, 4\n" \
  "s_cbranch_scc1 .LF_w4_%=\n" \
  "s_cmp_eq_u32 s58, 5\n" \
  "s_cbranch_scc1 .LF_w5_%=\n" \
  "v_accvgpr_write_b32 a160, v128\n" \
  "v_accvgpr_write_b32 a161, v129\n" \
  "v_accvgpr_write_b32 a162, v130\n" \
  "v_accvgpr_write_b32 a163, v131\n" \
  "v_accvgpr_write_b32 a164, v132\n" \
  "v_accvgpr_write_b32 a165, v133\n" \
  "v_accvgpr_write_b32 a166, v134\n" \
  "v_accvgpr_write_b32 a167, v135\n" \
  "v_accvgpr_write_b32 a168, v136\n" \
  "v_accvgpr_write_b32 a169, v137\n" \
  "v_accvgpr_write_b32 a170, v138\n" \
  "v_accvgpr_write_b32 a171, v139\n" \
  "v_accvgpr_write_b32 a172, v140\n" \
  "v_accvgpr_write_b32 a173, v141\n" \
  "v_accvgpr_write_b32 a174, v142\n" \
  "v_accvgpr_write_b32 a175, v143\n" \
  "v_accvgpr_write_b32 a176, v144\n" \
  "v_accvgpr_write_b32 a177, v145\n" \
  "v_accvgpr_write_b32 a178, v146\n" \
  "v_accvgpr_write_b32 a179, v147\n" \
  "v_accvgpr_write_b32 a180, v148\n" \
  "v_accvgpr_write_b32 a181, v149\n" \
  "v_accvgpr_write_b32 a182, v150\n" \
  "v_accvgpr_write_b32 a183, v151\n" \
  "v_accvgpr_write_b32 a184, v152\n" \
  "v_accvgpr_write_b32 a185, v153\n" \
  "v_accvgpr_write_b32 a186, v154\n" \
  "v_accvgpr_write_b32 a187, v155\n" \
  "v_accvgpr_write_b32 a188, v156\n" \
  "v_accvgpr_write_b32 a189, v157\n" \
  "v_accvgpr_write_b32 a190, v158\n" \
  "v_accvgpr_write_b32 a191, v159\n" \
  ".LF_w_b_%=:\n" \
  "s_cmp_lt_u32 s58, 6\n" \
  "s_waitcnt vmcnt(0)\n" \
  "s_cbranch_scc1 .LF_pre_%=\n" \
  "s_waitcnt vmcnt(0)\n" \
  ".LF_loop_%=:\n" \
  "v_mov_b64 v[40:41], 0\n" \
  "v_mov_b64 v[42:43], 0\n" \
  "v_mov_b64 v[44:45], 0\n" \
  "v_mov_b64 v[46:47], 0\n" \
  "v_mov_b64 v[48:49], 0\n" \
  "v_mov_b64 v[50:51], 0\n" \
  "v_mov_b64 v[52:53], 0\n" \
  "v_mov_b64 v[54:55], 0\n" \
  "s_cmp_lg_u32 %43, 0\n" \
  "s_cbranch_scc1 .LF_go_%=\n" \
  "s_mov_b32 s69, 0\n" \
  "s_add_u32 s53, s48, 58\n" \
  "s_cmp_eq_u32 s48, 5\n" \
  "s_cselect_b32 s54, 16, 0\n" \
  "s_lshr_b32 s54, %42, s54\n" \
  "s_and_b32 s54, s54, 0x7fff\n" \
  "s_branch .LF_tw1_%=\n" \
  ".LF_tw_%=:\n" \
  "buffer_load_dwordx2 v[62:63], off, s[40:43], s50 offset:256 sc1\n" \
  "s_waitcnt vmcnt(0)\n" \
  ".LF_tw1_%=:\n" \
  "s_memrealtime s[76:77]\n" \
  "v_readfirstlane_b32 s62, v62\n" \
  "v_readfirstlane_b32 s61, v63\n" \
  "s_add_u32 s55, s61, s54\n" \
  "s_waitcnt lgkmcnt(0)\n" \
  "s_sub_u32 s60, s55, s76\n" \
  "s_cmp_gt_i32 s62, s53\n" \
  "s_cbranch_scc1 .LF_go_%=\n" \
  "s_cmp_eq_u32 s62, s53\n" \
  "s_cselect_b32 s60, s60, 999\n" \
  "s_cmp_lt_i32 s60, 400\n" \
  "s_cbranch_scc1 .LF_clk_%=\n" \
  "s_sleep 8\n" \
  "s_add_u32 s69, s69, 1\n" \
  "s_cmp_lt_u32 s69, 20000\n" \
  "s_cbranch_scc1 .LF_tw_%=\n" \
  "s_branch .LF_gq_%=\n" \
  ".LF_clk_%=:\n" \
  "s_cmp_le_i32 s60, 3\n" \
  "s_cbranch_scc1 .LF_gq_%=\n" \
  "s_sleep 1\n" \
  "s_cmp_le_i32 s60, 30\n" \
  "s_cbranch_scc1 .LF_cl2_%=\n" \
  "s_sleep 6\n" \
  ".LF_cl2_%=:\n" \
  "s_memrealtime s[76:77]\n" \
  "s_waitcnt lgkmcnt(0)\n" \
  "s_sub_u32 s60, s55, s76\n" \
  "s_branch .LF_clk_%=\n" \
  ".LF_gq_%=:\n" \
  "s_cmp_eq_u32 s48, 5\n" \
  "s_cbranch_scc1 .LF_go_%=\n" \
  "s_cmp_lt_i32 s60, -100\n" \
  "s_cbranch_scc1 .LF_go_%=\n" \
  "buffer_load_dword v60, v212, s[40:43], 0 offen sc1\n" \
  "s_and_b32 s61, %45, 255\n" \
  "s_waitcnt vmcnt(0)\n" \
  "s_memrealtime s[76:77]\n" \
  "v_readlane_b32 s80, v60, 0\n" \
  "v_readlane_b32 s81, v60, 1\n" \
  "v_readlane_b32 s82, v60, 2\n" \
  "v_readlane_b32 s83, v60, 3\n" \
  "s_waitcnt lgkmcnt(0)\n" \
  "s_sub_u32 s61, s61, s76\n" \
  "s_add_u32 s80, s80, s61\n" \
  "s_add_u32 s81, s81, s61\n" \
  "s_add_u32 s82, s82, s61\n" \
  "s_add_u32 s83, s83, s61\n" \
  "s_min_u32 s80, s80, s81\n" \
  "s_min_u32 s82, s82, s83\n" \
  "s_min_u32 s80, s80, s82\n" \
  "s_lshr_b32 s61, %45, 8\n" \
  "s_cmp_gt_u32 s80, 300\n" \
  "s_cselect_b32 s80, 0, s80\n" \
  "s_sub_u32 s80, s80, s61\n" \
  "s_add_u32 s81, s76, s80\n" \
  ".LF_qf_%=:\n" \
  "s_cmp_le_i32 s80, 0\n" \
  "s_cbranch_scc1 .LF_qf_x_%=\n" \
  "s_sleep 2\n" \
  "s_memrealtime s[76:77]\n" \
  "s_waitcnt lgkmcnt(0)\n" \
  "s_sub_u32 s80, s81, s76\n" \
  "s_branch .LF_qf_%=\n" \
  ".LF_qf_x_%=:\n" \
  ".LF_go_%=:\n" \
  "s_barrier\n" \
  "s_mov_b32 s69, 0\n" \
  "s_lshr_b32 s52, s49, 8\n" \
  "s_and_b32 s52, s52, 8192\n" \
  "s_xor_b32 s52, s52, s49\n" \
  "s_xor_b32 s60, s50, s56\n" \
  "buffer_load_dwordx2 v[62:63], off, s[40:43], s60 offset:256 sc1\n" \
  ".LF_rd_%=:\n" \
  "buffer_load_dwordx4 v[0:3], v202, s[40:43], s52 offen sc1\n" \
  "buffer_load_dwordx4 v[4:7], v202, s[40:43], s52 offen offset:1024 sc1\n" \
  "s_mov_b32 s60, s51\n" \
  "buffer_load_dwordx4 v[8:11], v207, s[40:43], s60 offen sc1\n" \
  "s_add_u32 s60, s60, 4096\n" \
  "buffer_load_dwordx4 v[24:27], v207, s[40:43], s60 offen sc1\n" \
  "s_add_u32 s60, s60, 12288\n" \
  "buffer_load_dwordx4 v[12:15], v207, s[40:43], s60 offen sc1\n" \
  "s_add_u32 s60, s60, 4096\n" \
  "buffer_load_dwordx4 v[28:31], v207, s[40:43], s60 offen sc1\n" \
  "s_add_u32 s60, s60, 12288\n" \
  "buffer_load_dwordx4 v[16:19], v207, s[40:43], s60 offen sc1\n" \
  "s_add_u32 s60, s60, 4096\n" \
  "buffer_load_dwordx4 v[32:35], v207, s[40:43], s60 offen sc1\n" \
  "s_add_u32 s60, s60, 12288\n" \
  "buffer_load_dwordx4 v[20:23], v207, s[40:43], s60 offen sc1\n" \
  "s_add_u32 s60, s60, 4096\n" \
  "buffer_load_dwordx4 v[36:39], v207, s[40:43], s60 offen sc1\n" \
  "s_cmp_eq_u32 s48, 1\n" \
  "s_cbranch_scc1 .LF_r1_%=\n" \
  "s_cmp_eq_u32 s48, 2\n" \
  "s_cbranch_scc1 .LF_r2_%=\n" \
  "s_cmp_eq_u32 s48, 3\n" \
  "s_cbranch_scc1 .LF_r3_%=\n" \
  "s_cmp_eq_u32 s48, 4\n" \
  "s_cbranch_scc1 .LF_r4_%=\n" \
  "s_cmp_eq_u32 s48, 5\n" \
  "s_cbranch_scc1 .LF_r5_%=\n" \
  "v_mov_b32 v192, v213\n" \
  "v_mov_b32 v193, v214\n" \
  "v_mov_b32 v194, v215\n" \
  "v_accvgpr_read_b32 v128, a0\n" \
  "v_accvgpr_read_b32 v129, a1\n" \
  "v_accvgpr_read_b32 v130, a2\n" \
  "v_accvgpr_read_b32 v131, a3\n" \
  "v_accvgpr_read_b32 v132, a4\n" \
  "v_accvgpr_read_b32 v133, a5\n" \
  "v_accvgpr_read_b32 v134, a6\n" \
  "v_accvgpr_read_b32 v135, a7\n" \
  "v_accvgpr_read_b32 v136, a8\n" \
  "v_accvgpr_read_b32 v137, a9\n" \
  "v_accvgpr_read_b32 v138, a10\n" \
  "v_accvgpr_read_b32 v139, a11\n" \
  "v_accvgpr_read_b32 v140, a12\n" \
  "v_accvgpr_read_b32 v141, a13\n" \
  "v_accvgpr_read_b32 v142, a14\n" \
  "v_accvgpr_read_b32 v143, a15\n" \
  "v_accvgpr_read_b32 v144, a16\n" \
  "v_accvgpr_read_b32 v145, a17\n" \
  "v_accvgpr_read_b32 v146, a18\n" \
  "v_accvgpr_read_b32 v147, a19\n" \
  "v_accvgpr_read_b32 v148, a20\n" \
  "v_accvgpr_read_b32 v149, a21\n" \
  "v_accvgpr_read_b32 v150, a22\n" \
  "v_accvgpr_read_b32 v151, a23\n" \
  "v_accvgpr_read_b32 v152, a24\n" \
  "v_accvgpr_read_b32 v153, a25\n" \
  "v_accvgpr_read_b32 v154, a26\n" \
  "v_accvgpr_read_b32 v155, a27\n" \
  "v_accvgpr_read_b32 v156, a28\n" \
  "v_accvgpr_read_b32 v157, a29\n" \
  "v_accvgpr_read_b32 v158, a30\n" \
  "v_accvgpr_read_b32 v159, a31\n" \
  ".LF_r_b_%=:\n" \
  "s_waitcnt vmcnt(8)\n" \
  "v_permlane16_swap_b32 v0, v1\n" \
  "v_permlane16_swap_b32 v2, v3\n" \
  "v_permlane16_swap_b32 v4, v5\n" \
  "v_permlane16_swap_b32 v6, v7\n" \
  "s_mov_b64 s[78:79], 0\n" \
  "s_waitcnt vmcnt(7)\n" \
  "v_smfmac_f32_16x16x64_bf16 v[40:43], v[8:11], v[0:7], v200\n" \
  "s_waitcnt vmcnt(6)\n" \
  "v_smfmac_f32_16x16x64_bf16 v[40:43], v[24:27], v[128:135], v201\n" \
  "s_waitcnt vmcnt(5)\n" \
  "v_smfmac_f32_16x16x64_bf16 v[44:47], v[12:15], v[0:7], v200\n" \
  "s_waitcnt vmcnt(4)\n" \
  "v_smfmac_f32_16x16x64_bf16 v[44:47], v[28:31], v[136:143], v201\n" \
  "s_waitcnt vmcnt(3)\n" \
  "v_smfmac_f32_16x16x64_bf16 v[48:51], v[16:19], v[0:7], v200\n" \
  "s_waitcnt vmcnt(2)\n" \
  "v_smfmac_f32_16x16x64_bf16 v[48:51], v[32:35], v[144:151], v201\n" \
  "s_waitcnt vmcnt(1)\n" \
  "v_smfmac_f32_16x16x64_bf16 v[52:55], v[20:23], v[0:7], v200\n" \
  "s_waitcnt vmcnt(0)\n" \
  "v_smfmac_f32_16x16x64_bf16 v[52:55], v[36:39], v[152:159], v201\n" \
  "s_nop 1\n" \
  "v_permlane32_swap_b32 v40, v42\n" \
  "v_permlane32_swap_b32 v41, v43\n" \
  "v_add_f32 v40, v40, v42\n" \
  "v_add_f32 v41, v41, v43\n" \
  "v_cmp_u_f32 vcc, v40, v41\n" \
  "ds_write2st64_b32 v208, v40, v41 offset0:0 offset1:4\n" \
  "s_or_b64 s[78:79], s[78:79], vcc\n" \
  "v_permlane32_swap_b32 v44, v46\n" \
  "v_permlane32_swap_b32 v45, v47\n" \
  "v_add_f32 v44, v44, v46\n" \
  "v_add_f32 v45, v45, v47\n" \
  "v_cmp_u_f32 vcc, v44, v45\n" \
  "ds_write2st64_b32 v208, v44, v45 offset0:8 offset1:12\n" \
  "s_or_b64 s[78:79], s[78:79], vcc\n" \
  "v_permlane32_swap_b32 v48, v50\n" \
  "v_permlane32_swap_b32 v49, v51\n" \
  "v_add_f32 v48, v48, v50\n" \
  "v_add_f32 v49, v49, v51\n" \
  "v_cmp_u_f32 vcc, v48, v49\n" \
  "ds_write2st64_b32 v208, v48, v49 offset0:16 offset1:20\n" \
  "s_or_b64 s[78:79], s[78:79], vcc\n" \
  "v_permlane32_swap_b32 v52, v54\n" \
  "v_permlane32_swap_b32 v53, v55\n" \
  "v_add_f32 v52, v52, v54\n" \
  "v_add_f32 v53, v53, v55\n" \
  "v_cmp_u_f32 vcc, v52, v53\n" \
  "ds_write2st64_b32 v208, v52, v53 offset0:24 offset1:28\n" \
  "s_or_b64 s[78:79], s[78:79], vcc\n" \
  "s_cbranch_scc1 .LF_bad_%=\n" \
  ".LF_ok_%=:\n" \
  "s_waitcnt lgkmcnt(0)\n" \
  "s_barrier\n" \
  "ds_read_b128 v[40:43], v209\n" \
  "ds_read_b128 v[44:47], v209 offset:512\n" \
  "s_waitcnt lgkmcnt(1)\n" \
  "v_add_f32 v40, v40, v42\n" \
  "v_add_f32 v41, v41, v43\n" \
  "s_waitcnt lgkmcnt(0)\n" \
  "v_add_f32 v44, v44, v46\n" \
  "v_add_f32 v45, v45, v47\n" \
  "v_add_f32 v40, v40, v44\n" \
  "v_add_f32 v41, v41, v45\n" \
  "v_cvt_pk_bf16_f32 v40, v40, v41\n" \
  "v_and_b32 v41, 0xffff0000, v40\n" \
  "v_lshlrev_b32 v40, 16, v40\n" \
  "v_mul_f32 v40, v192, v40\n" \
  "v_mul_f32 v41, v192, v41\n" \
  "v_cvt_pk_bf16_f32 v40, v40, v41\n" \
  "v_and_b32 v41, 0xffff0000, v40\n" \
  "v_lshlrev_b32 v40, 16, v40\n" \
  "v_add_f32 v40, v40, v193\n" \
  "v_add_f32 v41, v41, v194\n" \
  "v_cvt_pk_bf16_f32 v237, v40, v41\n" \
  "s_cmp_eq_u32 s48, 5\n" \
  "s_cbranch_scc1 .LF_out_%=\n" \
  "v_mov_b32 v232, v233\n" \
  "v_mov_b32 v233, v234\n" \
  "v_mov_b32 v234, v235\n" \
  "v_mov_b32 v235, v236\n" \
  "v_mov_b32 v236, v237\n" \
  "v_xor_b32 v208, 8192, v208\n" \
  "v_xor_b32 v209, 8192, v209\n" \
  "s_add_u32 s49, s49, 1048576\n" \
  "s_xor_b32 s50, s50, s56\n" \
  "s_mov_b32 s60, s56\n" \
  "s_mov_b32 s56, s57\n" \
  "s_mov_b32 s57, s60\n" \
  "s_add_u32 s51, s51, 65536\n" \
  "s_add_u32 s48, s48, 1\n" \
  "s_cmp_lt_u32 s58, 6\n" \
  "s_cbranch_scc1 .LF_fu_%=\n" \
  "s_branch .LF_loop_%=\n" \
  ".LF_out_%=:\n" \
  "s_mov_b32 s60, 184320\n" \
  "buffer_store_dword v237, v210, s[44:47], s60 offen\n" \
  "s_mov_b32 s60, 147456\n" \
  "buffer_store_dword v236, v210, s[44:47], s60 offen\n" \
  "s_mov_b32 s60, 110592\n" \
  "buffer_store_dword v235, v210, s[44:47], s60 offen\n" \
  "s_mov_b32 s60, 73728\n" \
  "buffer_store_dword v234, v210, s[44:47], s60 offen\n" \
  "s_mov_b32 s60, 36864\n" \
  "buffer_store_dword v233, v210, s[44:47], s60 offen\n" \
  "s_mov_b32 s60, 0\n" \
  "buffer_store_dword v232, v210, s[44:47], s60 offen\n" \
  "s_branch .LF_end_%=\n" \
  ".LF_fl_%=:\n" \
  "s_add_u32 s61, s58, 58\n" \
  "s_and_b32 s61, s61, 15\n" \
  "s_lshl_b32 s61, s61, 16\n" \
  "s_xor_b32 s61, s61, %34\n" \
  "s_lshl_b32 s62, s58, 17\n" \
  "s_add_u32 s62, s62, %35\n" \
  "s_xor_b32 s60, s61, 0\n" \
  "buffer_load_dwordx4 v[32:35], v202, s[40:43], s60 offen sc1\n" \
  "buffer_load_dwordx4 v[64:67], v203, s[40:43], s62 offen sc1\n" \
  "buffer_load_dwordx4 v[68:71], v203, s[40:43], s62 offen offset:1024 sc1\n" \
  "s_xor_b32 s60, s61, 1024\n" \
  "buffer_load_dwordx4 v[36:39], v202, s[40:43], s60 offen sc1\n" \
  "buffer_load_dwordx4 v[72:75], v203, s[40:43], s62 offen offset:2048 sc1\n" \
  "buffer_load_dwordx4 v[76:79], v203, s[40:43], s62 offen offset:3072 sc1\n" \
  "s_xor_b32 s60, s61, 2048\n" \
  "buffer_load_dwordx4 v[40:43], v202, s[40:43], s60 offen sc1\n" \
  "buffer_load_dwordx4 v[80:83], v204, s[40:43], s62 offen sc1\n" \
  "buffer_load_dwordx4 v[84:87], v204, s[40:43], s62 offen offset:1024 sc1\n" \
  "s_xor_b32 s60, s61, 3072\n" \
  "buffer_load_dwordx4 v[44:47], v202, s[40:43], s60 offen sc1\n" \
  "buffer_load_dwordx4 v[88:91], v204, s[40:43], s62 offen offset:2048 sc1\n" \
  "buffer_load_dwordx4 v[92:95], v204, s[40:43], s62 offen offset:3072 sc1\n" \
  "s_xor_b32 s60, s61, 4096\n" \
  "buffer_load_dwordx4 v[48:51], v202, s[40:43], s60 offen sc1\n" \
  "buffer_load_dwordx4 v[96:99], v205, s[40:43], s62 offen sc1\n" \
  "buffer_load_dwordx4 v[100:103], v205, s[40:43], s62 offen offset:1024 sc1\n" \
  "s_xor_b32 s60, s61, 5120\n" \
  "buffer_load_dwordx4 v[52:55], v202, s[40:43], s60 offen sc1\n" \
  "buffer_load_dwordx4 v[104:107], v205, s[40:43], s62 offen offset:2048 sc1\n" \
  "buffer_load_dwordx4 v[108:111], v205, s[40:43], s62 offen offset:3072 sc1\n" \
  "s_xor_b32 s60, s61, 6144\n" \
  "buffer_load_dwordx4 v[56:59], v202, s[40:43], s60 offen sc1\n" \
  "buffer_load_dwordx4 v[112:115], v206, s[40:43], s62 offen sc1\n" \
  "buffer_load_dwordx4 v[116:119], v206, s[40:43], s62 offen offset:1024 sc1\n" \
  "s_xor_b32 s60, s61, 7168\n" \
  "buffer_load_dwordx4 v[60:63], v202, s[40:43], s60 offen sc1\n" \
  "buffer_load_dwordx4 v[120:123], v206, s[40:43], s62 offen offset:2048 sc1\n" \
  "buffer_load_dwordx4 v[124:127], v206, s[40:43], s62 offen offset:3072 sc1\n" \
  "s_add_u32 s64, s64, 128\n" \
  "s_branch .LF_fl_b_%=\n" \
  ".LF_w1_%=:\n" \
  "v_accvgpr_write_b32 a0, v128\n" \
  "v_accvgpr_write_b32 a1, v129\n" \
  "v_accvgpr_write_b32 a2, v130\n" \
  "v_accvgpr_write_b32 a3, v131\n" \
  "v_accvgpr_write_b32 a4, v132\n" \
  "v_accvgpr_write_b32 a5, v133\n" \
  "v_accvgpr_write_b32 a6, v134\n" \
  "v_accvgpr_write_b32 a7, v135\n" \
  "v_accvgpr_write_b32 a8, v136\n" \
  "v_accvgpr_write_b32 a9, v137\n" \
  "v_accvgpr_write_b32 a10, v138\n" \
  "v_accvgpr_write_b32 a11, v139\n" \
  "v_accvgpr_write_b32 a12, v140\n" \
  "v_accvgpr_write_b32 a13, v141\n" \
  "v_accvgpr_write_b32 a14, v142\n" \
  "v_accvgpr_write_b32 a15, v143\n" \
  "v_accvgpr_write_b32 a16, v144\n" \
  "v_accvgpr_write_b32 a17, v145\n" \
  "v_accvgpr_write_b32 a18, v146\n" \
  "v_accvgpr_write_b32 a19, v147\n" \
  "v_accvgpr_write_b32 a20, v148\n" \
  "v_accvgpr_write_b32 a21, v149\n" \
  "v_accvgpr_write_b32 a22, v150\n" \
  "v_accvgpr_write_b32 a23, v151\n" \
  "v_accvgpr_write_b32 a24, v152\n" \
  "v_accvgpr_write_b32 a25, v153\n" \
  "v_accvgpr_write_b32 a26, v154\n" \
  "v_accvgpr_write_b32 a27, v155\n" \
  "v_accvgpr_write_b32 a28, v156\n" \
  "v_accvgpr_write_b32 a29, v157\n" \
  "v_accvgpr_write_b32 a30, v158\n" \
  "v_accvgpr_write_b32 a31, v159\n" \
  "s_branch .LF_w_b_%=\n" \
  ".LF_w2_%=:\n" \
  "v_accvgpr_write_b32 a32, v128\n" \
  "v_accvgpr_write_b32 a33, v129\n" \
  "v_accvgpr_write_b32 a34, v130\n" \
  "v_accvgpr_write_b32 a35, v131\n" \
  "v_accvgpr_write_b32 a36, v132\n" \
  "v_accvgpr_write_b32 a37, v133\n" \
  "v_accvgpr_write_b32 a38, v134\n" \
  "v_accvgpr_write_b32 a39, v135\n" \
  "v_accvgpr_write_b32 a40, v136\n" \
  "v_accvgpr_write_b32 a41, v137\n" \
  "v_accvgpr_write_b32 a42, v138\n" \
  "v_accvgpr_write_b32 a43, v139\n" \
  "v_accvgpr_write_b32 a44, v140\n" \
  "v_accvgpr_write_b32 a45, v141\n" \
  "v_accvgpr_write_b32 a46, v142\n" \
  "v_accvgpr_write_b32 a47, v143\n" \
  "v_accvgpr_write_b32 a48, v144\n" \
  "v_accvgpr_write_b32 a49, v145\n" \
  "v_accvgpr_write_b32 a50, v146\n" \
  "v_accvgpr_write_b32 a51, v147\n" \
  "v_accvgpr_write_b32 a52, v148\n" \
  "v_accvgpr_write_b32 a53, v149\n" \
  "v_accvgpr_write_b32 a54, v150\n" \
  "v_accvgpr_write_b32 a55, v151\n" \
  "v_accvgpr_write_b32 a56, v152\n" \
  "v_accvgpr_write_b32 a57, v153\n" \
  "v_accvgpr_write_b32 a58, v154\n" \
  "v_accvgpr_write_b32 a59, v155\n" \
  "v_accvgpr_write_b32 a60, v156\n" \
  "v_accvgpr_write_b32 a61, v157\n" \
  "v_accvgpr_write_b32 a62, v158\n" \
  "v_accvgpr_write_b32 a63, v159\n" \
  "s_branch .LF_w_b_%=\n" \
  ".LF_w3_%=:\n" \
  "v_accvgpr_write_b32 a64, v128\n" \
  "v_accvgpr_write_b32 a65, v129\n" \
  "v_accvgpr_write_b32 a66, v130\n" \
  "v_accvgpr_write_b32 a67, v131\n" \
  "v_accvgpr_write_b32 a68, v132\n" \
  "v_accvgpr_write_b32 a69, v133\n" \
  "v_accvgpr_write_b32 a70, v134\n" \
  "v_accvgpr_write_b32 a71, v135\n" \
  "v_accvgpr_write_b32 a72, v136\n" \
  "v_accvgpr_write_b32 a73, v137\n" \
  "v_accvgpr_write_b32 a74, v138\n" \
  "v_accvgpr_write_b32 a75, v139\n" \
  "v_accvgpr_write_b32 a76, v140\n" \
  "v_accvgpr_write_b32 a77, v141\n" \
  "v_accvgpr_write_b32 a78, v142\n" \
  "v_accvgpr_write_b32 a79, v143\n" \
  "v_accvgpr_write_b32 a80, v144\n" \
  "v_accvgpr_write_b32 a81, v145\n" \
  "v_accvgpr_write_b32 a82, v146\n" \
  "v_accvgpr_write_b32 a83, v147\n" \
  "v_accvgpr_write_b32 a84, v148\n" \
  "v_accvgpr_write_b32 a85, v149\n" \
  "v_accvgpr_write_b32 a86, v150\n" \
  "v_accvgpr_write_b32 a87, v151\n" \
  "v_accvgpr_write_b32 a88, v152\n" \
  "v_accvgpr_write_b32 a89, v153\n" \
  "v_accvgpr_write_b32 a90, v154\n" \
  "v_accvgpr_write_b32 a91, v155\n" \
  "v_accvgpr_write_b32 a92, v156\n" \
  "v_accvgpr_write_b32 a93, v157\n" \
  "v_accvgpr_write_b32 a94, v158\n" \
  "v_accvgpr_write_b32 a95, v159\n" \
  "s_branch .LF_w_b_%=\n" \
  ".LF_w4_%=:\n" \
  "v_accvgpr_write_b32 a96, v128\n" \
  "v_accvgpr_write_b32 a97, v129\n" \
  "v_accvgpr_write_b32 a98, v130\n" \
  "v_accvgpr_write_b32 a99, v131\n" \
  "v_accvgpr_write_b32 a100, v132\n" \
  "v_accvgpr_write_b32 a101, v133\n" \
  "v_accvgpr_write_b32 a102, v134\n" \
  "v_accvgpr_write_b32 a103, v135\n" \
  "v_accvgpr_write_b32 a104, v136\n" \
  "v_accvgpr_write_b32 a105, v137\n" \
  "v_accvgpr_write_b32 a106, v138\n" \
  "v_accvgpr_write_b32 a107, v139\n" \
  "v_accvgpr_write_b32 a108, v140\n" \
  "v_accvgpr_write_b32 a109, v141\n" \
  "v_accvgpr_write_b32 a110, v142\n" \
  "v_accvgpr_write_b32 a111, v143\n" \
  "v_accvgpr_write_b32 a112, v144\n" \
  "v_accvgpr_write_b32 a113, v145\n" \
  "v_accvgpr_write_b32 a114, v146\n" \
  "v_accvgpr_write_b32 a115, v147\n" \
  "v_accvgpr_write_b32 a116, v148\n" \
  "v_accvgpr_write_b32 a117, v149\n" \
  "v_accvgpr_write_b32 a118, v150\n" \
  "v_accvgpr_write_b32 a119, v151\n" \
  "v_accvgpr_write_b32 a120, v152\n" \
  "v_accvgpr_write_b32 a121, v153\n" \
  "v_accvgpr_write_b32 a122, v154\n" \
  "v_accvgpr_write_b32 a123, v155\n" \
  "v_accvgpr_write_b32 a124, v156\n" \
  "v_accvgpr_write_b32 a125, v157\n" \
  "v_accvgpr_write_b32 a126, v158\n" \
  "v_accvgpr_write_b32 a127, v159\n" \
  "s_branch .LF_w_b_%=\n" \
  ".LF_w5_%=:\n" \
  "v_accvgpr_write_b32 a128, v128\n" \
  "v_accvgpr_write_b32 a129, v129\n" \
  "v_accvgpr_write_b32 a130, v130\n" \
  "v_accvgpr_write_b32 a131, v131\n" \
  "v_accvgpr_write_b32 a132, v132\n" \
  "v_accvgpr_write_b32 a133, v133\n" \
  "v_accvgpr_write_b32 a134, v134\n" \
  "v_accvgpr_write_b32 a135, v135\n" \
  "v_accvgpr_write_b32 a136, v136\n" \
  "v_accvgpr_write_b32 a137, v137\n" \
  "v_accvgpr_write_b32 a138, v138\n" \
  "v_accvgpr_write_b32 a139, v139\n" \
  "v_accvgpr_write_b32 a140, v140\n" \
  "v_accvgpr_write_b32 a141, v141\n" \
  "v_accvgpr_write_b32 a142, v142\n" \
  "v_accvgpr_write_b32 a143, v143\n" \
  "v_accvgpr_write_b32 a144, v144\n" \
  "v_accvgpr_write_b32 a145, v145\n" \
  "v_accvgpr_write_b32 a146, v146\n" \
  "v_accvgpr_write_b32 a147, v147\n" \
  "v_accvgpr_write_b32 a148, v148\n" \
  "v_accvgpr_write_b32 a149, v149\n" \
  "v_accvgpr_write_b32 a150, v150\n" \
  "v_accvgpr_write_b32 a151, v151\n" \
  "v_accvgpr_write_b32 a152, v152\n" \
  "v_accvgpr_write_b32 a153, v153\n" \
  "v_accvgpr_write_b32 a154, v154\n" \
  "v_accvgpr_write_b32 a155, v155\n" \
  "v_accvgpr_write_b32 a156, v156\n" \
  "v_accvgpr_write_b32 a157, v157\n" \
  "v_accvgpr_write_b32 a158, v158\n" \
  "v_accvgpr_write_b32 a159, v159\n" \
  "s_branch .LF_w_b_%=\n" \
  ".LF_r1_%=:\n" \
  "v_mov_b32 v192, v216\n" \
  "v_mov_b32 v193, v217\n" \
  "v_mov_b32 v194, v218\n" \
  "v_accvgpr_read_b32 v128, a32\n" \
  "v_accvgpr_read_b32 v129, a33\n" \
  "v_accvgpr_read_b32 v130, a34\n" \
  "v_accvgpr_read_b32 v131, a35\n" \
  "v_accvgpr_read_b32 v132, a36\n" \
  "v_accvgpr_read_b32 v133, a37\n" \
  "v_accvgpr_read_b32 v134, a38\n" \
  "v_accvgpr_read_b32 v135, a39\n" \
  "v_accvgpr_read_b32 v136, a40\n" \
  "v_accvgpr_read_b32 v137, a41\n" \
  "v_accvgpr_read_b32 v138, a42\n" \
  "v_accvgpr_read_b32 v139, a43\n" \
  "v_accvgpr_read_b32 v140, a44\n" \
  "v_accvgpr_read_b32 v141, a45\n" \
  "v_accvgpr_read_b32 v142, a46\n" \
  "v_accvgpr_read_b32 v143, a47\n" \
  "v_accvgpr_read_b32 v144, a48\n" \
  "v_accvgpr_read_b32 v145, a49\n" \
  "v_accvgpr_read_b32 v146, a50\n" \
  "v_accvgpr_read_b32 v147, a51\n" \
  "v_accvgpr_read_b32 v148, a52\n" \
  "v_accvgpr_read_b32 v149, a53\n" \
  "v_accvgpr_read_b32 v150, a54\n" \
  "v_accvgpr_read_b32 v151, a55\n" \
  "v_accvgpr_read_b32 v152, a56\n" \
  "v_accvgpr_read_b32 v153, a57\n" \
  "v_accvgpr_read_b32 v154, a58\n" \
  "v_accvgpr_read_b32 v155, a59\n" \
  "v_accvgpr_read_b32 v156, a60\n" \
  "v_accvgpr_read_b32 v157, a61\n" \
  "v_accvgpr_read_b32 v158, a62\n" \
  "v_accvgpr_read_b32 v159, a63\n" \
  "s_branch .LF_r_b_%=\n" \
  ".LF_r2_%=:\n" \
  "v_mov_b32 v192, v219\n" \
  "v_mov_b32 v193, v220\n" \
  "v_mov_b32 v194, v221\n" \
  "v_accvgpr_read_b32 v128, a64\n" \
  "v_accvgpr_read_b32 v129, a65\n" \
  "v_accvgpr_read_b32 v130, a66\n" \
  "v_accvgpr_read_b32 v131, a67\n" \
  "v_accvgpr_read_b32 v132, a68\n" \
  "v_accvgpr_read_b32 v133, a69\n" \
  "v_accvgpr_read_b32 v134, a70\n" \
  "v_accvgpr_read_b32 v135, a71\n" \
  "v_accvgpr_read_b32 v136, a72\n" \
  "v_accvgpr_read_b32 v137, a73\n" \
  "v_accvgpr_read_b32 v138, a74\n" \
  "v_accvgpr_read_b32 v139, a75\n" \
  "v_accvgpr_read_b32 v140, a76\n" \
  "v_accvgpr_read_b32 v141, a77\n" \
  "v_accvgpr_read_b32 v142, a78\n" \
  "v_accvgpr_read_b32 v143, a79\n" \
  "v_accvgpr_read_b32 v144, a80\n" \
  "v_accvgpr_read_b32 v145, a81\n" \
  "v_accvgpr_read_b32 v146, a82\n" \
  "v_accvgpr_read_b32 v147, a83\n" \
  "v_accvgpr_read_b32 v148, a84\n" \
  "v_accvgpr_read_b32 v149, a85\n" \
  "v_accvgpr_read_b32 v150, a86\n" \
  "v_accvgpr_read_b32 v151, a87\n" \
  "v_accvgpr_read_b32 v152, a88\n" \
  "v_accvgpr_read_b32 v153, a89\n" \
  "v_accvgpr_read_b32 v154, a90\n" \
  "v_accvgpr_read_b32 v155, a91\n" \
  "v_accvgpr_read_b32 v156, a92\n" \
  "v_accvgpr_read_b32 v157, a93\n" \
  "v_accvgpr_read_b32 v158, a94\n" \
  "v_accvgpr_read_b32 v159, a95\n" \
  "s_branch .LF_r_b_%=\n" \
  ".LF_r3_%=:\n" \
  "v_mov_b32 v192, v222\n" \
  "v_mov_b32 v193, v223\n" \
  "v_mov_b32 v194, v224\n" \
  "v_accvgpr_read_b32 v128, a96\n" \
  "v_accvgpr_read_b32 v129, a97\n" \
  "v_accvgpr_read_b32 v130, a98\n" \
  "v_accvgpr_read_b32 v131, a99\n" \
  "v_accvgpr_read_b32 v132, a100\n" \
  "v_accvgpr_read_b32 v133, a101\n" \
  "v_accvgpr_read_b32 v134, a102\n" \
  "v_accvgpr_read_b32 v135, a103\n" \
  "v_accvgpr_read_b32 v136, a104\n" \
  "v_accvgpr_read_b32 v137, a105\n" \
  "v_accvgpr_read_b32 v138, a106\n" \
  "v_accvgpr_read_b32 v139, a107\n" \
  "v_accvgpr_read_b32 v140, a108\n" \
  "v_accvgpr_read_b32 v141, a109\n" \
  "v_accvgpr_read_b32 v142, a110\n" \
  "v_accvgpr_read_b32 v143, a111\n" \
  "v_accvgpr_read_b32 v144, a112\n" \
  "v_accvgpr_read_b32 v145, a113\n" \
  "v_accvgpr_read_b32 v146, a114\n" \
  "v_accvgpr_read_b32 v147, a115\n" \
  "v_accvgpr_read_b32 v148, a116\n" \
  "v_accvgpr_read_b32 v149, a117\n" \
  "v_accvgpr_read_b32 v150, a118\n" \
  "v_accvgpr_read_b32 v151, a119\n" \
  "v_accvgpr_read_b32 v152, a120\n" \
  "v_accvgpr_read_b32 v153, a121\n" \
  "v_accvgpr_read_b32 v154, a122\n" \
  "v_accvgpr_read_b32 v155, a123\n" \
  "v_accvgpr_read_b32 v156, a124\n" \
  "v_accvgpr_read_b32 v157, a125\n" \
  "v_accvgpr_read_b32 v158, a126\n" \
  "v_accvgpr_read_b32 v159, a127\n" \
  "s_branch .LF_r_b_%=\n" \
  ".LF_r4_%=:\n" \
  "v_mov_b32 v192, v225\n" \
  "v_mov_b32 v193, v226\n" \
  "v_mov_b32 v194, v227\n" \
  "v_accvgpr_read_b32 v128, a128\n" \
  "v_accvgpr_read_b32 v129, a129\n" \
  "v_accvgpr_read_b32 v130, a130\n" \
  "v_accvgpr_read_b32 v131, a131\n" \
  "v_accvgpr_read_b32 v132, a132\n" \
  "v_accvgpr_read_b32 v133, a133\n" \
  "v_accvgpr_read_b32 v134, a134\n" \
  "v_accvgpr_read_b32 v135, a135\n" \
  "v_accvgpr_read_b32 v136, a136\n" \
  "v_accvgpr_read_b32 v137, a137\n" \
  "v_accvgpr_read_b32 v138, a138\n" \
  "v_accvgpr_read_b32 v139, a139\n" \
  "v_accvgpr_read_b32 v140, a140\n" \
  "v_accvgpr_read_b32 v141, a141\n" \
  "v_accvgpr_read_b32 v142, a142\n" \
  "v_accvgpr_read_b32 v143, a143\n" \
  "v_accvgpr_read_b32 v144, a144\n" \
  "v_accvgpr_read_b32 v145, a145\n" \
  "v_accvgpr_read_b32 v146, a146\n" \
  "v_accvgpr_read_b32 v147, a147\n" \
  "v_accvgpr_read_b32 v148, a148\n" \
  "v_accvgpr_read_b32 v149, a149\n" \
  "v_accvgpr_read_b32 v150, a150\n" \
  "v_accvgpr_read_b32 v151, a151\n" \
  "v_accvgpr_read_b32 v152, a152\n" \
  "v_accvgpr_read_b32 v153, a153\n" \
  "v_accvgpr_read_b32 v154, a154\n" \
  "v_accvgpr_read_b32 v155, a155\n" \
  "v_accvgpr_read_b32 v156, a156\n" \
  "v_accvgpr_read_b32 v157, a157\n" \
  "v_accvgpr_read_b32 v158, a158\n" \
  "v_accvgpr_read_b32 v159, a159\n" \
  "s_branch .LF_r_b_%=\n" \
  ".LF_r5_%=:\n" \
  "v_mov_b32 v192, v228\n" \
  "v_mov_b32 v193, v229\n" \
  "v_mov_b32 v194, v230\n" \
  "v_accvgpr_read_b32 v128, a160\n" \
  "v_accvgpr_read_b32 v129, a161\n" \
  "v_accvgpr_read_b32 v130, a162\n" \
  "v_accvgpr_read_b32 v131, a163\n" \
  "v_accvgpr_read_b32 v132, a164\n" \
  "v_accvgpr_read_b32 v133, a165\n" \
  "v_accvgpr_read_b32 v134, a166\n" \
  "v_accvgpr_read_b32 v135, a167\n" \
  "v_accvgpr_read_b32 v136, a168\n" \
  "v_accvgpr_read_b32 v137, a169\n" \
  "v_accvgpr_read_b32 v138, a170\n" \
  "v_accvgpr_read_b32 v139, a171\n" \
  "v_accvgpr_read_b32 v140, a172\n" \
  "v_accvgpr_read_b32 v141, a173\n" \
  "v_accvgpr_read_b32 v142, a174\n" \
  "v_accvgpr_read_b32 v143, a175\n" \
  "v_accvgpr_read_b32 v144, a176\n" \
  "v_accvgpr_read_b32 v145, a177\n" \
  "v_accvgpr_read_b32 v146, a178\n" \
  "v_accvgpr_read_b32 v147, a179\n" \
  "v_accvgpr_read_b32 v148, a180\n" \
  "v_accvgpr_read_b32 v149, a181\n" \
  "v_accvgpr_read_b32 v150, a182\n" \
  "v_accvgpr_read_b32 v151, a183\n" \
  "v_accvgpr_read_b32 v152, a184\n" \
  "v_accvgpr_read_b32 v153, a185\n" \
  "v_accvgpr_read_b32 v154, a186\n" \
  "v_accvgpr_read_b32 v155, a187\n" \
  "v_accvgpr_read_b32 v156, a188\n" \
  "v_accvgpr_read_b32 v157, a189\n" \
  "v_accvgpr_read_b32 v158, a190\n" \
  "v_accvgpr_read_b32 v159, a191\n" \
  "s_branch .LF_r_b_%=\n" \
  ".LF_bad_%=:\n" \
  "v_max_u32 v60, v0, v1\n" \
  "v_max3_u32 v60, v60, v2, v3\n" \
  "v_max3_u32 v60, v60, v4, v5\n" \
  "v_max3_u32 v60, v60, v6, v7\n" \
  "v_max3_u32 v60, v60, v8, v9\n" \
  "v_max3_u32 v60, v60, v10, v11\n" \
  "v_max3_u32 v60, v60, v12, v13\n" \
  "v_max3_u32 v60, v60, v14, v15\n" \
  "v_max3_u32 v60, v60, v16, v17\n" \
  "v_max3_u32 v60, v60, v18, v19\n" \
  "v_max3_u32 v60, v60, v20, v21\n" \
  "v_max3_u32 v60, v60, v22, v23\n" \
  "v_max3_u32 v60, v60, v24, v25\n" \
  "v_max3_u32 v60, v60, v26, v27\n" \
  "v_max3_u32 v60, v60, v28, v29\n" \
  "v_max3_u32 v60, v60, v30, v31\n" \
  "v_max3_u32 v60, v60, v32, v33\n" \
  "v_max3_u32 v60, v60, v34, v35\n" \
  "v_max3_u32 v60, v60, v36, v37\n" \
  "v_max3_u32 v60, v60, v38, v39\n" \
  "v_cmp_eq_u32 vcc, -1, v60\n" \
  "s_cbranch_vccz .LF_ok_%=\n" \
  "v_mov_b64 v[40:41], 0\n" \
  "v_mov_b64 v[42:43], 0\n" \
  "v_mov_b64 v[44:45], 0\n" \
  "v_mov_b64 v[46:47], 0\n" \
  "v_mov_b64 v[48:49], 0\n" \
  "v_mov_b64 v[50:51], 0\n" \
  "v_mov_b64 v[52:53], 0\n" \
  "v_mov_b64 v[54:55], 0\n" \
  "s_sleep 2\n" \
  "s_cmp_eq_u32 s48, 5\n" \
  "s_cbranch_scc1 .LF_bd1_%=\n" \
  "s_sleep 20\n" \
  ".LF_bd1_%=:\n" \
  "s_add_u32 s69, s69, 1\n" \
  "s_and_b32 s63, s69, 1023\n" \
  "s_cbranch_scc1 .LF_rd_%=\n" \
  "buffer_load_dwordx2 v[60:61], off, s[40:43], %41 offset:128 sc1\n" \
  "s_waitcnt vmcnt(0)\n" \
  "v_readfirstlane_b32 s63, v61\n" \
  "s_cmp_eq_u32 s63, %33\n" \
  "s_cbranch_scc1 .LF_ok_%=\n" \
  "s_cmp_le_u32 s69, 2097152\n" \
  "s_cbranch_scc1 .LF_rd_%=\n" \
  "v_mov_b32 v60, 1\n" \
  "v_mov_b32 v61, %33\n" \
  "buffer_store_dwordx2 v[60:61], off, s[40:43], %41 offset:128 sc1\n" \
  "s_branch .LF_ok_%=\n" \
  ".LF_end_%=:\n"
#define F_ASM_VOPS "+{v200}"(fi.sidx), "+{v201}"(fi.cidx), "+{v202}"(fi.l16), "+{v203}"(fi.ae0), "+{v204}"(fi.ae1), "+{v205}"(fi.ae2), "+{v206}"(fi.ae3), "+{v207}"(fi.am), "+{v208}"(fi.lw), "+{v209}"(fi.lr), "+{v210}"(fi.oo), "+{v211}"(fi.d0), "+{v212}"(fi.tq), "+{v213}"(fi.g[0]), "+{v214}"(fi.b[0]), "+{v215}"(fi.c[0]), "+{v216}"(fi.g[1]), "+{v217}"(fi.b[1]), "+{v218}"(fi.c[1]), "+{v219}"(fi.g[2]), "+{v220}"(fi.b[2]), "+{v221}"(fi.c[2]), "+{v222}"(fi.g[3]), "+{v223}"(fi.b[3]), "+{v224}"(fi.c[3]), "+{v225}"(fi.g[4]), "+{v226}"(fi.b[4]), "+{v227}"(fi.c[4]), "+{v228}"(fi.g[5]), "+{v229}"(fi.b[5]), "+{v230}"(fi.c[5])
#define F_ASM_SOPS "s"(fi.ws), "s"(fi.out), "s"(fi.tag), "s"(fi.o_u), "s"(fi.o_e), "s"(fi.o_x), "s"(fi.o_va), "s"(fi.o_tw), "s"(fi.xa), "s"(fi.xb), "s"(fi.o_ctl), "s"(fi.pa), "s"(fi.w), "s"(fi.f_w), "s"(fi.pq)
#define F_ASM_CLOB "v0", "v1", "v2", "v3", "v4", "v5", "v6", "v7", "v8", "v9", "v10", "v11", "v12", "v13", "v14", "v15", "v16", "v17", "v18", "v19", "v20", "v21", "v22", "v23", "v24", "v25", "v26", "v27", "v28", "v29", "v30", "v31", "v32", "v33", "v34", "v35", "v36", "v37", "v38", "v39", "v40", "v41", "v42", "v43", "v44", "v45", "v46", "v47", "v48", "v49", "v50", "v51", "v52", "v53", "v54", "v55", "v56", "v57", "v58", "v59", "v60", "v61", "v62", "v63", "v64", "v65", "v66", "v67", "v68", "v69", "v70", "v71", "v72", "v73", "v74", "v75", "v76", "v77", "v78", "v79", "v80", "v81", "v82", "v83", "v84", "v85", "v86", "v87", "v88", "v89", "v90", "v91", "v92", "v93", "v94", "v95", "v96", "v97", "v98", "v99", "v100", "v101", "v102", "v103", "v104", "v105", "v106", "v107", "v108", "v109", "v110", "v111", "v112", "v113", "v114", "v115", "v116", "v117", "v118", "v119", "v120", "v121", "v122", "v123", "v124", "v125", "v126", "v127", "v128", "v129", "v130", "v131", "v132", "v133", "v134", "v135", "v136", "v137", "v138", "v139", "v140", "v141", "v142", "v143", "v144", "v145", "v146", "v147", "v148", "v149", "v150", "v151", "v152", "v153", "v154", "v155", "v156", "v157", "v158", "v159", "v160", "v161", "v162", "v163", "v164", "v165", "v166", "v167", "v168", "v169", "v170", "v171", "v172", "v173", "v174", "v175", "v176", "v177", "v178", "v179", "v180", "v181", "v182", "v183", "v184", "v185", "v186", "v187", "v188", "v189", "v190", "v191", "v192", "v193", "v194", "v195", "v196", "v197", "v232", "v233", "v234", "v235", "v236", "v237", "a0", "a1", "a2", "a3", "a4", "a5", "a6", "a7", "a8", "a9", "a10", "a11", "a12", "a13", "a14", "a15", "a16", "a17", "a18", "a19", "a20", "a21", "a22", "a23", "a24", "a25", "a26", "a27", "a28", "a29", "a30", "a31", "a32", "a33", "a34", "a35", "a36", "a37", "a38", "a39", "a40", "a41", "a42", "a43", "a44", "a45", "a46", "a47", "a48", "a49", "a50", "a51", "a52", "a53", "a54", "a55", "a56", "a57", "a58", "a59", "a60", "a61", "a62", "a63", "a64", "a65", "a66", "a67", "a68", "a69", "a70", "a71", "a72", "a73", "a74", "a75", "a76", "a77", "a78", "a79", "a80", "a81", "a82", "a83", "a84", "a85", "a86", "a87", "a88", "a89", "a90", "a91", "a92", "a93", "a94", "a95", "a96", "a97", "a98", "a99", "a100", "a101", "a102", "a103", "a104", "a105", "a106", "a107", "a108", "a109", "a110", "a111", "a112", "a113", "a114", "a115", "a116", "a117", "a118", "a119", "a120", "a121", "a122", "a123", "a124", "a125", "a126", "a127", "a128", "a129", "a130", "a131", "a132", "a133", "a134", "a135", "a136", "a137", "a138", "a139", "a140", "a141", "a142", "a143", "a144", "a145", "a146", "a147", "a148", "a149", "a150", "a151", "a152", "a153", "a154", "a155", "a156", "a157", "a158", "a159", "a160", "a161", "a162", "a163", "a164", "a165", "a166", "a167", "a168", "a169", "a170", "a171", "a172", "a173", "a174", "a175", "a176", "a177", "a178", "a179", "a180", "a181", "a182", "a183", "a184", "a185", "a186", "a187", "a188", "a189", "a190", "a191", "s40", "s41", "s42", "s43", "s44", "s45", "s46", "s47", "s48", "s49", "s50", "s51", "s52", "s53", "s54", "s55", "s56", "s57", "s58", "s59", "s60", "s61", "s62", "s63", "s64", "s65", "s66", "s67", "s68", "s69", "s70", "s71", "s72", "s73", "s74", "s75", "s76", "s77", "s78", "s79", "s80", "s81", "s82", "s83", "s84", "s85", "s86", "s87", "s88", "s89", "s90", "s91", "s92", "s93", "s94", "s95", "s96", "s97", "s98", "s99", "vcc", "scc", "memory"

struct RedIn {
  unsigned sidx, cidx, l16, ax, am, mo, sel, to, fu, p0, p1, p2, p3, d0, d1, d2, d3, d4;
  const void* ws;
  unsigned w, go, tag, o_va, o_vb, lab, o_u, kind, o_ctl, pa, pb, f_d, f_r, ph, f_l;
};
__device__ __forceinline__ void red_role(const Params& p, int w, int T, char* smem) {
  const int grp = T & 3;
  CODE_PAD(PAD_RED);
  char* const ws = p.ws;
  Ctx cx = {ws, 0u};
  const int l = lane_id(), n = l & 15, g = l >> 4;
  comm_init(cx, grp * 9 + (T >> 2), w, smem, p.wl2 & 4);
  Bulk bk;
  bk.init(cx, grp);
  bk.pl.load(cx, grp);
  const Place& pl = bk.pl;
  char* const base = comm_base(ws) - CO;
  const unsigned dl = (unsigned)(ws - base);
  RedIn ri;
  ri.sidx = (n & 8) ? 0xEEEEu : 0x4444u;
  ri.cidx = (n & 3) * 0x5555u;
  ri.l16 = l * 16;
  ri.ax = W_PX * 32768 + (n >> 3) * 512 + g * 128 + (n & 7) * 16;
  ri.am = W_PM * 32768 + l * 16;
  const unsigned mt = (W_MEM + (T >> 4)) * 32768 + (T & 15) * 256;
  ri.mo = mt + (2 * (g & 1) + (g >> 1)) * 64 + (n >> 3) * 32 + (n & 1) * 16 + ((n >> 1) & 3) * 4;
  ri.sel = w ? (g == 1 ? (U_THIRD ? 2 : NSEG) : g >> 1) : (n & 1) ? 0x03020706u : 0x05040100u;
  ri.to = l < 4 ? CO + TW_OFF + (16384u ^ pl.dyn(l)) + 256 : 0x7F000000u;
  ri.fu = CO + pl.near(N_U + min(10 * w - 10 + min(n, 11), w == 3 ? 31 : 10 * w - 1)) + (T < 32 ? (T >> 1) & 3 : T & 3) * 8 + (g == 1 ? 64 : l & 32);
  ri.d0 = 0; ri.d1 = 0; ri.d2 = 0; ri.d3 = 0; ri.d4 = 0;
  ri.ws = base; ri.w = w; ri.go = pl.go; ri.tag = cx.tag; ri.lab = grp; ri.o_ctl = dl;
  ri.o_va = __builtin_amdgcn_readfirstlane(CO + (win_static(W_VO + 2 * (T >> 2)) ^ pl.dyn(grp)));
  ri.o_vb = __builtin_amdgcn_readfirstlane(CO + (win_static(W_VO + 2 * (T >> 2) + 1) ^ pl.dyn(grp)));
  auto dead = [&](int i) { return i < 0 ? CO + (16384u ^ pl.dyn(g)) + mt + n * 16 : (i < 4 ? ri.o_va : ri.o_vb) + (i & 3) * 1024 + l * 16; };
  ri.p0 = w ? dead(3 * w - 4) : T < 8 && l < 4 ? ri.to - 116 : 0x7F000000u;
  ri.p1 = w ? dead(3 * w - 3) : l < 4 ? 0u : 0x7F000000u; ri.p2 = dead(3 * w - 2);
  const int pz = (T >> 2) * 3 + w - 1;
  ri.p3 = pz < 24 ? CO + (16384u ^ pl.dyn(grp)) + (W_PX + (pz >> 2)) * 32768 + (pz & 3) * 1024 + l * 16 : 0x7F000000u;
  ri.o_u = __builtin_amdgcn_readfirstlane(u_tile(T, 0, 0) ^ (~bk.w13 & 8192u));
  ri.kind = __builtin_amdgcn_readfirstlane(T == 35 ? CO + TW_OFF + (16384u ^ pl.dyn(0)) + 512 : 0u);
  ri.pa = p.vlead << 16;
  ri.pb = p.dret | (p.late_r << 16);
  ri.ph = p.hdel | (p.hdl2 << 8) | 0x10000;
  ri.f_d = __builtin_amdgcn_readfirstlane(T < 32 ? CO + pl.near(N_RD + T) : 0x7F000000u);
  ri.f_r = dl + F_R * 128 + T * 8;
  ri.f_l = __builtin_amdgcn_readfirstlane(CO + (pl.near(N_DUR) ^ 4096u));
  asm volatile(R_ASM_TEXT : R_ASM_VOPS : R_ASM_SOPS : R_ASM_CLOB);
}

__device__ __forceinline__ int c_line(int h) { return (h >> 2) * 512 + (h & 1) * 256 + (h & 2) * 64; }
__device__ __forceinline__ void first_role(const Params& p) {
  const int l = lane_id();
  const Ctx cx = {p.ws, __builtin_amdgcn_readfirstlane(gen_load(p.ws)) + 1u};
  Place pl;
  pl.load(cx, 0);
  unsigned spins = 0;
  for (int s = 0; s < U_WATCH; ++s) {
    GAS u64* const f = (GAS u64*)(comm_base(p.ws) + pl.near(N_U + (l & 31))) + (l >> 5) * 2 + (s & 1) * 4;
    while (__any((int)min(flag_get(cx, f), flag_get(cx, f + 1)) <= s) && !spin_fail(spins, cx)) sleep_n(p.fsl);
    if (l == 0) flag_put(cx, (GAS u64*)(comm_base(p.ws) + (pl.near(N_DUR + s) ^ 4096u)), s + 1);
  }
}
struct ChainIn {
  unsigned sidx, l16, qw, kw, fq, fk, kxo, pst, ps2, lx, sid2, qxo, sto, plx, mk, ck0, ck2, hx, viw, fva, pk3, fvs, uo, po0, po1, po2, co0, co1, co2;
  unsigned vq0, vq1, vq2, vq3, vo0, vo1, vo2, vo3, v50, v51, v52, v53, tro0, tro1, tro2, tro3, tro4, m0o;
  const void *ws, *Wa, *Wb, *mem0;
  unsigned w, o_a, o_b, o_nx, o_exp, lab, f_p, f_m, f_dur, f_go, o_ctl, o_dbg, dret, o_qx, f_s, f_r;
};

template <bool SC>
__device__ __forceinline__ void chain_role(const Params& p, int w, int h, int grp) {
  constexpr unsigned QT = 9216, KT = 13312, SX = 16384, VI = 9216, VX = 16384;
  const int l = lane_id(), n = l & 15, g = l >> 4, q4 = (l >> 2) & 3, p4 = l & 3;
  const int ty = (n >> 1) & 1, row = 2 * (n >> 2) + (n & 1), cid = h * 2 + (SC ? 0 : 1);
  char* const base = comm_base(p.ws) - CO;
  const unsigned dl = (unsigned)(p.ws - base);
  ChainIn ci;
  ci.sidx = (n & 2) ? 0xEEEEu : 0x4444u;
  ci.l16 = l * 16;
  ci.m0o = row * (DM * 2) + g * 32 + ty * 16;
  if (SC) {
    ci.qw = QT + (32 * w + n) * 16 + g * 4;
    ci.kw = KT + (32 * w + n) * 16 + (((g & 1) << 1) | (g >> 1)) * 4;
    ci.fq = QT + (4 * g + q4) * 16 + (p4 & 1) * 8;
    ci.fk = KT + (4 * g + q4) * 16 + (p4 >> 1) * 8;
    ci.kxo = (g * 16 + 8 * (q4 & 1) + 4 * (q4 >> 1) + p4) * 16;
    const unsigned ex = CO + win_static(W_PX + (h >> 3)) + (h & 7) * 512 + (g & 1) * 128 + (n & 7) * 16 + (g >> 1) * 8 + (n >> 3);
    if (w < 2) ci.pst = ex + w * 256;
    else if (g & 1) ci.pst = CO + win_static(W_PC) + c_line(h) + (n & 7) * 16 + (((n >> 2) ^ (g >> 1)) & 1) * 8 + 2 * (w & 1) + (n >> 3);
    else ci.pst = CO + win_static(W_PM) + (h >> 1) * 256 + (VO_SWAP ? (g >> 1) * 8 + (h & 1) * 128 : (g >> 1) * 128 + (h & 1) * 8) + (n & 7) * 16 + 2 * (w & 1) + (n >> 3);
    ci.ps2 = w < 2 ? ci.pst + 2 : ex + 2 * (w & 1);
    ci.mk = (g & 1) ? 0u : 0x3F800000u;
    ci.ck0 = g == 1 && !(h & 2) ? 0x3F800000u : 0u;
    ci.ck2 = g == 1 && (h & 2) ? 0x3F800000u : 0u;
    ci.hx = SX + 2048 + ((w & 1) * 64 + l) * HAND_B;
    ci.lx = SX + l * 8 + (w == 1 ? 4 : 0);
    ci.qxo = (w >> 1) * 1024 + (g * 16 + 8 * (w & 1) + (n & 7)) * 16;
    ci.sto = 0x80000 + h * 256 + (8 * w + (n & 7)) * 8;
    ci.plx = n >= 8 ? 0x7F000000u : w * 16384 + n * 16 + ((g & 1) ? 4096 + c_line(h) + (((n >> 2) ^ (g >> 1)) & 1) * 8 : h * 128 + (g >> 1) * 8);
  } else {
    ci.viw = VI + (32 * w + n) * 16 + g * 4;
    ci.fva = VI + (4 * g + q4) * 16 + (p4 & 1) * 8;
    ci.pk3 = VI + n * 16 + g * 4; ci.fvs = w * 1024 + l * 16;
    ci.uo = w == 1 ? l * 16 : 0x7F000000u;
    auto vo_at = [&](int T0) {
      const int T = T0 + 2 * w + (g >> 1);
      return CO + win_static(W_VO + 2 * (T >> 2) + (h >> 4)) + (h & 15) * 256 + n * 16 + (T & 3);
    };
    ci.po0 = vo_at(8 * (g & 1)); ci.po1 = vo_at(16 + 8 * (g & 1));
    auto co_at = [&](int T0) { return (T0 + 2 * w + (g >> 1)) * 16384 + h * 256 + n * 16; };
    ci.co0 = co_at(8 * (g & 1)); ci.co1 = co_at(16 + 8 * (g & 1)); ci.co2 = co_at(w == 0 ? 16 : w == 1 ? 22 : 28) + (g & 1) * 8;
    if (w == 0) ci.po1 = h < 4 && l < 4 ? CO + TW_OFF + 16384 + 384 + l : 0x7F000000u;
    ci.po2 = vo_at(w == 0 ? 16 : w == 1 ? 22 : 28) + (g & 1) * 8;
  }
  ci.sid2 = (n & 8) ? 0xEEEEu : 0x4444u;
  unsigned vq[4], vo[4], v5[4];
#pragma unroll
  for (int i = 0; i < 4; ++i) {
    const int ch = n ^ (2 * ((g & 1) | (i << 1))), tp = ch >> 1;
    vq[i] = (g + 4 * i) * (HD * 2) + ch * 16 - 1024 * i;
    vo[i] = (g + 4 * i) * (DM * 2) + ch * 16 - 1024 * i;
    v5[i] = (g + 4 * i + 16 - 16 * (tp >> 2)) * (DM * 2) + (32 + (tp & 3)) * 32 + (ch & 1) * 16 - 1024 * i;
  }
  ci.vq0 = vq[0]; ci.vq1 = vq[1]; ci.vq2 = vq[2]; ci.vq3 = vq[3];
  ci.vo0 = vo[0]; ci.vo1 = vo[1]; ci.vo2 = vo[2]; ci.vo3 = vo[3];
  ci.v50 = v5[0]; ci.v51 = v5[1]; ci.v52 = v5[2]; ci.v53 = v5[3];
  {
    const int rowl = 16 * (g >> 1) + 8 * (q4 >> 1) + 4 * (g & 1) + (q4 & 1), rho = (rowl & 1) | (((rowl >> 2) & 3) << 1);
    ci.tro0 = rowl * 256 + (((4 * w + (p4 >> 1)) ^ (2 * rho)) * 16) + (p4 & 1) * 8;
    ci.tro1 = rowl * 256 + (((4 * w + 2 + (p4 >> 1)) ^ (2 * rho)) * 16) + (p4 & 1) * 8;
    const int row2 = 16 * (g & 1) + 4 * (g >> 1) + (q4 & 1), rho2 = (row2 & 1) | (((row2 >> 2) & 3) << 1);
    ci.tro2 = row2 * 256 + (((4 * w + 2 * (q4 >> 1) + (p4 >> 1)) ^ (2 * rho2)) * 16) + (p4 & 1) * 8;
    ci.tro3 = w < 2 ? (ci.tro2 ^ (w * 64)) + 7 * 16384 : ci.tro2 + 9 * 16384;
    ci.tro4 = w < 2 ? ci.tro3 + 16384 : ci.tro3 ^ 128u;
  }
  ci.ws = base; ci.Wa = SC ? p.Wq : p.Wv; ci.Wb = SC ? p.Wk : p.Wo; ci.mem0 = p.mem0;
  ci.w = w;
  ci.o_a = 16 * w * (HD * 2) + h * 256;
  ci.o_b = (h * DH + 32 * w) * (DM * 2);
  ci.o_nx = near_head(B_K, h) + (w & 1) * 4096;
  ci.o_exp = near_head(B_K, h) + (SC ? 0 : 4096);
  ci.o_qx = PB + 2 * B_O + (SC ? h * 16384 : 0); ci.f_s = __builtin_amdgcn_readfirstlane(dl + (F_S + h) * 128);
  ci.f_r = dl + F_R * 128;
  ci.lab = grp;
  ci.f_p = __builtin_amdgcn_readfirstlane(CO + near_static(N_K + h) + (SC ? 0 : 8));
  ci.f_m = SC ? dl + (F_M + cid) * 128 + (w & 1) * 8 : w == 1 ? dl + (F_M + cid) * 128 : 0x7F000000u; ci.f_dur = __builtin_amdgcn_readfirstlane(CO + near_static(N_DUR + h));
  ci.f_go = dl + (F_GO + grp) * 128; ci.o_ctl = dl;
  ci.o_dbg = dl + (unsigned)WS_END + (cid * 4 + w) * 4096;
  ci.dret = p.dret | (p.late_s << 16);
  if (SC) asm volatile(X_ASM_TEXT M_ASM_TEXT : X_ASM_VOPS : X_ASM_SOPS : X_ASM_CLOB);
  else asm volatile(W_ASM_TEXT V_ASM_TEXT : V_ASM_VOPS : V_ASM_SOPS : V_ASM_CLOB);
}

struct ProjIn {
  unsigned l16, xr0, xr1, ew, er, uo, xs0, xs1, xs2, xs3, xs4, xs5, xs6, xs7, xs8;
  unsigned vq0, vq1, vq2, vq3, vo0, vo1, vo2, vo3, v50, v51, v52, v53, tro0, tro1;
  const void *ws, *Wa, *Wb, *x;
  unsigned w, o_a, o_b, o_p, o_q, f_a, f_b, f_go, o_ctl, f_c, u8, rng, f_d;
};
template <bool VU, int QO = 0>
__device__ __forceinline__ unsigned proj_role(const Params& p, int w, int h) {
  const int l = lane_id(), n = l & 15, g = l >> 4, q4 = (l >> 2) & 3, p4 = l & 3;
  char* const base = comm_base(p.ws) - CO;
  const unsigned dl = (unsigned)(p.ws - base);
  ProjIn pi;
  pi.l16 = l * 16; pi.uo = 0;
  unsigned xs[9];
#pragma unroll
  for (int i = 0; i < 9; ++i) {
    const int ci = (9 * w + i) * 64 + l, r = ci / 72, cc = ci - r * 72;
    xs[i] = (r * 72 + (cc ^ ((r >> 1) & 7))) * 16;
  }
  pi.xs0 = xs[0]; pi.xs1 = xs[1]; pi.xs2 = xs[2]; pi.xs3 = xs[3]; pi.xs4 = xs[4]; pi.xs5 = xs[5]; pi.xs6 = xs[6]; pi.xs7 = xs[7]; pi.xs8 = xs[8];
  pi.xr0 = 16384 + n * 1152 + ((g ^ ((n >> 1) & 7)) * 16); pi.xr1 = 16384 + n * 1152 + (((4 + g) ^ ((n >> 1) & 7)) * 16);
  pi.ew = w * 2048 + n * 64 + ((g ^ ((n >> 1) & 1) ^ ((n >> 2) & 2)) * 16);
  pi.er = 8192 + (g >> 1) * 1032 + (8 * (g & 1) + q4) * 64 + ((p4 ^ (q4 >> 1) ^ (2 * (g & 1))) * 16);
  unsigned vq[4], vo[4], v5[4];
#pragma unroll
  for (int i = 0; i < 4; ++i) {
    const int ch = n ^ (2 * (g | ((i >> 1) << 2))), tp = ch >> 1;
    vq[i] = (g + 4 * i) * (HD * 2) + ch * 16 - 1024 * i;
    vo[i] = (g + 4 * i) * (DM * 2) + ch * 16 - 1024 * i;
    v5[i] = (g + 4 * i + 64 * (tp & 1)) * (DM * 2) + (32 + (tp >> 1)) * 32 + (ch & 1) * 16 - 1024 * i;
  }
  pi.vq0 = vq[0]; pi.vq1 = vq[1]; pi.vq2 = vq[2]; pi.vq3 = vq[3];
  pi.vo0 = vo[0]; pi.vo1 = vo[1]; pi.vo2 = vo[2]; pi.vo3 = vo[3];
  pi.v50 = v5[0]; pi.v51 = v5[1]; pi.v52 = v5[2]; pi.v53 = v5[3];
  const int rho = q4 | ((g & 1) << 2);
  pi.tro0 = (8 * g + q4) * 256 + (((4 * w + (p4 >> 1)) ^ (2 * rho)) * 16) + (p4 & 1) * 8;
  pi.tro1 = (8 * g + q4) * 256 + (((4 * w + 2 + (p4 >> 1)) ^ (2 * rho)) * 16) + (p4 & 1) * 8;
  pi.ws = base; pi.Wa = VU ? p.Wv : QO ? p.Wq : p.Wk; pi.Wb = VU ? p.Wo : p.Wq; pi.x = p.x;
  pi.w = w;
  pi.o_a = 16 * w * (HD * 2) + h * 256;
  pi.o_b = (h * DH + 16 * w) * (DM * 2);
  pi.o_p = PB + 2 * (VU ? B_V : QO ? B_Q : B_K) + h * 16384 + w * 2048 + (VU ? QO * 524288 : 0);
  pi.o_q = VU ? u_tile(2 * w, h >> 3, h & 7) ^ 8192u : PB + 2 * B_Q + h * 16384 + w * 2048 + (K_FIRST - 1) * 524288;
  pi.f_a = __builtin_amdgcn_readfirstlane(w || (QO && !VU) || QO == 2 ? 0x7F000000u : CO + near_static(N_K + h) + (VU ? 8 + 8 * QO : 0));
  pi.f_b = __builtin_amdgcn_readfirstlane(VU ? CO + near_static(N_U + h) + w * 8 + 32 * QO : w || QO ? 0x7F000000u : dl + (F_Q + h) * 128);
  pi.f_go = dl + (F_GO + 4 + (h & 3)) * 128; pi.o_ctl = dl;
  pi.f_c = VU ? dl + (F_M + 2 * h + 1) * 128 : QO ? 77 : 78;
  pi.f_d = __builtin_amdgcn_readfirstlane(QO ? 0x7F000000u : dl + (F_U + h) * 128 + w * 8);
  pi.rng = QO | (QO == 2 ? 4 : QO ? 2 * U_HELP + 1 : NSEG) << 8 | U_LEAD << 16;
  pi.u8 = QO && !VU ? 0x7F000000u : (VU ? u_tile(32 + w, h >> 3, h & 7) : near_head(B_K, h) + w * 2048) ^ 8192u;
  unsigned tag;
  if (!VU) sleep_n(QO ? p.q0 : p.pj0);
  if (VU) asm volatile(U_ASM_TEXT : P_ASM_VOPS : P_ASM_SOPS : P_ASM_CLOB);
  else asm volatile(K_ASM_TEXT : P_ASM_VOPS : P_ASM_SOPS : P_ASM_CLOB);
  return tag;
}

constexpr int NS4 = 67;
__device__ __forceinline__ unsigned y_at(const Bulk& bk, int quad, unsigned o) { return bk.at(B_YT + quad * 16384 + o) ^ (quad >= Y_NEAR && quad < Y_FAR ? 8192u : 0u); }
__device__ __forceinline__ void mix_role(const Params& p, unsigned tag, int w, int ct, int s0, int ds, int s1, bool late) {
  const int l = lane_id(), n = l & 15, g = l >> 4;
  const Ctx cx = {p.ws, tag};
  Bulk bk;
  bk.init(cx, 4 + (ct & 3));
  const int tr = (w >> 1) * 16 + n, dc = ct * 96 + (w & 1) * 48 + 4 * g;
  const rsrc_t rx = mk_rsrc(p.x, (size_t)SL * DM * 2), rout = mk_rsrc(p.out, (size_t)SL * DM * 2);
  const int ro = (((w >> 1) * 16 + (l >> 2)) * DM + ct * 96 + (w & 1) * 48 + 4 * (l & 3)) * 2, pin = (4 * n + g) * 4, pout = (16 * (l & 3) + (l >> 2)) * 4;
  auto perm = [](int a, u32x2 v) { return (u32x2){(unsigned)__builtin_amdgcn_ds_bpermute(a, v[0]), (unsigned)__builtin_amdgcn_ds_bpermute(a, v[1])}; };
  u32x2 dv[3];
  unsigned yo[3];
#pragma unroll
  for (int i = 0; i < 3; ++i) { dv[i] = *(const u32x2*)(p.dsk + dc + 16 * i); yo[i] = y_at(bk, (dc + 16 * i) >> 2, tr * 8); }
  struct In { u32x4 a[4]; u32x2 b[4]; u32x2 x[3], y[3]; float g; bool ok; };
  auto ldb = [&](In& in, int sg) {
    in.g = __uint_as_float(__builtin_amdgcn_raw_buffer_load_b32(bk.rs, tr * 4, bk.at(B_G) + sg * 128, 16));
#pragma unroll
    for (int i = 0; i < 3; ++i) {
      in.y[i] = __builtin_amdgcn_raw_buffer_load_b64(bk.rs, yo[i], (sg >> 5) * 16384 + (sg & 31) * 256, 16);
      in.x[i] = __builtin_amdgcn_raw_buffer_load_b64(rx, ro + 32 * i, sg * (32 * DM * 2), 0);
    }
  };
  int seen = 0;
  const int ob0 = late ? NSEG : p.ob0, obn = p.ob1 - p.ob0, fa = F_A + (late ? 24 : 0) + ct * 4;
  auto seg = [=](int i) { return min(i < ob0 ? i : i + obn, NSEG - 1); };
  auto lda = [&](In& in, int sg) {
    in.ok = seen > sg;
    const unsigned l8 = in.ok ? l * 8 : 0x3F000000u;
#pragma unroll
    for (int kq = 0; kq < 4; ++kq) {
      const unsigned o = bk.at(B_AP + ((sg * 6 + ct) * 4 + kq) * 8192 + w * 2048);
      in.a[kq] = ld128(bk.rs, 2 * l8, o);
      in.b[kq] = __builtin_amdgcn_raw_buffer_load_b64(bk.rs, l8, o + 1024, 16);
    }
  };
  auto step = [&](In& in, In& n2, int k) {
    const int sg = seg(k);
    if (!in.ok) {
      wait_ge_n<2>(cx, fa, 1, 4, sg + 1, seen);
      lda(in, sg);
#pragma unroll
      for (int i = 0; i < 4; ++i) asm volatile("" : "+v"(in.a[i]), "+v"(in.b[i]));
    }
    ldb(n2, seg(k + 2 * ds));
    lda(n2, seg(k + 2 * ds));
    SB();
#pragma unroll
    for (int i = 0; i < 3; ++i) {
      float o[4], b[4];
      const u32x2 xv = perm(pin, in.x[i]);
#pragma unroll
      for (int j = 0; j < 4; ++j) {
        auto part = [&](int kq) { const unsigned u = i < 2 ? in.a[kq][2 * (i & 1) + (j >> 1)] : in.b[kq][j >> 1]; return (j & 1) ? hi16(u) : lo16(u); };
        const float av = (part(0) + part(1)) + (part(2) + part(3));
        const float y = (j & 1) ? hi16(in.y[i][j >> 1]) : lo16(in.y[i][j >> 1]);
        const float xf = (j & 1) ? hi16(xv[j >> 1]) : lo16(xv[j >> 1]), df = (j & 1) ? hi16(dv[i][j >> 1]) : lo16(dv[i][j >> 1]);
        o[j] = rbf(in.g * rbf(av));
        b[j] = rbf(1.0f - in.g) * rbf(y + rbf(xf * df));
      }
      u32x2 bp = {pk2(b[0], b[1]), pk2(b[2], b[3])};
      asm volatile("" : "+v"(bp));
#pragma unroll
      for (int j = 0; j < 4; ++j) o[j] += (j & 1) ? hi16(bp[j >> 1]) : lo16(bp[j >> 1]);
      __builtin_amdgcn_raw_buffer_store_b64(perm(pout, (u32x2){pk2(o[0], o[1]), pk2(o[2], o[3])}), rout, ro + 32 * i, sg * (32 * DM * 2), OUT_AUX);
    }
    return seg(k + ds) < s1;
  };
  wait_all<30>(cx, F_W, 1);
  In a, b, c;
  wait_ge_n<2>(cx, fa, 1, 4, 1, seen);
  ldb(a, seg(s0)); lda(a, seg(s0));
  ldb(b, seg(s0 + ds)); lda(b, seg(s0 + ds));
  for (int sg = s0; step(a, c, sg) && step(b, a, sg + ds) && step(c, b, sg + 2 * ds); sg += 3 * ds) {}
}

__device__ __forceinline__ void gate_rows(const Params& p, const Ctx& cx, const Bulk& bk, int blk) {
  const int l = lane_id(), n = l & 15, g = l >> 4;
  const rsrc_t rx = mk_rsrc(p.x, (size_t)SL * DM * 2), rg = mk_rsrc(p.gw, DM * 2);
  const int t = blk * 16 + g;
  u32x4 gw[4], xv[4][4];
  u32x2 gt, xt[4];
#pragma unroll
  for (int k = 0; k < 4; ++k) gw[k] = __builtin_amdgcn_raw_buffer_load_b128(rg, n * 16, k * 256, 0);
  gt = __builtin_amdgcn_raw_buffer_load_b64(rg, n * 8, 1024, 0);
#pragma unroll
  for (int r = 0; r < 4; ++r) {
#pragma unroll
    for (int k = 0; k < 4; ++k) xv[r][k] = __builtin_amdgcn_raw_buffer_load_b128(rx, t * (DM * 2) + n * 16, r * 4 * (DM * 2) + k * 256, 0);
    xt[r] = __builtin_amdgcn_raw_buffer_load_b64(rx, t * (DM * 2) + n * 8, r * 4 * (DM * 2) + 1024, 0);
  }
  SB();
#pragma unroll
  for (int r = 0; r < 4; ++r) {
    float acc = 0.f;
    auto term = [&](unsigned a, unsigned b) { const unsigned u = pk2(lo16(a) * lo16(b), hi16(a) * hi16(b)); acc += lo16(u) + hi16(u); };
#pragma unroll
    for (int k = 0; k < 4; ++k)
#pragma unroll
      for (int j = 0; j < 4; ++j) term(xv[r][k][j], gw[k][j]);
#pragma unroll
    for (int j = 0; j < 2; ++j) term(xt[r][j], gt[j]);
#pragma unroll
    for (int m = 8; m >= 1; m >>= 1) acc += __shfl_xor(acc, m);
    const float gg = rbf(1.0f / (1.0f + expf(-rbf(acc))));
    if (n == 0) __builtin_amdgcn_raw_buffer_store_b32(__float_as_uint(gg), bk.rs, (t + 4 * r) * 4, bk.at(B_G), 16);
  }
  WAIT_VM(0);
  if (l == 0) flag_put(cx, flag_ptr(p.ws, F_G) + blk, 1u);
}

__device__ __forceinline__ void att_role(const Params& p, unsigned tag, int w, int h, int sa, int sb) {
  CODE_PAD(PAD_ATT);
  char* const ws = p.ws;
  const int l = lane_id(), n = l & 15, g = l >> 4;
  const Ctx cx = {ws, tag};
  Bulk bk;
  bk.init(cx, 4 + (h & 3));
  const unsigned l16 = l * 16, vmo = (n + 16 * (((g & 1) << 1) | (g >> 1))) * 16;
  auto soft = [&](const f32x4* st, int qt, u32x4& px, u32x2& pm) {
    const int nv = qt ? 10 : 6;
    float v[10] = {(g & 1) ? st[0][2] : st[0][0], (g & 1) ? st[0][3] : st[0][1]};
#pragma unroll
    for (int j = 0; j < 4; ++j) {
      v[2 + j] = qt || 4 * g + j <= n ? st[1][j] : -__builtin_inff();
      v[6 + j] = 4 * g + j <= n ? st[2][j] : -__builtin_inff();
    }
    float mx = v[0];
#pragma unroll
    for (int j = 1; j < nv; ++j) mx = fmaxf(mx, v[j]);
    const float c = 0.08838834764831845f * 1.4426950408889634f, nx = -red_max_g(mx) * c;
    float sum = 0.f;
#pragma unroll
    for (int j = 0; j < nv; ++j) { v[j] = __builtin_amdgcn_exp2f(__builtin_fmaf(v[j], c, nx)); sum += v[j]; }
    const float inv = __builtin_amdgcn_rcpf(red_sum_g(sum));
    px = (u32x4){pk2(v[2] * inv, v[3] * inv), pk2(v[4] * inv, v[5] * inv), qt ? pk2(v[6] * inv, v[7] * inv) : 0u, qt ? pk2(v[8] * inv, v[9] * inv) : 0u};
    pm = (u32x2){pk2(v[0] * inv, v[1] * inv), 0u};
  };
  int seenp = 0, seenm = 0;
  auto wait_proj = [&](int target) {
    unsigned spins = 0;
    while (seenp < target) {
      const int v = (int)flag_get(cx, bk.proj_flag(cx, min(l, 3), h)), v2 = __builtin_amdgcn_readlane(v, 3);
      seenp = min(min(max(__builtin_amdgcn_readlane(v, 0), K_FIRST), __builtin_amdgcn_readlane(v, 1)), min(__builtin_amdgcn_readlane(v, 2), v2));
      if (seenp < target) { __builtin_amdgcn_s_sleep(4); if (spin_fail(spins, cx)) break; }
    }
    __builtin_amdgcn_fence(__ATOMIC_ACQUIRE, "wavefront");
  };
  GAS u64* const fm = flag_ptr(ws, F_M + 2 * h + ((l >> 1) & 1)) + (l & 1 & ~(l >> 1));
  auto min8 = [&](u64 f) {
    int v = (unsigned)(f >> 32) == cx.tag ? (int)(unsigned)f : 0;
#pragma unroll
    for (int m = 1; m < 4; m <<= 1) v = min(v, __shfl_xor(v, m));
    return __builtin_amdgcn_readfirstlane(v);
  };
  auto wait_m = [&](int target) {
    unsigned spins = 0;
    while (seenm < target) {
      seenm = min8(__hip_atomic_load(fm, RLX_AGENT));
      if (seenm < target) { __builtin_amdgcn_s_sleep(2); if (spin_fail(spins, cx)) break; }
    }
    __builtin_amdgcn_fence(__ATOMIC_ACQUIRE, "wavefront");
  };
  bf16x8 qn[4][2], kx[4][2], vn[8];
  auto ahead = [&](int s) {
    const unsigned so = (s * NH + h) * 8192, oq = bk.at(B_Q + so), ok = bk.at(B_K + so), ov = bk.at(B_V + so);
#pragma unroll
    for (int k = 0; k < 8; ++k) {
      qn[k >> 1][k & 1] = ldfrag(bk.rs, l16, oq + k * 1024);
      kx[k >> 1][k & 1] = ldfrag(bk.rs, l16, ok + k * 1024);
    }
#pragma unroll
    for (int et = 0; et < 8; ++et) vn[et] = ldfrag(bk.rs, l16, ov + et * 1024);
  };
  bf16x8 km[4];
  u32x2 vm[4];
  auto ldm = [&](int s) {
    const unsigned om = near_head(B_K, h) ^ (~bk.w13 & 8192u) ^ ((s ? s - 1 : 8192) << 14);
#pragma unroll
    for (int ks = 0; ks < 4; ++ks) km[ks] = ldfrag(bk.rs, l16, om + ks * 1024);
#pragma unroll
    for (int k = 0; k < 4; ++k) vm[k] = __builtin_amdgcn_raw_buffer_load_b64(bk.rs, vmo, om + 4096 + k * 1024, 16);
  };
  GAS u64* const fo = flag_ptr(ws, F_O + h) + w;
  if (l == 0) flag_put(cx, fo, sa + w);
  wait_proj(sa + w + 1);
  ahead(sa + w);
  wait_m(sa + w + 1);
  ldm(sa + w);
  bool pend = false, fq = false, gate = true;
  u64 fw = 0;
  for (int s = sa + w; s < sb; s += 4) {
    bf16x8 vx[8];
    u32x2 vc[4];
    f32x4 st[2][3] = {};
#pragma unroll
    for (int ks = 0; ks < 4; ++ks) { st[0][1] = MF(kx[ks][0], qn[ks][0], st[0][1]); st[1][1] = MF(kx[ks][0], qn[ks][1], st[1][1]); st[1][2] = MF(kx[ks][1], qn[ks][1], st[1][2]); }
#pragma unroll
    for (int ks = 0; ks < 4; ++ks) { st[0][0] = MF(km[ks], qn[ks][0], st[0][0]); st[1][0] = MF(km[ks], qn[ks][1], st[1][0]); vc[ks] = vm[ks]; vx[ks] = vn[ks]; vx[ks + 4] = vn[ks + 4]; }
    SB();
    if (fq) {
      seenm = max(seenm, min8(fw));
      __builtin_amdgcn_fence(__ATOMIC_ACQUIRE, "wavefront");
    }
    const bool more = s + 4 < sb, havep = more && seenp > s + 4, have = havep && seenm > s + 4;
    fq = have;
    if (have) {
      ldm(s + 4);
      fw = __hip_atomic_load(fm, RLX_AGENT);
    }
    if (havep) ahead(s + 4);
    SB();
    const unsigned oo = bk.at(B_O + (s * NH + h) * 8192);
#pragma unroll
    for (int qt = 0; qt < 2; ++qt) {
      u32x4 px;
      u32x2 pm;
      soft(st[qt], qt, px, pm);
      const u32x4 pxs = {px[2], px[3], px[0], px[1]};
      f32x4 o[8];
#pragma unroll
      for (int et = 0; et < 8; ++et) {
        const f32x4 z = {};
        o[et] = MF(vx[et], __builtin_bit_cast(bf16x8, (et & 1) ? pxs : px), z);
        o[et] = MF(zext8((u32x2){vc[et >> 1][et & 1], 0u}), zext8(pm), o[et]);
      }
      if (qt == 0 && pend) {
        if (havep) WAIT_VM(24); else WAIT_VM(0);
        if (l == 0) flag_put(cx, fo, s);
      }
#pragma unroll
      for (int k = 0; k < 4; ++k) st128(pack8(o[2 * k], o[2 * k + 1]), bk.rs, l16, oo + qt * 1024 + k * 2048);
    }
    SB();
    pend = true;
    if (!have) {
      pend = false;
      WAIT_VM(0);
      if (l == 0) flag_put(cx, fo, s + 4);
      if (gate && (s >= 20 || !more)) { gate_rows(p, cx, bk, 4 * h + w); gate = false; }
      if (more && !havep) { wait_proj(s + 5); ahead(s + 4); }
      if (more) { wait_m(s + 5); ldm(s + 4); }
    }
  }
}

__device__ __forceinline__ void xprep_role(const Params& p, unsigned tag, int w, int h0) {
  const int l = lane_id(), n = l & 15, g = l >> 4, qt = w & 1, par = w >> 1;
  const Ctx cx = {p.ws, tag};
  Bulk bk;
  bk.init(cx, 4 + (h0 & 3));
  const unsigned l16 = l * 16;
  const unsigned xo = X_AT + ((bk.w13 ^ 8192u) & 8192u);
  for (int i = 0; i < NDIR * 9; ++i)
    st128((u32x4){~0u, ~0u, ~0u, ~0u}, bk.rs, g * 16384 + (h0 + 8 * w) * 256 + n * 16, (bk.at(B_O + (i / 9) * 524288) ^ 8192u) + (i % 9) * 65536);
  for (unsigned spins = 0; __any(flag_get(cx, (l & 4) ? flag_ptr(p.ws, F_Q + h0 + 8 * (l & 3)) : flag_ptr(p.ws, F_Y) + 35 + h0 + 8 * (l & 3)) == 0u) && !spin_fail(spins, cx);) __builtin_amdgcn_s_sleep(8);
  __builtin_amdgcn_fence(__ATOMIC_ACQUIRE, "wavefront");
#pragma unroll 1
  for (int h = h0; h < NH; h += 8) {
  bf16x8 qq[NDIR / 2][4], kk[NDIR / 2][8];
#pragma unroll
  for (int i2 = 0; i2 < NDIR; i2 += 2) {
    const unsigned so = ((SDIR + i2 + par) * NH + h) * 8192, oq = bk.at(B_Q + so) + qt * 1024, ok = bk.at(B_K + so);
#pragma unroll
    for (int ks = 0; ks < 4; ++ks) {
      qq[i2 >> 1][ks] = ldfrag(bk.rs, l16, oq + ks * 2048);
      kk[i2 >> 1][2 * ks] = ldfrag(bk.rs, l16, ok + ks * 2048); kk[i2 >> 1][2 * ks + 1] = ldfrag(bk.rs, l16, ok + ks * 2048 + 1024);
    }
  }
  SB();
#pragma unroll
  for (int i2 = 0; i2 < NDIR; i2 += 2) {
    const int i = i2 + par;
    const unsigned oc = (bk.at(B_O + ((SDIR + i) * NH + h) * 8192) ^ 8192u) + qt * 1024;
    f32x4 a = {}, b = {};
#pragma unroll
    for (int ks = 0; ks < 4; ++ks) {
      const bf16x8 q = qq[i2 >> 1][ks];
      st128(__builtin_bit_cast(u32x4, q), bk.rs, l16, oc + ks * 2048);
      a = MF(kk[i2 >> 1][2 * ks], q, a); b = MF(kk[i2 >> 1][2 * ks + 1], q, b);
    }
    float mx = -__builtin_inff(), sum = 0.f;
#pragma unroll
    for (int j = 0; j < 4; ++j) {
      a[j] = 4 * g + j <= 16 * qt + n ? a[j] : -__builtin_inff(); b[j] = 16 + 4 * g + j <= 16 * qt + n ? b[j] : -__builtin_inff();
      mx = fmaxf(mx, fmaxf(a[j], b[j]));
    }
    const float c = 0.08838834764831845f * 1.4426950408889634f, nx = -red_max_g(mx) * c;
#pragma unroll
    for (int j = 0; j < 4; ++j) { a[j] = __builtin_amdgcn_exp2f(__builtin_fmaf(a[j], c, nx)); b[j] = __builtin_amdgcn_exp2f(__builtin_fmaf(b[j], c, nx)); sum += a[j] + b[j]; }
    sum = red_sum_g(sum);
    const unsigned eo = xo + 0x100000 + (i * 4 + 2 * qt + (n >> 3)) * 32768 + (h >> 4) * 16384 + (h & 15) * 512 + (g >> 1) * 128 + (n & 7) * 16 + (g & 1) * 8;
    __builtin_amdgcn_raw_buffer_store_b64((u32x2){pk2(a[0], a[1]), pk2(a[2], a[3])}, bk.rs, eo, 0, 16);
    __builtin_amdgcn_raw_buffer_store_b64((u32x2){pk2(b[0], b[1]), pk2(b[2], b[3])}, bk.rs, eo, 256, 16);
    if (g == 0) __builtin_amdgcn_raw_buffer_store_b64((u32x2){__float_as_uint(nx), __float_as_uint(sum)}, bk.rs, h * 256 + (16 * qt + n) * 8, xo + 0x80000 + i * 65536, 16);
    __builtin_amdgcn_raw_buffer_store_b64((u32x2){0xFFFFFFFFu, 0xFFFFFFFFu}, bk.rs, (2 * qt + (g >> 1)) * 16384 + ((g & 1) ? 4096 + c_line(h) : h * 128) + n * 8, xo + i * 65536, 16);
  }
  }
  WAIT_VM(0);
  BARRIER();
  if (l == 0) flag_put(cx, flag_ptr(p.ws, F_S + h0 + 8 * w), 1u);
}

struct OutIn {
  unsigned l16, rw, rr, fo, wv0, wv1, wv2, tro, l8, rw0, rr0;
  const void *ws, *Wo;
  unsigned w, tag, o_w, o_o, o_a, f_a, o_ctl, ns;
};
__device__ __forceinline__ void op_role(const Params& p, unsigned tag, int w, int ct, int kq, int grp, bool late) {
  const Ctx cx = {p.ws, tag};
  Bulk bk;
  bk.init(cx, grp);
  const int l = lane_id(), g = l >> 4, q4 = (l >> 2) & 3, p4 = l & 3, h0 = kq * 8 + w * 2;
  char* const base = comm_base(p.ws) - CO;
  const unsigned dl = (unsigned)(p.ws - base);
  OutIn oi;
  oi.l16 = l * 16; oi.l8 = l * 8;
  oi.rw = 65536 + w * 12288 + l * 16;
  oi.rr = 65536 + w * 3072 + l * 16;
  oi.fo = dl + (F_O + h0 + ((l >> 2) & 1)) * 128 + (l & 3) * 8;
  auto piece = [&](int i) { const int idx = i * 64 + l, row = idx / 12; return row * (DM * 2) + (idx - row * 12) * 16; };
  oi.wv0 = piece(0); oi.wv1 = piece(1); oi.wv2 = piece(2);
  oi.tro = w * 24576 + (4 * g + q4) * 192 + p4 * 8;
  oi.ws = base; oi.Wo = p.Wo; oi.w = w; oi.tag = __builtin_amdgcn_readfirstlane(tag); oi.o_ctl = dl;
  oi.ns = __builtin_amdgcn_readfirstlane(late ? p.ob0 | p.ob1 << 8 : p.ob0 ? p.ob0 << 8 | p.ob1 << 16 | SDIR << 24 : p.ob1 | SDIR << 8);
  oi.rw0 = oi.rw; oi.rr0 = oi.rr;
  oi.o_w = __builtin_amdgcn_readfirstlane((kq * 1024 + w * 256) * (DM * 2) + ct * 192);
  oi.o_o = __builtin_amdgcn_readfirstlane(bk.w13 + 2 * B_O + h0 * 16384);
  oi.o_a = __builtin_amdgcn_readfirstlane(bk.w13 + 2 * B_AP + (ct * 4 + kq) * 16384 + w * 2048);
  oi.f_a = __builtin_amdgcn_readfirstlane(w ? 0x7F000000u : dl + (F_A + ct * 4 + kq) * 128 + (late ? 24 * 128 : 0));
  asm volatile(O_ASM_TEXT : O_ASM_VOPS : O_ASM_SOPS : O_ASM_CLOB);
}

typedef __attribute__((ext_vector_type(16))) float f32x16;
constexpr int S4_PRM = 0;
constexpr int S4_KD = S4_PRM + 2560;
constexpr int S4_KH = S4_KD + 2624;
constexpr int S4_U = S4_KH + 5200;
constexpr int S4_USTR = 4352 + 64;
constexpr int S4_WSZ = S4_U + 4 * S4_USTR;
constexpr int S4_RAW = 4 * S4_WSZ;

#define S4M(i) do {} while (0)
struct S4In { u32x2 u[8]; unsigned la, ai, cr, ci, dt; };
__device__ __forceinline__ float s4_params(LAS char* my, const S4In& in) {
  const int l = lane_id();
  const u16 pl_[2] = {(u16)in.la, (u16)(in.la >> 16)}, pa_[2] = {(u16)in.ai, (u16)(in.ai >> 16)}, pr_[2] = {(u16)in.cr, (u16)(in.cr >> 16)}, pi_[2] = {(u16)in.ci, (u16)(in.ci >> 16)};
  const float dt = rbf(expf(bf2f((u16)in.dt)));
  f32x4 pr[2];
#pragma unroll
  for (int k = 0; k < 2; ++k) {
    const float a = -rbf(expf(bf2f(pl_[k]))) * dt;
    const float b = bf2f(pa_[k]) * dt;
    const float c_hi = 0.15915494f, c_lo = (float)(0.15915494309189535 - (double)0.15915494f);
    const float bh = b * c_hi;
    const float bl = __builtin_fmaf(b, c_hi, -bh) + b * c_lo;
    const float cr = bf2f(pr_[k]), ci = bf2f(pi_[k]);
    pr[k] = (f32x4){a * 1.4426950408889634f, bh, bl, log2f(hypotf(cr, ci))};
    *(LAS float*)(my + S4_PRM + 2048 + (l + 64 * k) * 4) = -atan2f(ci, cr) * c_hi;
  }
  float top = fmaxf(fmaxf(pr[0][3], pr[1][3]), -100.f);
#pragma unroll
  for (int m = 1; m < 64; m <<= 1) top = fmaxf(top, __shfl_xor(top, m));
  top = ceilf(top);
#pragma unroll
  for (int k = 0; k < 2; ++k) { pr[k][3] += 14.f - top; *(LAS f32x4*)(my + S4_PRM + (l + 64 * k) * 16) = pr[k]; }
  return exp2f(top - 27.f) * dt;
}
typedef __attribute__((ext_vector_type(2))) _Float16 f16x2;
typedef __attribute__((ext_vector_type(8))) _Float16 f16x8;
__device__ __forceinline__ void s4_modes(LAS char* my, int q0, int q1, f32x4 (&D)[3]) {
  const int l = lane_id(), i = l & 15, kk = l >> 4;
  const f32x2 pe[2] = {{(float)(8 * i), (float)(128 * (i >> 3) + (i & 7))},
                       {(float)(i < 8 ? 256 + 128 * (i >> 2) + 2 * (i & 3) : 2048), (float)(i < 8 ? 512 + 128 * (i >> 1) + 4 * (i & 1) : 128 * i)}};
  const f32x2 pp[2] = {{-pe[0][0], pe[0][1]}, pe[1]};
#pragma unroll 1
  for (int q = q0; q < q1; ++q) {
    u32x4 op[4];
    const f32x4 pc = *(LAS f32x4*)(my + S4_PRM + 2048 + (4 * q + kk) * 16);
#pragma unroll
    for (int j = 0; j < 4; ++j) {
      const f32x4 pr = *(LAS f32x4*)(my + S4_PRM + (16 * q + 4 * kk + j) * 16);
#pragma unroll
      for (int m = 0; m < 2; ++m) {
        const f32x2 ea = __builtin_elementwise_fma((f32x2){pr[0], pr[0]}, pe[m], (f32x2){m ? 14.f : pr[3], 14.f});
        const f32x2 t = pp[m] * pr[1];
        f32x2 fr = __builtin_elementwise_fma((f32x2){pr[1], pr[1]}, pp[m], -(f32x2){__builtin_rintf(t[0]), __builtin_rintf(t[1])});
        fr = __builtin_elementwise_fma((f32x2){pr[2], pr[2]}, pp[m], fr);
        if (!m) fr[0] += pc[j];
#pragma unroll
        for (int h = 0; h < 2; ++h) {
          const f32x2 v = (f32x2){__builtin_amdgcn_cosf(fr[h]), __builtin_amdgcn_sinf(fr[h])} * __builtin_amdgcn_exp2f(ea[h]);
          op[2 * m + h][j] = __builtin_bit_cast(unsigned, __builtin_convertvector(v, f16x2));
        }
      }
    }
#pragma unroll
    for (int m = 0; m < 3; ++m) D[m] = __builtin_amdgcn_mfma_f32_16x16x32_f16(__builtin_bit_cast(f16x8, op[m + 1]), __builtin_bit_cast(f16x8, op[0]), D[m], 0, 0, 0);
  }
}
__device__ __forceinline__ void s4d_task(const Ctx& cx, const Bulk& bk, unsigned yflip, int gentle, int w, int task, char* smem, const S4In& in, char* ws, int q0, f32x4 (&D)[3], float ksc) {
  const int l = lane_id(), tid = w * 64 + l;
  LAS char* const lds = (LAS char*)smem;
  LAS char* const my = lds + w * S4_WSZ;
  S4M(2);
  s4_modes(my, q0, 8, D);
  {
    const int i = l & 15, kk = l >> 4;
#pragma unroll
    for (int r = 0; r < 4; ++r) {
      const int row = 4 * kk + r;
      *(LAS float*)(my + S4_KD + (128 * (row >> 3) + (row & 7) + 8 * i) * 4) = ksc * D[0][r];
      *(LAS float*)(my + S4_KD + (row < 8 ? 256 + 64 * kk + r + 4 * i : 640 + i) * 4) = ksc * D[1][r];
      *(LAS float*)(my + S4_KD + (row < 8 ? 384 + 32 * (row >> 1) + (row & 1) + 2 * i : 384 + 16 * row + i) * 4) = ksc * D[2][r];
    }
  }
  S4M(3);
#pragma unroll
  for (int k = 0; k < 2; ++k)
#pragma unroll
    for (int j = 0; j < 8; j += 4) *(LAS u32x4*)(lds + S4_RAW + k * 8208 + tid * 32 + j * 4) = (u32x4){in.u[j][k], in.u[j + 1][k], in.u[j + 2][k], in.u[j + 3][k]};
  BARRIER();
  u32x2 r[11];
#pragma unroll
  for (int k = 0; k < 2; ++k)
#pragma unroll
    for (int j = 0; j < 12; j += 4) {
      const u32x4 v = *(LAS u32x4*)(lds + S4_RAW + k * 8208 + tid * 32 + j * 4);
#pragma unroll
      for (int i = 0; i < 4; ++i) if (j + i < 11) r[j + i][k] = v[i];
    }
#pragma unroll
  for (int ch = 0; ch < 4; ++ch) {
    unsigned pr[10];
#pragma unroll
    for (int j = 0; j < 10; ++j) pr[j] = __builtin_amdgcn_perm(r[j + 1][ch >> 1], r[j][ch >> 1], (ch & 1) ? 0x07060302u : 0x05040100u);
    if (tid == 255) { pr[7] &= 0xFFFFu; pr[8] = 0u; pr[9] = 0u; }
    LAS char* ub = lds + ch * S4_WSZ + S4_U + 64 + tid * 16;
#pragma unroll
    for (int sft = 0; sft < 4; ++sft) *(LAS u32x4*)(ub + sft * S4_USTR) = (u32x4){pr[sft], pr[sft + 2], pr[sft + 4], pr[sft + 6]};
    if (tid < 4) {
#pragma unroll
      for (int sft = 0; sft < 4; ++sft) {
        const u32x4 v = {0u, 0u, sft == 3 ? pr[0] << 16 : 0u, sft == 0 ? 0u : sft == 1 ? pr[0] << 16 : pr[sft - 2]};
        *(LAS u32x4*)(lds + ch * S4_WSZ + S4_U + sft * S4_USTR + (3 - tid) * 16) = tid == 0 ? v : (u32x4){0u, 0u, 0u, 0u};
      }
    }
  }
  {
    if (l < 5) {
      *(LAS u32x4*)(my + S4_KH + l * 16) = (u32x4){0u, 0u, 0u, 0u};
    }
#pragma unroll
    for (int k = 0; k < 4; ++k) {
      const int grp = l + 64 * k;
      float v[8];
#pragma unroll
      for (int j = 0; j < 8; j += 2) {
        const unsigned c = pk2((float)(8 * grp + j), (float)(8 * grp + j + 1)) - 0x42804280u;
        const bool low = k == 0 && grp < 16;
        v[j] = *(LAS float*)(my + S4_KD + (low ? 8 * grp + j : c & 0xFFFFu) * 4);
        v[j + 1] = *(LAS float*)(my + S4_KD + (low ? 8 * grp + j + 1 : c >> 16) * 4);
      }
      *(LAS u32x4*)(my + S4_KH + 80 + (grp >> 2) * 80 + (grp & 3) * 16) = (u32x4){pk2(v[7], v[6]), pk2(v[5], v[4]), pk2(v[3], v[2]), pk2(v[1], v[0])};
    }
  }
  __syncthreads();
  S4M(4);
  {
    const int r = l & 31, hh = l >> 5, sft = (r + 1) & 3;
    unsigned ua = w * S4_WSZ + S4_U + sft * S4_USTR + (r + 25 - sft - 8 * hh - 16) * 2;
    const unsigned kz = w * S4_WSZ + S4_KH + hh * 16;
    unsigned kb1 = kz + (33 + r) * 80;
    unsigned kb0 = kz + (1 + r) * 80;
    f32x16 acc0 = {}, acc1 = {};
    unsigned ub = ua + 72;
    asm volatile("" : "+v"(ub));
    struct Ops { bf16x8 a0, a1, b0, b1; };
    auto ld = [&](int ks) {
      Ops o;
      const unsigned uo = ua + (1 - ks) * 32;
      o.a0 = cat2(*(LAS bf16x4*)(lds + uo), *(LAS bf16x4*)(lds + uo + 8));
      o.a1 = cat2(*(LAS bf16x4*)(lds + uo + 2048), *(LAS bf16x4*)(lds + uo + 2056));
      o.b0 = *(LAS bf16x8*)(lds + kb0 + ks * 32);
      o.b1 = *(LAS bf16x8*)(lds + kb1 + ks * 32);
      return o;
    };
    auto step = [&](const Ops& o, Ops& nx, int ks) {
      const unsigned uo = ua + (1 - ks) * 32, up = ub + (1 - ks) * 32;
      acc1 = __builtin_amdgcn_mfma_f32_32x32x16_bf16(o.a0, o.b1, acc1, 0, 0, 0); SB();
      nx.a0 = cat2(*(LAS bf16x4*)(lds + uo), *(LAS bf16x4*)(lds + up)); nx.b1 = *(LAS bf16x8*)(lds + kb1 + ks * 32); SB();
      acc0 = __builtin_amdgcn_mfma_f32_32x32x16_bf16(o.a0, o.b0, acc0, 0, 0, 0); SB();
      nx.b0 = *(LAS bf16x8*)(lds + kb0 + ks * 32);
      nx.a1 = cat2(*(LAS bf16x4*)(lds + uo + 2048), *(LAS bf16x4*)(lds + up + 2048));
      if (ks) {
        ub += 64;
        ua += 64;
        kb1 -= 80;
        kb0 = max(kb0 - 80, kz);
      }
      SB();
      acc1 = __builtin_amdgcn_mfma_f32_32x32x16_bf16(o.a1, o.b0, acc1, 0, 0, 0); SB();
    };
    Ops e0 = ld(0), e1 = ld(1), o0, o1;
    ua += 64; kb1 -= 80; kb0 = max(kb0 - 80, kz);
#pragma unroll 1
    for (int D = 0; D < 32; D += 2) {
      step(e0, o0, 0);
      step(e1, o1, 1);
      step(o0, e0, 0);
      step(o1, e1, 1);
      sleep_n(gentle);
    }
    S4M(5);
#pragma unroll
    for (int j = 0; j < 4; ++j) {
      const unsigned off = (32 * r + 8 * j + 4 * hh) * 2;
      *(LAS u32x2*)(my + off) = (u32x2){pk2(acc0[4 * j], acc0[4 * j + 1]), pk2(acc0[4 * j + 2], acc0[4 * j + 3])};
      *(LAS u32x2*)(my + off + 2048) = (u32x2){pk2(acc1[4 * j], acc1[4 * j + 1]), pk2(acc1[4 * j + 2], acc1[4 * j + 3])};
    }
    __syncthreads();
#pragma unroll
    for (int j = 0; j < 2; ++j) {
      u32x2 c[4];
#pragma unroll
      for (int k = 0; k < 4; ++k) c[k] = *(LAS u32x2*)(lds + k * S4_WSZ + (j * 256 + tid) * 8);
      const unsigned o = bk.at(B_YT + task * 16384 + j * 8192) ^ yflip;
#pragma unroll
      for (int k = 0; k < 2; ++k)
        st128((u32x4){__builtin_amdgcn_perm(c[1][k], c[0][k], 0x05040100u), __builtin_amdgcn_perm(c[3][k], c[2][k], 0x05040100u),
                      __builtin_amdgcn_perm(c[1][k], c[0][k], 0x07060302u), __builtin_amdgcn_perm(c[3][k], c[2][k], 0x07060302u)}, bk.rs, tid * 32 + k * 16, o);
    }
  }
  __syncthreads();
  S4M(6);
}

__device__ __forceinline__ void warm_l2(const u16* W, unsigned off, bool rows256, int w) {
  const rsrc_t rw = mk_rsrc(W, (size_t)DM * HD * 2);
  const int l = lane_id();
  const unsigned vo = rows256 ? (l >> 4) * (HD * 2) + (l & 15) * 16 : l * 16, st = rows256 ? 4 * HD * 2 : 1024;
  u32x4 v[36];
#pragma unroll
  for (int i = 0; i < 36; ++i) v[i] = __builtin_amdgcn_raw_buffer_load_b128(rw, vo, off + (i * 4 + w) * st, 0);
#pragma unroll
  for (int i = 0; i < 36; ++i) asm volatile("" :: "v"(v[i]));
}

__device__ __forceinline__ void x_ahead(const Params& p, int w, int idx) {
  const rsrc_t rx = mk_rsrc(p.x, (size_t)SL * DM * 2);
  for (int i = 0; i < p.xpre; ++i) dma16(rx, lane_id() * 16, ((((idx - 35) >> 2) * 4 + w) + i * 32) * 1024, LDS_BYTES - 1024);
}
__device__ __forceinline__ unsigned s4_role(const Params& p, int w, int idx, char* smem) {
  CODE_PAD(PAD_S4);
  char* const ws = p.ws;
  const int l = lane_id(), tid = w * 64 + l;
  const bool near = idx >= 8 && idx < 35;
  const int r_ = idx - 8, t0 = 5 * r_ + (r_ + 2) / 3;
  const int t1 = t0 + 5 + (r_ % 3 == 0) - p.s4skip;
  const rsrc_t rx = mk_rsrc(p.x, (size_t)SL * DM * 2);
  S4In in[6];
#pragma unroll
  for (int k = 0; k < 6; ++k) {
    const int d = min(t0 + k, t1 - 1) * 4 + w, o = d * (NST / 2) + l;
    in[k].la = ((const unsigned*)p.lar)[o]; in[k].ai = ((const unsigned*)p.aim)[o]; in[k].cr = ((const unsigned*)p.cre)[o]; in[k].ci = ((const unsigned*)p.cim)[o];
    in[k].dt = __builtin_amdgcn_raw_buffer_load_b16(mk_rsrc(p.ldt, DM * 2), d * 2, 0, 0);
  }
  const unsigned gen = gen_load(ws);
  LAS char* const my = (LAS char*)smem + w * S4_WSZ;
  float ksc = s4_params(my, in[0]);
  f32x4 D[3] = {};
  const int qs = near ? p.s4q : p.s4f;
  s4_modes(my, 0, qs, D);
  const Ctx cx = {ws, __builtin_amdgcn_readfirstlane(gen) + 1u};
  Bulk bk;
  bk.init(cx, idx & 7);
  for (int m = 4 * t0; m < 4 * t1; ++m) st128((u32x4){0u, 0u, 0u, 0u}, bk.rs, tid * 16, bk.at(B_YT + m * 4096) ^ 8192u);
#pragma unroll
  for (int m = 0; m < 6; ++m)
#pragma unroll
    for (int j = 0; j < 8; ++j) in[m].u[j] = __builtin_amdgcn_raw_buffer_load_b64(rx, t0 + m < t1 ? (tid * 8 + j) * (DM * 2) : 0x7F000000u, (t0 + m) * 8, 0);
  {
    sleep_n((r_ % 3 ? r_ % 7 : 0) * p.stag);
  }
  s4_modes(my, qs, 8, D);
#pragma unroll 1
  for (int k = t0; k < t1; ++k) {
    s4d_task(cx, bk, near ? 8192u : 0u, near ? p.gentle : 0, w, k, smem, in[0], ws, k == t0 ? 8 : 0, D, ksc);
    in[0] = in[1]; in[1] = in[2]; in[2] = in[3]; in[3] = in[4]; in[4] = in[5];
    ksc = s4_params(my, in[0]);
    D[0] = D[1] = D[2] = (f32x4){0.f, 0.f, 0.f, 0.f};
  }
  WAIT_VM(0);
  BARRIER();
  if (tid == 0) flag_put(cx, flag_ptr(ws, F_Y) + idx, 1u);
  return cx.tag;
}
struct FinIn {
  unsigned sidx, cidx, l16, ae0, ae1, ae2, ae3, am, lw, lr, oo, d0, tq;
  float g[NDIR], b[NDIR], c[NDIR];
  const void *ws, *out;
  unsigned tag, o_u, o_e, o_x, o_va, o_tw, xa, xb, o_ctl, pa, w, f_w, pq;
};
__device__ __forceinline__ void watch_role(const Params& p, unsigned tag) {
  const int l = lane_id();
  const Ctx cx = {p.ws, tag};
  for (unsigned spins = 0; __any(flag_get(cx, flag_ptr(p.ws, F_Y) + min(8 + l, NS4 - 1)) == 0u) && !spin_fail(spins, cx);) __builtin_amdgcn_s_sleep(30);
  wait_all<30>(cx, F_G, 64);
  wait_all<30>(cx, F_G + 4, 64);
  unsigned spins = 0;
  while (__any(flag_get(cx, flag_ptr(p.ws, F_S + (l & 31))) == 0u) && !spin_fail(spins, cx)) __builtin_amdgcn_s_sleep(30);
  publish(cx, 0, F_W, 1);
  for (int i = 0; i < NDIR; ++i) {
    GAS u64* const f = flag_ptr(p.ws, F_U + (l & 31)) + (l >> 5) * 2;
    while (__any((int)min(flag_get(cx, f), flag_get(cx, f + 1)) <= SDIR + i) && !spin_fail(spins, cx)) __builtin_amdgcn_s_sleep(8);
    publish(cx, 0, F_W + 1 + i, 1);
  }
}
struct FinX { unsigned x[NDIR], d; };
__device__ __forceinline__ int fin_col(int T) { const int l = lane_id(); return T * 16 + 8 * ((l >> 3) & 1) + 2 * (l & 3); }
__device__ __forceinline__ int fin_row(int w) { const int l = lane_id(); return SDIR * 32 + 8 * w + 2 * (l >> 4) + ((l >> 2) & 1); }
__device__ __forceinline__ FinX fin_x(const Params& p, int w, int T) {
  FinX f;
#pragma unroll
  for (int i = 0; i < NDIR; ++i) f.x[i] = *(const unsigned*)(p.x + (size_t)(fin_row(w) + 32 * i) * DM + fin_col(T));
  f.d = *(const unsigned*)(p.dsk + fin_col(T));
  return f;
}
__device__ __forceinline__ void fin_role(const Params& p, unsigned tag, int w, int T, const FinX& fx) {
  char* const ws = p.ws;
  const int l = lane_id(), n = l & 15, g = l >> 4;
  const Ctx cx = {ws, tag};
  Bulk bk;
  bk.init(cx, T & 7);
  bk.pl.load(cx, T & 7);
  const Place& pl = bk.pl;
  FinIn fi;
  if (w == 0) wait_all<30>(cx, F_W, 1);
  BARRIER();
  const int d = fin_col(T), r0 = fin_row(w);
  float (&gg)[NDIR] = fi.g, (&b0)[NDIR] = fi.b, (&b1)[NDIR] = fi.c;
#pragma unroll
  for (int i = 0; i < NDIR; ++i) {
    const int t = r0 + 32 * i;
    gg[i] = __uint_as_float(__builtin_amdgcn_raw_buffer_load_b32(bk.rs, t * 4, bk.at(B_G), 16));
    const unsigned yv = __builtin_amdgcn_raw_buffer_load_b32(bk.rs, y_at(bk, d >> 2, t * 8 + (d & 2) * 2), 0, 16);
    const unsigned xv = fx.x[i], dv = fx.d;
    b0[i] = rbf(rbf(1.0f - gg[i]) * rbf(lo16(yv) + rbf(lo16(xv) * lo16(dv)))); b1[i] = rbf(rbf(1.0f - gg[i]) * rbf(hi16(yv) + rbf(hi16(xv) * hi16(dv))));
  }
#pragma unroll
  for (int i = 0; i < NDIR; ++i) asm volatile("" : "+v"(b0[i]), "+v"(b1[i]));
  char* const base = comm_base(ws) - CO;
  fi.sidx = (n & 8) ? 0xEEEEu : 0x4444u;
  fi.l16 = l * 16; fi.d0 = 0;
  fi.cidx = (n & 3) * 0x5555u;
  auto ae = [&](int j) { return g <= j ? j * 32768 + (n >> 3) * 512 + g * 128 + (n & 7) * 16 : 0x7F000000u; };
  fi.ae0 = ae(0); fi.ae1 = ae(1); fi.ae2 = ae(2); fi.ae3 = ae(3);
  fi.am = w * 1024 + l * 16;
  fi.lw = (w >> 1) * 512 + (2 * (g & 1) + (g >> 1)) * 128 + (n >> 1) * 16 + (w & 1) * 8 + (n & 1) * 4;
  fi.lr = w * 2048 + ((l >> 2) & 1) * 1024 + g * 128 + (((l >> 3) & 1) * 4 + (l & 3)) * 16;
  fi.oo = (r0 * DM + d) * 2;
  fi.f_w = (unsigned)(ws - base) + (F_W + 1) * 128;
  fi.ws = base; fi.out = p.out; fi.tag = __builtin_amdgcn_readfirstlane(tag); fi.o_ctl = (unsigned)(ws - base); fi.xa = pl.slot(SDIR - 1) ^ pl.slot(SDIR); fi.xb = pl.slot(SDIR) ^ pl.slot(SDIR + 1);
  fi.o_u = __builtin_amdgcn_readfirstlane(u_tile(T, w, 0) ^ (~bk.w13 & 8192u));
  fi.o_x = X_AT + ((bk.w13 ^ 8192u) & 8192u);
  fi.o_e = __builtin_amdgcn_readfirstlane(fi.o_x + 0x100000 + (w >> 1) * 16384 + (w & 1) * 4096);
  fi.o_va = __builtin_amdgcn_readfirstlane((bk.w13 ^ 8192u) + 2 * B_O + T * 16384 + w * 2048);
  fi.o_tw = __builtin_amdgcn_readfirstlane(CO + TW_OFF + (16384u ^ pl.dyn(T & 3) ^ pl.slot(SDIR - 1)));
  fi.tq = T == 0 || T >= 28 ? 0x7F000000u : CO + TW_OFF + 260 + (16384u ^ pl.dyn(T & 3) ^ pl.slot(l));
  fi.pq = p.qb | ((p.qb - p.qa) << 8);
  fi.pa = p.xt0 | (p.xt1 << 16); fi.w = w;
  asm volatile(F_ASM_TEXT : F_ASM_VOPS : F_ASM_SOPS : F_ASM_CLOB);
  if (T == 0 && w == 0 && lane_id() == 0) __hip_atomic_store((GAS unsigned*)flag_ptr(ws, F_GEN), tag, RLX_AGENT);
}

__device__ __forceinline__ void lines_ahead(const Params& p, int w, int h) {
  const int i = h * 4 + w, l = lane_id();
  const rsrc_t ro = mk_rsrc(p.out, (size_t)SL * DM * 2), rc = mk_rsrc(p.ws, CTL_BYTES), rb = mk_rsrc(comm_base(p.ws) - CO, BASE_BYTES);
  const unsigned z0 = __builtin_amdgcn_raw_buffer_load_b32(rc, l * 128, i * 8192, 16);
  const unsigned z1 = __builtin_amdgcn_raw_buffer_load_b32(rb, l * 128, (w & 2 ? PB + 2 * B_G : near_head(B_K, h) ^ (8192u << 14)) ^ ((w & 1) * 8192), 16);
  if (p.wl2 & 2)
    for (int k = 0; k < 18; ++k) st128((u32x4){0u, 0u, 0u, 0u}, ro, l * 16, (i * 18 + k) * 1024);
  asm volatile("" :: "v"(z0), "v"(z1));
}
__device__ __forceinline__ void code_ahead(int i, int n) {
  const char* pc;
  unsigned back, len;
  asm volatile("s_getpc_b64 %0\n.Lcp_%=:\n\ts_mov_b32 %1, .Lcp_%=-_Z6k_mega6Params\n\ts_mov_b32 %2, k_mega_end-_Z6k_mega6Params" : "=s"(pc), "=s"(back), "=s"(len));
  const rsrc_t rc = mk_rsrc(pc - back, len);
  for (unsigned o = i * 1024; o < len; o += n * 1024) dma16(rc, lane_id() * 16, o, LDS_BYTES - 1024);
  WAIT_VM(0);
}

__global__ void __launch_bounds__(256, 1) k_mega(Params p) {
  extern __shared__ __attribute__((aligned(16))) char smem[];
  const int x = blockIdx.x & 7, j = blockIdx.x >> 3;
  const int w = __builtin_amdgcn_readfirstlane(threadIdx.x >> 6);
  if (w == 1 && blockIdx.x != 128 && ((p.tlb >> (x < 4 ? (j < 16 ? 0 : j < 25 ? 1 : 2) : (j & 24) == 16 ? 4 : 3)) & 1)) touch_pages(p.ws, j);
  if (x < 4 && (j < 16 || j > 24)) sleep_n(x ? p.v0 : p.s0);
  if (x < 4 && j < 8) { chain_role<true>(p, w, x + 4 * j, x); return; }
  else if (x < 4 && j < 16) { chain_role<false>(p, w, x + 4 * (j - 8), x); return; }
  else if (x < 4 && j < 25) red_role(p, w, x + 4 * (j - 16), smem);
  else if (x == 0 && j == 25) {
    if (w == 0) keep_role(p);
    else if (w == 1) first_role(p);
    else if (p.cpf & 4) code_ahead(w - 1, 3);
  }
  else {
    unsigned tag;
    int T = -1, opl = -1, mix = -1, ms0 = 0, mds = 1, ms1 = p.mixs;
    FinX fx;
    if (x >= 4 && j < 16) {
      const int h = (x & 3) + 4 * (j & 7);
      tag = j >= 8 ? proj_role<false>(p, w, h) : proj_role<true>(p, w, h);
      if (j >= 8 && h < 24) opl = h, mix = h >> 2, ms0 = p.ob0 + (h & 3), mds = 4, ms1 = p.ob1;
      else if (h < 30) mix = h % 6, ms0 = p.mixs + (j < 8 ? 1 + h / 6 : 0), mds = 6, ms1 = SDIR;
      if (j >= 8 && h >= 30) { T = (h - 30) * 35; fx = fin_x(p, w, T); }
    } else {
      if (x >= 4 && j < 24) {
        const int h = (x & 3) + 4 * (j & 7);
        lines_ahead(p, w, h);
        if (p.cpf & 1) code_ahead((j & 7) * 4 + w, 32);
        if (p.wl2 & 1) warm_l2(p.Wo, h * DH * DM * 2, false, w);
        x_ahead(p, w, 35 + h);
        WAIT_VM(0);
        BARRIER();
        if (U_THIRD) proj_role<true, 2>(p, w, h);
        tag = proj_role<false, 1>(p, w, h);
        if (w == 0 && lane_id() == 0) flag_put({p.ws, tag}, flag_ptr(p.ws, F_Y) + 35 + h, 1u);
        att_role(p, tag, w, h, 0, SDIR);
      } else {
        if (x >= 4) {
          tag = proj_role<true, 1>(p, w, (x & 3) + 4 * (j & 7));
          if (j >= 30) xprep_role(p, tag, w, (x & 3) + 4 * (j & 1));
        }
        if (x >= 4 && j < 30) opl = (j - 24) * 4 + (x & 3);
        else {
          if (x < 4 && j >= 26 && (p.cpf & 2)) code_ahead((j - 26) * 4 + w, 24);
          T = x < 4 ? (j - 25) * 4 + x : (j - 23) * 4 + (x & 3);
          fx = fin_x(p, w, T);
#pragma unroll
          for (int i = 0; i < NDIR; ++i) asm volatile("" : "+v"(fx.x[i]), "+v"(fx.d));
          if (x < 4) tag = s4_role(p, w, 7 + x * 7 + (j - 25), smem);
          if (T == 35) { if (w == 0) watch_role(p, tag); T = -1; }
          if (T > 28 && p.mixs) mix = T - 29;
        }
      }
    }
    if (opl >= 0) op_role(p, tag, w, opl >> 2, opl & 3, x, j < 16);
    if (mix >= 0) mix_role(p, tag, w, mix, ms0, mds, ms1, opl >= 0);
    if (T >= 0) fin_role(p, tag, w, T, fx);
  }
}

extern "C" __device__ __attribute__((used, noinline, visibility("hidden"))) void k_mega_end() {}

extern "C" void kernel_launch(void* const* d_in, const int* in_sizes, int n_in, void* d_out, int out_size, void* d_ws, size_t ws_size,
                              hipStream_t stream) {
  Params p;
  p.x = (const u16*)d_in[0]; p.mem0 = (const u16*)d_in[1];
  p.Wq = (const u16*)d_in[2]; p.Wk = (const u16*)d_in[3]; p.Wv = (const u16*)d_in[4]; p.Wo = (const u16*)d_in[5];
  p.lar = (const u16*)d_in[8]; p.aim = (const u16*)d_in[9]; p.cre = (const u16*)d_in[10]; p.cim = (const u16*)d_in[11];
  p.ldt = (const u16*)d_in[12]; p.dsk = (const u16*)d_in[13]; p.gw = (const u16*)d_in[14];
  p.out = (u16*)d_out; p.ws = (char*)d_ws;
  (void)hipFuncSetAttribute((const void*)k_mega, hipFuncAttributeMaxDynamicSharedMemorySize, LDS_BYTES);
  {
    k_mega<<<256, 256, LDS_BYTES, stream>>>(p);
  }
}
```

```cpp
#include <hip/hip_runtime.h>
#include <hip/hip_bf16.h>
#include <stdint.h>
#include <type_traits>

typedef __attribute__((ext_vector_type(2))) __bf16 bf16x2;
typedef __attribute__((ext_vector_type(4))) __bf16 bf16x4;
typedef __attribute__((ext_vector_type(8))) __bf16 bf16x8;
typedef __attribute__((ext_vector_type(2))) float f32x2;
typedef __attribute__((ext_vector_type(4))) float f32x4;
typedef __attribute__((ext_vector_type(2))) unsigned u32x2;
typedef __attribute__((ext_vector_type(4))) unsigned u32x4;
typedef unsigned short u16;
typedef __amdgpu_buffer_rsrc_t rsrc_t;
#define LAS __attribute__((address_space(3)))
#define GAS __attribute__((address_space(1)))

constexpr int SL = 2048, DM = 576, NH = 32, DH = 128, HD = 4096, NSEG = 64, NST = 128;

constexpr size_t WS_CTL = 0;
constexpr int CTL_BYTES = 65536;
constexpr int F_GEN = 0, F_TMO = 1, F_GO = 2, F_XCC = 10, F_PRB = 14, F_INITW = 16, F_Y = 20;
constexpr int F_U = 320;
constexpr int F_W = 360;
constexpr int F_R = 300;
constexpr int F_M = 65, F_O = 129, F_A = 200, F_Q = 256;
constexpr int W_VO = 1, W_PX = 19, W_PM = 23, W_PC = 24, W_MEM = 25;
constexpr int N_DUR = 0, N_K = 32, N_U = 64, N_RD = 96;
constexpr size_t COMM_BYTES = (size_t)8 << 20;
constexpr unsigned FR_BYTES = SL * HD * 2;
constexpr unsigned B_K = 0;
constexpr unsigned B_V = FR_BYTES;
constexpr unsigned B_U = FR_BYTES;
constexpr unsigned B_O = 3 * FR_BYTES;
constexpr unsigned B_Q = 4 * FR_BYTES;
constexpr unsigned B_AP = 5 * FR_BYTES + NH * 8192;
constexpr unsigned B_YT = B_AP + NSEG * 24 * 16384;
constexpr unsigned B_G = B_YT + DM * SL * 2;
constexpr int SDIR = 58, NDIR = NSEG - SDIR;
constexpr unsigned X_AT = 8u << 20;
constexpr int Y_NEAR = 0, Y_FAR = 144, F_G = 310;
constexpr int F_S = 26;
#define RG_U 3
#define UNEAR 8192u
#define NCOPY 4
#define RG_QKV 63
#define RG_M 63
#define RG_O 63
#define RG_AP 63
#define PB_MB 12u
constexpr unsigned CO = 4u << 20, PB = PB_MB << 20, BASE_BYTES = 244u << 20;
constexpr unsigned U_AT = (128u << 20) - PB;
constexpr size_t WS_END = (size_t)240 << 20;
constexpr int LDS_BYTES = 163840;
constexpr unsigned SPIN_CAP = 1u << 21;

struct Params {
  const u16 *x, *mem0, *Wq, *Wk, *Wv, *Wo, *lar, *aim, *cre, *cim, *ldt, *dsk, *gw;
  u16* out;
  char* ws;
#define TUNE_LIST(X) X(keep_at, "KD", 20) X(t_a, "TA", -6) X(t_first, "TY", 0) X(t_d, "TD", 11) X(rd0, "RD0", 55) X(gentle, "GENTLE", 0) X(dur_dec, "DDEC", 8) X(dur_inc, "DINC", 40) X(dur0, "DP0", 77) X(boost, "BOOST", 3) X(stag, "STAG", 40) X(dret, "DRET", 76) X(cpf, "CPF", 3) X(sa, "SA", 8) X(sb, "SB", 50) X(sa1, "SA1", 8) X(sa2, "SA2", 25) X(x2, "X2", 2) X(g2, "G2", 4) X(xg0, "XG0", 8) X(xg1, "XG1", 5) X(xtra, "XTRA", 9) X(s4q, "S4Q", 0) X(s4f, "S4F", 8) X(wl2, "WL2", 3) X(s0, "S0", 30) X(v0, "V0", 0) X(xl0, "XL0", 56) X(tlb, "TLB", 0) X(xt0, "XT0", 8) X(xt1, "XT1", 80) X(vlead, "VLEAD", 23) X(xpre, "XPRE", 7) X(pj0, "PJ0", 0) X(xlast, "XLAST", 8) X(hdel, "HDEL", 0) X(hdl2, "HDL2", 0) X(q0, "Q0", 0) X(s4skip, "S4SKIP", 0) X(mixs, "MIXS", 0) X(ob0, "OB0", 0) X(ob1, "OB1", 6) X(fsl, "FSL", 2) X(late_r, "LR", 8) X(late_s, "LS", 8)
#define TUNE_DECL(n, e, v) static constexpr int n = v;
  TUNE_LIST(TUNE_DECL)
};

__device__ __forceinline__ int lane_id() {
  int z = 0;
  asm volatile("" : "+v"(z));
  return __builtin_amdgcn_mbcnt_hi(~0u, __builtin_amdgcn_mbcnt_lo(~0u, z));
}
__device__ __forceinline__ float bf2f(u16 x) { return __uint_as_float((unsigned)x << 16); }
__device__ __forceinline__ float rbf(float x) { return (float)(__bf16)x; }
__device__ __forceinline__ unsigned pk2(float a, float b) {
  f32x2 v = {a, b};
  return __builtin_bit_cast(unsigned, __builtin_convertvector(v, bf16x2));
}
__device__ __forceinline__ float lo16(unsigned p) { return __uint_as_float(p << 16); }
__device__ __forceinline__ float hi16(unsigned p) { return __uint_as_float(p & 0xFFFF0000u); }
__device__ __forceinline__ bf16x8 zext8(u32x2 v) {
  u32x4 z = {v[0], v[1], 0u, 0u};
  return __builtin_bit_cast(bf16x8, z);
}
__device__ __forceinline__ bf16x4 tr_read(const char* smem_base, unsigned off) {
  return __builtin_amdgcn_ds_read_tr16_b64_v4bf16((LAS bf16x4*)((LAS char*)smem_base + off));
}
__device__ __forceinline__ float red_max_g(float x) {
  auto r = __builtin_amdgcn_permlane16_swap(__float_as_uint(x), __float_as_uint(x), false, false);
  x = fmaxf(__uint_as_float(r[0]), __uint_as_float(r[1]));
  auto q = __builtin_amdgcn_permlane32_swap(__float_as_uint(x), __float_as_uint(x), false, false);
  return fmaxf(__uint_as_float(q[0]), __uint_as_float(q[1]));
}
__device__ __forceinline__ float red_sum_g(float x) {
  auto r = __builtin_amdgcn_permlane16_swap(__float_as_uint(x), __float_as_uint(x), false, false);
  x = __uint_as_float(r[0]) + __uint_as_float(r[1]);
  auto q = __builtin_amdgcn_permlane32_swap(__float_as_uint(x), __float_as_uint(x), false, false);
  return __uint_as_float(q[0]) + __uint_as_float(q[1]);
}
__device__ __forceinline__ rsrc_t mk_rsrc(const void* p, size_t bytes) {
  return __builtin_amdgcn_make_buffer_rsrc((void*)p, 0, (int)bytes, 0x00020000);
}

#define STR2(x) #x
#define STR(x) STR2(x)
#define CODE_PAD(N) asm volatile("s_branch .Lpad_%=\n\t.fill " STR(N) ", 4, 0xBF800000\n\t.Lpad_%=:" ::: "memory")
#define PAD_RED 0
#define PAD_ATT 0
#define PAD_OP 0
#define PAD_S4 0
#define PAD_CH 0
#define WAIT_VM(n) asm volatile("s_waitcnt vmcnt(" #n ")" ::: "memory")
#define WAIT_LGKM0() asm volatile("s_waitcnt lgkmcnt(0)" ::: "memory")
#define BARRIER() do { WAIT_LGKM0(); __builtin_amdgcn_s_barrier(); asm volatile("" ::: "memory"); } while (0)
#define SB() __builtin_amdgcn_sched_barrier(0)
#define MF(a, b, c) __builtin_amdgcn_mfma_f32_16x16x32_bf16(a, b, c, 0, 0, 0)
#define RLX_AGENT __ATOMIC_RELAXED, __HIP_MEMORY_SCOPE_AGENT
#define DEV_COUNT(i, cond) do {} while (0)
#define DEV_COUNT_DECL do {} while (0)
#define DEV_COUNT_FLUSH() do {} while (0)
__device__ __forceinline__ bf16x8 cat2(bf16x4 a, bf16x4 b) { return __builtin_shufflevector(a, b, 0, 1, 2, 3, 4, 5, 6, 7); }
__device__ __forceinline__ u32x4 pack8(const f32x4& a, const f32x4& b) {
  return (u32x4){pk2(a[0], a[1]), pk2(a[2], a[3]), pk2(b[0], b[1]), pk2(b[2], b[3])};
}
__device__ __forceinline__ void dma16(rsrc_t rs, unsigned voff, unsigned soff, unsigned lds_addr) {
  unsigned keep;
  asm volatile("s_mov_b32 %0, m0\n\ts_mov_b32 m0, %3\n\ts_nop 0\n\tbuffer_load_dwordx4 %1, %2, %4 offen lds\n\ts_mov_b32 m0, %0"
               : "=&s"(keep) : "v"(voff), "s"(rs), "s"(lds_addr), "s"(soff) : "memory");
}
__device__ __forceinline__ void st128(u32x4 v, rsrc_t rs, unsigned voff, unsigned soff) {
  __builtin_amdgcn_raw_buffer_store_b128(v, rs, voff, soff, 16);
  asm volatile("s_nop 1" ::"v"(v) : "memory");
}
__device__ __forceinline__ u32x4 ld128(rsrc_t rs, unsigned voff, unsigned soff) { return __builtin_amdgcn_raw_buffer_load_b128(rs, voff, soff, 16); }
__device__ __forceinline__ bf16x8 ldfrag(rsrc_t rs, unsigned voff, unsigned soff) { return __builtin_bit_cast(bf16x8, ld128(rs, voff, soff)); }
__device__ __forceinline__ void sleep_n(int n) { for (int i = 0; i < n; ++i) __builtin_amdgcn_s_sleep(1); }

__device__ __forceinline__ char* comm_base(char* ws) {
  return (char*)(((uintptr_t)ws + CTL_BYTES + ((2u << 20) - 1)) & ~(uintptr_t)((2u << 20) - 1));
}
__device__ __forceinline__ unsigned rt_now() {
  unsigned long long t;
  asm volatile("s_memrealtime %0\n\ts_waitcnt lgkmcnt(0)" : "=s"(t) :: "memory");
  return (unsigned)t;
}
__device__ __forceinline__ int wait_rt(unsigned t) {
  int d = (int)(t - rt_now());
  if (d <= 0 || d > 2000) return -d;
  for (d -= 14; d > 0; d -= 3) __builtin_amdgcn_s_sleep(1);
  do { d = (int)(t - rt_now()); } while (d > 0);
  return -d;
}
__device__ __forceinline__ unsigned xcc_id() { return (unsigned)__builtin_amdgcn_s_getreg(20 | (3 << 11)) & 7u; }
typedef unsigned long long u64;
struct Ctx { char* ws; unsigned tag; };
__device__ __forceinline__ GAS u64* flag_ptr(char* ws, int idx) { return (GAS u64*)(ws + WS_CTL + idx * 128); }
__device__ __forceinline__ unsigned gen_load(char* ws) { return __hip_atomic_load((GAS unsigned*)flag_ptr(ws, F_GEN), RLX_AGENT); }
__device__ __forceinline__ unsigned flag_get(const Ctx& cx, GAS u64* p) {
  const u64 v = __hip_atomic_load(p, RLX_AGENT);
  return (unsigned)(v >> 32) == cx.tag ? (unsigned)v : 0u;
}
__device__ __forceinline__ void flag_put(const Ctx& cx, GAS u64* p, unsigned v) { __hip_atomic_store(p, ((u64)cx.tag << 32) | v, RLX_AGENT); }
__device__ __forceinline__ bool spin_fail(unsigned& spins, const Ctx& cx) {
  ++spins;
  if (spins > SPIN_CAP) { flag_put(cx, flag_ptr(cx.ws, F_TMO), 1u); return true; }
  return (spins & 1023u) == 0 && __builtin_amdgcn_readfirstlane(flag_get(cx, flag_ptr(cx.ws, F_TMO)));
}
template <int SLEEP = 1>
__device__ __forceinline__ void wait_ge_n(const Ctx& cx, int idx, int stride, int cnt, int target, int& seen) {
  unsigned spins = 0;
  const int k = lane_id() & 3;
  while (seen < target) {
    int v = (int)flag_get(cx, flag_ptr(cx.ws, idx + (k < cnt ? k : 0) * stride));
    seen = min(min(__builtin_amdgcn_readlane(v, 0), __builtin_amdgcn_readlane(v, 1)), min(__builtin_amdgcn_readlane(v, 2), __builtin_amdgcn_readlane(v, 3)));
    if (seen < target) {
      __builtin_amdgcn_s_sleep(SLEEP);
      if (spin_fail(spins, cx)) break;
    }
  }
  __builtin_amdgcn_fence(__ATOMIC_ACQUIRE, "wavefront");
}
template <int SLEEP>
__device__ __forceinline__ void wait_all(const Ctx& cx, int idx, int cnt) {
  unsigned spins = 0;
  const int k = lane_id();
  while (__any(flag_get(cx, flag_ptr(cx.ws, idx) + (k < cnt ? k : 0)) == 0u)) {
    __builtin_amdgcn_s_sleep(SLEEP);
    if (spin_fail(spins, cx)) break;
  }
  __builtin_amdgcn_fence(__ATOMIC_ACQUIRE, "wavefront");
}
__device__ __forceinline__ void publish(const Ctx& cx, int w, int idx, int value) {
  if (w == 0 && lane_id() == 0) flag_put(cx, flag_ptr(cx.ws, idx), (unsigned)value);
}

constexpr unsigned X1 = 0x101000u, TW_OFF = 27 * 32768 + 1024;
__device__ __forceinline__ unsigned win_static(int win) { return win * 32768 + (win >> 4) * 16384; }
__device__ __forceinline__ unsigned near_static(int i) { return (win_static(28 + (i >> 5)) ^ ((i & 32) << 7) ^ ((i & 64) << 8)) + (i & 31) * 128; }
struct Place {
  unsigned go;
  __device__ __forceinline__ void load(const Ctx& cx, int grp, unsigned need = 0xC0000000u) {
    unsigned spins = 0;
    while ((go = __builtin_amdgcn_readfirstlane(flag_get(cx, flag_ptr(cx.ws, F_GO + grp)))) < need && !spin_fail(spins, cx)) __builtin_amdgcn_s_sleep(8);
    __builtin_amdgcn_fence(__ATOMIC_ACQUIRE, "wavefront");
  }
  __device__ __forceinline__ unsigned dyn(int x) const { return ((go >> (4 * x)) & 7u) << 12; }
  __device__ __forceinline__ unsigned tog() const { return (((go >> 16) & 7u) << 12) | (2u << 20); }
  __device__ __forceinline__ unsigned slot(int s) const { return (((s ^ (s >> 1)) & 1) ? tog() : 0u) ^ ((s & 2) ? X1 : 0u); }
  __device__ __forceinline__ unsigned near(int i) const { return near_static(i) ^ (dyn(0) & 8192u); }
};
#define BULK_FLIP 0u
#define OUT_AUX 0
#define PROBE_N 6
struct Bulk {
  rsrc_t rs;
  unsigned w13;
  Place pl;
  __device__ __forceinline__ GAS u64* proj_flag(const Ctx& cx, int m, int h) const {
    return m == 0 ? flag_ptr(cx.ws, F_Q + h) : (GAS u64*)(comm_base(cx.ws) + pl.near(N_K + h) + (m - 1) * 8);
  }
  __device__ __forceinline__ void init(const Ctx& cx, int grp, u64 early = 0) {
    pl.go = __builtin_amdgcn_readfirstlane((unsigned)(early >> 32) == cx.tag ? (unsigned)early : 0u);
    if (pl.go < 0x80000000u) pl.load(cx, grp, 0x80000000u);
    rs = mk_rsrc(comm_base(cx.ws) - CO, BASE_BYTES);
    w13 = PB + ((~pl.go ^ BULK_FLIP) & 2u) * 4096;
  }
  __device__ __forceinline__ unsigned at(unsigned o) const {
    const unsigned q = w13 + ((o >> 13) << 14) + (o & 8191u);
    return q ^ ((q >> 8) & 8192u);
  }
};
__device__ __forceinline__ unsigned near_head(unsigned b, int h) { return PB + 2 * b + h * (1u << 20) + ((h >> 1) & 1) * 8192 + ((2 * h) & 63) * 16384; }
__device__ __forceinline__ unsigned u_tile(int T, int hb, int h7) {
  const int wv = T < 32 ? (T >> 1) & 3 : T & 3;
  return PB + 2 * B_U + T * (1u << 20) + ((T >> 1) & 1) * 8192 + (((T >> 3) & 3) * 4 + wv) * 65536 + (hb ^ ((T >> 3) & 3)) * 16384 + (h7 ^ (2 * wv + (T < 32 ? T & 1 : 0))) * 1024;
}
__device__ __forceinline__ unsigned xcc_page(unsigned xc) { return ((xc >> 1) & 1u) | ((xc >> 2) << 1) | ((xc & 1u) << 2); }

__device__ __forceinline__ void touch_pages(char* ws, int i) {
  const rsrc_t rs = mk_rsrc(comm_base(ws) - CO, BASE_BYTES);
  const unsigned o = lane_id() * (2u << 20) + (1u << 20) + (i & 63) * 128;
  const unsigned a = __builtin_amdgcn_raw_buffer_load_b32(rs, o, 4u << 20, 0), b = __builtin_amdgcn_raw_buffer_load_b32(rs, o, 132u << 20, 0);
  asm volatile("" :: "v"(a), "v"(b));
}

__device__ __forceinline__ void comm_init(Ctx& cx, int rid, int w, char* smem, bool all) {
  const int l = lane_id(), tid = w * 64 + l;
  const rsrc_t rs = mk_rsrc(comm_base(cx.ws), COMM_BYTES);
  const unsigned gen = gen_load(cx.ws);
  if (rid == 0) {
    LAS unsigned* const best = (LAS unsigned*)smem + 16;
    const unsigned base = w * 1024, r0 = rt_now(), c0 = (unsigned)__builtin_amdgcn_s_memtime();
    best[32] = 0u;
#pragma unroll
    for (int k = 0; k < 8; ++k) __builtin_amdgcn_raw_buffer_store_b128((u32x4){0u, 0u, 0u, 0u}, rs, base + k * 4096 + l * 16, 0, 16);
    WAIT_VM(0);
    BARRIER();
    unsigned mine = 0xFFFFu;
    auto sample = [&](int pg, int k) {
      unsigned long long t0, t1;
      asm volatile("s_memtime %0\n\ts_waitcnt lgkmcnt(0)" : "=s"(t0) :: "memory");
      const unsigned v = __builtin_amdgcn_raw_buffer_load_b32(rs, base + pg * 4096 + k * 128, 0, 16);
      asm volatile("s_waitcnt vmcnt(0)\n\ts_memtime %0\n\ts_waitcnt lgkmcnt(0)" : "=s"(t1) : "v"(v) : "memory");
      if (l == pg) mine = min(mine, (unsigned)(t1 - t0));
      return (unsigned)(t1 - t0);
    };
    auto pages = [&]() {
      if (l < 8) best[w * 8 + l] = mine;
      __syncthreads();
      const int k8 = l & 7;
      const unsigned b = min(min(best[k8], best[8 + k8]), min(best[16 + k8], best[24 + k8]));
      __syncthreads();
      return b;
    };
    auto page = [](int j) { return (j & 1) | ((j & 2) << 1); };
    const unsigned lim = 25u * ((unsigned)__builtin_amdgcn_s_memtime() - c0) / max(rt_now() - r0, 1u);
    cx.tag = __builtin_amdgcn_readfirstlane(gen) + 1u;
    volatile LAS unsigned* const found = best + 32;
    for (int k = 0; k < 40 && !__builtin_amdgcn_readfirstlane(*found); ++k) {
      const int pg = page(w + (k >> 1)) | ((k & 1) << 1);
      if (sample(pg, k >> 3) < lim && l == 0) atomicCAS((unsigned*)found, 0u, 1u | (pg & 2));
    }
    unsigned b = pages();
    b = min(b, (unsigned)__shfl_xor((int)b, 1)); b = min(b, (unsigned)__shfl_xor((int)b, 4));
    const unsigned fd = __builtin_amdgcn_readfirstlane(*found);
    const unsigned half = fd ? fd & 2u : __builtin_amdgcn_readlane(b, 2) < __builtin_amdgcn_readlane(b, 0) ? 2u : 0u;
    if (w == 3 && l < 8) flag_put(cx, flag_ptr(cx.ws, F_GO + l), 0x80000000u | half);
    unsigned xc = 0;
    if (w < 3) {
#pragma unroll
      for (int k = 0; k < PROBE_N; ++k) sample(page(w + k) | half, 5 + (k >> 2));
    } else {
      for (unsigned spins = 0; __any((xc = flag_get(cx, flag_ptr(cx.ws, F_INITW) + min(l, 35))) == 0u) && !spin_fail(spins, cx);) __builtin_amdgcn_s_sleep(2);
    }
    b = (pages() << 3) | (l & 7);
    b = min(b, (unsigned)__shfl_xor((int)b, 1)); b = min(b, (unsigned)__shfl_xor((int)b, 4));
    if (w == 3) {
      const unsigned v = l ? __shfl((int)xc, min(l, 3) * 9 - 1) - 1u : xcc_id();
      const unsigned p0 = (__builtin_amdgcn_readlane(b, half) & 7u) ^ xcc_page(xcc_id());
      unsigned seen = 0, go = 0xC0020000u;
#pragma unroll
      for (int x = 0; x < 4; ++x) seen |= 1u << (__builtin_amdgcn_readlane(v, x) & 7u);
#pragma unroll
      for (int x = 0; x < 4; ++x)
        go |= (seen == 15u || seen == 240u ? (xcc_page(__builtin_amdgcn_readlane(v, x)) ^ p0) & 7u : page(x) | half) << (4 * x);
      if (l < 8) flag_put(cx, flag_ptr(cx.ws, F_GO + l), go);
    }
    return;
  }
  if (all) {
    for (int j = (rid - 1) * 256 + tid; j < 78 * 2048; j += 35 * 256) {
      const int wi = j >> 11, win = wi < 2 ? 25 + wi : wi < 28 ? 31 + wi : wi < 52 ? 37 + wi : 45 + wi;
      __builtin_amdgcn_raw_buffer_store_b128((u32x4){~0u, ~0u, ~0u, ~0u}, rs, win * 32768 + (j & 2047) * 16, 0, 16);
    }
  }
  for (int j = (rid - 1) * 256 + tid; j < 34 * 2048; j += 35 * 256) {
    const int wi = 33 - (j >> 11), ch = j & 2047;
    const int win = wi < 24 ? wi + 1 : wi < 27 ? wi + 65 : wi == 27 ? 59 : wi == 28 ? 123 : wi - 2;
    const unsigned f = wi < 30 ? 0xFFFFFFFFu : 0u;
    __builtin_amdgcn_raw_buffer_store_b128((u32x4){f, f, f, f}, rs, win * 32768 + ch * 16, 0, 16);
  }
  cx.tag = __builtin_amdgcn_readfirstlane(gen) + 1u;
  WAIT_VM(0);
  BARRIER();
  if (tid == 0) flag_put(cx, flag_ptr(cx.ws, F_INITW) + rid - 1, xcc_id() + 1u);
}

__device__ __forceinline__ void keep_role(const Params& p) {
  char* const ws = p.ws;
  const int l = lane_id();
  const Ctx cx = {ws, __builtin_amdgcn_readfirstlane(gen_load(ws)) + 1u};
  if (l == 0) flag_put(cx, flag_ptr(ws, F_INITW) + 35, 1u);
  const rsrc_t crs = mk_rsrc(comm_base(ws), COMM_BYTES);
  Place pl;
  pl.load(cx, 0);
  int dp[2] = {4 * p.dur0, 4 * p.rd0}, boost = 0;
  for (unsigned spins = 0; p.t_first && __any(min(flag_get(cx, flag_ptr(ws, F_U + (l & 31)) + (l >> 5)), flag_get(cx, flag_ptr(ws, F_U + (l & 31)) + 2 + (l >> 5))) == 0u) && !spin_fail(spins, cx);)
    __builtin_amdgcn_s_sleep(1);
  unsigned t = rt_now() + p.t_first - 2 * (p.dur0 + p.t_a + p.xtra) - (p.rd0 + p.t_d + p.xg0), ta = 0, shift = 0;
  auto plan = [&](int b) {
    u32x4 wd = {0u, 0u, 0u, 0u};
#pragma unroll
    for (int k = 0; k < 4; ++k) {
      const int s = 4 * b + k;
      const unsigned t1 = t + ((dp[0] + k) >> 2) + p.t_a + boost + (s < p.sa ? p.xtra : s >= p.sa1 && s < p.sa2 ? p.x2 : s > p.xl0 ? p.xlast : 0);
      t = t1 + ((dp[1] + 3 - k) >> 2) + p.t_d + boost + (s < p.sa ? p.xg0 : s >= p.sa1 && s < p.sa2 ? p.g2 : s >= p.sb ? p.xg1 : 0);
      if ((l & 7) == k) wd = (u32x4){(unsigned)s, t1, t, shift};
      if (k == 1) ta = t;
    }
    st128(wd, crs, l < 32 ? (16384u ^ pl.dyn(l >> 3)) + (l & 7) * 16 : 0x7F000000u, TW_OFF + 1024 + b * 128);
  };
  plan(0);
#pragma unroll 1
  for (int b = 1; b < NSEG / 4; ++b) {
    u32x2 dw;
    const unsigned before = shift;
    for (unsigned spins = 0; !spin_fail(spins, cx); shift += p.dret) {
      const int d = (int)(ta + shift + p.keep_at - rt_now());
      if (d < -20 && d > -30000) continue;
      while ((unsigned)(ta + shift + p.keep_at - rt_now() - 120u) < 2000u) __builtin_amdgcn_s_sleep(24);
      while ((unsigned)(ta + shift + p.keep_at - rt_now() - 1u) < 2000u) __builtin_amdgcn_s_sleep(2);
      const unsigned f = flag_get(cx, (GAS u64*)(comm_base(ws) + (pl.near(N_K + b - 1) ^ 4096u)));
      dw = __builtin_amdgcn_raw_buffer_load_b64(crs, pl.near((l < 32 ? N_DUR : N_RD - 32) + l), 0, 16);
      if (__builtin_amdgcn_readfirstlane(f)) { shift = __builtin_amdgcn_readfirstlane(f) - 1u; break; }
    }
    if (b > 2) boost = shift - before > (unsigned)p.dret ? min(boost + p.boost, 12) : shift == before ? max(boost - 1, 0) : boost;
    unsigned dur = dw[1] == cx.tag && (dw[0] & 1023u) ? (unsigned)((float)(dw[0] >> 10) * 64.f * __builtin_amdgcn_rcpf((float)(dw[0] & 1023u))) : 0u;
    const unsigned long long none = __ballot(dur == 0u);
#pragma unroll
    for (int m = 1; m < 32; m <<= 1) dur = max(dur, (unsigned)__shfl_xor((int)dur, m));
#pragma unroll
    for (int k = 0; k < 2; ++k) {
      const int dm = __builtin_amdgcn_readlane(dur, 32 * k), d = dp[k];
      if ((unsigned)(none >> (32 * k)) == 0u) dp[k] = max(min(dm, d + p.dur_inc), d - p.dur_dec);
    }
    plan(b);
  }
}

#define X_ASM_TEXT \
  "s_cmp_gt_u32 %28, 1\n" \
  "s_cbranch_scc1 .LX_other_%=\n" \
  "s_mov_b64 s[40:41], %24\n" \
  "s_and_b32 s41, s41, 0xffff\n" \
  "s_mov_b32 s42, 255852544\n" \
  "s_mov_b32 s43, 0x00020000\n" \
  "s_mov_b32 s46, 4718592\n" \
  "s_mov_b32 s47, 0x00020000\n" \
  "s_lshl_b32 s72, %28, 11\n" \
  "buffer_load_dword v196, off, s[40:43], %38 sc1\n" \
  "s_mov_b64 s[44:45], %25\n" \
  "s_and_b32 s45, s45, 0xffff\n" \
  "s_add_u32 s60, %29, 0\n" \
  "s_lshl_b32 s61, s72, 1\n" \
  "s_add_u32 m0, s61, 16384\n" \
  "s_nop 0\n" \
  "buffer_load_dwordx4 v200, s[44:47], s60 offen lds\n" \
  "buffer_load_dwordx4 v201, s[44:47], s60 offen offset:1024 lds\n" \
  "buffer_load_dwordx4 v202, s[44:47], s60 offen offset:2048 lds\n" \
  "buffer_load_dwordx4 v203, s[44:47], s60 offen offset:3072 lds\n" \
  "s_add_u32 s60, %29, 524288\n" \
  "s_lshl_b32 s61, s72, 1\n" \
  "s_add_u32 m0, s61, 32768\n" \
  "s_nop 0\n" \
  "buffer_load_dwordx4 v200, s[44:47], s60 offen lds\n" \
  "buffer_load_dwordx4 v201, s[44:47], s60 offen offset:1024 lds\n" \
  "buffer_load_dwordx4 v202, s[44:47], s60 offen offset:2048 lds\n" \
  "buffer_load_dwordx4 v203, s[44:47], s60 offen offset:3072 lds\n" \
  "s_add_u32 s60, %29, 1048576\n" \
  "s_lshl_b32 s61, s72, 1\n" \
  "s_add_u32 m0, s61, 49152\n" \
  "s_nop 0\n" \
  "buffer_load_dwordx4 v200, s[44:47], s60 offen lds\n" \
  "buffer_load_dwordx4 v201, s[44:47], s60 offen offset:1024 lds\n" \
  "buffer_load_dwordx4 v202, s[44:47], s60 offen offset:2048 lds\n" \
  "buffer_load_dwordx4 v203, s[44:47], s60 offen offset:3072 lds\n" \
  "s_add_u32 s60, %29, 1572864\n" \
  "s_lshl_b32 s61, s72, 1\n" \
  "s_add_u32 m0, s61, 65536\n" \
  "s_nop 0\n" \
  "buffer_load_dwordx4 v200, s[44:47], s60 offen lds\n" \
  "buffer_load_dwordx4 v201, s[44:47], s60 offen offset:1024 lds\n" \
  "buffer_load_dwordx4 v202, s[44:47], s60 offen offset:2048 lds\n" \
  "buffer_load_dwordx4 v203, s[44:47], s60 offen offset:3072 lds\n" \
  "s_add_u32 s60, %29, 2097152\n" \
  "s_lshl_b32 s61, s72, 1\n" \
  "s_add_u32 m0, s61, 81920\n" \
  "s_nop 0\n" \
  "buffer_load_dwordx4 v200, s[44:47], s60 offen lds\n" \
  "buffer_load_dwordx4 v201, s[44:47], s60 offen offset:1024 lds\n" \
  "buffer_load_dwordx4 v202, s[44:47], s60 offen offset:2048 lds\n" \
  "buffer_load_dwordx4 v203, s[44:47], s60 offen offset:3072 lds\n" \
  "s_add_u32 s60, %29, 2621440\n" \
  "s_lshl_b32 s61, s72, 1\n" \
  "s_add_u32 m0, s61, 98304\n" \
  "s_nop 0\n" \
  "buffer_load_dwordx4 v200, s[44:47], s60 offen lds\n" \
  "buffer_load_dwordx4 v201, s[44:47], s60 offen offset:1024 lds\n" \
  "buffer_load_dwordx4 v202, s[44:47], s60 offen offset:2048 lds\n" \
  "buffer_load_dwordx4 v203, s[44:47], s60 offen offset:3072 lds\n" \
  "s_add_u32 s60, %29, 3145728\n" \
  "s_lshl_b32 s61, s72, 1\n" \
  "s_add_u32 m0, s61, 114688\n" \
  "s_nop 0\n" \
  "buffer_load_dwordx4 v200, s[44:47], s60 offen lds\n" \
  "buffer_load_dwordx4 v201, s[44:47], s60 offen offset:1024 lds\n" \
  "buffer_load_dwordx4 v202, s[44:47], s60 offen offset:2048 lds\n" \
  "buffer_load_dwordx4 v203, s[44:47], s60 offen offset:3072 lds\n" \
  "s_add_u32 s60, %29, 3670016\n" \
  "s_lshl_b32 s61, s72, 1\n" \
  "s_add_u32 m0, s61, 131072\n" \
  "s_nop 0\n" \
  "buffer_load_dwordx4 v200, s[44:47], s60 offen lds\n" \
  "buffer_load_dwordx4 v201, s[44:47], s60 offen offset:1024 lds\n" \
  "buffer_load_dwordx4 v202, s[44:47], s60 offen offset:2048 lds\n" \
  "buffer_load_dwordx4 v203, s[44:47], s60 offen offset:3072 lds\n" \
  "s_add_u32 s60, %29, 4194304\n" \
  "s_lshl_b32 s61, s72, 1\n" \
  "s_add_u32 m0, s61, 147456\n" \
  "s_nop 0\n" \
  "buffer_load_dwordx4 v200, s[44:47], s60 offen lds\n" \
  "buffer_load_dwordx4 v201, s[44:47], s60 offen offset:1024 lds\n" \
  "buffer_load_dwordx4 v202, s[44:47], s60 offen offset:2048 lds\n" \
  "buffer_load_dwordx4 v203, s[44:47], s60 offen offset:3072 lds\n" \
  "s_mov_b64 s[44:45], %27\n" \
  "s_and_b32 s45, s45, 0xffff\n" \
  "s_lshr_b32 s60, s72, 3\n" \
  "s_mov_b32 m0, s72\n" \
  "s_nop 0\n" \
  "buffer_load_dwordx4 v206, s[44:47], s60 offen lds\n" \
  "s_add_u32 s60, s60, 128\n" \
  "s_add_u32 m0, s72, 1024\n" \
  "s_nop 0\n" \
  "buffer_load_dwordx4 v206, s[44:47], s60 offen lds\n" \
  "s_movk_i32 s60, 1024\n" \
  "s_movk_i32 m0, 8192\n" \
  "s_nop 0\n" \
  "buffer_load_dwordx4 v206, s[44:47], s60 offen lds\n" \
  "s_waitcnt vmcnt(27)\n" \
  "s_barrier\n" \
  "v_add_u32 v194, 16384, v204\n" \
  "ds_read_b64_tr_b16 a[0:1], v194\n" \
  "ds_read_b64_tr_b16 a[2:3], v194 offset:512\n" \
  "ds_read_b64_tr_b16 a[4:5], v194 offset:8192\n" \
  "ds_read_b64_tr_b16 a[6:7], v194 offset:8704\n" \
  "v_add_u32 v195, 16384, v205\n" \
  "ds_read_b64_tr_b16 a[72:73], v195\n" \
  "ds_read_b64_tr_b16 a[74:75], v195 offset:512\n" \
  "ds_read_b64_tr_b16 a[76:77], v195 offset:8192\n" \
  "ds_read_b64_tr_b16 a[78:79], v195 offset:8704\n" \
  "v_add_u32 v194, 32768, v204\n" \
  "ds_read_b64_tr_b16 a[8:9], v194\n" \
  "ds_read_b64_tr_b16 a[10:11], v194 offset:512\n" \
  "ds_read_b64_tr_b16 a[12:13], v194 offset:8192\n" \
  "ds_read_b64_tr_b16 a[14:15], v194 offset:8704\n" \
  "v_add_u32 v195, 32768, v205\n" \
  "ds_read_b64_tr_b16 a[80:81], v195\n" \
  "ds_read_b64_tr_b16 a[82:83], v195 offset:512\n" \
  "ds_read_b64_tr_b16 a[84:85], v195 offset:8192\n" \
  "ds_read_b64_tr_b16 a[86:87], v195 offset:8704\n" \
  "v_add_u32 v194, 49152, v204\n" \
  "ds_read_b64_tr_b16 a[16:17], v194\n" \
  "ds_read_b64_tr_b16 a[18:19], v194 offset:512\n" \
  "ds_read_b64_tr_b16 a[20:21], v194 offset:8192\n" \
  "ds_read_b64_tr_b16 a[22:23], v194 offset:8704\n" \
  "v_add_u32 v195, 49152, v205\n" \
  "ds_read_b64_tr_b16 a[88:89], v195\n" \
  "ds_read_b64_tr_b16 a[90:91], v195 offset:512\n" \
  "ds_read_b64_tr_b16 a[92:93], v195 offset:8192\n" \
  "ds_read_b64_tr_b16 a[94:95], v195 offset:8704\n" \
  "s_waitcnt lgkmcnt(0)\n" \
  "s_barrier\n" \
  "s_mov_b64 s[44:45], %26\n" \
  "s_and_b32 s45, s45, 0xffff\n" \
  "s_add_u32 s60, %29, 0\n" \
  "s_lshl_b32 s61, s72, 1\n" \
  "s_add_u32 m0, s61, 16384\n" \
  "s_nop 0\n" \
  "buffer_load_dwordx4 v200, s[44:47], s60 offen lds\n" \
  "buffer_load_dwordx4 v201, s[44:47], s60 offen offset:1024 lds\n" \
  "buffer_load_dwordx4 v202, s[44:47], s60 offen offset:2048 lds\n" \
  "buffer_load_dwordx4 v203, s[44:47], s60 offen offset:3072 lds\n" \
  "s_add_u32 s60, %29, 524288\n" \
  "s_lshl_b32 s61, s72, 1\n" \
  "s_add_u32 m0, s61, 32768\n" \
  "s_nop 0\n" \
  "buffer_load_dwordx4 v200, s[44:47], s60 offen lds\n" \
  "buffer_load_dwordx4 v201, s[44:47], s60 offen offset:1024 lds\n" \
  "buffer_load_dwordx4 v202, s[44:47], s60 offen offset:2048 lds\n" \
  "buffer_load_dwordx4 v203, s[44:47], s60 offen offset:3072 lds\n" \
  "s_add_u32 s60, %29, 1048576\n" \
  "s_lshl_b32 s61, s72, 1\n" \
  "s_add_u32 m0, s61, 49152\n" \
  "s_nop 0\n" \
  "buffer_load_dwordx4 v200, s[44:47], s60 offen lds\n" \
  "buffer_load_dwordx4 v201, s[44:47], s60 offen offset:1024 lds\n" \
  "buffer_load_dwordx4 v202, s[44:47], s60 offen offset:2048 lds\n" \
  "buffer_load_dwordx4 v203, s[44:47], s60 offen offset:3072 lds\n" \
  "v_readfirstlane_b32 s55, v196\n" \
  "s_add_u32 s55, s55, 1\n" \
  "s_waitcnt vmcnt(27)\n" \
  "s_barrier\n" \
  "v_add_u32 v194, 65536, v204\n" \
  "ds_read_b64_tr_b16 a[24:25], v194\n" \
  "ds_read_b64_tr_b16 a[26:27], v194 offset:512\n" \
  "ds_read_b64_tr_b16 a[28:29], v194 offset:8192\n" \
  "ds_read_b64_tr_b16 a[30:31], v194 offset:8704\n" \
  "v_add_u32 v195, 65536, v205\n" \
  "ds_read_b64_tr_b16 a[96:97], v195\n" \
  "ds_read_b64_tr_b16 a[98:99], v195 offset:512\n" \
  "ds_read_b64_tr_b16 a[100:101], v195 offset:8192\n" \
  "ds_read_b64_tr_b16 a[102:103], v195 offset:8704\n" \
  "v_add_u32 v194, 81920, v204\n" \
  "ds_read_b64_tr_b16 a[32:33], v194\n" \
  "ds_read_b64_tr_b16 a[34:35], v194 offset:512\n" \
  "ds_read_b64_tr_b16 a[36:37], v194 offset:8192\n" \
  "ds_read_b64_tr_b16 a[38:39], v194 offset:8704\n" \
  "v_add_u32 v195, 81920, v205\n" \
  "ds_read_b64_tr_b16 a[104:105], v195\n" \
  "ds_read_b64_tr_b16 a[106:107], v195 offset:512\n" \
  "ds_read_b64_tr_b16 a[108:109], v195 offset:8192\n" \
  "ds_read_b64_tr_b16 a[110:111], v195 offset:8704\n" \
  "v_add_u32 v194, 98304, v204\n" \
  "ds_read_b64_tr_b16 a[40:41], v194\n" \
  "ds_read_b64_tr_b16 a[42:43], v194 offset:512\n" \
  "ds_read_b64_tr_b16 a[44:45], v194 offset:8192\n" \
  "ds_read_b64_tr_b16 a[46:47], v194 offset:8704\n" \
  "v_add_u32 v195, 98304, v205\n" \
  "ds_read_b64_tr_b16 a[112:113], v195\n" \
  "ds_read_b64_tr_b16 a[114:115], v195 offset:512\n" \
  "ds_read_b64_tr_b16 a[116:117], v195 offset:8192\n" \
  "ds_read_b64_tr_b16 a[118:119], v195 offset:8704\n" \
  "s_waitcnt lgkmcnt(0)\n" \
  "s_barrier\n" \
  "s_add_u32 s60, %29, 1572864\n" \
  "s_lshl_b32 s61, s72, 1\n" \
  "s_add_u32 m0, s61, 65536\n" \
  "s_nop 0\n" \
  "buffer_load_dwordx4 v200, s[44:47], s60 offen lds\n" \
  "buffer_load_dwordx4 v201, s[44:47], s60 offen offset:1024 lds\n" \
  "buffer_load_dwordx4 v202, s[44:47], s60 offen offset:2048 lds\n" \
  "buffer_load_dwordx4 v203, s[44:47], s60 offen offset:3072 lds\n" \
  "s_add_u32 s60, %29, 2097152\n" \
  "s_lshl_b32 s61, s72, 1\n" \
  "s_add_u32 m0, s61, 81920\n" \
  "s_nop 0\n" \
  "buffer_load_dwordx4 v200, s[44:47], s60 offen lds\n" \
  "buffer_load_dwordx4 v201, s[44:47], s60 offen offset:1024 lds\n" \
  "buffer_load_dwordx4 v202, s[44:47], s60 offen offset:2048 lds\n" \
  "buffer_load_dwordx4 v203, s[44:47], s60 offen offset:3072 lds\n" \
  "s_add_u32 s60, %29, 2621440\n" \
  "s_lshl_b32 s61, s72, 1\n" \
  "s_add_u32 m0, s61, 98304\n" \
  "s_nop 0\n" \
  "buffer_load_dwordx4 v200, s[44:47], s60 offen lds\n" \
  "buffer_load_dwordx4 v201, s[44:47], s60 offen offset:1024 lds\n" \
  "buffer_load_dwordx4 v202, s[44:47], s60 offen offset:2048 lds\n" \
  "buffer_load_dwordx4 v203, s[44:47], s60 offen offset:3072 lds\n" \
  "s_waitcnt vmcnt(27)\n" \
  "s_barrier\n" \
  "v_add_u32 v194, 114688, v204\n" \
  "ds_read_b64_tr_b16 a[48:49], v194\n" \
  "ds_read_b64_tr_b16 a[50:51], v194 offset:512\n" \
  "ds_read_b64_tr_b16 a[52:53], v194 offset:8192\n" \
  "ds_read_b64_tr_b16 a[54:55], v194 offset:8704\n" \
  "v_add_u32 v195, 114688, v205\n" \
  "ds_read_b64_tr_b16 a[120:121], v195\n" \
  "ds_read_b64_tr_b16 a[122:123], v195 offset:512\n" \
  "ds_read_b64_tr_b16 a[124:125], v195 offset:8192\n" \
  "ds_read_b64_tr_b16 a[126:127], v195 offset:8704\n" \
  "v_add_u32 v194, 131072, v204\n" \
  "ds_read_b64_tr_b16 a[56:57], v194\n" \
  "ds_read_b64_tr_b16 a[58:59], v194 offset:512\n" \
  "ds_read_b64_tr_b16 a[60:61], v194 offset:8192\n" \
  "ds_read_b64_tr_b16 a[62:63], v194 offset:8704\n" \
  "v_add_u32 v195, 131072, v205\n" \
  "ds_read_b64_tr_b16 a[128:129], v195\n" \
  "ds_read_b64_tr_b16 a[130:131], v195 offset:512\n" \
  "ds_read_b64_tr_b16 a[132:133], v195 offset:8192\n" \
  "ds_read_b64_tr_b16 a[134:135], v195 offset:8704\n" \
  "v_add_u32 v194, 147456, v204\n" \
  "ds_read_b64_tr_b16 a[64:65], v194\n" \
  "ds_read_b64_tr_b16 a[66:67], v194 offset:512\n" \
  "ds_read_b64_tr_b16 a[68:69], v194 offset:8192\n" \
  "ds_read_b64_tr_b16 a[70:71], v194 offset:8704\n" \
  "v_add_u32 v195, 147456, v205\n" \
  "ds_read_b64_tr_b16 a[136:137], v195\n" \
  "ds_read_b64_tr_b16 a[138:139], v195 offset:512\n" \
  "ds_read_b64_tr_b16 a[140:141], v195 offset:8192\n" \
  "ds_read_b64_tr_b16 a[142:143], v195 offset:8704\n" \
  "s_waitcnt lgkmcnt(0)\n" \
  "s_barrier\n" \
  "s_add_u32 s60, %29, 3145728\n" \
  "s_lshl_b32 s61, s72, 1\n" \
  "s_add_u32 m0, s61, 114688\n" \
  "s_nop 0\n" \
  "buffer_load_dwordx4 v200, s[44:47], s60 offen lds\n" \
  "buffer_load_dwordx4 v201, s[44:47], s60 offen offset:1024 lds\n" \
  "buffer_load_dwordx4 v202, s[44:47], s60 offen offset:2048 lds\n" \
  "buffer_load_dwordx4 v203, s[44:47], s60 offen offset:3072 lds\n" \
  "s_add_u32 s60, %29, 3670016\n" \
  "s_lshl_b32 s61, s72, 1\n" \
  "s_add_u32 m0, s61, 131072\n" \
  "s_nop 0\n" \
  "buffer_load_dwordx4 v200, s[44:47], s60 offen lds\n" \
  "buffer_load_dwordx4 v201, s[44:47], s60 offen offset:1024 lds\n" \
  "buffer_load_dwordx4 v202, s[44:47], s60 offen offset:2048 lds\n" \
  "buffer_load_dwordx4 v203, s[44:47], s60 offen offset:3072 lds\n" \
  "s_add_u32 s60, %29, 4194304\n" \
  "s_lshl_b32 s61, s72, 1\n" \
  "s_add_u32 m0, s61, 147456\n" \
  "s_nop 0\n" \
  "buffer_load_dwordx4 v200, s[44:47], s60 offen lds\n" \
  "buffer_load_dwordx4 v201, s[44:47], s60 offen offset:1024 lds\n" \
  "buffer_load_dwordx4 v202, s[44:47], s60 offen offset:2048 lds\n" \
  "buffer_load_dwordx4 v203, s[44:47], s60 offen offset:3072 lds\n" \
  "buffer_load_dwordx2 v[198:199], off, s[40:43], %37 sc1\n" \
  "s_waitcnt vmcnt(25)\n" \
  "s_barrier\n" \
  "v_add_u32 v195, 16384, v205\n" \
  "ds_read_b64_tr_b16 a[216:217], v195\n" \
  "ds_read_b64_tr_b16 a[218:219], v195 offset:512\n" \
  "ds_read_b64_tr_b16 a[220:221], v195 offset:8192\n" \
  "ds_read_b64_tr_b16 a[222:223], v195 offset:8704\n" \
  "v_add_u32 v195, 32768, v205\n" \
  "ds_read_b64_tr_b16 a[224:225], v195\n" \
  "ds_read_b64_tr_b16 a[226:227], v195 offset:512\n" \
  "ds_read_b64_tr_b16 a[228:229], v195 offset:8192\n" \
  "ds_read_b64_tr_b16 a[230:231], v195 offset:8704\n" \
  "v_add_u32 v195, 49152, v205\n" \
  "ds_read_b64_tr_b16 a[232:233], v195\n" \
  "ds_read_b64_tr_b16 a[234:235], v195 offset:512\n" \
  "ds_read_b64_tr_b16 a[236:237], v195 offset:8192\n" \
  "ds_read_b64_tr_b16 a[238:239], v195 offset:8704\n" \
  "s_waitcnt lgkmcnt(0)\n" \
  "s_waitcnt vmcnt(13)\n" \
  "s_barrier\n" \
  "v_add_u32 v195, 65536, v205\n" \
  "ds_read_b64_tr_b16 a[240:241], v195\n" \
  "ds_read_b64_tr_b16 a[242:243], v195 offset:512\n" \
  "ds_read_b64_tr_b16 a[244:245], v195 offset:8192\n" \
  "ds_read_b64_tr_b16 a[246:247], v195 offset:8704\n" \
  "v_add_u32 v194, 81920, v204\n" \
  "ds_read_b64_tr_b16 a[176:177], v194\n" \
  "ds_read_b64_tr_b16 a[178:179], v194 offset:512\n" \
  "ds_read_b64_tr_b16 a[180:181], v194 offset:8192\n" \
  "ds_read_b64_tr_b16 a[182:183], v194 offset:8704\n" \
  "v_add_u32 v195, 81920, v205\n" \
  "ds_read_b64_tr_b16 a[248:249], v195\n" \
  "ds_read_b64_tr_b16 a[250:251], v195 offset:512\n" \
  "ds_read_b64_tr_b16 a[252:253], v195 offset:8192\n" \
  "ds_read_b64_tr_b16 a[254:255], v195 offset:8704\n" \
  "v_add_u32 v194, 98304, v204\n" \
  "ds_read_b64_tr_b16 a[184:185], v194\n" \
  "ds_read_b64_tr_b16 a[186:187], v194 offset:512\n" \
  "ds_read_b64_tr_b16 a[188:189], v194 offset:8192\n" \
  "ds_read_b64_tr_b16 a[190:191], v194 offset:8704\n" \
  "v_add_u32 v195, 98304, v205\n" \
  "ds_read_b64_tr_b16 v[0:1], v195\n" \
  "ds_read_b64_tr_b16 v[2:3], v195 offset:512\n" \
  "ds_read_b64_tr_b16 v[4:5], v195 offset:8192\n" \
  "ds_read_b64_tr_b16 v[6:7], v195 offset:8704\n" \
  "s_waitcnt lgkmcnt(0)\n" \
  "s_waitcnt vmcnt(1)\n" \
  "s_barrier\n" \
  "v_add_u32 v194, 114688, v204\n" \
  "ds_read_b64_tr_b16 a[192:193], v194\n" \
  "ds_read_b64_tr_b16 a[194:195], v194 offset:512\n" \
  "ds_read_b64_tr_b16 a[196:197], v194 offset:8192\n" \
  "ds_read_b64_tr_b16 a[198:199], v194 offset:8704\n" \
  "v_add_u32 v195, 114688, v205\n" \
  "ds_read_b64_tr_b16 v[8:9], v195\n" \
  "ds_read_b64_tr_b16 v[10:11], v195 offset:512\n" \
  "ds_read_b64_tr_b16 v[12:13], v195 offset:8192\n" \
  "ds_read_b64_tr_b16 v[14:15], v195 offset:8704\n" \
  "v_add_u32 v194, 131072, v204\n" \
  "ds_read_b64_tr_b16 a[200:201], v194\n" \
  "ds_read_b64_tr_b16 a[202:203], v194 offset:512\n" \
  "ds_read_b64_tr_b16 a[204:205], v194 offset:8192\n" \
  "ds_read_b64_tr_b16 a[206:207], v194 offset:8704\n" \
  "v_add_u32 v195, 131072, v205\n" \
  "ds_read_b64_tr_b16 v[16:17], v195\n" \
  "ds_read_b64_tr_b16 v[18:19], v195 offset:512\n" \
  "ds_read_b64_tr_b16 v[20:21], v195 offset:8192\n" \
  "ds_read_b64_tr_b16 v[22:23], v195 offset:8704\n" \
  "v_add_u32 v194, 147456, v204\n" \
  "ds_read_b64_tr_b16 a[208:209], v194\n" \
  "ds_read_b64_tr_b16 a[210:211], v194 offset:512\n" \
  "ds_read_b64_tr_b16 a[212:213], v194 offset:8192\n" \
  "ds_read_b64_tr_b16 a[214:215], v194 offset:8704\n" \
  "v_add_u32 v195, 147456, v205\n" \
  "ds_read_b64_tr_b16 v[24:25], v195\n" \
  "ds_read_b64_tr_b16 v[26:27], v195 offset:512\n" \
  "ds_read_b64_tr_b16 v[28:29], v195 offset:8192\n" \
  "ds_read_b64_tr_b16 v[30:31], v195 offset:8704\n" \
  "s_waitcnt lgkmcnt(0)\n" \
  "s_waitcnt vmcnt(0)\n" \
  "v_readfirstlane_b32 s96, v198\n" \
  "v_readfirstlane_b32 s63, v199\n" \
  "s_cmp_eq_u32 s63, s55\n" \
  "s_cselect_b32 s96, s96, 0\n" \
  "s_cmp_ge_u32 s96, 0xc0000000\n" \
  "s_cbranch_scc1 .LX_go_x_%=\n" \
  "s_mov_b32 s69, 0\n" \
  ".LX_go_%=:\n" \
  "buffer_load_dwordx2 v[196:197], off, s[40:43], %37 sc1\n" \
  "s_waitcnt vmcnt(0)\n" \
  "v_readfirstlane_b32 s96, v196\n" \
  "v_readfirstlane_b32 s63, v197\n" \
  "s_cmp_eq_u32 s63, s55\n" \
  "s_cselect_b32 s96, s96, 0\n" \
  "s_cmp_ge_u32 s96, 0xc0000000\n" \
  "s_cbranch_scc1 .LX_go_x_%=\n" \
  "s_sleep 8\n" \
  "s_add_u32 s69, s69, 1\n" \
  "s_and_b32 s63, s69, 1023\n" \
  "s_cbranch_scc1 .LX_go_%=\n" \
  "s_cmp_gt_u32 s69, 2097152\n" \
  "s_cbranch_scc1 .LX_go_set_%=\n" \
  "buffer_load_dwordx2 v[196:197], off, s[40:43], %38 offset:128 sc1\n" \
  "s_waitcnt vmcnt(0)\n" \
  "v_readfirstlane_b32 s63, v197\n" \
  "s_cmp_eq_u32 s63, s55\n" \
  "s_cbranch_scc0 .LX_go_%=\n" \
  "s_branch .LX_go_x_%=\n" \
  ".LX_go_set_%=:\n" \
  "v_mov_b32 v196, 1\n" \
  "v_mov_b32 v197, s55\n" \
  "buffer_store_dwordx2 v[196:197], off, s[40:43], %38 offset:128 sc1\n" \
  "s_waitcnt vmcnt(0)\n" \
  ".LX_go_x_%=:\n" \
  "s_and_b32 s60, s96, 2\n" \
  "s_lshl_b32 s60, s60, 12\n" \
  "s_xor_b32 s74, %34, s60\n" \
  "s_xor_b32 s75, %36, s60\n" \
  "s_bfe_u32 s56, s96, 0x30010\n" \
  "s_lshl_b32 s56, s56, 12\n" \
  "s_or_b32 s56, s56, 0x200000\n" \
  "s_mov_b32 s57, 1052672\n" \
  "s_lshl_b32 s61, %33, 2\n" \
  "s_lshr_b32 s51, s96, s61\n" \
  "s_and_b32 s51, s51, 7\n" \
  "s_lshl_b32 s51, s51, 12\n" \
  "s_xor_b32 s51, s51, 4210688\n" \
  "s_mov_b32 s71, 0\n" \
  "s_add_u32 s45, s51, 886784\n" \
  "s_mov_b32 s46, -1\n" \
  "v_mov_b64 v[32:33], -1\n" \
  "v_mov_b64 v[34:35], -1\n" \
  "v_and_b32 v194, 3, v246\n" \
  "v_lshlrev_b32 v194, 2, v194\n" \
  "v_lshrrev_b32 v194, v194, s96\n" \
  "v_and_b32 v194, 7, v194\n" \
  "v_lshlrev_b32 v194, 12, v194\n" \
  "v_and_b32 v246, -4, v246\n" \
  "v_xor_b32 v246, v246, v194\n" \
  "v_and_b32 v194, 3, v247\n" \
  "v_lshlrev_b32 v194, 2, v194\n" \
  "v_lshrrev_b32 v194, v194, s96\n" \
  "v_and_b32 v194, 7, v194\n" \
  "v_lshlrev_b32 v194, 12, v194\n" \
  "v_and_b32 v247, -4, v247\n" \
  "v_xor_b32 v247, v247, v194\n" \
  "s_xor_b32 s62, s56, s57\n" \
  "s_nop 1\n" \
  "s_andn2_b32 s60, 2, s96\n" \
  "s_lshl_b32 s60, s60, 12\n" \
  "s_xor_b32 s60, s60, 8192\n" \
  "s_xor_b32 s58, %31, s60\n" \
  "s_mov_b32 s49, 0\n" \
  "s_add_u32 s47, %41, s60\n" \
  "s_add_u32 s97, s60, 8388608\n" \
  "s_xor_b32 s50, %32, s60\n" \
  "s_mov_b32 s48, 0\n" \
  "s_mov_b32 s54, 0\n" \
  "s_cmp_eq_u32 %28, 0\n" \
  "s_cselect_b64 s[88:89], -1, 0\n" \
  "s_mov_b32 s68, 0\n" \
  "s_mov_b32 s70, 1\n" \
  "s_mov_b32 s73, 0\n" \
  "s_mov_b32 s44, 0\n" \
  "s_mov_b32 s53, 0\n" \
  "s_memrealtime s[86:87]\n" \
  "s_memtime s[76:77]\n" \
  "s_waitcnt lgkmcnt(0)\n" \
  "s_mov_b32 s90, s76\n" \
  "s_mov_b32 s87, 340\n" \
  "s_mov_b32 s64, 0x3e0293ee\n" \
  "s_mov_b32 s65, 0xbe0293ee\n" \
  "v_mov_b64 v[44:45], 0\n" \
  "v_mov_b64 v[46:47], 0\n" \
  "v_mov_b64 v[48:49], 0\n" \
  "v_mov_b64 v[50:51], 0\n" \
  "v_mov_b64 v[52:53], 0\n" \
  "v_mov_b64 v[54:55], 0\n" \
  "v_mov_b64 v[56:57], 0\n" \
  "v_mov_b64 v[58:59], 0\n" \
  "v_mov_b32 v40, 0\n" \
  "v_mov_b32 v41, s55\n" \
  "v_min_u32 v42, 1, v240\n" \
  "v_lshlrev_b32 v42, 30, v42\n" \
  "s_mov_b64 s[92:93], 0\n" \
  "s_mov_b32 s92, 0xffff0000\n" \
  "s_lshr_b32 s60, s72, 1\n" \
  "s_add_u32 s60, s60, 32768\n" \
  "v_add_u32 v178, s60, v240\n" \
  "v_xor_b32 v246, 1073741824, v246\n" \
  "v_xor_b32 v247, 1073741824, v247\n" \
  "v_mov_b32 v182, 1065369472\n" \
  "v_mov_b32 v183, 1065369472\n" \
  "s_and_b32 s60, s72, 2048\n" \
  "s_add_u32 s50, s50, s60\n" \
  "s_lshr_b32 s60, s60, 1\n" \
  "v_add_u32 v179, s60, v244\n" \
  "s_lshr_b32 s60, s72, 9\n" \
  "s_add_u32 s60, s60, 17408\n" \
  "v_mov_b32 v184, s60\n" \
  "s_mov_b32 s52, 0\n" \
  "s_barrier\n" \
  "ds_write_b32 v184, v40\n" \
  "s_branch .LX_xr_%=\n" \
  ".LX_dry_b_%=:\n" \
  "s_waitcnt vmcnt(0)\n" \
  "s_barrier\n" \
  "ds_read_b64 v[64:65], v178\n" \
  "ds_read_b64 v[66:67], v178 offset:2048\n" \
  "ds_read_b64 v[68:69], v178 offset:4096\n" \
  "ds_read_b64 v[70:71], v178 offset:6144\n" \
  "ds_read_b64 v[72:73], v178 offset:8\n" \
  "ds_read_b64 v[74:75], v178 offset:2056\n" \
  "ds_read_b64 v[76:77], v178 offset:4104\n" \
  "ds_read_b64 v[78:79], v178 offset:6152\n" \
  "v_xor_b32 v178, 8192, v178\n" \
  "s_mov_b32 s67, 2130706432\n" \
  "s_waitcnt vmcnt(0) lgkmcnt(0)\n" \
  "s_barrier\n" \
  "s_branch .LX_p_%=\n" \
  "s_nop 7\n" \
  ".p2align 6\n" \
  ".fill (((40 - (.LX_p_%= - .LX_rot_%=)) & 63) >> 2), 4, 0xBF800000\n" \
  ".LX_rot_%=:\n" \
  "s_xor_b32 s51, s51, s56\n" \
  "s_xor_b32 s71, s71, s56\n" \
  "v_xor_b32 v246, s56, v246\n" \
  "v_xor_b32 v247, s56, v247\n" \
  "s_mov_b32 s60, s56\n" \
  "s_mov_b32 s56, s57\n" \
  "s_mov_b32 s57, s60\n" \
  "s_add_u32 s59, s51, 819200\n" \
  "s_mov_b32 m0, 0\n" \
  "s_mov_b32 s70, 0\n" \
  "s_mov_b32 s68, 0\n" \
  "s_waitcnt lgkmcnt(0)\n" \
  "s_cmp_lg_u32 %28, 0\n" \
  "s_cbranch_scc1 .LX_g3_%=\n" \
  "s_mov_b32 s69, 0\n" \
  ".LX_tw_%=:\n" \
  "v_readfirstlane_b32 s63, v36\n" \
  "s_cmp_lg_u32 s63, s48\n" \
  "s_cbranch_scc1 .LX_tw_re_%=\n" \
  "v_readfirstlane_b32 s53, v38\n" \
  ".LX_rt_%=:\n" \
  "s_memrealtime s[60:61]\n" \
  "s_add_u32 s73, s53, s44\n" \
  "s_waitcnt lgkmcnt(0)\n" \
  "s_sub_u32 s62, s73, s60\n" \
  "s_cmp_ge_u32 s62, 2000\n" \
  "s_cbranch_scc1 .LX_lat_%=\n" \
  "s_sub_u32 s62, s62, 30\n" \
  ".LX_slp_%=:\n" \
  "s_cmp_le_i32 s62, 0\n" \
  "s_cbranch_scc1 .LX_clk_%=\n" \
  "s_sleep 1\n" \
  "s_sub_u32 s62, s62, 3\n" \
  "s_branch .LX_slp_%=\n" \
  ".LX_clk_%=:\n" \
  "s_memrealtime s[60:61]\n" \
  "s_waitcnt lgkmcnt(0)\n" \
  "s_sub_u32 s62, s73, s60\n" \
  "s_cmp_gt_i32 s62, 21\n" \
  "s_cbranch_scc1 .LX_clk_%=\n" \
  "s_cmp_le_i32 s62, 11\n" \
  "s_cbranch_scc1 .LX_clk2_%=\n" \
  "s_memrealtime s[76:77]\n" \
  "s_nop 4\n" \
  "s_memrealtime s[78:79]\n" \
  "s_nop 4\n" \
  "s_memrealtime s[80:81]\n" \
  "s_nop 4\n" \
  "s_memrealtime s[82:83]\n" \
  "s_waitcnt lgkmcnt(0)\n" \
  "s_mov_b32 s77, 1\n" \
  "s_cmp_eq_u32 s78, s76\n" \
  "s_addc_u32 s77, s77, 0\n" \
  "s_cmp_eq_u32 s80, s76\n" \
  "s_addc_u32 s77, s77, 0\n" \
  "s_cmp_eq_u32 s82, s76\n" \
  "s_addc_u32 s77, s77, 0\n" \
  "s_mul_i32 s77, s77, 24\n" \
  "s_lshr_b32 s78, s87, 2\n" \
  "s_sub_u32 s77, s78, s77\n" \
  "s_max_i32 s77, s77, 0\n" \
  "s_sub_u32 s76, s73, s76\n" \
  "s_mul_i32 s76, s76, s87\n" \
  "s_ashr_i32 s76, s76, 4\n" \
  "s_sub_u32 s76, s76, s77\n" \
  "s_sub_u32 s76, s76, 200\n" \
  "s_max_i32 s76, s76, 0\n" \
  "s_min_i32 s76, s76, 320\n" \
  "s_lshr_b32 s76, s76, 3\n" \
  "s_lshl_b32 s76, s76, 2\n" \
  "s_mov_b32 s68, 1\n" \
  "s_getpc_b64 s[78:79]\n" \
  "s_sub_u32 s76, 180, s76\n" \
  "s_add_u32 s78, s78, s76\n" \
  "s_addc_u32 s79, s79, 0\n" \
  "s_setpc_b64 s[78:79]\n" \
  "s_nop 1\n" \
  "s_nop 1\n" \
  "s_nop 1\n" \
  "s_nop 1\n" \
  "s_nop 1\n" \
  "s_nop 1\n" \
  "s_nop 1\n" \
  "s_nop 1\n" \
  "s_nop 1\n" \
  "s_nop 1\n" \
  "s_nop 1\n" \
  "s_nop 1\n" \
  "s_nop 1\n" \
  "s_nop 1\n" \
  "s_nop 1\n" \
  "s_nop 1\n" \
  "s_nop 1\n" \
  "s_nop 1\n" \
  "s_nop 1\n" \
  "s_nop 1\n" \
  "s_nop 1\n" \
  "s_nop 1\n" \
  "s_nop 1\n" \
  "s_nop 1\n" \
  "s_nop 1\n" \
  "s_nop 1\n" \
  "s_nop 1\n" \
  "s_nop 1\n" \
  "s_nop 1\n" \
  "s_nop 1\n" \
  "s_nop 1\n" \
  "s_nop 1\n" \
  "s_nop 1\n" \
  "s_nop 1\n" \
  "s_nop 1\n" \
  "s_nop 1\n" \
  "s_nop 1\n" \
  "s_nop 1\n" \
  "s_nop 1\n" \
  "s_nop 1\n" \
  ".LX_noclk_%=:\n" \
  "buffer_load_dwordx4 v240, s[40:43], s59 offen sc1 lds\n" \
  "s_memtime s[66:67]\n" \
  "buffer_load_dwordx4 v240, s[40:43], s59 offen offset:1024 sc1 lds\n" \
  "buffer_load_dwordx4 v240, s[40:43], s59 offen offset:2048 sc1 lds\n" \
  "buffer_load_dwordx4 v240, s[40:43], s59 offen offset:3072 sc1 lds\n" \
  "s_add_u32 s60, s59, 32768\n" \
  "s_movk_i32 m0, 4096\n" \
  "s_nop 0\n" \
  "buffer_load_dwordx4 v240, s[40:43], s60 offen sc1 lds\n" \
  "buffer_load_dwordx4 v240, s[40:43], s60 offen offset:1024 sc1 lds\n" \
  "buffer_load_dwordx4 v240, s[40:43], s60 offen offset:2048 sc1 lds\n" \
  "buffer_load_dwordx4 v240, s[40:43], s60 offen offset:3072 sc1 lds\n" \
  "s_add_u32 s60, s59, 65536\n" \
  "s_movk_i32 m0, 8192\n" \
  "s_nop 0\n" \
  "buffer_load_dwordx4 v240, s[40:43], s60 offen sc1 lds\n" \
  ".LX_g3_%=:\n" \
  "s_waitcnt vmcnt(6)\n" \
  "s_barrier\n" \
  ".LX_p_%=:\n" \
  "ds_read_b128 v[100:103], v240\n" \
  "ds_read_b128 v[104:107], v240 offset:1024\n" \
  "ds_read_b128 v[108:111], v240 offset:2048\n" \
  "s_waitcnt lgkmcnt(2)\n" \
  "v_smfmac_f32_16x16x64_bf16 v[44:47], v[100:103], a[0:7], v239\n" \
  "s_waitcnt vmcnt(0)\n" \
  "s_barrier\n" \
  "s_waitcnt lgkmcnt(1)\n" \
  "v_smfmac_f32_16x16x64_bf16 v[44:47], v[104:107], a[8:15], v239\n" \
  "ds_read_b128 v[112:115], v240 offset:3072\n" \
  "v_smfmac_f32_16x16x64_bf16 v[48:51], v[100:103], a[72:79], v239\n" \
  "ds_read_b128 v[116:119], v240 offset:6144\n" \
  "s_waitcnt lgkmcnt(2)\n" \
  "v_smfmac_f32_16x16x64_bf16 v[44:47], v[108:111], a[16:23], v239\n" \
  "ds_read_b128 v[120:123], v240 offset:4096\n" \
  "v_smfmac_f32_16x16x64_bf16 v[48:51], v[104:107], a[80:87], v239\n" \
  "ds_read_b128 v[124:127], v240 offset:5120\n" \
  "v_smfmac_f32_16x16x64_bf16 v[48:51], v[108:111], a[88:95], v239\n" \
  "ds_read_b128 v[128:131], v240 offset:7168\n" \
  "s_waitcnt lgkmcnt(4)\n" \
  "v_smfmac_f32_16x16x64_bf16 v[44:47], v[112:115], a[24:31], v239\n" \
  "ds_read_b128 v[132:135], v240 offset:8192\n" \
  "v_smfmac_f32_16x16x64_bf16 v[48:51], v[112:115], a[96:103], v239\n" \
  "s_waitcnt lgkmcnt(3)\n" \
  "v_smfmac_f32_16x16x64_bf16 v[44:47], v[120:123], a[32:39], v239\n" \
  "s_waitcnt lgkmcnt(2)\n" \
  "v_smfmac_f32_16x16x64_bf16 v[44:47], v[124:127], a[40:47], v239\n" \
  "v_smfmac_f32_16x16x64_bf16 v[44:47], v[116:119], a[48:55], v239\n" \
  "s_waitcnt lgkmcnt(1)\n" \
  "v_smfmac_f32_16x16x64_bf16 v[44:47], v[128:131], a[56:63], v239\n" \
  "s_waitcnt lgkmcnt(0)\n" \
  "v_smfmac_f32_16x16x64_bf16 v[44:47], v[132:135], a[64:71], v239\n" \
  "s_nop 2\n" \
  "v_smfmac_f32_16x16x64_bf16 v[48:51], v[120:123], a[104:111], v239\n" \
  "s_nop 2\n" \
  "v_smfmac_f32_16x16x64_bf16 v[48:51], v[124:127], a[112:119], v239\n" \
  "v_add_f32 v45, v45, v47\n" \
  "v_smfmac_f32_16x16x64_bf16 v[48:51], v[116:119], a[120:127], v239\n" \
  "v_add_f32 v44, v44, v46\n" \
  "v_cmp_u_f32 vcc, v44, v45\n" \
  "v_smfmac_f32_16x16x64_bf16 v[48:51], v[128:131], a[128:135], v239\n" \
  "v_mul_f32 v45, s64, v45\n" \
  "v_smfmac_f32_16x16x64_bf16 v[48:51], v[132:135], a[136:143], v239\n" \
  "s_cbranch_vccnz .LX_slow_%=\n" \
  ".LX_back_%=:\n" \
  "s_cmp_eq_u32 s48, 63\n" \
  "v_mul_f32 v44, s64, v44\n" \
  "v_cvt_pk_bf16_f32 v44, v44, v45\n" \
  "v_smfmac_f32_16x16x64_bf16 v[52:55], v[120:123], a[176:183], v239\n" \
  "ds_write_b32 v241, v44\n" \
  "s_nop 1\n" \
  "v_add_f32 v48, v48, v50\n" \
  "v_add_f32 v49, v49, v51\n" \
  "v_mul_f32 v49, s64, v49\n" \
  "v_mul_f32 v48, s64, v48\n" \
  "v_cvt_pk_bf16_f32 v48, v48, v49\n" \
  "v_smfmac_f32_16x16x64_bf16 v[52:55], v[124:127], a[184:191], v239\n" \
  "ds_write_b32 v241, v48 offset:256\n" \
  "v_smfmac_f32_16x16x64_bf16 v[52:55], v[116:119], a[192:199], v239\n" \
  "v_smfmac_f32_16x16x64_bf16 v[56:59], v[100:103], a[216:223], v239\n" \
  "s_waitcnt lgkmcnt(1)\n" \
  "s_barrier\n" \
  "v_smfmac_f32_16x16x64_bf16 v[52:55], v[128:131], a[200:207], v239\n" \
  "ds_read_b64_tr_b16 v[102:103], v243 offset:1536\n" \
  "ds_read_b64_tr_b16 v[100:101], v243 offset:1024\n" \
  "v_smfmac_f32_16x16x64_bf16 v[52:55], v[132:135], a[208:215], v239\n" \
  "ds_read_b64_tr_b16 v[138:139], v243 offset:512\n" \
  "ds_read_b64_tr_b16 v[136:137], v243\n" \
  "v_smfmac_f32_16x16x64_bf16 v[56:59], v[104:107], a[224:231], v239\n" \
  "s_waitcnt lgkmcnt(4)\n" \
  "s_barrier\n" \
  "v_smfmac_f32_16x16x64_bf16 v[56:59], v[108:111], a[232:239], v239\n" \
  "ds_read_b64_tr_b16 v[104:105], v243 offset:256\n" \
  "ds_read_b64_tr_b16 v[106:107], v243 offset:768\n" \
  "ds_read_b64_tr_b16 v[110:111], v243 offset:1792\n" \
  "v_smfmac_f32_16x16x64_bf16 v[56:59], v[112:115], a[240:247], v239\n" \
  "ds_read_b64_tr_b16 v[108:109], v243 offset:1280\n" \
  "s_waitcnt lgkmcnt(4)\n" \
  "v_mfma_f32_16x16x32_bf16 v[112:115], v[64:67], v[136:139], 0\n" \
  "v_add_f32 v52, v52, v54\n" \
  "v_mfma_f32_16x16x32_bf16 v[112:115], v[68:71], v[100:103], v[112:115]\n" \
  "s_waitcnt lgkmcnt(2)\n" \
  "v_mfma_f32_16x16x32_bf16 v[112:115], v[72:75], v[104:107], v[112:115]\n" \
  "s_waitcnt lgkmcnt(0)\n" \
  "v_mfma_f32_16x16x32_bf16 v[112:115], v[76:79], v[108:111], v[112:115]\n" \
  "s_nop 1\n" \
  "v_add_f32 v53, v53, v55\n" \
  "v_smfmac_f32_16x16x64_bf16 v[56:59], v[120:123], a[248:255], v239\n" \
  "s_barrier\n" \
  "ds_read_b64 v[188:189], v255\n" \
  "s_nop 1\n" \
  "v_exp_f32 v100, v112\n" \
  "v_exp_f32 v101, v113\n" \
  "v_exp_f32 v103, v115\n" \
  "v_exp_f32 v102, v114\n" \
  "v_cvt_pk_bf16_f32 v186, v100, v101\n" \
  "v_cvt_pk_bf16_f32 v187, v102, v103\n" \
  "v_smfmac_f32_16x16x64_bf16 v[56:59], v[124:127], v[0:7], v239\n" \
  "buffer_store_dwordx2 v[186:187], v246, s[40:43], 0 offen sc1\n" \
  "buffer_store_dwordx2 v[186:187], v247, s[40:43], 0 offen sc1\n" \
  "v_mfma_f32_16x16x16_bf16 v[100:103], v[182:183], v[186:187], 0\n" \
  "s_nop 0\n" \
  "s_waitcnt lgkmcnt(0)\n" \
  "v_add_f32 v53, v53, v189\n" \
  "v_smfmac_f32_16x16x64_bf16 v[56:59], v[116:119], v[8:15], v239\n" \
  "v_add_f32 v52, v52, v188\n" \
  "v_smfmac_f32_16x16x64_bf16 v[56:59], v[128:131], v[16:23], v239\n" \
  "v_cvt_pk_bf16_f32 v52, v52, v53\n" \
  "v_smfmac_f32_16x16x64_bf16 v[56:59], v[132:135], v[24:31], v239\n" \
  "ds_write_b32 v248, v100\n" \
  "ds_write_b32 v242, v52\n" \
  "s_nop 5\n" \
  "v_add_f32 v57, v57, v59\n" \
  "v_add_f32 v56, v56, v58\n" \
  "v_cvt_pk_bf16_f32 v56, v56, v57\n" \
  "ds_write_b32 v242, v56 offset:256\n" \
  "s_waitcnt lgkmcnt(1)\n" \
  "s_barrier\n" \
  "s_waitcnt lgkmcnt(0)\n" \
  "s_barrier\n" \
  "s_cbranch_scc1 .LX_xr_%=\n" \
  "s_memrealtime s[98:99]\n" \
  "s_memtime s[84:85]\n" \
  ".LX_loop_%=:\n" \
  "s_cmp_ge_u32 s48, 55\n" \
  "s_cbranch_scc1 .LX_xl_%=\n" \
  ".LX_xr_b_%=:\n" \
  "s_add_u32 s95, s48, 1\n" \
  "s_and_b32 s63, s95, 3\n" \
  "s_lshl_b32 s95, s95, 5\n" \
  "s_lshl_b32 s63, s63, 4\n" \
  "s_sub_u32 s95, s95, s63\n" \
  "s_add_u32 s95, s95, s45\n" \
  "s_mov_b64 exec, s[88:89]\n" \
  "buffer_load_dwordx4 v[36:39], off, s[40:43], s95 sc1\n" \
  "s_mov_b64 exec, -1\n" \
  "v_and_b32 v246, 3221225471, v246\n" \
  "v_and_b32 v247, 3221225471, v247\n" \
  "s_waitcnt lgkmcnt(0)\n" \
  "s_lshl_b32 s60, s66, 10\n" \
  "s_or_b32 s60, s60, s87\n" \
  "s_cmp_lg_u32 s68, 0\n" \
  "s_cselect_b32 s60, s60, 0\n" \
  "v_mov_b32 v187, s60\n" \
  "s_sub_u32 s76, s84, s90\n" \
  "s_sub_u32 s77, s98, s86\n" \
  "v_cvt_f32_u32 v188, s76\n" \
  "v_cvt_f32_u32 v189, s77\n" \
  "v_rcp_f32 v189, v189\n" \
  "v_mul_f32 v188, 16.0, v188\n" \
  "v_fma_f32 v188, v188, v189, 0.5\n" \
  "v_cvt_u32_f32 v188, v188\n" \
  "s_nop 0\n" \
  "v_readfirstlane_b32 s76, v188\n" \
  "s_max_u32 s76, s76, 160\n" \
  "s_min_u32 s76, s76, 400\n" \
  "s_add_u32 s77, s48, 0\n" \
  "s_and_b32 s77, s77, 3\n" \
  "s_cselect_b32 s87, s87, s76\n" \
  "s_cselect_b32 s86, s86, s98\n" \
  "s_cselect_b32 s90, s90, s84\n" \
  "s_add_u32 s48, s48, 1\n" \
  "ds_read_b64 v[64:65], v178\n" \
  "ds_read_b64 v[66:67], v178 offset:2048\n" \
  "ds_read_b64 v[68:69], v178 offset:4096\n" \
  "ds_read_b64 v[70:71], v178 offset:6144\n" \
  "ds_read_b64 v[72:73], v178 offset:8\n" \
  "ds_read_b64 v[74:75], v178 offset:2056\n" \
  "ds_read_b64 v[76:77], v178 offset:4104\n" \
  "ds_read_b64 v[78:79], v178 offset:6152\n" \
  "v_xor_b32 v178, 8192, v178\n" \
  "v_mov_b64 v[44:45], 0\n" \
  "v_mov_b64 v[46:47], 0\n" \
  "v_mov_b64 v[48:49], 0\n" \
  "v_mov_b64 v[50:51], 0\n" \
  "v_mov_b64 v[52:53], 0\n" \
  "v_mov_b64 v[54:55], 0\n" \
  "v_mov_b64 v[56:57], 0\n" \
  "v_mov_b64 v[58:59], 0\n" \
  "v_mov_b32 v40, s48\n" \
  "s_waitcnt vmcnt(0)\n" \
  "ds_write2_b32 v184, v40, v187 offset1:2\n" \
  "s_cmp_eq_u32 s48, 58\n" \
  "s_cbranch_scc1 .LX_ef_%=\n" \
  ".LX_ef_b_%=:\n" \
  "s_branch .LX_rot_%=\n" \
  "s_nop 7\n" \
  ".LX_slow_%=:\n" \
  "s_bitcmp1_b32 s70, 0\n" \
  "s_cbranch_scc1 .LX_back_%=\n" \
  "s_waitcnt lgkmcnt(0)\n" \
  "s_cmp_lt_u32 s70, 10\n" \
  "s_nop 3\n" \
  "s_cbranch_scc1 .LX_nol_%=\n" \
  "v_max_u32 v189, v100, v101\n" \
  "v_max3_u32 v189, v189, v102, v103\n" \
  "v_max3_u32 v189, v189, v104, v105\n" \
  "v_max3_u32 v189, v189, v106, v107\n" \
  "v_max3_u32 v189, v189, v108, v109\n" \
  "v_max3_u32 v189, v189, v110, v111\n" \
  "v_max3_u32 v189, v189, v112, v113\n" \
  "v_max3_u32 v189, v189, v114, v115\n" \
  "v_max3_u32 v189, v189, v120, v121\n" \
  "v_max3_u32 v189, v189, v122, v123\n" \
  "v_max3_u32 v189, v189, v124, v125\n" \
  "v_max3_u32 v189, v189, v126, v127\n" \
  "v_max3_u32 v189, v189, v116, v117\n" \
  "v_max3_u32 v189, v189, v118, v119\n" \
  "v_max3_u32 v189, v189, v128, v129\n" \
  "v_max3_u32 v189, v189, v130, v131\n" \
  "v_max3_u32 v189, v189, v132, v133\n" \
  "v_max3_u32 v189, v189, v134, v135\n" \
  "v_cmp_eq_u32 vcc, -1, v189\n" \
  "s_cbranch_vccz .LX_back_%=\n" \
  ".LX_nol_%=:\n" \
  "s_add_u32 s70, s70, 2\n" \
  "s_cmp_gt_u32 s70, 65536\n" \
  "s_cbranch_scc1 .LX_dset_%=\n" \
  "s_barrier\n" \
  "v_mov_b64 v[44:45], 0\n" \
  "v_mov_b64 v[46:47], 0\n" \
  "v_mov_b64 v[48:49], 0\n" \
  "v_mov_b64 v[50:51], 0\n" \
  "v_mov_b64 v[52:53], 0\n" \
  "v_mov_b64 v[54:55], 0\n" \
  "v_mov_b64 v[56:57], 0\n" \
  "v_mov_b64 v[58:59], 0\n" \
  "s_cmp_lg_u32 %28, 0\n" \
  "s_cbranch_scc1 .LX_g3_%=\n" \
  "s_and_b32 s62, %40, 0xffff\n" \
  "s_add_u32 s44, s44, s62\n" \
  "s_xor_b32 s59, s59, 0\n" \
  "s_mov_b32 s68, 0\n" \
  "s_mov_b32 m0, 0\n" \
  "s_branch .LX_rt_%=\n" \
  ".LX_dset_%=:\n" \
  "v_mov_b32 v124, 1\n" \
  "v_mov_b32 v125, s55\n" \
  "buffer_store_dwordx2 v[124:125], off, s[40:43], %38 offset:128 sc1\n" \
  "s_mov_b32 s70, 1\n" \
  "v_mov_b64 v[44:45], 0\n" \
  "v_mov_b64 v[46:47], 0\n" \
  "v_mov_b64 v[48:49], 0\n" \
  "v_mov_b64 v[50:51], 0\n" \
  "v_mov_b64 v[52:53], 0\n" \
  "v_mov_b64 v[54:55], 0\n" \
  "v_mov_b64 v[56:57], 0\n" \
  "v_mov_b64 v[58:59], 0\n" \
  "s_waitcnt vmcnt(0) lgkmcnt(0)\n" \
  "s_barrier\n" \
  "s_branch .LX_p_%=\n" \
  ".LX_xr_%=:\n" \
  "s_waitcnt lgkmcnt(0)\n" \
  "s_nop 7\n" \
  "ds_read_b64_tr_b16 v[132:133], v244\n" \
  "ds_read_b64_tr_b16 v[134:135], v244 offset:256\n" \
  "ds_read_b64_tr_b16 v[136:137], v244 offset:512\n" \
  "ds_read_b64_tr_b16 v[138:139], v244 offset:768\n" \
  "ds_read_b64_tr_b16 v[140:141], v244 offset:1024\n" \
  "ds_read_b64_tr_b16 v[142:143], v244 offset:1280\n" \
  "ds_read_b64_tr_b16 v[144:145], v244 offset:1536\n" \
  "ds_read_b64_tr_b16 v[146:147], v244 offset:1792\n" \
  "s_waitcnt lgkmcnt(6)\n" \
  "v_mfma_f32_16x16x32_bf16 v[148:151], v[132:135], v[84:87], 0\n" \
  "s_waitcnt lgkmcnt(4)\n" \
  "v_mfma_f32_16x16x32_bf16 v[148:151], v[136:139], v[88:91], v[148:151]\n" \
  "s_waitcnt lgkmcnt(2)\n" \
  "v_mfma_f32_16x16x32_bf16 v[148:151], v[140:143], v[92:95], v[148:151]\n" \
  "s_waitcnt lgkmcnt(0)\n" \
  "v_mfma_f32_16x16x32_bf16 v[148:151], v[144:147], v[96:99], v[148:151]\n" \
  "s_sub_u32 s60, s48, 58\n" \
  "s_lshl_b32 s60, s60, 16\n" \
  "s_add_u32 s60, s60, s97\n" \
  "s_cmp_eq_u32 s48, 0\n" \
  "s_cselect_b32 s60, 2130706432, s60\n" \
  "s_nop 2\n" \
  "v_max_f32 v158, v148, v149\n" \
  "v_max3_f32 v158, v150, v151, v158\n" \
  "v_mov_b32 v159, v158\n" \
  "s_nop 1\n" \
  "v_permlane32_swap_b32 v158, v159\n" \
  "v_max_f32 v158, v158, v159\n" \
  "v_mul_f32 v152, s65, v158\n" \
  "v_min_f32 v152, v152, v180\n" \
  "v_fma_f32 v148, v148, s64, v152\n" \
  "v_fma_f32 v149, v149, s64, v152\n" \
  "v_fma_f32 v150, v150, s64, v152\n" \
  "v_fma_f32 v151, v151, s64, v152\n" \
  "v_sub_f32 v160, v152, v180\n" \
  "v_exp_f32 v148, v148\n" \
  "v_exp_f32 v149, v149\n" \
  "v_exp_f32 v150, v150\n" \
  "v_exp_f32 v151, v151\n" \
  "v_exp_f32 v160, v160\n" \
  "v_add_f32 v153, v148, v149\n" \
  "v_add_f32 v158, v150, v151\n" \
  "v_add_f32 v153, v153, v158\n" \
  "v_mov_b32 v159, v153\n" \
  "s_nop 1\n" \
  "v_permlane32_swap_b32 v153, v159\n" \
  "v_add_f32 v153, v153, v159\n" \
  "v_fmac_f32 v153, v160, v181\n" \
  "v_rcp_f32 v154, v153\n" \
  "s_nop 0\n" \
  "v_mul_f32 v160, v160, v154\n" \
  "v_mul_f32 v154, v154, v252\n" \
  "v_mul_f32 v148, v148, v154\n" \
  "v_mul_f32 v149, v149, v154\n" \
  "v_mul_f32 v150, v150, v154\n" \
  "v_mul_f32 v151, v151, v154\n" \
  "v_fmac_f32 v148, v160, v253\n" \
  "v_fmac_f32 v150, v160, v254\n" \
  "v_cvt_pk_bf16_f32 v156, v148, v149\n" \
  "v_cvt_pk_bf16_f32 v157, v150, v151\n" \
  "buffer_store_dwordx2 v[156:157], v251, s[40:43], s60 offen sc1\n" \
  "s_cmp_eq_u32 s48, 63\n" \
  "s_cbranch_scc1 .LX_end_%=\n" \
  ".LX_xq_%=:\n" \
  "s_add_u32 s61, s48, 1\n" \
  "s_lshl_b32 s60, s61, 19\n" \
  "s_add_u32 s60, s60, s47\n" \
  "s_lshr_b32 s62, s60, 8\n" \
  "s_and_b32 s62, s62, 8192\n" \
  "s_xor_b32 s62, s62, s60\n" \
  "s_cmp_eq_u32 s48, 0\n" \
  "s_cselect_b32 s62, 2130706432, s62\n" \
  "s_add_u32 s63, s62, 4096\n" \
  "buffer_load_dwordx4 v[84:87], v249, s[40:43], s62 offen sc1\n" \
  "buffer_load_dwordx4 v[88:91], v249, s[40:43], s62 offen offset:2048 sc1\n" \
  "buffer_load_dwordx4 v[92:95], v249, s[40:43], s63 offen sc1\n" \
  "buffer_load_dwordx4 v[96:99], v249, s[40:43], s63 offen offset:2048 sc1\n" \
  "s_sub_u32 s60, s61, 58\n" \
  "s_lshl_b32 s60, s60, 16\n" \
  "s_add_u32 s60, s60, s97\n" \
  "s_cmp_eq_u32 s48, 0\n" \
  "s_cselect_b32 s60, 2130706432, s60\n" \
  "buffer_load_dwordx2 v[180:181], v250, s[40:43], s60 offen sc1\n" \
  "s_cbranch_scc1 .LX_dry_b_%=\n" \
  "s_branch .LX_xr_b_%=\n" \
  ".LX_xl_%=:\n" \
  "s_cmp_eq_u32 s48, 57\n" \
  "s_cbranch_scc1 .LX_xq_%=\n" \
  "s_cmp_eq_u32 s48, 56\n" \
  "s_cbranch_scc1 .LX_xc_%=\n" \
  "s_cmp_lg_u32 s48, 55\n" \
  "s_cbranch_scc1 .LX_xr_%=\n" \
  "buffer_load_dwordx2 v[180:181], off, s[40:43], %42 sc1\n" \
  "s_branch .LX_xr_b_%=\n" \
  ".LX_xc_%=:\n" \
  "v_readfirstlane_b32 s63, v181\n" \
  "s_cmp_eq_u32 s63, s55\n" \
  "s_cbranch_scc1 .LX_xr_b_%=\n" \
  "s_mov_b32 s69, 0\n" \
  ".LX_xs_%=:\n" \
  "buffer_load_dwordx2 v[180:181], off, s[40:43], %42 sc1\n" \
  "s_waitcnt vmcnt(0)\n" \
  "v_readfirstlane_b32 s62, v180\n" \
  "v_readfirstlane_b32 s63, v181\n" \
  "s_cmp_eq_u32 s63, s55\n" \
  "s_cselect_b32 s62, s62, 0\n" \
  "s_cmp_lg_u32 s62, 0\n" \
  "s_cbranch_scc1 .LX_xs_x_%=\n" \
  "s_sleep 2\n" \
  "s_add_u32 s69, s69, 1\n" \
  "s_and_b32 s63, s69, 1023\n" \
  "s_cbranch_scc1 .LX_xs_%=\n" \
  "s_cmp_gt_u32 s69, 2097152\n" \
  "s_cbranch_scc1 .LX_xs_set_%=\n" \
  "buffer_load_dwordx2 v[180:181], off, s[40:43], %38 offset:128 sc1\n" \
  "s_waitcnt vmcnt(0)\n" \
  "v_readfirstlane_b32 s63, v181\n" \
  "s_cmp_eq_u32 s63, s55\n" \
  "s_cbranch_scc0 .LX_xs_%=\n" \
  "s_branch .LX_xs_x_%=\n" \
  ".LX_xs_set_%=:\n" \
  "v_mov_b32 v180, 1\n" \
  "v_mov_b32 v181, s55\n" \
  "buffer_store_dwordx2 v[180:181], off, s[40:43], %38 offset:128 sc1\n" \
  "s_waitcnt vmcnt(0)\n" \
  ".LX_xs_x_%=:\n" \
  "s_branch .LX_xr_b_%=\n" \
  ".LX_ef_%=:\n" \
  "s_waitcnt vmcnt(0) lgkmcnt(0)\n" \
  "s_branch .LX_ef_b_%=\n" \
  ".LX_tw_re_%=:\n" \
  "s_add_u32 s69, s69, 1\n" \
  "s_cmp_gt_u32 s69, 200\n" \
  "s_cbranch_scc1 .LX_noclk_%=\n" \
  "s_sleep 2\n" \
  "buffer_load_dwordx4 v[36:39], off, s[40:43], s95 sc1\n" \
  "s_waitcnt vmcnt(0)\n" \
  "s_branch .LX_tw_%=\n" \
  ".LX_lat_%=:\n" \
  "s_lshr_b32 s63, %40, 16\n" \
  "s_add_u32 s62, s62, s63\n" \
  "s_cmp_lt_u32 s62, 4294937296\n" \
  "s_cbranch_scc1 .LX_noclk_%=\n" \
  "s_and_b32 s63, %40, 0xffff\n" \
  ".LX_lat1_%=:\n" \
  "s_add_u32 s44, s44, s63\n" \
  "s_add_u32 s62, s62, s63\n" \
  "s_cbranch_scc0 .LX_lat1_%=\n" \
  "s_branch .LX_rt_%=\n" \
  ".LX_clk2_%=:\n" \
  "s_cmp_le_i32 s62, 3\n" \
  "s_cbranch_scc1 .LX_clk_done_%=\n" \
  "s_memrealtime s[60:61]\n" \
  "s_waitcnt lgkmcnt(0)\n" \
  "s_sub_u32 s62, s73, s60\n" \
  "s_branch .LX_clk2_%=\n" \
  ".LX_clk_done_%=:\n" \
  "s_branch .LX_noclk_%=\n" \
  ".LX_end_%=:\n" \
  "s_waitcnt vmcnt(0)\n" \
  "s_waitcnt vmcnt(0)\n" \
  "s_nop 7\n" \
  "s_endpgm\n" \
  ".LX_other_%=:\n"
#define X_ASM_VOPS "+{v239}"(ci.sidx), "+{v240}"(ci.l16), "+{v241}"(ci.qw), "+{v242}"(ci.kw), "+{v243}"(ci.fq), "+{v244}"(ci.fk), "+{v245}"(ci.kxo), "+{v246}"(ci.pst), "+{v247}"(ci.ps2), "+{v248}"(ci.lx), "+{v249}"(ci.qxo), "+{v250}"(ci.sto), "+{v251}"(ci.plx), "+{v252}"(ci.mk), "+{v253}"(ci.ck0), "+{v254}"(ci.ck2), "+{v255}"(ci.hx), "+{v200}"(ci.vq0), "+{v201}"(ci.vq1), "+{v202}"(ci.vq2), "+{v203}"(ci.vq3), "+{v204}"(ci.tro0), "+{v205}"(ci.tro1), "+{v206}"(ci.m0o)
#define X_ASM_SOPS "s"(ci.ws), "s"(ci.Wa), "s"(ci.Wb), "s"(ci.mem0), "s"(ci.w), "s"(ci.o_a), "s"(ci.o_b), "s"(ci.o_nx), "s"(ci.o_exp), "s"(ci.lab), "s"(ci.f_p), "s"(ci.f_m), "s"(ci.f_dur), "s"(ci.f_go), "s"(ci.o_ctl), "s"(ci.o_dbg), "s"(ci.dret), "s"(ci.o_qx), "s"(ci.f_s), "s"(ci.f_r)
#define X_ASM_CLOB "v0", "v1", "v2", "v3", "v4", "v5", "v6", "v7", "v8", "v9", "v10", "v11", "v12", "v13", "v14", "v15", "v16", "v17", "v18", "v19", "v20", "v21", "v22", "v23", "v24", "v25", "v26", "v27", "v28", "v29", "v30", "v31", "v32", "v33", "v34", "v35", "v36", "v37", "v38", "v39", "v40", "v41", "v42", "v43", "v44", "v45", "v46", "v47", "v48", "v49", "v50", "v51", "v52", "v53", "v54", "v55", "v56", "v57", "v58", "v59", "v60", "v61", "v62", "v63", "v64", "v65", "v66", "v67", "v68", "v69", "v70", "v71", "v72", "v73", "v74", "v75", "v76", "v77", "v78", "v79", "v80", "v81", "v82", "v83", "v84", "v85", "v86", "v87", "v88", "v89", "v90", "v91", "v92", "v93", "v94", "v95", "v96", "v97", "v98", "v99", "v100", "v101", "v102", "v103", "v104", "v105", "v106", "v107", "v108", "v109", "v110", "v111", "v112", "v113", "v114", "v115", "v116", "v117", "v118", "v119", "v120", "v121", "v122", "v123", "v124", "v125", "v126", "v127", "v128", "v129", "v130", "v131", "v132", "v133", "v134", "v135", "v136", "v137", "v138", "v139", "v140", "v141", "v142", "v143", "v144", "v145", "v146", "v147", "v148", "v149", "v150", "v151", "v152", "v153", "v154", "v155", "v156", "v157", "v158", "v159", "v160", "v161", "v162", "v163", "v164", "v165", "v166", "v167", "v168", "v169", "v170", "v171", "v172", "v173", "v174", "v175", "v176", "v177", "v178", "v179", "v180", "v181", "v182", "v183", "v184", "v185", "v186", "v187", "v188", "v189", "v190", "v191", "v192", "v193", "v194", "v195", "v196", "v197", "v198", "v199", "a0", "a1", "a2", "a3", "a4", "a5", "a6", "a7", "a8", "a9", "a10", "a11", "a12", "a13", "a14", "a15", "a16", "a17", "a18", "a19", "a20", "a21", "a22", "a23", "a24", "a25", "a26", "a27", "a28", "a29", "a30", "a31", "a32", "a33", "a34", "a35", "a36", "a37", "a38", "a39", "a40", "a41", "a42", "a43", "a44", "a45", "a46", "a47", "a48", "a49", "a50", "a51", "a52", "a53", "a54", "a55", "a56", "a57", "a58", "a59", "a60", "a61", "a62", "a63", "a64", "a65", "a66", "a67", "a68", "a69", "a70", "a71", "a72", "a73", "a74", "a75", "a76", "a77", "a78", "a79", "a80", "a81", "a82", "a83", "a84", "a85", "a86", "a87", "a88", "a89", "a90", "a91", "a92", "a93", "a94", "a95", "a96", "a97", "a98", "a99", "a100", "a101", "a102", "a103", "a104", "a105", "a106", "a107", "a108", "a109", "a110", "a111", "a112", "a113", "a114", "a115", "a116", "a117", "a118", "a119", "a120", "a121", "a122", "a123", "a124", "a125", "a126", "a127", "a128", "a129", "a130", "a131", "a132", "a133", "a134", "a135", "a136", "a137", "a138", "a139", "a140", "a141", "a142", "a143", "a144", "a145", "a146", "a147", "a148", "a149", "a150", "a151", "a152", "a153", "a154", "a155", "a156", "a157", "a158", "a159", "a160", "a161", "a162", "a163", "a164", "a165", "a166", "a167", "a168", "a169", "a170", "a171", "a172", "a173", "a174", "a175", "a176", "a177", "a178", "a179", "a180", "a181", "a182", "a183", "a184", "a185", "a186", "a187", "a188", "a189", "a190", "a191", "a192", "a193", "a194", "a195", "a196", "a197", "a198", "a199", "a200", "a201", "a202", "a203", "a204", "a205", "a206", "a207", "a208", "a209", "a210", "a211", "a212", "a213", "a214", "a215", "a216", "a217", "a218", "a219", "a220", "a221", "a222", "a223", "a224", "a225", "a226", "a227", "a228", "a229", "a230", "a231", "a232", "a233", "a234", "a235", "a236", "a237", "a238", "a239", "a240", "a241", "a242", "a243", "a244", "a245", "a246", "a247", "a248", "a249", "a250", "a251", "a252", "a253", "a254", "a255", "s40", "s41", "s42", "s43", "s44", "s45", "s46", "s47", "s48", "s49", "s50", "s51", "s52", "s53", "s54", "s55", "s56", "s57", "s58", "s59", "s60", "s61", "s62", "s63", "s64", "s65", "s66", "s67", "s68", "s69", "s70", "s71", "s72", "s73", "s74", "s75", "s76", "s77", "s78", "s79", "s80", "s81", "s82", "s83", "s84", "s85", "s86", "s87", "s88", "s89", "s90", "s91", "s92", "s93", "s94", "s95", "s96", "s97", "s98", "s99", "vcc", "scc", "memory"
#define M_ASM_TEXT \
  "s_mov_b64 s[40:41], %24\n" \
  "s_and_b32 s41, s41, 0xffff\n" \
  "s_mov_b32 s42, 255852544\n" \
  "s_mov_b32 s43, 0x00020000\n" \
  "s_mov_b32 s46, 4718592\n" \
  "s_mov_b32 s47, 0x00020000\n" \
  "s_lshl_b32 s72, %28, 11\n" \
  "buffer_load_dword v196, off, s[40:43], %38 sc1\n" \
  "s_mov_b64 s[44:45], %25\n" \
  "s_and_b32 s45, s45, 0xffff\n" \
  "s_add_u32 s60, %29, 0\n" \
  "s_lshl_b32 s61, s72, 1\n" \
  "s_add_u32 m0, s61, 16384\n" \
  "s_nop 0\n" \
  "buffer_load_dwordx4 v200, s[44:47], s60 offen lds\n" \
  "buffer_load_dwordx4 v201, s[44:47], s60 offen offset:1024 lds\n" \
  "buffer_load_dwordx4 v202, s[44:47], s60 offen offset:2048 lds\n" \
  "buffer_load_dwordx4 v203, s[44:47], s60 offen offset:3072 lds\n" \
  "s_add_u32 s60, %29, 524288\n" \
  "s_lshl_b32 s61, s72, 1\n" \
  "s_add_u32 m0, s61, 32768\n" \
  "s_nop 0\n" \
  "buffer_load_dwordx4 v200, s[44:47], s60 offen lds\n" \
  "buffer_load_dwordx4 v201, s[44:47], s60 offen offset:1024 lds\n" \
  "buffer_load_dwordx4 v202, s[44:47], s60 offen offset:2048 lds\n" \
  "buffer_load_dwordx4 v203, s[44:47], s60 offen offset:3072 lds\n" \
  "s_add_u32 s60, %29, 1048576\n" \
  "s_lshl_b32 s61, s72, 1\n" \
  "s_add_u32 m0, s61, 49152\n" \
  "s_nop 0\n" \
  "buffer_load_dwordx4 v200, s[44:47], s60 offen lds\n" \
  "buffer_load_dwordx4 v201, s[44:47], s60 offen offset:1024 lds\n" \
  "buffer_load_dwordx4 v202, s[44:47], s60 offen offset:2048 lds\n" \
  "buffer_load_dwordx4 v203, s[44:47], s60 offen offset:3072 lds\n" \
  "s_add_u32 s60, %29, 1572864\n" \
  "s_lshl_b32 s61, s72, 1\n" \
  "s_add_u32 m0, s61, 65536\n" \
  "s_nop 0\n" \
  "buffer_load_dwordx4 v200, s[44:47], s60 offen lds\n" \
  "buffer_load_dwordx4 v201, s[44:47], s60 offen offset:1024 lds\n" \
  "buffer_load_dwordx4 v202, s[44:47], s60 offen offset:2048 lds\n" \
  "buffer_load_dwordx4 v203, s[44:47], s60 offen offset:3072 lds\n" \
  "s_add_u32 s60, %29, 2097152\n" \
  "s_lshl_b32 s61, s72, 1\n" \
  "s_add_u32 m0, s61, 81920\n" \
  "s_nop 0\n" \
  "buffer_load_dwordx4 v200, s[44:47], s60 offen lds\n" \
  "buffer_load_dwordx4 v201, s[44:47], s60 offen offset:1024 lds\n" \
  "buffer_load_dwordx4 v202, s[44:47], s60 offen offset:2048 lds\n" \
  "buffer_load_dwordx4 v203, s[44:47], s60 offen offset:3072 lds\n" \
  "s_add_u32 s60, %29, 2621440\n" \
  "s_lshl_b32 s61, s72, 1\n" \
  "s_add_u32 m0, s61, 98304\n" \
  "s_nop 0\n" \
  "buffer_load_dwordx4 v200, s[44:47], s60 offen lds\n" \
  "buffer_load_dwordx4 v201, s[44:47], s60 offen offset:1024 lds\n" \
  "buffer_load_dwordx4 v202, s[44:47], s60 offen offset:2048 lds\n" \
  "buffer_load_dwordx4 v203, s[44:47], s60 offen offset:3072 lds\n" \
  "s_add_u32 s60, %29, 3145728\n" \
  "s_lshl_b32 s61, s72, 1\n" \
  "s_add_u32 m0, s61, 114688\n" \
  "s_nop 0\n" \
  "buffer_load_dwordx4 v200, s[44:47], s60 offen lds\n" \
  "buffer_load_dwordx4 v201, s[44:47], s60 offen offset:1024 lds\n" \
  "buffer_load_dwordx4 v202, s[44:47], s60 offen offset:2048 lds\n" \
  "buffer_load_dwordx4 v203, s[44:47], s60 offen offset:3072 lds\n" \
  "s_add_u32 s60, %29, 3670016\n" \
  "s_lshl_b32 s61, s72, 1\n" \
  "s_add_u32 m0, s61, 131072\n" \
  "s_nop 0\n" \
  "buffer_load_dwordx4 v200, s[44:47], s60 offen lds\n" \
  "buffer_load_dwordx4 v201, s[44:47], s60 offen offset:1024 lds\n" \
  "buffer_load_dwordx4 v202, s[44:47], s60 offen offset:2048 lds\n" \
  "buffer_load_dwordx4 v203, s[44:47], s60 offen offset:3072 lds\n" \
  "s_add_u32 s60, %29, 4194304\n" \
  "s_lshl_b32 s61, s72, 1\n" \
  "s_add_u32 m0, s61, 147456\n" \
  "s_nop 0\n" \
  "buffer_load_dwordx4 v200, s[44:47], s60 offen lds\n" \
  "buffer_load_dwordx4 v201, s[44:47], s60 offen offset:1024 lds\n" \
  "buffer_load_dwordx4 v202, s[44:47], s60 offen offset:2048 lds\n" \
  "buffer_load_dwordx4 v203, s[44:47], s60 offen offset:3072 lds\n" \
  "s_mov_b64 s[44:45], %27\n" \
  "s_and_b32 s45, s45, 0xffff\n" \
  "s_lshr_b32 s60, s72, 3\n" \
  "s_mov_b32 m0, s72\n" \
  "s_nop 0\n" \
  "buffer_load_dwordx4 v206, s[44:47], s60 offen lds\n" \
  "s_add_u32 s60, s60, 128\n" \
  "s_add_u32 m0, s72, 1024\n" \
  "s_nop 0\n" \
  "buffer_load_dwordx4 v206, s[44:47], s60 offen lds\n" \
  "s_movk_i32 s60, 1024\n" \
  "s_movk_i32 m0, 8192\n" \
  "s_nop 0\n" \
  "buffer_load_dwordx4 v206, s[44:47], s60 offen lds\n" \
  "s_waitcnt vmcnt(27)\n" \
  "s_barrier\n" \
  "v_add_u32 v194, 16384, v204\n" \
  "ds_read_b64_tr_b16 a[0:1], v194\n" \
  "ds_read_b64_tr_b16 a[2:3], v194 offset:512\n" \
  "ds_read_b64_tr_b16 a[4:5], v194 offset:8192\n" \
  "ds_read_b64_tr_b16 a[6:7], v194 offset:8704\n" \
  "v_add_u32 v195, 16384, v205\n" \
  "ds_read_b64_tr_b16 a[72:73], v195\n" \
  "ds_read_b64_tr_b16 a[74:75], v195 offset:512\n" \
  "ds_read_b64_tr_b16 a[76:77], v195 offset:8192\n" \
  "ds_read_b64_tr_b16 a[78:79], v195 offset:8704\n" \
  "v_add_u32 v194, 32768, v204\n" \
  "ds_read_b64_tr_b16 a[8:9], v194\n" \
  "ds_read_b64_tr_b16 a[10:11], v194 offset:512\n" \
  "ds_read_b64_tr_b16 a[12:13], v194 offset:8192\n" \
  "ds_read_b64_tr_b16 a[14:15], v194 offset:8704\n" \
  "v_add_u32 v195, 32768, v205\n" \
  "ds_read_b64_tr_b16 a[80:81], v195\n" \
  "ds_read_b64_tr_b16 a[82:83], v195 offset:512\n" \
  "ds_read_b64_tr_b16 a[84:85], v195 offset:8192\n" \
  "ds_read_b64_tr_b16 a[86:87], v195 offset:8704\n" \
  "v_add_u32 v194, 49152, v204\n" \
  "ds_read_b64_tr_b16 a[16:17], v194\n" \
  "ds_read_b64_tr_b16 a[18:19], v194 offset:512\n" \
  "ds_read_b64_tr_b16 a[20:21], v194 offset:8192\n" \
  "ds_read_b64_tr_b16 a[22:23], v194 offset:8704\n" \
  "v_add_u32 v195, 49152, v205\n" \
  "ds_read_b64_tr_b16 a[88:89], v195\n" \
  "ds_read_b64_tr_b16 a[90:91], v195 offset:512\n" \
  "ds_read_b64_tr_b16 a[92:93], v195 offset:8192\n" \
  "ds_read_b64_tr_b16 a[94:95], v195 offset:8704\n" \
  "s_waitcnt lgkmcnt(0)\n" \
  "s_barrier\n" \
  "s_mov_b64 s[44:45], %26\n" \
  "s_and_b32 s45, s45, 0xffff\n" \
  "s_add_u32 s60, %29, 0\n" \
  "s_lshl_b32 s61, s72, 1\n" \
  "s_add_u32 m0, s61, 16384\n" \
  "s_nop 0\n" \
  "buffer_load_dwordx4 v200, s[44:47], s60 offen lds\n" \
  "buffer_load_dwordx4 v201, s[44:47], s60 offen offset:1024 lds\n" \
  "buffer_load_dwordx4 v202, s[44:47], s60 offen offset:2048 lds\n" \
  "buffer_load_dwordx4 v203, s[44:47], s60 offen offset:3072 lds\n" \
  "s_add_u32 s60, %29, 524288\n" \
  "s_lshl_b32 s61, s72, 1\n" \
  "s_add_u32 m0, s61, 32768\n" \
  "s_nop 0\n" \
  "buffer_load_dwordx4 v200, s[44:47], s60 offen lds\n" \
  "buffer_load_dwordx4 v201, s[44:47], s60 offen offset:1024 lds\n" \
  "buffer_load_dwordx4 v202, s[44:47], s60 offen offset:2048 lds\n" \
  "buffer_load_dwordx4 v203, s[44:47], s60 offen offset:3072 lds\n" \
  "s_add_u32 s60, %29, 1048576\n" \
  "s_lshl_b32 s61, s72, 1\n" \
  "s_add_u32 m0, s61, 49152\n" \
  "s_nop 0\n" \
  "buffer_load_dwordx4 v200, s[44:47], s60 offen lds\n" \
  "buffer_load_dwordx4 v201, s[44:47], s60 offen offset:1024 lds\n" \
  "buffer_load_dwordx4 v202, s[44:47], s60 offen offset:2048 lds\n" \
  "buffer_load_dwordx4 v203, s[44:47], s60 offen offset:3072 lds\n" \
  "v_readfirstlane_b32 s55, v196\n" \
  "s_add_u32 s55, s55, 1\n" \
  "s_waitcnt vmcnt(27)\n" \
  "s_barrier\n" \
  "v_add_u32 v194, 65536, v204\n" \
  "ds_read_b64_tr_b16 a[24:25], v194\n" \
  "ds_read_b64_tr_b16 a[26:27], v194 offset:512\n" \
  "ds_read_b64_tr_b16 a[28:29], v194 offset:8192\n" \
  "ds_read_b64_tr_b16 a[30:31], v194 offset:8704\n" \
  "v_add_u32 v195, 65536, v205\n" \
  "ds_read_b64_tr_b16 a[96:97], v195\n" \
  "ds_read_b64_tr_b16 a[98:99], v195 offset:512\n" \
  "ds_read_b64_tr_b16 a[100:101], v195 offset:8192\n" \
  "ds_read_b64_tr_b16 a[102:103], v195 offset:8704\n" \
  "v_add_u32 v194, 81920, v204\n" \
  "ds_read_b64_tr_b16 a[32:33], v194\n" \
  "ds_read_b64_tr_b16 a[34:35], v194 offset:512\n" \
  "ds_read_b64_tr_b16 a[36:37], v194 offset:8192\n" \
  "ds_read_b64_tr_b16 a[38:39], v194 offset:8704\n" \
  "v_add_u32 v195, 81920, v205\n" \
  "ds_read_b64_tr_b16 a[104:105], v195\n" \
  "ds_read_b64_tr_b16 a[106:107], v195 offset:512\n" \
  "ds_read_b64_tr_b16 a[108:109], v195 offset:8192\n" \
  "ds_read_b64_tr_b16 a[110:111], v195 offset:8704\n" \
  "v_add_u32 v194, 98304, v204\n" \
  "ds_read_b64_tr_b16 a[40:41], v194\n" \
  "ds_read_b64_tr_b16 a[42:43], v194 offset:512\n" \
  "ds_read_b64_tr_b16 a[44:45], v194 offset:8192\n" \
  "ds_read_b64_tr_b16 a[46:47], v194 offset:8704\n" \
  "v_add_u32 v195, 98304, v205\n" \
  "ds_read_b64_tr_b16 a[112:113], v195\n" \
  "ds_read_b64_tr_b16 a[114:115], v195 offset:512\n" \
  "ds_read_b64_tr_b16 a[116:117], v195 offset:8192\n" \
  "ds_read_b64_tr_b16 a[118:119], v195 offset:8704\n" \
  "s_waitcnt lgkmcnt(0)\n" \
  "s_barrier\n" \
  "s_add_u32 s60, %29, 1572864\n" \
  "s_lshl_b32 s61, s72, 1\n" \
  "s_add_u32 m0, s61, 65536\n" \
  "s_nop 0\n" \
  "buffer_load_dwordx4 v200, s[44:47], s60 offen lds\n" \
  "buffer_load_dwordx4 v201, s[44:47], s60 offen offset:1024 lds\n" \
  "buffer_load_dwordx4 v202, s[44:47], s60 offen offset:2048 lds\n" \
  "buffer_load_dwordx4 v203, s[44:47], s60 offen offset:3072 lds\n" \
  "s_add_u32 s60, %29, 2097152\n" \
  "s_lshl_b32 s61, s72, 1\n" \
  "s_add_u32 m0, s61, 81920\n" \
  "s_nop 0\n" \
  "buffer_load_dwordx4 v200, s[44:47], s60 offen lds\n" \
  "buffer_load_dwordx4 v201, s[44:47], s60 offen offset:1024 lds\n" \
  "buffer_load_dwordx4 v202, s[44:47], s60 offen offset:2048 lds\n" \
  "buffer_load_dwordx4 v203, s[44:47], s60 offen offset:3072 lds\n" \
  "s_add_u32 s60, %29, 2621440\n" \
  "s_lshl_b32 s61, s72, 1\n" \
  "s_add_u32 m0, s61, 98304\n" \
  "s_nop 0\n" \
  "buffer_load_dwordx4 v200, s[44:47], s60 offen lds\n" \
  "buffer_load_dwordx4 v201, s[44:47], s60 offen offset:1024 lds\n" \
  "buffer_load_dwordx4 v202, s[44:47], s60 offen offset:2048 lds\n" \
  "buffer_load_dwordx4 v203, s[44:47], s60 offen offset:3072 lds\n" \
  "s_waitcnt vmcnt(27)\n" \
  "s_barrier\n" \
  "v_add_u32 v194, 114688, v204\n" \
  "ds_read_b64_tr_b16 a[48:49], v194\n" \
  "ds_read_b64_tr_b16 a[50:51], v194 offset:512\n" \
  "ds_read_b64_tr_b16 a[52:53], v194 offset:8192\n" \
  "ds_read_b64_tr_b16 a[54:55], v194 offset:8704\n" \
  "v_add_u32 v195, 114688, v205\n" \
  "ds_read_b64_tr_b16 a[120:121], v195\n" \
  "ds_read_b64_tr_b16 a[122:123], v195 offset:512\n" \
  "ds_read_b64_tr_b16 a[124:125], v195 offset:8192\n" \
  "ds_read_b64_tr_b16 a[126:127], v195 offset:8704\n" \
  "v_add_u32 v194, 131072, v204\n" \
  "ds_read_b64_tr_b16 a[56:57], v194\n" \
  "ds_read_b64_tr_b16 a[58:59], v194 offset:512\n" \
  "ds_read_b64_tr_b16 a[60:61], v194 offset:8192\n" \
  "ds_read_b64_tr_b16 a[62:63], v194 offset:8704\n" \
  "v_add_u32 v195, 131072, v205\n" \
  "ds_read_b64_tr_b16 a[128:129], v195\n" \
  "ds_read_b64_tr_b16 a[130:131], v195 offset:512\n" \
  "ds_read_b64_tr_b16 a[132:133], v195 offset:8192\n" \
  "ds_read_b64_tr_b16 a[134:135], v195 offset:8704\n" \
  "v_add_u32 v194, 147456, v204\n" \
  "ds_read_b64_tr_b16 a[64:65], v194\n" \
  "ds_read_b64_tr_b16 a[66:67], v194 offset:512\n" \
  "ds_read_b64_tr_b16 a[68:69], v194 offset:8192\n" \
  "ds_read_b64_tr_b16 a[70:71], v194 offset:8704\n" \
  "v_add_u32 v195, 147456, v205\n" \
  "ds_read_b64_tr_b16 a[136:137], v195\n" \
  "ds_read_b64_tr_b16 a[138:139], v195 offset:512\n" \
  "ds_read_b64_tr_b16 a[140:141], v195 offset:8192\n" \
  "ds_read_b64_tr_b16 a[142:143], v195 offset:8704\n" \
  "s_waitcnt lgkmcnt(0)\n" \
  "s_barrier\n" \
  "s_add_u32 s60, %29, 3145728\n" \
  "s_lshl_b32 s61, s72, 1\n" \
  "s_add_u32 m0, s61, 114688\n" \
  "s_nop 0\n" \
  "buffer_load_dwordx4 v200, s[44:47], s60 offen lds\n" \
  "buffer_load_dwordx4 v201, s[44:47], s60 offen offset:1024 lds\n" \
  "buffer_load_dwordx4 v202, s[44:47], s60 offen offset:2048 lds\n" \
  "buffer_load_dwordx4 v203, s[44:47], s60 offen offset:3072 lds\n" \
  "s_add_u32 s60, %29, 3670016\n" \
  "s_lshl_b32 s61, s72, 1\n" \
  "s_add_u32 m0, s61, 131072\n" \
  "s_nop 0\n" \
  "buffer_load_dwordx4 v200, s[44:47], s60 offen lds\n" \
  "buffer_load_dwordx4 v201, s[44:47], s60 offen offset:1024 lds\n" \
  "buffer_load_dwordx4 v202, s[44:47], s60 offen offset:2048 lds\n" \
  "buffer_load_dwordx4 v203, s[44:47], s60 offen offset:3072 lds\n" \
  "s_add_u32 s60, %29, 4194304\n" \
  "s_lshl_b32 s61, s72, 1\n" \
  "s_add_u32 m0, s61, 147456\n" \
  "s_nop 0\n" \
  "buffer_load_dwordx4 v200, s[44:47], s60 offen lds\n" \
  "buffer_load_dwordx4 v201, s[44:47], s60 offen offset:1024 lds\n" \
  "buffer_load_dwordx4 v202, s[44:47], s60 offen offset:2048 lds\n" \
  "buffer_load_dwordx4 v203, s[44:47], s60 offen offset:3072 lds\n" \
  "buffer_load_dwordx2 v[198:199], off, s[40:43], %37 sc1\n" \
  "s_waitcnt vmcnt(25)\n" \
  "s_barrier\n" \
  "v_add_u32 v194, 16384, v204\n" \
  "ds_read_b64_tr_b16 a[144:145], v194\n" \
  "ds_read_b64_tr_b16 a[146:147], v194 offset:512\n" \
  "ds_read_b64_tr_b16 a[148:149], v194 offset:8192\n" \
  "ds_read_b64_tr_b16 a[150:151], v194 offset:8704\n" \
  "v_add_u32 v195, 16384, v205\n" \
  "ds_read_b64_tr_b16 a[216:217], v195\n" \
  "ds_read_b64_tr_b16 a[218:219], v195 offset:512\n" \
  "ds_read_b64_tr_b16 a[220:221], v195 offset:8192\n" \
  "ds_read_b64_tr_b16 a[222:223], v195 offset:8704\n" \
  "v_xor_b32 v195, 128, v194\n" \
  "ds_read_b64_tr_b16 v[32:33], v195\n" \
  "ds_read_b64_tr_b16 v[34:35], v195 offset:512\n" \
  "ds_read_b64_tr_b16 v[36:37], v195 offset:8192\n" \
  "ds_read_b64_tr_b16 v[38:39], v195 offset:8704\n" \
  "v_add_u32 v194, 32768, v204\n" \
  "ds_read_b64_tr_b16 a[152:153], v194\n" \
  "ds_read_b64_tr_b16 a[154:155], v194 offset:512\n" \
  "ds_read_b64_tr_b16 a[156:157], v194 offset:8192\n" \
  "ds_read_b64_tr_b16 a[158:159], v194 offset:8704\n" \
  "v_add_u32 v195, 32768, v205\n" \
  "ds_read_b64_tr_b16 a[224:225], v195\n" \
  "ds_read_b64_tr_b16 a[226:227], v195 offset:512\n" \
  "ds_read_b64_tr_b16 a[228:229], v195 offset:8192\n" \
  "ds_read_b64_tr_b16 a[230:231], v195 offset:8704\n" \
  "v_xor_b32 v195, 128, v194\n" \
  "ds_read_b64_tr_b16 v[40:41], v195\n" \
  "ds_read_b64_tr_b16 v[42:43], v195 offset:512\n" \
  "ds_read_b64_tr_b16 v[44:45], v195 offset:8192\n" \
  "ds_read_b64_tr_b16 v[46:47], v195 offset:8704\n" \
  "v_add_u32 v194, 49152, v204\n" \
  "ds_read_b64_tr_b16 a[160:161], v194\n" \
  "ds_read_b64_tr_b16 a[162:163], v194 offset:512\n" \
  "ds_read_b64_tr_b16 a[164:165], v194 offset:8192\n" \
  "ds_read_b64_tr_b16 a[166:167], v194 offset:8704\n" \
  "v_add_u32 v195, 49152, v205\n" \
  "ds_read_b64_tr_b16 a[232:233], v195\n" \
  "ds_read_b64_tr_b16 a[234:235], v195 offset:512\n" \
  "ds_read_b64_tr_b16 a[236:237], v195 offset:8192\n" \
  "ds_read_b64_tr_b16 a[238:239], v195 offset:8704\n" \
  "v_xor_b32 v195, 128, v194\n" \
  "ds_read_b64_tr_b16 v[48:49], v195\n" \
  "ds_read_b64_tr_b16 v[50:51], v195 offset:512\n" \
  "ds_read_b64_tr_b16 v[52:53], v195 offset:8192\n" \
  "ds_read_b64_tr_b16 v[54:55], v195 offset:8704\n" \
  "s_waitcnt lgkmcnt(0)\n" \
  "s_waitcnt vmcnt(13)\n" \
  "s_barrier\n" \
  "v_add_u32 v194, 65536, v204\n" \
  "ds_read_b64_tr_b16 a[168:169], v194\n" \
  "ds_read_b64_tr_b16 a[170:171], v194 offset:512\n" \
  "ds_read_b64_tr_b16 a[172:173], v194 offset:8192\n" \
  "ds_read_b64_tr_b16 a[174:175], v194 offset:8704\n" \
  "v_add_u32 v195, 65536, v205\n" \
  "ds_read_b64_tr_b16 a[240:241], v195\n" \
  "ds_read_b64_tr_b16 a[242:243], v195 offset:512\n" \
  "ds_read_b64_tr_b16 a[244:245], v195 offset:8192\n" \
  "ds_read_b64_tr_b16 a[246:247], v195 offset:8704\n" \
  "v_xor_b32 v195, 128, v194\n" \
  "ds_read_b64_tr_b16 v[56:57], v195\n" \
  "ds_read_b64_tr_b16 v[58:59], v195 offset:512\n" \
  "ds_read_b64_tr_b16 v[60:61], v195 offset:8192\n" \
  "ds_read_b64_tr_b16 v[62:63], v195 offset:8704\n" \
  "v_add_u32 v194, 81920, v204\n" \
  "ds_read_b64_tr_b16 a[176:177], v194\n" \
  "ds_read_b64_tr_b16 a[178:179], v194 offset:512\n" \
  "ds_read_b64_tr_b16 a[180:181], v194 offset:8192\n" \
  "ds_read_b64_tr_b16 a[182:183], v194 offset:8704\n" \
  "v_add_u32 v195, 81920, v205\n" \
  "ds_read_b64_tr_b16 a[248:249], v195\n" \
  "ds_read_b64_tr_b16 a[250:251], v195 offset:512\n" \
  "ds_read_b64_tr_b16 a[252:253], v195 offset:8192\n" \
  "ds_read_b64_tr_b16 a[254:255], v195 offset:8704\n" \
  "v_add_u32 v194, 98304, v204\n" \
  "ds_read_b64_tr_b16 a[184:185], v194\n" \
  "ds_read_b64_tr_b16 a[186:187], v194 offset:512\n" \
  "ds_read_b64_tr_b16 a[188:189], v194 offset:8192\n" \
  "ds_read_b64_tr_b16 a[190:191], v194 offset:8704\n" \
  "v_add_u32 v195, 98304, v205\n" \
  "ds_read_b64_tr_b16 v[0:1], v195\n" \
  "ds_read_b64_tr_b16 v[2:3], v195 offset:512\n" \
  "ds_read_b64_tr_b16 v[4:5], v195 offset:8192\n" \
  "ds_read_b64_tr_b16 v[6:7], v195 offset:8704\n" \
  "s_waitcnt lgkmcnt(0)\n" \
  "s_waitcnt vmcnt(1)\n" \
  "s_barrier\n" \
  "v_add_u32 v194, 114688, v204\n" \
  "ds_read_b64_tr_b16 a[192:193], v194\n" \
  "ds_read_b64_tr_b16 a[194:195], v194 offset:512\n" \
  "ds_read_b64_tr_b16 a[196:197], v194 offset:8192\n" \
  "ds_read_b64_tr_b16 a[198:199], v194 offset:8704\n" \
  "v_add_u32 v195, 114688, v205\n" \
  "ds_read_b64_tr_b16 v[8:9], v195\n" \
  "ds_read_b64_tr_b16 v[10:11], v195 offset:512\n" \
  "ds_read_b64_tr_b16 v[12:13], v195 offset:8192\n" \
  "ds_read_b64_tr_b16 v[14:15], v195 offset:8704\n" \
  "v_add_u32 v194, 131072, v204\n" \
  "ds_read_b64_tr_b16 a[200:201], v194\n" \
  "ds_read_b64_tr_b16 a[202:203], v194 offset:512\n" \
  "ds_read_b64_tr_b16 a[204:205], v194 offset:8192\n" \
  "ds_read_b64_tr_b16 a[206:207], v194 offset:8704\n" \
  "v_add_u32 v195, 131072, v205\n" \
  "ds_read_b64_tr_b16 v[16:17], v195\n" \
  "ds_read_b64_tr_b16 v[18:19], v195 offset:512\n" \
  "ds_read_b64_tr_b16 v[20:21], v195 offset:8192\n" \
  "ds_read_b64_tr_b16 v[22:23], v195 offset:8704\n" \
  "v_add_u32 v194, 147456, v204\n" \
  "ds_read_b64_tr_b16 a[208:209], v194\n" \
  "ds_read_b64_tr_b16 a[210:211], v194 offset:512\n" \
  "ds_read_b64_tr_b16 a[212:213], v194 offset:8192\n" \
  "ds_read_b64_tr_b16 a[214:215], v194 offset:8704\n" \
  "v_add_u32 v195, 147456, v205\n" \
  "ds_read_b64_tr_b16 v[24:25], v195\n" \
  "ds_read_b64_tr_b16 v[26:27], v195 offset:512\n" \
  "ds_read_b64_tr_b16 v[28:29], v195 offset:8192\n" \
  "ds_read_b64_tr_b16 v[30:31], v195 offset:8704\n" \
  "s_waitcnt lgkmcnt(0)\n" \
  "s_waitcnt vmcnt(0)\n" \
  "v_readfirstlane_b32 s96, v198\n" \
  "v_readfirstlane_b32 s63, v199\n" \
  "s_cmp_eq_u32 s63, s55\n" \
  "s_cselect_b32 s96, s96, 0\n" \
  "s_cmp_ge_u32 s96, 0xc0000000\n" \
  "s_cbranch_scc1 .LM_go_x_%=\n" \
  "s_mov_b32 s69, 0\n" \
  ".LM_go_%=:\n" \
  "buffer_load_dwordx2 v[196:197], off, s[40:43], %37 sc1\n" \
  "s_waitcnt vmcnt(0)\n" \
  "v_readfirstlane_b32 s96, v196\n" \
  "v_readfirstlane_b32 s63, v197\n" \
  "s_cmp_eq_u32 s63, s55\n" \
  "s_cselect_b32 s96, s96, 0\n" \
  "s_cmp_ge_u32 s96, 0xc0000000\n" \
  "s_cbranch_scc1 .LM_go_x_%=\n" \
  "s_sleep 8\n" \
  "s_add_u32 s69, s69, 1\n" \
  "s_and_b32 s63, s69, 1023\n" \
  "s_cbranch_scc1 .LM_go_%=\n" \
  "s_cmp_gt_u32 s69, 2097152\n" \
  "s_cbranch_scc1 .LM_go_set_%=\n" \
  "buffer_load_dwordx2 v[196:197], off, s[40:43], %38 offset:128 sc1\n" \
  "s_waitcnt vmcnt(0)\n" \
  "v_readfirstlane_b32 s63, v197\n" \
  "s_cmp_eq_u32 s63, s55\n" \
  "s_cbranch_scc0 .LM_go_%=\n" \
  "s_branch .LM_go_x_%=\n" \
  ".LM_go_set_%=:\n" \
  "v_mov_b32 v196, 1\n" \
  "v_mov_b32 v197, s55\n" \
  "buffer_store_dwordx2 v[196:197], off, s[40:43], %38 offset:128 sc1\n" \
  "s_waitcnt vmcnt(0)\n" \
  ".LM_go_x_%=:\n" \
  "s_and_b32 s60, s96, 2\n" \
  "s_lshl_b32 s60, s60, 12\n" \
  "s_xor_b32 s74, %34, s60\n" \
  "s_xor_b32 s75, %36, s60\n" \
  "s_bfe_u32 s56, s96, 0x30010\n" \
  "s_lshl_b32 s56, s56, 12\n" \
  "s_or_b32 s56, s56, 0x200000\n" \
  "s_mov_b32 s57, 1052672\n" \
  "s_lshl_b32 s61, %33, 2\n" \
  "s_lshr_b32 s51, s96, s61\n" \
  "s_and_b32 s51, s51, 7\n" \
  "s_lshl_b32 s51, s51, 12\n" \
  "s_xor_b32 s51, s51, 4210688\n" \
  "s_mov_b32 s71, 0\n" \
  "s_add_u32 s45, s51, 886784\n" \
  "s_mov_b32 s46, -1\n" \
  "v_mov_b64 v[64:65], -1\n" \
  "v_mov_b64 v[66:67], -1\n" \
  "v_and_b32 v194, 3, v246\n" \
  "v_lshlrev_b32 v194, 2, v194\n" \
  "v_lshrrev_b32 v194, v194, s96\n" \
  "v_and_b32 v194, 7, v194\n" \
  "v_lshlrev_b32 v194, 12, v194\n" \
  "v_and_b32 v246, -4, v246\n" \
  "v_xor_b32 v246, v246, v194\n" \
  "v_and_b32 v194, 3, v247\n" \
  "v_lshlrev_b32 v194, 2, v194\n" \
  "v_lshrrev_b32 v194, v194, s96\n" \
  "v_and_b32 v194, 7, v194\n" \
  "v_lshlrev_b32 v194, 12, v194\n" \
  "v_and_b32 v247, -4, v247\n" \
  "v_xor_b32 v247, v247, v194\n" \
  "v_xor_b32 v190, s56, v246\n" \
  "buffer_store_dwordx2 v[64:65], v190, s[40:43], 0 offen sc1\n" \
  "v_xor_b32 v191, s56, v247\n" \
  "buffer_store_dwordx2 v[64:65], v191, s[40:43], 0 offen sc1\n" \
  "buffer_store_dwordx2 v[64:65], v191, s[40:43], 0 offen offset:256 sc1\n" \
  "s_xor_b32 s62, s56, s57\n" \
  "s_nop 1\n" \
  "v_xor_b32 v190, s62, v246\n" \
  "buffer_store_dwordx2 v[64:65], v190, s[40:43], 0 offen sc1\n" \
  "v_xor_b32 v191, s62, v247\n" \
  "buffer_store_dwordx2 v[64:65], v191, s[40:43], 0 offen sc1\n" \
  "buffer_store_dwordx2 v[64:65], v191, s[40:43], 0 offen offset:256 sc1\n" \
  "s_andn2_b32 s60, 2, s96\n" \
  "s_lshl_b32 s60, s60, 12\n" \
  "s_xor_b32 s60, s60, 8192\n" \
  "s_xor_b32 s58, %31, s60\n" \
  "s_mov_b32 s49, 0\n" \
  "s_add_u32 s47, %41, s60\n" \
  "s_add_u32 s97, s60, 8388608\n" \
  "s_xor_b32 s50, %32, s60\n" \
  "s_mov_b32 s48, 0\n" \
  "s_mov_b32 s54, 0\n" \
  "s_cmp_eq_u32 %28, 0\n" \
  "s_cselect_b64 s[88:89], -1, 0\n" \
  "s_mov_b32 s68, 0\n" \
  "s_mov_b32 s70, 1\n" \
  "s_mov_b32 s73, 0\n" \
  "s_mov_b32 s44, 0\n" \
  "s_mov_b32 s53, 0\n" \
  "s_memrealtime s[86:87]\n" \
  "s_memtime s[76:77]\n" \
  "s_waitcnt lgkmcnt(0)\n" \
  "s_mov_b32 s90, s76\n" \
  "s_mov_b32 s87, 340\n" \
  "s_mov_b32 s64, 0x3e0293ee\n" \
  "s_mov_b32 s65, 0xbe0293ee\n" \
  "v_mov_b64 v[76:77], 0\n" \
  "v_mov_b64 v[78:79], 0\n" \
  "v_mov_b64 v[80:81], 0\n" \
  "v_mov_b64 v[82:83], 0\n" \
  "v_mov_b64 v[84:85], 0\n" \
  "v_mov_b64 v[86:87], 0\n" \
  "v_mov_b64 v[88:89], 0\n" \
  "v_mov_b64 v[90:91], 0\n" \
  "v_mov_b64 v[92:93], 0\n" \
  "v_mov_b64 v[94:95], 0\n" \
  "v_mov_b32 v72, 0\n" \
  "v_mov_b32 v73, s55\n" \
  "v_min_u32 v74, 1, v240\n" \
  "v_lshlrev_b32 v74, 30, v74\n" \
  "s_mov_b64 s[92:93], 0\n" \
  "s_mov_b32 s92, 0xffff0000\n" \
  "s_mov_b32 s60, 8192\n" \
  "s_add_u32 s60, s60, 32768\n" \
  "v_add_u32 v176, s60, v240\n" \
  "v_mov_b32 v180, v252\n" \
  "v_mov_b64 v[96:97], 0\n" \
  "v_mov_b64 v[98:99], 0\n" \
  "v_mov_b32 v96, v253\n" \
  "v_mov_b32 v98, v254\n" \
  "s_and_b32 s60, s72, 2048\n" \
  "s_add_u32 s50, s50, s60\n" \
  "s_lshr_b32 s60, s60, 1\n" \
  "v_add_u32 v177, s60, v244\n" \
  "s_lshr_b32 s60, s72, 9\n" \
  "s_add_u32 s60, s60, 17408\n" \
  "v_mov_b32 v184, s60\n" \
  "s_mov_b32 s52, 0\n" \
  "s_barrier\n" \
  "ds_write_b32 v184, v72\n" \
  "s_branch .LM_xr_%=\n" \
  ".LM_dry_b_%=:\n" \
  "s_and_b32 s60, s72, 2048\n" \
  "s_lshl_b32 s91, s60, 1\n" \
  "s_add_u32 s91, s91, 40960\n" \
  "v_mov_b64 v[182:183], 0\n" \
  "s_mov_b32 s69, 0\n" \
  ".LM_k0_%=:\n" \
  "buffer_load_dwordx2 v[196:197], off, s[40:43], s74 sc1\n" \
  "s_waitcnt vmcnt(0)\n" \
  "v_readfirstlane_b32 s54, v196\n" \
  "v_readfirstlane_b32 s63, v197\n" \
  "s_cmp_eq_u32 s63, s55\n" \
  "s_cselect_b32 s54, s54, 0\n" \
  "s_cmp_gt_i32 s54, 1\n" \
  "s_cbranch_scc1 .LM_k0_x_%=\n" \
  "s_sleep 2\n" \
  "s_add_u32 s69, s69, 1\n" \
  "s_and_b32 s63, s69, 1023\n" \
  "s_cbranch_scc1 .LM_k0_%=\n" \
  "s_cmp_gt_u32 s69, 2097152\n" \
  "s_cbranch_scc1 .LM_k0_set_%=\n" \
  "buffer_load_dwordx2 v[196:197], off, s[40:43], %38 offset:128 sc1\n" \
  "s_waitcnt vmcnt(0)\n" \
  "v_readfirstlane_b32 s63, v197\n" \
  "s_cmp_eq_u32 s63, s55\n" \
  "s_cbranch_scc0 .LM_k0_%=\n" \
  "s_branch .LM_k0_x_%=\n" \
  ".LM_k0_set_%=:\n" \
  "v_mov_b32 v196, 1\n" \
  "v_mov_b32 v197, s55\n" \
  "buffer_store_dwordx2 v[196:197], off, s[40:43], %38 offset:128 sc1\n" \
  "s_waitcnt vmcnt(0)\n" \
  ".LM_k0_x_%=:\n" \
  "s_xor_b32 s60, s49, s58\n" \
  "s_add_u32 s49, s49, 16384\n" \
  "s_xor_b32 s91, s91, 8192\n" \
  "s_mov_b32 m0, s91\n" \
  "s_nop 0\n" \
  "buffer_load_dwordx4 v245, s[40:43], s60 offen sc1 lds\n" \
  "buffer_load_dwordx4 v245, s[40:43], s60 offen offset:1024 sc1 lds\n" \
  "buffer_load_dwordx4 v245, s[40:43], s60 offen offset:2048 sc1 lds\n" \
  "buffer_load_dwordx4 v245, s[40:43], s60 offen offset:3072 sc1 lds\n" \
  "s_xor_b32 s60, s49, s58\n" \
  "s_add_u32 s49, s49, 16384\n" \
  "s_xor_b32 s91, s91, 8192\n" \
  "s_mov_b32 m0, s91\n" \
  "s_nop 0\n" \
  "buffer_load_dwordx4 v245, s[40:43], s60 offen sc1 lds\n" \
  "buffer_load_dwordx4 v245, s[40:43], s60 offen offset:1024 sc1 lds\n" \
  "buffer_load_dwordx4 v245, s[40:43], s60 offen offset:2048 sc1 lds\n" \
  "buffer_load_dwordx4 v245, s[40:43], s60 offen offset:3072 sc1 lds\n" \
  "s_waitcnt vmcnt(0)\n" \
  "s_barrier\n" \
  "v_xor_b32 v176, 8192, v176\n" \
  "s_mov_b32 s67, 2130706432\n" \
  "s_waitcnt vmcnt(0) lgkmcnt(0)\n" \
  "s_barrier\n" \
  "s_branch .LM_p_%=\n" \
  "s_nop 7\n" \
  ".p2align 6\n" \
  ".fill (((24 - (.LM_p_%= - .LM_rot_%=)) & 63) >> 2), 4, 0xBF800000\n" \
  ".LM_rot_%=:\n" \
  "s_xor_b32 s51, s51, s56\n" \
  "s_xor_b32 s71, s71, s56\n" \
  "v_xor_b32 v246, s56, v246\n" \
  "v_xor_b32 v247, s56, v247\n" \
  "s_mov_b32 s60, s56\n" \
  "s_mov_b32 s56, s57\n" \
  "s_mov_b32 s57, s60\n" \
  "s_add_u32 s59, s51, 819200\n" \
  "s_mov_b32 m0, 0\n" \
  "s_mov_b32 s70, 0\n" \
  "s_waitcnt lgkmcnt(0)\n" \
  ".LM_rt_%=:\n" \
  "s_barrier\n" \
  ".LM_p_%=:\n" \
  "ds_read_b128 v[116:119], v240\n" \
  "ds_read_b128 v[120:123], v240 offset:1024\n" \
  "ds_read_b128 v[124:127], v240 offset:2048\n" \
  "s_waitcnt lgkmcnt(2)\n" \
  "v_smfmac_f32_16x16x64_bf16 v[80:83], v[116:119], a[72:79], v239\n" \
  "s_barrier\n" \
  "v_smfmac_f32_16x16x64_bf16 v[76:79], v[116:119], a[0:7], v239\n" \
  "ds_read_b128 v[128:131], v240 offset:3072\n" \
  "s_waitcnt lgkmcnt(2)\n" \
  "v_smfmac_f32_16x16x64_bf16 v[80:83], v[120:123], a[80:87], v239\n" \
  "ds_read_b128 v[132:135], v240 offset:4096\n" \
  "v_smfmac_f32_16x16x64_bf16 v[76:79], v[120:123], a[8:15], v239\n" \
  "ds_read_b128 v[136:139], v240 offset:5120\n" \
  "s_waitcnt lgkmcnt(3)\n" \
  "v_smfmac_f32_16x16x64_bf16 v[76:79], v[124:127], a[16:23], v239\n" \
  "ds_read_b128 v[140:143], v240 offset:6144\n" \
  "ds_read_b128 v[144:147], v240 offset:7168\n" \
  "v_smfmac_f32_16x16x64_bf16 v[80:83], v[124:127], a[88:95], v239\n" \
  "s_waitcnt lgkmcnt(4)\n" \
  "v_smfmac_f32_16x16x64_bf16 v[76:79], v[128:131], a[24:31], v239\n" \
  "ds_read_b128 v[148:151], v240 offset:8192\n" \
  "s_waitcnt lgkmcnt(4)\n" \
  "v_smfmac_f32_16x16x64_bf16 v[76:79], v[132:135], a[32:39], v239\n" \
  "s_waitcnt lgkmcnt(3)\n" \
  "v_smfmac_f32_16x16x64_bf16 v[76:79], v[136:139], a[40:47], v239\n" \
  "s_waitcnt lgkmcnt(2)\n" \
  "v_smfmac_f32_16x16x64_bf16 v[76:79], v[140:143], a[48:55], v239\n" \
  "s_waitcnt lgkmcnt(1)\n" \
  "v_smfmac_f32_16x16x64_bf16 v[76:79], v[144:147], a[56:63], v239\n" \
  "s_waitcnt lgkmcnt(0)\n" \
  "v_smfmac_f32_16x16x64_bf16 v[76:79], v[148:151], a[64:71], v239\n" \
  "s_nop 2\n" \
  "v_smfmac_f32_16x16x64_bf16 v[80:83], v[128:131], a[96:103], v239\n" \
  "s_nop 2\n" \
  "v_smfmac_f32_16x16x64_bf16 v[80:83], v[132:135], a[104:111], v239\n" \
  "v_add_f32 v76, v76, v78\n" \
  "v_add_f32 v77, v77, v79\n" \
  "v_smfmac_f32_16x16x64_bf16 v[80:83], v[136:139], a[112:119], v239\n" \
  "v_cmp_u_f32 vcc, v76, v77\n" \
  "v_smfmac_f32_16x16x64_bf16 v[80:83], v[140:143], a[120:127], v239\n" \
  "v_mul_f32 v77, s64, v77\n" \
  "v_smfmac_f32_16x16x64_bf16 v[80:83], v[144:147], a[128:135], v239\n" \
  "v_mul_f32 v76, s64, v76\n" \
  "v_smfmac_f32_16x16x64_bf16 v[80:83], v[148:151], a[136:143], v239\n" \
  "s_cbranch_vccnz .LM_slow_%=\n" \
  ".LM_back_%=:\n" \
  "s_cmp_eq_u32 s48, 63\n" \
  "v_cvt_pk_bf16_f32 v76, v76, v77\n" \
  "v_smfmac_f32_16x16x64_bf16 v[92:95], v[116:119], v[32:39], v239\n" \
  "ds_write_b32 v241, v76\n" \
  "s_nop 2\n" \
  "v_add_f32 v80, v80, v82\n" \
  "v_mul_f32 v80, s64, v80\n" \
  "v_add_f32 v81, v81, v83\n" \
  "v_mul_f32 v81, s64, v81\n" \
  "v_cvt_pk_bf16_f32 v80, v80, v81\n" \
  "v_smfmac_f32_16x16x64_bf16 v[92:95], v[120:123], v[40:47], v239\n" \
  "ds_write_b32 v241, v80 offset:256\n" \
  "v_smfmac_f32_16x16x64_bf16 v[92:95], v[124:127], v[48:55], v239\n" \
  "v_smfmac_f32_16x16x64_bf16 v[92:95], v[128:131], v[56:63], v239\n" \
  "s_waitcnt lgkmcnt(1)\n" \
  "s_barrier\n" \
  "v_smfmac_f32_16x16x64_bf16 v[84:87], v[116:119], a[144:151], v239\n" \
  "s_nop 0\n" \
  "ds_read_b64_tr_b16 v[152:153], v243 offset:1024\n" \
  "v_smfmac_f32_16x16x64_bf16 v[88:91], v[116:119], a[216:223], v239\n" \
  "s_nop 1\n" \
  "v_add_f32 v93, v93, v95\n" \
  "s_waitcnt lgkmcnt(1)\n" \
  "s_barrier\n" \
  "v_add_f32 v92, v92, v94\n" \
  "v_smfmac_f32_16x16x64_bf16 v[84:87], v[120:123], a[152:159], v239\n" \
  "ds_read_b64_tr_b16 v[116:117], v243\n" \
  "v_smfmac_f32_16x16x64_bf16 v[84:87], v[124:127], a[160:167], v239\n" \
  "ds_write_b64 v255, v[92:93]\n" \
  "v_smfmac_f32_16x16x64_bf16 v[84:87], v[128:131], a[168:175], v239\n" \
  "v_smfmac_f32_16x16x64_bf16 v[84:87], v[132:135], a[176:183], v239\n" \
  "s_waitcnt vmcnt(0) \n" \
  "buffer_store_dwordx2 v[72:73], v74, s[40:43], %35 offen sc1\n" \
  "v_smfmac_f32_16x16x64_bf16 v[84:87], v[136:139], a[184:191], v239\n" \
  "v_smfmac_f32_16x16x64_bf16 v[84:87], v[140:143], a[192:199], v239\n" \
  "s_waitcnt lgkmcnt(0)\n" \
  "s_barrier\n" \
  "v_smfmac_f32_16x16x64_bf16 v[84:87], v[144:147], a[200:207], v239\n" \
  "v_smfmac_f32_16x16x64_bf16 v[84:87], v[148:151], a[208:215], v239\n" \
  "ds_read_b64_tr_b16 v[154:155], v243 offset:1536\n" \
  "ds_read_b64_tr_b16 v[158:159], v243 offset:1792\n" \
  "v_smfmac_f32_16x16x64_bf16 v[88:91], v[120:123], a[224:231], v239\n" \
  "s_nop 2\n" \
  "v_smfmac_f32_16x16x64_bf16 v[88:91], v[124:127], a[232:239], v239\n" \
  "s_nop 0\n" \
  "v_add_f32 v84, v84, v86\n" \
  "v_smfmac_f32_16x16x64_bf16 v[88:91], v[128:131], a[240:247], v239\n" \
  "ds_read_b64_tr_b16 v[120:121], v243 offset:256\n" \
  "v_add_f32 v85, v85, v87\n" \
  "v_smfmac_f32_16x16x64_bf16 v[88:91], v[132:135], a[248:255], v239\n" \
  "ds_read_b64_tr_b16 v[122:123], v243 offset:768\n" \
  "v_cvt_pk_bf16_f32 v84, v84, v85\n" \
  "v_smfmac_f32_16x16x64_bf16 v[88:91], v[136:139], v[0:7], v239\n" \
  "v_smfmac_f32_16x16x64_bf16 v[88:91], v[140:143], v[8:15], v239\n" \
  "ds_write_b32 v242, v84\n" \
  "v_smfmac_f32_16x16x64_bf16 v[88:91], v[144:147], v[16:23], v239\n" \
  "v_smfmac_f32_16x16x64_bf16 v[88:91], v[148:151], v[24:31], v239\n" \
  "ds_read_b64_tr_b16 v[118:119], v243 offset:512\n" \
  "s_nop 6\n" \
  "v_add_f32 v88, v88, v90\n" \
  "v_add_f32 v89, v89, v91\n" \
  "v_cvt_pk_bf16_f32 v88, v88, v89\n" \
  "ds_write_b32 v242, v88 offset:256\n" \
  "s_waitcnt lgkmcnt(2)\n" \
  "s_barrier\n" \
  "ds_read_b64_tr_b16 v[124:125], v244 offset:1024\n" \
  "ds_read_b64_tr_b16 v[126:127], v244 offset:1536\n" \
  "ds_read_b64_tr_b16 v[130:131], v244 offset:512\n" \
  "ds_read_b64_tr_b16 v[128:129], v244\n" \
  "s_waitcnt lgkmcnt(4)\n" \
  "s_barrier\n" \
  "s_cbranch_scc1 .LM_xr_%=\n" \
  "ds_read_b64_tr_b16 v[134:135], v244 offset:768\n" \
  "ds_read_b64_tr_b16 v[132:133], v244 offset:256\n" \
  "ds_read_b64_tr_b16 v[156:157], v243 offset:1280\n" \
  "ds_read_b64_tr_b16 v[136:137], v244 offset:1280\n" \
  "ds_read_b64 v[188:189], v248\n" \
  "ds_read_b64_tr_b16 v[138:139], v244 offset:1792\n" \
  "s_waitcnt lgkmcnt(6)\n" \
  "v_mfma_f32_16x16x32_bf16 v[140:143], v[128:131], v[116:119], 0\n" \
  "v_mfma_f32_16x16x32_bf16 v[140:143], v[124:127], v[152:155], v[140:143]\n" \
  "s_waitcnt lgkmcnt(4)\n" \
  "v_mfma_f32_16x16x32_bf16 v[140:143], v[132:135], v[120:123], v[140:143]\n" \
  "s_waitcnt lgkmcnt(0)\n" \
  "v_mfma_f32_16x16x32_bf16 v[140:143], v[136:139], v[156:159], v[140:143]\n" \
  "s_nop 7\n" \
  "v_exp_f32 v119, v143\n" \
  "v_exp_f32 v117, v141\n" \
  "v_exp_f32 v118, v142\n" \
  "v_exp_f32 v116, v140\n" \
  "v_add_f32 v188, v188, v189\n" \
  "v_pk_add_f32 v[186:187], v[116:117], v[118:119]\n" \
  "s_nop 0\n" \
  "v_pk_add_f32 v[174:175], v[186:187], v[186:187] op_sel:[0,1] op_sel_hi:[1,0]\n" \
  "v_pk_fma_f32 v[116:117], v[116:117], v[180:181], v[96:97] op_sel_hi:[1,0,1]\n" \
  "v_pk_fma_f32 v[118:119], v[118:119], v[180:181], v[98:99] op_sel_hi:[1,0,1]\n" \
  "v_permlane32_swap_b32 v174, v175\n" \
  "v_add_f32 v174, v174, v175\n" \
  "v_add_f32 v174, v174, v188\n" \
  "v_rcp_f32 v188, v174\n" \
  "v_add_u32 v187, 3766484992, v174\n" \
  "v_pk_mul_f32 v[116:117], v[116:117], v[188:189] op_sel_hi:[1,0]\n" \
  "v_pk_mul_f32 v[118:119], v[118:119], v[188:189] op_sel_hi:[1,0]\n" \
  "v_cvt_pk_bf16_f32 v188, v116, v117\n" \
  "v_cvt_pk_bf16_f32 v189, v118, v119\n" \
  "v_cmp_le_u32 vcc, s52, v187\n" \
  "s_cbranch_vccnz .LM_ex_%=\n" \
  ".LM_ex_b_%=:\n" \
  "buffer_store_dwordx2 v[188:189], v246, s[40:43], 0 offen sc1\n" \
  "s_memrealtime s[98:99]\n" \
  "s_memtime s[84:85]\n" \
  ".LM_loop_%=:\n" \
  "s_cmp_ge_u32 s48, 55\n" \
  "s_cbranch_scc1 .LM_xl_%=\n" \
  ".LM_xr_b_%=:\n" \
  "ds_read_b64_tr_b16 v[116:117], v177\n" \
  "ds_read_b64_tr_b16 v[118:119], v177 offset:256\n" \
  "ds_read_b64_tr_b16 v[120:121], v177 offset:512\n" \
  "ds_read_b64_tr_b16 v[122:123], v177 offset:768\n" \
  "ds_read_b32 v188, v184\n" \
  "v_readfirstlane_b32 s63, v183\n" \
  "v_readfirstlane_b32 s62, v182\n" \
  "s_cmp_eq_u32 s63, s55\n" \
  "s_cselect_b32 s62, s62, 0\n" \
  "s_max_i32 s54, s54, s62\n" \
  "s_add_u32 s48, s48, 1\n" \
  "s_add_u32 s62, s48, 1\n" \
  "s_min_u32 s62, s62, 63\n" \
  "s_cmp_le_i32 s54, s62\n" \
  "s_cbranch_scc1 .LM_pk_go_%=\n" \
  ".LM_pk_ok_%=:\n" \
  "s_xor_b32 s60, s49, s58\n" \
  "s_add_u32 s49, s49, 16384\n" \
  "s_xor_b32 s91, s91, 8192\n" \
  "s_mov_b32 m0, s91\n" \
  "s_nop 0\n" \
  "buffer_load_dwordx4 v245, s[40:43], s60 offen sc1 lds\n" \
  "buffer_load_dwordx4 v245, s[40:43], s60 offen offset:1024 sc1 lds\n" \
  "buffer_load_dwordx4 v245, s[40:43], s60 offen offset:2048 sc1 lds\n" \
  "buffer_load_dwordx4 v245, s[40:43], s60 offen offset:3072 sc1 lds\n" \
  "s_add_u32 s62, s48, 2\n" \
  "s_cmp_le_i32 s54, s62\n" \
  "s_cselect_b32 s62, s74, 2130706432\n" \
  "buffer_load_dwordx2 v[182:183], off, s[40:43], s62 sc1\n" \
  "v_xor_b32 v176, 8192, v176\n" \
  "v_mov_b64 v[76:77], 0\n" \
  "v_mov_b64 v[78:79], 0\n" \
  "v_mov_b64 v[80:81], 0\n" \
  "v_mov_b64 v[82:83], 0\n" \
  "v_mov_b64 v[84:85], 0\n" \
  "v_mov_b64 v[86:87], 0\n" \
  "v_mov_b64 v[88:89], 0\n" \
  "v_mov_b64 v[90:91], 0\n" \
  "v_mov_b64 v[92:93], 0\n" \
  "v_mov_b64 v[94:95], 0\n" \
  "v_mov_b32 v72, s48\n" \
  "s_sub_u32 s60, s48, 2\n" \
  "s_cselect_b32 s60, 8192, s60\n" \
  "s_lshl_b32 s60, s60, 14\n" \
  "s_xor_b32 s60, s60, s50\n" \
  "s_cmp_le_u32 s48, 58\n" \
  "s_cselect_b32 s60, s60, 2130706432\n" \
  "s_waitcnt lgkmcnt(3)\n" \
  "buffer_store_dwordx4 v[116:119], v240, s[40:43], s60 offen sc1\n" \
  "s_waitcnt lgkmcnt(1)\n" \
  "buffer_store_dwordx4 v[120:123], v240, s[40:43], s60 offen offset:1024 sc1\n" \
  "s_waitcnt lgkmcnt(0)\n" \
  "s_waitcnt lgkmcnt(0)\n" \
  "v_readfirstlane_b32 s60, v188\n" \
  "v_mov_b32 v189, s55\n" \
  "s_lshl_b32 s66, s66, 10\n" \
  "s_and_b32 s61, s60, 1023\n" \
  "s_cselect_b32 s62, 4096000, 0\n" \
  "s_andn2_b32 s60, s60, 1023\n" \
  "s_sub_u32 s60, s66, s60\n" \
  "s_mov_b32 s66, s84\n" \
  "s_or_b32 s60, s60, s61\n" \
  "s_cmp_lt_u32 s60, s62\n" \
  "s_cselect_b32 s60, s60, -1\n" \
  "s_min_u32 s46, s46, s60\n" \
  "v_mov_b32 v188, s46\n" \
  "s_cmp_lg_u32 s46, -1\n" \
  "s_cselect_b32 s61, s75, 2130706432\n" \
  "s_and_b32 s60, s48, 3\n" \
  "s_cmp_eq_u32 s60, 1\n" \
  "s_cselect_b32 s61, s61, 2130706432\n" \
  "s_cselect_b32 s46, -1, s46\n" \
  "buffer_store_dwordx2 v[188:189], v74, s[40:43], s61 offen sc1\n" \
  "s_cmp_eq_u32 s48, 58\n" \
  "s_cbranch_scc1 .LM_ef_%=\n" \
  ".LM_ef_b_%=:\n" \
  "s_branch .LM_rot_%=\n" \
  "s_nop 7\n" \
  ".LM_slow_%=:\n" \
  "s_bitcmp1_b32 s70, 0\n" \
  "s_cbranch_scc1 .LM_back_%=\n" \
  "s_waitcnt lgkmcnt(0)\n" \
  "s_cmp_lt_u32 s70, 10\n" \
  "s_nop 3\n" \
  "s_cbranch_scc1 .LM_nol_%=\n" \
  "v_max_u32 v189, v116, v117\n" \
  "v_max3_u32 v189, v189, v118, v119\n" \
  "v_max3_u32 v189, v189, v120, v121\n" \
  "v_max3_u32 v189, v189, v122, v123\n" \
  "v_max3_u32 v189, v189, v124, v125\n" \
  "v_max3_u32 v189, v189, v126, v127\n" \
  "v_max3_u32 v189, v189, v128, v129\n" \
  "v_max3_u32 v189, v189, v130, v131\n" \
  "v_max3_u32 v189, v189, v132, v133\n" \
  "v_max3_u32 v189, v189, v134, v135\n" \
  "v_max3_u32 v189, v189, v136, v137\n" \
  "v_max3_u32 v189, v189, v138, v139\n" \
  "v_max3_u32 v189, v189, v140, v141\n" \
  "v_max3_u32 v189, v189, v142, v143\n" \
  "v_max3_u32 v189, v189, v144, v145\n" \
  "v_max3_u32 v189, v189, v146, v147\n" \
  "v_max3_u32 v189, v189, v148, v149\n" \
  "v_max3_u32 v189, v189, v150, v151\n" \
  "v_cmp_eq_u32 vcc, -1, v189\n" \
  "s_cbranch_vccz .LM_back_%=\n" \
  ".LM_nol_%=:\n" \
  "s_add_u32 s70, s70, 2\n" \
  "s_cmp_gt_u32 s70, 65536\n" \
  "s_cbranch_scc1 .LM_dset_%=\n" \
  "s_barrier\n" \
  "v_mov_b64 v[76:77], 0\n" \
  "v_mov_b64 v[78:79], 0\n" \
  "v_mov_b64 v[80:81], 0\n" \
  "v_mov_b64 v[82:83], 0\n" \
  "v_mov_b64 v[84:85], 0\n" \
  "v_mov_b64 v[86:87], 0\n" \
  "v_mov_b64 v[88:89], 0\n" \
  "v_mov_b64 v[90:91], 0\n" \
  "v_mov_b64 v[92:93], 0\n" \
  "v_mov_b64 v[94:95], 0\n" \
  "s_branch .LM_rt_%=\n" \
  ".LM_dset_%=:\n" \
  "v_mov_b32 v136, 1\n" \
  "v_mov_b32 v137, s55\n" \
  "buffer_store_dwordx2 v[136:137], off, s[40:43], %38 offset:128 sc1\n" \
  "s_mov_b32 s70, 1\n" \
  "v_mov_b64 v[76:77], 0\n" \
  "v_mov_b64 v[78:79], 0\n" \
  "v_mov_b64 v[80:81], 0\n" \
  "v_mov_b64 v[82:83], 0\n" \
  "v_mov_b64 v[84:85], 0\n" \
  "v_mov_b64 v[86:87], 0\n" \
  "v_mov_b64 v[88:89], 0\n" \
  "v_mov_b64 v[90:91], 0\n" \
  "v_mov_b64 v[92:93], 0\n" \
  "v_mov_b64 v[94:95], 0\n" \
  "s_waitcnt vmcnt(0) lgkmcnt(0)\n" \
  "s_barrier\n" \
  "s_branch .LM_p_%=\n" \
  ".LM_xr_%=:\n" \
  "s_waitcnt lgkmcnt(0)\n" \
  "s_nop 7\n" \
  "ds_read_b64_tr_b16 v[132:133], v244\n" \
  "ds_read_b64_tr_b16 v[134:135], v244 offset:256\n" \
  "ds_read_b64_tr_b16 v[136:137], v244 offset:512\n" \
  "ds_read_b64_tr_b16 v[138:139], v244 offset:768\n" \
  "ds_read_b64_tr_b16 v[140:141], v244 offset:1024\n" \
  "ds_read_b64_tr_b16 v[142:143], v244 offset:1280\n" \
  "ds_read_b64_tr_b16 v[144:145], v244 offset:1536\n" \
  "ds_read_b64_tr_b16 v[146:147], v244 offset:1792\n" \
  "s_waitcnt lgkmcnt(6)\n" \
  "v_mfma_f32_16x16x32_bf16 v[148:151], v[132:135], v[100:103], 0\n" \
  "s_waitcnt lgkmcnt(4)\n" \
  "v_mfma_f32_16x16x32_bf16 v[148:151], v[136:139], v[104:107], v[148:151]\n" \
  "s_waitcnt lgkmcnt(2)\n" \
  "v_mfma_f32_16x16x32_bf16 v[148:151], v[140:143], v[108:111], v[148:151]\n" \
  "s_waitcnt lgkmcnt(0)\n" \
  "v_mfma_f32_16x16x32_bf16 v[148:151], v[144:147], v[112:115], v[148:151]\n" \
  "s_sub_u32 s60, s48, 58\n" \
  "s_lshl_b32 s60, s60, 16\n" \
  "s_add_u32 s60, s60, s97\n" \
  "s_cmp_eq_u32 s48, 0\n" \
  "s_cselect_b32 s60, 2130706432, s60\n" \
  "s_nop 2\n" \
  "v_max_f32 v158, v148, v149\n" \
  "v_max3_f32 v158, v150, v151, v158\n" \
  "v_mov_b32 v159, v158\n" \
  "s_nop 1\n" \
  "v_permlane32_swap_b32 v158, v159\n" \
  "v_max_f32 v158, v158, v159\n" \
  "v_mul_f32 v152, s65, v158\n" \
  "v_min_f32 v152, v152, v178\n" \
  "v_fma_f32 v148, v148, s64, v152\n" \
  "v_fma_f32 v149, v149, s64, v152\n" \
  "v_fma_f32 v150, v150, s64, v152\n" \
  "v_fma_f32 v151, v151, s64, v152\n" \
  "v_sub_f32 v160, v152, v178\n" \
  "v_exp_f32 v148, v148\n" \
  "v_exp_f32 v149, v149\n" \
  "v_exp_f32 v150, v150\n" \
  "v_exp_f32 v151, v151\n" \
  "v_exp_f32 v160, v160\n" \
  "v_add_f32 v153, v148, v149\n" \
  "v_add_f32 v158, v150, v151\n" \
  "v_add_f32 v153, v153, v158\n" \
  "v_mov_b32 v159, v153\n" \
  "s_nop 1\n" \
  "v_permlane32_swap_b32 v153, v159\n" \
  "v_add_f32 v153, v153, v159\n" \
  "v_fmac_f32 v153, v160, v179\n" \
  "v_rcp_f32 v154, v153\n" \
  "s_nop 0\n" \
  "v_mul_f32 v160, v160, v154\n" \
  "v_mul_f32 v154, v154, v252\n" \
  "v_mul_f32 v148, v148, v154\n" \
  "v_mul_f32 v149, v149, v154\n" \
  "v_mul_f32 v150, v150, v154\n" \
  "v_mul_f32 v151, v151, v154\n" \
  "v_fmac_f32 v148, v160, v253\n" \
  "v_fmac_f32 v150, v160, v254\n" \
  "v_cvt_pk_bf16_f32 v156, v148, v149\n" \
  "v_cvt_pk_bf16_f32 v157, v150, v151\n" \
  "buffer_store_dwordx2 v[156:157], v251, s[40:43], s60 offen sc1\n" \
  "s_cmp_eq_u32 s48, 63\n" \
  "s_cbranch_scc1 .LM_end_%=\n" \
  ".LM_xq_%=:\n" \
  "s_add_u32 s61, s48, 1\n" \
  "s_lshl_b32 s60, s61, 19\n" \
  "s_add_u32 s60, s60, s47\n" \
  "s_lshr_b32 s62, s60, 8\n" \
  "s_and_b32 s62, s62, 8192\n" \
  "s_xor_b32 s62, s62, s60\n" \
  "s_cmp_eq_u32 s48, 0\n" \
  "s_cselect_b32 s62, 2130706432, s62\n" \
  "s_add_u32 s63, s62, 4096\n" \
  "buffer_load_dwordx4 v[100:103], v249, s[40:43], s62 offen sc1\n" \
  "buffer_load_dwordx4 v[104:107], v249, s[40:43], s62 offen offset:2048 sc1\n" \
  "buffer_load_dwordx4 v[108:111], v249, s[40:43], s63 offen sc1\n" \
  "buffer_load_dwordx4 v[112:115], v249, s[40:43], s63 offen offset:2048 sc1\n" \
  "s_sub_u32 s60, s61, 58\n" \
  "s_lshl_b32 s60, s60, 16\n" \
  "s_add_u32 s60, s60, s97\n" \
  "s_cmp_eq_u32 s48, 0\n" \
  "s_cselect_b32 s60, 2130706432, s60\n" \
  "buffer_load_dwordx2 v[178:179], v250, s[40:43], s60 offen sc1\n" \
  "s_cbranch_scc1 .LM_dry_b_%=\n" \
  "s_branch .LM_xr_b_%=\n" \
  ".LM_xl_%=:\n" \
  "s_cmp_eq_u32 s48, 57\n" \
  "s_cbranch_scc1 .LM_xq_%=\n" \
  "s_cmp_eq_u32 s48, 56\n" \
  "s_cbranch_scc1 .LM_xc_%=\n" \
  "s_cmp_lg_u32 s48, 55\n" \
  "s_cbranch_scc1 .LM_xr_%=\n" \
  "buffer_load_dwordx2 v[178:179], off, s[40:43], %42 sc1\n" \
  "s_branch .LM_xr_b_%=\n" \
  ".LM_xc_%=:\n" \
  "v_readfirstlane_b32 s63, v179\n" \
  "s_cmp_eq_u32 s63, s55\n" \
  "s_cbranch_scc1 .LM_xr_b_%=\n" \
  "s_mov_b32 s69, 0\n" \
  ".LM_xs_%=:\n" \
  "buffer_load_dwordx2 v[178:179], off, s[40:43], %42 sc1\n" \
  "s_waitcnt vmcnt(0)\n" \
  "v_readfirstlane_b32 s62, v178\n" \
  "v_readfirstlane_b32 s63, v179\n" \
  "s_cmp_eq_u32 s63, s55\n" \
  "s_cselect_b32 s62, s62, 0\n" \
  "s_cmp_lg_u32 s62, 0\n" \
  "s_cbranch_scc1 .LM_xs_x_%=\n" \
  "s_sleep 2\n" \
  "s_add_u32 s69, s69, 1\n" \
  "s_and_b32 s63, s69, 1023\n" \
  "s_cbranch_scc1 .LM_xs_%=\n" \
  "s_cmp_gt_u32 s69, 2097152\n" \
  "s_cbranch_scc1 .LM_xs_set_%=\n" \
  "buffer_load_dwordx2 v[178:179], off, s[40:43], %38 offset:128 sc1\n" \
  "s_waitcnt vmcnt(0)\n" \
  "v_readfirstlane_b32 s63, v179\n" \
  "s_cmp_eq_u32 s63, s55\n" \
  "s_cbranch_scc0 .LM_xs_%=\n" \
  "s_branch .LM_xs_x_%=\n" \
  ".LM_xs_set_%=:\n" \
  "v_mov_b32 v178, 1\n" \
  "v_mov_b32 v179, s55\n" \
  "buffer_store_dwordx2 v[178:179], off, s[40:43], %38 offset:128 sc1\n" \
  "s_waitcnt vmcnt(0)\n" \
  ".LM_xs_x_%=:\n" \
  "s_branch .LM_xr_b_%=\n" \
  ".LM_pk_go_%=:\n" \
  "s_mov_b32 s94, s62\n" \
  "s_mov_b32 s69, 0\n" \
  ".LM_pk_%=:\n" \
  "buffer_load_dwordx2 v[186:187], off, s[40:43], s74 sc1\n" \
  "s_waitcnt vmcnt(0)\n" \
  "v_readfirstlane_b32 s54, v186\n" \
  "v_readfirstlane_b32 s63, v187\n" \
  "s_cmp_eq_u32 s63, s55\n" \
  "s_cselect_b32 s54, s54, 0\n" \
  "s_cmp_gt_i32 s54, s94\n" \
  "s_cbranch_scc1 .LM_pk_x_%=\n" \
  "s_sleep 2\n" \
  "s_add_u32 s69, s69, 1\n" \
  "s_and_b32 s63, s69, 1023\n" \
  "s_cbranch_scc1 .LM_pk_%=\n" \
  "s_cmp_gt_u32 s69, 2097152\n" \
  "s_cbranch_scc1 .LM_pk_set_%=\n" \
  "buffer_load_dwordx2 v[186:187], off, s[40:43], %38 offset:128 sc1\n" \
  "s_waitcnt vmcnt(0)\n" \
  "v_readfirstlane_b32 s63, v187\n" \
  "s_cmp_eq_u32 s63, s55\n" \
  "s_cbranch_scc0 .LM_pk_%=\n" \
  "s_branch .LM_pk_x_%=\n" \
  ".LM_pk_set_%=:\n" \
  "v_mov_b32 v186, 1\n" \
  "v_mov_b32 v187, s55\n" \
  "buffer_store_dwordx2 v[186:187], off, s[40:43], %38 offset:128 sc1\n" \
  "s_waitcnt vmcnt(0)\n" \
  ".LM_pk_x_%=:\n" \
  "s_branch .LM_pk_ok_%=\n" \
  ".LM_ef_%=:\n" \
  "s_waitcnt vmcnt(0) lgkmcnt(0)\n" \
  "buffer_store_dwordx2 v[72:73], v74, s[40:43], %35 offen sc1\n" \
  "s_branch .LM_ef_b_%=\n" \
  ".LM_ex_%=:\n" \
  "s_waitcnt vmcnt(0) lgkmcnt(0)\n" \
  "s_nop 7\n" \
  "ds_read_b64_tr_b16 v[120:121], v243\n" \
  "ds_read_b64_tr_b16 v[122:123], v243 offset:256\n" \
  "ds_read_b64_tr_b16 v[124:125], v243 offset:512\n" \
  "ds_read_b64_tr_b16 v[126:127], v243 offset:768\n" \
  "ds_read_b64_tr_b16 v[128:129], v243 offset:1024\n" \
  "ds_read_b64_tr_b16 v[130:131], v243 offset:1280\n" \
  "ds_read_b64_tr_b16 v[132:133], v243 offset:1536\n" \
  "ds_read_b64_tr_b16 v[134:135], v243 offset:1792\n" \
  "ds_read_b128 v[136:139], v176\n" \
  "ds_read_b128 v[144:147], v176 offset:2048\n" \
  "ds_read_b128 v[148:151], v176 offset:4096\n" \
  "ds_read_b128 v[152:155], v176 offset:6144\n" \
  "s_waitcnt lgkmcnt(3)\n" \
  "v_mfma_f32_16x16x32_bf16 v[156:159], v[136:139], v[120:123], 0\n" \
  "s_waitcnt lgkmcnt(2)\n" \
  "v_mfma_f32_16x16x32_bf16 v[156:159], v[144:147], v[124:127], v[156:159]\n" \
  "s_waitcnt lgkmcnt(1)\n" \
  "v_mfma_f32_16x16x32_bf16 v[156:159], v[148:151], v[128:131], v[156:159]\n" \
  "s_waitcnt lgkmcnt(0)\n" \
  "v_mfma_f32_16x16x32_bf16 v[156:159], v[152:155], v[132:135], v[156:159]\n" \
  "s_nop 7\n" \
  "ds_read_b128 v[136:139], v176 offset:1024\n" \
  "ds_read_b128 v[144:147], v176 offset:3072\n" \
  "ds_read_b128 v[148:151], v176 offset:5120\n" \
  "ds_read_b128 v[152:155], v176 offset:7168\n" \
  "s_waitcnt lgkmcnt(3)\n" \
  "v_mfma_f32_16x16x32_bf16 v[160:163], v[136:139], v[120:123], 0\n" \
  "s_waitcnt lgkmcnt(2)\n" \
  "v_mfma_f32_16x16x32_bf16 v[160:163], v[144:147], v[124:127], v[160:163]\n" \
  "s_waitcnt lgkmcnt(1)\n" \
  "v_mfma_f32_16x16x32_bf16 v[160:163], v[148:151], v[128:131], v[160:163]\n" \
  "s_waitcnt lgkmcnt(0)\n" \
  "v_mfma_f32_16x16x32_bf16 v[160:163], v[152:155], v[132:135], v[160:163]\n" \
  "s_nop 7\n" \
  "v_max_f32 v164, v140, v141\n" \
  "v_max3_f32 v164, v164, v142, v143\n" \
  "v_max3_f32 v164, v164, v156, v157\n" \
  "v_max3_f32 v164, v164, v158, v159\n" \
  "s_nop 2\n" \
  "v_max3_f32 v164, v164, v160, v161\n" \
  "v_max3_f32 v164, v164, v162, v163\n" \
  "v_mov_b32 v165, v164\n" \
  "s_nop 1\n" \
  "v_permlane32_swap_b32 v164, v165\n" \
  "v_max_f32 v164, v164, v165\n" \
  "v_mov_b32 v165, v164\n" \
  "s_nop 1\n" \
  "v_permlane16_swap_b32 v164, v165\n" \
  "v_max_f32 v164, v164, v165\n" \
  "v_sub_f32 v116, v140, v164\n" \
  "v_sub_f32 v117, v141, v164\n" \
  "v_sub_f32 v118, v142, v164\n" \
  "v_sub_f32 v119, v143, v164\n" \
  "v_sub_f32 v156, v156, v164\n" \
  "v_sub_f32 v157, v157, v164\n" \
  "v_sub_f32 v158, v158, v164\n" \
  "v_sub_f32 v159, v159, v164\n" \
  "v_sub_f32 v160, v160, v164\n" \
  "v_sub_f32 v161, v161, v164\n" \
  "v_sub_f32 v162, v162, v164\n" \
  "v_sub_f32 v163, v163, v164\n" \
  "v_exp_f32 v116, v116\n" \
  "v_exp_f32 v117, v117\n" \
  "v_exp_f32 v118, v118\n" \
  "v_exp_f32 v119, v119\n" \
  "v_exp_f32 v156, v156\n" \
  "v_exp_f32 v157, v157\n" \
  "v_exp_f32 v158, v158\n" \
  "v_exp_f32 v159, v159\n" \
  "v_exp_f32 v160, v160\n" \
  "v_exp_f32 v161, v161\n" \
  "v_exp_f32 v162, v162\n" \
  "v_exp_f32 v163, v163\n" \
  "v_add_f32 v164, v116, v117\n" \
  "v_add_f32 v164, v164, v118\n" \
  "v_add_f32 v164, v164, v119\n" \
  "v_mul_f32 v164, 0.5, v164\n" \
  "v_add_f32 v164, v164, v156\n" \
  "v_add_f32 v164, v164, v157\n" \
  "v_add_f32 v164, v164, v158\n" \
  "v_add_f32 v164, v164, v159\n" \
  "v_add_f32 v164, v164, v160\n" \
  "v_add_f32 v164, v164, v161\n" \
  "v_add_f32 v164, v164, v162\n" \
  "v_add_f32 v164, v164, v163\n" \
  "v_mov_b32 v165, v164\n" \
  "s_nop 1\n" \
  "v_permlane32_swap_b32 v164, v165\n" \
  "v_add_f32 v164, v164, v165\n" \
  "v_mov_b32 v165, v164\n" \
  "s_nop 1\n" \
  "v_permlane16_swap_b32 v164, v165\n" \
  "v_add_f32 v164, v164, v165\n" \
  "v_rcp_f32 v188, v164\n" \
  "v_cvt_pk_bf16_f32 v156, v156, v157\n" \
  "v_cvt_pk_bf16_f32 v157, v158, v159\n" \
  "v_cvt_pk_bf16_f32 v160, v160, v161\n" \
  "v_cvt_pk_bf16_f32 v161, v162, v163\n" \
  "v_mov_b32 v165, 17408\n" \
  "s_cmp_eq_u32 s48, 0\n" \
  "s_cbranch_scc1 .LM_ex2_%=\n" \
  ".LM_ex1_%=:\n" \
  "ds_read_b64 v[166:167], v165\n" \
  "s_waitcnt lgkmcnt(0)\n" \
  "v_min_u32 v166, v166, v167\n" \
  "v_cmp_ge_u32 vcc, s48, v166\n" \
  "s_cbranch_vccnz .LM_ex1_%=\n" \
  ".LM_ex2_%=:\n" \
  "buffer_store_dwordx2 v[156:157], v247, s[40:43], 0 offen sc1\n" \
  "buffer_store_dwordx2 v[160:161], v247, s[40:43], 0 offen offset:256 sc1\n" \
  "v_pk_fma_f32 v[116:117], v[116:117], v[180:181], v[96:97] op_sel_hi:[1,0,1]\n" \
  "v_pk_fma_f32 v[118:119], v[118:119], v[180:181], v[98:99] op_sel_hi:[1,0,1]\n" \
  "v_pk_mul_f32 v[116:117], v[116:117], v[188:189] op_sel_hi:[1,0]\n" \
  "v_pk_mul_f32 v[118:119], v[118:119], v[188:189] op_sel_hi:[1,0]\n" \
  "v_cvt_pk_bf16_f32 v188, v116, v117\n" \
  "v_cvt_pk_bf16_f32 v189, v118, v119\n" \
  "s_waitcnt vmcnt(0)\n" \
  "s_mov_b32 s52, 2.0\n" \
  "s_cmp_eq_u32 s48, 0\n" \
  "s_cbranch_scc1 .LM_ex_b_%=\n" \
  "v_lshrrev_b32 v165, 1, v240\n" \
  "v_min_u32 v165, 280, v165\n" \
  "buffer_load_dwordx2 v[166:167], v165, s[40:43], %43 offen sc1\n" \
  "s_waitcnt vmcnt(0)\n" \
  "v_cmp_eq_u32 vcc, s55, v167\n" \
  "s_nop 1\n" \
  "v_cndmask_b32 v166, 0, v166, vcc\n" \
  "v_add_u32 v164, 2, v166\n" \
  "s_mov_b32 s69, 0\n" \
  ".LM_ex3_%=:\n" \
  "s_sleep 8\n" \
  "buffer_load_dwordx2 v[166:167], v165, s[40:43], %43 offen sc1\n" \
  "s_waitcnt vmcnt(0)\n" \
  "v_cmp_eq_u32 vcc, s55, v167\n" \
  "s_nop 1\n" \
  "v_cndmask_b32 v166, 0, v166, vcc\n" \
  "v_cmp_gt_u32 vcc, v164, v166\n" \
  "s_cbranch_vccz .LM_ex_b_%=\n" \
  "s_add_u32 s69, s69, 1\n" \
  "s_and_b32 s63, s69, 1023\n" \
  "s_cbranch_scc1 .LM_ex3_%=\n" \
  "s_cmp_gt_u32 s69, 2097152\n" \
  "s_cbranch_scc1 .LM_ex3_set_%=\n" \
  "buffer_load_dwordx2 v[166:167], off, s[40:43], %38 offset:128 sc1\n" \
  "s_waitcnt vmcnt(0)\n" \
  "v_readfirstlane_b32 s63, v167\n" \
  "s_cmp_eq_u32 s63, s55\n" \
  "s_cbranch_scc0 .LM_ex3_%=\n" \
  "s_branch .LM_ex_b_%=\n" \
  ".LM_ex3_set_%=:\n" \
  "v_mov_b32 v166, 1\n" \
  "v_mov_b32 v167, s55\n" \
  "buffer_store_dwordx2 v[166:167], off, s[40:43], %38 offset:128 sc1\n" \
  "s_waitcnt vmcnt(0)\n" \
  "s_branch .LM_ex_b_%=\n" \
  ".LM_end_%=:\n" \
  "s_waitcnt vmcnt(0)\n" \
  "s_waitcnt vmcnt(0)\n" \
  "s_nop 7\n" \
  "s_endpgm\n"
#define M_ASM_VOPS "+{v239}"(ci.sidx), "+{v240}"(ci.l16), "+{v241}"(ci.qw), "+{v242}"(ci.kw), "+{v243}"(ci.fq), "+{v244}"(ci.fk), "+{v245}"(ci.kxo), "+{v246}"(ci.pst), "+{v247}"(ci.ps2), "+{v248}"(ci.lx), "+{v249}"(ci.qxo), "+{v250}"(ci.sto), "+{v251}"(ci.plx), "+{v252}"(ci.mk), "+{v253}"(ci.ck0), "+{v254}"(ci.ck2), "+{v255}"(ci.hx), "+{v200}"(ci.vq0), "+{v201}"(ci.vq1), "+{v202}"(ci.vq2), "+{v203}"(ci.vq3), "+{v204}"(ci.tro0), "+{v205}"(ci.tro1), "+{v206}"(ci.m0o)
#define M_ASM_SOPS "s"(ci.ws), "s"(ci.Wa), "s"(ci.Wb), "s"(ci.mem0), "s"(ci.w), "s"(ci.o_a), "s"(ci.o_b), "s"(ci.o_nx), "s"(ci.o_exp), "s"(ci.lab), "s"(ci.f_p), "s"(ci.f_m), "s"(ci.f_dur), "s"(ci.f_go), "s"(ci.o_ctl), "s"(ci.o_dbg), "s"(ci.dret), "s"(ci.o_qx), "s"(ci.f_s), "s"(ci.f_r)
#define M_ASM_CLOB "v0", "v1", "v2", "v3", "v4", "v5", "v6", "v7", "v8", "v9", "v10", "v11", "v12", "v13", "v14", "v15", "v16", "v17", "v18", "v19", "v20", "v21", "v22", "v23", "v24", "v25", "v26", "v27", "v28", "v29", "v30", "v31", "v32", "v33", "v34", "v35", "v36", "v37", "v38", "v39", "v40", "v41", "v42", "v43", "v44", "v45", "v46", "v47", "v48", "v49", "v50", "v51", "v52", "v53", "v54", "v55", "v56", "v57", "v58", "v59", "v60", "v61", "v62", "v63", "v64", "v65", "v66", "v67", "v68", "v69", "v70", "v71", "v72", "v73", "v74", "v75", "v76", "v77", "v78", "v79", "v80", "v81", "v82", "v83", "v84", "v85", "v86", "v87", "v88", "v89", "v90", "v91", "v92", "v93", "v94", "v95", "v96", "v97", "v98", "v99", "v100", "v101", "v102", "v103", "v104", "v105", "v106", "v107", "v108", "v109", "v110", "v111", "v112", "v113", "v114", "v115", "v116", "v117", "v118", "v119", "v120", "v121", "v122", "v123", "v124", "v125", "v126", "v127", "v128", "v129", "v130", "v131", "v132", "v133", "v134", "v135", "v136", "v137", "v138", "v139", "v140", "v141", "v142", "v143", "v144", "v145", "v146", "v147", "v148", "v149", "v150", "v151", "v152", "v153", "v154", "v155", "v156", "v157", "v158", "v159", "v160", "v161", "v162", "v163", "v164", "v165", "v166", "v167", "v168", "v169", "v170", "v171", "v172", "v173", "v174", "v175", "v176", "v177", "v178", "v179", "v180", "v181", "v182", "v183", "v184", "v185", "v186", "v187", "v188", "v189", "v190", "v191", "v192", "v193", "v194", "v195", "v196", "v197", "v198", "v199", "a0", "a1", "a2", "a3", "a4", "a5", "a6", "a7", "a8", "a9", "a10", "a11", "a12", "a13", "a14", "a15", "a16", "a17", "a18", "a19", "a20", "a21", "a22", "a23", "a24", "a25", "a26", "a27", "a28", "a29", "a30", "a31", "a32", "a33", "a34", "a35", "a36", "a37", "a38", "a39", "a40", "a41", "a42", "a43", "a44", "a45", "a46", "a47", "a48", "a49", "a50", "a51", "a52", "a53", "a54", "a55", "a56", "a57", "a58", "a59", "a60", "a61", "a62", "a63", "a64", "a65", "a66", "a67", "a68", "a69", "a70", "a71", "a72", "a73", "a74", "a75", "a76", "a77", "a78", "a79", "a80", "a81", "a82", "a83", "a84", "a85", "a86", "a87", "a88", "a89", "a90", "a91", "a92", "a93", "a94", "a95", "a96", "a97", "a98", "a99", "a100", "a101", "a102", "a103", "a104", "a105", "a106", "a107", "a108", "a109", "a110", "a111", "a112", "a113", "a114", "a115", "a116", "a117", "a118", "a119", "a120", "a121", "a122", "a123", "a124", "a125", "a126", "a127", "a128", "a129", "a130", "a131", "a132", "a133", "a134", "a135", "a136", "a137", "a138", "a139", "a140", "a141", "a142", "a143", "a144", "a145", "a146", "a147", "a148", "a149", "a150", "a151", "a152", "a153", "a154", "a155", "a156", "a157", "a158", "a159", "a160", "a161", "a162", "a163", "a164", "a165", "a166", "a167", "a168", "a169", "a170", "a171", "a172", "a173", "a174", "a175", "a176", "a177", "a178", "a179", "a180", "a181", "a182", "a183", "a184", "a185", "a186", "a187", "a188", "a189", "a190", "a191", "a192", "a193", "a194", "a195", "a196", "a197", "a198", "a199", "a200", "a201", "a202", "a203", "a204", "a205", "a206", "a207", "a208", "a209", "a210", "a211", "a212", "a213", "a214", "a215", "a216", "a217", "a218", "a219", "a220", "a221", "a222", "a223", "a224", "a225", "a226", "a227", "a228", "a229", "a230", "a231", "a232", "a233", "a234", "a235", "a236", "a237", "a238", "a239", "a240", "a241", "a242", "a243", "a244", "a245", "a246", "a247", "a248", "a249", "a250", "a251", "a252", "a253", "a254", "a255", "s40", "s41", "s42", "s43", "s44", "s45", "s46", "s47", "s48", "s49", "s50", "s51", "s52", "s53", "s54", "s55", "s56", "s57", "s58", "s59", "s60", "s61", "s62", "s63", "s64", "s65", "s66", "s67", "s68", "s69", "s70", "s71", "s72", "s73", "s74", "s75", "s76", "s77", "s78", "s79", "s80", "s81", "s82", "s83", "s84", "s85", "s86", "s87", "s88", "s89", "s90", "s91", "s92", "s93", "s94", "s95", "s96", "s97", "s98", "s99", "vcc", "scc", "memory"
#define V_ASM_TEXT \
  "s_mov_b64 s[40:41], %32\n" \
  "s_and_b32 s41, s41, 0xffff\n" \
  "s_mov_b32 s42, 255852544\n" \
  "s_mov_b32 s43, 0x00020000\n" \
  "s_mov_b32 s46, 4718592\n" \
  "s_mov_b32 s47, 0x00020000\n" \
  "s_lshl_b32 s72, %36, 11\n" \
  "buffer_load_dword v196, off, s[40:43], %46 sc1\n" \
  "s_mov_b64 s[44:45], %34\n" \
  "s_and_b32 s45, s45, 0xffff\n" \
  "s_add_u32 s60, %38, 0\n" \
  "s_lshl_b32 s61, s72, 1\n" \
  "s_add_u32 m0, s61, 16384\n" \
  "s_nop 0\n" \
  "buffer_load_dwordx4 v204, s[44:47], s60 offen lds\n" \
  "buffer_load_dwordx4 v205, s[44:47], s60 offen offset:1024 lds\n" \
  "buffer_load_dwordx4 v206, s[44:47], s60 offen offset:2048 lds\n" \
  "buffer_load_dwordx4 v207, s[44:47], s60 offen offset:3072 lds\n" \
  "s_add_u32 s60, %38, 18432\n" \
  "s_lshl_b32 s61, s72, 1\n" \
  "s_add_u32 m0, s61, 32768\n" \
  "s_nop 0\n" \
  "buffer_load_dwordx4 v204, s[44:47], s60 offen lds\n" \
  "buffer_load_dwordx4 v205, s[44:47], s60 offen offset:1024 lds\n" \
  "buffer_load_dwordx4 v206, s[44:47], s60 offen offset:2048 lds\n" \
  "buffer_load_dwordx4 v207, s[44:47], s60 offen offset:3072 lds\n" \
  "s_add_u32 s60, %38, 256\n" \
  "s_lshl_b32 s61, s72, 1\n" \
  "s_add_u32 m0, s61, 49152\n" \
  "s_nop 0\n" \
  "buffer_load_dwordx4 v204, s[44:47], s60 offen lds\n" \
  "buffer_load_dwordx4 v205, s[44:47], s60 offen offset:1024 lds\n" \
  "buffer_load_dwordx4 v206, s[44:47], s60 offen offset:2048 lds\n" \
  "buffer_load_dwordx4 v207, s[44:47], s60 offen offset:3072 lds\n" \
  "s_add_u32 s60, %38, 18688\n" \
  "s_lshl_b32 s61, s72, 1\n" \
  "s_add_u32 m0, s61, 65536\n" \
  "s_nop 0\n" \
  "buffer_load_dwordx4 v204, s[44:47], s60 offen lds\n" \
  "buffer_load_dwordx4 v205, s[44:47], s60 offen offset:1024 lds\n" \
  "buffer_load_dwordx4 v206, s[44:47], s60 offen offset:2048 lds\n" \
  "buffer_load_dwordx4 v207, s[44:47], s60 offen offset:3072 lds\n" \
  "s_add_u32 s60, %38, 512\n" \
  "s_lshl_b32 s61, s72, 1\n" \
  "s_add_u32 m0, s61, 81920\n" \
  "s_nop 0\n" \
  "buffer_load_dwordx4 v204, s[44:47], s60 offen lds\n" \
  "buffer_load_dwordx4 v205, s[44:47], s60 offen offset:1024 lds\n" \
  "buffer_load_dwordx4 v206, s[44:47], s60 offen offset:2048 lds\n" \
  "buffer_load_dwordx4 v207, s[44:47], s60 offen offset:3072 lds\n" \
  "s_add_u32 s60, %38, 18944\n" \
  "s_lshl_b32 s61, s72, 1\n" \
  "s_add_u32 m0, s61, 98304\n" \
  "s_nop 0\n" \
  "buffer_load_dwordx4 v204, s[44:47], s60 offen lds\n" \
  "buffer_load_dwordx4 v205, s[44:47], s60 offen offset:1024 lds\n" \
  "buffer_load_dwordx4 v206, s[44:47], s60 offen offset:2048 lds\n" \
  "buffer_load_dwordx4 v207, s[44:47], s60 offen offset:3072 lds\n" \
  "s_add_u32 s60, %38, 768\n" \
  "s_lshl_b32 s61, s72, 1\n" \
  "s_add_u32 m0, s61, 114688\n" \
  "s_nop 0\n" \
  "buffer_load_dwordx4 v204, s[44:47], s60 offen lds\n" \
  "buffer_load_dwordx4 v205, s[44:47], s60 offen offset:1024 lds\n" \
  "buffer_load_dwordx4 v206, s[44:47], s60 offen offset:2048 lds\n" \
  "buffer_load_dwordx4 v207, s[44:47], s60 offen offset:3072 lds\n" \
  "s_add_u32 s60, %38, 19200\n" \
  "s_lshl_b32 s61, s72, 1\n" \
  "s_add_u32 m0, s61, 131072\n" \
  "s_nop 0\n" \
  "buffer_load_dwordx4 v204, s[44:47], s60 offen lds\n" \
  "buffer_load_dwordx4 v205, s[44:47], s60 offen offset:1024 lds\n" \
  "buffer_load_dwordx4 v206, s[44:47], s60 offen offset:2048 lds\n" \
  "buffer_load_dwordx4 v207, s[44:47], s60 offen offset:3072 lds\n" \
  "s_mov_b32 s60, %38\n" \
  "s_lshl_b32 s61, s72, 1\n" \
  "s_add_u32 m0, s61, 147456\n" \
  "s_nop 0\n" \
  "buffer_load_dwordx4 v208, s[44:47], s60 offen lds\n" \
  "buffer_load_dwordx4 v209, s[44:47], s60 offen offset:1024 lds\n" \
  "buffer_load_dwordx4 v210, s[44:47], s60 offen offset:2048 lds\n" \
  "buffer_load_dwordx4 v211, s[44:47], s60 offen offset:3072 lds\n" \
  "s_mov_b64 s[44:45], %35\n" \
  "s_and_b32 s45, s45, 0xffff\n" \
  "s_lshr_b32 s60, s72, 3\n" \
  "s_mov_b32 m0, s72\n" \
  "s_nop 0\n" \
  "buffer_load_dwordx4 v214, s[44:47], s60 offen lds\n" \
  "s_add_u32 s60, s60, 128\n" \
  "s_add_u32 m0, s72, 1024\n" \
  "s_nop 0\n" \
  "buffer_load_dwordx4 v214, s[44:47], s60 offen lds\n" \
  "s_movk_i32 s60, 1024\n" \
  "s_movk_i32 m0, 8192\n" \
  "s_nop 0\n" \
  "buffer_load_dwordx4 v214, s[44:47], s60 offen lds\n" \
  "s_waitcnt vmcnt(27)\n" \
  "s_barrier\n" \
  "v_add_u32 v194, 16384, v215\n" \
  "v_xor_b32 v195, 2176, v194\n" \
  "ds_read_b64_tr_b16 a[144:145], v194\n" \
  "ds_read_b64_tr_b16 a[146:147], v194 offset:512\n" \
  "ds_read_b64_tr_b16 a[148:149], v195\n" \
  "ds_read_b64_tr_b16 a[150:151], v195 offset:512\n" \
  "ds_read_b64_tr_b16 a[152:153], v194 offset:8192\n" \
  "ds_read_b64_tr_b16 a[154:155], v194 offset:8704\n" \
  "ds_read_b64_tr_b16 a[156:157], v195 offset:8192\n" \
  "ds_read_b64_tr_b16 a[158:159], v195 offset:8704\n" \
  "v_add_u32 v194, 32768, v215\n" \
  "v_xor_b32 v195, 2176, v194\n" \
  "ds_read_b64_tr_b16 a[160:161], v194\n" \
  "ds_read_b64_tr_b16 a[162:163], v194 offset:512\n" \
  "ds_read_b64_tr_b16 a[164:165], v195\n" \
  "ds_read_b64_tr_b16 a[166:167], v195 offset:512\n" \
  "ds_read_b64_tr_b16 a[168:169], v194 offset:8192\n" \
  "ds_read_b64_tr_b16 a[170:171], v194 offset:8704\n" \
  "ds_read_b64_tr_b16 a[172:173], v195 offset:8192\n" \
  "ds_read_b64_tr_b16 a[174:175], v195 offset:8704\n" \
  "v_add_u32 v194, 49152, v215\n" \
  "v_xor_b32 v195, 2176, v194\n" \
  "ds_read_b64_tr_b16 a[176:177], v194\n" \
  "ds_read_b64_tr_b16 a[178:179], v194 offset:512\n" \
  "ds_read_b64_tr_b16 a[180:181], v195\n" \
  "ds_read_b64_tr_b16 a[182:183], v195 offset:512\n" \
  "ds_read_b64_tr_b16 a[184:185], v194 offset:8192\n" \
  "ds_read_b64_tr_b16 a[186:187], v194 offset:8704\n" \
  "ds_read_b64_tr_b16 a[188:189], v195 offset:8192\n" \
  "ds_read_b64_tr_b16 a[190:191], v195 offset:8704\n" \
  "s_waitcnt lgkmcnt(0)\n" \
  "s_barrier\n" \
  "s_mov_b64 s[44:45], %33\n" \
  "s_and_b32 s45, s45, 0xffff\n" \
  "s_add_u32 s60, %37, 0\n" \
  "s_lshl_b32 s61, s72, 1\n" \
  "s_add_u32 m0, s61, 16384\n" \
  "s_nop 0\n" \
  "buffer_load_dwordx4 v200, s[44:47], s60 offen lds\n" \
  "buffer_load_dwordx4 v201, s[44:47], s60 offen offset:1024 lds\n" \
  "buffer_load_dwordx4 v202, s[44:47], s60 offen offset:2048 lds\n" \
  "buffer_load_dwordx4 v203, s[44:47], s60 offen offset:3072 lds\n" \
  "s_add_u32 s60, %37, 524288\n" \
  "s_lshl_b32 s61, s72, 1\n" \
  "s_add_u32 m0, s61, 32768\n" \
  "s_nop 0\n" \
  "buffer_load_dwordx4 v200, s[44:47], s60 offen lds\n" \
  "buffer_load_dwordx4 v201, s[44:47], s60 offen offset:1024 lds\n" \
  "buffer_load_dwordx4 v202, s[44:47], s60 offen offset:2048 lds\n" \
  "buffer_load_dwordx4 v203, s[44:47], s60 offen offset:3072 lds\n" \
  "s_add_u32 s60, %37, 1048576\n" \
  "s_lshl_b32 s61, s72, 1\n" \
  "s_add_u32 m0, s61, 49152\n" \
  "s_nop 0\n" \
  "buffer_load_dwordx4 v200, s[44:47], s60 offen lds\n" \
  "buffer_load_dwordx4 v201, s[44:47], s60 offen offset:1024 lds\n" \
  "buffer_load_dwordx4 v202, s[44:47], s60 offen offset:2048 lds\n" \
  "buffer_load_dwordx4 v203, s[44:47], s60 offen offset:3072 lds\n" \
  "v_readfirstlane_b32 s55, v196\n" \
  "s_add_u32 s55, s55, 1\n" \
  "s_waitcnt vmcnt(27)\n" \
  "s_barrier\n" \
  "v_add_u32 v194, 65536, v215\n" \
  "v_xor_b32 v195, 2176, v194\n" \
  "ds_read_b64_tr_b16 a[192:193], v194\n" \
  "ds_read_b64_tr_b16 a[194:195], v194 offset:512\n" \
  "ds_read_b64_tr_b16 a[196:197], v195\n" \
  "ds_read_b64_tr_b16 a[198:199], v195 offset:512\n" \
  "ds_read_b64_tr_b16 a[200:201], v194 offset:8192\n" \
  "ds_read_b64_tr_b16 a[202:203], v194 offset:8704\n" \
  "ds_read_b64_tr_b16 a[204:205], v195 offset:8192\n" \
  "ds_read_b64_tr_b16 a[206:207], v195 offset:8704\n" \
  "v_add_u32 v194, 81920, v215\n" \
  "v_xor_b32 v195, 2176, v194\n" \
  "ds_read_b64_tr_b16 a[208:209], v194\n" \
  "ds_read_b64_tr_b16 a[210:211], v194 offset:512\n" \
  "ds_read_b64_tr_b16 a[212:213], v195\n" \
  "ds_read_b64_tr_b16 a[214:215], v195 offset:512\n" \
  "ds_read_b64_tr_b16 a[216:217], v194 offset:8192\n" \
  "ds_read_b64_tr_b16 a[218:219], v194 offset:8704\n" \
  "ds_read_b64_tr_b16 a[220:221], v195 offset:8192\n" \
  "ds_read_b64_tr_b16 a[222:223], v195 offset:8704\n" \
  "v_add_u32 v194, 98304, v215\n" \
  "v_xor_b32 v195, 2176, v194\n" \
  "ds_read_b64_tr_b16 a[224:225], v194\n" \
  "ds_read_b64_tr_b16 a[226:227], v194 offset:512\n" \
  "ds_read_b64_tr_b16 a[228:229], v195\n" \
  "ds_read_b64_tr_b16 a[230:231], v195 offset:512\n" \
  "ds_read_b64_tr_b16 a[232:233], v194 offset:8192\n" \
  "ds_read_b64_tr_b16 a[234:235], v194 offset:8704\n" \
  "ds_read_b64_tr_b16 a[236:237], v195 offset:8192\n" \
  "ds_read_b64_tr_b16 a[238:239], v195 offset:8704\n" \
  "s_waitcnt lgkmcnt(0)\n" \
  "s_barrier\n" \
  "s_add_u32 s60, %37, 1572864\n" \
  "s_lshl_b32 s61, s72, 1\n" \
  "s_add_u32 m0, s61, 65536\n" \
  "s_nop 0\n" \
  "buffer_load_dwordx4 v200, s[44:47], s60 offen lds\n" \
  "buffer_load_dwordx4 v201, s[44:47], s60 offen offset:1024 lds\n" \
  "buffer_load_dwordx4 v202, s[44:47], s60 offen offset:2048 lds\n" \
  "buffer_load_dwordx4 v203, s[44:47], s60 offen offset:3072 lds\n" \
  "s_add_u32 s60, %37, 2097152\n" \
  "s_lshl_b32 s61, s72, 1\n" \
  "s_add_u32 m0, s61, 81920\n" \
  "s_nop 0\n" \
  "buffer_load_dwordx4 v200, s[44:47], s60 offen lds\n" \
  "buffer_load_dwordx4 v201, s[44:47], s60 offen offset:1024 lds\n" \
  "buffer_load_dwordx4 v202, s[44:47], s60 offen offset:2048 lds\n" \
  "buffer_load_dwordx4 v203, s[44:47], s60 offen offset:3072 lds\n" \
  "s_add_u32 s60, %37, 2621440\n" \
  "s_lshl_b32 s61, s72, 1\n" \
  "s_add_u32 m0, s61, 98304\n" \
  "s_nop 0\n" \
  "buffer_load_dwordx4 v200, s[44:47], s60 offen lds\n" \
  "buffer_load_dwordx4 v201, s[44:47], s60 offen offset:1024 lds\n" \
  "buffer_load_dwordx4 v202, s[44:47], s60 offen offset:2048 lds\n" \
  "buffer_load_dwordx4 v203, s[44:47], s60 offen offset:3072 lds\n" \
  "s_waitcnt vmcnt(27)\n" \
  "s_barrier\n" \
  "v_add_u32 v194, 114688, v215\n" \
  "v_xor_b32 v195, 2176, v194\n" \
  "ds_read_b64_tr_b16 a[240:241], v194\n" \
  "ds_read_b64_tr_b16 a[242:243], v194 offset:512\n" \
  "ds_read_b64_tr_b16 a[244:245], v195\n" \
  "ds_read_b64_tr_b16 a[246:247], v195 offset:512\n" \
  "ds_read_b64_tr_b16 a[248:249], v194 offset:8192\n" \
  "ds_read_b64_tr_b16 a[250:251], v194 offset:8704\n" \
  "ds_read_b64_tr_b16 a[252:253], v195 offset:8192\n" \
  "ds_read_b64_tr_b16 a[254:255], v195 offset:8704\n" \
  "v_add_u32 v194, 131072, v215\n" \
  "v_xor_b32 v195, 2176, v194\n" \
  "ds_read_b64_tr_b16 v[0:1], v194\n" \
  "ds_read_b64_tr_b16 v[2:3], v194 offset:512\n" \
  "ds_read_b64_tr_b16 v[4:5], v195\n" \
  "ds_read_b64_tr_b16 v[6:7], v195 offset:512\n" \
  "ds_read_b64_tr_b16 v[8:9], v194 offset:8192\n" \
  "ds_read_b64_tr_b16 v[10:11], v194 offset:8704\n" \
  "ds_read_b64_tr_b16 v[12:13], v195 offset:8192\n" \
  "ds_read_b64_tr_b16 v[14:15], v195 offset:8704\n" \
  "v_mov_b32 v194, v216\n" \
  "v_xor_b32 v195, 2176, v194\n" \
  "ds_read_b64_tr_b16 v[16:17], v194\n" \
  "ds_read_b64_tr_b16 v[18:19], v194 offset:512\n" \
  "ds_read_b64_tr_b16 v[20:21], v195\n" \
  "ds_read_b64_tr_b16 v[22:23], v195 offset:512\n" \
  "ds_read_b64_tr_b16 v[24:25], v194 offset:8192\n" \
  "ds_read_b64_tr_b16 v[26:27], v194 offset:8704\n" \
  "ds_read_b64_tr_b16 v[28:29], v195 offset:8192\n" \
  "ds_read_b64_tr_b16 v[30:31], v195 offset:8704\n" \
  "v_mov_b32 v194, v217\n" \
  "v_xor_b32 v195, 2176, v194\n" \
  "ds_read_b64_tr_b16 v[32:33], v194\n" \
  "ds_read_b64_tr_b16 v[34:35], v194 offset:512\n" \
  "ds_read_b64_tr_b16 v[36:37], v195\n" \
  "ds_read_b64_tr_b16 v[38:39], v195 offset:512\n" \
  "ds_read_b64_tr_b16 v[40:41], v194 offset:8192\n" \
  "ds_read_b64_tr_b16 v[42:43], v194 offset:8704\n" \
  "ds_read_b64_tr_b16 v[44:45], v195 offset:8192\n" \
  "ds_read_b64_tr_b16 v[46:47], v195 offset:8704\n" \
  "s_waitcnt lgkmcnt(0)\n" \
  "s_barrier\n" \
  "s_add_u32 s60, %37, 3145728\n" \
  "s_lshl_b32 s61, s72, 1\n" \
  "s_add_u32 m0, s61, 114688\n" \
  "s_nop 0\n" \
  "buffer_load_dwordx4 v200, s[44:47], s60 offen lds\n" \
  "buffer_load_dwordx4 v201, s[44:47], s60 offen offset:1024 lds\n" \
  "buffer_load_dwordx4 v202, s[44:47], s60 offen offset:2048 lds\n" \
  "buffer_load_dwordx4 v203, s[44:47], s60 offen offset:3072 lds\n" \
  "s_add_u32 s60, %37, 3670016\n" \
  "s_lshl_b32 s61, s72, 1\n" \
  "s_add_u32 m0, s61, 131072\n" \
  "s_nop 0\n" \
  "buffer_load_dwordx4 v200, s[44:47], s60 offen lds\n" \
  "buffer_load_dwordx4 v201, s[44:47], s60 offen offset:1024 lds\n" \
  "buffer_load_dwordx4 v202, s[44:47], s60 offen offset:2048 lds\n" \
  "buffer_load_dwordx4 v203, s[44:47], s60 offen offset:3072 lds\n" \
  "s_add_u32 s60, %37, 4194304\n" \
  "s_lshl_b32 s61, s72, 1\n" \
  "s_add_u32 m0, s61, 147456\n" \
  "s_nop 0\n" \
  "buffer_load_dwordx4 v200, s[44:47], s60 offen lds\n" \
  "buffer_load_dwordx4 v201, s[44:47], s60 offen offset:1024 lds\n" \
  "buffer_load_dwordx4 v202, s[44:47], s60 offen offset:2048 lds\n" \
  "buffer_load_dwordx4 v203, s[44:47], s60 offen offset:3072 lds\n" \
  "buffer_load_dwordx2 v[198:199], off, s[40:43], %45 sc1\n" \
  "s_waitcnt vmcnt(25)\n" \
  "s_barrier\n" \
  "v_add_u32 v194, 16384, v212\n" \
  "ds_read_b64_tr_b16 a[0:1], v194\n" \
  "ds_read_b64_tr_b16 a[2:3], v194 offset:512\n" \
  "ds_read_b64_tr_b16 a[4:5], v194 offset:8192\n" \
  "ds_read_b64_tr_b16 a[6:7], v194 offset:8704\n" \
  "v_add_u32 v195, 16384, v213\n" \
  "ds_read_b64_tr_b16 a[72:73], v195\n" \
  "ds_read_b64_tr_b16 a[74:75], v195 offset:512\n" \
  "ds_read_b64_tr_b16 a[76:77], v195 offset:8192\n" \
  "ds_read_b64_tr_b16 a[78:79], v195 offset:8704\n" \
  "v_add_u32 v194, 32768, v212\n" \
  "ds_read_b64_tr_b16 a[8:9], v194\n" \
  "ds_read_b64_tr_b16 a[10:11], v194 offset:512\n" \
  "ds_read_b64_tr_b16 a[12:13], v194 offset:8192\n" \
  "ds_read_b64_tr_b16 a[14:15], v194 offset:8704\n" \
  "v_add_u32 v195, 32768, v213\n" \
  "ds_read_b64_tr_b16 a[80:81], v195\n" \
  "ds_read_b64_tr_b16 a[82:83], v195 offset:512\n" \
  "ds_read_b64_tr_b16 a[84:85], v195 offset:8192\n" \
  "ds_read_b64_tr_b16 a[86:87], v195 offset:8704\n" \
  "v_add_u32 v194, 49152, v212\n" \
  "ds_read_b64_tr_b16 a[16:17], v194\n" \
  "ds_read_b64_tr_b16 a[18:19], v194 offset:512\n" \
  "ds_read_b64_tr_b16 a[20:21], v194 offset:8192\n" \
  "ds_read_b64_tr_b16 a[22:23], v194 offset:8704\n" \
  "v_add_u32 v195, 49152, v213\n" \
  "ds_read_b64_tr_b16 a[88:89], v195\n" \
  "ds_read_b64_tr_b16 a[90:91], v195 offset:512\n" \
  "ds_read_b64_tr_b16 a[92:93], v195 offset:8192\n" \
  "ds_read_b64_tr_b16 a[94:95], v195 offset:8704\n" \
  "s_waitcnt lgkmcnt(0)\n" \
  "s_waitcnt vmcnt(13)\n" \
  "s_barrier\n" \
  "v_add_u32 v194, 65536, v212\n" \
  "ds_read_b64_tr_b16 a[24:25], v194\n" \
  "ds_read_b64_tr_b16 a[26:27], v194 offset:512\n" \
  "ds_read_b64_tr_b16 a[28:29], v194 offset:8192\n" \
  "ds_read_b64_tr_b16 a[30:31], v194 offset:8704\n" \
  "v_add_u32 v195, 65536, v213\n" \
  "ds_read_b64_tr_b16 a[96:97], v195\n" \
  "ds_read_b64_tr_b16 a[98:99], v195 offset:512\n" \
  "ds_read_b64_tr_b16 a[100:101], v195 offset:8192\n" \
  "ds_read_b64_tr_b16 a[102:103], v195 offset:8704\n" \
  "v_add_u32 v194, 81920, v212\n" \
  "ds_read_b64_tr_b16 a[32:33], v194\n" \
  "ds_read_b64_tr_b16 a[34:35], v194 offset:512\n" \
  "ds_read_b64_tr_b16 a[36:37], v194 offset:8192\n" \
  "ds_read_b64_tr_b16 a[38:39], v194 offset:8704\n" \
  "v_add_u32 v195, 81920, v213\n" \
  "ds_read_b64_tr_b16 a[104:105], v195\n" \
  "ds_read_b64_tr_b16 a[106:107], v195 offset:512\n" \
  "ds_read_b64_tr_b16 a[108:109], v195 offset:8192\n" \
  "ds_read_b64_tr_b16 a[110:111], v195 offset:8704\n" \
  "v_add_u32 v194, 98304, v212\n" \
  "ds_read_b64_tr_b16 a[40:41], v194\n" \
  "ds_read_b64_tr_b16 a[42:43], v194 offset:512\n" \
  "ds_read_b64_tr_b16 a[44:45], v194 offset:8192\n" \
  "ds_read_b64_tr_b16 a[46:47], v194 offset:8704\n" \
  "v_add_u32 v195, 98304, v213\n" \
  "ds_read_b64_tr_b16 a[112:113], v195\n" \
  "ds_read_b64_tr_b16 a[114:115], v195 offset:512\n" \
  "ds_read_b64_tr_b16 a[116:117], v195 offset:8192\n" \
  "ds_read_b64_tr_b16 a[118:119], v195 offset:8704\n" \
  "s_waitcnt lgkmcnt(0)\n" \
  "s_waitcnt vmcnt(1)\n" \
  "s_barrier\n" \
  "v_add_u32 v194, 114688, v212\n" \
  "ds_read_b64_tr_b16 a[48:49], v194\n" \
  "ds_read_b64_tr_b16 a[50:51], v194 offset:512\n" \
  "ds_read_b64_tr_b16 a[52:53], v194 offset:8192\n" \
  "ds_read_b64_tr_b16 a[54:55], v194 offset:8704\n" \
  "v_add_u32 v195, 114688, v213\n" \
  "ds_read_b64_tr_b16 a[120:121], v195\n" \
  "ds_read_b64_tr_b16 a[122:123], v195 offset:512\n" \
  "ds_read_b64_tr_b16 a[124:125], v195 offset:8192\n" \
  "ds_read_b64_tr_b16 a[126:127], v195 offset:8704\n" \
  "v_add_u32 v194, 131072, v212\n" \
  "ds_read_b64_tr_b16 a[56:57], v194\n" \
  "ds_read_b64_tr_b16 a[58:59], v194 offset:512\n" \
  "ds_read_b64_tr_b16 a[60:61], v194 offset:8192\n" \
  "ds_read_b64_tr_b16 a[62:63], v194 offset:8704\n" \
  "v_add_u32 v195, 131072, v213\n" \
  "ds_read_b64_tr_b16 a[128:129], v195\n" \
  "ds_read_b64_tr_b16 a[130:131], v195 offset:512\n" \
  "ds_read_b64_tr_b16 a[132:133], v195 offset:8192\n" \
  "ds_read_b64_tr_b16 a[134:135], v195 offset:8704\n" \
  "v_add_u32 v194, 147456, v212\n" \
  "ds_read_b64_tr_b16 a[64:65], v194\n" \
  "ds_read_b64_tr_b16 a[66:67], v194 offset:512\n" \
  "ds_read_b64_tr_b16 a[68:69], v194 offset:8192\n" \
  "ds_read_b64_tr_b16 a[70:71], v194 offset:8704\n" \
  "v_add_u32 v195, 147456, v213\n" \
  "ds_read_b64_tr_b16 a[136:137], v195\n" \
  "ds_read_b64_tr_b16 a[138:139], v195 offset:512\n" \
  "ds_read_b64_tr_b16 a[140:141], v195 offset:8192\n" \
  "ds_read_b64_tr_b16 a[142:143], v195 offset:8704\n" \
  "s_waitcnt lgkmcnt(0)\n" \
  "s_waitcnt vmcnt(0)\n" \
  "v_readfirstlane_b32 s96, v198\n" \
  "v_readfirstlane_b32 s63, v199\n" \
  "s_cmp_eq_u32 s63, s55\n" \
  "s_cselect_b32 s96, s96, 0\n" \
  "s_cmp_ge_u32 s96, 0xc0000000\n" \
  "s_cbranch_scc1 .LV_go_x_%=\n" \
  "s_mov_b32 s69, 0\n" \
  ".LV_go_%=:\n" \
  "buffer_load_dwordx2 v[196:197], off, s[40:43], %45 sc1\n" \
  "s_waitcnt vmcnt(0)\n" \
  "v_readfirstlane_b32 s96, v196\n" \
  "v_readfirstlane_b32 s63, v197\n" \
  "s_cmp_eq_u32 s63, s55\n" \
  "s_cselect_b32 s96, s96, 0\n" \
  "s_cmp_ge_u32 s96, 0xc0000000\n" \
  "s_cbranch_scc1 .LV_go_x_%=\n" \
  "s_sleep 8\n" \
  "s_add_u32 s69, s69, 1\n" \
  "s_and_b32 s63, s69, 1023\n" \
  "s_cbranch_scc1 .LV_go_%=\n" \
  "s_cmp_gt_u32 s69, 2097152\n" \
  "s_cbranch_scc1 .LV_go_set_%=\n" \
  "buffer_load_dwordx2 v[196:197], off, s[40:43], %46 offset:128 sc1\n" \
  "s_waitcnt vmcnt(0)\n" \
  "v_readfirstlane_b32 s63, v197\n" \
  "s_cmp_eq_u32 s63, s55\n" \
  "s_cbranch_scc0 .LV_go_%=\n" \
  "s_branch .LV_go_x_%=\n" \
  ".LV_go_set_%=:\n" \
  "v_mov_b32 v196, 1\n" \
  "v_mov_b32 v197, s55\n" \
  "buffer_store_dwordx2 v[196:197], off, s[40:43], %46 offset:128 sc1\n" \
  "s_waitcnt vmcnt(0)\n" \
  ".LV_go_x_%=:\n" \
  "s_and_b32 s60, s96, 2\n" \
  "s_lshl_b32 s60, s60, 12\n" \
  "s_xor_b32 s74, %42, s60\n" \
  "s_xor_b32 s75, %44, s60\n" \
  "s_bfe_u32 s56, s96, 0x30010\n" \
  "s_lshl_b32 s56, s56, 12\n" \
  "s_or_b32 s56, s56, 0x200000\n" \
  "s_mov_b32 s57, 1052672\n" \
  "s_lshl_b32 s61, %41, 2\n" \
  "s_lshr_b32 s51, s96, s61\n" \
  "s_and_b32 s51, s51, 7\n" \
  "s_lshl_b32 s51, s51, 12\n" \
  "s_xor_b32 s51, s51, 4210688\n" \
  "s_mov_b32 s71, 0\n" \
  "s_add_u32 s45, s51, 886784\n" \
  "s_mov_b32 s46, -1\n" \
  "v_mov_b64 v[48:49], -1\n" \
  "v_mov_b64 v[50:51], -1\n" \
  "v_and_b32 v194, 3, v250\n" \
  "v_lshlrev_b32 v194, 2, v194\n" \
  "v_lshrrev_b32 v194, v194, s96\n" \
  "v_and_b32 v194, 7, v194\n" \
  "v_lshlrev_b32 v194, 12, v194\n" \
  "v_and_b32 v250, -4, v250\n" \
  "v_xor_b32 v250, v250, v194\n" \
  "v_and_b32 v194, 3, v251\n" \
  "v_lshlrev_b32 v194, 2, v194\n" \
  "v_lshrrev_b32 v194, v194, s96\n" \
  "v_and_b32 v194, 7, v194\n" \
  "v_lshlrev_b32 v194, 12, v194\n" \
  "v_and_b32 v251, -4, v251\n" \
  "v_xor_b32 v251, v251, v194\n" \
  "v_and_b32 v194, 3, v252\n" \
  "v_lshlrev_b32 v194, 2, v194\n" \
  "v_lshrrev_b32 v194, v194, s96\n" \
  "v_and_b32 v194, 7, v194\n" \
  "v_lshlrev_b32 v194, 12, v194\n" \
  "v_and_b32 v252, -4, v252\n" \
  "v_xor_b32 v252, v252, v194\n" \
  "v_xor_b32 v185, s56, v250\n" \
  "buffer_store_dwordx4 v[48:51], v185, s[40:43], 0 offen sc1\n" \
  "v_xor_b32 v186, s56, v251\n" \
  "buffer_store_dwordx4 v[48:51], v186, s[40:43], 0 offen sc1\n" \
  "v_xor_b32 v187, s56, v252\n" \
  "buffer_store_dwordx2 v[48:49], v187, s[40:43], 0 offen sc1\n" \
  "s_xor_b32 s62, s56, s57\n" \
  "s_nop 1\n" \
  "v_xor_b32 v185, s62, v250\n" \
  "buffer_store_dwordx4 v[48:51], v185, s[40:43], 0 offen sc1\n" \
  "v_xor_b32 v186, s62, v251\n" \
  "buffer_store_dwordx4 v[48:51], v186, s[40:43], 0 offen sc1\n" \
  "v_xor_b32 v187, s62, v252\n" \
  "buffer_store_dwordx2 v[48:49], v187, s[40:43], 0 offen sc1\n" \
  "s_andn2_b32 s60, 2, s96\n" \
  "s_lshl_b32 s60, s60, 12\n" \
  "s_xor_b32 s60, s60, 8192\n" \
  "s_xor_b32 s58, %39, s60\n" \
  "s_mov_b32 s49, 0\n" \
  "s_add_u32 s47, %49, s60\n" \
  "s_add_u32 s97, s60, 8388608\n" \
  "s_xor_b32 s50, %40, s60\n" \
  "s_mov_b32 s48, 0\n" \
  "s_mov_b32 s54, 0\n" \
  "s_cmp_eq_u32 %36, 0\n" \
  "s_cselect_b64 s[88:89], -1, 0\n" \
  "s_mov_b32 s68, 0\n" \
  "s_mov_b32 s70, 1\n" \
  "s_mov_b32 s73, 0\n" \
  "s_mov_b32 s44, 0\n" \
  "s_mov_b32 s53, 0\n" \
  "s_memrealtime s[86:87]\n" \
  "s_memtime s[76:77]\n" \
  "s_waitcnt lgkmcnt(0)\n" \
  "s_mov_b32 s90, s76\n" \
  "s_mov_b32 s87, 340\n" \
  "s_mov_b32 s64, 0x3e0293ee\n" \
  "s_mov_b32 s65, 0xbe0293ee\n" \
  "v_mov_b64 v[60:61], 0\n" \
  "v_mov_b64 v[62:63], 0\n" \
  "v_mov_b64 v[64:65], 0\n" \
  "v_mov_b64 v[66:67], 0\n" \
  "v_mov_b64 v[68:69], 0\n" \
  "v_mov_b64 v[70:71], 0\n" \
  "v_mov_b64 v[72:73], 0\n" \
  "v_mov_b64 v[74:75], 0\n" \
  "v_mov_b64 v[76:77], 0\n" \
  "v_mov_b64 v[78:79], 0\n" \
  "v_mov_b64 v[80:81], 0\n" \
  "v_mov_b64 v[82:83], 0\n" \
  "v_mov_b64 v[84:85], 0\n" \
  "v_mov_b64 v[86:87], 0\n" \
  "v_mov_b32 v56, 0\n" \
  "v_mov_b32 v57, s55\n" \
  "v_min_u32 v58, 1, v243\n" \
  "v_lshlrev_b32 v58, 30, v58\n" \
  "v_mov_b64 v[90:91], 0\n" \
  "s_mov_b32 s67, 2130706432\n" \
  "s_waitcnt vmcnt(0) lgkmcnt(0)\n" \
  "s_barrier\n" \
  "s_branch .LV_p_%=\n" \
  "s_nop 7\n" \
  ".p2align 6\n" \
  ".fill (((20 - (.LV_p_%= - .LV_rot_%=)) & 63) >> 2), 4, 0xBF800000\n" \
  ".LV_rot_%=:\n" \
  "s_xor_b32 s51, s51, s56\n" \
  "s_xor_b32 s71, s71, s56\n" \
  "v_xor_b32 v250, s56, v250\n" \
  "v_xor_b32 v251, s56, v251\n" \
  "v_xor_b32 v252, s56, v252\n" \
  "s_mov_b32 s60, s56\n" \
  "s_mov_b32 s56, s57\n" \
  "s_mov_b32 s57, s60\n" \
  "s_add_u32 s59, s51, 819200\n" \
  "s_mov_b32 m0, 0\n" \
  "s_mov_b32 s70, 0\n" \
  "s_waitcnt lgkmcnt(0)\n" \
  ".LV_rt_%=:\n" \
  "s_barrier\n" \
  ".LV_p_%=:\n" \
  "ds_read_b128 v[92:95], v243\n" \
  "ds_read_b128 v[96:99], v243 offset:3072\n" \
  "ds_read_b128 v[100:103], v243 offset:1024\n" \
  "ds_read_b128 v[104:107], v243 offset:2048\n" \
  "s_waitcnt lgkmcnt(3)\n" \
  "v_smfmac_f32_16x16x64_bf16 v[60:63], v[92:95], a[0:7], v242\n" \
  "s_barrier\n" \
  "ds_read_b128 v[108:111], v243 offset:4096\n" \
  "v_smfmac_f32_16x16x64_bf16 v[64:67], v[92:95], a[72:79], v242\n" \
  "ds_read_b128 v[112:115], v243 offset:5120\n" \
  "s_waitcnt lgkmcnt(3)\n" \
  "v_smfmac_f32_16x16x64_bf16 v[60:63], v[100:103], a[8:15], v242\n" \
  "ds_read_b128 v[116:119], v243 offset:6144\n" \
  "s_waitcnt lgkmcnt(3)\n" \
  "v_smfmac_f32_16x16x64_bf16 v[60:63], v[104:107], a[16:23], v242\n" \
  "ds_read_b128 v[120:123], v243 offset:7168\n" \
  "v_smfmac_f32_16x16x64_bf16 v[60:63], v[96:99], a[24:31], v242\n" \
  "ds_read_b128 v[124:127], v243 offset:8192\n" \
  "s_waitcnt lgkmcnt(4)\n" \
  "v_smfmac_f32_16x16x64_bf16 v[60:63], v[108:111], a[32:39], v242\n" \
  "s_waitcnt lgkmcnt(3)\n" \
  "v_smfmac_f32_16x16x64_bf16 v[60:63], v[112:115], a[40:47], v242\n" \
  "s_waitcnt lgkmcnt(2)\n" \
  "v_smfmac_f32_16x16x64_bf16 v[60:63], v[116:119], a[48:55], v242\n" \
  "s_waitcnt lgkmcnt(1)\n" \
  "v_smfmac_f32_16x16x64_bf16 v[60:63], v[120:123], a[56:63], v242\n" \
  "s_waitcnt lgkmcnt(0)\n" \
  "v_smfmac_f32_16x16x64_bf16 v[60:63], v[124:127], a[64:71], v242\n" \
  "s_nop 2\n" \
  "v_smfmac_f32_16x16x64_bf16 v[64:67], v[100:103], a[80:87], v242\n" \
  "s_nop 2\n" \
  "v_smfmac_f32_16x16x64_bf16 v[64:67], v[104:107], a[88:95], v242\n" \
  "v_add_f32 v60, v60, v62\n" \
  "v_add_f32 v61, v61, v63\n" \
  "v_cvt_pk_bf16_f32 v88, v60, v61\n" \
  "v_cmp_u_f32 vcc, v60, v61\n" \
  "v_smfmac_f32_16x16x64_bf16 v[64:67], v[96:99], a[96:103], v242\n" \
  "s_cbranch_vccnz .LV_slow_%=\n" \
  ".LV_back_%=:\n" \
  "ds_write_b32 v244, v88\n" \
  "v_smfmac_f32_16x16x64_bf16 v[64:67], v[108:111], a[104:111], v242\n" \
  "v_smfmac_f32_16x16x64_bf16 v[64:67], v[112:115], a[112:119], v242\n" \
  "v_smfmac_f32_16x16x64_bf16 v[64:67], v[116:119], a[120:127], v242\n" \
  "v_smfmac_f32_16x16x64_bf16 v[64:67], v[120:123], a[128:135], v242\n" \
  "s_waitcnt lgkmcnt(0)\n" \
  "s_waitcnt vmcnt(0) \n" \
  "s_barrier\n" \
  "ds_read_b64_tr_b16 v[92:93], v245\n" \
  "ds_read_b64_tr_b16 v[94:95], v245 offset:512\n" \
  "v_smfmac_f32_16x16x64_bf16 v[64:67], v[124:127], a[136:143], v242\n" \
  "ds_read_b64_tr_b16 v[96:97], v245 offset:1024\n" \
  "ds_read_b64_tr_b16 v[98:99], v245 offset:1536\n" \
  "s_nop 5\n" \
  "v_add_f32 v64, v64, v66\n" \
  "v_add_f32 v65, v65, v67\n" \
  "v_cvt_pk_bf16_f32 v89, v64, v65\n" \
  "s_waitcnt lgkmcnt(2)\n" \
  "v_smfmac_f32_16x16x64_bf16 v[76:79], v[92:95], a[208:215], v248\n" \
  "ds_write_b32 v244, v89 offset:256\n" \
  "s_waitcnt lgkmcnt(1)\n" \
  "v_smfmac_f32_16x16x64_bf16 v[76:79], v[96:99], a[216:223], v248\n" \
  "v_smfmac_f32_16x16x64_bf16 v[68:71], v[92:95], a[144:151], v248\n" \
  "v_smfmac_f32_16x16x64_bf16 v[72:75], v[92:95], a[176:183], v248\n" \
  "buffer_store_dwordx2 v[56:57], v58, s[40:43], %43 offen sc1\n" \
  "v_smfmac_f32_16x16x64_bf16 v[68:71], v[96:99], a[152:159], v248\n" \
  "s_waitcnt lgkmcnt(0)\n" \
  "s_barrier\n" \
  "v_smfmac_f32_16x16x64_bf16 v[80:83], v[92:95], a[240:247], v248\n" \
  "ds_read_b64_tr_b16 v[102:103], v245 offset:768\n" \
  "ds_read_b64_tr_b16 v[100:101], v245 offset:256\n" \
  "v_smfmac_f32_16x16x64_bf16 v[84:87], v[92:95], v[16:23], v248\n" \
  "ds_read_b64_tr_b16 v[94:95], v245 offset:1792\n" \
  "ds_read_b64_tr_b16 v[92:93], v245 offset:1280\n" \
  "v_smfmac_f32_16x16x64_bf16 v[80:83], v[96:99], a[248:255], v248\n" \
  "v_smfmac_f32_16x16x64_bf16 v[72:75], v[96:99], a[184:191], v248\n" \
  "s_waitcnt lgkmcnt(2)\n" \
  "v_smfmac_f32_16x16x64_bf16 v[68:71], v[100:103], a[160:167], v248\n" \
  "s_waitcnt lgkmcnt(0)\n" \
  "v_smfmac_f32_16x16x64_bf16 v[68:71], v[92:95], a[168:175], v248\n" \
  "s_nop 2\n" \
  "v_smfmac_f32_16x16x64_bf16 v[72:75], v[100:103], a[192:199], v248\n" \
  "s_nop 2\n" \
  "v_smfmac_f32_16x16x64_bf16 v[72:75], v[92:95], a[200:207], v248\n" \
  "s_nop 1\n" \
  "v_cvt_pk_bf16_f32 v68, v68, v69\n" \
  "v_smfmac_f32_16x16x64_bf16 v[84:87], v[96:99], v[24:31], v248\n" \
  "s_nop 0\n" \
  "v_cvt_pk_bf16_f32 v69, v70, v71\n" \
  "v_smfmac_f32_16x16x64_bf16 v[76:79], v[100:103], a[224:231], v248\n" \
  "v_smfmac_f32_16x16x64_bf16 v[80:83], v[100:103], v[0:7], v248\n" \
  "v_cvt_pk_bf16_f32 v71, v74, v75\n" \
  "v_smfmac_f32_16x16x64_bf16 v[84:87], v[100:103], v[32:39], v248\n" \
  "v_cvt_pk_bf16_f32 v70, v72, v73\n" \
  "v_smfmac_f32_16x16x64_bf16 v[76:79], v[92:95], a[232:239], v248\n" \
  "s_nop 1\n" \
  "v_permlane16_swap_b32 v69, v71\n" \
  "v_smfmac_f32_16x16x64_bf16 v[80:83], v[92:95], v[8:15], v248\n" \
  "s_nop 0\n" \
  "s_nop 0\n" \
  "v_permlane16_swap_b32 v68, v70\n" \
  "v_smfmac_f32_16x16x64_bf16 v[84:87], v[92:95], v[40:47], v248\n" \
  "buffer_store_dwordx4 v[68:71], v250, s[40:43], 0 offen sc1\n" \
  "v_cvt_pk_bf16_f32 v76, v76, v77\n" \
  "v_cvt_pk_bf16_f32 v77, v78, v79\n" \
  "s_nop 0\n" \
  "v_cvt_pk_bf16_f32 v78, v80, v81\n" \
  "v_cvt_pk_bf16_f32 v79, v82, v83\n" \
  "s_nop 0\n" \
  "v_permlane16_swap_b32 v76, v78\n" \
  "v_cvt_pk_bf16_f32 v84, v84, v85\n" \
  "v_cvt_pk_bf16_f32 v85, v86, v87\n" \
  "v_permlane16_swap_b32 v77, v79\n" \
  "buffer_store_dwordx4 v[76:79], v251, s[40:43], 0 offen sc1\n" \
  "buffer_store_dwordx2 v[84:85], v252, s[40:43], 0 offen sc1\n" \
  ".LV_loop_%=:\n" \
  "s_cmp_ge_u32 s48, 58\n" \
  "s_cbranch_scc1 .LV_cp_%=\n" \
  ".LV_cp_b_%=:\n" \
  "v_mov_b64 v[60:61], 0\n" \
  "v_mov_b64 v[62:63], 0\n" \
  "v_mov_b64 v[64:65], 0\n" \
  "v_mov_b64 v[66:67], 0\n" \
  "v_mov_b64 v[68:69], 0\n" \
  "v_mov_b64 v[70:71], 0\n" \
  "v_mov_b64 v[72:73], 0\n" \
  "v_mov_b64 v[74:75], 0\n" \
  "v_mov_b64 v[76:77], 0\n" \
  "v_mov_b64 v[78:79], 0\n" \
  "v_mov_b64 v[80:81], 0\n" \
  "v_mov_b64 v[82:83], 0\n" \
  "v_mov_b64 v[84:85], 0\n" \
  "v_mov_b64 v[86:87], 0\n" \
  "ds_read_b32 v92, v246\n" \
  "ds_read_b32 v93, v246 offset:256\n" \
  "v_mov_b64 v[94:95], 0\n" \
  "s_sub_u32 s60, s48, 1\n" \
  "s_cselect_b32 s60, 8192, s60\n" \
  "s_lshl_b32 s60, s60, 14\n" \
  "s_xor_b32 s60, s60, s50\n" \
  "buffer_store_dwordx4 v[88:91], v247, s[40:43], s60 offen sc1\n" \
  "s_waitcnt lgkmcnt(0)\n" \
  "buffer_store_dwordx4 v[92:95], v249, s[40:43], s60 offen sc1\n" \
  "s_add_u32 s48, s48, 1\n" \
  "v_mov_b32 v56, s48\n" \
  "s_waitcnt lgkmcnt(0)\n" \
  "s_cmp_gt_u32 s48, 57\n" \
  "s_cbranch_scc1 .LV_ef_%=\n" \
  ".LV_ef_b_%=:\n" \
  "s_branch .LV_rot_%=\n" \
  "s_nop 7\n" \
  ".LV_slow_%=:\n" \
  "s_bitcmp1_b32 s70, 0\n" \
  "s_cbranch_scc1 .LV_back_%=\n" \
  "s_waitcnt lgkmcnt(0)\n" \
  "s_cmp_lt_u32 s70, 10\n" \
  "s_nop 3\n" \
  "s_cbranch_scc1 .LV_nol_%=\n" \
  "v_max_u32 v217, v92, v93\n" \
  "v_max3_u32 v217, v217, v94, v95\n" \
  "v_max3_u32 v217, v217, v100, v101\n" \
  "v_max3_u32 v217, v217, v102, v103\n" \
  "v_max3_u32 v217, v217, v104, v105\n" \
  "v_max3_u32 v217, v217, v106, v107\n" \
  "v_max3_u32 v217, v217, v96, v97\n" \
  "v_max3_u32 v217, v217, v98, v99\n" \
  "v_max3_u32 v217, v217, v108, v109\n" \
  "v_max3_u32 v217, v217, v110, v111\n" \
  "v_max3_u32 v217, v217, v112, v113\n" \
  "v_max3_u32 v217, v217, v114, v115\n" \
  "v_max3_u32 v217, v217, v116, v117\n" \
  "v_max3_u32 v217, v217, v118, v119\n" \
  "v_max3_u32 v217, v217, v120, v121\n" \
  "v_max3_u32 v217, v217, v122, v123\n" \
  "v_max3_u32 v217, v217, v124, v125\n" \
  "v_max3_u32 v217, v217, v126, v127\n" \
  "v_cmp_eq_u32 vcc, -1, v217\n" \
  "s_cbranch_vccz .LV_back_%=\n" \
  ".LV_nol_%=:\n" \
  "s_add_u32 s70, s70, 2\n" \
  "s_cmp_gt_u32 s70, 65536\n" \
  "s_cbranch_scc1 .LV_dset_%=\n" \
  "s_barrier\n" \
  "v_mov_b64 v[60:61], 0\n" \
  "v_mov_b64 v[62:63], 0\n" \
  "v_mov_b64 v[64:65], 0\n" \
  "v_mov_b64 v[66:67], 0\n" \
  "v_mov_b64 v[68:69], 0\n" \
  "v_mov_b64 v[70:71], 0\n" \
  "v_mov_b64 v[72:73], 0\n" \
  "v_mov_b64 v[74:75], 0\n" \
  "v_mov_b64 v[76:77], 0\n" \
  "v_mov_b64 v[78:79], 0\n" \
  "v_mov_b64 v[80:81], 0\n" \
  "v_mov_b64 v[82:83], 0\n" \
  "v_mov_b64 v[84:85], 0\n" \
  "v_mov_b64 v[86:87], 0\n" \
  "s_branch .LV_rt_%=\n" \
  ".LV_dset_%=:\n" \
  "v_mov_b32 v112, 1\n" \
  "v_mov_b32 v113, s55\n" \
  "buffer_store_dwordx2 v[112:113], off, s[40:43], %46 offset:128 sc1\n" \
  "s_mov_b32 s70, 1\n" \
  "v_mov_b64 v[60:61], 0\n" \
  "v_mov_b64 v[62:63], 0\n" \
  "v_mov_b64 v[64:65], 0\n" \
  "v_mov_b64 v[66:67], 0\n" \
  "v_mov_b64 v[68:69], 0\n" \
  "v_mov_b64 v[70:71], 0\n" \
  "v_mov_b64 v[72:73], 0\n" \
  "v_mov_b64 v[74:75], 0\n" \
  "v_mov_b64 v[76:77], 0\n" \
  "v_mov_b64 v[78:79], 0\n" \
  "v_mov_b64 v[80:81], 0\n" \
  "v_mov_b64 v[82:83], 0\n" \
  "v_mov_b64 v[84:85], 0\n" \
  "v_mov_b64 v[86:87], 0\n" \
  "s_waitcnt vmcnt(0) lgkmcnt(0)\n" \
  "s_barrier\n" \
  "s_branch .LV_p_%=\n" \
  ".LV_cp_%=:\n" \
  "s_sub_u32 s60, s48, 58\n" \
  "s_lshl_b32 s60, s60, 20\n" \
  "s_add_u32 s60, s60, s47\n" \
  "s_lshr_b32 s62, s60, 8\n" \
  "s_and_b32 s62, s62, 8192\n" \
  "s_xor_b32 s62, s62, s60\n" \
  "buffer_store_dwordx4 v[68:71], v253, s[40:43], s62 offen sc1\n" \
  "buffer_store_dwordx4 v[76:79], v254, s[40:43], s62 offen sc1\n" \
  "buffer_store_dwordx2 v[84:85], v255, s[40:43], s62 offen sc1\n" \
  "s_nop 1\n" \
  "s_branch .LV_cp_b_%=\n" \
  ".LV_ef_%=:\n" \
  "s_cmp_eq_u32 s48, 64\n" \
  "s_cbranch_scc1 .LV_end_%=\n" \
  "s_waitcnt vmcnt(0)\n" \
  "s_barrier\n" \
  "buffer_store_dwordx2 v[56:57], v58, s[40:43], %43 offen sc1\n" \
  "s_branch .LV_ef_b_%=\n" \
  ".LV_end_%=:\n" \
  "s_waitcnt vmcnt(0)\n" \
  "s_waitcnt vmcnt(0)\n" \
  "s_nop 7\n" \
  "s_endpgm\n"
#define V_ASM_VOPS "+{v242}"(ci.sidx), "+{v243}"(ci.l16), "+{v244}"(ci.viw), "+{v245}"(ci.fva), "+{v246}"(ci.pk3), "+{v247}"(ci.fvs), "+{v248}"(ci.sid2), "+{v249}"(ci.uo), "+{v250}"(ci.po0), "+{v251}"(ci.po1), "+{v252}"(ci.po2), "+{v253}"(ci.co0), "+{v254}"(ci.co1), "+{v255}"(ci.co2), "+{v200}"(ci.vq0), "+{v201}"(ci.vq1), "+{v202}"(ci.vq2), "+{v203}"(ci.vq3), "+{v204}"(ci.vo0), "+{v205}"(ci.vo1), "+{v206}"(ci.vo2), "+{v207}"(ci.vo3), "+{v208}"(ci.v50), "+{v209}"(ci.v51), "+{v210}"(ci.v52), "+{v211}"(ci.v53), "+{v212}"(ci.tro0), "+{v213}"(ci.tro1), "+{v214}"(ci.m0o), "+{v215}"(ci.tro2), "+{v216}"(ci.tro3), "+{v217}"(ci.tro4)
#define V_ASM_SOPS "s"(ci.ws), "s"(ci.Wa), "s"(ci.Wb), "s"(ci.mem0), "s"(ci.w), "s"(ci.o_a), "s"(ci.o_b), "s"(ci.o_nx), "s"(ci.o_exp), "s"(ci.lab), "s"(ci.f_p), "s"(ci.f_m), "s"(ci.f_dur), "s"(ci.f_go), "s"(ci.o_ctl), "s"(ci.o_dbg), "s"(ci.dret), "s"(ci.o_qx), "s"(ci.f_s), "s"(ci.f_r)
#define V_ASM_CLOB "v0", "v1", "v2", "v3", "v4", "v5", "v6", "v7", "v8", "v9", "v10", "v11", "v12", "v13", "v14", "v15", "v16", "v17", "v18", "v19", "v20", "v21", "v22", "v23", "v24", "v25", "v26", "v27", "v28", "v29", "v30", "v31", "v32", "v33", "v34", "v35", "v36", "v37", "v38", "v39", "v40", "v41", "v42", "v43", "v44", "v45", "v46", "v47", "v48", "v49", "v50", "v51", "v52", "v53", "v54", "v55", "v56", "v57", "v58", "v59", "v60", "v61", "v62", "v63", "v64", "v65", "v66", "v67", "v68", "v69", "v70", "v71", "v72", "v73", "v74", "v75", "v76", "v77", "v78", "v79", "v80", "v81", "v82", "v83", "v84", "v85", "v86", "v87", "v88", "v89", "v90", "v91", "v92", "v93", "v94", "v95", "v96", "v97", "v98", "v99", "v100", "v101", "v102", "v103", "v104", "v105", "v106", "v107", "v108", "v109", "v110", "v111", "v112", "v113", "v114", "v115", "v116", "v117", "v118", "v119", "v120", "v121", "v122", "v123", "v124", "v125", "v126", "v127", "v128", "v129", "v130", "v131", "v132", "v133", "v134", "v135", "v136", "v137", "v138", "v139", "v140", "v141", "v142", "v143", "v144", "v145", "v146", "v147", "v148", "v149", "v150", "v151", "v152", "v153", "v154", "v155", "v156", "v157", "v158", "v159", "v160", "v161", "v162", "v163", "v164", "v165", "v166", "v167", "v168", "v169", "v170", "v171", "v172", "v173", "v174", "v175", "v176", "v177", "v178", "v179", "v180", "v181", "v182", "v183", "v184", "v185", "v186", "v187", "v188", "v189", "v190", "v191", "v192", "v193", "v194", "v195", "v196", "v197", "v198", "v199", "a0", "a1", "a2", "a3", "a4", "a5", "a6", "a7", "a8", "a9", "a10", "a11", "a12", "a13", "a14", "a15", "a16", "a17", "a18", "a19", "a20", "a21", "a22", "a23", "a24", "a25", "a26", "a27", "a28", "a29", "a30", "a31", "a32", "a33", "a34", "a35", "a36", "a37", "a38", "a39", "a40", "a41", "a42", "a43", "a44", "a45", "a46", "a47", "a48", "a49", "a50", "a51", "a52", "a53", "a54", "a55", "a56", "a57", "a58", "a59", "a60", "a61", "a62", "a63", "a64", "a65", "a66", "a67", "a68", "a69", "a70", "a71", "a72", "a73", "a74", "a75", "a76", "a77", "a78", "a79", "a80", "a81", "a82", "a83", "a84", "a85", "a86", "a87", "a88", "a89", "a90", "a91", "a92", "a93", "a94", "a95", "a96", "a97", "a98", "a99", "a100", "a101", "a102", "a103", "a104", "a105", "a106", "a107", "a108", "a109", "a110", "a111", "a112", "a113", "a114", "a115", "a116", "a117", "a118", "a119", "a120", "a121", "a122", "a123", "a124", "a125", "a126", "a127", "a128", "a129", "a130", "a131", "a132", "a133", "a134", "a135", "a136", "a137", "a138", "a139", "a140", "a141", "a142", "a143", "a144", "a145", "a146", "a147", "a148", "a149", "a150", "a151", "a152", "a153", "a154", "a155", "a156", "a157", "a158", "a159", "a160", "a161", "a162", "a163", "a164", "a165", "a166", "a167", "a168", "a169", "a170", "a171", "a172", "a173", "a174", "a175", "a176", "a177", "a178", "a179", "a180", "a181", "a182", "a183", "a184", "a185", "a186", "a187", "a188", "a189", "a190", "a191", "a192", "a193", "a194", "a195", "a196", "a197", "a198", "a199", "a200", "a201", "a202", "a203", "a204", "a205", "a206", "a207", "a208", "a209", "a210", "a211", "a212", "a213", "a214", "a215", "a216", "a217", "a218", "a219", "a220", "a221", "a222", "a223", "a224", "a225", "a226", "a227", "a228", "a229", "a230", "a231", "a232", "a233", "a234", "a235", "a236", "a237", "a238", "a239", "a240", "a241", "a242", "a243", "a244", "a245", "a246", "a247", "a248", "a249", "a250", "a251", "a252", "a253", "a254", "a255", "s40", "s41", "s42", "s43", "s44", "s45", "s46", "s47", "s48", "s49", "s50", "s51", "s52", "s53", "s54", "s55", "s56", "s57", "s58", "s59", "s60", "s61", "s62", "s63", "s64", "s65", "s66", "s67", "s68", "s69", "s70", "s71", "s72", "s73", "s74", "s75", "s76", "s77", "s78", "s79", "s80", "s81", "s82", "s83", "s84", "s85", "s86", "s87", "s88", "s89", "s90", "s91", "s92", "s93", "s94", "s95", "s96", "s97", "s98", "s99", "vcc", "scc", "memory"
#define W_ASM_TEXT \
  "s_cmp_gt_u32 %36, 0\n" \
  "s_cbranch_scc1 .LW_other_%=\n" \
  "s_mov_b64 s[40:41], %32\n" \
  "s_and_b32 s41, s41, 0xffff\n" \
  "s_mov_b32 s42, 255852544\n" \
  "s_mov_b32 s43, 0x00020000\n" \
  "s_mov_b32 s46, 4718592\n" \
  "s_mov_b32 s47, 0x00020000\n" \
  "s_lshl_b32 s72, %36, 11\n" \
  "buffer_load_dword v196, off, s[40:43], %46 sc1\n" \
  "s_mov_b64 s[44:45], %34\n" \
  "s_and_b32 s45, s45, 0xffff\n" \
  "s_add_u32 s60, %38, 0\n" \
  "s_lshl_b32 s61, s72, 1\n" \
  "s_add_u32 m0, s61, 16384\n" \
  "s_nop 0\n" \
  "buffer_load_dwordx4 v204, s[44:47], s60 offen lds\n" \
  "buffer_load_dwordx4 v205, s[44:47], s60 offen offset:1024 lds\n" \
  "buffer_load_dwordx4 v206, s[44:47], s60 offen offset:2048 lds\n" \
  "buffer_load_dwordx4 v207, s[44:47], s60 offen offset:3072 lds\n" \
  "s_add_u32 s60, %38, 18432\n" \
  "s_lshl_b32 s61, s72, 1\n" \
  "s_add_u32 m0, s61, 32768\n" \
  "s_nop 0\n" \
  "buffer_load_dwordx4 v204, s[44:47], s60 offen lds\n" \
  "buffer_load_dwordx4 v205, s[44:47], s60 offen offset:1024 lds\n" \
  "buffer_load_dwordx4 v206, s[44:47], s60 offen offset:2048 lds\n" \
  "buffer_load_dwordx4 v207, s[44:47], s60 offen offset:3072 lds\n" \
  "s_add_u32 s60, %38, 256\n" \
  "s_lshl_b32 s61, s72, 1\n" \
  "s_add_u32 m0, s61, 49152\n" \
  "s_nop 0\n" \
  "buffer_load_dwordx4 v204, s[44:47], s60 offen lds\n" \
  "buffer_load_dwordx4 v205, s[44:47], s60 offen offset:1024 lds\n" \
  "buffer_load_dwordx4 v206, s[44:47], s60 offen offset:2048 lds\n" \
  "buffer_load_dwordx4 v207, s[44:47], s60 offen offset:3072 lds\n" \
  "s_add_u32 s60, %38, 18688\n" \
  "s_lshl_b32 s61, s72, 1\n" \
  "s_add_u32 m0, s61, 65536\n" \
  "s_nop 0\n" \
  "buffer_load_dwordx4 v204, s[44:47], s60 offen lds\n" \
  "buffer_load_dwordx4 v205, s[44:47], s60 offen offset:1024 lds\n" \
  "buffer_load_dwordx4 v206, s[44:47], s60 offen offset:2048 lds\n" \
  "buffer_load_dwordx4 v207, s[44:47], s60 offen offset:3072 lds\n" \
  "s_add_u32 s60, %38, 512\n" \
  "s_lshl_b32 s61, s72, 1\n" \
  "s_add_u32 m0, s61, 81920\n" \
  "s_nop 0\n" \
  "buffer_load_dwordx4 v204, s[44:47], s60 offen lds\n" \
  "buffer_load_dwordx4 v205, s[44:47], s60 offen offset:1024 lds\n" \
  "buffer_load_dwordx4 v206, s[44:47], s60 offen offset:2048 lds\n" \
  "buffer_load_dwordx4 v207, s[44:47], s60 offen offset:3072 lds\n" \
  "s_add_u32 s60, %38, 18944\n" \
  "s_lshl_b32 s61, s72, 1\n" \
  "s_add_u32 m0, s61, 98304\n" \
  "s_nop 0\n" \
  "buffer_load_dwordx4 v204, s[44:47], s60 offen lds\n" \
  "buffer_load_dwordx4 v205, s[44:47], s60 offen offset:1024 lds\n" \
  "buffer_load_dwordx4 v206, s[44:47], s60 offen offset:2048 lds\n" \
  "buffer_load_dwordx4 v207, s[44:47], s60 offen offset:3072 lds\n" \
  "s_add_u32 s60, %38, 768\n" \
  "s_lshl_b32 s61, s72, 1\n" \
  "s_add_u32 m0, s61, 114688\n" \
  "s_nop 0\n" \
  "buffer_load_dwordx4 v204, s[44:47], s60 offen lds\n" \
  "buffer_load_dwordx4 v205, s[44:47], s60 offen offset:1024 lds\n" \
  "buffer_load_dwordx4 v206, s[44:47], s60 offen offset:2048 lds\n" \
  "buffer_load_dwordx4 v207, s[44:47], s60 offen offset:3072 lds\n" \
  "s_add_u32 s60, %38, 19200\n" \
  "s_lshl_b32 s61, s72, 1\n" \
  "s_add_u32 m0, s61, 131072\n" \
  "s_nop 0\n" \
  "buffer_load_dwordx4 v204, s[44:47], s60 offen lds\n" \
  "buffer_load_dwordx4 v205, s[44:47], s60 offen offset:1024 lds\n" \
  "buffer_load_dwordx4 v206, s[44:47], s60 offen offset:2048 lds\n" \
  "buffer_load_dwordx4 v207, s[44:47], s60 offen offset:3072 lds\n" \
  "s_mov_b32 s60, %38\n" \
  "s_lshl_b32 s61, s72, 1\n" \
  "s_add_u32 m0, s61, 147456\n" \
  "s_nop 0\n" \
  "buffer_load_dwordx4 v208, s[44:47], s60 offen lds\n" \
  "buffer_load_dwordx4 v209, s[44:47], s60 offen offset:1024 lds\n" \
  "buffer_load_dwordx4 v210, s[44:47], s60 offen offset:2048 lds\n" \
  "buffer_load_dwordx4 v211, s[44:47], s60 offen offset:3072 lds\n" \
  "s_mov_b64 s[44:45], %35\n" \
  "s_and_b32 s45, s45, 0xffff\n" \
  "s_lshr_b32 s60, s72, 3\n" \
  "s_mov_b32 m0, s72\n" \
  "s_nop 0\n" \
  "buffer_load_dwordx4 v214, s[44:47], s60 offen lds\n" \
  "s_add_u32 s60, s60, 128\n" \
  "s_add_u32 m0, s72, 1024\n" \
  "s_nop 0\n" \
  "buffer_load_dwordx4 v214, s[44:47], s60 offen lds\n" \
  "s_movk_i32 s60, 1024\n" \
  "s_movk_i32 m0, 8192\n" \
  "s_nop 0\n" \
  "buffer_load_dwordx4 v214, s[44:47], s60 offen lds\n" \
  "s_waitcnt vmcnt(27)\n" \
  "s_barrier\n" \
  "v_add_u32 v194, 16384, v215\n" \
  "v_xor_b32 v195, 2176, v194\n" \
  "ds_read_b64_tr_b16 a[144:145], v194\n" \
  "ds_read_b64_tr_b16 a[146:147], v194 offset:512\n" \
  "ds_read_b64_tr_b16 a[148:149], v195\n" \
  "ds_read_b64_tr_b16 a[150:151], v195 offset:512\n" \
  "ds_read_b64_tr_b16 a[152:153], v194 offset:8192\n" \
  "ds_read_b64_tr_b16 a[154:155], v194 offset:8704\n" \
  "ds_read_b64_tr_b16 a[156:157], v195 offset:8192\n" \
  "ds_read_b64_tr_b16 a[158:159], v195 offset:8704\n" \
  "v_add_u32 v194, 32768, v215\n" \
  "v_xor_b32 v195, 2176, v194\n" \
  "ds_read_b64_tr_b16 a[160:161], v194\n" \
  "ds_read_b64_tr_b16 a[162:163], v194 offset:512\n" \
  "ds_read_b64_tr_b16 a[164:165], v195\n" \
  "ds_read_b64_tr_b16 a[166:167], v195 offset:512\n" \
  "ds_read_b64_tr_b16 a[168:169], v194 offset:8192\n" \
  "ds_read_b64_tr_b16 a[170:171], v194 offset:8704\n" \
  "ds_read_b64_tr_b16 a[172:173], v195 offset:8192\n" \
  "ds_read_b64_tr_b16 a[174:175], v195 offset:8704\n" \
  "v_add_u32 v194, 49152, v215\n" \
  "v_xor_b32 v195, 2176, v194\n" \
  "ds_read_b64_tr_b16 a[176:177], v194\n" \
  "ds_read_b64_tr_b16 a[178:179], v194 offset:512\n" \
  "ds_read_b64_tr_b16 a[180:181], v195\n" \
  "ds_read_b64_tr_b16 a[182:183], v195 offset:512\n" \
  "ds_read_b64_tr_b16 a[184:185], v194 offset:8192\n" \
  "ds_read_b64_tr_b16 a[186:187], v194 offset:8704\n" \
  "ds_read_b64_tr_b16 a[188:189], v195 offset:8192\n" \
  "ds_read_b64_tr_b16 a[190:191], v195 offset:8704\n" \
  "s_waitcnt lgkmcnt(0)\n" \
  "s_barrier\n" \
  "s_mov_b64 s[44:45], %33\n" \
  "s_and_b32 s45, s45, 0xffff\n" \
  "s_add_u32 s60, %37, 0\n" \
  "s_lshl_b32 s61, s72, 1\n" \
  "s_add_u32 m0, s61, 16384\n" \
  "s_nop 0\n" \
  "buffer_load_dwordx4 v200, s[44:47], s60 offen lds\n" \
  "buffer_load_dwordx4 v201, s[44:47], s60 offen offset:1024 lds\n" \
  "buffer_load_dwordx4 v202, s[44:47], s60 offen offset:2048 lds\n" \
  "buffer_load_dwordx4 v203, s[44:47], s60 offen offset:3072 lds\n" \
  "s_add_u32 s60, %37, 524288\n" \
  "s_lshl_b32 s61, s72, 1\n" \
  "s_add_u32 m0, s61, 32768\n" \
  "s_nop 0\n" \
  "buffer_load_dwordx4 v200, s[44:47], s60 offen lds\n" \
  "buffer_load_dwordx4 v201, s[44:47], s60 offen offset:1024 lds\n" \
  "buffer_load_dwordx4 v202, s[44:47], s60 offen offset:2048 lds\n" \
  "buffer_load_dwordx4 v203, s[44:47], s60 offen offset:3072 lds\n" \
  "s_add_u32 s60, %37, 1048576\n" \
  "s_lshl_b32 s61, s72, 1\n" \
  "s_add_u32 m0, s61, 49152\n" \
  "s_nop 0\n" \
  "buffer_load_dwordx4 v200, s[44:47], s60 offen lds\n" \
  "buffer_load_dwordx4 v201, s[44:47], s60 offen offset:1024 lds\n" \
  "buffer_load_dwordx4 v202, s[44:47], s60 offen offset:2048 lds\n" \
  "buffer_load_dwordx4 v203, s[44:47], s60 offen offset:3072 lds\n" \
  "v_readfirstlane_b32 s55, v196\n" \
  "s_add_u32 s55, s55, 1\n" \
  "s_waitcnt vmcnt(27)\n" \
  "s_barrier\n" \
  "v_add_u32 v194, 65536, v215\n" \
  "v_xor_b32 v195, 2176, v194\n" \
  "ds_read_b64_tr_b16 a[192:193], v194\n" \
  "ds_read_b64_tr_b16 a[194:195], v194 offset:512\n" \
  "ds_read_b64_tr_b16 a[196:197], v195\n" \
  "ds_read_b64_tr_b16 a[198:199], v195 offset:512\n" \
  "ds_read_b64_tr_b16 a[200:201], v194 offset:8192\n" \
  "ds_read_b64_tr_b16 a[202:203], v194 offset:8704\n" \
  "ds_read_b64_tr_b16 a[204:205], v195 offset:8192\n" \
  "ds_read_b64_tr_b16 a[206:207], v195 offset:8704\n" \
  "v_add_u32 v194, 81920, v215\n" \
  "v_xor_b32 v195, 2176, v194\n" \
  "ds_read_b64_tr_b16 a[208:209], v194\n" \
  "ds_read_b64_tr_b16 a[210:211], v194 offset:512\n" \
  "ds_read_b64_tr_b16 a[212:213], v195\n" \
  "ds_read_b64_tr_b16 a[214:215], v195 offset:512\n" \
  "ds_read_b64_tr_b16 a[216:217], v194 offset:8192\n" \
  "ds_read_b64_tr_b16 a[218:219], v194 offset:8704\n" \
  "ds_read_b64_tr_b16 a[220:221], v195 offset:8192\n" \
  "ds_read_b64_tr_b16 a[222:223], v195 offset:8704\n" \
  "v_add_u32 v194, 98304, v215\n" \
  "v_xor_b32 v195, 2176, v194\n" \
  "ds_read_b64_tr_b16 a[224:225], v194\n" \
  "ds_read_b64_tr_b16 a[226:227], v194 offset:512\n" \
  "ds_read_b64_tr_b16 a[228:229], v195\n" \
  "ds_read_b64_tr_b16 a[230:231], v195 offset:512\n" \
  "ds_read_b64_tr_b16 a[232:233], v194 offset:8192\n" \
  "ds_read_b64_tr_b16 a[234:235], v194 offset:8704\n" \
  "ds_read_b64_tr_b16 a[236:237], v195 offset:8192\n" \
  "ds_read_b64_tr_b16 a[238:239], v195 offset:8704\n" \
  "s_waitcnt lgkmcnt(0)\n" \
  "s_barrier\n" \
  "s_add_u32 s60, %37, 1572864\n" \
  "s_lshl_b32 s61, s72, 1\n" \
  "s_add_u32 m0, s61, 65536\n" \
  "s_nop 0\n" \
  "buffer_load_dwordx4 v200, s[44:47], s60 offen lds\n" \
  "buffer_load_dwordx4 v201, s[44:47], s60 offen offset:1024 lds\n" \
  "buffer_load_dwordx4 v202, s[44:47], s60 offen offset:2048 lds\n" \
  "buffer_load_dwordx4 v203, s[44:47], s60 offen offset:3072 lds\n" \
  "s_add_u32 s60, %37, 2097152\n" \
  "s_lshl_b32 s61, s72, 1\n" \
  "s_add_u32 m0, s61, 81920\n" \
  "s_nop 0\n" \
  "buffer_load_dwordx4 v200, s[44:47], s60 offen lds\n" \
  "buffer_load_dwordx4 v201, s[44:47], s60 offen offset:1024 lds\n" \
  "buffer_load_dwordx4 v202, s[44:47], s60 offen offset:2048 lds\n" \
  "buffer_load_dwordx4 v203, s[44:47], s60 offen offset:3072 lds\n" \
  "s_add_u32 s60, %37, 2621440\n" \
  "s_lshl_b32 s61, s72, 1\n" \
  "s_add_u32 m0, s61, 98304\n" \
  "s_nop 0\n" \
  "buffer_load_dwordx4 v200, s[44:47], s60 offen lds\n" \
  "buffer_load_dwordx4 v201, s[44:47], s60 offen offset:1024 lds\n" \
  "buffer_load_dwordx4 v202, s[44:47], s60 offen offset:2048 lds\n" \
  "buffer_load_dwordx4 v203, s[44:47], s60 offen offset:3072 lds\n" \
  "s_waitcnt vmcnt(27)\n" \
  "s_barrier\n" \
  "s_waitcnt lgkmcnt(0)\n" \
  "s_barrier\n" \
  "s_add_u32 s60, %37, 3145728\n" \
  "s_lshl_b32 s61, s72, 1\n" \
  "s_add_u32 m0, s61, 114688\n" \
  "s_nop 0\n" \
  "buffer_load_dwordx4 v200, s[44:47], s60 offen lds\n" \
  "buffer_load_dwordx4 v201, s[44:47], s60 offen offset:1024 lds\n" \
  "buffer_load_dwordx4 v202, s[44:47], s60 offen offset:2048 lds\n" \
  "buffer_load_dwordx4 v203, s[44:47], s60 offen offset:3072 lds\n" \
  "s_add_u32 s60, %37, 3670016\n" \
  "s_lshl_b32 s61, s72, 1\n" \
  "s_add_u32 m0, s61, 131072\n" \
  "s_nop 0\n" \
  "buffer_load_dwordx4 v200, s[44:47], s60 offen lds\n" \
  "buffer_load_dwordx4 v201, s[44:47], s60 offen offset:1024 lds\n" \
  "buffer_load_dwordx4 v202, s[44:47], s60 offen offset:2048 lds\n" \
  "buffer_load_dwordx4 v203, s[44:47], s60 offen offset:3072 lds\n" \
  "s_add_u32 s60, %37, 4194304\n" \
  "s_lshl_b32 s61, s72, 1\n" \
  "s_add_u32 m0, s61, 147456\n" \
  "s_nop 0\n" \
  "buffer_load_dwordx4 v200, s[44:47], s60 offen lds\n" \
  "buffer_load_dwordx4 v201, s[44:47], s60 offen offset:1024 lds\n" \
  "buffer_load_dwordx4 v202, s[44:47], s60 offen offset:2048 lds\n" \
  "buffer_load_dwordx4 v203, s[44:47], s60 offen offset:3072 lds\n" \
  "buffer_load_dwordx2 v[198:199], off, s[40:43], %45 sc1\n" \
  "s_waitcnt vmcnt(25)\n" \
  "s_barrier\n" \
  "v_add_u32 v194, 16384, v212\n" \
  "ds_read_b64_tr_b16 a[0:1], v194\n" \
  "ds_read_b64_tr_b16 a[2:3], v194 offset:512\n" \
  "ds_read_b64_tr_b16 a[4:5], v194 offset:8192\n" \
  "ds_read_b64_tr_b16 a[6:7], v194 offset:8704\n" \
  "v_add_u32 v195, 16384, v213\n" \
  "ds_read_b64_tr_b16 a[72:73], v195\n" \
  "ds_read_b64_tr_b16 a[74:75], v195 offset:512\n" \
  "ds_read_b64_tr_b16 a[76:77], v195 offset:8192\n" \
  "ds_read_b64_tr_b16 a[78:79], v195 offset:8704\n" \
  "v_add_u32 v194, 32768, v212\n" \
  "ds_read_b64_tr_b16 a[8:9], v194\n" \
  "ds_read_b64_tr_b16 a[10:11], v194 offset:512\n" \
  "ds_read_b64_tr_b16 a[12:13], v194 offset:8192\n" \
  "ds_read_b64_tr_b16 a[14:15], v194 offset:8704\n" \
  "v_add_u32 v195, 32768, v213\n" \
  "ds_read_b64_tr_b16 a[80:81], v195\n" \
  "ds_read_b64_tr_b16 a[82:83], v195 offset:512\n" \
  "ds_read_b64_tr_b16 a[84:85], v195 offset:8192\n" \
  "ds_read_b64_tr_b16 a[86:87], v195 offset:8704\n" \
  "v_add_u32 v194, 49152, v212\n" \
  "ds_read_b64_tr_b16 a[16:17], v194\n" \
  "ds_read_b64_tr_b16 a[18:19], v194 offset:512\n" \
  "ds_read_b64_tr_b16 a[20:21], v194 offset:8192\n" \
  "ds_read_b64_tr_b16 a[22:23], v194 offset:8704\n" \
  "v_add_u32 v195, 49152, v213\n" \
  "ds_read_b64_tr_b16 a[88:89], v195\n" \
  "ds_read_b64_tr_b16 a[90:91], v195 offset:512\n" \
  "ds_read_b64_tr_b16 a[92:93], v195 offset:8192\n" \
  "ds_read_b64_tr_b16 a[94:95], v195 offset:8704\n" \
  "s_waitcnt lgkmcnt(0)\n" \
  "s_waitcnt vmcnt(13)\n" \
  "s_barrier\n" \
  "v_add_u32 v194, 65536, v212\n" \
  "ds_read_b64_tr_b16 a[24:25], v194\n" \
  "ds_read_b64_tr_b16 a[26:27], v194 offset:512\n" \
  "ds_read_b64_tr_b16 a[28:29], v194 offset:8192\n" \
  "ds_read_b64_tr_b16 a[30:31], v194 offset:8704\n" \
  "v_add_u32 v195, 65536, v213\n" \
  "ds_read_b64_tr_b16 a[96:97], v195\n" \
  "ds_read_b64_tr_b16 a[98:99], v195 offset:512\n" \
  "ds_read_b64_tr_b16 a[100:101], v195 offset:8192\n" \
  "ds_read_b64_tr_b16 a[102:103], v195 offset:8704\n" \
  "v_add_u32 v194, 81920, v212\n" \
  "ds_read_b64_tr_b16 a[32:33], v194\n" \
  "ds_read_b64_tr_b16 a[34:35], v194 offset:512\n" \
  "ds_read_b64_tr_b16 a[36:37], v194 offset:8192\n" \
  "ds_read_b64_tr_b16 a[38:39], v194 offset:8704\n" \
  "v_add_u32 v195, 81920, v213\n" \
  "ds_read_b64_tr_b16 a[104:105], v195\n" \
  "ds_read_b64_tr_b16 a[106:107], v195 offset:512\n" \
  "ds_read_b64_tr_b16 a[108:109], v195 offset:8192\n" \
  "ds_read_b64_tr_b16 a[110:111], v195 offset:8704\n" \
  "v_add_u32 v194, 98304, v212\n" \
  "ds_read_b64_tr_b16 a[40:41], v194\n" \
  "ds_read_b64_tr_b16 a[42:43], v194 offset:512\n" \
  "ds_read_b64_tr_b16 a[44:45], v194 offset:8192\n" \
  "ds_read_b64_tr_b16 a[46:47], v194 offset:8704\n" \
  "v_add_u32 v195, 98304, v213\n" \
  "ds_read_b64_tr_b16 a[112:113], v195\n" \
  "ds_read_b64_tr_b16 a[114:115], v195 offset:512\n" \
  "ds_read_b64_tr_b16 a[116:117], v195 offset:8192\n" \
  "ds_read_b64_tr_b16 a[118:119], v195 offset:8704\n" \
  "s_waitcnt lgkmcnt(0)\n" \
  "s_waitcnt vmcnt(1)\n" \
  "s_barrier\n" \
  "v_add_u32 v194, 114688, v212\n" \
  "ds_read_b64_tr_b16 a[48:49], v194\n" \
  "ds_read_b64_tr_b16 a[50:51], v194 offset:512\n" \
  "ds_read_b64_tr_b16 a[52:53], v194 offset:8192\n" \
  "ds_read_b64_tr_b16 a[54:55], v194 offset:8704\n" \
  "v_add_u32 v195, 114688, v213\n" \
  "ds_read_b64_tr_b16 a[120:121], v195\n" \
  "ds_read_b64_tr_b16 a[122:123], v195 offset:512\n" \
  "ds_read_b64_tr_b16 a[124:125], v195 offset:8192\n" \
  "ds_read_b64_tr_b16 a[126:127], v195 offset:8704\n" \
  "v_add_u32 v194, 131072, v212\n" \
  "ds_read_b64_tr_b16 a[56:57], v194\n" \
  "ds_read_b64_tr_b16 a[58:59], v194 offset:512\n" \
  "ds_read_b64_tr_b16 a[60:61], v194 offset:8192\n" \
  "ds_read_b64_tr_b16 a[62:63], v194 offset:8704\n" \
  "v_add_u32 v195, 131072, v213\n" \
  "ds_read_b64_tr_b16 a[128:129], v195\n" \
  "ds_read_b64_tr_b16 a[130:131], v195 offset:512\n" \
  "ds_read_b64_tr_b16 a[132:133], v195 offset:8192\n" \
  "ds_read_b64_tr_b16 a[134:135], v195 offset:8704\n" \
  "v_add_u32 v194, 147456, v212\n" \
  "ds_read_b64_tr_b16 a[64:65], v194\n" \
  "ds_read_b64_tr_b16 a[66:67], v194 offset:512\n" \
  "ds_read_b64_tr_b16 a[68:69], v194 offset:8192\n" \
  "ds_read_b64_tr_b16 a[70:71], v194 offset:8704\n" \
  "v_add_u32 v195, 147456, v213\n" \
  "ds_read_b64_tr_b16 a[136:137], v195\n" \
  "ds_read_b64_tr_b16 a[138:139], v195 offset:512\n" \
  "ds_read_b64_tr_b16 a[140:141], v195 offset:8192\n" \
  "ds_read_b64_tr_b16 a[142:143], v195 offset:8704\n" \
  "s_waitcnt lgkmcnt(0)\n" \
  "s_waitcnt vmcnt(0)\n" \
  "v_readfirstlane_b32 s96, v198\n" \
  "v_readfirstlane_b32 s63, v199\n" \
  "s_cmp_eq_u32 s63, s55\n" \
  "s_cselect_b32 s96, s96, 0\n" \
  "s_cmp_ge_u32 s96, 0xc0000000\n" \
  "s_cbranch_scc1 .LW_go_x_%=\n" \
  "s_mov_b32 s69, 0\n" \
  ".LW_go_%=:\n" \
  "buffer_load_dwordx2 v[196:197], off, s[40:43], %45 sc1\n" \
  "s_waitcnt vmcnt(0)\n" \
  "v_readfirstlane_b32 s96, v196\n" \
  "v_readfirstlane_b32 s63, v197\n" \
  "s_cmp_eq_u32 s63, s55\n" \
  "s_cselect_b32 s96, s96, 0\n" \
  "s_cmp_ge_u32 s96, 0xc0000000\n" \
  "s_cbranch_scc1 .LW_go_x_%=\n" \
  "s_sleep 8\n" \
  "s_add_u32 s69, s69, 1\n" \
  "s_and_b32 s63, s69, 1023\n" \
  "s_cbranch_scc1 .LW_go_%=\n" \
  "s_cmp_gt_u32 s69, 2097152\n" \
  "s_cbranch_scc1 .LW_go_set_%=\n" \
  "buffer_load_dwordx2 v[196:197], off, s[40:43], %46 offset:128 sc1\n" \
  "s_waitcnt vmcnt(0)\n" \
  "v_readfirstlane_b32 s63, v197\n" \
  "s_cmp_eq_u32 s63, s55\n" \
  "s_cbranch_scc0 .LW_go_%=\n" \
  "s_branch .LW_go_x_%=\n" \
  ".LW_go_set_%=:\n" \
  "v_mov_b32 v196, 1\n" \
  "v_mov_b32 v197, s55\n" \
  "buffer_store_dwordx2 v[196:197], off, s[40:43], %46 offset:128 sc1\n" \
  "s_waitcnt vmcnt(0)\n" \
  ".LW_go_x_%=:\n" \
  "s_and_b32 s60, s96, 2\n" \
  "s_lshl_b32 s60, s60, 12\n" \
  "s_xor_b32 s74, %42, s60\n" \
  "s_xor_b32 s75, %44, s60\n" \
  "s_bfe_u32 s56, s96, 0x30010\n" \
  "s_lshl_b32 s56, s56, 12\n" \
  "s_or_b32 s56, s56, 0x200000\n" \
  "s_mov_b32 s57, 1052672\n" \
  "s_lshl_b32 s61, %41, 2\n" \
  "s_lshr_b32 s51, s96, s61\n" \
  "s_and_b32 s51, s51, 7\n" \
  "s_lshl_b32 s51, s51, 12\n" \
  "s_xor_b32 s51, s51, 4210688\n" \
  "s_mov_b32 s71, 0\n" \
  "s_add_u32 s45, s51, 886784\n" \
  "s_mov_b32 s46, -1\n" \
  "v_mov_b64 v[48:49], -1\n" \
  "v_mov_b64 v[50:51], -1\n" \
  "v_and_b32 v194, 3, v250\n" \
  "v_lshlrev_b32 v194, 2, v194\n" \
  "v_lshrrev_b32 v194, v194, s96\n" \
  "v_and_b32 v194, 7, v194\n" \
  "v_lshlrev_b32 v194, 12, v194\n" \
  "v_and_b32 v250, -4, v250\n" \
  "v_xor_b32 v250, v250, v194\n" \
  "v_and_b32 v194, 3, v252\n" \
  "v_lshlrev_b32 v194, 2, v194\n" \
  "v_lshrrev_b32 v194, v194, s96\n" \
  "v_and_b32 v194, 7, v194\n" \
  "v_lshlrev_b32 v194, 12, v194\n" \
  "v_and_b32 v252, -4, v252\n" \
  "v_xor_b32 v252, v252, v194\n" \
  "v_and_b32 v194, 3, v251\n" \
  "v_lshlrev_b32 v194, 2, v194\n" \
  "v_lshrrev_b32 v194, v194, s96\n" \
  "v_and_b32 v194, 7, v194\n" \
  "v_lshlrev_b32 v194, 12, v194\n" \
  "v_and_b32 v251, -4, v251\n" \
  "v_xor_b32 v251, v251, v194\n" \
  "v_xor_b32 v185, s56, v250\n" \
  "buffer_store_dwordx4 v[48:51], v185, s[40:43], 0 offen sc1\n" \
  "v_xor_b32 v186, s56, v252\n" \
  "buffer_store_dwordx2 v[48:49], v186, s[40:43], 0 offen sc1\n" \
  "s_xor_b32 s62, s56, s57\n" \
  "s_nop 1\n" \
  "v_xor_b32 v185, s62, v250\n" \
  "buffer_store_dwordx4 v[48:51], v185, s[40:43], 0 offen sc1\n" \
  "v_xor_b32 v186, s62, v252\n" \
  "buffer_store_dwordx2 v[48:49], v186, s[40:43], 0 offen sc1\n" \
  "s_andn2_b32 s60, 2, s96\n" \
  "s_lshl_b32 s60, s60, 12\n" \
  "s_xor_b32 s60, s60, 8192\n" \
  "s_xor_b32 s58, %39, s60\n" \
  "s_mov_b32 s49, 0\n" \
  "s_add_u32 s47, %49, s60\n" \
  "s_add_u32 s97, s60, 8388608\n" \
  "s_xor_b32 s50, %40, s60\n" \
  "s_mov_b32 s48, 0\n" \
  "s_mov_b32 s54, 0\n" \
  "s_cmp_eq_u32 %36, 0\n" \
  "s_cselect_b64 s[88:89], -1, 0\n" \
  "s_mov_b32 s68, 0\n" \
  "s_mov_b32 s70, 1\n" \
  "s_mov_b32 s73, 0\n" \
  "s_mov_b32 s44, 0\n" \
  "s_mov_b32 s53, 0\n" \
  "s_memrealtime s[86:87]\n" \
  "s_memtime s[76:77]\n" \
  "s_waitcnt lgkmcnt(0)\n" \
  "s_mov_b32 s90, s76\n" \
  "s_mov_b32 s87, 340\n" \
  "s_mov_b32 s64, 0x3e0293ee\n" \
  "s_mov_b32 s65, 0xbe0293ee\n" \
  "v_mov_b64 v[60:61], 0\n" \
  "v_mov_b64 v[62:63], 0\n" \
  "v_mov_b64 v[64:65], 0\n" \
  "v_mov_b64 v[66:67], 0\n" \
  "v_mov_b64 v[68:69], 0\n" \
  "v_mov_b64 v[70:71], 0\n" \
  "v_mov_b64 v[72:73], 0\n" \
  "v_mov_b64 v[74:75], 0\n" \
  "v_mov_b64 v[76:77], 0\n" \
  "v_mov_b64 v[78:79], 0\n" \
  "v_mov_b32 v56, 0\n" \
  "v_mov_b32 v57, s55\n" \
  "v_min_u32 v58, 1, v243\n" \
  "v_lshlrev_b32 v58, 30, v58\n" \
  "v_mov_b64 v[82:83], 0\n" \
  "s_mov_b32 s67, 2130706432\n" \
  "s_waitcnt vmcnt(0) lgkmcnt(0)\n" \
  "s_barrier\n" \
  "s_branch .LW_p_%=\n" \
  "s_nop 7\n" \
  ".p2align 6\n" \
  ".fill (((40 - (.LW_p_%= - .LW_rot_%=)) & 63) >> 2), 4, 0xBF800000\n" \
  ".LW_rot_%=:\n" \
  "s_xor_b32 s51, s51, s56\n" \
  "s_xor_b32 s71, s71, s56\n" \
  "v_xor_b32 v250, s56, v250\n" \
  "v_xor_b32 v252, s56, v252\n" \
  "s_mov_b32 s60, s56\n" \
  "s_mov_b32 s56, s57\n" \
  "s_mov_b32 s57, s60\n" \
  "s_add_u32 s59, s51, 819200\n" \
  "s_mov_b32 m0, 0\n" \
  "s_mov_b32 s70, 0\n" \
  "s_mov_b32 s68, 0\n" \
  "s_waitcnt lgkmcnt(0)\n" \
  "s_mov_b32 s69, 0\n" \
  ".LW_tw_%=:\n" \
  "v_readfirstlane_b32 s63, v52\n" \
  "s_cmp_lg_u32 s63, s48\n" \
  "s_cbranch_scc1 .LW_tw_re_%=\n" \
  "v_readfirstlane_b32 s53, v54\n" \
  ".LW_rt_%=:\n" \
  "s_memrealtime s[60:61]\n" \
  "s_add_u32 s73, s53, s44\n" \
  "s_waitcnt lgkmcnt(0)\n" \
  "s_sub_u32 s62, s73, s60\n" \
  "s_cmp_ge_u32 s62, 2000\n" \
  "s_cbranch_scc1 .LW_lat_%=\n" \
  "s_sub_u32 s62, s62, 30\n" \
  ".LW_slp_%=:\n" \
  "s_cmp_le_i32 s62, 0\n" \
  "s_cbranch_scc1 .LW_clk_%=\n" \
  "s_sleep 1\n" \
  "s_sub_u32 s62, s62, 3\n" \
  "s_branch .LW_slp_%=\n" \
  ".LW_clk_%=:\n" \
  "s_memrealtime s[60:61]\n" \
  "s_waitcnt lgkmcnt(0)\n" \
  "s_sub_u32 s62, s73, s60\n" \
  "s_cmp_gt_i32 s62, 21\n" \
  "s_cbranch_scc1 .LW_clk_%=\n" \
  "s_cmp_le_i32 s62, 11\n" \
  "s_cbranch_scc1 .LW_clk2_%=\n" \
  "s_memrealtime s[76:77]\n" \
  "s_nop 4\n" \
  "s_memrealtime s[78:79]\n" \
  "s_nop 4\n" \
  "s_memrealtime s[80:81]\n" \
  "s_nop 4\n" \
  "s_memrealtime s[82:83]\n" \
  "s_waitcnt lgkmcnt(0)\n" \
  "s_mov_b32 s77, 1\n" \
  "s_cmp_eq_u32 s78, s76\n" \
  "s_addc_u32 s77, s77, 0\n" \
  "s_cmp_eq_u32 s80, s76\n" \
  "s_addc_u32 s77, s77, 0\n" \
  "s_cmp_eq_u32 s82, s76\n" \
  "s_addc_u32 s77, s77, 0\n" \
  "s_mul_i32 s77, s77, 24\n" \
  "s_lshr_b32 s78, s87, 2\n" \
  "s_sub_u32 s77, s78, s77\n" \
  "s_max_i32 s77, s77, 0\n" \
  "s_sub_u32 s76, s73, s76\n" \
  "s_mul_i32 s76, s76, s87\n" \
  "s_ashr_i32 s76, s76, 4\n" \
  "s_sub_u32 s76, s76, s77\n" \
  "s_sub_u32 s76, s76, 200\n" \
  "s_max_i32 s76, s76, 0\n" \
  "s_min_i32 s76, s76, 320\n" \
  "s_lshr_b32 s76, s76, 3\n" \
  "s_lshl_b32 s76, s76, 2\n" \
  "s_mov_b32 s68, 1\n" \
  "s_getpc_b64 s[78:79]\n" \
  "s_sub_u32 s76, 180, s76\n" \
  "s_add_u32 s78, s78, s76\n" \
  "s_addc_u32 s79, s79, 0\n" \
  "s_setpc_b64 s[78:79]\n" \
  "s_nop 1\n" \
  "s_nop 1\n" \
  "s_nop 1\n" \
  "s_nop 1\n" \
  "s_nop 1\n" \
  "s_nop 1\n" \
  "s_nop 1\n" \
  "s_nop 1\n" \
  "s_nop 1\n" \
  "s_nop 1\n" \
  "s_nop 1\n" \
  "s_nop 1\n" \
  "s_nop 1\n" \
  "s_nop 1\n" \
  "s_nop 1\n" \
  "s_nop 1\n" \
  "s_nop 1\n" \
  "s_nop 1\n" \
  "s_nop 1\n" \
  "s_nop 1\n" \
  "s_nop 1\n" \
  "s_nop 1\n" \
  "s_nop 1\n" \
  "s_nop 1\n" \
  "s_nop 1\n" \
  "s_nop 1\n" \
  "s_nop 1\n" \
  "s_nop 1\n" \
  "s_nop 1\n" \
  "s_nop 1\n" \
  "s_nop 1\n" \
  "s_nop 1\n" \
  "s_nop 1\n" \
  "s_nop 1\n" \
  "s_nop 1\n" \
  "s_nop 1\n" \
  "s_nop 1\n" \
  "s_nop 1\n" \
  "s_nop 1\n" \
  "s_nop 1\n" \
  ".LW_noclk_%=:\n" \
  "buffer_load_dwordx4 v243, s[40:43], s59 offen sc1 lds\n" \
  "buffer_load_dwordx4 v243, s[40:43], s59 offen offset:1024 sc1 lds\n" \
  "buffer_load_dwordx4 v243, s[40:43], s59 offen offset:2048 sc1 lds\n" \
  "buffer_load_dwordx4 v243, s[40:43], s59 offen offset:3072 sc1 lds\n" \
  "s_add_u32 s60, s59, 32768\n" \
  "s_movk_i32 m0, 4096\n" \
  "s_nop 0\n" \
  "buffer_load_dwordx4 v243, s[40:43], s60 offen sc1 lds\n" \
  "buffer_load_dwordx4 v243, s[40:43], s60 offen offset:1024 sc1 lds\n" \
  "buffer_load_dwordx4 v243, s[40:43], s60 offen offset:2048 sc1 lds\n" \
  "buffer_load_dwordx4 v243, s[40:43], s60 offen offset:3072 sc1 lds\n" \
  "s_add_u32 s60, s59, 65536\n" \
  "s_movk_i32 m0, 8192\n" \
  "s_nop 0\n" \
  "buffer_load_dwordx4 v243, s[40:43], s60 offen sc1 lds\n" \
  ".LW_g3_%=:\n" \
  "s_waitcnt vmcnt(5)\n" \
  "s_barrier\n" \
  ".LW_p_%=:\n" \
  "ds_read_b128 v[84:87], v243\n" \
  "ds_read_b128 v[88:91], v243 offset:1024\n" \
  "ds_read_b128 v[92:95], v243 offset:2048\n" \
  "ds_read_b128 v[96:99], v243 offset:3072\n" \
  "s_waitcnt lgkmcnt(3)\n" \
  "v_smfmac_f32_16x16x64_bf16 v[60:63], v[84:87], a[0:7], v242\n" \
  "s_waitcnt vmcnt(0)\n" \
  "s_barrier\n" \
  "ds_read_b128 v[100:103], v243 offset:5120\n" \
  "ds_read_b128 v[104:107], v243 offset:4096\n" \
  "s_waitcnt lgkmcnt(4)\n" \
  "v_smfmac_f32_16x16x64_bf16 v[60:63], v[88:91], a[8:15], v242\n" \
  "ds_read_b128 v[108:111], v243 offset:7168\n" \
  "s_waitcnt lgkmcnt(4)\n" \
  "v_smfmac_f32_16x16x64_bf16 v[60:63], v[92:95], a[16:23], v242\n" \
  "ds_read_b128 v[112:115], v243 offset:6144\n" \
  "v_smfmac_f32_16x16x64_bf16 v[64:67], v[84:87], a[72:79], v242\n" \
  "ds_read_b128 v[116:119], v243 offset:8192\n" \
  "s_waitcnt lgkmcnt(5)\n" \
  "v_smfmac_f32_16x16x64_bf16 v[60:63], v[96:99], a[24:31], v242\n" \
  "s_waitcnt lgkmcnt(3)\n" \
  "v_smfmac_f32_16x16x64_bf16 v[60:63], v[104:107], a[32:39], v242\n" \
  "v_smfmac_f32_16x16x64_bf16 v[60:63], v[100:103], a[40:47], v242\n" \
  "s_waitcnt lgkmcnt(1)\n" \
  "v_smfmac_f32_16x16x64_bf16 v[60:63], v[112:115], a[48:55], v242\n" \
  "v_smfmac_f32_16x16x64_bf16 v[60:63], v[108:111], a[56:63], v242\n" \
  "s_waitcnt lgkmcnt(0)\n" \
  "v_smfmac_f32_16x16x64_bf16 v[60:63], v[116:119], a[64:71], v242\n" \
  "s_nop 2\n" \
  "v_smfmac_f32_16x16x64_bf16 v[64:67], v[88:91], a[80:87], v242\n" \
  "s_nop 3\n" \
  "v_add_f32 v60, v60, v62\n" \
  "v_add_f32 v61, v61, v63\n" \
  "v_cmp_u_f32 vcc, v60, v61\n" \
  "v_cvt_pk_bf16_f32 v80, v60, v61\n" \
  "v_smfmac_f32_16x16x64_bf16 v[64:67], v[92:95], a[88:95], v242\n" \
  "s_cbranch_vccnz .LW_slow_%=\n" \
  ".LW_back_%=:\n" \
  "ds_write_b32 v244, v80\n" \
  "v_smfmac_f32_16x16x64_bf16 v[64:67], v[96:99], a[96:103], v242\n" \
  "v_smfmac_f32_16x16x64_bf16 v[64:67], v[104:107], a[104:111], v242\n" \
  "v_smfmac_f32_16x16x64_bf16 v[64:67], v[100:103], a[112:119], v242\n" \
  "v_smfmac_f32_16x16x64_bf16 v[64:67], v[112:115], a[120:127], v242\n" \
  "s_waitcnt lgkmcnt(0)\n" \
  "s_barrier\n" \
  "v_smfmac_f32_16x16x64_bf16 v[64:67], v[108:111], a[128:135], v242\n" \
  "ds_read_b64_tr_b16 v[86:87], v245 offset:1536\n" \
  "ds_read_b64_tr_b16 v[90:91], v245 offset:512\n" \
  "v_smfmac_f32_16x16x64_bf16 v[64:67], v[116:119], a[136:143], v242\n" \
  "ds_read_b64_tr_b16 v[88:89], v245\n" \
  "s_nop 6\n" \
  "v_add_f32 v65, v65, v67\n" \
  "v_add_f32 v64, v64, v66\n" \
  "v_cvt_pk_bf16_f32 v81, v64, v65\n" \
  "ds_write_b32 v244, v81 offset:256\n" \
  "s_waitcnt lgkmcnt(1)\n" \
  "v_smfmac_f32_16x16x64_bf16 v[76:79], v[88:91], a[208:215], v248\n" \
  "v_smfmac_f32_16x16x64_bf16 v[72:75], v[88:91], a[176:183], v248\n" \
  "ds_read_b64_tr_b16 v[84:85], v245 offset:1024\n" \
  "v_smfmac_f32_16x16x64_bf16 v[68:71], v[88:91], a[144:151], v248\n" \
  "s_waitcnt lgkmcnt(1)\n" \
  "s_barrier\n" \
  "ds_read_b64_tr_b16 v[88:89], v245 offset:256\n" \
  "ds_read_b64_tr_b16 v[90:91], v245 offset:768\n" \
  "ds_read_b64_tr_b16 v[94:95], v245 offset:1792\n" \
  "s_waitcnt lgkmcnt(3)\n" \
  "v_smfmac_f32_16x16x64_bf16 v[68:71], v[84:87], a[152:159], v248\n" \
  "ds_read_b64_tr_b16 v[92:93], v245 offset:1280\n" \
  "v_smfmac_f32_16x16x64_bf16 v[72:75], v[84:87], a[184:191], v248\n" \
  "v_smfmac_f32_16x16x64_bf16 v[76:79], v[84:87], a[216:223], v248\n" \
  "s_waitcnt lgkmcnt(2)\n" \
  "v_smfmac_f32_16x16x64_bf16 v[68:71], v[88:91], a[160:167], v248\n" \
  "s_waitcnt lgkmcnt(0)\n" \
  "v_smfmac_f32_16x16x64_bf16 v[68:71], v[92:95], a[168:175], v248\n" \
  "s_nop 2\n" \
  "v_smfmac_f32_16x16x64_bf16 v[72:75], v[88:91], a[192:199], v248\n" \
  "s_nop 0\n" \
  "v_smfmac_f32_16x16x64_bf16 v[72:75], v[92:95], a[200:207], v248\n" \
  "s_nop 2\n" \
  "v_smfmac_f32_16x16x64_bf16 v[76:79], v[88:91], a[224:231], v248\n" \
  "s_nop 0\n" \
  "v_smfmac_f32_16x16x64_bf16 v[76:79], v[92:95], a[232:239], v248\n" \
  "v_cvt_pk_bf16_f32 v68, v68, v69\n" \
  "v_cvt_pk_bf16_f32 v69, v70, v71\n" \
  "v_cvt_pk_bf16_f32 v71, v74, v75\n" \
  "v_cvt_pk_bf16_f32 v70, v72, v73\n" \
  "s_nop 1\n" \
  "v_permlane16_swap_b32 v68, v70\n" \
  "v_permlane16_swap_b32 v69, v71\n" \
  "v_cvt_pk_bf16_f32 v76, v76, v77\n" \
  "v_cvt_pk_bf16_f32 v77, v78, v79\n" \
  "buffer_store_dwordx4 v[68:71], v250, s[40:43], 0 offen sc1\n" \
  "buffer_store_dwordx2 v[76:77], v252, s[40:43], 0 offen sc1\n" \
  ".LW_loop_%=:\n" \
  "s_memrealtime s[98:99]\n" \
  "s_memtime s[84:85]\n" \
  "s_cmp_ge_u32 s48, 58\n" \
  "s_cbranch_scc1 .LW_cp_%=\n" \
  ".LW_cp_b_%=:\n" \
  "v_mov_b64 v[60:61], 0\n" \
  "v_mov_b64 v[62:63], 0\n" \
  "v_mov_b64 v[64:65], 0\n" \
  "v_mov_b64 v[66:67], 0\n" \
  "v_mov_b64 v[68:69], 0\n" \
  "v_mov_b64 v[70:71], 0\n" \
  "v_mov_b64 v[72:73], 0\n" \
  "v_mov_b64 v[74:75], 0\n" \
  "v_mov_b64 v[76:77], 0\n" \
  "v_mov_b64 v[78:79], 0\n" \
  "s_add_u32 s95, s48, 1\n" \
  "s_and_b32 s63, s95, 3\n" \
  "s_lshl_b32 s95, s95, 5\n" \
  "s_lshl_b32 s63, s63, 4\n" \
  "s_sub_u32 s95, s95, s63\n" \
  "s_add_u32 s95, s95, s45\n" \
  "buffer_load_dwordx4 v[52:55], off, s[40:43], s95 sc1\n" \
  "s_add_u32 s48, s48, 1\n" \
  "v_mov_b32 v56, s48\n" \
  "s_waitcnt lgkmcnt(0)\n" \
  "s_cmp_gt_u32 s48, 57\n" \
  "s_cbranch_scc1 .LW_ef_%=\n" \
  ".LW_ef_b_%=:\n" \
  "s_sub_u32 s76, s84, s90\n" \
  "s_sub_u32 s77, s98, s86\n" \
  "v_cvt_f32_u32 v216, s76\n" \
  "v_cvt_f32_u32 v217, s77\n" \
  "v_rcp_f32 v217, v217\n" \
  "v_mul_f32 v216, 16.0, v216\n" \
  "v_fma_f32 v216, v216, v217, 0.5\n" \
  "v_cvt_u32_f32 v216, v216\n" \
  "s_nop 0\n" \
  "v_readfirstlane_b32 s76, v216\n" \
  "s_max_u32 s76, s76, 160\n" \
  "s_min_u32 s76, s76, 400\n" \
  "s_add_u32 s77, s48, 3\n" \
  "s_and_b32 s77, s77, 3\n" \
  "s_cselect_b32 s87, s87, s76\n" \
  "s_cselect_b32 s86, s86, s98\n" \
  "s_cselect_b32 s90, s90, s84\n" \
  "s_waitcnt vmcnt(0)\n" \
  "s_branch .LW_rot_%=\n" \
  "s_nop 7\n" \
  ".LW_slow_%=:\n" \
  "s_bitcmp1_b32 s70, 0\n" \
  "s_cbranch_scc1 .LW_back_%=\n" \
  "s_waitcnt lgkmcnt(0)\n" \
  "s_cmp_lt_u32 s70, 10\n" \
  "s_nop 3\n" \
  "s_cbranch_scc1 .LW_nol_%=\n" \
  "v_max_u32 v217, v84, v85\n" \
  "v_max3_u32 v217, v217, v86, v87\n" \
  "v_max3_u32 v217, v217, v88, v89\n" \
  "v_max3_u32 v217, v217, v90, v91\n" \
  "v_max3_u32 v217, v217, v92, v93\n" \
  "v_max3_u32 v217, v217, v94, v95\n" \
  "v_max3_u32 v217, v217, v96, v97\n" \
  "v_max3_u32 v217, v217, v98, v99\n" \
  "v_max3_u32 v217, v217, v104, v105\n" \
  "v_max3_u32 v217, v217, v106, v107\n" \
  "v_max3_u32 v217, v217, v100, v101\n" \
  "v_max3_u32 v217, v217, v102, v103\n" \
  "v_max3_u32 v217, v217, v112, v113\n" \
  "v_max3_u32 v217, v217, v114, v115\n" \
  "v_max3_u32 v217, v217, v108, v109\n" \
  "v_max3_u32 v217, v217, v110, v111\n" \
  "v_max3_u32 v217, v217, v116, v117\n" \
  "v_max3_u32 v217, v217, v118, v119\n" \
  "v_cmp_eq_u32 vcc, -1, v217\n" \
  "s_cbranch_vccz .LW_back_%=\n" \
  ".LW_nol_%=:\n" \
  "s_add_u32 s70, s70, 2\n" \
  "s_cmp_gt_u32 s70, 65536\n" \
  "s_cbranch_scc1 .LW_dset_%=\n" \
  "s_barrier\n" \
  "v_mov_b64 v[60:61], 0\n" \
  "v_mov_b64 v[62:63], 0\n" \
  "v_mov_b64 v[64:65], 0\n" \
  "v_mov_b64 v[66:67], 0\n" \
  "v_mov_b64 v[68:69], 0\n" \
  "v_mov_b64 v[70:71], 0\n" \
  "v_mov_b64 v[72:73], 0\n" \
  "v_mov_b64 v[74:75], 0\n" \
  "v_mov_b64 v[76:77], 0\n" \
  "v_mov_b64 v[78:79], 0\n" \
  "s_and_b32 s62, %48, 0xffff\n" \
  "s_add_u32 s44, s44, s62\n" \
  "s_xor_b32 s59, s59, 0\n" \
  "s_mov_b32 s68, 0\n" \
  "s_mov_b32 m0, 0\n" \
  "s_branch .LW_rt_%=\n" \
  ".LW_dset_%=:\n" \
  "v_mov_b32 v100, 1\n" \
  "v_mov_b32 v101, s55\n" \
  "buffer_store_dwordx2 v[100:101], off, s[40:43], %46 offset:128 sc1\n" \
  "s_mov_b32 s70, 1\n" \
  "v_mov_b64 v[60:61], 0\n" \
  "v_mov_b64 v[62:63], 0\n" \
  "v_mov_b64 v[64:65], 0\n" \
  "v_mov_b64 v[66:67], 0\n" \
  "v_mov_b64 v[68:69], 0\n" \
  "v_mov_b64 v[70:71], 0\n" \
  "v_mov_b64 v[72:73], 0\n" \
  "v_mov_b64 v[74:75], 0\n" \
  "v_mov_b64 v[76:77], 0\n" \
  "v_mov_b64 v[78:79], 0\n" \
  "s_waitcnt vmcnt(0) lgkmcnt(0)\n" \
  "s_barrier\n" \
  "s_branch .LW_p_%=\n" \
  ".LW_cp_%=:\n" \
  "s_sub_u32 s60, s48, 58\n" \
  "s_lshl_b32 s60, s60, 20\n" \
  "s_add_u32 s60, s60, s47\n" \
  "s_lshr_b32 s62, s60, 8\n" \
  "s_and_b32 s62, s62, 8192\n" \
  "s_xor_b32 s62, s62, s60\n" \
  "buffer_store_dwordx4 v[68:71], v253, s[40:43], s62 offen sc1\n" \
  "buffer_store_dwordx2 v[76:77], v255, s[40:43], s62 offen sc1\n" \
  "s_nop 1\n" \
  "s_branch .LW_cp_b_%=\n" \
  ".LW_ef_%=:\n" \
  "s_cmp_eq_u32 s48, 64\n" \
  "s_cbranch_scc1 .LW_end_%=\n" \
  "s_waitcnt vmcnt(0)\n" \
  "s_barrier\n" \
  "s_branch .LW_ef_b_%=\n" \
  ".LW_tw_re_%=:\n" \
  "s_add_u32 s69, s69, 1\n" \
  "s_cmp_gt_u32 s69, 200\n" \
  "s_cbranch_scc1 .LW_noclk_%=\n" \
  "s_sleep 2\n" \
  "buffer_load_dwordx4 v[52:55], off, s[40:43], s95 sc1\n" \
  "s_waitcnt vmcnt(0)\n" \
  "s_branch .LW_tw_%=\n" \
  ".LW_lat_%=:\n" \
  "s_lshr_b32 s63, %48, 16\n" \
  "s_add_u32 s62, s62, s63\n" \
  "s_cmp_lt_u32 s62, 4294937296\n" \
  "s_cbranch_scc1 .LW_noclk_%=\n" \
  "s_and_b32 s63, %48, 0xffff\n" \
  ".LW_lat1_%=:\n" \
  "s_add_u32 s44, s44, s63\n" \
  "s_add_u32 s62, s62, s63\n" \
  "s_cbranch_scc0 .LW_lat1_%=\n" \
  "s_branch .LW_rt_%=\n" \
  ".LW_clk2_%=:\n" \
  "s_cmp_le_i32 s62, 3\n" \
  "s_cbranch_scc1 .LW_clk_done_%=\n" \
  "s_memrealtime s[60:61]\n" \
  "s_waitcnt lgkmcnt(0)\n" \
  "s_sub_u32 s62, s73, s60\n" \
  "s_branch .LW_clk2_%=\n" \
  ".LW_clk_done_%=:\n" \
  "s_branch .LW_noclk_%=\n" \
  ".LW_end_%=:\n" \
  "s_waitcnt vmcnt(0)\n" \
  "s_waitcnt vmcnt(0)\n" \
  "s_nop 7\n" \
  "s_endpgm\n" \
  ".LW_other_%=:\n"
#define W_ASM_VOPS "+{v242}"(ci.sidx), "+{v243}"(ci.l16), "+{v244}"(ci.viw), "+{v245}"(ci.fva), "+{v246}"(ci.pk3), "+{v247}"(ci.fvs), "+{v248}"(ci.sid2), "+{v249}"(ci.uo), "+{v250}"(ci.po0), "+{v251}"(ci.po1), "+{v252}"(ci.po2), "+{v253}"(ci.co0), "+{v254}"(ci.co1), "+{v255}"(ci.co2), "+{v200}"(ci.vq0), "+{v201}"(ci.vq1), "+{v202}"(ci.vq2), "+{v203}"(ci.vq3), "+{v204}"(ci.vo0), "+{v205}"(ci.vo1), "+{v206}"(ci.vo2), "+{v207}"(ci.vo3), "+{v208}"(ci.v50), "+{v209}"(ci.v51), "+{v210}"(ci.v52), "+{v211}"(ci.v53), "+{v212}"(ci.tro0), "+{v213}"(ci.tro1), "+{v214}"(ci.m0o), "+{v215}"(ci.tro2), "+{v216}"(ci.tro3), "+{v217}"(ci.tro4)
#define W_ASM_SOPS "s"(ci.ws), "s"(ci.Wa), "s"(ci.Wb), "s"(ci.mem0), "s"(ci.w), "s"(ci.o_a), "s"(ci.o_b), "s"(ci.o_nx), "s"(ci.o_exp), "s"(ci.lab), "s"(ci.f_p), "s"(ci.f_m), "s"(ci.f_dur), "s"(ci.f_go), "s"(ci.o_ctl), "s"(ci.o_dbg), "s"(ci.dret), "s"(ci.o_qx), "s"(ci.f_s), "s"(ci.f_r)
#define W_ASM_CLOB "v0", "v1", "v2", "v3", "v4", "v5", "v6", "v7", "v8", "v9", "v10", "v11", "v12", "v13", "v14", "v15", "v16", "v17", "v18", "v19", "v20", "v21", "v22", "v23", "v24", "v25", "v26", "v27", "v28", "v29", "v30", "v31", "v32", "v33", "v34", "v35", "v36", "v37", "v38", "v39", "v40", "v41", "v42", "v43", "v44", "v45", "v46", "v47", "v48", "v49", "v50", "v51", "v52", "v53", "v54", "v55", "v56", "v57", "v58", "v59", "v60", "v61", "v62", "v63", "v64", "v65", "v66", "v67", "v68", "v69", "v70", "v71", "v72", "v73", "v74", "v75", "v76", "v77", "v78", "v79", "v80", "v81", "v82", "v83", "v84", "v85", "v86", "v87", "v88", "v89", "v90", "v91", "v92", "v93", "v94", "v95", "v96", "v97", "v98", "v99", "v100", "v101", "v102", "v103", "v104", "v105", "v106", "v107", "v108", "v109", "v110", "v111", "v112", "v113", "v114", "v115", "v116", "v117", "v118", "v119", "v120", "v121", "v122", "v123", "v124", "v125", "v126", "v127", "v128", "v129", "v130", "v131", "v132", "v133", "v134", "v135", "v136", "v137", "v138", "v139", "v140", "v141", "v142", "v143", "v144", "v145", "v146", "v147", "v148", "v149", "v150", "v151", "v152", "v153", "v154", "v155", "v156", "v157", "v158", "v159", "v160", "v161", "v162", "v163", "v164", "v165", "v166", "v167", "v168", "v169", "v170", "v171", "v172", "v173", "v174", "v175", "v176", "v177", "v178", "v179", "v180", "v181", "v182", "v183", "v184", "v185", "v186", "v187", "v188", "v189", "v190", "v191", "v192", "v193", "v194", "v195", "v196", "v197", "v198", "v199", "a0", "a1", "a2", "a3", "a4", "a5", "a6", "a7", "a8", "a9", "a10", "a11", "a12", "a13", "a14", "a15", "a16", "a17", "a18", "a19", "a20", "a21", "a22", "a23", "a24", "a25", "a26", "a27", "a28", "a29", "a30", "a31", "a32", "a33", "a34", "a35", "a36", "a37", "a38", "a39", "a40", "a41", "a42", "a43", "a44", "a45", "a46", "a47", "a48", "a49", "a50", "a51", "a52", "a53", "a54", "a55", "a56", "a57", "a58", "a59", "a60", "a61", "a62", "a63", "a64", "a65", "a66", "a67", "a68", "a69", "a70", "a71", "a72", "a73", "a74", "a75", "a76", "a77", "a78", "a79", "a80", "a81", "a82", "a83", "a84", "a85", "a86", "a87", "a88", "a89", "a90", "a91", "a92", "a93", "a94", "a95", "a96", "a97", "a98", "a99", "a100", "a101", "a102", "a103", "a104", "a105", "a106", "a107", "a108", "a109", "a110", "a111", "a112", "a113", "a114", "a115", "a116", "a117", "a118", "a119", "a120", "a121", "a122", "a123", "a124", "a125", "a126", "a127", "a128", "a129", "a130", "a131", "a132", "a133", "a134", "a135", "a136", "a137", "a138", "a139", "a140", "a141", "a142", "a143", "a144", "a145", "a146", "a147", "a148", "a149", "a150", "a151", "a152", "a153", "a154", "a155", "a156", "a157", "a158", "a159", "a160", "a161", "a162", "a163", "a164", "a165", "a166", "a167", "a168", "a169", "a170", "a171", "a172", "a173", "a174", "a175", "a176", "a177", "a178", "a179", "a180", "a181", "a182", "a183", "a184", "a185", "a186", "a187", "a188", "a189", "a190", "a191", "a192", "a193", "a194", "a195", "a196", "a197", "a198", "a199", "a200", "a201", "a202", "a203", "a204", "a205", "a206", "a207", "a208", "a209", "a210", "a211", "a212", "a213", "a214", "a215", "a216", "a217", "a218", "a219", "a220", "a221", "a222", "a223", "a224", "a225", "a226", "a227", "a228", "a229", "a230", "a231", "a232", "a233", "a234", "a235", "a236", "a237", "a238", "a239", "a240", "a241", "a242", "a243", "a244", "a245", "a246", "a247", "a248", "a249", "a250", "a251", "a252", "a253", "a254", "a255", "s40", "s41", "s42", "s43", "s44", "s45", "s46", "s47", "s48", "s49", "s50", "s51", "s52", "s53", "s54", "s55", "s56", "s57", "s58", "s59", "s60", "s61", "s62", "s63", "s64", "s65", "s66", "s67", "s68", "s69", "s70", "s71", "s72", "s73", "s74", "s75", "s76", "s77", "s78", "s79", "s80", "s81", "s82", "s83", "s84", "s85", "s86", "s87", "s88", "s89", "s90", "s91", "s92", "s93", "s94", "s95", "s96", "s97", "s98", "s99", "vcc", "scc", "memory"
#define R_ASM_TEXT \
  "s_mov_b64 s[40:41], %18\n" \
  "s_and_b32 s41, s41, 0xffff\n" \
  "s_mov_b32 s42, 255852544\n" \
  "s_mov_b32 s43, 0x00020000\n" \
  "s_bfe_u32 s56, %20, 0x30010\n" \
  "s_lshl_b32 s56, s56, 12\n" \
  "s_or_b32 s56, s56, 0x200000\n" \
  "s_mov_b32 s57, 1052672\n" \
  "s_mov_b32 s48, 0\n" \
  "s_cmp_lg_u32 %19, 0\n" \
  "s_cbranch_scc1 .LR_help_%=\n" \
  "s_lshl_b32 s61, %24, 2\n" \
  "s_lshr_b32 s51, %20, s61\n" \
  "s_and_b32 s51, s51, 7\n" \
  "s_lshl_b32 s51, s51, 12\n" \
  "s_xor_b32 s51, s51, 4210688\n" \
  "s_mov_b32 s71, s56\n" \
  "s_add_u32 s47, s51, 886784\n" \
  "s_lshr_b32 s52, %20, 0\n" \
  "s_and_b32 s52, s52, 7\n" \
  "s_lshl_b32 s52, s52, 12\n" \
  "s_xor_b32 s52, s52, 4210688\n" \
  "s_xor_b32 s52, s52, s56\n" \
  "s_lshr_b32 s64, %20, 4\n" \
  "s_and_b32 s64, s64, 7\n" \
  "s_lshl_b32 s64, s64, 12\n" \
  "s_xor_b32 s64, s64, 4210688\n" \
  "s_xor_b32 s64, s64, s56\n" \
  "s_lshr_b32 s65, %20, 8\n" \
  "s_and_b32 s65, s65, 7\n" \
  "s_lshl_b32 s65, s65, 12\n" \
  "s_xor_b32 s65, s65, 4210688\n" \
  "s_xor_b32 s65, s65, s56\n" \
  "s_lshr_b32 s67, %20, 12\n" \
  "s_and_b32 s67, s67, 7\n" \
  "s_lshl_b32 s67, s67, 12\n" \
  "s_xor_b32 s67, s67, 4210688\n" \
  "s_xor_b32 s67, s67, s56\n" \
  "v_add_u32 v208, 32768, v215\n" \
  "v_add_u32 v209, 65536, v215\n" \
  "v_add_u32 v210, 98304, v215\n" \
  "v_add_u32 v211, 40960, v214\n" \
  "s_mov_b32 s49, %22\n" \
  "s_mov_b32 s58, %23\n" \
  "s_memrealtime s[86:87]\n" \
  "s_memtime s[90:91]\n" \
  "s_waitcnt lgkmcnt(0)\n" \
  "s_mov_b32 s87, 900\n" \
  "s_mov_b32 s50, 0\n" \
  "s_mov_b32 s44, 0\n" \
  "s_mov_b32 s88, 0\n" \
  "s_mov_b32 s45, 0\n" \
  "s_mov_b32 s72, 2130706432\n" \
  "s_mov_b32 s46, 4096\n" \
  "v_mov_b32 v199, %21\n" \
  "s_mov_b32 s89, 2130706432\n" \
  "s_xor_b32 s51, s51, 0x40000000\n" \
  "s_xor_b32 s49, s49, 0x40000000\n" \
  "s_xor_b32 s58, s58, 0x40000000\n" \
  "s_xor_b32 s47, s47, 0x40000000\n" \
  "s_xor_b32 s52, s52, 0x40000000\n" \
  "s_xor_b32 s64, s64, 0x40000000\n" \
  "s_xor_b32 s65, s65, 0x40000000\n" \
  "s_xor_b32 s67, s67, 0x40000000\n" \
  ".LR_loop_%=:\n" \
  "s_add_u32 s95, s48, 0\n" \
  "s_and_b32 s63, s95, 3\n" \
  "s_lshl_b32 s95, s95, 5\n" \
  "s_lshl_b32 s63, s63, 4\n" \
  "s_sub_u32 s95, s95, s63\n" \
  "s_add_u32 s95, s95, s47\n" \
  "s_mov_b32 s54, s51\n" \
  "s_add_u32 s94, s51, 32768\n" \
  "s_mov_b32 s70, 0\n" \
  "s_mov_b32 s96, 0\n" \
  "buffer_load_dwordx4 v[200:203], off, s[40:43], s95 sc1\n" \
  "s_sub_u32 s76, s84, s90\n" \
  "s_sub_u32 s77, s98, s86\n" \
  "v_cvt_f32_u32 v196, s76\n" \
  "v_cvt_f32_u32 v197, s77\n" \
  "v_rcp_f32 v197, v197\n" \
  "v_mul_f32 v196, 16.0, v196\n" \
  "v_fma_f32 v196, v196, v197, 0.5\n" \
  "v_cvt_u32_f32 v196, v196\n" \
  "s_nop 0\n" \
  "v_readfirstlane_b32 s76, v196\n" \
  "s_max_u32 s76, s76, 160\n" \
  "s_min_u32 s76, s76, 400\n" \
  "s_add_u32 s77, s48, 2\n" \
  "s_and_b32 s77, s77, 3\n" \
  "s_cselect_b32 s87, s87, s76\n" \
  "s_cselect_b32 s86, s86, s98\n" \
  "s_cselect_b32 s90, s90, s84\n" \
  "v_mov_b64 v[64:65], 0\n" \
  "v_mov_b64 v[66:67], 0\n" \
  "v_mov_b64 v[68:69], 0\n" \
  "v_mov_b64 v[70:71], 0\n" \
  "v_mov_b64 v[72:73], 0\n" \
  "v_mov_b64 v[74:75], 0\n" \
  "v_mov_b64 v[76:77], 0\n" \
  "v_mov_b64 v[78:79], 0\n" \
  "v_mov_b64 v[80:81], 0\n" \
  "v_mov_b64 v[82:83], 0\n" \
  "v_mov_b64 v[84:85], 0\n" \
  "v_mov_b64 v[86:87], 0\n" \
  "v_mov_b64 v[88:89], 0\n" \
  "v_mov_b64 v[90:91], 0\n" \
  "v_mov_b64 v[92:93], 0\n" \
  "v_mov_b64 v[94:95], 0\n" \
  "v_mov_b64 v[96:97], 0\n" \
  "v_mov_b64 v[98:99], 0\n" \
  "v_mov_b64 v[100:101], 0\n" \
  "v_mov_b64 v[102:103], 0\n" \
  "v_mov_b64 v[104:105], 0\n" \
  "v_mov_b64 v[106:107], 0\n" \
  "v_mov_b64 v[108:109], 0\n" \
  "v_mov_b64 v[110:111], 0\n" \
  "v_mov_b64 v[112:113], 0\n" \
  "v_mov_b64 v[114:115], 0\n" \
  "v_mov_b64 v[116:117], 0\n" \
  "v_mov_b64 v[118:119], 0\n" \
  "v_mov_b64 v[120:121], 0\n" \
  "v_mov_b64 v[122:123], 0\n" \
  "v_mov_b64 v[124:125], 0\n" \
  "v_mov_b64 v[126:127], 0\n" \
  "v_mov_b64 v[192:193], 0\n" \
  "v_mov_b64 v[194:195], 0\n" \
  "s_mov_b64 exec, 1\n" \
  "buffer_store_dwordx2 v[198:199], off, s[40:43], s89 sc1\n" \
  "s_mov_b64 exec, -1\n" \
  "s_barrier\n" \
  "ds_read_b128 a[0:3], v211 offset:0\n" \
  "ds_read_b128 a[4:7], v211 offset:1024\n" \
  "ds_read_b128 a[8:11], v211 offset:2048\n" \
  "ds_read_b128 a[12:15], v211 offset:3072\n" \
  "ds_read_b128 a[16:19], v211 offset:4096\n" \
  "ds_read_b128 a[20:23], v211 offset:5120\n" \
  "ds_read_b128 a[24:27], v211 offset:6144\n" \
  "ds_read_b128 a[28:31], v211 offset:7168\n" \
  "ds_read_b128 a[32:35], v211 offset:8192\n" \
  "ds_read_b128 a[36:39], v211 offset:9216\n" \
  "ds_read_b128 a[40:43], v211 offset:10240\n" \
  "ds_read_b128 a[44:47], v211 offset:11264\n" \
  "ds_read_b128 a[48:51], v211 offset:12288\n" \
  "ds_read_b128 a[52:55], v211 offset:13312\n" \
  "ds_read_b128 a[56:59], v211 offset:14336\n" \
  "ds_read_b128 a[60:63], v211 offset:15360\n" \
  "ds_read_b128 a[64:67], v211 offset:16384\n" \
  "ds_read_b128 a[68:71], v211 offset:17408\n" \
  "ds_read_b128 a[72:75], v211 offset:18432\n" \
  "ds_read_b128 a[76:79], v211 offset:19456\n" \
  "ds_read_b128 a[80:83], v211 offset:20480\n" \
  "ds_read_b128 a[84:87], v211 offset:21504\n" \
  "ds_read_b128 a[88:91], v211 offset:22528\n" \
  "ds_read_b128 a[92:95], v211 offset:23552\n" \
  "ds_read_b128 a[96:99], v211 offset:24576\n" \
  "ds_read_b128 a[100:103], v211 offset:25600\n" \
  "ds_read_b128 a[104:107], v211 offset:26624\n" \
  "ds_read_b128 a[108:111], v211 offset:27648\n" \
  "ds_read_b128 a[112:115], v211 offset:28672\n" \
  "ds_read_b128 a[116:119], v211 offset:29696\n" \
  "ds_read_b128 a[120:123], v211 offset:30720\n" \
  "ds_read_b128 a[124:127], v211 offset:31744\n" \
  "v_xor_b32 v211, 32768, v211\n" \
  "s_mov_b32 s69, 0\n" \
  "s_waitcnt vmcnt(1)\n" \
  ".LR_tw_%=:\n" \
  "v_readfirstlane_b32 s60, v200\n" \
  "s_cmp_lg_u32 s60, s48\n" \
  "s_cbranch_scc1 .LR_tw_re_%=\n" \
  "v_readfirstlane_b32 s50, v201\n" \
  "s_cmp_eq_u32 s48, 0\n" \
  "s_cselect_b32 s50, s86, s50\n" \
  "s_cmp_lg_u32 s44, s45\n" \
  "s_cselect_b32 s60, s46, 0\n" \
  "s_mov_b32 s45, s44\n" \
  "s_xor_b32 s54, s54, s60\n" \
  "s_xor_b32 s94, s94, s60\n" \
  ".LR_again_%=:\n" \
  "s_memrealtime s[60:61]\n" \
  "s_add_u32 s53, s50, s44\n" \
  "s_bfe_u32 s63, %28, 0x80010\n" \
  "s_waitcnt lgkmcnt(0)\n" \
  "s_sub_u32 s62, s53, s60\n" \
  "s_sub_u32 s61, s62, 14\n" \
  "s_cmp_ge_u32 s61, 2000\n" \
  "s_cbranch_scc1 .LR_near_%=\n" \
  "s_sub_u32 s62, s62, s63\n" \
  "s_cmp_le_i32 s62, 0\n" \
  "s_cbranch_scc1 .LR_fin_%=\n" \
  "s_sub_u32 s62, s62, 9\n" \
  ".LR_slp_%=:\n" \
  "s_cmp_le_i32 s62, 0\n" \
  "s_cbranch_scc1 .LR_clk_%=\n" \
  "s_sleep 1\n" \
  "s_sub_u32 s62, s62, 3\n" \
  "s_branch .LR_slp_%=\n" \
  ".LR_clk_%=:\n" \
  "s_memrealtime s[60:61]\n" \
  "s_waitcnt lgkmcnt(0)\n" \
  "s_sub_u32 s62, s53, s60\n" \
  "s_cmp_gt_i32 s62, s63\n" \
  "s_cbranch_scc1 .LR_clk_%=\n" \
  ".LR_fin_%=:\n" \
  "s_memrealtime s[76:77]\n" \
  "s_nop 4\n" \
  "s_memrealtime s[78:79]\n" \
  "s_nop 4\n" \
  "s_memrealtime s[80:81]\n" \
  "s_nop 4\n" \
  "s_memrealtime s[82:83]\n" \
  "s_waitcnt lgkmcnt(0)\n" \
  "s_mov_b32 s77, 1\n" \
  "s_cmp_eq_u32 s78, s76\n" \
  "s_addc_u32 s77, s77, 0\n" \
  "s_cmp_eq_u32 s80, s76\n" \
  "s_addc_u32 s77, s77, 0\n" \
  "s_cmp_eq_u32 s82, s76\n" \
  "s_addc_u32 s77, s77, 0\n" \
  "s_mul_i32 s77, s77, 24\n" \
  "s_lshr_b32 s78, s87, 2\n" \
  "s_sub_u32 s77, s78, s77\n" \
  "s_max_i32 s77, s77, 0\n" \
  "s_sub_u32 s76, s53, s76\n" \
  "s_mul_i32 s76, s76, s87\n" \
  "s_ashr_i32 s76, s76, 4\n" \
  "s_sub_u32 s76, s76, s77\n" \
  "s_sub_u32 s76, s76, 220\n" \
  "s_max_i32 s76, s76, 0\n" \
  "s_min_i32 s76, s76, 400\n" \
  "s_lshr_b32 s76, s76, 3\n" \
  "s_lshl_b32 s76, s76, 2\n" \
  "s_getpc_b64 s[78:79]\n" \
  "s_sub_u32 s76, 220, s76\n" \
  "s_add_u32 s78, s78, s76\n" \
  "s_addc_u32 s79, s79, 0\n" \
  "s_setpc_b64 s[78:79]\n" \
  "s_nop 1\n" \
  "s_nop 1\n" \
  "s_nop 1\n" \
  "s_nop 1\n" \
  "s_nop 1\n" \
  "s_nop 1\n" \
  "s_nop 1\n" \
  "s_nop 1\n" \
  "s_nop 1\n" \
  "s_nop 1\n" \
  "s_nop 1\n" \
  "s_nop 1\n" \
  "s_nop 1\n" \
  "s_nop 1\n" \
  "s_nop 1\n" \
  "s_nop 1\n" \
  "s_nop 1\n" \
  "s_nop 1\n" \
  "s_nop 1\n" \
  "s_nop 1\n" \
  "s_nop 1\n" \
  "s_nop 1\n" \
  "s_nop 1\n" \
  "s_nop 1\n" \
  "s_nop 1\n" \
  "s_nop 1\n" \
  "s_nop 1\n" \
  "s_nop 1\n" \
  "s_nop 1\n" \
  "s_nop 1\n" \
  "s_nop 1\n" \
  "s_nop 1\n" \
  "s_nop 1\n" \
  "s_nop 1\n" \
  "s_nop 1\n" \
  "s_nop 1\n" \
  "s_nop 1\n" \
  "s_nop 1\n" \
  "s_nop 1\n" \
  "s_nop 1\n" \
  "s_nop 1\n" \
  "s_nop 1\n" \
  "s_nop 1\n" \
  "s_nop 1\n" \
  "s_nop 1\n" \
  "s_nop 1\n" \
  "s_nop 1\n" \
  "s_nop 1\n" \
  "s_nop 1\n" \
  "s_nop 1\n" \
  ".LR_b2_%=:\n" \
  "buffer_load_dwordx4 v[0:3], v215, s[40:43], s54 offen sc1\n" \
  "s_memtime s[92:93]\n" \
  "s_cmp_lg_u32 s96, 0\n" \
  "buffer_load_dwordx4 v[4:7], v215, s[40:43], s54 offen offset:1024 sc1\n" \
  "buffer_load_dwordx4 v[8:11], v215, s[40:43], s54 offen offset:2048 sc1\n" \
  "buffer_load_dwordx4 v[12:15], v215, s[40:43], s54 offen offset:3072 sc1\n" \
  "buffer_load_dwordx4 v[16:19], v208, s[40:43], s54 offen sc1\n" \
  "buffer_load_dwordx4 v[20:23], v208, s[40:43], s54 offen offset:1024 sc1\n" \
  "buffer_load_dwordx4 v[24:27], v208, s[40:43], s54 offen offset:2048 sc1\n" \
  "buffer_load_dwordx4 v[28:31], v208, s[40:43], s54 offen offset:3072 sc1\n" \
  "buffer_load_dwordx4 v[32:35], v209, s[40:43], s54 offen sc1\n" \
  "buffer_load_dwordx4 v[36:39], v209, s[40:43], s54 offen offset:1024 sc1\n" \
  "buffer_load_dwordx4 v[40:43], v209, s[40:43], s54 offen offset:2048 sc1\n" \
  "buffer_load_dwordx4 v[44:47], v209, s[40:43], s54 offen offset:3072 sc1\n" \
  "buffer_load_dwordx4 v[48:51], v210, s[40:43], s54 offen sc1\n" \
  "buffer_load_dwordx4 v[52:55], v210, s[40:43], s54 offen offset:1024 sc1\n" \
  "buffer_load_dwordx4 v[56:59], v210, s[40:43], s54 offen offset:2048 sc1\n" \
  "buffer_load_dwordx4 v[60:63], v210, s[40:43], s54 offen offset:3072 sc1\n" \
  "buffer_load_dwordx4 v[128:131], v214, s[40:43], s49 offen sc1\n" \
  "buffer_load_dwordx4 v[132:135], v214, s[40:43], s49 offen offset:1024 sc1\n" \
  "buffer_load_dwordx4 v[136:139], v214, s[40:43], s49 offen offset:2048 sc1\n" \
  "buffer_load_dwordx4 v[140:143], v214, s[40:43], s49 offen offset:3072 sc1\n" \
  "buffer_load_dwordx4 v[144:147], v214, s[40:43], s58 offen sc1\n" \
  "buffer_load_dwordx4 v[148:151], v214, s[40:43], s58 offen offset:1024 sc1\n" \
  "buffer_load_dwordx4 v[152:155], v214, s[40:43], s58 offen offset:2048 sc1\n" \
  "buffer_load_dwordx4 v[156:159], v214, s[40:43], s58 offen offset:3072 sc1\n" \
  "buffer_load_dwordx4 v[204:207], v215, s[40:43], s54 offen sc1\n" \
  "buffer_load_dwordx4 v[204:207], v215, s[40:43], s54 offen sc1\n" \
  "buffer_load_dwordx4 v[204:207], v215, s[40:43], s54 offen sc1\n" \
  "buffer_load_dwordx4 v[160:163], v216, s[40:43], s54 offen sc1\n" \
  "buffer_load_dwordx4 v[164:167], v216, s[40:43], s54 offen offset:1024 sc1\n" \
  "buffer_load_dwordx4 v[168:171], v216, s[40:43], s54 offen offset:2048 sc1\n" \
  "buffer_load_dwordx4 v[172:175], v216, s[40:43], s54 offen offset:3072 sc1\n" \
  "buffer_load_dwordx4 v[176:179], v216, s[40:43], s94 offen sc1\n" \
  "buffer_load_dwordx4 v[180:183], v216, s[40:43], s94 offen offset:1024 sc1\n" \
  "buffer_load_dwordx4 v[184:187], v216, s[40:43], s94 offen offset:2048 sc1\n" \
  "buffer_load_dwordx4 v[188:191], v216, s[40:43], s94 offen offset:3072 sc1\n" \
  "s_cbranch_scc1 .LR_nb_%=\n" \
  "s_barrier\n" \
  ".LR_nb_%=:\n" \
  "s_waitcnt vmcnt(34)\n" \
  "v_smfmac_f32_16x16x64_bf16 v[64:67], v[0:3], a[0:7], v212\n" \
  "s_waitcnt vmcnt(33)\n" \
  "s_nop 1\n" \
  "v_smfmac_f32_16x16x64_bf16 v[68:71], v[4:7], a[8:15], v212\n" \
  "s_waitcnt vmcnt(32)\n" \
  "s_nop 1\n" \
  "v_smfmac_f32_16x16x64_bf16 v[72:75], v[8:11], a[16:23], v212\n" \
  "s_waitcnt vmcnt(31)\n" \
  "v_cvt_pk_bf16_f32 v64, v64, v65\n" \
  "v_cvt_pk_bf16_f32 v65, v66, v67\n" \
  "v_smfmac_f32_16x16x64_bf16 v[76:79], v[12:15], a[24:31], v212\n" \
  "s_waitcnt vmcnt(30)\n" \
  "v_cvt_pk_bf16_f32 v66, v68, v69\n" \
  "v_cvt_pk_bf16_f32 v67, v70, v71\n" \
  "v_smfmac_f32_16x16x64_bf16 v[80:83], v[16:19], a[32:39], v212\n" \
  "s_waitcnt vmcnt(29)\n" \
  "v_cvt_pk_bf16_f32 v68, v72, v73\n" \
  "v_cvt_pk_bf16_f32 v69, v74, v75\n" \
  "v_smfmac_f32_16x16x64_bf16 v[84:87], v[20:23], a[40:47], v212\n" \
  "s_waitcnt vmcnt(28)\n" \
  "v_cvt_pk_bf16_f32 v70, v76, v77\n" \
  "v_cvt_pk_bf16_f32 v71, v78, v79\n" \
  "v_smfmac_f32_16x16x64_bf16 v[88:91], v[24:27], a[48:55], v212\n" \
  "s_waitcnt vmcnt(27)\n" \
  "v_cvt_pk_bf16_f32 v80, v80, v81\n" \
  "v_cvt_pk_bf16_f32 v81, v82, v83\n" \
  "v_smfmac_f32_16x16x64_bf16 v[92:95], v[28:31], a[56:63], v212\n" \
  "s_waitcnt vmcnt(26)\n" \
  "v_cvt_pk_bf16_f32 v82, v84, v85\n" \
  "v_cvt_pk_bf16_f32 v83, v86, v87\n" \
  "v_smfmac_f32_16x16x64_bf16 v[96:99], v[32:35], a[64:71], v212\n" \
  "s_waitcnt vmcnt(25)\n" \
  "v_cvt_pk_bf16_f32 v84, v88, v89\n" \
  "v_cvt_pk_bf16_f32 v85, v90, v91\n" \
  "v_smfmac_f32_16x16x64_bf16 v[100:103], v[36:39], a[72:79], v212\n" \
  "s_waitcnt vmcnt(24)\n" \
  "v_cvt_pk_bf16_f32 v86, v92, v93\n" \
  "v_cvt_pk_bf16_f32 v87, v94, v95\n" \
  "v_smfmac_f32_16x16x64_bf16 v[104:107], v[40:43], a[80:87], v212\n" \
  "s_waitcnt vmcnt(23)\n" \
  "v_cvt_pk_bf16_f32 v96, v96, v97\n" \
  "v_cvt_pk_bf16_f32 v97, v98, v99\n" \
  "v_smfmac_f32_16x16x64_bf16 v[108:111], v[44:47], a[88:95], v212\n" \
  "s_waitcnt vmcnt(22)\n" \
  "v_cvt_pk_bf16_f32 v98, v100, v101\n" \
  "v_cvt_pk_bf16_f32 v99, v102, v103\n" \
  "v_smfmac_f32_16x16x64_bf16 v[112:115], v[48:51], a[96:103], v212\n" \
  "s_waitcnt vmcnt(21)\n" \
  "v_cvt_pk_bf16_f32 v100, v104, v105\n" \
  "v_cvt_pk_bf16_f32 v101, v106, v107\n" \
  "v_smfmac_f32_16x16x64_bf16 v[116:119], v[52:55], a[104:111], v212\n" \
  "s_waitcnt vmcnt(20)\n" \
  "v_cvt_pk_bf16_f32 v102, v108, v109\n" \
  "v_cvt_pk_bf16_f32 v103, v110, v111\n" \
  "v_smfmac_f32_16x16x64_bf16 v[120:123], v[56:59], a[112:119], v212\n" \
  "s_waitcnt vmcnt(19)\n" \
  "v_cvt_pk_bf16_f32 v112, v112, v113\n" \
  "v_cvt_pk_bf16_f32 v113, v114, v115\n" \
  "v_smfmac_f32_16x16x64_bf16 v[124:127], v[60:63], a[120:127], v212\n" \
  "s_mov_b32 s96, 1\n" \
  "v_cvt_pk_bf16_f32 v114, v116, v117\n" \
  "v_cvt_pk_bf16_f32 v115, v118, v119\n" \
  "s_nop 0\n" \
  "v_cvt_pk_bf16_f32 v116, v120, v121\n" \
  "v_cvt_pk_bf16_f32 v117, v122, v123\n" \
  "s_nop 1\n" \
  "v_cvt_pk_bf16_f32 v118, v124, v125\n" \
  "v_cvt_pk_bf16_f32 v119, v126, v127\n" \
  "s_waitcnt vmcnt(18)\n" \
  "v_permlane16_swap_b32 v128, v129\n" \
  "v_permlane16_swap_b32 v130, v131\n" \
  "s_waitcnt vmcnt(17)\n" \
  "v_permlane16_swap_b32 v132, v133\n" \
  "v_permlane16_swap_b32 v134, v135\n" \
  "s_waitcnt vmcnt(16)\n" \
  "v_permlane16_swap_b32 v136, v137\n" \
  "v_permlane16_swap_b32 v138, v139\n" \
  "s_waitcnt vmcnt(15)\n" \
  "v_permlane16_swap_b32 v140, v141\n" \
  "v_permlane16_swap_b32 v142, v143\n" \
  "s_waitcnt vmcnt(14)\n" \
  "v_permlane16_swap_b32 v144, v145\n" \
  "v_permlane16_swap_b32 v146, v147\n" \
  "s_waitcnt vmcnt(13)\n" \
  "v_permlane16_swap_b32 v148, v149\n" \
  "v_permlane16_swap_b32 v150, v151\n" \
  "s_waitcnt vmcnt(12)\n" \
  "v_permlane16_swap_b32 v152, v153\n" \
  "v_permlane16_swap_b32 v154, v155\n" \
  "s_waitcnt vmcnt(11)\n" \
  "v_permlane16_swap_b32 v156, v157\n" \
  "v_permlane16_swap_b32 v158, v159\n" \
  "s_nop 0\n" \
  "s_waitcnt vmcnt(7)\n" \
  "v_smfmac_f32_16x16x64_bf16 v[192:195], v[160:163], v[128:135], v212\n" \
  "s_waitcnt vmcnt(6)\n" \
  "v_smfmac_f32_16x16x64_bf16 v[192:195], v[164:167], v[136:143], v212\n" \
  "s_waitcnt vmcnt(5)\n" \
  "v_smfmac_f32_16x16x64_bf16 v[192:195], v[168:171], v[144:151], v212\n" \
  "s_waitcnt vmcnt(4)\n" \
  "v_smfmac_f32_16x16x64_bf16 v[192:195], v[172:175], v[152:159], v212\n" \
  "s_waitcnt vmcnt(3)\n" \
  "v_smfmac_f32_16x16x64_bf16 v[192:195], v[176:179], v[64:71], v213\n" \
  "s_waitcnt vmcnt(2)\n" \
  "v_smfmac_f32_16x16x64_bf16 v[192:195], v[180:183], v[80:87], v213\n" \
  "s_waitcnt vmcnt(1)\n" \
  "v_smfmac_f32_16x16x64_bf16 v[192:195], v[184:187], v[96:103], v213\n" \
  "s_waitcnt vmcnt(0)\n" \
  "v_smfmac_f32_16x16x64_bf16 v[192:195], v[188:191], v[112:119], v213\n" \
  "s_nop 7\n" \
  "v_permlane32_swap_b32 v192, v194\n" \
  "v_permlane32_swap_b32 v193, v195\n" \
  "v_add_f32 v192, v192, v194\n" \
  "v_add_f32 v193, v193, v195\n" \
  "v_cvt_pk_bf16_f32 v194, v192, v193\n" \
  "v_cmp_u_f32 vcc, v192, v193\n" \
  "s_nop 0\n" \
  "v_mov_b32_dpp v195, v194 quad_perm:[1,0,3,2] row_mask:0xf bank_mask:0xf\n" \
  "v_perm_b32 v194, v195, v194, v218\n" \
  "s_cbranch_vccnz .LR_bad_%=\n" \
  ".LR_good_%=:\n" \
  "buffer_store_dword v194, v217, s[40:43], s52 offen sc1\n" \
  "buffer_store_dword v194, v217, s[40:43], s64 offen sc1\n" \
  "buffer_store_dword v194, v217, s[40:43], s65 offen sc1\n" \
  "buffer_store_dword v194, v217, s[40:43], s67 offen sc1\n" \
  "s_memrealtime s[98:99]\n" \
  "s_memtime s[84:85]\n" \
  "s_waitcnt lgkmcnt(0)\n" \
  "s_sub_u32 s60, s84, s92\n" \
  "s_cmp_lt_u32 s60, 4000\n" \
  "s_cselect_b32 s60, s60, 4194304\n" \
  "s_cmp_eq_u32 s70, 0\n" \
  "s_cselect_b32 s60, s60, 4194304\n" \
  "s_min_u32 s66, s66, s60\n" \
  "s_lshl_b32 s60, s66, 10\n" \
  "s_cselect_b32 s89, %30, 2130706432\n" \
  "s_or_b32 s60, s60, s87\n" \
  "v_mov_b32 v198, s60\n" \
  "s_and_b32 s61, s48, 3\n" \
  "s_cmp_eq_u32 s61, 0\n" \
  "s_cselect_b32 s89, s89, 2130706432\n" \
  "s_cselect_b32 s66, 4194304, s66\n" \
  "s_cmp_eq_u32 s48, 0\n" \
  "s_cbranch_scc1 .LR_go0_%=\n" \
  "s_cmp_lg_u32 %26, 0\n" \
  "s_cbranch_scc1 .LR_extra_%=\n" \
  ".LR_extra_b_%=:\n" \
  "s_xor_b32 s51, s51, s56\n" \
  "s_xor_b32 s49, s49, s56\n" \
  "s_xor_b32 s58, s58, s56\n" \
  "s_xor_b32 s52, s52, s57\n" \
  "s_xor_b32 s64, s64, s57\n" \
  "s_xor_b32 s65, s65, s57\n" \
  "s_xor_b32 s67, s67, s57\n" \
  "s_xor_b32 s71, s71, s57\n" \
  "s_mov_b32 s60, s56\n" \
  "s_mov_b32 s56, s57\n" \
  "s_mov_b32 s57, s60\n" \
  ".LR_go_%=:\n" \
  "s_add_u32 s48, s48, 1\n" \
  "s_cmp_lt_u32 s48, 64\n" \
  "s_cbranch_scc1 .LR_loop_%=\n" \
  "s_branch .LR_end_%=\n" \
  ".LR_tw_re_%=:\n" \
  "s_add_u32 s69, s69, 1\n" \
  "s_cmp_gt_u32 s69, 64\n" \
  "s_cbranch_scc1 .LR_now_%=\n" \
  "buffer_load_dwordx4 v[200:203], off, s[40:43], s95 sc1\n" \
  "s_waitcnt vmcnt(0)\n" \
  "s_branch .LR_tw_%=\n" \
  ".LR_now_%=:\n" \
  "s_waitcnt lgkmcnt(0)\n" \
  "s_branch .LR_b2_%=\n" \
  ".LR_near_%=:\n" \
  "s_cmp_ge_u32 s62, 14\n" \
  "s_cbranch_scc1 .LR_lat_%=\n" \
  "s_cmp_le_u32 s62, 4\n" \
  "s_cbranch_scc1 .LR_b2_%=\n" \
  "s_memrealtime s[60:61]\n" \
  "s_waitcnt lgkmcnt(0)\n" \
  "s_sub_u32 s62, s53, s60\n" \
  "s_branch .LR_near_%=\n" \
  ".LR_lat_%=:\n" \
  "s_lshr_b32 s63, %29, 16\n" \
  "s_add_u32 s62, s62, s63\n" \
  "s_cmp_lt_u32 s62, 4294937296\n" \
  "s_cbranch_scc1 .LR_b2_%=\n" \
  "s_and_b32 s63, %29, 0xffff\n" \
  ".LR_lat1_%=:\n" \
  "s_add_u32 s44, s44, s63\n" \
  "s_add_u32 s62, s62, s63\n" \
  "s_cbranch_scc0 .LR_lat1_%=\n" \
  "s_branch .LR_again_%=\n" \
  ".LR_mv_%=:\n" \
  "s_sub_u32 s76, s77, s44\n" \
  "s_cmp_gt_u32 s76, 2000\n" \
  "s_cbranch_scc1 .LR_b2_%=\n" \
  "s_mov_b32 s44, s77\n" \
  "s_branch .LR_again_%=\n" \
  ".LR_go0_%=:\n" \
  "s_xor_b32 s51, s51, 0x40000000\n" \
  "s_xor_b32 s49, s49, 0x40000000\n" \
  "s_xor_b32 s58, s58, 0x40000000\n" \
  "s_xor_b32 s47, s47, 0x40000000\n" \
  "s_xor_b32 s52, s52, 0x40000000\n" \
  "s_xor_b32 s64, s64, 0x40000000\n" \
  "s_xor_b32 s65, s65, 0x40000000\n" \
  "s_xor_b32 s67, s67, 0x40000000\n" \
  "s_mov_b32 s44, 0\n" \
  "s_mov_b32 s89, 2130706432\n" \
  "s_mov_b32 s87, 340\n" \
  "s_mov_b32 s66, 4194304\n" \
  "s_add_u32 s95, s47, 16\n" \
  "s_mov_b32 s69, 0\n" \
  ".LR_go1_%=:\n" \
  "buffer_load_dwordx4 v[200:203], off, s[40:43], s95 sc1\n" \
  "s_add_u32 s69, s69, 1\n" \
  "s_waitcnt vmcnt(0)\n" \
  "v_readfirstlane_b32 s60, v200\n" \
  "s_sleep 1\n" \
  "s_cmp_gt_u32 s69, 200\n" \
  "s_cselect_b32 s60, 1, s60\n" \
  "s_cmp_lg_u32 s60, 1\n" \
  "s_cbranch_scc1 .LR_go1_%=\n" \
  "s_branch .LR_go_%=\n" \
  ".LR_extra_%=:\n" \
  "s_cmp_lg_u32 s61, 1\n" \
  "s_cbranch_scc1 .LR_extra_b_%=\n" \
  "s_lshl_b32 s62, s48, 5\n" \
  "s_add_u32 s62, s62, %26\n" \
  "s_add_u32 s60, s44, 1\n" \
  "v_mov_b32 v196, s60\n" \
  "v_mov_b32 v197, %21\n" \
  "s_mov_b64 exec, 1\n" \
  "buffer_store_dwordx2 v[196:197], off, s[40:43], s62 sc1\n" \
  "s_mov_b64 exec, -1\n" \
  "s_branch .LR_extra_b_%=\n" \
  ".LR_bad_%=:\n" \
  "s_cmp_eq_u32 s48, 0\n" \
  "s_cbranch_scc1 .LR_good_%=\n" \
  "s_add_u32 s70, s70, 1\n" \
  "s_cmp_gt_u32 s70, 5\n" \
  "s_cbranch_scc1 .LR_bad1_%=\n" \
  ".LR_redo_%=:\n" \
  "s_and_b32 s62, %29, 0xffff\n" \
  "s_add_u32 s44, s44, s62\n" \
  "v_mov_b32 v196, s44\n" \
  "v_xor_b32 v197, s71, v221\n" \
  "s_xor_b32 s54, s54, s46\n" \
  "s_xor_b32 s94, s94, s46\n" \
  "v_mov_b64 v[64:65], 0\n" \
  "v_mov_b64 v[66:67], 0\n" \
  "v_mov_b64 v[68:69], 0\n" \
  "v_mov_b64 v[70:71], 0\n" \
  "v_mov_b64 v[72:73], 0\n" \
  "v_mov_b64 v[74:75], 0\n" \
  "v_mov_b64 v[76:77], 0\n" \
  "v_mov_b64 v[78:79], 0\n" \
  "v_mov_b64 v[80:81], 0\n" \
  "v_mov_b64 v[82:83], 0\n" \
  "v_mov_b64 v[84:85], 0\n" \
  "v_mov_b64 v[86:87], 0\n" \
  "v_mov_b64 v[88:89], 0\n" \
  "v_mov_b64 v[90:91], 0\n" \
  "v_mov_b64 v[92:93], 0\n" \
  "v_mov_b64 v[94:95], 0\n" \
  "v_mov_b64 v[96:97], 0\n" \
  "v_mov_b64 v[98:99], 0\n" \
  "v_mov_b64 v[100:101], 0\n" \
  "v_mov_b64 v[102:103], 0\n" \
  "v_mov_b64 v[104:105], 0\n" \
  "v_mov_b64 v[106:107], 0\n" \
  "v_mov_b64 v[108:109], 0\n" \
  "v_mov_b64 v[110:111], 0\n" \
  "v_mov_b64 v[112:113], 0\n" \
  "v_mov_b64 v[114:115], 0\n" \
  "v_mov_b64 v[116:117], 0\n" \
  "v_mov_b64 v[118:119], 0\n" \
  "v_mov_b64 v[120:121], 0\n" \
  "v_mov_b64 v[122:123], 0\n" \
  "v_mov_b64 v[124:125], 0\n" \
  "v_mov_b64 v[126:127], 0\n" \
  "v_mov_b64 v[192:193], 0\n" \
  "v_mov_b64 v[194:195], 0\n" \
  "s_branch .LR_again_%=\n" \
  ".LR_bad1_%=:\n" \
  "v_max_u32 v196, v0, v1\n" \
  "v_max3_u32 v196, v196, v2, v3\n" \
  "v_max3_u32 v196, v196, v4, v5\n" \
  "v_max3_u32 v196, v196, v6, v7\n" \
  "v_max3_u32 v196, v196, v8, v9\n" \
  "v_max3_u32 v196, v196, v10, v11\n" \
  "v_max3_u32 v196, v196, v12, v13\n" \
  "v_max3_u32 v196, v196, v14, v15\n" \
  "v_max3_u32 v196, v196, v16, v17\n" \
  "v_max3_u32 v196, v196, v18, v19\n" \
  "v_max3_u32 v196, v196, v20, v21\n" \
  "v_max3_u32 v196, v196, v22, v23\n" \
  "v_max3_u32 v196, v196, v24, v25\n" \
  "v_max3_u32 v196, v196, v26, v27\n" \
  "v_max3_u32 v196, v196, v28, v29\n" \
  "v_max3_u32 v196, v196, v30, v31\n" \
  "v_max3_u32 v196, v196, v32, v33\n" \
  "v_max3_u32 v196, v196, v34, v35\n" \
  "v_max3_u32 v196, v196, v36, v37\n" \
  "v_max3_u32 v196, v196, v38, v39\n" \
  "v_max3_u32 v196, v196, v40, v41\n" \
  "v_max3_u32 v196, v196, v42, v43\n" \
  "v_max3_u32 v196, v196, v44, v45\n" \
  "v_max3_u32 v196, v196, v46, v47\n" \
  "v_max3_u32 v196, v196, v48, v49\n" \
  "v_max3_u32 v196, v196, v50, v51\n" \
  "v_max3_u32 v196, v196, v52, v53\n" \
  "v_max3_u32 v196, v196, v54, v55\n" \
  "v_max3_u32 v196, v196, v56, v57\n" \
  "v_max3_u32 v196, v196, v58, v59\n" \
  "v_max3_u32 v196, v196, v60, v61\n" \
  "v_max3_u32 v196, v196, v62, v63\n" \
  "v_max_u32 v197, v128, v129\n" \
  "v_max3_u32 v197, v197, v130, v131\n" \
  "v_max3_u32 v197, v197, v132, v133\n" \
  "v_max3_u32 v197, v197, v134, v135\n" \
  "v_max3_u32 v197, v197, v136, v137\n" \
  "v_max3_u32 v197, v197, v138, v139\n" \
  "v_max3_u32 v197, v197, v140, v141\n" \
  "v_max3_u32 v197, v197, v142, v143\n" \
  "v_max3_u32 v197, v197, v144, v145\n" \
  "v_max3_u32 v197, v197, v146, v147\n" \
  "v_max3_u32 v197, v197, v148, v149\n" \
  "v_max3_u32 v197, v197, v150, v151\n" \
  "v_max3_u32 v197, v197, v152, v153\n" \
  "v_max3_u32 v197, v197, v154, v155\n" \
  "v_max3_u32 v197, v197, v156, v157\n" \
  "v_max3_u32 v197, v197, v158, v159\n" \
  "v_max_u32 v204, v160, v161\n" \
  "v_max3_u32 v204, v204, v162, v163\n" \
  "v_max3_u32 v204, v204, v164, v165\n" \
  "v_max3_u32 v204, v204, v166, v167\n" \
  "v_max3_u32 v204, v204, v168, v169\n" \
  "v_max3_u32 v204, v204, v170, v171\n" \
  "v_max3_u32 v204, v204, v172, v173\n" \
  "v_max3_u32 v204, v204, v174, v175\n" \
  "v_max3_u32 v204, v204, v176, v177\n" \
  "v_max3_u32 v204, v204, v178, v179\n" \
  "v_max3_u32 v204, v204, v180, v181\n" \
  "v_max3_u32 v204, v204, v182, v183\n" \
  "v_max3_u32 v204, v204, v184, v185\n" \
  "v_max3_u32 v204, v204, v186, v187\n" \
  "v_max3_u32 v204, v204, v188, v189\n" \
  "v_max3_u32 v204, v204, v190, v191\n" \
  "v_max3_u32 v196, v196, v197, v204\n" \
  "v_cmp_eq_u32 vcc, -1, v196\n" \
  "s_cbranch_vccz .LR_nan_%=\n" \
  "s_add_u32 s88, s88, 1\n" \
  "v_mov_b32 v196, s88\n" \
  "v_mov_b32 v197, %21\n" \
  "buffer_store_dwordx2 v[196:197], off, s[40:43], %31 sc1\n" \
  "s_waitcnt vmcnt(0)\n" \
  "s_and_b32 s63, s70, 255\n" \
  "s_cbranch_scc1 .LR_redo_%=\n" \
  "buffer_load_dwordx2 v[196:197], off, s[40:43], %27 offset:128 sc1\n" \
  "s_waitcnt vmcnt(0)\n" \
  "v_readfirstlane_b32 s63, v197\n" \
  "s_cmp_eq_u32 s63, %21\n" \
  "s_cbranch_scc1 .LR_nan_%=\n" \
  "s_cmp_lt_u32 s70, 262144\n" \
  "s_cbranch_scc1 .LR_redo_%=\n" \
  "v_mov_b32 v196, 1\n" \
  "v_mov_b32 v197, %21\n" \
  "buffer_store_dwordx2 v[196:197], off, s[40:43], %27 offset:128 sc1\n" \
  ".LR_nan_%=:\n" \
  "v_min_u32 v194, -2, v194\n" \
  "s_branch .LR_good_%=\n" \
  ".LR_fu_%=:\n" \
  "s_add_u32 s60, s48, 0\n" \
  "s_lshl_b32 s62, s60, 7\n" \
  "s_add_u32 s62, s62, %33\n" \
  "v_mov_b32 v146, s62\n" \
  "s_sub_u32 s60, s60, 0\n" \
  "s_cmp_lt_u32 s60, 10\n" \
  "s_cselect_b64 s[76:77], -1, 0\n" \
  "v_cndmask_b32 v146, v220, v146, s[76:77]\n" \
  "buffer_load_dwordx2 v[140:141], v146, s[40:43], 0 offen sc1\n" \
  "s_waitcnt vmcnt(0)\n" \
  "v_cmp_ne_u32 vcc, %21, v141\n" \
  "s_nop 1\n" \
  "v_cndmask_b32 v140, v140, 0, vcc\n" \
  "v_max_i32 v140, v140, v218\n" \
  "v_cmp_ge_i32 vcc, s48, v140\n" \
  "s_cbranch_vccnz .LR_fu0_%=\n" \
  "s_cmp_lg_u32 %19, 3\n" \
  "s_cbranch_scc1 .LR_h8_%=\n" \
  "s_xor_b32 s60, s61, s75\n" \
  "buffer_load_dwordx4 v[40:43], v214, s[40:43], s60 offen sc1\n" \
  "s_xor_b32 s60, s60, 1024\n" \
  "buffer_load_dwordx4 v[44:47], v214, s[40:43], s60 offen sc1\n" \
  ".LR_h8_%=:\n" \
  "s_xor_b32 s60, s61, s64\n" \
  "buffer_load_dwordx4 v[0:3], v214, s[40:43], s60 offen sc1\n" \
  "s_xor_b32 s60, s60, 1024\n" \
  "buffer_load_dwordx4 v[4:7], v214, s[40:43], s60 offen sc1\n" \
  "s_xor_b32 s60, s61, s65\n" \
  "buffer_load_dwordx4 v[8:11], v214, s[40:43], s60 offen sc1\n" \
  "s_xor_b32 s60, s60, 1024\n" \
  "buffer_load_dwordx4 v[12:15], v214, s[40:43], s60 offen sc1\n" \
  "s_xor_b32 s60, s61, s67\n" \
  "buffer_load_dwordx4 v[16:19], v214, s[40:43], s60 offen sc1\n" \
  "s_xor_b32 s60, s60, 1024\n" \
  "buffer_load_dwordx4 v[20:23], v214, s[40:43], s60 offen sc1\n" \
  "s_cmp_lt_u32 s48, 62\n" \
  "s_cselect_b32 s60, s71, 2130706432\n" \
  "v_xor_b32 v142, s60, v221\n" \
  "v_xor_b32 v143, s60, v222\n" \
  "v_xor_b32 v144, s60, v223\n" \
  "buffer_store_dwordx4 v[136:139], v142, s[40:43], 0 offen sc1\n" \
  "buffer_store_dwordx4 v[136:139], v143, s[40:43], 0 offen sc1\n" \
  "buffer_store_dwordx4 v[136:139], v144, s[40:43], 0 offen sc1\n" \
  "s_barrier\n" \
  "s_xor_b32 s60, s61, s68\n" \
  "buffer_load_dwordx4 v[24:27], v214, s[40:43], s60 offen sc1\n" \
  "s_xor_b32 s60, s60, 1024\n" \
  "buffer_load_dwordx4 v[28:31], v214, s[40:43], s60 offen sc1\n" \
  "s_xor_b32 s60, s61, s74\n" \
  "buffer_load_dwordx4 v[32:35], v214, s[40:43], s60 offen sc1\n" \
  "s_xor_b32 s60, s60, 1024\n" \
  "buffer_load_dwordx4 v[36:39], v214, s[40:43], s60 offen sc1\n" \
  "s_add_u32 s60, s48, 1\n" \
  "s_lshl_b32 s62, s60, 7\n" \
  "s_add_u32 s62, s62, %33\n" \
  "v_mov_b32 v146, s62\n" \
  "s_sub_u32 s60, s60, 0\n" \
  "s_cmp_lt_u32 s60, 10\n" \
  "s_cselect_b64 s[76:77], -1, 0\n" \
  "v_cndmask_b32 v146, v220, v146, s[76:77]\n" \
  "buffer_load_dwordx2 v[140:141], v146, s[40:43], 0 offen sc1\n" \
  "s_waitcnt vmcnt(0)\n" \
  "v_mov_b32 v72, v24\n" \
  "v_mov_b32 v73, v28\n" \
  "v_mov_b32 v74, v25\n" \
  "v_mov_b32 v75, v29\n" \
  "v_mov_b32 v76, v26\n" \
  "v_mov_b32 v77, v30\n" \
  "v_mov_b32 v78, v27\n" \
  "v_mov_b32 v79, v31\n" \
  "ds_write_b128 v211, v[72:75] offset:6144\n" \
  "ds_write_b128 v211, v[76:79] offset:7168\n" \
  "v_mov_b32 v80, v32\n" \
  "v_mov_b32 v81, v36\n" \
  "v_mov_b32 v82, v33\n" \
  "v_mov_b32 v83, v37\n" \
  "v_mov_b32 v84, v34\n" \
  "v_mov_b32 v85, v38\n" \
  "v_mov_b32 v86, v35\n" \
  "v_mov_b32 v87, v39\n" \
  "ds_write_b128 v211, v[80:83] offset:8192\n" \
  "ds_write_b128 v211, v[84:87] offset:9216\n" \
  "s_branch .LR_fu_b_%=\n" \
  ".LR_fu0_%=:\n" \
  "s_cmp_lt_u32 s48, 62\n" \
  "s_cselect_b32 s60, s71, 2130706432\n" \
  "v_xor_b32 v142, s60, v221\n" \
  "v_xor_b32 v143, s60, v222\n" \
  "v_xor_b32 v144, s60, v223\n" \
  "buffer_store_dwordx4 v[136:139], v142, s[40:43], 0 offen sc1\n" \
  "buffer_store_dwordx4 v[136:139], v143, s[40:43], 0 offen sc1\n" \
  "buffer_store_dwordx4 v[136:139], v144, s[40:43], 0 offen sc1\n" \
  "s_barrier\n" \
  "s_sleep 0\n" \
  "s_mov_b32 s69, 0\n" \
  ".LR_fu1_%=:\n" \
  "s_add_u32 s60, s48, 0\n" \
  "s_lshl_b32 s62, s60, 7\n" \
  "s_add_u32 s62, s62, %33\n" \
  "v_mov_b32 v146, s62\n" \
  "s_sub_u32 s60, s60, 0\n" \
  "s_cmp_lt_u32 s60, 10\n" \
  "s_cselect_b64 s[76:77], -1, 0\n" \
  "v_cndmask_b32 v146, v220, v146, s[76:77]\n" \
  "buffer_load_dwordx2 v[140:141], v146, s[40:43], 0 offen sc1\n" \
  "s_waitcnt vmcnt(0)\n" \
  "v_cmp_ne_u32 vcc, %21, v141\n" \
  "s_nop 1\n" \
  "v_cndmask_b32 v140, v140, 0, vcc\n" \
  "v_max_i32 v140, v140, v218\n" \
  "v_cmp_ge_i32 vcc, s48, v140\n" \
  "s_cbranch_vccz .LR_fu_x_%=\n" \
  "s_sleep 2\n" \
  "s_add_u32 s69, s69, 1\n" \
  "s_and_b32 s63, s69, 1023\n" \
  "s_cbranch_scc1 .LR_fu1_%=\n" \
  "s_cmp_gt_u32 s69, 2097152\n" \
  "s_cbranch_scc1 .LR_fu1_set_%=\n" \
  "buffer_load_dwordx2 v[196:197], off, s[40:43], %27 offset:128 sc1\n" \
  "s_waitcnt vmcnt(0)\n" \
  "v_readfirstlane_b32 s63, v197\n" \
  "s_cmp_eq_u32 s63, %21\n" \
  "s_cbranch_scc0 .LR_fu1_%=\n" \
  "s_branch .LR_fu_x_%=\n" \
  ".LR_fu1_set_%=:\n" \
  "v_mov_b32 v196, 1\n" \
  "v_mov_b32 v197, %21\n" \
  "buffer_store_dwordx2 v[196:197], off, s[40:43], %27 offset:128 sc1\n" \
  "s_waitcnt vmcnt(0)\n" \
  "s_branch .LR_fu_x_%=\n" \
  ".LR_fu_x_%=:\n" \
  "s_xor_b32 s60, s61, s68\n" \
  "buffer_load_dwordx4 v[24:27], v214, s[40:43], s60 offen sc1\n" \
  "s_xor_b32 s60, s60, 1024\n" \
  "buffer_load_dwordx4 v[28:31], v214, s[40:43], s60 offen sc1\n" \
  "s_xor_b32 s60, s61, s74\n" \
  "buffer_load_dwordx4 v[32:35], v214, s[40:43], s60 offen sc1\n" \
  "s_xor_b32 s60, s60, 1024\n" \
  "buffer_load_dwordx4 v[36:39], v214, s[40:43], s60 offen sc1\n" \
  "s_cmp_lg_u32 %19, 3\n" \
  "s_cbranch_scc1 .LR_h7_%=\n" \
  "s_xor_b32 s60, s61, s75\n" \
  "buffer_load_dwordx4 v[40:43], v214, s[40:43], s60 offen sc1\n" \
  "s_xor_b32 s60, s60, 1024\n" \
  "buffer_load_dwordx4 v[44:47], v214, s[40:43], s60 offen sc1\n" \
  ".LR_h7_%=:\n" \
  "s_xor_b32 s60, s61, s64\n" \
  "buffer_load_dwordx4 v[0:3], v214, s[40:43], s60 offen sc1\n" \
  "s_xor_b32 s60, s60, 1024\n" \
  "buffer_load_dwordx4 v[4:7], v214, s[40:43], s60 offen sc1\n" \
  "s_xor_b32 s60, s61, s65\n" \
  "buffer_load_dwordx4 v[8:11], v214, s[40:43], s60 offen sc1\n" \
  "s_xor_b32 s60, s60, 1024\n" \
  "buffer_load_dwordx4 v[12:15], v214, s[40:43], s60 offen sc1\n" \
  "s_xor_b32 s60, s61, s67\n" \
  "buffer_load_dwordx4 v[16:19], v214, s[40:43], s60 offen sc1\n" \
  "s_xor_b32 s60, s60, 1024\n" \
  "buffer_load_dwordx4 v[20:23], v214, s[40:43], s60 offen sc1\n" \
  "s_add_u32 s60, s48, 1\n" \
  "s_lshl_b32 s62, s60, 7\n" \
  "s_add_u32 s62, s62, %33\n" \
  "v_mov_b32 v146, s62\n" \
  "s_sub_u32 s60, s60, 0\n" \
  "s_cmp_lt_u32 s60, 10\n" \
  "s_cselect_b64 s[76:77], -1, 0\n" \
  "v_cndmask_b32 v146, v220, v146, s[76:77]\n" \
  "buffer_load_dwordx2 v[140:141], v146, s[40:43], 0 offen sc1\n" \
  "s_waitcnt vmcnt(7)\n" \
  "v_mov_b32 v72, v24\n" \
  "v_mov_b32 v73, v28\n" \
  "v_mov_b32 v74, v25\n" \
  "v_mov_b32 v75, v29\n" \
  "v_mov_b32 v76, v26\n" \
  "v_mov_b32 v77, v30\n" \
  "v_mov_b32 v78, v27\n" \
  "v_mov_b32 v79, v31\n" \
  "ds_write_b128 v211, v[72:75] offset:6144\n" \
  "ds_write_b128 v211, v[76:79] offset:7168\n" \
  "v_mov_b32 v80, v32\n" \
  "v_mov_b32 v81, v36\n" \
  "v_mov_b32 v82, v33\n" \
  "v_mov_b32 v83, v37\n" \
  "v_mov_b32 v84, v34\n" \
  "v_mov_b32 v85, v38\n" \
  "v_mov_b32 v86, v35\n" \
  "v_mov_b32 v87, v39\n" \
  "ds_write_b128 v211, v[80:83] offset:8192\n" \
  "ds_write_b128 v211, v[84:87] offset:9216\n" \
  "s_waitcnt vmcnt(0)\n" \
  "s_branch .LR_fu_b_%=\n" \
  ".LR_nb0_%=:\n" \
  "s_waitcnt vmcnt(0)\n" \
  "s_branch .LR_nb1_%=\n" \
  ".LR_help_%=:\n" \
  "s_mul_i32 s60, %19, 5\n" \
  "s_sub_u32 s60, s60, 5\n" \
  "s_lshl_b32 s61, s60, 11\n" \
  "s_add_u32 s61, s61, 8192\n" \
  "v_add_u32 v211, s61, v214\n" \
  "s_lshr_b32 s61, s60, 2\n" \
  "s_lshl_b32 s61, s61, 14\n" \
  "s_and_b32 s62, s60, 3\n" \
  "s_lshl_b32 s62, s62, 11\n" \
  "s_xor_b32 s61, s61, s62\n" \
  "s_xor_b32 s64, s61, %25\n" \
  "s_add_u32 s60, s60, 1\n" \
  "s_lshr_b32 s61, s60, 2\n" \
  "s_lshl_b32 s61, s61, 14\n" \
  "s_and_b32 s62, s60, 3\n" \
  "s_lshl_b32 s62, s62, 11\n" \
  "s_xor_b32 s61, s61, s62\n" \
  "s_xor_b32 s65, s61, %25\n" \
  "s_add_u32 s60, s60, 1\n" \
  "s_lshr_b32 s61, s60, 2\n" \
  "s_lshl_b32 s61, s61, 14\n" \
  "s_and_b32 s62, s60, 3\n" \
  "s_lshl_b32 s62, s62, 11\n" \
  "s_xor_b32 s61, s61, s62\n" \
  "s_xor_b32 s67, s61, %25\n" \
  "s_add_u32 s60, s60, 1\n" \
  "s_lshr_b32 s61, s60, 2\n" \
  "s_lshl_b32 s61, s61, 14\n" \
  "s_and_b32 s62, s60, 3\n" \
  "s_lshl_b32 s62, s62, 11\n" \
  "s_xor_b32 s61, s61, s62\n" \
  "s_xor_b32 s68, s61, %25\n" \
  "s_add_u32 s60, s60, 1\n" \
  "s_lshr_b32 s61, s60, 2\n" \
  "s_lshl_b32 s61, s61, 14\n" \
  "s_and_b32 s62, s60, 3\n" \
  "s_lshl_b32 s62, s62, 11\n" \
  "s_xor_b32 s61, s61, s62\n" \
  "s_xor_b32 s74, s61, %25\n" \
  "s_add_u32 s60, s60, 1\n" \
  "s_lshr_b32 s61, s60, 2\n" \
  "s_lshl_b32 s61, s61, 14\n" \
  "s_and_b32 s62, s60, 3\n" \
  "s_lshl_b32 s62, s62, 11\n" \
  "s_xor_b32 s61, s61, s62\n" \
  "s_xor_b32 s75, s61, %25\n" \
  "s_add_u32 s60, s60, 1\n" \
  "s_xor_b32 s71, s56, s57\n" \
  "s_movk_i32 s88, -32768\n" \
  "v_mov_b64 v[136:137], -1\n" \
  "v_mov_b64 v[138:139], -1\n" \
  "v_mov_b64 v[140:141], 0\n" \
  "s_mov_b64 vcc, -1\n" \
  "s_barrier\n" \
  ".LR_hl_%=:\n" \
  "s_and_b32 s61, s48, 15\n" \
  "s_lshl_b32 s61, s61, 16\n" \
  "s_bfe_u32 s60, %32, 524288\n" \
  ".LR_hs_%=:\n" \
  "s_cmp_eq_u32 s60, 0\n" \
  "s_cbranch_scc1 .LR_hs_x_%=\n" \
  "s_sleep 1\n" \
  "s_sub_u32 s60, s60, 1\n" \
  "s_branch .LR_hs_%=\n" \
  ".LR_hs_x_%=:\n" \
  "s_xor_b32 s60, s71, s57\n" \
  "v_xor_b32 v145, s60, v224\n" \
  "buffer_store_dwordx4 v[136:139], v145, s[40:43], 0 offen sc1\n" \
  "s_cbranch_vccnz .LR_fu_%=\n" \
  "s_cmp_lg_u32 %19, 3\n" \
  "s_cbranch_scc1 .LR_h5_%=\n" \
  "s_xor_b32 s60, s61, s75\n" \
  "buffer_load_dwordx4 v[40:43], v214, s[40:43], s60 offen sc1\n" \
  "s_xor_b32 s60, s60, 1024\n" \
  "buffer_load_dwordx4 v[44:47], v214, s[40:43], s60 offen sc1\n" \
  ".LR_h5_%=:\n" \
  "s_xor_b32 s60, s61, s64\n" \
  "buffer_load_dwordx4 v[0:3], v214, s[40:43], s60 offen sc1\n" \
  "s_xor_b32 s60, s60, 1024\n" \
  "buffer_load_dwordx4 v[4:7], v214, s[40:43], s60 offen sc1\n" \
  "s_xor_b32 s60, s61, s65\n" \
  "buffer_load_dwordx4 v[8:11], v214, s[40:43], s60 offen sc1\n" \
  "s_xor_b32 s60, s60, 1024\n" \
  "buffer_load_dwordx4 v[12:15], v214, s[40:43], s60 offen sc1\n" \
  "s_xor_b32 s60, s61, s67\n" \
  "buffer_load_dwordx4 v[16:19], v214, s[40:43], s60 offen sc1\n" \
  "s_xor_b32 s60, s60, 1024\n" \
  "buffer_load_dwordx4 v[20:23], v214, s[40:43], s60 offen sc1\n" \
  "s_cmp_lt_u32 s48, 62\n" \
  "s_cselect_b32 s60, s71, 2130706432\n" \
  "v_xor_b32 v142, s60, v221\n" \
  "v_xor_b32 v143, s60, v222\n" \
  "v_xor_b32 v144, s60, v223\n" \
  "buffer_store_dwordx4 v[136:139], v142, s[40:43], 0 offen sc1\n" \
  "buffer_store_dwordx4 v[136:139], v143, s[40:43], 0 offen sc1\n" \
  "buffer_store_dwordx4 v[136:139], v144, s[40:43], 0 offen sc1\n" \
  "s_waitcnt vmcnt(9)\n" \
  "v_mov_b32 v72, v24\n" \
  "v_mov_b32 v73, v28\n" \
  "v_mov_b32 v74, v25\n" \
  "v_mov_b32 v75, v29\n" \
  "v_mov_b32 v76, v26\n" \
  "v_mov_b32 v77, v30\n" \
  "v_mov_b32 v78, v27\n" \
  "v_mov_b32 v79, v31\n" \
  "ds_write_b128 v211, v[72:75] offset:6144\n" \
  "ds_write_b128 v211, v[76:79] offset:7168\n" \
  "v_mov_b32 v80, v32\n" \
  "v_mov_b32 v81, v36\n" \
  "v_mov_b32 v82, v33\n" \
  "v_mov_b32 v83, v37\n" \
  "v_mov_b32 v84, v34\n" \
  "v_mov_b32 v85, v38\n" \
  "v_mov_b32 v86, v35\n" \
  "v_mov_b32 v87, v39\n" \
  "ds_write_b128 v211, v[80:83] offset:8192\n" \
  "ds_write_b128 v211, v[84:87] offset:9216\n" \
  "s_barrier\n" \
  ".LR_fu_b_%=:\n" \
  "s_add_u32 s48, s48, 1\n" \
  "v_cmp_ne_u32 vcc, %21, v141\n" \
  "s_nop 1\n" \
  "v_cndmask_b32 v140, v140, 0, vcc\n" \
  "v_max_i32 v140, v140, v218\n" \
  "v_cmp_ge_i32 vcc, s48, v140\n" \
  "s_and_b32 s61, s48, 15\n" \
  "s_lshl_b32 s61, s61, 16\n" \
  "s_cbranch_vccnz .LR_nb0_%=\n" \
  "s_bfe_u32 s60, %32, 524296\n" \
  ".LR_ht_%=:\n" \
  "s_cmp_eq_u32 s60, 0\n" \
  "s_cbranch_scc1 .LR_ht_x_%=\n" \
  "s_sleep 1\n" \
  "s_sub_u32 s60, s60, 1\n" \
  "s_branch .LR_ht_%=\n" \
  ".LR_ht_x_%=:\n" \
  "s_xor_b32 s60, s61, s68\n" \
  "buffer_load_dwordx4 v[24:27], v214, s[40:43], s60 offen sc1\n" \
  "s_xor_b32 s60, s60, 1024\n" \
  "buffer_load_dwordx4 v[28:31], v214, s[40:43], s60 offen sc1\n" \
  "s_xor_b32 s60, s61, s74\n" \
  "buffer_load_dwordx4 v[32:35], v214, s[40:43], s60 offen sc1\n" \
  "s_xor_b32 s60, s60, 1024\n" \
  "buffer_load_dwordx4 v[36:39], v214, s[40:43], s60 offen sc1\n" \
  "s_waitcnt vmcnt(4)\n" \
  ".LR_nb1_%=:\n" \
  "s_add_u32 s60, s48, 3\n" \
  "s_min_u32 s60, s60, 63\n" \
  "v_cmp_ge_i32 s[76:77], s60, v140\n" \
  "s_nop 1\n" \
  "s_cmp_eq_u64 s[76:77], 0\n" \
  "s_cbranch_scc1 .LR_nf_%=\n" \
  "s_add_u32 s60, s48, 1\n" \
  "s_lshl_b32 s62, s60, 7\n" \
  "s_add_u32 s62, s62, %33\n" \
  "v_mov_b32 v146, s62\n" \
  "s_sub_u32 s60, s60, 0\n" \
  "s_cmp_lt_u32 s60, 10\n" \
  "s_cselect_b64 s[76:77], -1, 0\n" \
  "v_cndmask_b32 v146, v220, v146, s[76:77]\n" \
  "buffer_load_dwordx2 v[140:141], v146, s[40:43], 0 offen sc1\n" \
  ".LR_nf_%=:\n" \
  "v_mov_b32 v48, v0\n" \
  "v_mov_b32 v49, v4\n" \
  "v_mov_b32 v50, v1\n" \
  "v_mov_b32 v51, v5\n" \
  "v_mov_b32 v52, v2\n" \
  "v_mov_b32 v53, v6\n" \
  "v_mov_b32 v54, v3\n" \
  "v_mov_b32 v55, v7\n" \
  "ds_write_b128 v211, v[48:51] offset:0\n" \
  "ds_write_b128 v211, v[52:55] offset:1024\n" \
  "v_mov_b32 v56, v8\n" \
  "v_mov_b32 v57, v12\n" \
  "v_mov_b32 v58, v9\n" \
  "v_mov_b32 v59, v13\n" \
  "v_mov_b32 v60, v10\n" \
  "v_mov_b32 v61, v14\n" \
  "v_mov_b32 v62, v11\n" \
  "v_mov_b32 v63, v15\n" \
  "ds_write_b128 v211, v[56:59] offset:2048\n" \
  "ds_write_b128 v211, v[60:63] offset:3072\n" \
  "v_mov_b32 v64, v16\n" \
  "v_mov_b32 v65, v20\n" \
  "v_mov_b32 v66, v17\n" \
  "v_mov_b32 v67, v21\n" \
  "v_mov_b32 v68, v18\n" \
  "v_mov_b32 v69, v22\n" \
  "v_mov_b32 v70, v19\n" \
  "v_mov_b32 v71, v23\n" \
  "ds_write_b128 v211, v[64:67] offset:4096\n" \
  "ds_write_b128 v211, v[68:71] offset:5120\n" \
  "s_cmp_lg_u32 %19, 3\n" \
  "s_cbranch_scc1 .LR_h6_%=\n" \
  "v_mov_b32 v88, v40\n" \
  "v_mov_b32 v89, v44\n" \
  "v_mov_b32 v90, v41\n" \
  "v_mov_b32 v91, v45\n" \
  "v_mov_b32 v92, v42\n" \
  "v_mov_b32 v93, v46\n" \
  "v_mov_b32 v94, v43\n" \
  "v_mov_b32 v95, v47\n" \
  "ds_write_b128 v211, v[88:91] offset:10240\n" \
  "ds_write_b128 v211, v[92:95] offset:11264\n" \
  ".LR_h6_%=:\n" \
  "s_sub_u32 s88, 0, s88\n" \
  "s_xor_b32 s71, s71, s56\n" \
  "s_mov_b32 s61, s56\n" \
  "s_mov_b32 s56, s57\n" \
  "s_mov_b32 s57, s61\n" \
  "s_waitcnt lgkmcnt(0)\n" \
  "v_add_u32 v211, s88, v211\n" \
  "s_barrier\n" \
  "s_cmp_lt_u32 s48, 63\n" \
  "s_cbranch_scc1 .LR_hl_%=\n" \
  "s_barrier\n" \
  ".LR_end_%=:\n" \
  "s_waitcnt vmcnt(0)\n"
#define R_ASM_VOPS "+{v212}"(ri.sidx), "+{v213}"(ri.cidx), "+{v214}"(ri.l16), "+{v215}"(ri.ax), "+{v216}"(ri.am), "+{v217}"(ri.mo), "+{v218}"(ri.sel), "+{v219}"(ri.to), "+{v220}"(ri.fu), "+{v221}"(ri.p0), "+{v222}"(ri.p1), "+{v223}"(ri.p2), "+{v224}"(ri.p3), "+{v225}"(ri.d0), "+{v226}"(ri.d1), "+{v227}"(ri.d2), "+{v228}"(ri.d3), "+{v229}"(ri.d4)
#define R_ASM_SOPS "s"(ri.ws), "s"(ri.w), "s"(ri.go), "s"(ri.tag), "s"(ri.o_va), "s"(ri.o_vb), "s"(ri.lab), "s"(ri.o_u), "s"(ri.kind), "s"(ri.o_ctl), "s"(ri.pa), "s"(ri.pb), "s"(ri.f_d), "s"(ri.f_r), "s"(ri.ph), "s"(ri.f_l)
#define R_ASM_CLOB "v0", "v1", "v2", "v3", "v4", "v5", "v6", "v7", "v8", "v9", "v10", "v11", "v12", "v13", "v14", "v15", "v16", "v17", "v18", "v19", "v20", "v21", "v22", "v23", "v24", "v25", "v26", "v27", "v28", "v29", "v30", "v31", "v32", "v33", "v34", "v35", "v36", "v37", "v38", "v39", "v40", "v41", "v42", "v43", "v44", "v45", "v46", "v47", "v48", "v49", "v50", "v51", "v52", "v53", "v54", "v55", "v56", "v57", "v58", "v59", "v60", "v61", "v62", "v63", "v64", "v65", "v66", "v67", "v68", "v69", "v70", "v71", "v72", "v73", "v74", "v75", "v76", "v77", "v78", "v79", "v80", "v81", "v82", "v83", "v84", "v85", "v86", "v87", "v88", "v89", "v90", "v91", "v92", "v93", "v94", "v95", "v96", "v97", "v98", "v99", "v100", "v101", "v102", "v103", "v104", "v105", "v106", "v107", "v108", "v109", "v110", "v111", "v112", "v113", "v114", "v115", "v116", "v117", "v118", "v119", "v120", "v121", "v122", "v123", "v124", "v125", "v126", "v127", "v128", "v129", "v130", "v131", "v132", "v133", "v134", "v135", "v136", "v137", "v138", "v139", "v140", "v141", "v142", "v143", "v144", "v145", "v146", "v147", "v148", "v149", "v150", "v151", "v152", "v153", "v154", "v155", "v156", "v157", "v158", "v159", "v160", "v161", "v162", "v163", "v164", "v165", "v166", "v167", "v168", "v169", "v170", "v171", "v172", "v173", "v174", "v175", "v176", "v177", "v178", "v179", "v180", "v181", "v182", "v183", "v184", "v185", "v186", "v187", "v188", "v189", "v190", "v191", "v192", "v193", "v194", "v195", "v196", "v197", "v198", "v199", "v200", "v201", "v202", "v203", "v204", "v205", "v206", "v207", "v208", "v209", "v210", "v211", "a0", "a1", "a2", "a3", "a4", "a5", "a6", "a7", "a8", "a9", "a10", "a11", "a12", "a13", "a14", "a15", "a16", "a17", "a18", "a19", "a20", "a21", "a22", "a23", "a24", "a25", "a26", "a27", "a28", "a29", "a30", "a31", "a32", "a33", "a34", "a35", "a36", "a37", "a38", "a39", "a40", "a41", "a42", "a43", "a44", "a45", "a46", "a47", "a48", "a49", "a50", "a51", "a52", "a53", "a54", "a55", "a56", "a57", "a58", "a59", "a60", "a61", "a62", "a63", "a64", "a65", "a66", "a67", "a68", "a69", "a70", "a71", "a72", "a73", "a74", "a75", "a76", "a77", "a78", "a79", "a80", "a81", "a82", "a83", "a84", "a85", "a86", "a87", "a88", "a89", "a90", "a91", "a92", "a93", "a94", "a95", "a96", "a97", "a98", "a99", "a100", "a101", "a102", "a103", "a104", "a105", "a106", "a107", "a108", "a109", "a110", "a111", "a112", "a113", "a114", "a115", "a116", "a117", "a118", "a119", "a120", "a121", "a122", "a123", "a124", "a125", "a126", "a127", "s40", "s41", "s42", "s43", "s44", "s45", "s46", "s47", "s48", "s49", "s50", "s51", "s52", "s53", "s54", "s55", "s56", "s57", "s58", "s59", "s60", "s61", "s62", "s63", "s64", "s65", "s66", "s67", "s68", "s69", "s70", "s71", "s72", "s73", "s74", "s75", "s76", "s77", "s78", "s79", "s80", "s81", "s82", "s83", "s84", "s85", "s86", "s87", "s88", "s89", "s90", "s91", "s92", "s93", "s94", "s95", "s96", "s97", "s98", "s99", "vcc", "scc", "memory"
#define K_ASM_TEXT \
  "s_mov_b64 s[40:41], %30\n" \
  "s_and_b32 s41, s41, 0xffff\n" \
  "s_mov_b32 s42, 255852544\n" \
  "s_mov_b32 s43, 0x00020000\n" \
  "s_mov_b32 s47, 0x00020000\n" \
  "s_lshl_b32 s72, %34, 12\n" \
  "buffer_load_dword v184, off, s[40:43], %42 sc1\n" \
  "s_mov_b64 s[44:45], %31\n" \
  "s_and_b32 s45, s45, 0xffff\n" \
  "s_mov_b32 s46, 4718592\n" \
  "s_add_u32 s60, %35, 0\n" \
  "s_add_u32 m0, s72, 16384\n" \
  "s_nop 0\n" \
  "buffer_load_dwordx4 v170, s[44:47], s60 offen lds\n" \
  "buffer_load_dwordx4 v171, s[44:47], s60 offen offset:1024 lds\n" \
  "buffer_load_dwordx4 v172, s[44:47], s60 offen offset:2048 lds\n" \
  "buffer_load_dwordx4 v173, s[44:47], s60 offen offset:3072 lds\n" \
  "s_add_u32 s60, %35, 524288\n" \
  "s_add_u32 m0, s72, 32768\n" \
  "s_nop 0\n" \
  "buffer_load_dwordx4 v170, s[44:47], s60 offen lds\n" \
  "buffer_load_dwordx4 v171, s[44:47], s60 offen offset:1024 lds\n" \
  "buffer_load_dwordx4 v172, s[44:47], s60 offen offset:2048 lds\n" \
  "buffer_load_dwordx4 v173, s[44:47], s60 offen offset:3072 lds\n" \
  "s_add_u32 s60, %35, 1048576\n" \
  "s_add_u32 m0, s72, 49152\n" \
  "s_nop 0\n" \
  "buffer_load_dwordx4 v170, s[44:47], s60 offen lds\n" \
  "buffer_load_dwordx4 v171, s[44:47], s60 offen offset:1024 lds\n" \
  "buffer_load_dwordx4 v172, s[44:47], s60 offen offset:2048 lds\n" \
  "buffer_load_dwordx4 v173, s[44:47], s60 offen offset:3072 lds\n" \
  "s_add_u32 s60, %35, 1572864\n" \
  "s_add_u32 m0, s72, 65536\n" \
  "s_nop 0\n" \
  "buffer_load_dwordx4 v170, s[44:47], s60 offen lds\n" \
  "buffer_load_dwordx4 v171, s[44:47], s60 offen offset:1024 lds\n" \
  "buffer_load_dwordx4 v172, s[44:47], s60 offen offset:2048 lds\n" \
  "buffer_load_dwordx4 v173, s[44:47], s60 offen offset:3072 lds\n" \
  "s_add_u32 s60, %35, 2097152\n" \
  "s_add_u32 m0, s72, 81920\n" \
  "s_nop 0\n" \
  "buffer_load_dwordx4 v170, s[44:47], s60 offen lds\n" \
  "buffer_load_dwordx4 v171, s[44:47], s60 offen offset:1024 lds\n" \
  "buffer_load_dwordx4 v172, s[44:47], s60 offen offset:2048 lds\n" \
  "buffer_load_dwordx4 v173, s[44:47], s60 offen offset:3072 lds\n" \
  "s_add_u32 s60, %35, 2621440\n" \
  "s_add_u32 m0, s72, 98304\n" \
  "s_nop 0\n" \
  "buffer_load_dwordx4 v170, s[44:47], s60 offen lds\n" \
  "buffer_load_dwordx4 v171, s[44:47], s60 offen offset:1024 lds\n" \
  "buffer_load_dwordx4 v172, s[44:47], s60 offen offset:2048 lds\n" \
  "buffer_load_dwordx4 v173, s[44:47], s60 offen offset:3072 lds\n" \
  "s_add_u32 s60, %35, 3145728\n" \
  "s_add_u32 m0, s72, 114688\n" \
  "s_nop 0\n" \
  "buffer_load_dwordx4 v170, s[44:47], s60 offen lds\n" \
  "buffer_load_dwordx4 v171, s[44:47], s60 offen offset:1024 lds\n" \
  "buffer_load_dwordx4 v172, s[44:47], s60 offen offset:2048 lds\n" \
  "buffer_load_dwordx4 v173, s[44:47], s60 offen offset:3072 lds\n" \
  "s_add_u32 s60, %35, 3670016\n" \
  "s_add_u32 m0, s72, 131072\n" \
  "s_nop 0\n" \
  "buffer_load_dwordx4 v170, s[44:47], s60 offen lds\n" \
  "buffer_load_dwordx4 v171, s[44:47], s60 offen offset:1024 lds\n" \
  "buffer_load_dwordx4 v172, s[44:47], s60 offen offset:2048 lds\n" \
  "buffer_load_dwordx4 v173, s[44:47], s60 offen offset:3072 lds\n" \
  "s_add_u32 s60, %35, 4194304\n" \
  "s_add_u32 m0, s72, 147456\n" \
  "s_nop 0\n" \
  "buffer_load_dwordx4 v170, s[44:47], s60 offen lds\n" \
  "buffer_load_dwordx4 v171, s[44:47], s60 offen offset:1024 lds\n" \
  "buffer_load_dwordx4 v172, s[44:47], s60 offen offset:2048 lds\n" \
  "buffer_load_dwordx4 v173, s[44:47], s60 offen offset:3072 lds\n" \
  "s_waitcnt vmcnt(24)\n" \
  "s_barrier\n" \
  "v_add_u32 v186, 16384, v182\n" \
  "ds_read_b64_tr_b16 a[0:1], v186\n" \
  "ds_read_b64_tr_b16 a[2:3], v186 offset:1024\n" \
  "ds_read_b64_tr_b16 a[4:5], v186 offset:8192\n" \
  "ds_read_b64_tr_b16 a[6:7], v186 offset:9216\n" \
  "v_add_u32 v187, 16384, v183\n" \
  "ds_read_b64_tr_b16 a[72:73], v187\n" \
  "ds_read_b64_tr_b16 a[74:75], v187 offset:1024\n" \
  "ds_read_b64_tr_b16 a[76:77], v187 offset:8192\n" \
  "ds_read_b64_tr_b16 a[78:79], v187 offset:9216\n" \
  "v_add_u32 v186, 32768, v182\n" \
  "ds_read_b64_tr_b16 a[8:9], v186\n" \
  "ds_read_b64_tr_b16 a[10:11], v186 offset:1024\n" \
  "ds_read_b64_tr_b16 a[12:13], v186 offset:8192\n" \
  "ds_read_b64_tr_b16 a[14:15], v186 offset:9216\n" \
  "v_add_u32 v187, 32768, v183\n" \
  "ds_read_b64_tr_b16 a[80:81], v187\n" \
  "ds_read_b64_tr_b16 a[82:83], v187 offset:1024\n" \
  "ds_read_b64_tr_b16 a[84:85], v187 offset:8192\n" \
  "ds_read_b64_tr_b16 a[86:87], v187 offset:9216\n" \
  "v_add_u32 v186, 49152, v182\n" \
  "ds_read_b64_tr_b16 a[16:17], v186\n" \
  "ds_read_b64_tr_b16 a[18:19], v186 offset:1024\n" \
  "ds_read_b64_tr_b16 a[20:21], v186 offset:8192\n" \
  "ds_read_b64_tr_b16 a[22:23], v186 offset:9216\n" \
  "v_add_u32 v187, 49152, v183\n" \
  "ds_read_b64_tr_b16 a[88:89], v187\n" \
  "ds_read_b64_tr_b16 a[90:91], v187 offset:1024\n" \
  "ds_read_b64_tr_b16 a[92:93], v187 offset:8192\n" \
  "ds_read_b64_tr_b16 a[94:95], v187 offset:9216\n" \
  "s_waitcnt lgkmcnt(0)\n" \
  "s_barrier\n" \
  "v_readfirstlane_b32 s55, v184\n" \
  "s_add_u32 s55, s55, 1\n" \
  "s_mov_b64 s[44:45], %33\n" \
  "s_and_b32 s45, s45, 0xffff\n" \
  "s_mov_b32 s46, 2359296\n" \
  "s_mov_b32 s51, 0\n" \
  "s_mul_i32 s52, %34, 9216\n" \
  "s_add_u32 s52, s52, 16384\n" \
  "s_add_u32 m0, s52, 0\n" \
  "s_nop 0\n" \
  "buffer_load_dwordx4 v196, s[44:47], s51 offen lds\n" \
  "s_add_u32 m0, s52, 1024\n" \
  "s_nop 0\n" \
  "buffer_load_dwordx4 v197, s[44:47], s51 offen lds\n" \
  "s_add_u32 m0, s52, 2048\n" \
  "s_nop 0\n" \
  "buffer_load_dwordx4 v198, s[44:47], s51 offen lds\n" \
  "s_add_u32 m0, s52, 3072\n" \
  "s_nop 0\n" \
  "buffer_load_dwordx4 v199, s[44:47], s51 offen lds\n" \
  "s_add_u32 m0, s52, 4096\n" \
  "s_nop 0\n" \
  "buffer_load_dwordx4 v200, s[44:47], s51 offen lds\n" \
  "s_add_u32 m0, s52, 5120\n" \
  "s_nop 0\n" \
  "buffer_load_dwordx4 v201, s[44:47], s51 offen lds\n" \
  "s_add_u32 m0, s52, 6144\n" \
  "s_nop 0\n" \
  "buffer_load_dwordx4 v202, s[44:47], s51 offen lds\n" \
  "s_add_u32 m0, s52, 7168\n" \
  "s_nop 0\n" \
  "buffer_load_dwordx4 v203, s[44:47], s51 offen lds\n" \
  "s_add_u32 m0, s52, 8192\n" \
  "s_nop 0\n" \
  "buffer_load_dwordx4 v204, s[44:47], s51 offen lds\n" \
  "s_add_u32 s51, s51, 36864\n" \
  "s_min_u32 s51, s51, 2322432\n" \
  "s_add_u32 s52, s52, 36864\n" \
  "s_cmp_ge_u32 s52, 163840\n" \
  "s_cselect_b32 s60, 147456, 0\n" \
  "s_sub_u32 s52, s52, s60\n" \
  "s_waitcnt vmcnt(21)\n" \
  "s_barrier\n" \
  "v_add_u32 v186, 65536, v182\n" \
  "ds_read_b64_tr_b16 a[24:25], v186\n" \
  "ds_read_b64_tr_b16 a[26:27], v186 offset:1024\n" \
  "ds_read_b64_tr_b16 a[28:29], v186 offset:8192\n" \
  "ds_read_b64_tr_b16 a[30:31], v186 offset:9216\n" \
  "v_add_u32 v187, 65536, v183\n" \
  "ds_read_b64_tr_b16 a[96:97], v187\n" \
  "ds_read_b64_tr_b16 a[98:99], v187 offset:1024\n" \
  "ds_read_b64_tr_b16 a[100:101], v187 offset:8192\n" \
  "ds_read_b64_tr_b16 a[102:103], v187 offset:9216\n" \
  "v_add_u32 v186, 81920, v182\n" \
  "ds_read_b64_tr_b16 a[32:33], v186\n" \
  "ds_read_b64_tr_b16 a[34:35], v186 offset:1024\n" \
  "ds_read_b64_tr_b16 a[36:37], v186 offset:8192\n" \
  "ds_read_b64_tr_b16 a[38:39], v186 offset:9216\n" \
  "v_add_u32 v187, 81920, v183\n" \
  "ds_read_b64_tr_b16 a[104:105], v187\n" \
  "ds_read_b64_tr_b16 a[106:107], v187 offset:1024\n" \
  "ds_read_b64_tr_b16 a[108:109], v187 offset:8192\n" \
  "ds_read_b64_tr_b16 a[110:111], v187 offset:9216\n" \
  "v_add_u32 v186, 98304, v182\n" \
  "ds_read_b64_tr_b16 a[40:41], v186\n" \
  "ds_read_b64_tr_b16 a[42:43], v186 offset:1024\n" \
  "ds_read_b64_tr_b16 a[44:45], v186 offset:8192\n" \
  "ds_read_b64_tr_b16 a[46:47], v186 offset:9216\n" \
  "v_add_u32 v187, 98304, v183\n" \
  "ds_read_b64_tr_b16 a[112:113], v187\n" \
  "ds_read_b64_tr_b16 a[114:115], v187 offset:1024\n" \
  "ds_read_b64_tr_b16 a[116:117], v187 offset:8192\n" \
  "ds_read_b64_tr_b16 a[118:119], v187 offset:9216\n" \
  "s_waitcnt lgkmcnt(0)\n" \
  "s_barrier\n" \
  "buffer_load_dwordx2 v[184:185], off, s[40:43], %41 sc1\n" \
  "s_add_u32 m0, s52, 0\n" \
  "s_nop 0\n" \
  "buffer_load_dwordx4 v196, s[44:47], s51 offen lds\n" \
  "s_add_u32 m0, s52, 1024\n" \
  "s_nop 0\n" \
  "buffer_load_dwordx4 v197, s[44:47], s51 offen lds\n" \
  "s_add_u32 m0, s52, 2048\n" \
  "s_nop 0\n" \
  "buffer_load_dwordx4 v198, s[44:47], s51 offen lds\n" \
  "s_add_u32 m0, s52, 3072\n" \
  "s_nop 0\n" \
  "buffer_load_dwordx4 v199, s[44:47], s51 offen lds\n" \
  "s_add_u32 m0, s52, 4096\n" \
  "s_nop 0\n" \
  "buffer_load_dwordx4 v200, s[44:47], s51 offen lds\n" \
  "s_add_u32 m0, s52, 5120\n" \
  "s_nop 0\n" \
  "buffer_load_dwordx4 v201, s[44:47], s51 offen lds\n" \
  "s_add_u32 m0, s52, 6144\n" \
  "s_nop 0\n" \
  "buffer_load_dwordx4 v202, s[44:47], s51 offen lds\n" \
  "s_add_u32 m0, s52, 7168\n" \
  "s_nop 0\n" \
  "buffer_load_dwordx4 v203, s[44:47], s51 offen lds\n" \
  "s_add_u32 m0, s52, 8192\n" \
  "s_nop 0\n" \
  "buffer_load_dwordx4 v204, s[44:47], s51 offen lds\n" \
  "s_add_u32 s51, s51, 36864\n" \
  "s_min_u32 s51, s51, 2322432\n" \
  "s_add_u32 s52, s52, 36864\n" \
  "s_cmp_ge_u32 s52, 163840\n" \
  "s_cselect_b32 s60, 147456, 0\n" \
  "s_sub_u32 s52, s52, s60\n" \
  "s_waitcnt vmcnt(19)\n" \
  "s_barrier\n" \
  "v_add_u32 v186, 114688, v182\n" \
  "ds_read_b64_tr_b16 a[48:49], v186\n" \
  "ds_read_b64_tr_b16 a[50:51], v186 offset:1024\n" \
  "ds_read_b64_tr_b16 a[52:53], v186 offset:8192\n" \
  "ds_read_b64_tr_b16 a[54:55], v186 offset:9216\n" \
  "v_add_u32 v187, 114688, v183\n" \
  "ds_read_b64_tr_b16 a[120:121], v187\n" \
  "ds_read_b64_tr_b16 a[122:123], v187 offset:1024\n" \
  "ds_read_b64_tr_b16 a[124:125], v187 offset:8192\n" \
  "ds_read_b64_tr_b16 a[126:127], v187 offset:9216\n" \
  "v_add_u32 v186, 131072, v182\n" \
  "ds_read_b64_tr_b16 a[56:57], v186\n" \
  "ds_read_b64_tr_b16 a[58:59], v186 offset:1024\n" \
  "ds_read_b64_tr_b16 a[60:61], v186 offset:8192\n" \
  "ds_read_b64_tr_b16 a[62:63], v186 offset:9216\n" \
  "v_add_u32 v187, 131072, v183\n" \
  "ds_read_b64_tr_b16 a[128:129], v187\n" \
  "ds_read_b64_tr_b16 a[130:131], v187 offset:1024\n" \
  "ds_read_b64_tr_b16 a[132:133], v187 offset:8192\n" \
  "ds_read_b64_tr_b16 a[134:135], v187 offset:9216\n" \
  "v_add_u32 v186, 147456, v182\n" \
  "ds_read_b64_tr_b16 a[64:65], v186\n" \
  "ds_read_b64_tr_b16 a[66:67], v186 offset:1024\n" \
  "ds_read_b64_tr_b16 a[68:69], v186 offset:8192\n" \
  "ds_read_b64_tr_b16 a[70:71], v186 offset:9216\n" \
  "v_add_u32 v187, 147456, v183\n" \
  "ds_read_b64_tr_b16 a[136:137], v187\n" \
  "ds_read_b64_tr_b16 a[138:139], v187 offset:1024\n" \
  "ds_read_b64_tr_b16 a[140:141], v187 offset:8192\n" \
  "ds_read_b64_tr_b16 a[142:143], v187 offset:9216\n" \
  "s_waitcnt lgkmcnt(0)\n" \
  "s_mov_b32 s69, 0\n" \
  "s_waitcnt vmcnt(9)\n" \
  "s_branch .LK_go1_%=\n" \
  ".LK_go_%=:\n" \
  "buffer_load_dwordx2 v[184:185], off, s[40:43], %41 sc1\n" \
  "s_waitcnt vmcnt(0)\n" \
  ".LK_go1_%=:\n" \
  "v_readfirstlane_b32 s96, v184\n" \
  "v_readfirstlane_b32 s63, v185\n" \
  "s_cmp_eq_u32 s63, s55\n" \
  "s_cselect_b32 s96, s96, 0\n" \
  "s_cmp_ge_u32 s96, 0x80000000\n" \
  "s_cbranch_scc1 .LK_go_x_%=\n" \
  "s_sleep 4\n" \
  "s_add_u32 s69, s69, 1\n" \
  "s_cmp_lt_u32 s69, 2097152\n" \
  "s_cbranch_scc1 .LK_go_%=\n" \
  "v_mov_b32 v184, 1\n" \
  "v_mov_b32 v185, s55\n" \
  "buffer_store_dwordx2 v[184:185], off, s[40:43], %42 offset:128 sc1\n" \
  ".LK_go_x_%=:\n" \
  "s_and_b32 s60, s96, 2\n" \
  "s_lshl_b32 s60, s60, 12\n" \
  "s_xor_b32 s53, %39, s60\n" \
  "s_mov_b32 s54, %40\n" \
  "s_andn2_b32 s60, 2, s96\n" \
  "s_lshl_b32 s60, s60, 12\n" \
  "s_add_u32 s49, %37, s60\n" \
  "s_add_u32 s50, %38, s60\n" \
  "s_xor_b32 s66, %38, s60\n" \
  "s_xor_b32 s59, %44, s60\n" \
  "s_or_b32 s50, s50, 0x40000000\n" \
  "s_mov_b32 s57, 2130706432\n" \
  "s_mov_b32 s48, 0\n" \
  "v_mov_b32 v169, s55\n" \
  "v_mov_b32 v167, 2130706432\n" \
  "v_mov_b32 v163, 2130706432\n" \
  "v_mov_b32 v166, -1\n" \
  "s_mov_b64 exec, 3\n" \
  "v_mov_b32 v167, s54\n" \
  "s_mov_b64 exec, 1\n" \
  "v_mov_b32 v167, s53\n" \
  "v_mov_b32 v163, s53\n" \
  "v_mov_b32 v166, 0\n" \
  "s_mov_b64 exec, -1\n" \
  "s_barrier\n" \
  "ds_read_b128 v[32:35], v191\n" \
  "ds_read_b128 v[36:39], v191 offset:18432\n" \
  "ds_read_b128 v[40:43], v192\n" \
  "ds_read_b128 v[44:47], v192 offset:18432\n" \
  "ds_read_b128 v[48:51], v191 offset:128\n" \
  "ds_read_b128 v[52:55], v191 offset:18560\n" \
  "s_waitcnt lgkmcnt(0)\n" \
  "s_sub_u32 s49, s49, 524288\n" \
  "s_or_b32 s49, s49, 0x40000000\n" \
  "s_mov_b32 s56, -1\n" \
  "s_mov_b32 s57, 0x10000\n" \
  "s_mov_b32 s71, 36864\n" \
  ".LK_ma_%=:\n" \
  "s_waitcnt lgkmcnt(5)\n" \
  "v_mfma_f32_16x16x32_bf16 v[64:67], a[0:3], v[32:35], 0\n" \
  "ds_read_b128 v[56:59], v192 offset:128\n" \
  "s_waitcnt lgkmcnt(5)\n" \
  "v_mfma_f32_16x16x32_bf16 v[72:75], a[0:3], v[36:39], 0\n" \
  "ds_read_b128 v[60:63], v192 offset:18560\n" \
  "s_lshr_b32 s62, s49, 8\n" \
  "v_mfma_f32_16x16x32_bf16 v[68:71], a[72:75], v[32:35], 0\n" \
  "s_add_u32 m0, s52, 0\n" \
  "s_and_b32 s62, s62, 8192\n" \
  "s_xor_b32 s62, s62, s49\n" \
  "v_mfma_f32_16x16x32_bf16 v[76:79], a[72:75], v[36:39], 0\n" \
  "s_waitcnt lgkmcnt(5)\n" \
  "buffer_load_dwordx4 v196, s[44:47], s51 offen lds\n" \
  "v_mfma_f32_16x16x32_bf16 v[64:67], a[4:7], v[40:43], v[64:67]\n" \
  "ds_read_b128 v[32:35], v191 offset:256\n" \
  "s_waitcnt lgkmcnt(5)\n" \
  "v_cvt_pk_bf16_f32 v96, v80, v81\n" \
  "v_mfma_f32_16x16x32_bf16 v[72:75], a[4:7], v[44:47], v[72:75]\n" \
  "ds_read_b128 v[36:39], v191 offset:18688\n" \
  "v_cvt_pk_bf16_f32 v97, v82, v83\n" \
  "v_mfma_f32_16x16x32_bf16 v[68:71], a[76:79], v[40:43], v[68:71]\n" \
  "s_lshl_b32 s63, s57, 14\n" \
  "v_cvt_pk_bf16_f32 v98, v84, v85\n" \
  "v_cvt_pk_bf16_f32 v99, v86, v87\n" \
  "v_mfma_f32_16x16x32_bf16 v[76:79], a[76:79], v[44:47], v[76:79]\n" \
  "s_xor_b32 s63, s63, s59\n" \
  "s_waitcnt lgkmcnt(5)\n" \
  "v_mfma_f32_16x16x32_bf16 v[64:67], a[8:11], v[48:51], v[64:67]\n" \
  "ds_read_b128 v[40:43], v192 offset:256\n" \
  "s_waitcnt lgkmcnt(5)\n" \
  "v_mfma_f32_16x16x32_bf16 v[72:75], a[8:11], v[52:55], v[72:75]\n" \
  "ds_read_b128 v[44:47], v192 offset:18688\n" \
  "v_mfma_f32_16x16x32_bf16 v[68:71], a[80:83], v[48:51], v[68:71]\n" \
  "s_add_u32 m0, s52, 1024\n" \
  "s_waitcnt lgkmcnt(5)\n" \
  "v_mfma_f32_16x16x32_bf16 v[76:79], a[80:83], v[52:55], v[76:79]\n" \
  "buffer_load_dwordx4 v197, s[44:47], s51 offen lds\n" \
  "v_mfma_f32_16x16x32_bf16 v[64:67], a[12:15], v[56:59], v[64:67]\n" \
  "ds_read_b128 v[48:51], v191 offset:384\n" \
  "s_waitcnt lgkmcnt(5)\n" \
  "v_mfma_f32_16x16x32_bf16 v[72:75], a[12:15], v[60:63], v[72:75]\n" \
  "ds_read_b128 v[52:55], v191 offset:18816\n" \
  "v_mfma_f32_16x16x32_bf16 v[68:71], a[84:87], v[56:59], v[68:71]\n" \
  "buffer_store_dwordx4 v[96:99], v190, s[40:43], s62 offen sc1\n" \
  "v_mfma_f32_16x16x32_bf16 v[76:79], a[84:87], v[60:63], v[76:79]\n" \
  "s_waitcnt lgkmcnt(5)\n" \
  "buffer_store_dwordx4 v[96:99], v190, s[40:43], s63 offen sc1\n" \
  "v_mfma_f32_16x16x32_bf16 v[64:67], a[16:19], v[32:35], v[64:67]\n" \
  "ds_read_b128 v[56:59], v192 offset:384\n" \
  "s_waitcnt lgkmcnt(5)\n" \
  "v_cvt_pk_bf16_f32 v100, v88, v89\n" \
  "v_mfma_f32_16x16x32_bf16 v[72:75], a[16:19], v[36:39], v[72:75]\n" \
  "ds_read_b128 v[60:63], v192 offset:18816\n" \
  "v_cvt_pk_bf16_f32 v101, v90, v91\n" \
  "v_mfma_f32_16x16x32_bf16 v[68:71], a[88:91], v[32:35], v[68:71]\n" \
  "s_add_u32 m0, s52, 2048\n" \
  "v_cvt_pk_bf16_f32 v102, v92, v93\n" \
  "v_cvt_pk_bf16_f32 v103, v94, v95\n" \
  "v_mfma_f32_16x16x32_bf16 v[76:79], a[88:91], v[36:39], v[76:79]\n" \
  "s_waitcnt lgkmcnt(5)\n" \
  "buffer_load_dwordx4 v198, s[44:47], s51 offen lds\n" \
  "v_mfma_f32_16x16x32_bf16 v[64:67], a[20:23], v[40:43], v[64:67]\n" \
  "ds_read_b128 v[32:35], v191 offset:512\n" \
  "s_waitcnt lgkmcnt(5)\n" \
  "v_mfma_f32_16x16x32_bf16 v[72:75], a[20:23], v[44:47], v[72:75]\n" \
  "ds_read_b128 v[36:39], v191 offset:18944\n" \
  "v_mfma_f32_16x16x32_bf16 v[68:71], a[92:95], v[40:43], v[68:71]\n" \
  "buffer_store_dwordx4 v[100:103], v190, s[40:43], s62 offen offset:1024 sc1\n" \
  "v_mfma_f32_16x16x32_bf16 v[76:79], a[92:95], v[44:47], v[76:79]\n" \
  "s_waitcnt lgkmcnt(5)\n" \
  "buffer_store_dwordx4 v[100:103], v190, s[40:43], s63 offen offset:1024 sc1\n" \
  "v_mfma_f32_16x16x32_bf16 v[64:67], a[24:27], v[48:51], v[64:67]\n" \
  "ds_read_b128 v[40:43], v192 offset:512\n" \
  "s_add_u32 s49, s49, 524288\n" \
  "s_waitcnt lgkmcnt(5)\n" \
  "v_mfma_f32_16x16x32_bf16 v[72:75], a[24:27], v[52:55], v[72:75]\n" \
  "ds_read_b128 v[44:47], v192 offset:18944\n" \
  "s_bitset0_b32 s49, 30\n" \
  "v_mfma_f32_16x16x32_bf16 v[68:71], a[96:99], v[48:51], v[68:71]\n" \
  "s_add_u32 m0, s52, 3072\n" \
  "s_waitcnt lgkmcnt(5)\n" \
  "v_mfma_f32_16x16x32_bf16 v[76:79], a[96:99], v[52:55], v[76:79]\n" \
  "buffer_load_dwordx4 v199, s[44:47], s51 offen lds\n" \
  "v_mfma_f32_16x16x32_bf16 v[64:67], a[28:31], v[56:59], v[64:67]\n" \
  "ds_read_b128 v[48:51], v191 offset:640\n" \
  "s_waitcnt lgkmcnt(5)\n" \
  "v_mfma_f32_16x16x32_bf16 v[72:75], a[28:31], v[60:63], v[72:75]\n" \
  "ds_read_b128 v[52:55], v191 offset:19072\n" \
  "v_mfma_f32_16x16x32_bf16 v[68:71], a[100:103], v[56:59], v[68:71]\n" \
  "s_cmp_eq_u32 s48, 20\n" \
  "s_cselect_b32 s60, 19922944, 0\n" \
  "s_add_u32 s49, s49, s60\n" \
  "v_mfma_f32_16x16x32_bf16 v[76:79], a[100:103], v[60:63], v[76:79]\n" \
  "s_add_u32 s58, s48, 1\n" \
  "s_and_b32 s58, s58, 3\n" \
  "s_cselect_b32 s58, 0, 147456\n" \
  "s_waitcnt lgkmcnt(5)\n" \
  "v_mfma_f32_16x16x32_bf16 v[64:67], a[32:35], v[32:35], v[64:67]\n" \
  "ds_read_b128 v[56:59], v192 offset:640\n" \
  "s_sub_u32 s58, 36864, s58\n" \
  "s_waitcnt lgkmcnt(5)\n" \
  "v_mfma_f32_16x16x32_bf16 v[72:75], a[32:35], v[36:39], v[72:75]\n" \
  "ds_read_b128 v[60:63], v192 offset:19072\n" \
  "v_add_u32 v165, s58, v191\n" \
  "v_mfma_f32_16x16x32_bf16 v[68:71], a[104:107], v[32:35], v[68:71]\n" \
  "s_add_u32 m0, s52, 4096\n" \
  "v_add_u32 v164, s58, v192\n" \
  "v_mfma_f32_16x16x32_bf16 v[76:79], a[104:107], v[36:39], v[76:79]\n" \
  "s_waitcnt lgkmcnt(5)\n" \
  "buffer_load_dwordx4 v200, s[44:47], s51 offen lds\n" \
  "v_mfma_f32_16x16x32_bf16 v[64:67], a[36:39], v[40:43], v[64:67]\n" \
  "ds_read_b128 v[32:35], v191 offset:768\n" \
  "s_waitcnt lgkmcnt(5)\n" \
  "v_mfma_f32_16x16x32_bf16 v[72:75], a[36:39], v[44:47], v[72:75]\n" \
  "ds_read_b128 v[36:39], v191 offset:19200\n" \
  "v_mfma_f32_16x16x32_bf16 v[68:71], a[108:111], v[40:43], v[68:71]\n" \
  "v_mfma_f32_16x16x32_bf16 v[76:79], a[108:111], v[44:47], v[76:79]\n" \
  "s_waitcnt lgkmcnt(5)\n" \
  "v_mfma_f32_16x16x32_bf16 v[64:67], a[40:43], v[48:51], v[64:67]\n" \
  "ds_read_b128 v[40:43], v192 offset:768\n" \
  "s_waitcnt lgkmcnt(5)\n" \
  "v_mfma_f32_16x16x32_bf16 v[72:75], a[40:43], v[52:55], v[72:75]\n" \
  "ds_read_b128 v[44:47], v192 offset:19200\n" \
  "v_mfma_f32_16x16x32_bf16 v[68:71], a[112:115], v[48:51], v[68:71]\n" \
  "s_add_u32 m0, s52, 5120\n" \
  "s_waitcnt lgkmcnt(5)\n" \
  "v_mfma_f32_16x16x32_bf16 v[76:79], a[112:115], v[52:55], v[76:79]\n" \
  "buffer_load_dwordx4 v201, s[44:47], s51 offen lds\n" \
  "v_mfma_f32_16x16x32_bf16 v[64:67], a[44:47], v[56:59], v[64:67]\n" \
  "ds_read_b128 v[48:51], v191 offset:896\n" \
  "s_waitcnt lgkmcnt(5)\n" \
  "v_mfma_f32_16x16x32_bf16 v[72:75], a[44:47], v[60:63], v[72:75]\n" \
  "ds_read_b128 v[52:55], v191 offset:19328\n" \
  "v_mfma_f32_16x16x32_bf16 v[68:71], a[116:119], v[56:59], v[68:71]\n" \
  "v_mfma_f32_16x16x32_bf16 v[76:79], a[116:119], v[60:63], v[76:79]\n" \
  "s_waitcnt lgkmcnt(5)\n" \
  "v_mfma_f32_16x16x32_bf16 v[64:67], a[48:51], v[32:35], v[64:67]\n" \
  "ds_read_b128 v[56:59], v192 offset:896\n" \
  "s_waitcnt lgkmcnt(5)\n" \
  "v_mfma_f32_16x16x32_bf16 v[72:75], a[48:51], v[36:39], v[72:75]\n" \
  "ds_read_b128 v[60:63], v192 offset:19328\n" \
  "v_mfma_f32_16x16x32_bf16 v[68:71], a[120:123], v[32:35], v[68:71]\n" \
  "s_add_u32 m0, s52, 6144\n" \
  "v_mov_b32 v168, s56\n" \
  "v_mfma_f32_16x16x32_bf16 v[76:79], a[120:123], v[36:39], v[76:79]\n" \
  "s_waitcnt lgkmcnt(5)\n" \
  "buffer_load_dwordx4 v202, s[44:47], s51 offen lds\n" \
  "v_mfma_f32_16x16x32_bf16 v[64:67], a[52:55], v[40:43], v[64:67]\n" \
  "ds_read_b128 v[32:35], v191 offset:1024\n" \
  "s_waitcnt lgkmcnt(5)\n" \
  "v_mfma_f32_16x16x32_bf16 v[72:75], a[52:55], v[44:47], v[72:75]\n" \
  "ds_read_b128 v[36:39], v191 offset:19456\n" \
  "v_mfma_f32_16x16x32_bf16 v[68:71], a[124:127], v[40:43], v[68:71]\n" \
  "s_waitcnt vmcnt(11)\n" \
  "s_barrier\n" \
  "v_mfma_f32_16x16x32_bf16 v[76:79], a[124:127], v[44:47], v[76:79]\n" \
  "s_waitcnt lgkmcnt(5)\n" \
  "buffer_store_dwordx2 v[168:169], v163, s[40:43], 0 offen sc1\n" \
  "v_mfma_f32_16x16x32_bf16 v[64:67], a[56:59], v[48:51], v[64:67]\n" \
  "ds_read_b128 v[40:43], v192 offset:1024\n" \
  "s_waitcnt lgkmcnt(5)\n" \
  "v_mfma_f32_16x16x32_bf16 v[72:75], a[56:59], v[52:55], v[72:75]\n" \
  "ds_read_b128 v[44:47], v192 offset:19456\n" \
  "v_mfma_f32_16x16x32_bf16 v[68:71], a[128:131], v[48:51], v[68:71]\n" \
  "s_add_u32 m0, s52, 7168\n" \
  "s_waitcnt lgkmcnt(5)\n" \
  "v_mfma_f32_16x16x32_bf16 v[76:79], a[128:131], v[52:55], v[76:79]\n" \
  "buffer_load_dwordx4 v203, s[44:47], s51 offen lds\n" \
  "v_mfma_f32_16x16x32_bf16 v[64:67], a[60:63], v[56:59], v[64:67]\n" \
  "ds_read_b128 v[48:51], v165\n" \
  "s_waitcnt lgkmcnt(5)\n" \
  "v_mfma_f32_16x16x32_bf16 v[72:75], a[60:63], v[60:63], v[72:75]\n" \
  "ds_read_b128 v[52:55], v165 offset:18432\n" \
  "v_mfma_f32_16x16x32_bf16 v[68:71], a[132:135], v[56:59], v[68:71]\n" \
  "s_min_u32 s56, s48, 20\n" \
  "s_cselect_b32 s57, 0, 38\n" \
  "s_add_u32 s57, s57, s48\n" \
  "v_mfma_f32_16x16x32_bf16 v[76:79], a[132:135], v[60:63], v[76:79]\n" \
  "s_add_u32 s48, s48, 1\n" \
  "v_mov_b32 v191, v165\n" \
  "v_mov_b32 v192, v164\n" \
  "s_waitcnt lgkmcnt(5)\n" \
  "v_mfma_f32_16x16x32_bf16 v[64:67], a[64:67], v[32:35], v[64:67]\n" \
  "ds_read_b128 v[56:59], v164\n" \
  "s_waitcnt lgkmcnt(5)\n" \
  "v_mfma_f32_16x16x32_bf16 v[72:75], a[64:67], v[36:39], v[72:75]\n" \
  "ds_read_b128 v[60:63], v164 offset:18432\n" \
  "v_mfma_f32_16x16x32_bf16 v[68:71], a[136:139], v[32:35], v[68:71]\n" \
  "s_add_u32 m0, s52, 8192\n" \
  "s_cmp_eq_u32 s51, 700416\n" \
  "s_cselect_b32 s70, 1437696, s71\n" \
  "v_mfma_f32_16x16x32_bf16 v[76:79], a[136:139], v[36:39], v[76:79]\n" \
  "s_waitcnt lgkmcnt(5)\n" \
  "buffer_load_dwordx4 v204, s[44:47], s51 offen lds\n" \
  "v_mfma_f32_16x16x32_bf16 v[64:67], a[68:71], v[40:43], v[64:67]\n" \
  "ds_read_b128 v[32:35], v165 offset:128\n" \
  "s_add_u32 s51, s51, s70\n" \
  "s_waitcnt lgkmcnt(5)\n" \
  "v_mfma_f32_16x16x32_bf16 v[72:75], a[68:71], v[44:47], v[72:75]\n" \
  "ds_read_b128 v[36:39], v165 offset:18560\n" \
  "s_min_u32 s51, s51, 2322432\n" \
  "v_mfma_f32_16x16x32_bf16 v[68:71], a[140:143], v[40:43], v[68:71]\n" \
  "s_add_u32 s52, s52, 36864\n" \
  "v_mfma_f32_16x16x32_bf16 v[76:79], a[140:143], v[44:47], v[76:79]\n" \
  "s_cmp_ge_u32 s52, 163840\n" \
  "s_cselect_b32 s60, 147456, 0\n" \
  "s_sub_u32 s52, s52, s60\n" \
  "s_cmp_le_u32 s48, 4\n" \
  "s_cbranch_scc1 .LK_mea_%=\n" \
  ".LK_mea_b_%=:\n" \
  "s_waitcnt lgkmcnt(5)\n" \
  "v_mfma_f32_16x16x32_bf16 v[80:83], a[0:3], v[48:51], 0\n" \
  "ds_read_b128 v[40:43], v192 offset:128\n" \
  "s_waitcnt lgkmcnt(5)\n" \
  "v_mfma_f32_16x16x32_bf16 v[88:91], a[0:3], v[52:55], 0\n" \
  "ds_read_b128 v[44:47], v192 offset:18560\n" \
  "s_lshr_b32 s62, s49, 8\n" \
  "v_mfma_f32_16x16x32_bf16 v[84:87], a[72:75], v[48:51], 0\n" \
  "s_add_u32 m0, s52, 0\n" \
  "s_and_b32 s62, s62, 8192\n" \
  "s_xor_b32 s62, s62, s49\n" \
  "v_mfma_f32_16x16x32_bf16 v[92:95], a[72:75], v[52:55], 0\n" \
  "s_waitcnt lgkmcnt(5)\n" \
  "buffer_load_dwordx4 v196, s[44:47], s51 offen lds\n" \
  "v_mfma_f32_16x16x32_bf16 v[80:83], a[4:7], v[56:59], v[80:83]\n" \
  "ds_read_b128 v[48:51], v191 offset:256\n" \
  "s_waitcnt lgkmcnt(5)\n" \
  "v_cvt_pk_bf16_f32 v96, v64, v65\n" \
  "v_mfma_f32_16x16x32_bf16 v[88:91], a[4:7], v[60:63], v[88:91]\n" \
  "ds_read_b128 v[52:55], v191 offset:18688\n" \
  "v_cvt_pk_bf16_f32 v97, v66, v67\n" \
  "v_mfma_f32_16x16x32_bf16 v[84:87], a[76:79], v[56:59], v[84:87]\n" \
  "s_lshl_b32 s63, s57, 14\n" \
  "v_cvt_pk_bf16_f32 v98, v68, v69\n" \
  "v_cvt_pk_bf16_f32 v99, v70, v71\n" \
  "v_mfma_f32_16x16x32_bf16 v[92:95], a[76:79], v[60:63], v[92:95]\n" \
  "s_xor_b32 s63, s63, s59\n" \
  "s_waitcnt lgkmcnt(5)\n" \
  "v_mfma_f32_16x16x32_bf16 v[80:83], a[8:11], v[32:35], v[80:83]\n" \
  "ds_read_b128 v[56:59], v192 offset:256\n" \
  "s_waitcnt lgkmcnt(5)\n" \
  "v_mfma_f32_16x16x32_bf16 v[88:91], a[8:11], v[36:39], v[88:91]\n" \
  "ds_read_b128 v[60:63], v192 offset:18688\n" \
  "v_mfma_f32_16x16x32_bf16 v[84:87], a[80:83], v[32:35], v[84:87]\n" \
  "s_add_u32 m0, s52, 1024\n" \
  "s_waitcnt lgkmcnt(5)\n" \
  "v_mfma_f32_16x16x32_bf16 v[92:95], a[80:83], v[36:39], v[92:95]\n" \
  "buffer_load_dwordx4 v197, s[44:47], s51 offen lds\n" \
  "v_mfma_f32_16x16x32_bf16 v[80:83], a[12:15], v[40:43], v[80:83]\n" \
  "ds_read_b128 v[32:35], v191 offset:384\n" \
  "s_waitcnt lgkmcnt(5)\n" \
  "v_mfma_f32_16x16x32_bf16 v[88:91], a[12:15], v[44:47], v[88:91]\n" \
  "ds_read_b128 v[36:39], v191 offset:18816\n" \
  "v_mfma_f32_16x16x32_bf16 v[84:87], a[84:87], v[40:43], v[84:87]\n" \
  "buffer_store_dwordx4 v[96:99], v190, s[40:43], s62 offen sc1\n" \
  "v_mfma_f32_16x16x32_bf16 v[92:95], a[84:87], v[44:47], v[92:95]\n" \
  "s_waitcnt lgkmcnt(5)\n" \
  "buffer_store_dwordx4 v[96:99], v190, s[40:43], s63 offen sc1\n" \
  "v_mfma_f32_16x16x32_bf16 v[80:83], a[16:19], v[48:51], v[80:83]\n" \
  "ds_read_b128 v[40:43], v192 offset:384\n" \
  "s_waitcnt lgkmcnt(5)\n" \
  "v_cvt_pk_bf16_f32 v100, v72, v73\n" \
  "v_mfma_f32_16x16x32_bf16 v[88:91], a[16:19], v[52:55], v[88:91]\n" \
  "ds_read_b128 v[44:47], v192 offset:18816\n" \
  "v_cvt_pk_bf16_f32 v101, v74, v75\n" \
  "v_mfma_f32_16x16x32_bf16 v[84:87], a[88:91], v[48:51], v[84:87]\n" \
  "s_add_u32 m0, s52, 2048\n" \
  "v_cvt_pk_bf16_f32 v102, v76, v77\n" \
  "v_cvt_pk_bf16_f32 v103, v78, v79\n" \
  "v_mfma_f32_16x16x32_bf16 v[92:95], a[88:91], v[52:55], v[92:95]\n" \
  "s_waitcnt lgkmcnt(5)\n" \
  "buffer_load_dwordx4 v198, s[44:47], s51 offen lds\n" \
  "v_mfma_f32_16x16x32_bf16 v[80:83], a[20:23], v[56:59], v[80:83]\n" \
  "ds_read_b128 v[48:51], v191 offset:512\n" \
  "s_waitcnt lgkmcnt(5)\n" \
  "v_mfma_f32_16x16x32_bf16 v[88:91], a[20:23], v[60:63], v[88:91]\n" \
  "ds_read_b128 v[52:55], v191 offset:18944\n" \
  "v_mfma_f32_16x16x32_bf16 v[84:87], a[92:95], v[56:59], v[84:87]\n" \
  "buffer_store_dwordx4 v[100:103], v190, s[40:43], s62 offen offset:1024 sc1\n" \
  "v_mfma_f32_16x16x32_bf16 v[92:95], a[92:95], v[60:63], v[92:95]\n" \
  "s_waitcnt lgkmcnt(5)\n" \
  "buffer_store_dwordx4 v[100:103], v190, s[40:43], s63 offen offset:1024 sc1\n" \
  "v_mfma_f32_16x16x32_bf16 v[80:83], a[24:27], v[32:35], v[80:83]\n" \
  "ds_read_b128 v[56:59], v192 offset:512\n" \
  "s_add_u32 s49, s49, 524288\n" \
  "s_waitcnt lgkmcnt(5)\n" \
  "v_mfma_f32_16x16x32_bf16 v[88:91], a[24:27], v[36:39], v[88:91]\n" \
  "ds_read_b128 v[60:63], v192 offset:18944\n" \
  "s_bitset0_b32 s49, 30\n" \
  "v_mfma_f32_16x16x32_bf16 v[84:87], a[96:99], v[32:35], v[84:87]\n" \
  "s_add_u32 m0, s52, 3072\n" \
  "s_waitcnt lgkmcnt(5)\n" \
  "v_mfma_f32_16x16x32_bf16 v[92:95], a[96:99], v[36:39], v[92:95]\n" \
  "buffer_load_dwordx4 v199, s[44:47], s51 offen lds\n" \
  "v_mfma_f32_16x16x32_bf16 v[80:83], a[28:31], v[40:43], v[80:83]\n" \
  "ds_read_b128 v[32:35], v191 offset:640\n" \
  "s_waitcnt lgkmcnt(5)\n" \
  "v_mfma_f32_16x16x32_bf16 v[88:91], a[28:31], v[44:47], v[88:91]\n" \
  "ds_read_b128 v[36:39], v191 offset:19072\n" \
  "v_mfma_f32_16x16x32_bf16 v[84:87], a[100:103], v[40:43], v[84:87]\n" \
  "s_cmp_eq_u32 s48, 20\n" \
  "s_cselect_b32 s60, 19922944, 0\n" \
  "s_add_u32 s49, s49, s60\n" \
  "v_mfma_f32_16x16x32_bf16 v[92:95], a[100:103], v[44:47], v[92:95]\n" \
  "s_add_u32 s58, s48, 1\n" \
  "s_and_b32 s58, s58, 3\n" \
  "s_cselect_b32 s58, 0, 147456\n" \
  "s_waitcnt lgkmcnt(5)\n" \
  "v_mfma_f32_16x16x32_bf16 v[80:83], a[32:35], v[48:51], v[80:83]\n" \
  "ds_read_b128 v[40:43], v192 offset:640\n" \
  "s_sub_u32 s58, 36864, s58\n" \
  "s_waitcnt lgkmcnt(5)\n" \
  "v_mfma_f32_16x16x32_bf16 v[88:91], a[32:35], v[52:55], v[88:91]\n" \
  "ds_read_b128 v[44:47], v192 offset:19072\n" \
  "v_add_u32 v165, s58, v191\n" \
  "v_mfma_f32_16x16x32_bf16 v[84:87], a[104:107], v[48:51], v[84:87]\n" \
  "s_add_u32 m0, s52, 4096\n" \
  "v_add_u32 v164, s58, v192\n" \
  "v_mfma_f32_16x16x32_bf16 v[92:95], a[104:107], v[52:55], v[92:95]\n" \
  "s_waitcnt lgkmcnt(5)\n" \
  "buffer_load_dwordx4 v200, s[44:47], s51 offen lds\n" \
  "v_mfma_f32_16x16x32_bf16 v[80:83], a[36:39], v[56:59], v[80:83]\n" \
  "ds_read_b128 v[48:51], v191 offset:768\n" \
  "s_waitcnt lgkmcnt(5)\n" \
  "v_mfma_f32_16x16x32_bf16 v[88:91], a[36:39], v[60:63], v[88:91]\n" \
  "ds_read_b128 v[52:55], v191 offset:19200\n" \
  "v_mfma_f32_16x16x32_bf16 v[84:87], a[108:111], v[56:59], v[84:87]\n" \
  "v_mfma_f32_16x16x32_bf16 v[92:95], a[108:111], v[60:63], v[92:95]\n" \
  "s_waitcnt lgkmcnt(5)\n" \
  "v_mfma_f32_16x16x32_bf16 v[80:83], a[40:43], v[32:35], v[80:83]\n" \
  "ds_read_b128 v[56:59], v192 offset:768\n" \
  "s_waitcnt lgkmcnt(5)\n" \
  "v_mfma_f32_16x16x32_bf16 v[88:91], a[40:43], v[36:39], v[88:91]\n" \
  "ds_read_b128 v[60:63], v192 offset:19200\n" \
  "v_mfma_f32_16x16x32_bf16 v[84:87], a[112:115], v[32:35], v[84:87]\n" \
  "s_add_u32 m0, s52, 5120\n" \
  "s_waitcnt lgkmcnt(5)\n" \
  "v_mfma_f32_16x16x32_bf16 v[92:95], a[112:115], v[36:39], v[92:95]\n" \
  "buffer_load_dwordx4 v201, s[44:47], s51 offen lds\n" \
  "v_mfma_f32_16x16x32_bf16 v[80:83], a[44:47], v[40:43], v[80:83]\n" \
  "ds_read_b128 v[32:35], v191 offset:896\n" \
  "s_waitcnt lgkmcnt(5)\n" \
  "v_mfma_f32_16x16x32_bf16 v[88:91], a[44:47], v[44:47], v[88:91]\n" \
  "ds_read_b128 v[36:39], v191 offset:19328\n" \
  "v_mfma_f32_16x16x32_bf16 v[84:87], a[116:119], v[40:43], v[84:87]\n" \
  "v_mfma_f32_16x16x32_bf16 v[92:95], a[116:119], v[44:47], v[92:95]\n" \
  "s_waitcnt lgkmcnt(5)\n" \
  "v_mfma_f32_16x16x32_bf16 v[80:83], a[48:51], v[48:51], v[80:83]\n" \
  "ds_read_b128 v[40:43], v192 offset:896\n" \
  "s_waitcnt lgkmcnt(5)\n" \
  "v_mfma_f32_16x16x32_bf16 v[88:91], a[48:51], v[52:55], v[88:91]\n" \
  "ds_read_b128 v[44:47], v192 offset:19328\n" \
  "v_mfma_f32_16x16x32_bf16 v[84:87], a[120:123], v[48:51], v[84:87]\n" \
  "s_add_u32 m0, s52, 6144\n" \
  "v_mov_b32 v168, s56\n" \
  "v_mfma_f32_16x16x32_bf16 v[92:95], a[120:123], v[52:55], v[92:95]\n" \
  "s_waitcnt lgkmcnt(5)\n" \
  "buffer_load_dwordx4 v202, s[44:47], s51 offen lds\n" \
  "v_mfma_f32_16x16x32_bf16 v[80:83], a[52:55], v[56:59], v[80:83]\n" \
  "ds_read_b128 v[48:51], v191 offset:1024\n" \
  "s_waitcnt lgkmcnt(5)\n" \
  "v_mfma_f32_16x16x32_bf16 v[88:91], a[52:55], v[60:63], v[88:91]\n" \
  "ds_read_b128 v[52:55], v191 offset:19456\n" \
  "v_mfma_f32_16x16x32_bf16 v[84:87], a[124:127], v[56:59], v[84:87]\n" \
  "s_waitcnt vmcnt(11)\n" \
  "s_barrier\n" \
  "v_mfma_f32_16x16x32_bf16 v[92:95], a[124:127], v[60:63], v[92:95]\n" \
  "s_waitcnt lgkmcnt(5)\n" \
  "buffer_store_dwordx2 v[168:169], v163, s[40:43], 0 offen sc1\n" \
  "v_mfma_f32_16x16x32_bf16 v[80:83], a[56:59], v[32:35], v[80:83]\n" \
  "ds_read_b128 v[56:59], v192 offset:1024\n" \
  "s_waitcnt lgkmcnt(5)\n" \
  "v_mfma_f32_16x16x32_bf16 v[88:91], a[56:59], v[36:39], v[88:91]\n" \
  "ds_read_b128 v[60:63], v192 offset:19456\n" \
  "v_mfma_f32_16x16x32_bf16 v[84:87], a[128:131], v[32:35], v[84:87]\n" \
  "s_add_u32 m0, s52, 7168\n" \
  "s_waitcnt lgkmcnt(5)\n" \
  "v_mfma_f32_16x16x32_bf16 v[92:95], a[128:131], v[36:39], v[92:95]\n" \
  "buffer_load_dwordx4 v203, s[44:47], s51 offen lds\n" \
  "v_mfma_f32_16x16x32_bf16 v[80:83], a[60:63], v[40:43], v[80:83]\n" \
  "ds_read_b128 v[32:35], v165\n" \
  "s_waitcnt lgkmcnt(5)\n" \
  "v_mfma_f32_16x16x32_bf16 v[88:91], a[60:63], v[44:47], v[88:91]\n" \
  "ds_read_b128 v[36:39], v165 offset:18432\n" \
  "v_mfma_f32_16x16x32_bf16 v[84:87], a[132:135], v[40:43], v[84:87]\n" \
  "s_min_u32 s56, s48, 20\n" \
  "s_cselect_b32 s57, 0, 38\n" \
  "s_add_u32 s57, s57, s48\n" \
  "v_mfma_f32_16x16x32_bf16 v[92:95], a[132:135], v[44:47], v[92:95]\n" \
  "s_add_u32 s48, s48, 1\n" \
  "v_mov_b32 v191, v165\n" \
  "v_mov_b32 v192, v164\n" \
  "s_waitcnt lgkmcnt(5)\n" \
  "v_mfma_f32_16x16x32_bf16 v[80:83], a[64:67], v[48:51], v[80:83]\n" \
  "ds_read_b128 v[40:43], v164\n" \
  "s_waitcnt lgkmcnt(5)\n" \
  "v_mfma_f32_16x16x32_bf16 v[88:91], a[64:67], v[52:55], v[88:91]\n" \
  "ds_read_b128 v[44:47], v164 offset:18432\n" \
  "v_mfma_f32_16x16x32_bf16 v[84:87], a[136:139], v[48:51], v[84:87]\n" \
  "s_add_u32 m0, s52, 8192\n" \
  "s_cmp_eq_u32 s51, 700416\n" \
  "s_cselect_b32 s70, 1437696, s71\n" \
  "v_mfma_f32_16x16x32_bf16 v[92:95], a[136:139], v[52:55], v[92:95]\n" \
  "s_waitcnt lgkmcnt(5)\n" \
  "buffer_load_dwordx4 v204, s[44:47], s51 offen lds\n" \
  "v_mfma_f32_16x16x32_bf16 v[80:83], a[68:71], v[56:59], v[80:83]\n" \
  "ds_read_b128 v[48:51], v165 offset:128\n" \
  "s_add_u32 s51, s51, s70\n" \
  "s_waitcnt lgkmcnt(5)\n" \
  "v_mfma_f32_16x16x32_bf16 v[88:91], a[68:71], v[60:63], v[88:91]\n" \
  "ds_read_b128 v[52:55], v165 offset:18560\n" \
  "s_min_u32 s51, s51, 2322432\n" \
  "v_mfma_f32_16x16x32_bf16 v[84:87], a[140:143], v[56:59], v[84:87]\n" \
  "s_add_u32 s52, s52, 36864\n" \
  "v_mfma_f32_16x16x32_bf16 v[92:95], a[140:143], v[60:63], v[92:95]\n" \
  "s_cmp_ge_u32 s52, 163840\n" \
  "s_cselect_b32 s60, 147456, 0\n" \
  "s_sub_u32 s52, s52, s60\n" \
  "s_cmp_le_u32 s48, 4\n" \
  "s_cbranch_scc1 .LK_meb_%=\n" \
  ".LK_meb_b_%=:\n" \
  "s_cmp_lt_u32 s48, 26\n" \
  "s_cbranch_scc1 .LK_ma_%=\n" \
  "s_waitcnt lgkmcnt(0)\n" \
  "s_nop 7\n" \
  "s_lshr_b32 s62, s49, 8\n" \
  "s_and_b32 s62, s62, 8192\n" \
  "s_xor_b32 s62, s62, s49\n" \
  "s_lshl_b32 s63, s57, 14\n" \
  "s_xor_b32 s63, s63, s59\n" \
  "v_cvt_pk_bf16_f32 v96, v80, v81\n" \
  "v_cvt_pk_bf16_f32 v97, v82, v83\n" \
  "v_cvt_pk_bf16_f32 v98, v84, v85\n" \
  "v_cvt_pk_bf16_f32 v99, v86, v87\n" \
  "buffer_store_dwordx4 v[96:99], v190, s[40:43], s62 offen sc1\n" \
  "buffer_store_dwordx4 v[96:99], v190, s[40:43], s63 offen sc1\n" \
  "v_cvt_pk_bf16_f32 v100, v88, v89\n" \
  "v_cvt_pk_bf16_f32 v101, v90, v91\n" \
  "v_cvt_pk_bf16_f32 v102, v92, v93\n" \
  "v_cvt_pk_bf16_f32 v103, v94, v95\n" \
  "buffer_store_dwordx4 v[100:103], v190, s[40:43], s62 offen offset:1024 sc1\n" \
  "buffer_store_dwordx4 v[100:103], v190, s[40:43], s63 offen offset:1024 sc1\n" \
  "s_sub_u32 s49, s49, 22544384\n" \
  "s_mov_b32 s48, 20\n" \
  "s_waitcnt vmcnt(0)\n" \
  "s_barrier\n" \
  "v_mov_b32 v168, 1\n" \
  "s_mov_b64 exec, 2\n" \
  "buffer_store_dwordx2 v[168:169], v167, s[40:43], 0 offen sc1\n" \
  "s_mov_b64 exec, -1\n" \
  "v_subrev_u32 v191, 73728, v191\n" \
  "v_subrev_u32 v192, 73728, v192\n" \
  "s_waitcnt vmcnt(0)\n" \
  "s_cmp_eq_u32 %43, 77\n" \
  "s_cbranch_scc1 .LK_end_%=\n" \
  "s_mov_b64 s[44:45], %32\n" \
  "s_and_b32 s45, s45, 0xffff\n" \
  "s_mov_b32 s46, 4718592\n" \
  "s_add_u32 s60, %35, 0\n" \
  "s_add_u32 m0, s72, 16384\n" \
  "s_nop 0\n" \
  "buffer_load_dwordx4 v170, s[44:47], s60 offen lds\n" \
  "buffer_load_dwordx4 v171, s[44:47], s60 offen offset:1024 lds\n" \
  "buffer_load_dwordx4 v172, s[44:47], s60 offen offset:2048 lds\n" \
  "buffer_load_dwordx4 v173, s[44:47], s60 offen offset:3072 lds\n" \
  "s_add_u32 s60, %35, 524288\n" \
  "s_add_u32 m0, s72, 32768\n" \
  "s_nop 0\n" \
  "buffer_load_dwordx4 v170, s[44:47], s60 offen lds\n" \
  "buffer_load_dwordx4 v171, s[44:47], s60 offen offset:1024 lds\n" \
  "buffer_load_dwordx4 v172, s[44:47], s60 offen offset:2048 lds\n" \
  "buffer_load_dwordx4 v173, s[44:47], s60 offen offset:3072 lds\n" \
  "s_add_u32 s60, %35, 1048576\n" \
  "s_add_u32 m0, s72, 49152\n" \
  "s_nop 0\n" \
  "buffer_load_dwordx4 v170, s[44:47], s60 offen lds\n" \
  "buffer_load_dwordx4 v171, s[44:47], s60 offen offset:1024 lds\n" \
  "buffer_load_dwordx4 v172, s[44:47], s60 offen offset:2048 lds\n" \
  "buffer_load_dwordx4 v173, s[44:47], s60 offen offset:3072 lds\n" \
  "s_add_u32 s60, %35, 1572864\n" \
  "s_add_u32 m0, s72, 65536\n" \
  "s_nop 0\n" \
  "buffer_load_dwordx4 v170, s[44:47], s60 offen lds\n" \
  "buffer_load_dwordx4 v171, s[44:47], s60 offen offset:1024 lds\n" \
  "buffer_load_dwordx4 v172, s[44:47], s60 offen offset:2048 lds\n" \
  "buffer_load_dwordx4 v173, s[44:47], s60 offen offset:3072 lds\n" \
  "s_add_u32 s60, %35, 2097152\n" \
  "s_add_u32 m0, s72, 81920\n" \
  "s_nop 0\n" \
  "buffer_load_dwordx4 v170, s[44:47], s60 offen lds\n" \
  "buffer_load_dwordx4 v171, s[44:47], s60 offen offset:1024 lds\n" \
  "buffer_load_dwordx4 v172, s[44:47], s60 offen offset:2048 lds\n" \
  "buffer_load_dwordx4 v173, s[44:47], s60 offen offset:3072 lds\n" \
  "s_add_u32 s60, %35, 2621440\n" \
  "s_add_u32 m0, s72, 98304\n" \
  "s_nop 0\n" \
  "buffer_load_dwordx4 v170, s[44:47], s60 offen lds\n" \
  "buffer_load_dwordx4 v171, s[44:47], s60 offen offset:1024 lds\n" \
  "buffer_load_dwordx4 v172, s[44:47], s60 offen offset:2048 lds\n" \
  "buffer_load_dwordx4 v173, s[44:47], s60 offen offset:3072 lds\n" \
  "s_add_u32 s60, %35, 3145728\n" \
  "s_add_u32 m0, s72, 114688\n" \
  "s_nop 0\n" \
  "buffer_load_dwordx4 v170, s[44:47], s60 offen lds\n" \
  "buffer_load_dwordx4 v171, s[44:47], s60 offen offset:1024 lds\n" \
  "buffer_load_dwordx4 v172, s[44:47], s60 offen offset:2048 lds\n" \
  "buffer_load_dwordx4 v173, s[44:47], s60 offen offset:3072 lds\n" \
  "s_add_u32 s60, %35, 3670016\n" \
  "s_add_u32 m0, s72, 131072\n" \
  "s_nop 0\n" \
  "buffer_load_dwordx4 v170, s[44:47], s60 offen lds\n" \
  "buffer_load_dwordx4 v171, s[44:47], s60 offen offset:1024 lds\n" \
  "buffer_load_dwordx4 v172, s[44:47], s60 offen offset:2048 lds\n" \
  "buffer_load_dwordx4 v173, s[44:47], s60 offen offset:3072 lds\n" \
  "s_add_u32 s60, %35, 4194304\n" \
  "s_add_u32 m0, s72, 147456\n" \
  "s_nop 0\n" \
  "buffer_load_dwordx4 v170, s[44:47], s60 offen lds\n" \
  "buffer_load_dwordx4 v171, s[44:47], s60 offen offset:1024 lds\n" \
  "buffer_load_dwordx4 v172, s[44:47], s60 offen offset:2048 lds\n" \
  "buffer_load_dwordx4 v173, s[44:47], s60 offen offset:3072 lds\n" \
  "s_waitcnt vmcnt(24)\n" \
  "s_barrier\n" \
  "v_add_u32 v186, 16384, v182\n" \
  "ds_read_b64_tr_b16 a[144:145], v186\n" \
  "ds_read_b64_tr_b16 a[146:147], v186 offset:1024\n" \
  "ds_read_b64_tr_b16 a[148:149], v186 offset:8192\n" \
  "ds_read_b64_tr_b16 a[150:151], v186 offset:9216\n" \
  "v_add_u32 v187, 16384, v183\n" \
  "ds_read_b64_tr_b16 a[216:217], v187\n" \
  "ds_read_b64_tr_b16 a[218:219], v187 offset:1024\n" \
  "ds_read_b64_tr_b16 a[220:221], v187 offset:8192\n" \
  "ds_read_b64_tr_b16 a[222:223], v187 offset:9216\n" \
  "v_add_u32 v186, 32768, v182\n" \
  "ds_read_b64_tr_b16 a[152:153], v186\n" \
  "ds_read_b64_tr_b16 a[154:155], v186 offset:1024\n" \
  "ds_read_b64_tr_b16 a[156:157], v186 offset:8192\n" \
  "ds_read_b64_tr_b16 a[158:159], v186 offset:9216\n" \
  "v_add_u32 v187, 32768, v183\n" \
  "ds_read_b64_tr_b16 a[224:225], v187\n" \
  "ds_read_b64_tr_b16 a[226:227], v187 offset:1024\n" \
  "ds_read_b64_tr_b16 a[228:229], v187 offset:8192\n" \
  "ds_read_b64_tr_b16 a[230:231], v187 offset:9216\n" \
  "v_add_u32 v186, 49152, v182\n" \
  "ds_read_b64_tr_b16 a[160:161], v186\n" \
  "ds_read_b64_tr_b16 a[162:163], v186 offset:1024\n" \
  "ds_read_b64_tr_b16 a[164:165], v186 offset:8192\n" \
  "ds_read_b64_tr_b16 a[166:167], v186 offset:9216\n" \
  "v_add_u32 v187, 49152, v183\n" \
  "ds_read_b64_tr_b16 a[232:233], v187\n" \
  "ds_read_b64_tr_b16 a[234:235], v187 offset:1024\n" \
  "ds_read_b64_tr_b16 a[236:237], v187 offset:8192\n" \
  "ds_read_b64_tr_b16 a[238:239], v187 offset:9216\n" \
  "s_waitcnt lgkmcnt(0)\n" \
  "s_barrier\n" \
  "s_mov_b64 s[44:45], %33\n" \
  "s_and_b32 s45, s45, 0xffff\n" \
  "s_mov_b32 s46, 2359296\n" \
  "s_mov_b32 s51, 737280\n" \
  "s_mul_i32 s52, %34, 9216\n" \
  "s_add_u32 s52, s52, 16384\n" \
  "s_add_u32 m0, s52, 0\n" \
  "s_nop 0\n" \
  "buffer_load_dwordx4 v196, s[44:47], s51 offen lds\n" \
  "s_add_u32 m0, s52, 1024\n" \
  "s_nop 0\n" \
  "buffer_load_dwordx4 v197, s[44:47], s51 offen lds\n" \
  "s_add_u32 m0, s52, 2048\n" \
  "s_nop 0\n" \
  "buffer_load_dwordx4 v198, s[44:47], s51 offen lds\n" \
  "s_add_u32 m0, s52, 3072\n" \
  "s_nop 0\n" \
  "buffer_load_dwordx4 v199, s[44:47], s51 offen lds\n" \
  "s_add_u32 m0, s52, 4096\n" \
  "s_nop 0\n" \
  "buffer_load_dwordx4 v200, s[44:47], s51 offen lds\n" \
  "s_add_u32 m0, s52, 5120\n" \
  "s_nop 0\n" \
  "buffer_load_dwordx4 v201, s[44:47], s51 offen lds\n" \
  "s_add_u32 m0, s52, 6144\n" \
  "s_nop 0\n" \
  "buffer_load_dwordx4 v202, s[44:47], s51 offen lds\n" \
  "s_add_u32 m0, s52, 7168\n" \
  "s_nop 0\n" \
  "buffer_load_dwordx4 v203, s[44:47], s51 offen lds\n" \
  "s_add_u32 m0, s52, 8192\n" \
  "s_nop 0\n" \
  "buffer_load_dwordx4 v204, s[44:47], s51 offen lds\n" \
  "s_add_u32 s51, s51, 36864\n" \
  "s_min_u32 s51, s51, 2322432\n" \
  "s_add_u32 s52, s52, 36864\n" \
  "s_cmp_ge_u32 s52, 163840\n" \
  "s_cselect_b32 s60, 147456, 0\n" \
  "s_sub_u32 s52, s52, s60\n" \
  "s_waitcnt vmcnt(21)\n" \
  "s_barrier\n" \
  "v_add_u32 v186, 65536, v182\n" \
  "ds_read_b64_tr_b16 a[168:169], v186\n" \
  "ds_read_b64_tr_b16 a[170:171], v186 offset:1024\n" \
  "ds_read_b64_tr_b16 a[172:173], v186 offset:8192\n" \
  "ds_read_b64_tr_b16 a[174:175], v186 offset:9216\n" \
  "v_add_u32 v187, 65536, v183\n" \
  "ds_read_b64_tr_b16 a[240:241], v187\n" \
  "ds_read_b64_tr_b16 a[242:243], v187 offset:1024\n" \
  "ds_read_b64_tr_b16 a[244:245], v187 offset:8192\n" \
  "ds_read_b64_tr_b16 a[246:247], v187 offset:9216\n" \
  "v_add_u32 v186, 81920, v182\n" \
  "ds_read_b64_tr_b16 a[176:177], v186\n" \
  "ds_read_b64_tr_b16 a[178:179], v186 offset:1024\n" \
  "ds_read_b64_tr_b16 a[180:181], v186 offset:8192\n" \
  "ds_read_b64_tr_b16 a[182:183], v186 offset:9216\n" \
  "v_add_u32 v187, 81920, v183\n" \
  "ds_read_b64_tr_b16 a[248:249], v187\n" \
  "ds_read_b64_tr_b16 a[250:251], v187 offset:1024\n" \
  "ds_read_b64_tr_b16 a[252:253], v187 offset:8192\n" \
  "ds_read_b64_tr_b16 a[254:255], v187 offset:9216\n" \
  "v_add_u32 v186, 98304, v182\n" \
  "ds_read_b64_tr_b16 a[184:185], v186\n" \
  "ds_read_b64_tr_b16 a[186:187], v186 offset:1024\n" \
  "ds_read_b64_tr_b16 a[188:189], v186 offset:8192\n" \
  "ds_read_b64_tr_b16 a[190:191], v186 offset:9216\n" \
  "v_add_u32 v187, 98304, v183\n" \
  "ds_read_b64_tr_b16 v[0:1], v187\n" \
  "ds_read_b64_tr_b16 v[2:3], v187 offset:1024\n" \
  "ds_read_b64_tr_b16 v[4:5], v187 offset:8192\n" \
  "ds_read_b64_tr_b16 v[6:7], v187 offset:9216\n" \
  "s_waitcnt lgkmcnt(0)\n" \
  "s_barrier\n" \
  "s_add_u32 m0, s52, 0\n" \
  "s_nop 0\n" \
  "buffer_load_dwordx4 v196, s[44:47], s51 offen lds\n" \
  "s_add_u32 m0, s52, 1024\n" \
  "s_nop 0\n" \
  "buffer_load_dwordx4 v197, s[44:47], s51 offen lds\n" \
  "s_add_u32 m0, s52, 2048\n" \
  "s_nop 0\n" \
  "buffer_load_dwordx4 v198, s[44:47], s51 offen lds\n" \
  "s_add_u32 m0, s52, 3072\n" \
  "s_nop 0\n" \
  "buffer_load_dwordx4 v199, s[44:47], s51 offen lds\n" \
  "s_add_u32 m0, s52, 4096\n" \
  "s_nop 0\n" \
  "buffer_load_dwordx4 v200, s[44:47], s51 offen lds\n" \
  "s_add_u32 m0, s52, 5120\n" \
  "s_nop 0\n" \
  "buffer_load_dwordx4 v201, s[44:47], s51 offen lds\n" \
  "s_add_u32 m0, s52, 6144\n" \
  "s_nop 0\n" \
  "buffer_load_dwordx4 v202, s[44:47], s51 offen lds\n" \
  "s_add_u32 m0, s52, 7168\n" \
  "s_nop 0\n" \
  "buffer_load_dwordx4 v203, s[44:47], s51 offen lds\n" \
  "s_add_u32 m0, s52, 8192\n" \
  "s_nop 0\n" \
  "buffer_load_dwordx4 v204, s[44:47], s51 offen lds\n" \
  "s_add_u32 s51, s51, 36864\n" \
  "s_min_u32 s51, s51, 2322432\n" \
  "s_add_u32 s52, s52, 36864\n" \
  "s_cmp_ge_u32 s52, 163840\n" \
  "s_cselect_b32 s60, 147456, 0\n" \
  "s_sub_u32 s52, s52, s60\n" \
  "s_waitcnt vmcnt(18)\n" \
  "s_barrier\n" \
  "v_add_u32 v186, 114688, v182\n" \
  "ds_read_b64_tr_b16 a[192:193], v186\n" \
  "ds_read_b64_tr_b16 a[194:195], v186 offset:1024\n" \
  "ds_read_b64_tr_b16 a[196:197], v186 offset:8192\n" \
  "ds_read_b64_tr_b16 a[198:199], v186 offset:9216\n" \
  "v_add_u32 v187, 114688, v183\n" \
  "ds_read_b64_tr_b16 v[8:9], v187\n" \
  "ds_read_b64_tr_b16 v[10:11], v187 offset:1024\n" \
  "ds_read_b64_tr_b16 v[12:13], v187 offset:8192\n" \
  "ds_read_b64_tr_b16 v[14:15], v187 offset:9216\n" \
  "v_add_u32 v186, 131072, v182\n" \
  "ds_read_b64_tr_b16 a[200:201], v186\n" \
  "ds_read_b64_tr_b16 a[202:203], v186 offset:1024\n" \
  "ds_read_b64_tr_b16 a[204:205], v186 offset:8192\n" \
  "ds_read_b64_tr_b16 a[206:207], v186 offset:9216\n" \
  "v_add_u32 v187, 131072, v183\n" \
  "ds_read_b64_tr_b16 v[16:17], v187\n" \
  "ds_read_b64_tr_b16 v[18:19], v187 offset:1024\n" \
  "ds_read_b64_tr_b16 v[20:21], v187 offset:8192\n" \
  "ds_read_b64_tr_b16 v[22:23], v187 offset:9216\n" \
  "v_add_u32 v186, 147456, v182\n" \
  "ds_read_b64_tr_b16 a[208:209], v186\n" \
  "ds_read_b64_tr_b16 a[210:211], v186 offset:1024\n" \
  "ds_read_b64_tr_b16 a[212:213], v186 offset:8192\n" \
  "ds_read_b64_tr_b16 a[214:215], v186 offset:9216\n" \
  "v_add_u32 v187, 147456, v183\n" \
  "ds_read_b64_tr_b16 v[24:25], v187\n" \
  "ds_read_b64_tr_b16 v[26:27], v187 offset:1024\n" \
  "ds_read_b64_tr_b16 v[28:29], v187 offset:8192\n" \
  "ds_read_b64_tr_b16 v[30:31], v187 offset:9216\n" \
  "s_waitcnt lgkmcnt(0)\n" \
  "s_waitcnt vmcnt(9)\n" \
  "s_barrier\n" \
  "ds_read_b128 v[32:35], v191\n" \
  "ds_read_b128 v[36:39], v191 offset:18432\n" \
  "ds_read_b128 v[40:43], v192\n" \
  "ds_read_b128 v[44:47], v192 offset:18432\n" \
  "ds_read_b128 v[48:51], v191 offset:128\n" \
  "ds_read_b128 v[52:55], v191 offset:18560\n" \
  "s_waitcnt lgkmcnt(0)\n" \
  ".LK_loop_%=:\n" \
  "s_waitcnt lgkmcnt(5)\n" \
  "v_mfma_f32_16x16x32_bf16 v[64:67], a[0:3], v[32:35], 0\n" \
  "ds_read_b128 v[56:59], v192 offset:128\n" \
  "s_waitcnt lgkmcnt(5)\n" \
  "v_mfma_f32_16x16x32_bf16 v[72:75], a[0:3], v[36:39], 0\n" \
  "ds_read_b128 v[60:63], v192 offset:18560\n" \
  "s_lshr_b32 s62, s50, 8\n" \
  "v_mfma_f32_16x16x32_bf16 v[68:71], a[72:75], v[32:35], 0\n" \
  "s_and_b32 s62, s62, 8192\n" \
  "s_xor_b32 s62, s62, s50\n" \
  "v_cvt_pk_bf16_f32 v96, v80, v81\n" \
  "v_mfma_f32_16x16x32_bf16 v[76:79], a[72:75], v[36:39], 0\n" \
  "s_waitcnt lgkmcnt(5)\n" \
  "v_cvt_pk_bf16_f32 v97, v82, v83\n" \
  "v_cvt_pk_bf16_f32 v98, v84, v85\n" \
  "v_mfma_f32_16x16x32_bf16 v[64:67], a[4:7], v[40:43], v[64:67]\n" \
  "ds_read_b128 v[32:35], v191 offset:256\n" \
  "s_waitcnt lgkmcnt(5)\n" \
  "v_cvt_pk_bf16_f32 v99, v86, v87\n" \
  "v_mfma_f32_16x16x32_bf16 v[72:75], a[4:7], v[44:47], v[72:75]\n" \
  "ds_read_b128 v[36:39], v191 offset:18688\n" \
  "v_mfma_f32_16x16x32_bf16 v[68:71], a[76:79], v[40:43], v[68:71]\n" \
  "buffer_store_dwordx4 v[96:99], v190, s[40:43], s62 offen sc1\n" \
  "v_mfma_f32_16x16x32_bf16 v[76:79], a[76:79], v[44:47], v[76:79]\n" \
  "s_waitcnt lgkmcnt(5)\n" \
  "v_cvt_pk_bf16_f32 v100, v88, v89\n" \
  "v_cvt_pk_bf16_f32 v101, v90, v91\n" \
  "v_mfma_f32_16x16x32_bf16 v[64:67], a[8:11], v[48:51], v[64:67]\n" \
  "ds_read_b128 v[40:43], v192 offset:256\n" \
  "s_waitcnt lgkmcnt(5)\n" \
  "v_cvt_pk_bf16_f32 v102, v92, v93\n" \
  "v_mfma_f32_16x16x32_bf16 v[72:75], a[8:11], v[52:55], v[72:75]\n" \
  "ds_read_b128 v[44:47], v192 offset:18688\n" \
  "v_cvt_pk_bf16_f32 v103, v94, v95\n" \
  "v_mfma_f32_16x16x32_bf16 v[68:71], a[80:83], v[48:51], v[68:71]\n" \
  "buffer_store_dwordx4 v[100:103], v190, s[40:43], s62 offen offset:1024 sc1\n" \
  "v_mfma_f32_16x16x32_bf16 v[76:79], a[80:83], v[52:55], v[76:79]\n" \
  "s_add_u32 s50, s50, 524288\n" \
  "s_bitset0_b32 s50, 30\n" \
  "s_waitcnt lgkmcnt(5)\n" \
  "v_mfma_f32_16x16x32_bf16 v[64:67], a[12:15], v[56:59], v[64:67]\n" \
  "ds_read_b128 v[48:51], v191 offset:384\n" \
  "s_waitcnt lgkmcnt(5)\n" \
  "v_mfma_f32_16x16x32_bf16 v[72:75], a[12:15], v[60:63], v[72:75]\n" \
  "ds_read_b128 v[52:55], v191 offset:18816\n" \
  "v_mfma_f32_16x16x32_bf16 v[68:71], a[84:87], v[56:59], v[68:71]\n" \
  "v_mfma_f32_16x16x32_bf16 v[76:79], a[84:87], v[60:63], v[76:79]\n" \
  "s_waitcnt lgkmcnt(5)\n" \
  "v_mfma_f32_16x16x32_bf16 v[64:67], a[16:19], v[32:35], v[64:67]\n" \
  "ds_read_b128 v[56:59], v192 offset:384\n" \
  "s_waitcnt lgkmcnt(5)\n" \
  "v_mfma_f32_16x16x32_bf16 v[72:75], a[16:19], v[36:39], v[72:75]\n" \
  "ds_read_b128 v[60:63], v192 offset:18816\n" \
  "v_mfma_f32_16x16x32_bf16 v[68:71], a[88:91], v[32:35], v[68:71]\n" \
  "v_mfma_f32_16x16x32_bf16 v[76:79], a[88:91], v[36:39], v[76:79]\n" \
  "s_waitcnt lgkmcnt(5)\n" \
  "v_mfma_f32_16x16x32_bf16 v[64:67], a[20:23], v[40:43], v[64:67]\n" \
  "ds_read_b128 v[32:35], v191 offset:512\n" \
  "s_waitcnt lgkmcnt(5)\n" \
  "v_mfma_f32_16x16x32_bf16 v[72:75], a[20:23], v[44:47], v[72:75]\n" \
  "ds_read_b128 v[36:39], v191 offset:18944\n" \
  "v_mfma_f32_16x16x32_bf16 v[68:71], a[92:95], v[40:43], v[68:71]\n" \
  "v_mfma_f32_16x16x32_bf16 v[76:79], a[92:95], v[44:47], v[76:79]\n" \
  "s_waitcnt lgkmcnt(5)\n" \
  "v_mfma_f32_16x16x32_bf16 v[64:67], a[24:27], v[48:51], v[64:67]\n" \
  "ds_read_b128 v[40:43], v192 offset:512\n" \
  "s_waitcnt lgkmcnt(5)\n" \
  "v_mfma_f32_16x16x32_bf16 v[72:75], a[24:27], v[52:55], v[72:75]\n" \
  "ds_read_b128 v[44:47], v192 offset:18944\n" \
  "v_mfma_f32_16x16x32_bf16 v[68:71], a[96:99], v[48:51], v[68:71]\n" \
  "v_mfma_f32_16x16x32_bf16 v[76:79], a[96:99], v[52:55], v[76:79]\n" \
  "s_waitcnt lgkmcnt(5)\n" \
  "v_mfma_f32_16x16x32_bf16 v[64:67], a[28:31], v[56:59], v[64:67]\n" \
  "ds_read_b128 v[48:51], v191 offset:640\n" \
  "s_waitcnt lgkmcnt(5)\n" \
  "v_mfma_f32_16x16x32_bf16 v[72:75], a[28:31], v[60:63], v[72:75]\n" \
  "ds_read_b128 v[52:55], v191 offset:19072\n" \
  "v_mfma_f32_16x16x32_bf16 v[68:71], a[100:103], v[56:59], v[68:71]\n" \
  "s_add_u32 m0, s52, 0\n" \
  "s_waitcnt lgkmcnt(5)\n" \
  "v_mfma_f32_16x16x32_bf16 v[76:79], a[100:103], v[60:63], v[76:79]\n" \
  "buffer_load_dwordx4 v196, s[44:47], s51 offen lds\n" \
  "v_mfma_f32_16x16x32_bf16 v[64:67], a[32:35], v[32:35], v[64:67]\n" \
  "ds_read_b128 v[56:59], v192 offset:640\n" \
  "s_waitcnt lgkmcnt(5)\n" \
  "v_mfma_f32_16x16x32_bf16 v[72:75], a[32:35], v[36:39], v[72:75]\n" \
  "ds_read_b128 v[60:63], v192 offset:19072\n" \
  "v_mfma_f32_16x16x32_bf16 v[68:71], a[104:107], v[32:35], v[68:71]\n" \
  "v_mfma_f32_16x16x32_bf16 v[76:79], a[104:107], v[36:39], v[76:79]\n" \
  "s_waitcnt lgkmcnt(5)\n" \
  "v_mfma_f32_16x16x32_bf16 v[64:67], a[36:39], v[40:43], v[64:67]\n" \
  "ds_read_b128 v[32:35], v191 offset:768\n" \
  "s_waitcnt lgkmcnt(5)\n" \
  "v_mfma_f32_16x16x32_bf16 v[72:75], a[36:39], v[44:47], v[72:75]\n" \
  "ds_read_b128 v[36:39], v191 offset:19200\n" \
  "v_mfma_f32_16x16x32_bf16 v[68:71], a[108:111], v[40:43], v[68:71]\n" \
  "s_add_u32 m0, s52, 1024\n" \
  "s_waitcnt lgkmcnt(5)\n" \
  "v_mfma_f32_16x16x32_bf16 v[76:79], a[108:111], v[44:47], v[76:79]\n" \
  "buffer_load_dwordx4 v197, s[44:47], s51 offen lds\n" \
  "v_mfma_f32_16x16x32_bf16 v[64:67], a[40:43], v[48:51], v[64:67]\n" \
  "ds_read_b128 v[40:43], v192 offset:768\n" \
  "s_waitcnt lgkmcnt(5)\n" \
  "v_mfma_f32_16x16x32_bf16 v[72:75], a[40:43], v[52:55], v[72:75]\n" \
  "ds_read_b128 v[44:47], v192 offset:19200\n" \
  "v_mfma_f32_16x16x32_bf16 v[68:71], a[112:115], v[48:51], v[68:71]\n" \
  "v_mfma_f32_16x16x32_bf16 v[76:79], a[112:115], v[52:55], v[76:79]\n" \
  "s_waitcnt lgkmcnt(5)\n" \
  "v_mfma_f32_16x16x32_bf16 v[64:67], a[44:47], v[56:59], v[64:67]\n" \
  "ds_read_b128 v[48:51], v191 offset:896\n" \
  "s_waitcnt lgkmcnt(5)\n" \
  "v_mfma_f32_16x16x32_bf16 v[72:75], a[44:47], v[60:63], v[72:75]\n" \
  "ds_read_b128 v[52:55], v191 offset:19328\n" \
  "v_mfma_f32_16x16x32_bf16 v[68:71], a[116:119], v[56:59], v[68:71]\n" \
  "s_add_u32 m0, s52, 2048\n" \
  "v_add_u32 v168, s48, v166\n" \
  "v_mfma_f32_16x16x32_bf16 v[76:79], a[116:119], v[60:63], v[76:79]\n" \
  "s_waitcnt lgkmcnt(5)\n" \
  "buffer_load_dwordx4 v198, s[44:47], s51 offen lds\n" \
  "v_mfma_f32_16x16x32_bf16 v[64:67], a[48:51], v[32:35], v[64:67]\n" \
  "ds_read_b128 v[56:59], v192 offset:896\n" \
  "s_waitcnt lgkmcnt(5)\n" \
  "v_mfma_f32_16x16x32_bf16 v[72:75], a[48:51], v[36:39], v[72:75]\n" \
  "ds_read_b128 v[60:63], v192 offset:19328\n" \
  "v_mfma_f32_16x16x32_bf16 v[68:71], a[120:123], v[32:35], v[68:71]\n" \
  "s_waitcnt vmcnt(5)\n" \
  "s_barrier\n" \
  "v_mfma_f32_16x16x32_bf16 v[76:79], a[120:123], v[36:39], v[76:79]\n" \
  "s_waitcnt lgkmcnt(5)\n" \
  "buffer_store_dwordx2 v[168:169], v167, s[40:43], 0 offen sc1\n" \
  "v_mfma_f32_16x16x32_bf16 v[64:67], a[52:55], v[40:43], v[64:67]\n" \
  "ds_read_b128 v[32:35], v191 offset:1024\n" \
  "s_waitcnt lgkmcnt(5)\n" \
  "v_mfma_f32_16x16x32_bf16 v[72:75], a[52:55], v[44:47], v[72:75]\n" \
  "ds_read_b128 v[36:39], v191 offset:19456\n" \
  "v_mfma_f32_16x16x32_bf16 v[68:71], a[124:127], v[40:43], v[68:71]\n" \
  "s_add_u32 m0, s52, 3072\n" \
  "s_waitcnt lgkmcnt(5)\n" \
  "v_mfma_f32_16x16x32_bf16 v[76:79], a[124:127], v[44:47], v[76:79]\n" \
  "buffer_load_dwordx4 v199, s[44:47], s51 offen lds\n" \
  "v_mfma_f32_16x16x32_bf16 v[64:67], a[56:59], v[48:51], v[64:67]\n" \
  "ds_read_b128 v[40:43], v192 offset:1024\n" \
  "s_waitcnt lgkmcnt(5)\n" \
  "v_mfma_f32_16x16x32_bf16 v[72:75], a[56:59], v[52:55], v[72:75]\n" \
  "ds_read_b128 v[44:47], v192 offset:19456\n" \
  "v_mfma_f32_16x16x32_bf16 v[68:71], a[128:131], v[48:51], v[68:71]\n" \
  "v_mfma_f32_16x16x32_bf16 v[76:79], a[128:131], v[52:55], v[76:79]\n" \
  "s_waitcnt lgkmcnt(5)\n" \
  "v_mfma_f32_16x16x32_bf16 v[64:67], a[60:63], v[56:59], v[64:67]\n" \
  "ds_read_b128 v[48:51], v191\n" \
  "s_waitcnt lgkmcnt(5)\n" \
  "v_mfma_f32_16x16x32_bf16 v[72:75], a[60:63], v[60:63], v[72:75]\n" \
  "ds_read_b128 v[52:55], v191 offset:18432\n" \
  "v_mfma_f32_16x16x32_bf16 v[68:71], a[132:135], v[56:59], v[68:71]\n" \
  "s_add_u32 m0, s52, 4096\n" \
  "s_waitcnt lgkmcnt(5)\n" \
  "v_mfma_f32_16x16x32_bf16 v[76:79], a[132:135], v[60:63], v[76:79]\n" \
  "buffer_load_dwordx4 v200, s[44:47], s51 offen lds\n" \
  "v_mfma_f32_16x16x32_bf16 v[64:67], a[64:67], v[32:35], v[64:67]\n" \
  "ds_read_b128 v[56:59], v192\n" \
  "s_waitcnt lgkmcnt(5)\n" \
  "v_mfma_f32_16x16x32_bf16 v[72:75], a[64:67], v[36:39], v[72:75]\n" \
  "ds_read_b128 v[60:63], v192 offset:18432\n" \
  "v_mfma_f32_16x16x32_bf16 v[68:71], a[136:139], v[32:35], v[68:71]\n" \
  "v_mfma_f32_16x16x32_bf16 v[76:79], a[136:139], v[36:39], v[76:79]\n" \
  "s_waitcnt lgkmcnt(5)\n" \
  "v_mfma_f32_16x16x32_bf16 v[64:67], a[68:71], v[40:43], v[64:67]\n" \
  "ds_read_b128 v[32:35], v191 offset:128\n" \
  "s_waitcnt lgkmcnt(5)\n" \
  "v_mfma_f32_16x16x32_bf16 v[72:75], a[68:71], v[44:47], v[72:75]\n" \
  "ds_read_b128 v[36:39], v191 offset:18560\n" \
  "v_mfma_f32_16x16x32_bf16 v[68:71], a[140:143], v[40:43], v[68:71]\n" \
  "v_mfma_f32_16x16x32_bf16 v[76:79], a[140:143], v[44:47], v[76:79]\n" \
  "s_waitcnt lgkmcnt(5)\n" \
  "v_mfma_f32_16x16x32_bf16 v[80:83], a[144:147], v[48:51], 0\n" \
  "ds_read_b128 v[40:43], v192 offset:128\n" \
  "s_waitcnt lgkmcnt(5)\n" \
  "v_mfma_f32_16x16x32_bf16 v[88:91], a[144:147], v[52:55], 0\n" \
  "ds_read_b128 v[44:47], v192 offset:18560\n" \
  "s_lshr_b32 s62, s49, 8\n" \
  "v_mfma_f32_16x16x32_bf16 v[84:87], a[216:219], v[48:51], 0\n" \
  "s_and_b32 s62, s62, 8192\n" \
  "s_xor_b32 s62, s62, s49\n" \
  "v_cvt_pk_bf16_f32 v96, v64, v65\n" \
  "v_mfma_f32_16x16x32_bf16 v[92:95], a[216:219], v[52:55], 0\n" \
  "s_waitcnt lgkmcnt(5)\n" \
  "v_cvt_pk_bf16_f32 v97, v66, v67\n" \
  "v_cvt_pk_bf16_f32 v98, v68, v69\n" \
  "v_mfma_f32_16x16x32_bf16 v[80:83], a[148:151], v[56:59], v[80:83]\n" \
  "ds_read_b128 v[48:51], v191 offset:256\n" \
  "s_waitcnt lgkmcnt(5)\n" \
  "v_cvt_pk_bf16_f32 v99, v70, v71\n" \
  "v_mfma_f32_16x16x32_bf16 v[88:91], a[148:151], v[60:63], v[88:91]\n" \
  "ds_read_b128 v[52:55], v191 offset:18688\n" \
  "s_lshl_b32 s63, s48, 14\n" \
  "v_mfma_f32_16x16x32_bf16 v[84:87], a[220:223], v[56:59], v[84:87]\n" \
  "s_xor_b32 s63, s63, s59\n" \
  "s_waitcnt lgkmcnt(5)\n" \
  "v_mfma_f32_16x16x32_bf16 v[92:95], a[220:223], v[60:63], v[92:95]\n" \
  "buffer_store_dwordx4 v[96:99], v190, s[40:43], s62 offen sc1\n" \
  "v_mfma_f32_16x16x32_bf16 v[80:83], a[152:155], v[32:35], v[80:83]\n" \
  "ds_read_b128 v[56:59], v192 offset:256\n" \
  "s_waitcnt lgkmcnt(5)\n" \
  "v_mfma_f32_16x16x32_bf16 v[88:91], a[152:155], v[36:39], v[88:91]\n" \
  "ds_read_b128 v[60:63], v192 offset:18688\n" \
  "v_mfma_f32_16x16x32_bf16 v[84:87], a[224:227], v[32:35], v[84:87]\n" \
  "buffer_store_dwordx4 v[96:99], v190, s[40:43], s63 offen sc1\n" \
  "v_mfma_f32_16x16x32_bf16 v[92:95], a[224:227], v[36:39], v[92:95]\n" \
  "s_waitcnt lgkmcnt(5)\n" \
  "v_cvt_pk_bf16_f32 v100, v72, v73\n" \
  "v_cvt_pk_bf16_f32 v101, v74, v75\n" \
  "v_mfma_f32_16x16x32_bf16 v[80:83], a[156:159], v[40:43], v[80:83]\n" \
  "ds_read_b128 v[32:35], v191 offset:384\n" \
  "s_waitcnt lgkmcnt(5)\n" \
  "v_cvt_pk_bf16_f32 v102, v76, v77\n" \
  "v_mfma_f32_16x16x32_bf16 v[88:91], a[156:159], v[44:47], v[88:91]\n" \
  "ds_read_b128 v[36:39], v191 offset:18816\n" \
  "v_cvt_pk_bf16_f32 v103, v78, v79\n" \
  "v_mfma_f32_16x16x32_bf16 v[84:87], a[228:231], v[40:43], v[84:87]\n" \
  "buffer_store_dwordx4 v[100:103], v190, s[40:43], s62 offen offset:1024 sc1\n" \
  "v_mfma_f32_16x16x32_bf16 v[92:95], a[228:231], v[44:47], v[92:95]\n" \
  "s_waitcnt lgkmcnt(5)\n" \
  "buffer_store_dwordx4 v[100:103], v190, s[40:43], s63 offen offset:1024 sc1\n" \
  "v_mfma_f32_16x16x32_bf16 v[80:83], a[160:163], v[48:51], v[80:83]\n" \
  "ds_read_b128 v[40:43], v192 offset:384\n" \
  "s_add_u32 s49, s49, 524288\n" \
  "s_waitcnt lgkmcnt(5)\n" \
  "v_mfma_f32_16x16x32_bf16 v[88:91], a[160:163], v[52:55], v[88:91]\n" \
  "ds_read_b128 v[44:47], v192 offset:18816\n" \
  "s_add_u32 s58, s48, 1\n" \
  "v_mfma_f32_16x16x32_bf16 v[84:87], a[232:235], v[48:51], v[84:87]\n" \
  "s_and_b32 s58, s58, 3\n" \
  "s_cselect_b32 s58, 0, 147456\n" \
  "s_sub_u32 s58, 36864, s58\n" \
  "v_mfma_f32_16x16x32_bf16 v[92:95], a[232:235], v[52:55], v[92:95]\n" \
  "s_waitcnt lgkmcnt(5)\n" \
  "v_add_u32 v165, s58, v191\n" \
  "v_add_u32 v164, s58, v192\n" \
  "v_mfma_f32_16x16x32_bf16 v[80:83], a[164:167], v[56:59], v[80:83]\n" \
  "ds_read_b128 v[48:51], v191 offset:512\n" \
  "s_waitcnt lgkmcnt(5)\n" \
  "v_mfma_f32_16x16x32_bf16 v[88:91], a[164:167], v[60:63], v[88:91]\n" \
  "ds_read_b128 v[52:55], v191 offset:18944\n" \
  "v_mfma_f32_16x16x32_bf16 v[84:87], a[236:239], v[56:59], v[84:87]\n" \
  "v_mfma_f32_16x16x32_bf16 v[92:95], a[236:239], v[60:63], v[92:95]\n" \
  "s_waitcnt lgkmcnt(5)\n" \
  "v_mfma_f32_16x16x32_bf16 v[80:83], a[168:171], v[32:35], v[80:83]\n" \
  "ds_read_b128 v[56:59], v192 offset:512\n" \
  "s_waitcnt lgkmcnt(5)\n" \
  "v_mfma_f32_16x16x32_bf16 v[88:91], a[168:171], v[36:39], v[88:91]\n" \
  "ds_read_b128 v[60:63], v192 offset:18944\n" \
  "v_mfma_f32_16x16x32_bf16 v[84:87], a[240:243], v[32:35], v[84:87]\n" \
  "v_mfma_f32_16x16x32_bf16 v[92:95], a[240:243], v[36:39], v[92:95]\n" \
  "s_waitcnt lgkmcnt(5)\n" \
  "v_mfma_f32_16x16x32_bf16 v[80:83], a[172:175], v[40:43], v[80:83]\n" \
  "ds_read_b128 v[32:35], v191 offset:640\n" \
  "s_waitcnt lgkmcnt(5)\n" \
  "v_mfma_f32_16x16x32_bf16 v[88:91], a[172:175], v[44:47], v[88:91]\n" \
  "ds_read_b128 v[36:39], v191 offset:19072\n" \
  "v_mfma_f32_16x16x32_bf16 v[84:87], a[244:247], v[40:43], v[84:87]\n" \
  "v_mfma_f32_16x16x32_bf16 v[92:95], a[244:247], v[44:47], v[92:95]\n" \
  "s_waitcnt lgkmcnt(5)\n" \
  "v_mfma_f32_16x16x32_bf16 v[80:83], a[176:179], v[48:51], v[80:83]\n" \
  "ds_read_b128 v[40:43], v192 offset:640\n" \
  "s_waitcnt lgkmcnt(5)\n" \
  "v_mfma_f32_16x16x32_bf16 v[88:91], a[176:179], v[52:55], v[88:91]\n" \
  "ds_read_b128 v[44:47], v192 offset:19072\n" \
  "v_mfma_f32_16x16x32_bf16 v[84:87], a[248:251], v[48:51], v[84:87]\n" \
  "s_add_u32 m0, s52, 5120\n" \
  "s_waitcnt lgkmcnt(5)\n" \
  "v_mfma_f32_16x16x32_bf16 v[92:95], a[248:251], v[52:55], v[92:95]\n" \
  "buffer_load_dwordx4 v201, s[44:47], s51 offen lds\n" \
  "v_mfma_f32_16x16x32_bf16 v[80:83], a[180:183], v[56:59], v[80:83]\n" \
  "ds_read_b128 v[48:51], v191 offset:768\n" \
  "s_waitcnt lgkmcnt(5)\n" \
  "v_mfma_f32_16x16x32_bf16 v[88:91], a[180:183], v[60:63], v[88:91]\n" \
  "ds_read_b128 v[52:55], v191 offset:19200\n" \
  "v_mfma_f32_16x16x32_bf16 v[84:87], a[252:255], v[56:59], v[84:87]\n" \
  "v_mfma_f32_16x16x32_bf16 v[92:95], a[252:255], v[60:63], v[92:95]\n" \
  "s_waitcnt lgkmcnt(5)\n" \
  "v_mfma_f32_16x16x32_bf16 v[80:83], a[184:187], v[32:35], v[80:83]\n" \
  "ds_read_b128 v[56:59], v192 offset:768\n" \
  "s_waitcnt lgkmcnt(5)\n" \
  "v_mfma_f32_16x16x32_bf16 v[88:91], a[184:187], v[36:39], v[88:91]\n" \
  "ds_read_b128 v[60:63], v192 offset:19200\n" \
  "v_mfma_f32_16x16x32_bf16 v[84:87], v[0:3], v[32:35], v[84:87]\n" \
  "s_add_u32 m0, s52, 6144\n" \
  "s_waitcnt lgkmcnt(5)\n" \
  "v_mfma_f32_16x16x32_bf16 v[92:95], v[0:3], v[36:39], v[92:95]\n" \
  "buffer_load_dwordx4 v202, s[44:47], s51 offen lds\n" \
  "v_mfma_f32_16x16x32_bf16 v[80:83], a[188:191], v[40:43], v[80:83]\n" \
  "ds_read_b128 v[32:35], v191 offset:896\n" \
  "s_waitcnt lgkmcnt(5)\n" \
  "v_mfma_f32_16x16x32_bf16 v[88:91], a[188:191], v[44:47], v[88:91]\n" \
  "ds_read_b128 v[36:39], v191 offset:19328\n" \
  "v_mfma_f32_16x16x32_bf16 v[84:87], v[4:7], v[40:43], v[84:87]\n" \
  "v_mfma_f32_16x16x32_bf16 v[92:95], v[4:7], v[44:47], v[92:95]\n" \
  "s_waitcnt lgkmcnt(5)\n" \
  "v_mfma_f32_16x16x32_bf16 v[80:83], a[192:195], v[48:51], v[80:83]\n" \
  "ds_read_b128 v[40:43], v192 offset:896\n" \
  "s_waitcnt lgkmcnt(5)\n" \
  "v_mfma_f32_16x16x32_bf16 v[88:91], a[192:195], v[52:55], v[88:91]\n" \
  "ds_read_b128 v[44:47], v192 offset:19328\n" \
  "v_mfma_f32_16x16x32_bf16 v[84:87], v[8:11], v[48:51], v[84:87]\n" \
  "s_add_u32 m0, s52, 7168\n" \
  "s_waitcnt lgkmcnt(5)\n" \
  "v_mfma_f32_16x16x32_bf16 v[92:95], v[8:11], v[52:55], v[92:95]\n" \
  "buffer_load_dwordx4 v203, s[44:47], s51 offen lds\n" \
  "v_mfma_f32_16x16x32_bf16 v[80:83], a[196:199], v[56:59], v[80:83]\n" \
  "ds_read_b128 v[48:51], v191 offset:1024\n" \
  "s_waitcnt lgkmcnt(5)\n" \
  "v_mfma_f32_16x16x32_bf16 v[88:91], a[196:199], v[60:63], v[88:91]\n" \
  "ds_read_b128 v[52:55], v191 offset:19456\n" \
  "v_mfma_f32_16x16x32_bf16 v[84:87], v[12:15], v[56:59], v[84:87]\n" \
  "v_mfma_f32_16x16x32_bf16 v[92:95], v[12:15], v[60:63], v[92:95]\n" \
  "s_waitcnt lgkmcnt(5)\n" \
  "v_mfma_f32_16x16x32_bf16 v[80:83], a[200:203], v[32:35], v[80:83]\n" \
  "ds_read_b128 v[56:59], v192 offset:1024\n" \
  "s_waitcnt lgkmcnt(5)\n" \
  "v_mfma_f32_16x16x32_bf16 v[88:91], a[200:203], v[36:39], v[88:91]\n" \
  "ds_read_b128 v[60:63], v192 offset:19456\n" \
  "v_mfma_f32_16x16x32_bf16 v[84:87], v[16:19], v[32:35], v[84:87]\n" \
  "s_add_u32 m0, s52, 8192\n" \
  "s_waitcnt lgkmcnt(5)\n" \
  "v_mfma_f32_16x16x32_bf16 v[92:95], v[16:19], v[36:39], v[92:95]\n" \
  "buffer_load_dwordx4 v204, s[44:47], s51 offen lds\n" \
  "v_mfma_f32_16x16x32_bf16 v[80:83], a[204:207], v[40:43], v[80:83]\n" \
  "ds_read_b128 v[32:35], v165\n" \
  "s_add_u32 s51, s51, 36864\n" \
  "s_waitcnt lgkmcnt(5)\n" \
  "v_mfma_f32_16x16x32_bf16 v[88:91], a[204:207], v[44:47], v[88:91]\n" \
  "ds_read_b128 v[36:39], v165 offset:18432\n" \
  "s_min_u32 s51, s51, 2322432\n" \
  "v_mfma_f32_16x16x32_bf16 v[84:87], v[20:23], v[40:43], v[84:87]\n" \
  "s_add_u32 s52, s52, 36864\n" \
  "v_mfma_f32_16x16x32_bf16 v[92:95], v[20:23], v[44:47], v[92:95]\n" \
  "s_cmp_ge_u32 s52, 163840\n" \
  "s_cselect_b32 s60, 147456, 0\n" \
  "s_sub_u32 s52, s52, s60\n" \
  "s_waitcnt lgkmcnt(5)\n" \
  "v_mfma_f32_16x16x32_bf16 v[80:83], a[208:211], v[48:51], v[80:83]\n" \
  "ds_read_b128 v[40:43], v164\n" \
  "s_waitcnt lgkmcnt(5)\n" \
  "v_mfma_f32_16x16x32_bf16 v[88:91], a[208:211], v[52:55], v[88:91]\n" \
  "ds_read_b128 v[44:47], v164 offset:18432\n" \
  "v_mfma_f32_16x16x32_bf16 v[84:87], v[24:27], v[48:51], v[84:87]\n" \
  "v_mov_b32 v191, v165\n" \
  "v_mov_b32 v192, v164\n" \
  "v_mfma_f32_16x16x32_bf16 v[92:95], v[24:27], v[52:55], v[92:95]\n" \
  "s_waitcnt lgkmcnt(5)\n" \
  "v_mfma_f32_16x16x32_bf16 v[80:83], a[212:215], v[56:59], v[80:83]\n" \
  "ds_read_b128 v[48:51], v165 offset:128\n" \
  "s_waitcnt lgkmcnt(5)\n" \
  "v_mfma_f32_16x16x32_bf16 v[88:91], a[212:215], v[60:63], v[88:91]\n" \
  "ds_read_b128 v[52:55], v165 offset:18560\n" \
  "v_mfma_f32_16x16x32_bf16 v[84:87], v[28:31], v[56:59], v[84:87]\n" \
  "s_add_u32 s48, s48, 1\n" \
  "s_cmp_lt_u32 s48, 58\n" \
  "v_mfma_f32_16x16x32_bf16 v[92:95], v[28:31], v[60:63], v[92:95]\n" \
  "s_cbranch_scc1 .LK_loop_%=\n" \
  "s_waitcnt lgkmcnt(0)\n" \
  "s_nop 7\n" \
  "s_lshr_b32 s62, s50, 8\n" \
  "s_and_b32 s62, s62, 8192\n" \
  "s_xor_b32 s62, s62, s50\n" \
  "v_cvt_pk_bf16_f32 v96, v80, v81\n" \
  "v_cvt_pk_bf16_f32 v97, v82, v83\n" \
  "v_cvt_pk_bf16_f32 v98, v84, v85\n" \
  "v_cvt_pk_bf16_f32 v99, v86, v87\n" \
  "buffer_store_dwordx4 v[96:99], v190, s[40:43], s62 offen sc1\n" \
  "v_cvt_pk_bf16_f32 v100, v88, v89\n" \
  "v_cvt_pk_bf16_f32 v101, v90, v91\n" \
  "v_cvt_pk_bf16_f32 v102, v92, v93\n" \
  "v_cvt_pk_bf16_f32 v103, v94, v95\n" \
  "buffer_store_dwordx4 v[100:103], v190, s[40:43], s62 offen offset:1024 sc1\n" \
  "s_waitcnt vmcnt(0)\n" \
  "s_barrier\n" \
  "s_mov_b32 s48, 64\n" \
  "s_mov_b32 s69, 0\n" \
  ".LK_ie_%=:\n" \
  "s_cmp_ge_u32 s96, 0xc0000000\n" \
  "s_cbranch_scc1 .LK_ie_x_%=\n" \
  "buffer_load_dwordx2 v[184:185], off, s[40:43], %41 sc1\n" \
  "s_waitcnt vmcnt(0)\n" \
  "s_add_u32 s69, s69, 1\n" \
  "s_cmp_lt_u32 s69, 2097152\n" \
  "v_readfirstlane_b32 s96, v184\n" \
  "s_cbranch_scc1 .LK_ie_%=\n" \
  ".LK_ie_x_%=:\n" \
  "v_mov_b32 v168, s48\n" \
  "buffer_store_dwordx2 v[168:169], v167, s[40:43], 0 offen sc1\n" \
  "s_branch .LK_end_%=\n" \
  ".LK_mea_%=:\n" \
  "s_waitcnt vmcnt(7)\n" \
  "s_barrier\n" \
  "v_mov_b32 v168, s56\n" \
  "s_mov_b64 exec, 1\n" \
  "buffer_store_dwordx2 v[168:169], v167, s[40:43], 0 offen sc1\n" \
  "s_mov_b64 exec, -1\n" \
  "s_branch .LK_mea_b_%=\n" \
  ".LK_meb_%=:\n" \
  "s_waitcnt vmcnt(7)\n" \
  "s_barrier\n" \
  "v_mov_b32 v168, s56\n" \
  "s_mov_b64 exec, 1\n" \
  "buffer_store_dwordx2 v[168:169], v167, s[40:43], 0 offen sc1\n" \
  "s_mov_b64 exec, -1\n" \
  "s_cmp_lg_u32 s48, 4\n" \
  "s_cbranch_scc1 .LK_in_x_%=\n" \
  "s_mov_b32 s69, 0\n" \
  ".LK_in_%=:\n" \
  "s_cmp_ge_u32 s96, 0xc0000000\n" \
  "s_cbranch_scc1 .LK_in_x_%=\n" \
  "buffer_load_dwordx2 v[184:185], off, s[40:43], %41 sc1\n" \
  "s_waitcnt vmcnt(0)\n" \
  "s_add_u32 s69, s69, 1\n" \
  "s_cmp_lt_u32 s69, 2097152\n" \
  "v_readfirstlane_b32 s96, v184\n" \
  "s_cbranch_scc1 .LK_in_%=\n" \
  ".LK_in_x_%=:\n" \
  "s_branch .LK_meb_b_%=\n" \
  ".LK_end_%=:\n" \
  "s_mov_b32 %0, s55\n"
#define K_FIRST 20
#define U_HELP 13
#define U_THIRD 0
#define U_WATCH 10
#define HAND_B 8
#define U_LEAD 14
#define VO_SWAP 1
static_assert(SDIR == 58, "generator");
#define U_ASM_TEXT \
  "s_mov_b64 s[40:41], %30\n" \
  "s_and_b32 s41, s41, 0xffff\n" \
  "s_mov_b32 s42, 255852544\n" \
  "s_mov_b32 s43, 0x00020000\n" \
  "s_mov_b32 s47, 0x00020000\n" \
  "s_lshl_b32 s72, %34, 12\n" \
  "buffer_load_dword v184, off, s[40:43], %42 sc1\n" \
  "s_and_b32 s73, %45, 255\n" \
  "s_bfe_u32 s74, %45, 0x80008\n" \
  "s_lshr_b32 s68, %45, 16\n" \
  "s_mov_b32 s75, s73\n" \
  "s_mov_b32 s70, 2\n" \
  "s_mov_b64 s[44:45], %31\n" \
  "s_and_b32 s45, s45, 0xffff\n" \
  "s_mov_b32 s46, 4718592\n" \
  "s_add_u32 s60, %35, 0\n" \
  "s_add_u32 m0, s72, 16384\n" \
  "s_nop 0\n" \
  "buffer_load_dwordx4 v170, s[44:47], s60 offen lds\n" \
  "buffer_load_dwordx4 v171, s[44:47], s60 offen offset:1024 lds\n" \
  "buffer_load_dwordx4 v172, s[44:47], s60 offen offset:2048 lds\n" \
  "buffer_load_dwordx4 v173, s[44:47], s60 offen offset:3072 lds\n" \
  "s_add_u32 s60, %35, 524288\n" \
  "s_add_u32 m0, s72, 32768\n" \
  "s_nop 0\n" \
  "buffer_load_dwordx4 v170, s[44:47], s60 offen lds\n" \
  "buffer_load_dwordx4 v171, s[44:47], s60 offen offset:1024 lds\n" \
  "buffer_load_dwordx4 v172, s[44:47], s60 offen offset:2048 lds\n" \
  "buffer_load_dwordx4 v173, s[44:47], s60 offen offset:3072 lds\n" \
  "s_add_u32 s60, %35, 1048576\n" \
  "s_add_u32 m0, s72, 49152\n" \
  "s_nop 0\n" \
  "buffer_load_dwordx4 v170, s[44:47], s60 offen lds\n" \
  "buffer_load_dwordx4 v171, s[44:47], s60 offen offset:1024 lds\n" \
  "buffer_load_dwordx4 v172, s[44:47], s60 offen offset:2048 lds\n" \
  "buffer_load_dwordx4 v173, s[44:47], s60 offen offset:3072 lds\n" \
  "s_add_u32 s60, %35, 1572864\n" \
  "s_add_u32 m0, s72, 65536\n" \
  "s_nop 0\n" \
  "buffer_load_dwordx4 v170, s[44:47], s60 offen lds\n" \
  "buffer_load_dwordx4 v171, s[44:47], s60 offen offset:1024 lds\n" \
  "buffer_load_dwordx4 v172, s[44:47], s60 offen offset:2048 lds\n" \
  "buffer_load_dwordx4 v173, s[44:47], s60 offen offset:3072 lds\n" \
  "s_add_u32 s60, %35, 2097152\n" \
  "s_add_u32 m0, s72, 81920\n" \
  "s_nop 0\n" \
  "buffer_load_dwordx4 v170, s[44:47], s60 offen lds\n" \
  "buffer_load_dwordx4 v171, s[44:47], s60 offen offset:1024 lds\n" \
  "buffer_load_dwordx4 v172, s[44:47], s60 offen offset:2048 lds\n" \
  "buffer_load_dwordx4 v173, s[44:47], s60 offen offset:3072 lds\n" \
  "s_add_u32 s60, %35, 2621440\n" \
  "s_add_u32 m0, s72, 98304\n" \
  "s_nop 0\n" \
  "buffer_load_dwordx4 v170, s[44:47], s60 offen lds\n" \
  "buffer_load_dwordx4 v171, s[44:47], s60 offen offset:1024 lds\n" \
  "buffer_load_dwordx4 v172, s[44:47], s60 offen offset:2048 lds\n" \
  "buffer_load_dwordx4 v173, s[44:47], s60 offen offset:3072 lds\n" \
  "s_add_u32 s60, %35, 3145728\n" \
  "s_add_u32 m0, s72, 114688\n" \
  "s_nop 0\n" \
  "buffer_load_dwordx4 v170, s[44:47], s60 offen lds\n" \
  "buffer_load_dwordx4 v171, s[44:47], s60 offen offset:1024 lds\n" \
  "buffer_load_dwordx4 v172, s[44:47], s60 offen offset:2048 lds\n" \
  "buffer_load_dwordx4 v173, s[44:47], s60 offen offset:3072 lds\n" \
  "s_add_u32 s60, %35, 3670016\n" \
  "s_add_u32 m0, s72, 131072\n" \
  "s_nop 0\n" \
  "buffer_load_dwordx4 v170, s[44:47], s60 offen lds\n" \
  "buffer_load_dwordx4 v171, s[44:47], s60 offen offset:1024 lds\n" \
  "buffer_load_dwordx4 v172, s[44:47], s60 offen offset:2048 lds\n" \
  "buffer_load_dwordx4 v173, s[44:47], s60 offen offset:3072 lds\n" \
  "s_add_u32 s60, %35, 4194304\n" \
  "s_add_u32 m0, s72, 147456\n" \
  "s_nop 0\n" \
  "buffer_load_dwordx4 v170, s[44:47], s60 offen lds\n" \
  "buffer_load_dwordx4 v171, s[44:47], s60 offen offset:1024 lds\n" \
  "buffer_load_dwordx4 v172, s[44:47], s60 offen offset:2048 lds\n" \
  "buffer_load_dwordx4 v173, s[44:47], s60 offen offset:3072 lds\n" \
  "s_waitcnt vmcnt(24)\n" \
  "s_barrier\n" \
  "v_add_u32 v186, 16384, v182\n" \
  "ds_read_b64_tr_b16 a[0:1], v186\n" \
  "ds_read_b64_tr_b16 a[2:3], v186 offset:1024\n" \
  "ds_read_b64_tr_b16 a[4:5], v186 offset:8192\n" \
  "ds_read_b64_tr_b16 a[6:7], v186 offset:9216\n" \
  "v_add_u32 v187, 16384, v183\n" \
  "ds_read_b64_tr_b16 a[72:73], v187\n" \
  "ds_read_b64_tr_b16 a[74:75], v187 offset:1024\n" \
  "ds_read_b64_tr_b16 a[76:77], v187 offset:8192\n" \
  "ds_read_b64_tr_b16 a[78:79], v187 offset:9216\n" \
  "v_add_u32 v186, 32768, v182\n" \
  "ds_read_b64_tr_b16 a[8:9], v186\n" \
  "ds_read_b64_tr_b16 a[10:11], v186 offset:1024\n" \
  "ds_read_b64_tr_b16 a[12:13], v186 offset:8192\n" \
  "ds_read_b64_tr_b16 a[14:15], v186 offset:9216\n" \
  "v_add_u32 v187, 32768, v183\n" \
  "ds_read_b64_tr_b16 a[80:81], v187\n" \
  "ds_read_b64_tr_b16 a[82:83], v187 offset:1024\n" \
  "ds_read_b64_tr_b16 a[84:85], v187 offset:8192\n" \
  "ds_read_b64_tr_b16 a[86:87], v187 offset:9216\n" \
  "v_add_u32 v186, 49152, v182\n" \
  "ds_read_b64_tr_b16 a[16:17], v186\n" \
  "ds_read_b64_tr_b16 a[18:19], v186 offset:1024\n" \
  "ds_read_b64_tr_b16 a[20:21], v186 offset:8192\n" \
  "ds_read_b64_tr_b16 a[22:23], v186 offset:9216\n" \
  "v_add_u32 v187, 49152, v183\n" \
  "ds_read_b64_tr_b16 a[88:89], v187\n" \
  "ds_read_b64_tr_b16 a[90:91], v187 offset:1024\n" \
  "ds_read_b64_tr_b16 a[92:93], v187 offset:8192\n" \
  "ds_read_b64_tr_b16 a[94:95], v187 offset:9216\n" \
  "s_waitcnt lgkmcnt(0)\n" \
  "s_barrier\n" \
  "s_mov_b64 s[44:45], %32\n" \
  "s_and_b32 s45, s45, 0xffff\n" \
  "s_mov_b32 s46, 4718592\n" \
  "s_add_u32 s60, %36, 0\n" \
  "s_add_u32 m0, s72, 16384\n" \
  "s_nop 0\n" \
  "buffer_load_dwordx4 v174, s[44:47], s60 offen lds\n" \
  "buffer_load_dwordx4 v175, s[44:47], s60 offen offset:1024 lds\n" \
  "buffer_load_dwordx4 v176, s[44:47], s60 offen offset:2048 lds\n" \
  "buffer_load_dwordx4 v177, s[44:47], s60 offen offset:3072 lds\n" \
  "s_add_u32 s60, %36, 73728\n" \
  "s_add_u32 m0, s72, 32768\n" \
  "s_nop 0\n" \
  "buffer_load_dwordx4 v174, s[44:47], s60 offen lds\n" \
  "buffer_load_dwordx4 v175, s[44:47], s60 offen offset:1024 lds\n" \
  "buffer_load_dwordx4 v176, s[44:47], s60 offen offset:2048 lds\n" \
  "buffer_load_dwordx4 v177, s[44:47], s60 offen offset:3072 lds\n" \
  "s_add_u32 s60, %36, 256\n" \
  "s_add_u32 m0, s72, 49152\n" \
  "s_nop 0\n" \
  "buffer_load_dwordx4 v174, s[44:47], s60 offen lds\n" \
  "buffer_load_dwordx4 v175, s[44:47], s60 offen offset:1024 lds\n" \
  "buffer_load_dwordx4 v176, s[44:47], s60 offen offset:2048 lds\n" \
  "buffer_load_dwordx4 v177, s[44:47], s60 offen offset:3072 lds\n" \
  "v_readfirstlane_b32 s55, v184\n" \
  "s_add_u32 s55, s55, 1\n" \
  "s_waitcnt vmcnt(24)\n" \
  "s_barrier\n" \
  "v_add_u32 v186, 65536, v182\n" \
  "ds_read_b64_tr_b16 a[24:25], v186\n" \
  "ds_read_b64_tr_b16 a[26:27], v186 offset:1024\n" \
  "ds_read_b64_tr_b16 a[28:29], v186 offset:8192\n" \
  "ds_read_b64_tr_b16 a[30:31], v186 offset:9216\n" \
  "v_add_u32 v187, 65536, v183\n" \
  "ds_read_b64_tr_b16 a[96:97], v187\n" \
  "ds_read_b64_tr_b16 a[98:99], v187 offset:1024\n" \
  "ds_read_b64_tr_b16 a[100:101], v187 offset:8192\n" \
  "ds_read_b64_tr_b16 a[102:103], v187 offset:9216\n" \
  "v_add_u32 v186, 81920, v182\n" \
  "ds_read_b64_tr_b16 a[32:33], v186\n" \
  "ds_read_b64_tr_b16 a[34:35], v186 offset:1024\n" \
  "ds_read_b64_tr_b16 a[36:37], v186 offset:8192\n" \
  "ds_read_b64_tr_b16 a[38:39], v186 offset:9216\n" \
  "v_add_u32 v187, 81920, v183\n" \
  "ds_read_b64_tr_b16 a[104:105], v187\n" \
  "ds_read_b64_tr_b16 a[106:107], v187 offset:1024\n" \
  "ds_read_b64_tr_b16 a[108:109], v187 offset:8192\n" \
  "ds_read_b64_tr_b16 a[110:111], v187 offset:9216\n" \
  "v_add_u32 v186, 98304, v182\n" \
  "ds_read_b64_tr_b16 a[40:41], v186\n" \
  "ds_read_b64_tr_b16 a[42:43], v186 offset:1024\n" \
  "ds_read_b64_tr_b16 a[44:45], v186 offset:8192\n" \
  "ds_read_b64_tr_b16 a[46:47], v186 offset:9216\n" \
  "v_add_u32 v187, 98304, v183\n" \
  "ds_read_b64_tr_b16 a[112:113], v187\n" \
  "ds_read_b64_tr_b16 a[114:115], v187 offset:1024\n" \
  "ds_read_b64_tr_b16 a[116:117], v187 offset:8192\n" \
  "ds_read_b64_tr_b16 a[118:119], v187 offset:9216\n" \
  "s_waitcnt lgkmcnt(0)\n" \
  "s_barrier\n" \
  "s_add_u32 s60, %36, 73984\n" \
  "s_add_u32 m0, s72, 65536\n" \
  "s_nop 0\n" \
  "buffer_load_dwordx4 v174, s[44:47], s60 offen lds\n" \
  "buffer_load_dwordx4 v175, s[44:47], s60 offen offset:1024 lds\n" \
  "buffer_load_dwordx4 v176, s[44:47], s60 offen offset:2048 lds\n" \
  "buffer_load_dwordx4 v177, s[44:47], s60 offen offset:3072 lds\n" \
  "s_add_u32 s60, %36, 512\n" \
  "s_add_u32 m0, s72, 81920\n" \
  "s_nop 0\n" \
  "buffer_load_dwordx4 v174, s[44:47], s60 offen lds\n" \
  "buffer_load_dwordx4 v175, s[44:47], s60 offen offset:1024 lds\n" \
  "buffer_load_dwordx4 v176, s[44:47], s60 offen offset:2048 lds\n" \
  "buffer_load_dwordx4 v177, s[44:47], s60 offen offset:3072 lds\n" \
  "s_add_u32 s60, %36, 74240\n" \
  "s_add_u32 m0, s72, 98304\n" \
  "s_nop 0\n" \
  "buffer_load_dwordx4 v174, s[44:47], s60 offen lds\n" \
  "buffer_load_dwordx4 v175, s[44:47], s60 offen offset:1024 lds\n" \
  "buffer_load_dwordx4 v176, s[44:47], s60 offen offset:2048 lds\n" \
  "buffer_load_dwordx4 v177, s[44:47], s60 offen offset:3072 lds\n" \
  "s_waitcnt vmcnt(24)\n" \
  "s_barrier\n" \
  "v_add_u32 v186, 114688, v182\n" \
  "ds_read_b64_tr_b16 a[48:49], v186\n" \
  "ds_read_b64_tr_b16 a[50:51], v186 offset:1024\n" \
  "ds_read_b64_tr_b16 a[52:53], v186 offset:8192\n" \
  "ds_read_b64_tr_b16 a[54:55], v186 offset:9216\n" \
  "v_add_u32 v187, 114688, v183\n" \
  "ds_read_b64_tr_b16 a[120:121], v187\n" \
  "ds_read_b64_tr_b16 a[122:123], v187 offset:1024\n" \
  "ds_read_b64_tr_b16 a[124:125], v187 offset:8192\n" \
  "ds_read_b64_tr_b16 a[126:127], v187 offset:9216\n" \
  "v_add_u32 v186, 131072, v182\n" \
  "ds_read_b64_tr_b16 a[56:57], v186\n" \
  "ds_read_b64_tr_b16 a[58:59], v186 offset:1024\n" \
  "ds_read_b64_tr_b16 a[60:61], v186 offset:8192\n" \
  "ds_read_b64_tr_b16 a[62:63], v186 offset:9216\n" \
  "v_add_u32 v187, 131072, v183\n" \
  "ds_read_b64_tr_b16 a[128:129], v187\n" \
  "ds_read_b64_tr_b16 a[130:131], v187 offset:1024\n" \
  "ds_read_b64_tr_b16 a[132:133], v187 offset:8192\n" \
  "ds_read_b64_tr_b16 a[134:135], v187 offset:9216\n" \
  "v_add_u32 v186, 147456, v182\n" \
  "ds_read_b64_tr_b16 a[64:65], v186\n" \
  "ds_read_b64_tr_b16 a[66:67], v186 offset:1024\n" \
  "ds_read_b64_tr_b16 a[68:69], v186 offset:8192\n" \
  "ds_read_b64_tr_b16 a[70:71], v186 offset:9216\n" \
  "v_add_u32 v187, 147456, v183\n" \
  "ds_read_b64_tr_b16 a[136:137], v187\n" \
  "ds_read_b64_tr_b16 a[138:139], v187 offset:1024\n" \
  "ds_read_b64_tr_b16 a[140:141], v187 offset:8192\n" \
  "ds_read_b64_tr_b16 a[142:143], v187 offset:9216\n" \
  "s_waitcnt lgkmcnt(0)\n" \
  "s_barrier\n" \
  "s_add_u32 s60, %36, 768\n" \
  "s_add_u32 m0, s72, 114688\n" \
  "s_nop 0\n" \
  "buffer_load_dwordx4 v174, s[44:47], s60 offen lds\n" \
  "buffer_load_dwordx4 v175, s[44:47], s60 offen offset:1024 lds\n" \
  "buffer_load_dwordx4 v176, s[44:47], s60 offen offset:2048 lds\n" \
  "buffer_load_dwordx4 v177, s[44:47], s60 offen offset:3072 lds\n" \
  "s_add_u32 s60, %36, 74496\n" \
  "s_add_u32 m0, s72, 131072\n" \
  "s_nop 0\n" \
  "buffer_load_dwordx4 v174, s[44:47], s60 offen lds\n" \
  "buffer_load_dwordx4 v175, s[44:47], s60 offen offset:1024 lds\n" \
  "buffer_load_dwordx4 v176, s[44:47], s60 offen offset:2048 lds\n" \
  "buffer_load_dwordx4 v177, s[44:47], s60 offen offset:3072 lds\n" \
  "s_add_u32 s60, %36, 0\n" \
  "s_add_u32 m0, s72, 147456\n" \
  "s_nop 0\n" \
  "buffer_load_dwordx4 v178, s[44:47], s60 offen lds\n" \
  "buffer_load_dwordx4 v179, s[44:47], s60 offen offset:1024 lds\n" \
  "buffer_load_dwordx4 v180, s[44:47], s60 offen offset:2048 lds\n" \
  "buffer_load_dwordx4 v181, s[44:47], s60 offen offset:3072 lds\n" \
  "s_waitcnt vmcnt(24)\n" \
  "s_barrier\n" \
  "v_add_u32 v186, 16384, v182\n" \
  "ds_read_b64_tr_b16 a[144:145], v186\n" \
  "ds_read_b64_tr_b16 a[146:147], v186 offset:1024\n" \
  "ds_read_b64_tr_b16 a[148:149], v186 offset:8192\n" \
  "ds_read_b64_tr_b16 a[150:151], v186 offset:9216\n" \
  "v_add_u32 v187, 16384, v183\n" \
  "ds_read_b64_tr_b16 a[160:161], v187\n" \
  "ds_read_b64_tr_b16 a[162:163], v187 offset:1024\n" \
  "ds_read_b64_tr_b16 a[164:165], v187 offset:8192\n" \
  "ds_read_b64_tr_b16 a[166:167], v187 offset:9216\n" \
  "v_add_u32 v186, 32768, v182\n" \
  "ds_read_b64_tr_b16 a[152:153], v186\n" \
  "ds_read_b64_tr_b16 a[154:155], v186 offset:1024\n" \
  "ds_read_b64_tr_b16 a[156:157], v186 offset:8192\n" \
  "ds_read_b64_tr_b16 a[158:159], v186 offset:9216\n" \
  "v_add_u32 v187, 32768, v183\n" \
  "ds_read_b64_tr_b16 a[168:169], v187\n" \
  "ds_read_b64_tr_b16 a[170:171], v187 offset:1024\n" \
  "ds_read_b64_tr_b16 a[172:173], v187 offset:8192\n" \
  "ds_read_b64_tr_b16 a[174:175], v187 offset:9216\n" \
  "v_add_u32 v186, 49152, v182\n" \
  "ds_read_b64_tr_b16 a[176:177], v186\n" \
  "ds_read_b64_tr_b16 a[178:179], v186 offset:1024\n" \
  "ds_read_b64_tr_b16 a[180:181], v186 offset:8192\n" \
  "ds_read_b64_tr_b16 a[182:183], v186 offset:9216\n" \
  "v_add_u32 v187, 49152, v183\n" \
  "ds_read_b64_tr_b16 a[192:193], v187\n" \
  "ds_read_b64_tr_b16 a[194:195], v187 offset:1024\n" \
  "ds_read_b64_tr_b16 a[196:197], v187 offset:8192\n" \
  "ds_read_b64_tr_b16 a[198:199], v187 offset:9216\n" \
  "s_waitcnt lgkmcnt(0)\n" \
  "s_barrier\n" \
  "s_mov_b64 s[44:45], %33\n" \
  "s_and_b32 s45, s45, 0xffff\n" \
  "s_mov_b32 s46, 2359296\n" \
  "s_mul_i32 s51, s73, 36864\n" \
  "s_mul_i32 s52, %34, 9216\n" \
  "s_add_u32 s52, s52, 16384\n" \
  "s_add_u32 m0, s52, 0\n" \
  "s_nop 0\n" \
  "buffer_load_dwordx4 v196, s[44:47], s51 offen lds\n" \
  "s_add_u32 m0, s52, 1024\n" \
  "s_nop 0\n" \
  "buffer_load_dwordx4 v197, s[44:47], s51 offen lds\n" \
  "s_add_u32 m0, s52, 2048\n" \
  "s_nop 0\n" \
  "buffer_load_dwordx4 v198, s[44:47], s51 offen lds\n" \
  "s_add_u32 m0, s52, 3072\n" \
  "s_nop 0\n" \
  "buffer_load_dwordx4 v199, s[44:47], s51 offen lds\n" \
  "s_add_u32 m0, s52, 4096\n" \
  "s_nop 0\n" \
  "buffer_load_dwordx4 v200, s[44:47], s51 offen lds\n" \
  "s_add_u32 m0, s52, 5120\n" \
  "s_nop 0\n" \
  "buffer_load_dwordx4 v201, s[44:47], s51 offen lds\n" \
  "s_add_u32 m0, s52, 6144\n" \
  "s_nop 0\n" \
  "buffer_load_dwordx4 v202, s[44:47], s51 offen lds\n" \
  "s_add_u32 m0, s52, 7168\n" \
  "s_nop 0\n" \
  "buffer_load_dwordx4 v203, s[44:47], s51 offen lds\n" \
  "s_add_u32 m0, s52, 8192\n" \
  "s_nop 0\n" \
  "buffer_load_dwordx4 v204, s[44:47], s51 offen lds\n" \
  "s_cmp_lt_u32 s51, 958464\n" \
  "s_cselect_b32 s60, 36864, 0\n" \
  "s_add_u32 s51, s51, s60\n" \
  "s_add_u32 s51, s51, 36864\n" \
  "s_min_u32 s51, s51, 2322432\n" \
  "s_add_u32 s52, s52, 36864\n" \
  "s_cmp_ge_u32 s52, 163840\n" \
  "s_cselect_b32 s60, 147456, 0\n" \
  "s_sub_u32 s52, s52, s60\n" \
  "s_cmp_eq_u32 s73, 0\n" \
  "s_cselect_b32 s51, 73728, s51\n" \
  "s_waitcnt vmcnt(21)\n" \
  "s_barrier\n" \
  "v_add_u32 v186, 65536, v182\n" \
  "ds_read_b64_tr_b16 a[184:185], v186\n" \
  "ds_read_b64_tr_b16 a[186:187], v186 offset:1024\n" \
  "ds_read_b64_tr_b16 a[188:189], v186 offset:8192\n" \
  "ds_read_b64_tr_b16 a[190:191], v186 offset:9216\n" \
  "v_add_u32 v187, 65536, v183\n" \
  "ds_read_b64_tr_b16 a[200:201], v187\n" \
  "ds_read_b64_tr_b16 a[202:203], v187 offset:1024\n" \
  "ds_read_b64_tr_b16 a[204:205], v187 offset:8192\n" \
  "ds_read_b64_tr_b16 a[206:207], v187 offset:9216\n" \
  "v_add_u32 v186, 81920, v182\n" \
  "ds_read_b64_tr_b16 a[208:209], v186\n" \
  "ds_read_b64_tr_b16 a[210:211], v186 offset:1024\n" \
  "ds_read_b64_tr_b16 a[212:213], v186 offset:8192\n" \
  "ds_read_b64_tr_b16 a[214:215], v186 offset:9216\n" \
  "v_add_u32 v187, 81920, v183\n" \
  "ds_read_b64_tr_b16 a[224:225], v187\n" \
  "ds_read_b64_tr_b16 a[226:227], v187 offset:1024\n" \
  "ds_read_b64_tr_b16 a[228:229], v187 offset:8192\n" \
  "ds_read_b64_tr_b16 a[230:231], v187 offset:9216\n" \
  "v_add_u32 v186, 98304, v182\n" \
  "ds_read_b64_tr_b16 a[216:217], v186\n" \
  "ds_read_b64_tr_b16 a[218:219], v186 offset:1024\n" \
  "ds_read_b64_tr_b16 a[220:221], v186 offset:8192\n" \
  "ds_read_b64_tr_b16 a[222:223], v186 offset:9216\n" \
  "v_add_u32 v187, 98304, v183\n" \
  "ds_read_b64_tr_b16 a[232:233], v187\n" \
  "ds_read_b64_tr_b16 a[234:235], v187 offset:1024\n" \
  "ds_read_b64_tr_b16 a[236:237], v187 offset:8192\n" \
  "ds_read_b64_tr_b16 a[238:239], v187 offset:9216\n" \
  "s_waitcnt lgkmcnt(0)\n" \
  "s_barrier\n" \
  "buffer_load_dwordx2 v[184:185], off, s[40:43], %41 sc1\n" \
  "s_add_u32 m0, s52, 0\n" \
  "s_nop 0\n" \
  "buffer_load_dwordx4 v196, s[44:47], s51 offen lds\n" \
  "s_add_u32 m0, s52, 1024\n" \
  "s_nop 0\n" \
  "buffer_load_dwordx4 v197, s[44:47], s51 offen lds\n" \
  "s_add_u32 m0, s52, 2048\n" \
  "s_nop 0\n" \
  "buffer_load_dwordx4 v198, s[44:47], s51 offen lds\n" \
  "s_add_u32 m0, s52, 3072\n" \
  "s_nop 0\n" \
  "buffer_load_dwordx4 v199, s[44:47], s51 offen lds\n" \
  "s_add_u32 m0, s52, 4096\n" \
  "s_nop 0\n" \
  "buffer_load_dwordx4 v200, s[44:47], s51 offen lds\n" \
  "s_add_u32 m0, s52, 5120\n" \
  "s_nop 0\n" \
  "buffer_load_dwordx4 v201, s[44:47], s51 offen lds\n" \
  "s_add_u32 m0, s52, 6144\n" \
  "s_nop 0\n" \
  "buffer_load_dwordx4 v202, s[44:47], s51 offen lds\n" \
  "s_add_u32 m0, s52, 7168\n" \
  "s_nop 0\n" \
  "buffer_load_dwordx4 v203, s[44:47], s51 offen lds\n" \
  "s_add_u32 m0, s52, 8192\n" \
  "s_nop 0\n" \
  "buffer_load_dwordx4 v204, s[44:47], s51 offen lds\n" \
  "s_cmp_lt_u32 s51, 958464\n" \
  "s_cselect_b32 s60, 36864, 0\n" \
  "s_add_u32 s51, s51, s60\n" \
  "s_add_u32 s51, s51, 36864\n" \
  "s_min_u32 s51, s51, 2322432\n" \
  "s_add_u32 s52, s52, 36864\n" \
  "s_cmp_ge_u32 s52, 163840\n" \
  "s_cselect_b32 s60, 147456, 0\n" \
  "s_sub_u32 s52, s52, s60\n" \
  "s_waitcnt vmcnt(19)\n" \
  "s_barrier\n" \
  "v_add_u32 v186, 114688, v182\n" \
  "ds_read_b64_tr_b16 a[240:241], v186\n" \
  "ds_read_b64_tr_b16 a[242:243], v186 offset:1024\n" \
  "ds_read_b64_tr_b16 a[244:245], v186 offset:8192\n" \
  "ds_read_b64_tr_b16 a[246:247], v186 offset:9216\n" \
  "v_add_u32 v187, 114688, v183\n" \
  "ds_read_b64_tr_b16 v[0:1], v187\n" \
  "ds_read_b64_tr_b16 v[2:3], v187 offset:1024\n" \
  "ds_read_b64_tr_b16 v[4:5], v187 offset:8192\n" \
  "ds_read_b64_tr_b16 v[6:7], v187 offset:9216\n" \
  "v_add_u32 v186, 131072, v182\n" \
  "ds_read_b64_tr_b16 a[248:249], v186\n" \
  "ds_read_b64_tr_b16 a[250:251], v186 offset:1024\n" \
  "ds_read_b64_tr_b16 a[252:253], v186 offset:8192\n" \
  "ds_read_b64_tr_b16 a[254:255], v186 offset:9216\n" \
  "v_add_u32 v187, 131072, v183\n" \
  "ds_read_b64_tr_b16 v[8:9], v187\n" \
  "ds_read_b64_tr_b16 v[10:11], v187 offset:1024\n" \
  "ds_read_b64_tr_b16 v[12:13], v187 offset:8192\n" \
  "ds_read_b64_tr_b16 v[14:15], v187 offset:9216\n" \
  "v_add_u32 v186, 147456, v182\n" \
  "ds_read_b64_tr_b16 v[16:17], v186\n" \
  "ds_read_b64_tr_b16 v[18:19], v186 offset:1024\n" \
  "ds_read_b64_tr_b16 v[20:21], v186 offset:8192\n" \
  "ds_read_b64_tr_b16 v[22:23], v186 offset:9216\n" \
  "v_add_u32 v187, 147456, v183\n" \
  "ds_read_b64_tr_b16 v[24:25], v187\n" \
  "ds_read_b64_tr_b16 v[26:27], v187 offset:1024\n" \
  "ds_read_b64_tr_b16 v[28:29], v187 offset:8192\n" \
  "ds_read_b64_tr_b16 v[30:31], v187 offset:9216\n" \
  "s_waitcnt lgkmcnt(0)\n" \
  "s_mov_b32 s69, 0\n" \
  "s_waitcnt vmcnt(9)\n" \
  "s_branch .LU_go1_%=\n" \
  ".LU_go_%=:\n" \
  "buffer_load_dwordx2 v[184:185], off, s[40:43], %41 sc1\n" \
  "s_waitcnt vmcnt(0)\n" \
  ".LU_go1_%=:\n" \
  "v_readfirstlane_b32 s96, v184\n" \
  "v_readfirstlane_b32 s63, v185\n" \
  "s_cmp_eq_u32 s63, s55\n" \
  "s_cselect_b32 s96, s96, 0\n" \
  "s_cmp_ge_u32 s96, 0x80000000\n" \
  "s_cbranch_scc1 .LU_go_x_%=\n" \
  "s_sleep 4\n" \
  "s_add_u32 s69, s69, 1\n" \
  "s_cmp_lt_u32 s69, 2097152\n" \
  "s_cbranch_scc1 .LU_go_%=\n" \
  "v_mov_b32 v184, 1\n" \
  "v_mov_b32 v185, s55\n" \
  "buffer_store_dwordx2 v[184:185], off, s[40:43], %42 offset:128 sc1\n" \
  ".LU_go_x_%=:\n" \
  "s_and_b32 s60, s96, 2\n" \
  "s_lshl_b32 s60, s60, 12\n" \
  "s_xor_b32 s53, %39, s60\n" \
  "s_xor_b32 s54, %40, s60\n" \
  "s_andn2_b32 s60, 2, s96\n" \
  "s_lshl_b32 s60, s60, 12\n" \
  "s_add_u32 s49, %37, s60\n" \
  "s_add_u32 s50, %38, s60\n" \
  "s_xor_b32 s66, %38, s60\n" \
  "s_xor_b32 s59, %44, s60\n" \
  "s_or_b32 s50, s50, 0x40000000\n" \
  "s_mov_b32 s57, 2130706432\n" \
  "s_mov_b32 s48, 0\n" \
  "v_mov_b32 v169, s55\n" \
  "v_mov_b32 v167, 2130706432\n" \
  "v_mov_b32 v161, 2130706432\n" \
  "v_mov_b32 v166, 0\n" \
  "s_mov_b64 exec, 7\n" \
  "v_mov_b32 v167, %46\n" \
  "s_mov_b64 exec, 3\n" \
  "v_mov_b32 v167, s54\n" \
  "s_mov_b64 exec, 1\n" \
  "v_mov_b32 v167, s53\n" \
  "v_mov_b32 v161, s53\n" \
  "v_mov_b32 v166, 0\n" \
  "s_mov_b64 exec, -1\n" \
  "s_barrier\n" \
  "ds_read_b128 v[32:35], v191\n" \
  "ds_read_b128 v[36:39], v191 offset:18432\n" \
  "ds_read_b128 v[40:43], v192\n" \
  "ds_read_b128 v[44:47], v192 offset:18432\n" \
  "ds_read_b128 v[48:51], v191 offset:128\n" \
  "ds_read_b128 v[52:55], v191 offset:18560\n" \
  "s_waitcnt lgkmcnt(0)\n" \
  "v_mov_b64 v[164:165], 0\n" \
  ".LU_loop_%=:\n" \
  "v_mfma_f32_16x16x32_bf16 v[64:67], v[32:35], a[0:3], 0\n" \
  "ds_read_b128 v[56:59], v192 offset:128\n" \
  "v_mfma_f32_16x16x32_bf16 v[72:75], v[36:39], a[0:3], 0\n" \
  "ds_read_b128 v[60:63], v192 offset:18560\n" \
  "v_mfma_f32_16x16x32_bf16 v[68:71], v[32:35], a[72:75], 0\n" \
  "s_mov_b32 s63, s57\n" \
  "v_cvt_pk_bf16_f32 v104, v88, v89\n" \
  "v_cvt_pk_bf16_f32 v105, v90, v91\n" \
  "v_mfma_f32_16x16x32_bf16 v[76:79], v[36:39], a[72:75], 0\n" \
  "v_cvt_pk_bf16_f32 v106, v92, v93\n" \
  "v_cvt_pk_bf16_f32 v107, v94, v95\n" \
  "v_mfma_f32_16x16x32_bf16 v[64:67], v[40:43], a[4:7], v[64:67]\n" \
  "ds_read_b128 v[32:35], v191 offset:256\n" \
  "v_mfma_f32_16x16x32_bf16 v[72:75], v[44:47], a[4:7], v[72:75]\n" \
  "ds_read_b128 v[36:39], v191 offset:18688\n" \
  "v_mfma_f32_16x16x32_bf16 v[68:71], v[40:43], a[76:79], v[68:71]\n" \
  "buffer_store_dwordx4 v[104:107], v190, s[40:43], s63 offen sc1\n" \
  "v_mfma_f32_16x16x32_bf16 v[76:79], v[44:47], a[76:79], v[76:79]\n" \
  "v_mfma_f32_16x16x32_bf16 v[64:67], v[48:51], a[8:11], v[64:67]\n" \
  "ds_read_b128 v[40:43], v192 offset:256\n" \
  "v_mfma_f32_16x16x32_bf16 v[72:75], v[52:55], a[8:11], v[72:75]\n" \
  "ds_read_b128 v[44:47], v192 offset:18688\n" \
  "v_mfma_f32_16x16x32_bf16 v[68:71], v[48:51], a[80:83], v[68:71]\n" \
  "s_add_u32 m0, s52, 0\n" \
  "s_waitcnt lgkmcnt(5)\n" \
  "v_mfma_f32_16x16x32_bf16 v[76:79], v[52:55], a[80:83], v[76:79]\n" \
  "buffer_load_dwordx4 v196, s[44:47], s51 offen lds\n" \
  "v_mfma_f32_16x16x32_bf16 v[64:67], v[56:59], a[12:15], v[64:67]\n" \
  "ds_read_b128 v[48:51], v191 offset:384\n" \
  "s_mov_b32 s58, s75\n" \
  "s_waitcnt lgkmcnt(5)\n" \
  "v_mfma_f32_16x16x32_bf16 v[72:75], v[60:63], a[12:15], v[72:75]\n" \
  "ds_read_b128 v[52:55], v191 offset:18816\n" \
  "s_and_b32 s58, s58, 15\n" \
  "v_mfma_f32_16x16x32_bf16 v[68:71], v[56:59], a[84:87], v[68:71]\n" \
  "s_mov_b32 s64, 8\n" \
  "s_lshl_b32 s58, s58, 16\n" \
  "s_xor_b32 s50, s58, s66\n" \
  "v_mfma_f32_16x16x32_bf16 v[76:79], v[60:63], a[84:87], v[76:79]\n" \
  "s_xor_b32 s57, s58, s59\n" \
  "v_mul_i32_i24 v161, s70, v166\n" \
  "s_waitcnt lgkmcnt(5)\n" \
  "v_mfma_f32_16x16x32_bf16 v[64:67], v[32:35], a[16:19], v[64:67]\n" \
  "ds_read_b128 v[56:59], v192 offset:384\n" \
  "s_waitcnt lgkmcnt(5)\n" \
  "v_mfma_f32_16x16x32_bf16 v[72:75], v[36:39], a[16:19], v[72:75]\n" \
  "ds_read_b128 v[60:63], v192 offset:18816\n" \
  "v_mfma_f32_16x16x32_bf16 v[68:71], v[32:35], a[88:91], v[68:71]\n" \
  "s_add_u32 m0, s52, 1024\n" \
  "s_cmp_lt_u32 s73, 26\n" \
  "s_cselect_b32 s70, 2, 1\n" \
  "v_mfma_f32_16x16x32_bf16 v[76:79], v[36:39], a[88:91], v[76:79]\n" \
  "s_waitcnt lgkmcnt(5)\n" \
  "buffer_load_dwordx4 v197, s[44:47], s51 offen lds\n" \
  "v_mfma_f32_16x16x32_bf16 v[64:67], v[40:43], a[20:23], v[64:67]\n" \
  "ds_read_b128 v[32:35], v191 offset:512\n" \
  "s_lshl_b32 s71, s70, 19\n" \
  "s_waitcnt lgkmcnt(5)\n" \
  "v_mfma_f32_16x16x32_bf16 v[72:75], v[44:47], a[20:23], v[72:75]\n" \
  "ds_read_b128 v[36:39], v191 offset:18944\n" \
  "s_mov_b32 s75, s73\n" \
  "v_mfma_f32_16x16x32_bf16 v[68:71], v[40:43], a[92:95], v[68:71]\n" \
  "v_mfma_f32_16x16x32_bf16 v[76:79], v[44:47], a[92:95], v[76:79]\n" \
  "s_waitcnt lgkmcnt(5)\n" \
  "v_mfma_f32_16x16x32_bf16 v[64:67], v[48:51], a[24:27], v[64:67]\n" \
  "ds_read_b128 v[40:43], v192 offset:512\n" \
  "s_waitcnt lgkmcnt(5)\n" \
  "v_xor_b32 v160, 8, v194\n" \
  "v_mfma_f32_16x16x32_bf16 v[72:75], v[52:55], a[24:27], v[72:75]\n" \
  "ds_read_b128 v[44:47], v192 offset:18944\n" \
  "v_mfma_f32_16x16x32_bf16 v[68:71], v[48:51], a[96:99], v[68:71]\n" \
  "s_add_u32 m0, s52, 2048\n" \
  "v_add_u32 v168, s73, v161\n" \
  "v_mfma_f32_16x16x32_bf16 v[76:79], v[52:55], a[96:99], v[76:79]\n" \
  "s_waitcnt lgkmcnt(5)\n" \
  "buffer_load_dwordx4 v198, s[44:47], s51 offen lds\n" \
  "v_mfma_f32_16x16x32_bf16 v[64:67], v[56:59], a[28:31], v[64:67]\n" \
  "ds_read_b128 v[48:51], v191 offset:640\n" \
  "s_waitcnt lgkmcnt(5)\n" \
  "v_mfma_f32_16x16x32_bf16 v[72:75], v[60:63], a[28:31], v[72:75]\n" \
  "ds_read_b128 v[52:55], v191 offset:19072\n" \
  "v_mfma_f32_16x16x32_bf16 v[68:71], v[56:59], a[100:103], v[68:71]\n" \
  "s_waitcnt vmcnt(14)\n" \
  "s_barrier\n" \
  "v_mfma_f32_16x16x32_bf16 v[76:79], v[60:63], a[100:103], v[76:79]\n" \
  "ds_read_b64_tr_b16 v[104:105], v194\n" \
  "s_waitcnt lgkmcnt(6)\n" \
  "v_mfma_f32_16x16x32_bf16 v[64:67], v[32:35], a[32:35], v[64:67]\n" \
  "ds_read_b128 v[56:59], v192 offset:640\n" \
  "s_waitcnt lgkmcnt(6)\n" \
  "v_mfma_f32_16x16x32_bf16 v[72:75], v[36:39], a[32:35], v[72:75]\n" \
  "ds_read_b128 v[60:63], v192 offset:19072\n" \
  "v_mfma_f32_16x16x32_bf16 v[68:71], v[32:35], a[104:107], v[68:71]\n" \
  "s_add_u32 m0, s52, 3072\n" \
  "ds_read_b64_tr_b16 v[106:107], v194 offset:256\n" \
  "s_waitcnt lgkmcnt(7)\n" \
  "v_mfma_f32_16x16x32_bf16 v[76:79], v[36:39], a[104:107], v[76:79]\n" \
  "buffer_load_dwordx4 v199, s[44:47], s51 offen lds\n" \
  "v_mfma_f32_16x16x32_bf16 v[64:67], v[40:43], a[36:39], v[64:67]\n" \
  "ds_read_b128 v[32:35], v191 offset:768\n" \
  "s_waitcnt lgkmcnt(7)\n" \
  "v_mfma_f32_16x16x32_bf16 v[72:75], v[44:47], a[36:39], v[72:75]\n" \
  "ds_read_b128 v[36:39], v191 offset:19200\n" \
  "v_mfma_f32_16x16x32_bf16 v[68:71], v[40:43], a[108:111], v[68:71]\n" \
  "s_cmp_lt_u32 s48, s64\n" \
  "s_cbranch_scc1 .LU_e38_%=\n" \
  ".LU_e38_b_%=:\n" \
  "v_mfma_f32_16x16x32_bf16 v[76:79], v[44:47], a[108:111], v[76:79]\n" \
  "ds_read_b64_tr_b16 v[108:109], v160\n" \
  "s_waitcnt lgkmcnt(8)\n" \
  "v_mfma_f32_16x16x32_bf16 v[64:67], v[48:51], a[40:43], v[64:67]\n" \
  "ds_read_b128 v[40:43], v192 offset:768\n" \
  "s_waitcnt lgkmcnt(8)\n" \
  "v_mfma_f32_16x16x32_bf16 v[72:75], v[52:55], a[40:43], v[72:75]\n" \
  "ds_read_b128 v[44:47], v192 offset:19200\n" \
  "v_mfma_f32_16x16x32_bf16 v[68:71], v[48:51], a[112:115], v[68:71]\n" \
  "s_add_u32 m0, s52, 4096\n" \
  "ds_read_b64_tr_b16 v[110:111], v160 offset:256\n" \
  "s_waitcnt lgkmcnt(8)\n" \
  "v_mfma_f32_16x16x32_bf16 v[76:79], v[52:55], a[112:115], v[76:79]\n" \
  "buffer_load_dwordx4 v200, s[44:47], s51 offen lds\n" \
  "v_mfma_f32_16x16x32_bf16 v[64:67], v[56:59], a[44:47], v[64:67]\n" \
  "ds_read_b128 v[48:51], v191 offset:896\n" \
  "s_waitcnt lgkmcnt(8)\n" \
  "v_mfma_f32_16x16x32_bf16 v[72:75], v[60:63], a[44:47], v[72:75]\n" \
  "ds_read_b128 v[52:55], v191 offset:19328\n" \
  "v_mfma_f32_16x16x32_bf16 v[68:71], v[56:59], a[116:119], v[68:71]\n" \
  "ds_read_b64_tr_b16 v[112:113], v194 offset:2048\n" \
  "v_mfma_f32_16x16x32_bf16 v[76:79], v[60:63], a[116:119], v[76:79]\n" \
  "ds_read_b64_tr_b16 v[114:115], v194 offset:2304\n" \
  "s_waitcnt lgkmcnt(9)\n" \
  "v_mfma_f32_16x16x32_bf16 v[64:67], v[32:35], a[48:51], v[64:67]\n" \
  "ds_read_b128 v[56:59], v192 offset:896\n" \
  "s_waitcnt lgkmcnt(9)\n" \
  "v_mfma_f32_16x16x32_bf16 v[72:75], v[36:39], a[48:51], v[72:75]\n" \
  "ds_read_b128 v[60:63], v192 offset:19328\n" \
  "v_mfma_f32_16x16x32_bf16 v[68:71], v[32:35], a[120:123], v[68:71]\n" \
  "s_add_u32 m0, s52, 5120\n" \
  "ds_read_b64_tr_b16 v[116:117], v160 offset:2048\n" \
  "s_waitcnt lgkmcnt(9)\n" \
  "v_mfma_f32_16x16x32_bf16 v[76:79], v[36:39], a[120:123], v[76:79]\n" \
  "buffer_load_dwordx4 v201, s[44:47], s51 offen lds\n" \
  "v_mfma_f32_16x16x32_bf16 v[64:67], v[40:43], a[52:55], v[64:67]\n" \
  "ds_read_b128 v[32:35], v191 offset:1024\n" \
  "s_waitcnt lgkmcnt(9)\n" \
  "v_mfma_f32_16x16x32_bf16 v[72:75], v[44:47], a[52:55], v[72:75]\n" \
  "ds_read_b128 v[36:39], v191 offset:19456\n" \
  "v_mfma_f32_16x16x32_bf16 v[68:71], v[40:43], a[124:127], v[68:71]\n" \
  "s_cmp_lt_u32 s48, s64\n" \
  "s_cbranch_scc1 .LU_e54_%=\n" \
  ".LU_e54_b_%=:\n" \
  "v_mfma_f32_16x16x32_bf16 v[76:79], v[44:47], a[124:127], v[76:79]\n" \
  "ds_read_b64_tr_b16 v[118:119], v160 offset:2304\n" \
  "s_waitcnt lgkmcnt(9)\n" \
  "v_mfma_f32_16x16x32_bf16 v[64:67], v[48:51], a[56:59], v[64:67]\n" \
  "ds_read_b128 v[40:43], v192 offset:1024\n" \
  "s_waitcnt lgkmcnt(9)\n" \
  "v_mfma_f32_16x16x32_bf16 v[72:75], v[52:55], a[56:59], v[72:75]\n" \
  "ds_read_b128 v[44:47], v192 offset:19456\n" \
  "v_mfma_f32_16x16x32_bf16 v[68:71], v[48:51], a[128:131], v[68:71]\n" \
  "s_add_u32 m0, s52, 6144\n" \
  "ds_read_b64_tr_b16 v[120:121], v194 offset:4096\n" \
  "s_waitcnt lgkmcnt(8)\n" \
  "v_mfma_f32_16x16x32_bf16 v[76:79], v[52:55], a[128:131], v[76:79]\n" \
  "buffer_load_dwordx4 v202, s[44:47], s51 offen lds\n" \
  "v_mfma_f32_16x16x32_bf16 v[64:67], v[56:59], a[60:63], v[64:67]\n" \
  "ds_read_b64_tr_b16 v[122:123], v194 offset:4352\n" \
  "s_waitcnt lgkmcnt(8)\n" \
  "v_mfma_f32_16x16x32_bf16 v[72:75], v[60:63], a[60:63], v[72:75]\n" \
  "ds_read_b64_tr_b16 v[124:125], v160 offset:4096\n" \
  "v_mfma_f32_16x16x32_bf16 v[68:71], v[56:59], a[132:135], v[68:71]\n" \
  "s_add_u32 m0, s52, 7168\n" \
  "ds_read_b64_tr_b16 v[126:127], v160 offset:4352\n" \
  "s_waitcnt lgkmcnt(8)\n" \
  "v_mfma_f32_16x16x32_bf16 v[76:79], v[60:63], a[132:135], v[76:79]\n" \
  "buffer_load_dwordx4 v203, s[44:47], s51 offen lds\n" \
  "v_mfma_f32_16x16x32_bf16 v[64:67], v[32:35], a[64:67], v[64:67]\n" \
  "ds_read_b64_tr_b16 v[128:129], v194 offset:6144\n" \
  "s_waitcnt lgkmcnt(8)\n" \
  "v_mfma_f32_16x16x32_bf16 v[72:75], v[36:39], a[64:67], v[72:75]\n" \
  "ds_read_b64_tr_b16 v[130:131], v194 offset:6400\n" \
  "v_mfma_f32_16x16x32_bf16 v[68:71], v[32:35], a[136:139], v[68:71]\n" \
  "ds_read_b64_tr_b16 v[132:133], v160 offset:6144\n" \
  "v_mfma_f32_16x16x32_bf16 v[76:79], v[36:39], a[136:139], v[76:79]\n" \
  "ds_read_b64_tr_b16 v[134:135], v160 offset:6400\n" \
  "s_waitcnt lgkmcnt(9)\n" \
  "v_mfma_f32_16x16x32_bf16 v[64:67], v[40:43], a[68:71], v[64:67]\n" \
  "s_waitcnt lgkmcnt(8)\n" \
  "v_mfma_f32_16x16x32_bf16 v[72:75], v[44:47], a[68:71], v[72:75]\n" \
  "s_cmp_lt_u32 s48, s64\n" \
  "s_cbranch_scc1 .LU_e69_%=\n" \
  ".LU_e69_b_%=:\n" \
  "v_mfma_f32_16x16x32_bf16 v[68:71], v[40:43], a[140:143], v[68:71]\n" \
  "s_add_u32 m0, s52, 8192\n" \
  "v_mfma_f32_16x16x32_bf16 v[76:79], v[44:47], a[140:143], v[76:79]\n" \
  "buffer_load_dwordx4 v204, s[44:47], s51 offen lds\n" \
  "s_cmp_le_u32 s48, 5\n" \
  "s_cbranch_scc1 .LU_first_%=\n" \
  ".LU_phb_%=:\n" \
  "v_mfma_f32_16x16x32_bf16 v[88:91], v[104:107], a[144:147], 0\n" \
  "v_mfma_f32_16x16x32_bf16 v[92:95], v[108:111], a[144:147], 0\n" \
  "s_lshr_b32 s62, s49, 8\n" \
  "s_and_b32 s62, s62, 8192\n" \
  "s_xor_b32 s62, s62, s49\n" \
  "v_mfma_f32_16x16x32_bf16 v[88:91], v[112:115], a[148:151], v[88:91]\n" \
  "v_cvt_pk_bf16_f32 v80, v64, v65\n" \
  "v_cvt_pk_bf16_f32 v81, v66, v67\n" \
  "v_mfma_f32_16x16x32_bf16 v[92:95], v[116:119], a[148:151], v[92:95]\n" \
  "s_waitcnt lgkmcnt(6)\n" \
  "v_cvt_pk_bf16_f32 v82, v72, v73\n" \
  "v_cvt_pk_bf16_f32 v83, v74, v75\n" \
  "v_mfma_f32_16x16x32_bf16 v[88:91], v[120:123], a[152:155], v[88:91]\n" \
  "s_waitcnt lgkmcnt(4)\n" \
  "buffer_store_dwordx4 v[80:83], v190, s[40:43], s62 offen sc1\n" \
  "v_mfma_f32_16x16x32_bf16 v[92:95], v[124:127], a[152:155], v[92:95]\n" \
  "ds_write_b128 v193, v[80:83]\n" \
  "s_waitcnt lgkmcnt(3)\n" \
  "v_mfma_f32_16x16x32_bf16 v[88:91], v[128:131], a[156:159], v[88:91]\n" \
  "s_waitcnt lgkmcnt(1)\n" \
  "v_cvt_pk_bf16_f32 v84, v76, v77\n" \
  "v_cvt_pk_bf16_f32 v85, v78, v79\n" \
  "v_mfma_f32_16x16x32_bf16 v[92:95], v[132:135], a[156:159], v[92:95]\n" \
  "v_cvt_pk_bf16_f32 v86, v68, v69\n" \
  "v_cvt_pk_bf16_f32 v87, v70, v71\n" \
  "v_mfma_f32_16x16x32_bf16 v[96:99], v[104:107], a[160:163], 0\n" \
  "buffer_store_dwordx4 v[84:87], v190, s[40:43], s62 offen offset:1024 sc1\n" \
  "v_mfma_f32_16x16x32_bf16 v[100:103], v[108:111], a[160:163], 0\n" \
  "ds_write_b128 v193, v[84:87] offset:1024\n" \
  "v_mfma_f32_16x16x32_bf16 v[96:99], v[112:115], a[164:167], v[96:99]\n" \
  "s_xor_b32 s63, s50, 0\n" \
  "v_cvt_pk_bf16_f32 v136, v88, v89\n" \
  "v_cvt_pk_bf16_f32 v137, v90, v91\n" \
  "v_mfma_f32_16x16x32_bf16 v[100:103], v[116:119], a[164:167], v[100:103]\n" \
  "s_add_u32 s63, s63, 0\n" \
  "v_cvt_pk_bf16_f32 v138, v92, v93\n" \
  "v_cvt_pk_bf16_f32 v139, v94, v95\n" \
  "v_mfma_f32_16x16x32_bf16 v[96:99], v[120:123], a[168:171], v[96:99]\n" \
  "buffer_store_dwordx4 v[136:139], v190, s[40:43], s63 offen sc1\n" \
  "v_mfma_f32_16x16x32_bf16 v[100:103], v[124:127], a[168:171], v[100:103]\n" \
  "s_add_u32 s58, s48, 1\n" \
  "s_and_b32 s58, s58, 3\n" \
  "s_cselect_b32 s58, 0, 147456\n" \
  "v_mfma_f32_16x16x32_bf16 v[96:99], v[128:131], a[172:175], v[96:99]\n" \
  "s_sub_u32 s58, 36864, s58\n" \
  "v_add_u32 v163, s58, v191\n" \
  "v_add_u32 v162, s58, v192\n" \
  "v_mfma_f32_16x16x32_bf16 v[100:103], v[132:135], a[172:175], v[100:103]\n" \
  "s_cmp_lt_u32 s51, 958464\n" \
  "s_cselect_b32 s60, 36864, 0\n" \
  "s_add_u32 s51, s51, s60\n" \
  "v_mfma_f32_16x16x32_bf16 v[88:91], v[104:107], a[176:179], 0\n" \
  "s_add_u32 s51, s51, 36864\n" \
  "s_min_u32 s51, s51, 2322432\n" \
  "s_add_u32 s52, s52, 36864\n" \
  "v_mfma_f32_16x16x32_bf16 v[92:95], v[108:111], a[176:179], 0\n" \
  "s_cmp_ge_u32 s52, 163840\n" \
  "s_cselect_b32 s60, 147456, 0\n" \
  "s_sub_u32 s52, s52, s60\n" \
  "v_mfma_f32_16x16x32_bf16 v[88:91], v[112:115], a[180:183], v[88:91]\n" \
  "s_xor_b32 s63, s50, 1024\n" \
  "v_cvt_pk_bf16_f32 v136, v96, v97\n" \
  "v_cvt_pk_bf16_f32 v137, v98, v99\n" \
  "v_mfma_f32_16x16x32_bf16 v[92:95], v[116:119], a[180:183], v[92:95]\n" \
  "s_add_u32 s63, s63, 1048576\n" \
  "v_cvt_pk_bf16_f32 v138, v100, v101\n" \
  "v_cvt_pk_bf16_f32 v139, v102, v103\n" \
  "v_mfma_f32_16x16x32_bf16 v[88:91], v[120:123], a[184:187], v[88:91]\n" \
  "buffer_store_dwordx4 v[136:139], v190, s[40:43], s63 offen sc1\n" \
  "v_mfma_f32_16x16x32_bf16 v[92:95], v[124:127], a[184:187], v[92:95]\n" \
  "v_mfma_f32_16x16x32_bf16 v[88:91], v[128:131], a[188:191], v[88:91]\n" \
  "s_nop 2\n" \
  "v_mfma_f32_16x16x32_bf16 v[92:95], v[132:135], a[188:191], v[92:95]\n" \
  "v_mfma_f32_16x16x32_bf16 v[96:99], v[104:107], a[192:195], 0\n" \
  "v_mfma_f32_16x16x32_bf16 v[100:103], v[108:111], a[192:195], 0\n" \
  "v_mfma_f32_16x16x32_bf16 v[96:99], v[112:115], a[196:199], v[96:99]\n" \
  "s_xor_b32 s63, s50, 278528\n" \
  "v_cvt_pk_bf16_f32 v136, v88, v89\n" \
  "v_cvt_pk_bf16_f32 v137, v90, v91\n" \
  "v_mfma_f32_16x16x32_bf16 v[100:103], v[116:119], a[196:199], v[100:103]\n" \
  "s_add_u32 s63, s63, 8388608\n" \
  "v_cvt_pk_bf16_f32 v138, v92, v93\n" \
  "v_cvt_pk_bf16_f32 v139, v94, v95\n" \
  "v_mfma_f32_16x16x32_bf16 v[96:99], v[120:123], a[200:203], v[96:99]\n" \
  "buffer_store_dwordx4 v[136:139], v190, s[40:43], s63 offen sc1\n" \
  "v_mfma_f32_16x16x32_bf16 v[100:103], v[124:127], a[200:203], v[100:103]\n" \
  "v_mfma_f32_16x16x32_bf16 v[96:99], v[128:131], a[204:207], v[96:99]\n" \
  "v_readfirstlane_b32 s58, v164\n" \
  "v_readfirstlane_b32 s61, v165\n" \
  "v_mfma_f32_16x16x32_bf16 v[100:103], v[132:135], a[204:207], v[100:103]\n" \
  "s_cmp_eq_u32 s61, s55\n" \
  "s_cselect_b32 s58, s58, 0\n" \
  "s_add_u32 s58, s58, s68\n" \
  "v_mfma_f32_16x16x32_bf16 v[88:91], v[104:107], a[208:211], 0\n" \
  "s_cmp_lt_u32 s58, s73\n" \
  "s_cbranch_scc1 .LU_fc_%=\n" \
  ".LU_fc_b_%=:\n" \
  "v_mfma_f32_16x16x32_bf16 v[92:95], v[108:111], a[208:211], 0\n" \
  "buffer_load_dwordx2 v[164:165], off, s[40:43], %43 sc1\n" \
  "v_mfma_f32_16x16x32_bf16 v[88:91], v[112:115], a[212:215], v[88:91]\n" \
  "s_xor_b32 s63, s50, 279552\n" \
  "v_cvt_pk_bf16_f32 v136, v96, v97\n" \
  "v_cvt_pk_bf16_f32 v137, v98, v99\n" \
  "v_mfma_f32_16x16x32_bf16 v[92:95], v[116:119], a[212:215], v[92:95]\n" \
  "s_add_u32 s63, s63, 9437184\n" \
  "v_cvt_pk_bf16_f32 v138, v100, v101\n" \
  "v_cvt_pk_bf16_f32 v139, v102, v103\n" \
  "v_mfma_f32_16x16x32_bf16 v[88:91], v[120:123], a[216:219], v[88:91]\n" \
  "buffer_store_dwordx4 v[136:139], v190, s[40:43], s63 offen sc1\n" \
  "v_mfma_f32_16x16x32_bf16 v[92:95], v[124:127], a[216:219], v[92:95]\n" \
  "v_mfma_f32_16x16x32_bf16 v[88:91], v[128:131], a[220:223], v[88:91]\n" \
  "s_nop 0\n" \
  "s_waitcnt vmcnt(16)\n" \
  "buffer_store_dwordx2 v[168:169], v167, s[40:43], 0 offen sc1\n" \
  "v_mfma_f32_16x16x32_bf16 v[92:95], v[132:135], a[220:223], v[92:95]\n" \
  "v_mfma_f32_16x16x32_bf16 v[96:99], v[104:107], a[224:227], 0\n" \
  "v_mfma_f32_16x16x32_bf16 v[100:103], v[108:111], a[224:227], 0\n" \
  "v_mfma_f32_16x16x32_bf16 v[96:99], v[112:115], a[228:231], v[96:99]\n" \
  "s_xor_b32 s63, s50, 557056\n" \
  "v_cvt_pk_bf16_f32 v136, v88, v89\n" \
  "v_cvt_pk_bf16_f32 v137, v90, v91\n" \
  "v_mfma_f32_16x16x32_bf16 v[100:103], v[116:119], a[228:231], v[100:103]\n" \
  "s_add_u32 s63, s63, 16777216\n" \
  "v_cvt_pk_bf16_f32 v138, v92, v93\n" \
  "v_cvt_pk_bf16_f32 v139, v94, v95\n" \
  "v_mfma_f32_16x16x32_bf16 v[96:99], v[120:123], a[232:235], v[96:99]\n" \
  "buffer_store_dwordx4 v[136:139], v190, s[40:43], s63 offen sc1\n" \
  "v_mfma_f32_16x16x32_bf16 v[100:103], v[124:127], a[232:235], v[100:103]\n" \
  "v_mfma_f32_16x16x32_bf16 v[96:99], v[128:131], a[236:239], v[96:99]\n" \
  "ds_read_b128 v[32:35], v163\n" \
  "v_mfma_f32_16x16x32_bf16 v[100:103], v[132:135], a[236:239], v[100:103]\n" \
  "ds_read_b128 v[36:39], v163 offset:18432\n" \
  "v_mfma_f32_16x16x32_bf16 v[88:91], v[104:107], a[240:243], 0\n" \
  "ds_read_b128 v[40:43], v162\n" \
  "v_mfma_f32_16x16x32_bf16 v[92:95], v[108:111], a[240:243], 0\n" \
  "ds_read_b128 v[44:47], v162 offset:18432\n" \
  "v_mfma_f32_16x16x32_bf16 v[88:91], v[112:115], a[244:247], v[88:91]\n" \
  "s_xor_b32 s63, s50, 558080\n" \
  "v_cvt_pk_bf16_f32 v136, v96, v97\n" \
  "v_cvt_pk_bf16_f32 v137, v98, v99\n" \
  "v_mfma_f32_16x16x32_bf16 v[92:95], v[116:119], a[244:247], v[92:95]\n" \
  "s_add_u32 s63, s63, 17825792\n" \
  "v_cvt_pk_bf16_f32 v138, v100, v101\n" \
  "v_cvt_pk_bf16_f32 v139, v102, v103\n" \
  "v_mfma_f32_16x16x32_bf16 v[88:91], v[120:123], a[248:251], v[88:91]\n" \
  "buffer_store_dwordx4 v[136:139], v190, s[40:43], s63 offen sc1\n" \
  "v_mfma_f32_16x16x32_bf16 v[92:95], v[124:127], a[248:251], v[92:95]\n" \
  "v_mfma_f32_16x16x32_bf16 v[88:91], v[128:131], a[252:255], v[88:91]\n" \
  "ds_read_b128 v[48:51], v163 offset:128\n" \
  "v_mfma_f32_16x16x32_bf16 v[92:95], v[132:135], a[252:255], v[92:95]\n" \
  "ds_read_b128 v[52:55], v163 offset:18560\n" \
  "v_mfma_f32_16x16x32_bf16 v[96:99], v[104:107], v[0:3], 0\n" \
  "v_mov_b32 v191, v163\n" \
  "v_mov_b32 v192, v162\n" \
  "v_mfma_f32_16x16x32_bf16 v[100:103], v[108:111], v[0:3], 0\n" \
  "s_add_u32 s49, s49, s71\n" \
  "v_xor_b32 v193, 8192, v193\n" \
  "v_xor_b32 v194, 8192, v194\n" \
  "v_mfma_f32_16x16x32_bf16 v[96:99], v[112:115], v[4:7], v[96:99]\n" \
  "s_xor_b32 s63, s50, 835584\n" \
  "v_cvt_pk_bf16_f32 v136, v88, v89\n" \
  "v_cvt_pk_bf16_f32 v137, v90, v91\n" \
  "v_mfma_f32_16x16x32_bf16 v[100:103], v[116:119], v[4:7], v[100:103]\n" \
  "s_add_u32 s63, s63, 25165824\n" \
  "v_cvt_pk_bf16_f32 v138, v92, v93\n" \
  "v_cvt_pk_bf16_f32 v139, v94, v95\n" \
  "v_mfma_f32_16x16x32_bf16 v[96:99], v[120:123], v[8:11], v[96:99]\n" \
  "buffer_store_dwordx4 v[136:139], v190, s[40:43], s63 offen sc1\n" \
  "v_mfma_f32_16x16x32_bf16 v[100:103], v[124:127], v[8:11], v[100:103]\n" \
  "s_add_u32 s48, s48, 1\n" \
  "s_add_u32 s73, s73, s70\n" \
  "v_mfma_f32_16x16x32_bf16 v[96:99], v[128:131], v[12:15], v[96:99]\n" \
  "s_cmp_eq_u32 s73, s74\n" \
  "s_cselect_b32 s49, 2130706432, s49\n" \
  "v_mfma_f32_16x16x32_bf16 v[100:103], v[132:135], v[12:15], v[100:103]\n" \
  "s_cmp_le_u32 s48, 5\n" \
  "s_addc_u32 s67, s73, 0\n" \
  "v_mfma_f32_16x16x32_bf16 v[88:91], v[104:107], v[16:19], 0\n" \
  "s_cmp_gt_u32 s67, s74\n" \
  "s_cselect_b32 s67, 2, 0\n" \
  "v_mfma_f32_16x16x32_bf16 v[92:95], v[108:111], v[16:19], 0\n" \
  "s_cmp_le_u32 s48, 0\n" \
  "s_addc_u32 s67, s67, 0\n" \
  "v_mfma_f32_16x16x32_bf16 v[88:91], v[112:115], v[20:23], v[88:91]\n" \
  "s_xor_b32 s63, s50, 836608\n" \
  "v_cvt_pk_bf16_f32 v104, v96, v97\n" \
  "v_cvt_pk_bf16_f32 v105, v98, v99\n" \
  "v_mfma_f32_16x16x32_bf16 v[92:95], v[116:119], v[20:23], v[92:95]\n" \
  "s_add_u32 s63, s63, 26214400\n" \
  "v_cvt_pk_bf16_f32 v106, v100, v101\n" \
  "v_cvt_pk_bf16_f32 v107, v102, v103\n" \
  "v_mfma_f32_16x16x32_bf16 v[88:91], v[120:123], v[24:27], v[88:91]\n" \
  "buffer_store_dwordx4 v[104:107], v190, s[40:43], s63 offen sc1\n" \
  "v_mfma_f32_16x16x32_bf16 v[92:95], v[124:127], v[24:27], v[92:95]\n" \
  "v_mfma_f32_16x16x32_bf16 v[88:91], v[128:131], v[28:31], v[88:91]\n" \
  "v_mfma_f32_16x16x32_bf16 v[92:95], v[132:135], v[28:31], v[92:95]\n" \
  "s_cmp_eq_u32 s67, 0\n" \
  "s_waitcnt lgkmcnt(0)\n" \
  ".LU_first_b_%=:\n" \
  "s_cbranch_scc1 .LU_loop_%=\n" \
  "s_cmp_eq_u32 s67, 1\n" \
  "s_cbranch_scc1 .LU_now_%=\n" \
  "s_waitcnt lgkmcnt(0)\n" \
  "s_nop 7\n" \
  "s_mov_b32 s63, s57\n" \
  "v_cvt_pk_bf16_f32 v104, v88, v89\n" \
  "v_cvt_pk_bf16_f32 v105, v90, v91\n" \
  "v_cvt_pk_bf16_f32 v106, v92, v93\n" \
  "v_cvt_pk_bf16_f32 v107, v94, v95\n" \
  "buffer_store_dwordx4 v[104:107], v190, s[40:43], s63 offen sc1\n" \
  "s_waitcnt vmcnt(0)\n" \
  "s_barrier\n" \
  "s_mov_b32 s48, 64\n" \
  "s_mov_b32 s69, 0\n" \
  ".LU_ie_%=:\n" \
  "s_cmp_ge_u32 s96, 0xc0000000\n" \
  "s_cbranch_scc1 .LU_ie_x_%=\n" \
  "buffer_load_dwordx2 v[184:185], off, s[40:43], %41 sc1\n" \
  "s_waitcnt vmcnt(0)\n" \
  "s_add_u32 s69, s69, 1\n" \
  "s_cmp_lt_u32 s69, 2097152\n" \
  "v_readfirstlane_b32 s96, v184\n" \
  "s_cbranch_scc1 .LU_ie_%=\n" \
  ".LU_ie_x_%=:\n" \
  "v_mov_b32 v168, s48\n" \
  "buffer_store_dwordx2 v[168:169], v167, s[40:43], 0 offen sc1\n" \
  "s_branch .LU_end_%=\n" \
  ".LU_e38_%=:\n" \
  "s_getreg_b32 s60, hwreg(HW_REG_IB_STS, 0, 4)\n" \
  "s_cmp_gt_u32 s60, 4\n" \
  "s_cbranch_scc1 .LU_e38_b_%=\n" \
  "s_waitcnt vmcnt(4)\n" \
  "v_add_u32 v168, s73, v161\n" \
  "s_mov_b64 exec, 2\n" \
  "buffer_store_dwordx2 v[168:169], v167, s[40:43], 0 offen sc1\n" \
  "s_mov_b64 exec, -1\n" \
  "s_mov_b32 s64, 0\n" \
  "s_branch .LU_e38_b_%=\n" \
  ".LU_e54_%=:\n" \
  "s_getreg_b32 s60, hwreg(HW_REG_IB_STS, 0, 4)\n" \
  "s_cmp_gt_u32 s60, 6\n" \
  "s_cbranch_scc1 .LU_e54_b_%=\n" \
  "s_waitcnt vmcnt(6)\n" \
  "v_add_u32 v168, s73, v161\n" \
  "s_mov_b64 exec, 2\n" \
  "buffer_store_dwordx2 v[168:169], v167, s[40:43], 0 offen sc1\n" \
  "s_mov_b64 exec, -1\n" \
  "s_mov_b32 s64, 0\n" \
  "s_branch .LU_e54_b_%=\n" \
  ".LU_e69_%=:\n" \
  "s_getreg_b32 s60, hwreg(HW_REG_IB_STS, 0, 4)\n" \
  "s_cmp_gt_u32 s60, 99\n" \
  "s_cbranch_scc1 .LU_e69_b_%=\n" \
  "s_waitcnt vmcnt(8)\n" \
  "v_add_u32 v168, s73, v161\n" \
  "s_mov_b64 exec, 2\n" \
  "buffer_store_dwordx2 v[168:169], v167, s[40:43], 0 offen sc1\n" \
  "s_mov_b64 exec, -1\n" \
  "s_mov_b32 s64, 0\n" \
  "s_branch .LU_e69_b_%=\n" \
  ".LU_fc_%=:\n" \
  "s_mov_b32 s69, 0\n" \
  ".LU_fc1_%=:\n" \
  "s_sleep 8\n" \
  "buffer_load_dwordx2 v[164:165], off, s[40:43], %43 sc1\n" \
  "s_waitcnt vmcnt(0)\n" \
  "v_readfirstlane_b32 s58, v164\n" \
  "v_readfirstlane_b32 s61, v165\n" \
  "s_cmp_eq_u32 s61, s55\n" \
  "s_cselect_b32 s58, s58, 0\n" \
  "s_add_u32 s58, s58, s68\n" \
  "s_cmp_lt_u32 s58, s73\n" \
  "s_cbranch_scc0 .LU_fc_b_%=\n" \
  "s_add_u32 s69, s69, 1\n" \
  "s_cmp_lt_u32 s69, 524288\n" \
  "s_cbranch_scc1 .LU_fc1_%=\n" \
  "v_mov_b32 v164, 1\n" \
  "v_mov_b32 v165, s55\n" \
  "buffer_store_dwordx2 v[164:165], off, s[40:43], %42 offset:128 sc1\n" \
  "s_waitcnt vmcnt(0)\n" \
  "v_mov_b32 v164, 100\n" \
  "s_branch .LU_fc_b_%=\n" \
  ".LU_first_%=:\n" \
  "s_waitcnt lgkmcnt(0)\n" \
  "s_cmp_eq_u32 s73, 0\n" \
  "s_cselect_b32 s70, 2, s70\n" \
  "s_lshl_b32 s71, s70, 19\n" \
  "s_nop 4\n" \
  "s_lshr_b32 s62, s49, 8\n" \
  "s_and_b32 s62, s62, 8192\n" \
  "s_xor_b32 s62, s62, s49\n" \
  "v_cvt_pk_bf16_f32 v80, v64, v65\n" \
  "v_cvt_pk_bf16_f32 v81, v66, v67\n" \
  "v_cvt_pk_bf16_f32 v82, v72, v73\n" \
  "v_cvt_pk_bf16_f32 v83, v74, v75\n" \
  "ds_write_b128 v193, v[80:83]\n" \
  "v_cvt_pk_bf16_f32 v84, v76, v77\n" \
  "v_cvt_pk_bf16_f32 v85, v78, v79\n" \
  "v_cvt_pk_bf16_f32 v86, v68, v69\n" \
  "v_cvt_pk_bf16_f32 v87, v70, v71\n" \
  "ds_write_b128 v193, v[84:87] offset:1024\n" \
  "s_cmp_eq_u32 s48, 5\n" \
  "s_cbranch_scc1 .LU_skip_%=\n" \
  "v_xor_b32 v160, 8, v194\n" \
  "s_waitcnt lgkmcnt(0)\n" \
  "s_barrier\n" \
  "v_xor_b32 v194, 8192, v194\n" \
  "v_xor_b32 v160, 8192, v160\n" \
  "ds_read_b64_tr_b16 v[104:105], v194\n" \
  "ds_read_b64_tr_b16 v[106:107], v194 offset:256\n" \
  "ds_read_b64_tr_b16 v[108:109], v160\n" \
  "ds_read_b64_tr_b16 v[110:111], v160 offset:256\n" \
  "ds_read_b64_tr_b16 v[112:113], v194 offset:2048\n" \
  "ds_read_b64_tr_b16 v[114:115], v194 offset:2304\n" \
  "ds_read_b64_tr_b16 v[116:117], v160 offset:2048\n" \
  "ds_read_b64_tr_b16 v[118:119], v160 offset:2304\n" \
  "ds_read_b64_tr_b16 v[120:121], v194 offset:4096\n" \
  "ds_read_b64_tr_b16 v[122:123], v194 offset:4352\n" \
  "ds_read_b64_tr_b16 v[124:125], v160 offset:4096\n" \
  "ds_read_b64_tr_b16 v[126:127], v160 offset:4352\n" \
  "ds_read_b64_tr_b16 v[128:129], v194 offset:6144\n" \
  "ds_read_b64_tr_b16 v[130:131], v194 offset:6400\n" \
  "ds_read_b64_tr_b16 v[132:133], v160 offset:6144\n" \
  "ds_read_b64_tr_b16 v[134:135], v160 offset:6400\n" \
  "s_mov_b32 s58, s73\n" \
  "s_and_b32 s58, s58, 15\n" \
  "s_lshl_b32 s58, s58, 16\n" \
  "s_xor_b32 s50, s58, s66\n" \
  "s_xor_b32 s57, s58, s59\n" \
  "v_xor_b32 v194, 8192, v194\n" \
  "s_waitcnt lgkmcnt(0)\n" \
  "s_branch .LU_phb_%=\n" \
  ".LU_skip_%=:\n" \
  "buffer_store_dwordx4 v[80:83], v190, s[40:43], s62 offen sc1\n" \
  "buffer_store_dwordx4 v[84:87], v190, s[40:43], s62 offen offset:1024 sc1\n" \
  "s_mov_b64 exec, 6\n" \
  "v_mov_b32 v166, -1\n" \
  "s_mov_b64 exec, -1\n" \
  "s_add_u32 s58, s48, 1\n" \
  "s_and_b32 s58, s58, 3\n" \
  "s_cselect_b32 s58, 0, 147456\n" \
  "s_sub_u32 s58, 36864, s58\n" \
  "s_cmp_lt_u32 s51, 958464\n" \
  "s_cselect_b32 s60, 36864, 0\n" \
  "s_add_u32 s51, s51, s60\n" \
  "s_add_u32 s51, s51, 36864\n" \
  "s_min_u32 s51, s51, 2322432\n" \
  "s_add_u32 s52, s52, 36864\n" \
  "s_cmp_ge_u32 s52, 163840\n" \
  "s_cselect_b32 s60, 147456, 0\n" \
  "s_sub_u32 s52, s52, s60\n" \
  "v_add_u32 v163, s58, v191\n" \
  "v_add_u32 v162, s58, v192\n" \
  "ds_read_b128 v[32:35], v163\n" \
  "ds_read_b128 v[36:39], v163 offset:18432\n" \
  "ds_read_b128 v[40:43], v162\n" \
  "ds_read_b128 v[44:47], v162 offset:18432\n" \
  "ds_read_b128 v[48:51], v163 offset:128\n" \
  "ds_read_b128 v[52:55], v163 offset:18560\n" \
  "s_add_u32 s49, s49, s71\n" \
  "s_mov_b32 s69, 0\n" \
  "v_mov_b32 v191, v163\n" \
  "v_mov_b32 v192, v162\n" \
  "v_xor_b32 v193, 8192, v193\n" \
  "v_xor_b32 v194, 8192, v194\n" \
  ".LU_in_%=:\n" \
  "s_cmp_ge_u32 s96, 0xc0000000\n" \
  "s_cbranch_scc1 .LU_in_x_%=\n" \
  "buffer_load_dwordx2 v[184:185], off, s[40:43], %41 sc1\n" \
  "s_waitcnt vmcnt(0)\n" \
  "s_add_u32 s69, s69, 1\n" \
  "s_cmp_lt_u32 s69, 2097152\n" \
  "v_readfirstlane_b32 s96, v184\n" \
  "s_cbranch_scc1 .LU_in_%=\n" \
  ".LU_in_x_%=:\n" \
  "s_mov_b32 s57, 2130706432\n" \
  "s_mov_b32 s69, 0\n" \
  ".LU_sk1_%=:\n" \
  "buffer_load_dwordx2 v[164:165], off, s[40:43], %43 sc1\n" \
  "s_waitcnt vmcnt(0)\n" \
  "v_readfirstlane_b32 s58, v164\n" \
  "v_readfirstlane_b32 s61, v165\n" \
  "s_cmp_eq_u32 s61, s55\n" \
  "s_cselect_b32 s58, s58, 0\n" \
  "s_add_u32 s58, s58, s68\n" \
  "s_cmp_lt_u32 s58, s73\n" \
  "s_cbranch_scc0 .LU_sk2_%=\n" \
  "v_mov_b32 v168, s73\n" \
  "s_mov_b64 exec, 2\n" \
  "buffer_store_dwordx2 v[168:169], v167, s[40:43], 0 offen sc1\n" \
  "s_mov_b64 exec, -1\n" \
  "s_sleep 16\n" \
  "s_add_u32 s69, s69, 1\n" \
  "s_cmp_lt_u32 s69, 524288\n" \
  "s_cbranch_scc1 .LU_sk1_%=\n" \
  "v_mov_b32 v164, 1\n" \
  "v_mov_b32 v165, s55\n" \
  "buffer_store_dwordx2 v[164:165], off, s[40:43], %42 offset:128 sc1\n" \
  "s_waitcnt vmcnt(0)\n" \
  ".LU_sk2_%=:\n" \
  "s_add_u32 s48, s48, 1\n" \
  "s_add_u32 s73, s73, s70\n" \
  "s_cmp_eq_u32 s73, s74\n" \
  "s_cselect_b32 s49, 2130706432, s49\n" \
  "s_cmp_le_u32 s48, 5\n" \
  "s_addc_u32 s67, s73, 0\n" \
  "s_cmp_gt_u32 s67, s74\n" \
  "s_cselect_b32 s67, 2, 0\n" \
  "s_cmp_le_u32 s48, 0\n" \
  "s_addc_u32 s67, s67, 0\n" \
  "s_cmp_eq_u32 s67, 0\n" \
  "s_waitcnt lgkmcnt(0)\n" \
  "s_branch .LU_first_b_%=\n" \
  ".LU_now_%=:\n" \
  "s_cmp_gt_u32 s48, 0\n" \
  "s_cbranch_scc1 .LU_loop_%=\n" \
  "s_nop 7\n" \
  "s_mov_b32 s63, s57\n" \
  "v_cvt_pk_bf16_f32 v104, v88, v89\n" \
  "v_cvt_pk_bf16_f32 v105, v90, v91\n" \
  "v_cvt_pk_bf16_f32 v106, v92, v93\n" \
  "v_cvt_pk_bf16_f32 v107, v94, v95\n" \
  "buffer_store_dwordx4 v[104:107], v190, s[40:43], s63 offen sc1\n" \
  "v_mov_b32 v168, s73\n" \
  "s_mov_b64 exec, 6\n" \
  "s_waitcnt vmcnt(0)\n" \
  "buffer_store_dwordx2 v[168:169], v167, s[40:43], 0 offen sc1\n" \
  "s_mov_b64 exec, -1\n" \
  "s_branch .LU_loop_%=\n" \
  ".LU_end_%=:\n" \
  "s_mov_b32 %0, s55\n"
#define P_ASM_VOPS "=&s"(tag), "+{v190}"(pi.l16), "+{v191}"(pi.xr0), "+{v192}"(pi.xr1), "+{v193}"(pi.ew), "+{v194}"(pi.er), "+{v195}"(pi.uo), "+{v196}"(pi.xs0), "+{v197}"(pi.xs1), "+{v198}"(pi.xs2), "+{v199}"(pi.xs3), "+{v200}"(pi.xs4), "+{v201}"(pi.xs5), "+{v202}"(pi.xs6), "+{v203}"(pi.xs7), "+{v204}"(pi.xs8), "+{v170}"(pi.vq0), "+{v171}"(pi.vq1), "+{v172}"(pi.vq2), "+{v173}"(pi.vq3), "+{v174}"(pi.vo0), "+{v175}"(pi.vo1), "+{v176}"(pi.vo2), "+{v177}"(pi.vo3), "+{v178}"(pi.v50), "+{v179}"(pi.v51), "+{v180}"(pi.v52), "+{v181}"(pi.v53), "+{v182}"(pi.tro0), "+{v183}"(pi.tro1)
#define P_ASM_SOPS "s"(pi.ws), "s"(pi.Wa), "s"(pi.Wb), "s"(pi.x), "s"(pi.w), "s"(pi.o_a), "s"(pi.o_b), "s"(pi.o_p), "s"(pi.o_q), "s"(pi.f_a), "s"(pi.f_b), "s"(pi.f_go), "s"(pi.o_ctl), "s"(pi.f_c), "s"(pi.u8), "s"(pi.rng), "s"(pi.f_d)
#define P_ASM_CLOB "v0", "v1", "v2", "v3", "v4", "v5", "v6", "v7", "v8", "v9", "v10", "v11", "v12", "v13", "v14", "v15", "v16", "v17", "v18", "v19", "v20", "v21", "v22", "v23", "v24", "v25", "v26", "v27", "v28", "v29", "v30", "v31", "v32", "v33", "v34", "v35", "v36", "v37", "v38", "v39", "v40", "v41", "v42", "v43", "v44", "v45", "v46", "v47", "v48", "v49", "v50", "v51", "v52", "v53", "v54", "v55", "v56", "v57", "v58", "v59", "v60", "v61", "v62", "v63", "v64", "v65", "v66", "v67", "v68", "v69", "v70", "v71", "v72", "v73", "v74", "v75", "v76", "v77", "v78", "v79", "v80", "v81", "v82", "v83", "v84", "v85", "v86", "v87", "v88", "v89", "v90", "v91", "v92", "v93", "v94", "v95", "v96", "v97", "v98", "v99", "v100", "v101", "v102", "v103", "v104", "v105", "v106", "v107", "v108", "v109", "v110", "v111", "v112", "v113", "v114", "v115", "v116", "v117", "v118", "v119", "v120", "v121", "v122", "v123", "v124", "v125", "v126", "v127", "v128", "v129", "v130", "v131", "v132", "v133", "v134", "v135", "v136", "v137", "v138", "v139", "v140", "v141", "v142", "v143", "v144", "v145", "v146", "v147", "v148", "v149", "v150", "v151", "v152", "v153", "v154", "v155", "v156", "v157", "v158", "v159", "v160", "v161", "v162", "v163", "v164", "v165", "v166", "v167", "v168", "v169", "v184", "v185", "v186", "v187", "v188", "v189", "a0", "a1", "a2", "a3", "a4", "a5", "a6", "a7", "a8", "a9", "a10", "a11", "a12", "a13", "a14", "a15", "a16", "a17", "a18", "a19", "a20", "a21", "a22", "a23", "a24", "a25", "a26", "a27", "a28", "a29", "a30", "a31", "a32", "a33", "a34", "a35", "a36", "a37", "a38", "a39", "a40", "a41", "a42", "a43", "a44", "a45", "a46", "a47", "a48", "a49", "a50", "a51", "a52", "a53", "a54", "a55", "a56", "a57", "a58", "a59", "a60", "a61", "a62", "a63", "a64", "a65", "a66", "a67", "a68", "a69", "a70", "a71", "a72", "a73", "a74", "a75", "a76", "a77", "a78", "a79", "a80", "a81", "a82", "a83", "a84", "a85", "a86", "a87", "a88", "a89", "a90", "a91", "a92", "a93", "a94", "a95", "a96", "a97", "a98", "a99", "a100", "a101", "a102", "a103", "a104", "a105", "a106", "a107", "a108", "a109", "a110", "a111", "a112", "a113", "a114", "a115", "a116", "a117", "a118", "a119", "a120", "a121", "a122", "a123", "a124", "a125", "a126", "a127", "a128", "a129", "a130", "a131", "a132", "a133", "a134", "a135", "a136", "a137", "a138", "a139", "a140", "a141", "a142", "a143", "a144", "a145", "a146", "a147", "a148", "a149", "a150", "a151", "a152", "a153", "a154", "a155", "a156", "a157", "a158", "a159", "a160", "a161", "a162", "a163", "a164", "a165", "a166", "a167", "a168", "a169", "a170", "a171", "a172", "a173", "a174", "a175", "a176", "a177", "a178", "a179", "a180", "a181", "a182", "a183", "a184", "a185", "a186", "a187", "a188", "a189", "a190", "a191", "a192", "a193", "a194", "a195", "a196", "a197", "a198", "a199", "a200", "a201", "a202", "a203", "a204", "a205", "a206", "a207", "a208", "a209", "a210", "a211", "a212", "a213", "a214", "a215", "a216", "a217", "a218", "a219", "a220", "a221", "a222", "a223", "a224", "a225", "a226", "a227", "a228", "a229", "a230", "a231", "a232", "a233", "a234", "a235", "a236", "a237", "a238", "a239", "a240", "a241", "a242", "a243", "a244", "a245", "a246", "a247", "a248", "a249", "a250", "a251", "a252", "a253", "a254", "a255", "s40", "s41", "s42", "s43", "s44", "s45", "s46", "s47", "s48", "s49", "s50", "s51", "s52", "s53", "s54", "s55", "s56", "s57", "s58", "s59", "s60", "s61", "s62", "s63", "s64", "s65", "s66", "s67", "s68", "s69", "s70", "s71", "s72", "s73", "s74", "s75", "s76", "s77", "s78", "s79", "s80", "s81", "s82", "s83", "s84", "s85", "s86", "s87", "s88", "s89", "s90", "s91", "s92", "s93", "s94", "s95", "s96", "s97", "s98", "s99", "vcc", "scc", "memory"
#define O_ASM_TEXT \
  "s_mov_b64 s[40:41], %11\n" \
  "s_and_b32 s41, s41, 0xffff\n" \
  "s_mov_b32 s42, 255852544\n" \
  "s_mov_b32 s43, 0x00020000\n" \
  "s_mov_b64 s[44:45], %12\n" \
  "s_and_b32 s45, s45, 0xffff\n" \
  "s_mov_b32 s46, 4718592\n" \
  "s_mov_b32 s47, 0x00020000\n" \
  "s_mul_i32 s62, %13, 24576\n" \
  "s_add_u32 s60, %15, 0\n" \
  "s_add_u32 m0, s62, 0\n" \
  "s_nop 0\n" \
  "buffer_load_dwordx4 v194, s[44:47], s60 offen lds\n" \
  "s_add_u32 m0, s62, 1024\n" \
  "s_nop 0\n" \
  "buffer_load_dwordx4 v195, s[44:47], s60 offen lds\n" \
  "s_add_u32 m0, s62, 2048\n" \
  "s_nop 0\n" \
  "buffer_load_dwordx4 v196, s[44:47], s60 offen lds\n" \
  "s_add_u32 s60, %15, 18432\n" \
  "s_add_u32 m0, s62, 3072\n" \
  "s_nop 0\n" \
  "buffer_load_dwordx4 v194, s[44:47], s60 offen lds\n" \
  "s_add_u32 m0, s62, 4096\n" \
  "s_nop 0\n" \
  "buffer_load_dwordx4 v195, s[44:47], s60 offen lds\n" \
  "s_add_u32 m0, s62, 5120\n" \
  "s_nop 0\n" \
  "buffer_load_dwordx4 v196, s[44:47], s60 offen lds\n" \
  "s_add_u32 s60, %15, 36864\n" \
  "s_add_u32 m0, s62, 6144\n" \
  "s_nop 0\n" \
  "buffer_load_dwordx4 v194, s[44:47], s60 offen lds\n" \
  "s_add_u32 m0, s62, 7168\n" \
  "s_nop 0\n" \
  "buffer_load_dwordx4 v195, s[44:47], s60 offen lds\n" \
  "s_add_u32 m0, s62, 8192\n" \
  "s_nop 0\n" \
  "buffer_load_dwordx4 v196, s[44:47], s60 offen lds\n" \
  "s_add_u32 s60, %15, 55296\n" \
  "s_add_u32 m0, s62, 9216\n" \
  "s_nop 0\n" \
  "buffer_load_dwordx4 v194, s[44:47], s60 offen lds\n" \
  "s_add_u32 m0, s62, 10240\n" \
  "s_nop 0\n" \
  "buffer_load_dwordx4 v195, s[44:47], s60 offen lds\n" \
  "s_add_u32 m0, s62, 11264\n" \
  "s_nop 0\n" \
  "buffer_load_dwordx4 v196, s[44:47], s60 offen lds\n" \
  "s_add_u32 s60, %15, 73728\n" \
  "s_add_u32 m0, s62, 12288\n" \
  "s_nop 0\n" \
  "buffer_load_dwordx4 v194, s[44:47], s60 offen lds\n" \
  "s_add_u32 m0, s62, 13312\n" \
  "s_nop 0\n" \
  "buffer_load_dwordx4 v195, s[44:47], s60 offen lds\n" \
  "s_add_u32 m0, s62, 14336\n" \
  "s_nop 0\n" \
  "buffer_load_dwordx4 v196, s[44:47], s60 offen lds\n" \
  "s_add_u32 s60, %15, 92160\n" \
  "s_add_u32 m0, s62, 15360\n" \
  "s_nop 0\n" \
  "buffer_load_dwordx4 v194, s[44:47], s60 offen lds\n" \
  "s_add_u32 m0, s62, 16384\n" \
  "s_nop 0\n" \
  "buffer_load_dwordx4 v195, s[44:47], s60 offen lds\n" \
  "s_add_u32 m0, s62, 17408\n" \
  "s_nop 0\n" \
  "buffer_load_dwordx4 v196, s[44:47], s60 offen lds\n" \
  "s_add_u32 s60, %15, 110592\n" \
  "s_add_u32 m0, s62, 18432\n" \
  "s_nop 0\n" \
  "buffer_load_dwordx4 v194, s[44:47], s60 offen lds\n" \
  "s_add_u32 m0, s62, 19456\n" \
  "s_nop 0\n" \
  "buffer_load_dwordx4 v195, s[44:47], s60 offen lds\n" \
  "s_add_u32 m0, s62, 20480\n" \
  "s_nop 0\n" \
  "buffer_load_dwordx4 v196, s[44:47], s60 offen lds\n" \
  "s_add_u32 s60, %15, 129024\n" \
  "s_add_u32 m0, s62, 21504\n" \
  "s_nop 0\n" \
  "buffer_load_dwordx4 v194, s[44:47], s60 offen lds\n" \
  "s_add_u32 m0, s62, 22528\n" \
  "s_nop 0\n" \
  "buffer_load_dwordx4 v195, s[44:47], s60 offen lds\n" \
  "s_add_u32 m0, s62, 23552\n" \
  "s_nop 0\n" \
  "buffer_load_dwordx4 v196, s[44:47], s60 offen lds\n" \
  "s_waitcnt vmcnt(0)\n" \
  "ds_read_b64_tr_b16 a[0:1], v197\n" \
  "ds_read_b64_tr_b16 a[2:3], v197 offset:3072\n" \
  "ds_read_b64_tr_b16 a[4:5], v197 offset:32\n" \
  "ds_read_b64_tr_b16 a[6:7], v197 offset:3104\n" \
  "ds_read_b64_tr_b16 a[8:9], v197 offset:64\n" \
  "ds_read_b64_tr_b16 a[10:11], v197 offset:3136\n" \
  "ds_read_b64_tr_b16 a[12:13], v197 offset:96\n" \
  "ds_read_b64_tr_b16 a[14:15], v197 offset:3168\n" \
  "ds_read_b64_tr_b16 a[16:17], v197 offset:128\n" \
  "ds_read_b64_tr_b16 a[18:19], v197 offset:3200\n" \
  "ds_read_b64_tr_b16 a[20:21], v197 offset:160\n" \
  "ds_read_b64_tr_b16 a[22:23], v197 offset:3232\n" \
  "ds_read_b64_tr_b16 a[24:25], v197 offset:6144\n" \
  "ds_read_b64_tr_b16 a[26:27], v197 offset:9216\n" \
  "ds_read_b64_tr_b16 a[28:29], v197 offset:6176\n" \
  "ds_read_b64_tr_b16 a[30:31], v197 offset:9248\n" \
  "ds_read_b64_tr_b16 a[32:33], v197 offset:6208\n" \
  "ds_read_b64_tr_b16 a[34:35], v197 offset:9280\n" \
  "ds_read_b64_tr_b16 a[36:37], v197 offset:6240\n" \
  "ds_read_b64_tr_b16 a[38:39], v197 offset:9312\n" \
  "ds_read_b64_tr_b16 a[40:41], v197 offset:6272\n" \
  "ds_read_b64_tr_b16 a[42:43], v197 offset:9344\n" \
  "ds_read_b64_tr_b16 a[44:45], v197 offset:6304\n" \
  "ds_read_b64_tr_b16 a[46:47], v197 offset:9376\n" \
  "ds_read_b64_tr_b16 a[48:49], v197 offset:12288\n" \
  "ds_read_b64_tr_b16 a[50:51], v197 offset:15360\n" \
  "ds_read_b64_tr_b16 a[52:53], v197 offset:12320\n" \
  "ds_read_b64_tr_b16 a[54:55], v197 offset:15392\n" \
  "ds_read_b64_tr_b16 a[56:57], v197 offset:12352\n" \
  "ds_read_b64_tr_b16 a[58:59], v197 offset:15424\n" \
  "ds_read_b64_tr_b16 a[60:61], v197 offset:12384\n" \
  "ds_read_b64_tr_b16 a[62:63], v197 offset:15456\n" \
  "ds_read_b64_tr_b16 a[64:65], v197 offset:12416\n" \
  "ds_read_b64_tr_b16 a[66:67], v197 offset:15488\n" \
  "ds_read_b64_tr_b16 a[68:69], v197 offset:12448\n" \
  "ds_read_b64_tr_b16 a[70:71], v197 offset:15520\n" \
  "ds_read_b64_tr_b16 a[72:73], v197 offset:18432\n" \
  "ds_read_b64_tr_b16 a[74:75], v197 offset:21504\n" \
  "ds_read_b64_tr_b16 a[76:77], v197 offset:18464\n" \
  "ds_read_b64_tr_b16 a[78:79], v197 offset:21536\n" \
  "ds_read_b64_tr_b16 a[80:81], v197 offset:18496\n" \
  "ds_read_b64_tr_b16 a[82:83], v197 offset:21568\n" \
  "ds_read_b64_tr_b16 a[84:85], v197 offset:18528\n" \
  "ds_read_b64_tr_b16 a[86:87], v197 offset:21600\n" \
  "ds_read_b64_tr_b16 a[88:89], v197 offset:18560\n" \
  "ds_read_b64_tr_b16 a[90:91], v197 offset:21632\n" \
  "ds_read_b64_tr_b16 a[92:93], v197 offset:18592\n" \
  "ds_read_b64_tr_b16 a[94:95], v197 offset:21664\n" \
  "s_waitcnt lgkmcnt(0)\n" \
  "s_add_u32 s60, %15, 147456\n" \
  "s_add_u32 m0, s62, 0\n" \
  "s_nop 0\n" \
  "buffer_load_dwordx4 v194, s[44:47], s60 offen lds\n" \
  "s_add_u32 m0, s62, 1024\n" \
  "s_nop 0\n" \
  "buffer_load_dwordx4 v195, s[44:47], s60 offen lds\n" \
  "s_add_u32 m0, s62, 2048\n" \
  "s_nop 0\n" \
  "buffer_load_dwordx4 v196, s[44:47], s60 offen lds\n" \
  "s_add_u32 s60, %15, 165888\n" \
  "s_add_u32 m0, s62, 3072\n" \
  "s_nop 0\n" \
  "buffer_load_dwordx4 v194, s[44:47], s60 offen lds\n" \
  "s_add_u32 m0, s62, 4096\n" \
  "s_nop 0\n" \
  "buffer_load_dwordx4 v195, s[44:47], s60 offen lds\n" \
  "s_add_u32 m0, s62, 5120\n" \
  "s_nop 0\n" \
  "buffer_load_dwordx4 v196, s[44:47], s60 offen lds\n" \
  "s_add_u32 s60, %15, 184320\n" \
  "s_add_u32 m0, s62, 6144\n" \
  "s_nop 0\n" \
  "buffer_load_dwordx4 v194, s[44:47], s60 offen lds\n" \
  "s_add_u32 m0, s62, 7168\n" \
  "s_nop 0\n" \
  "buffer_load_dwordx4 v195, s[44:47], s60 offen lds\n" \
  "s_add_u32 m0, s62, 8192\n" \
  "s_nop 0\n" \
  "buffer_load_dwordx4 v196, s[44:47], s60 offen lds\n" \
  "s_add_u32 s60, %15, 202752\n" \
  "s_add_u32 m0, s62, 9216\n" \
  "s_nop 0\n" \
  "buffer_load_dwordx4 v194, s[44:47], s60 offen lds\n" \
  "s_add_u32 m0, s62, 10240\n" \
  "s_nop 0\n" \
  "buffer_load_dwordx4 v195, s[44:47], s60 offen lds\n" \
  "s_add_u32 m0, s62, 11264\n" \
  "s_nop 0\n" \
  "buffer_load_dwordx4 v196, s[44:47], s60 offen lds\n" \
  "s_add_u32 s60, %15, 221184\n" \
  "s_add_u32 m0, s62, 12288\n" \
  "s_nop 0\n" \
  "buffer_load_dwordx4 v194, s[44:47], s60 offen lds\n" \
  "s_add_u32 m0, s62, 13312\n" \
  "s_nop 0\n" \
  "buffer_load_dwordx4 v195, s[44:47], s60 offen lds\n" \
  "s_add_u32 m0, s62, 14336\n" \
  "s_nop 0\n" \
  "buffer_load_dwordx4 v196, s[44:47], s60 offen lds\n" \
  "s_add_u32 s60, %15, 239616\n" \
  "s_add_u32 m0, s62, 15360\n" \
  "s_nop 0\n" \
  "buffer_load_dwordx4 v194, s[44:47], s60 offen lds\n" \
  "s_add_u32 m0, s62, 16384\n" \
  "s_nop 0\n" \
  "buffer_load_dwordx4 v195, s[44:47], s60 offen lds\n" \
  "s_add_u32 m0, s62, 17408\n" \
  "s_nop 0\n" \
  "buffer_load_dwordx4 v196, s[44:47], s60 offen lds\n" \
  "s_add_u32 s60, %15, 258048\n" \
  "s_add_u32 m0, s62, 18432\n" \
  "s_nop 0\n" \
  "buffer_load_dwordx4 v194, s[44:47], s60 offen lds\n" \
  "s_add_u32 m0, s62, 19456\n" \
  "s_nop 0\n" \
  "buffer_load_dwordx4 v195, s[44:47], s60 offen lds\n" \
  "s_add_u32 m0, s62, 20480\n" \
  "s_nop 0\n" \
  "buffer_load_dwordx4 v196, s[44:47], s60 offen lds\n" \
  "s_add_u32 s60, %15, 276480\n" \
  "s_add_u32 m0, s62, 21504\n" \
  "s_nop 0\n" \
  "buffer_load_dwordx4 v194, s[44:47], s60 offen lds\n" \
  "s_add_u32 m0, s62, 22528\n" \
  "s_nop 0\n" \
  "buffer_load_dwordx4 v195, s[44:47], s60 offen lds\n" \
  "s_add_u32 m0, s62, 23552\n" \
  "s_nop 0\n" \
  "buffer_load_dwordx4 v196, s[44:47], s60 offen lds\n" \
  "s_waitcnt vmcnt(0)\n" \
  "ds_read_b64_tr_b16 a[96:97], v197\n" \
  "ds_read_b64_tr_b16 a[98:99], v197 offset:3072\n" \
  "ds_read_b64_tr_b16 a[100:101], v197 offset:32\n" \
  "ds_read_b64_tr_b16 a[102:103], v197 offset:3104\n" \
  "ds_read_b64_tr_b16 a[104:105], v197 offset:64\n" \
  "ds_read_b64_tr_b16 a[106:107], v197 offset:3136\n" \
  "ds_read_b64_tr_b16 a[108:109], v197 offset:96\n" \
  "ds_read_b64_tr_b16 a[110:111], v197 offset:3168\n" \
  "ds_read_b64_tr_b16 a[112:113], v197 offset:128\n" \
  "ds_read_b64_tr_b16 a[114:115], v197 offset:3200\n" \
  "ds_read_b64_tr_b16 a[116:117], v197 offset:160\n" \
  "ds_read_b64_tr_b16 a[118:119], v197 offset:3232\n" \
  "ds_read_b64_tr_b16 a[120:121], v197 offset:6144\n" \
  "ds_read_b64_tr_b16 a[122:123], v197 offset:9216\n" \
  "ds_read_b64_tr_b16 a[124:125], v197 offset:6176\n" \
  "ds_read_b64_tr_b16 a[126:127], v197 offset:9248\n" \
  "ds_read_b64_tr_b16 a[128:129], v197 offset:6208\n" \
  "ds_read_b64_tr_b16 a[130:131], v197 offset:9280\n" \
  "ds_read_b64_tr_b16 a[132:133], v197 offset:6240\n" \
  "ds_read_b64_tr_b16 a[134:135], v197 offset:9312\n" \
  "ds_read_b64_tr_b16 a[136:137], v197 offset:6272\n" \
  "ds_read_b64_tr_b16 a[138:139], v197 offset:9344\n" \
  "ds_read_b64_tr_b16 a[140:141], v197 offset:6304\n" \
  "ds_read_b64_tr_b16 a[142:143], v197 offset:9376\n" \
  "ds_read_b64_tr_b16 a[144:145], v197 offset:12288\n" \
  "ds_read_b64_tr_b16 a[146:147], v197 offset:15360\n" \
  "ds_read_b64_tr_b16 a[148:149], v197 offset:12320\n" \
  "ds_read_b64_tr_b16 a[150:151], v197 offset:15392\n" \
  "ds_read_b64_tr_b16 a[152:153], v197 offset:12352\n" \
  "ds_read_b64_tr_b16 a[154:155], v197 offset:15424\n" \
  "ds_read_b64_tr_b16 a[156:157], v197 offset:12384\n" \
  "ds_read_b64_tr_b16 a[158:159], v197 offset:15456\n" \
  "ds_read_b64_tr_b16 a[160:161], v197 offset:12416\n" \
  "ds_read_b64_tr_b16 a[162:163], v197 offset:15488\n" \
  "ds_read_b64_tr_b16 a[164:165], v197 offset:12448\n" \
  "ds_read_b64_tr_b16 a[166:167], v197 offset:15520\n" \
  "ds_read_b64_tr_b16 a[168:169], v197 offset:18432\n" \
  "ds_read_b64_tr_b16 a[170:171], v197 offset:21504\n" \
  "ds_read_b64_tr_b16 a[172:173], v197 offset:18464\n" \
  "ds_read_b64_tr_b16 a[174:175], v197 offset:21536\n" \
  "ds_read_b64_tr_b16 a[176:177], v197 offset:18496\n" \
  "ds_read_b64_tr_b16 a[178:179], v197 offset:21568\n" \
  "ds_read_b64_tr_b16 a[180:181], v197 offset:18528\n" \
  "ds_read_b64_tr_b16 a[182:183], v197 offset:21600\n" \
  "ds_read_b64_tr_b16 a[184:185], v197 offset:18560\n" \
  "ds_read_b64_tr_b16 a[186:187], v197 offset:21632\n" \
  "ds_read_b64_tr_b16 a[188:189], v197 offset:18592\n" \
  "ds_read_b64_tr_b16 a[190:191], v197 offset:21664\n" \
  "s_waitcnt lgkmcnt(0)\n" \
  "s_barrier\n" \
  "s_mov_b32 s56, %20\n" \
  "s_mov_b32 s48, 0\n" \
  "s_mov_b32 s53, 2130706432\n" \
  "v_mov_b32 v187, %14\n" \
  "v_min_u32 v197, 1, v190\n" \
  "v_lshlrev_b32 v197, 30, v197\n" \
  "s_branch .LO_fl_%=\n" \
  ".LO_fl_b_%=:\n" \
  "s_and_b32 s48, s56, 255\n" \
  "s_bfe_u32 s55, s56, 0x80008\n" \
  "s_lshr_b32 s56, s56, 16\n" \
  "s_lshl_b32 s49, s48, 19\n" \
  "s_add_u32 s49, s49, %16\n" \
  "s_mul_i32 s50, s48, 393216\n" \
  "s_add_u32 s50, s50, %17\n" \
  "s_mov_b32 s53, 2130706432\n" \
  "s_mov_b32 s94, s48\n" \
  "v_mov_b32 v191, v199\n" \
  "v_mov_b32 v192, v200\n" \
  "s_mov_b32 s69, 0\n" \
  ".LO_p0_%=:\n" \
  "buffer_load_dwordx2 v[188:189], v193, s[40:43], 0 offen sc1\n" \
  "s_waitcnt vmcnt(0)\n" \
  "v_cmp_ne_u32 vcc, %14, v189\n" \
  "s_nop 1\n" \
  "v_cndmask_b32 v188, v188, 0, vcc\n" \
  "v_cmp_ge_i32 vcc, s94, v188\n" \
  "s_cbranch_vccz .LO_p0_x_%=\n" \
  "s_sleep 2\n" \
  "s_add_u32 s69, s69, 1\n" \
  "s_and_b32 s63, s69, 1023\n" \
  "s_cbranch_scc1 .LO_p0_%=\n" \
  "buffer_load_dwordx2 v[188:189], off, s[40:43], %19 offset:128 sc1\n" \
  "s_waitcnt vmcnt(0)\n" \
  "v_readfirstlane_b32 s63, v189\n" \
  "s_cmp_eq_u32 s63, %14\n" \
  "s_cbranch_scc1 .LO_p0_x_%=\n" \
  "s_cmp_le_u32 s69, 2097152\n" \
  "s_cbranch_scc1 .LO_p0_%=\n" \
  "v_mov_b32 v188, 1\n" \
  "v_mov_b32 v189, %14\n" \
  "buffer_store_dwordx2 v[188:189], off, s[40:43], %19 offset:128 sc1\n" \
  "s_waitcnt vmcnt(0)\n" \
  ".LO_p0_x_%=:\n" \
  "s_lshr_b32 s76, s49, 8\n" \
  "s_and_b32 s76, s76, 8192\n" \
  "s_xor_b32 s76, s76, s49\n" \
  "s_add_u32 s77, s76, 4096\n" \
  "s_add_u32 s78, s76, 16384\n" \
  "s_add_u32 s79, s76, 20480\n" \
  "s_add_u32 s49, s49, 524288\n" \
  "buffer_load_dwordx4 v[0:3], v190, s[40:43], s76 offen sc1\n" \
  "buffer_load_dwordx4 v[8:11], v190, s[40:43], s76 offen offset:2048 sc1\n" \
  "buffer_load_dwordx4 v[16:19], v190, s[40:43], s77 offen sc1\n" \
  "buffer_load_dwordx4 v[24:27], v190, s[40:43], s77 offen offset:2048 sc1\n" \
  "buffer_load_dwordx4 v[32:35], v190, s[40:43], s78 offen sc1\n" \
  "buffer_load_dwordx4 v[40:43], v190, s[40:43], s78 offen offset:2048 sc1\n" \
  "buffer_load_dwordx4 v[48:51], v190, s[40:43], s79 offen sc1\n" \
  "buffer_load_dwordx4 v[56:59], v190, s[40:43], s79 offen offset:2048 sc1\n" \
  "buffer_load_dwordx4 v[4:7], v190, s[40:43], s76 offen offset:1024 sc1\n" \
  "buffer_load_dwordx4 v[12:15], v190, s[40:43], s76 offen offset:3072 sc1\n" \
  "buffer_load_dwordx4 v[20:23], v190, s[40:43], s77 offen offset:1024 sc1\n" \
  "buffer_load_dwordx4 v[28:31], v190, s[40:43], s77 offen offset:3072 sc1\n" \
  "buffer_load_dwordx4 v[36:39], v190, s[40:43], s78 offen offset:1024 sc1\n" \
  "buffer_load_dwordx4 v[44:47], v190, s[40:43], s78 offen offset:3072 sc1\n" \
  "buffer_load_dwordx4 v[52:55], v190, s[40:43], s79 offen offset:1024 sc1\n" \
  "buffer_load_dwordx4 v[60:63], v190, s[40:43], s79 offen offset:3072 sc1\n" \
  "s_lshr_b32 s76, s49, 8\n" \
  "s_and_b32 s76, s76, 8192\n" \
  "s_xor_b32 s76, s76, s49\n" \
  "s_add_u32 s77, s76, 4096\n" \
  "s_add_u32 s78, s76, 16384\n" \
  "s_add_u32 s79, s76, 20480\n" \
  "s_add_u32 s49, s49, 524288\n" \
  "s_add_u32 s94, s94, 1\n" \
  "s_mov_b32 s54, 0\n" \
  "s_waitcnt vmcnt(0)\n" \
  "v_cmp_ne_u32 vcc, %14, v189\n" \
  "s_nop 1\n" \
  "v_cndmask_b32 v188, v188, 0, vcc\n" \
  "v_cmp_ge_i32 vcc, s94, v188\n" \
  ".LO_loop_%=:\n" \
  "s_cbranch_vccnz .LO_pl_%=\n" \
  ".LO_pl_b_%=:\n" \
  "s_waitcnt vmcnt(18)\n" \
  "v_mfma_f32_16x16x32_bf16 v[64:67], a[0:3], v[0:3], 0\n" \
  "s_waitcnt lgkmcnt(4)\n" \
  "buffer_load_dwordx2 v[188:189], v193, s[40:43], 0 offen sc1\n" \
  "v_mfma_f32_16x16x32_bf16 v[68:71], a[4:7], v[0:3], 0\n" \
  "s_waitcnt lgkmcnt(3)\n" \
  "v_mfma_f32_16x16x32_bf16 v[72:75], a[8:11], v[0:3], 0\n" \
  "ds_write_b128 v191, v[88:91] offset:6144\n" \
  "s_waitcnt lgkmcnt(3)\n" \
  "v_mfma_f32_16x16x32_bf16 v[76:79], a[12:15], v[0:3], 0\n" \
  "s_mov_b32 s81, s79\n" \
  "s_waitcnt lgkmcnt(2)\n" \
  "v_mfma_f32_16x16x32_bf16 v[80:83], a[16:19], v[0:3], 0\n" \
  "s_waitcnt lgkmcnt(1)\n" \
  "v_mfma_f32_16x16x32_bf16 v[84:87], a[20:23], v[0:3], 0\n" \
  "buffer_load_dwordx4 v[0:3], v190, s[40:43], s76 offen sc1\n" \
  "s_waitcnt vmcnt(19)\n" \
  "v_mfma_f32_16x16x32_bf16 v[64:67], a[24:27], v[8:11], v[64:67]\n" \
  "v_mfma_f32_16x16x32_bf16 v[68:71], a[28:31], v[8:11], v[68:71]\n" \
  "v_mfma_f32_16x16x32_bf16 v[72:75], a[32:35], v[8:11], v[72:75]\n" \
  "ds_write_b128 v191, v[92:95] offset:7168\n" \
  "v_mfma_f32_16x16x32_bf16 v[76:79], a[36:39], v[8:11], v[76:79]\n" \
  "v_mfma_f32_16x16x32_bf16 v[80:83], a[40:43], v[8:11], v[80:83]\n" \
  "s_waitcnt vmcnt(18)\n" \
  "v_mfma_f32_16x16x32_bf16 v[84:87], a[44:47], v[8:11], v[84:87]\n" \
  "buffer_load_dwordx4 v[8:11], v190, s[40:43], s76 offen offset:2048 sc1\n" \
  "v_mfma_f32_16x16x32_bf16 v[64:67], a[48:51], v[16:19], v[64:67]\n" \
  "v_mfma_f32_16x16x32_bf16 v[68:71], a[52:55], v[16:19], v[68:71]\n" \
  "v_mfma_f32_16x16x32_bf16 v[72:75], a[56:59], v[16:19], v[72:75]\n" \
  "ds_write_b128 v191, v[96:99] offset:8192\n" \
  "v_mfma_f32_16x16x32_bf16 v[76:79], a[60:63], v[16:19], v[76:79]\n" \
  "v_mfma_f32_16x16x32_bf16 v[80:83], a[64:67], v[16:19], v[80:83]\n" \
  "s_waitcnt vmcnt(18)\n" \
  "v_mfma_f32_16x16x32_bf16 v[84:87], a[68:71], v[16:19], v[84:87]\n" \
  "buffer_load_dwordx4 v[16:19], v190, s[40:43], s77 offen sc1\n" \
  "v_mfma_f32_16x16x32_bf16 v[64:67], a[72:75], v[24:27], v[64:67]\n" \
  "v_mfma_f32_16x16x32_bf16 v[68:71], a[76:79], v[24:27], v[68:71]\n" \
  "v_mfma_f32_16x16x32_bf16 v[72:75], a[80:83], v[24:27], v[72:75]\n" \
  "ds_write_b128 v191, v[100:103] offset:9216\n" \
  "v_mfma_f32_16x16x32_bf16 v[76:79], a[84:87], v[24:27], v[76:79]\n" \
  "v_mfma_f32_16x16x32_bf16 v[80:83], a[88:91], v[24:27], v[80:83]\n" \
  "s_waitcnt vmcnt(18)\n" \
  "v_mfma_f32_16x16x32_bf16 v[84:87], a[92:95], v[24:27], v[84:87]\n" \
  "buffer_load_dwordx4 v[24:27], v190, s[40:43], s77 offen offset:2048 sc1\n" \
  "v_mfma_f32_16x16x32_bf16 v[64:67], a[96:99], v[32:35], v[64:67]\n" \
  "v_mfma_f32_16x16x32_bf16 v[68:71], a[100:103], v[32:35], v[68:71]\n" \
  "v_mfma_f32_16x16x32_bf16 v[72:75], a[104:107], v[32:35], v[72:75]\n" \
  "ds_write_b128 v191, v[104:107] offset:10240\n" \
  "v_mfma_f32_16x16x32_bf16 v[76:79], a[108:111], v[32:35], v[76:79]\n" \
  "v_mfma_f32_16x16x32_bf16 v[80:83], a[112:115], v[32:35], v[80:83]\n" \
  "s_waitcnt vmcnt(18)\n" \
  "v_mfma_f32_16x16x32_bf16 v[84:87], a[116:119], v[32:35], v[84:87]\n" \
  "buffer_load_dwordx4 v[32:35], v190, s[40:43], s78 offen sc1\n" \
  "v_mfma_f32_16x16x32_bf16 v[64:67], a[120:123], v[40:43], v[64:67]\n" \
  "v_mfma_f32_16x16x32_bf16 v[68:71], a[124:127], v[40:43], v[68:71]\n" \
  "v_mfma_f32_16x16x32_bf16 v[72:75], a[128:131], v[40:43], v[72:75]\n" \
  "ds_write_b128 v191, v[108:111] offset:11264\n" \
  "v_mfma_f32_16x16x32_bf16 v[76:79], a[132:135], v[40:43], v[76:79]\n" \
  "v_xor_b32 v191, 65536, v191\n" \
  "v_mfma_f32_16x16x32_bf16 v[80:83], a[136:139], v[40:43], v[80:83]\n" \
  "s_waitcnt vmcnt(18)\n" \
  "v_mfma_f32_16x16x32_bf16 v[84:87], a[140:143], v[40:43], v[84:87]\n" \
  "buffer_load_dwordx4 v[40:43], v190, s[40:43], s78 offen offset:2048 sc1\n" \
  "v_mfma_f32_16x16x32_bf16 v[64:67], a[144:147], v[48:51], v[64:67]\n" \
  "v_mfma_f32_16x16x32_bf16 v[68:71], a[148:151], v[48:51], v[68:71]\n" \
  "s_add_i32 s60, s48, -1\n" \
  "v_mov_b32 v186, s60\n" \
  "v_mfma_f32_16x16x32_bf16 v[72:75], a[152:155], v[48:51], v[72:75]\n" \
  "v_mfma_f32_16x16x32_bf16 v[76:79], a[156:159], v[48:51], v[76:79]\n" \
  "s_waitcnt lgkmcnt(0)\n" \
  "s_waitcnt vmcnt(8)\n" \
  "s_barrier\n" \
  "v_mfma_f32_16x16x32_bf16 v[80:83], a[160:163], v[48:51], v[80:83]\n" \
  "ds_read_b128 v[120:123], v192\n" \
  "v_mfma_f32_16x16x32_bf16 v[84:87], a[164:167], v[48:51], v[84:87]\n" \
  "buffer_load_dwordx4 v[48:51], v190, s[40:43], s79 offen sc1\n" \
  "v_mfma_f32_16x16x32_bf16 v[64:67], a[168:171], v[56:59], v[64:67]\n" \
  "ds_read_b128 v[112:115], v192 offset:12288\n" \
  "v_mfma_f32_16x16x32_bf16 v[68:71], a[172:175], v[56:59], v[68:71]\n" \
  "buffer_store_dwordx2 v[186:187], v197, s[40:43], %18 offen sc1\n" \
  "v_mfma_f32_16x16x32_bf16 v[72:75], a[176:179], v[56:59], v[72:75]\n" \
  "ds_read_b128 v[116:119], v192 offset:24576\n" \
  "v_mfma_f32_16x16x32_bf16 v[76:79], a[180:183], v[56:59], v[76:79]\n" \
  "ds_read_b128 v[124:127], v192 offset:36864\n" \
  "v_mfma_f32_16x16x32_bf16 v[80:83], a[184:187], v[56:59], v[80:83]\n" \
  "ds_read_b128 v[128:131], v192 offset:1024\n" \
  "v_mfma_f32_16x16x32_bf16 v[84:87], a[188:191], v[56:59], v[84:87]\n" \
  "buffer_load_dwordx4 v[56:59], v190, s[40:43], s79 offen offset:2048 sc1\n" \
  "v_mfma_f32_16x16x32_bf16 v[88:91], a[0:3], v[4:7], 0\n" \
  "ds_read_b128 v[132:135], v192 offset:13312\n" \
  "ds_read_b128 v[136:139], v192 offset:25600\n" \
  "v_mfma_f32_16x16x32_bf16 v[92:95], a[4:7], v[4:7], 0\n" \
  "ds_read_b128 v[140:143], v192 offset:37888\n" \
  "ds_read_b128 v[144:147], v192 offset:2048\n" \
  "v_mfma_f32_16x16x32_bf16 v[96:99], a[8:11], v[4:7], 0\n" \
  "ds_write_b128 v191, v[64:67]\n" \
  "v_mfma_f32_16x16x32_bf16 v[100:103], a[12:15], v[4:7], 0\n" \
  "s_lshr_b32 s80, s49, 8\n" \
  "s_and_b32 s80, s80, 8192\n" \
  "v_mfma_f32_16x16x32_bf16 v[104:107], a[16:19], v[4:7], 0\n" \
  "s_xor_b32 s80, s80, s49\n" \
  "s_add_u32 s49, s49, 524288\n" \
  "v_mfma_f32_16x16x32_bf16 v[108:111], a[20:23], v[4:7], 0\n" \
  "buffer_load_dwordx4 v[4:7], v190, s[40:43], s76 offen offset:1024 sc1\n" \
  "v_mfma_f32_16x16x32_bf16 v[88:91], a[24:27], v[12:15], v[88:91]\n" \
  "ds_read_b128 v[148:151], v192 offset:14336\n" \
  "v_mfma_f32_16x16x32_bf16 v[92:95], a[28:31], v[12:15], v[92:95]\n" \
  "ds_read_b128 v[152:155], v192 offset:26624\n" \
  "ds_read_b128 v[156:159], v192 offset:38912\n" \
  "v_mfma_f32_16x16x32_bf16 v[96:99], a[32:35], v[12:15], v[96:99]\n" \
  "ds_write_b128 v191, v[68:71] offset:1024\n" \
  "v_mfma_f32_16x16x32_bf16 v[100:103], a[36:39], v[12:15], v[100:103]\n" \
  "s_add_u32 s79, s80, 20480\n" \
  "s_add_u32 s48, s48, 1\n" \
  "v_mfma_f32_16x16x32_bf16 v[104:107], a[40:43], v[12:15], v[104:107]\n" \
  "s_waitcnt lgkmcnt(12)\n" \
  "v_add_f32 v120, v120, v112\n" \
  "v_add_f32 v121, v121, v113\n" \
  "v_mfma_f32_16x16x32_bf16 v[108:111], a[44:47], v[12:15], v[108:111]\n" \
  "buffer_load_dwordx4 v[12:15], v190, s[40:43], s76 offen offset:3072 sc1\n" \
  "v_mfma_f32_16x16x32_bf16 v[88:91], a[48:51], v[20:23], v[88:91]\n" \
  "s_mov_b32 s76, s80\n" \
  "v_add_f32 v122, v122, v114\n" \
  "v_add_f32 v123, v123, v115\n" \
  "v_mfma_f32_16x16x32_bf16 v[92:95], a[52:55], v[20:23], v[92:95]\n" \
  "s_waitcnt lgkmcnt(10)\n" \
  "s_add_u32 s94, s48, 1\n" \
  "v_add_f32 v116, v116, v124\n" \
  "v_add_f32 v117, v117, v125\n" \
  "v_mfma_f32_16x16x32_bf16 v[96:99], a[56:59], v[20:23], v[96:99]\n" \
  "ds_write_b128 v191, v[72:75] offset:2048\n" \
  "v_mfma_f32_16x16x32_bf16 v[100:103], a[60:63], v[20:23], v[100:103]\n" \
  "v_add_f32 v118, v118, v126\n" \
  "v_add_f32 v119, v119, v127\n" \
  "v_mfma_f32_16x16x32_bf16 v[104:107], a[64:67], v[20:23], v[104:107]\n" \
  "v_add_f32 v120, v120, v116\n" \
  "v_add_f32 v121, v121, v117\n" \
  "v_mfma_f32_16x16x32_bf16 v[108:111], a[68:71], v[20:23], v[108:111]\n" \
  "buffer_load_dwordx4 v[20:23], v190, s[40:43], s77 offen offset:1024 sc1\n" \
  "v_mfma_f32_16x16x32_bf16 v[88:91], a[72:75], v[28:31], v[88:91]\n" \
  "v_add_f32 v122, v122, v118\n" \
  "v_add_f32 v123, v123, v119\n" \
  "v_mfma_f32_16x16x32_bf16 v[92:95], a[76:79], v[28:31], v[92:95]\n" \
  "v_cvt_pk_bf16_f32 v112, v120, v121\n" \
  "v_cvt_pk_bf16_f32 v113, v122, v123\n" \
  "v_mfma_f32_16x16x32_bf16 v[96:99], a[80:83], v[28:31], v[96:99]\n" \
  "ds_write_b128 v191, v[76:79] offset:3072\n" \
  "v_mfma_f32_16x16x32_bf16 v[100:103], a[84:87], v[28:31], v[100:103]\n" \
  "s_waitcnt lgkmcnt(10)\n" \
  "v_add_f32 v128, v128, v132\n" \
  "v_add_f32 v129, v129, v133\n" \
  "v_mfma_f32_16x16x32_bf16 v[104:107], a[88:91], v[28:31], v[104:107]\n" \
  "v_add_f32 v130, v130, v134\n" \
  "v_add_f32 v131, v131, v135\n" \
  "v_mfma_f32_16x16x32_bf16 v[108:111], a[92:95], v[28:31], v[108:111]\n" \
  "buffer_load_dwordx4 v[28:31], v190, s[40:43], s77 offen offset:3072 sc1\n" \
  "v_mfma_f32_16x16x32_bf16 v[88:91], a[96:99], v[36:39], v[88:91]\n" \
  "s_waitcnt lgkmcnt(8)\n" \
  "s_add_u32 s77, s80, 4096\n" \
  "v_add_f32 v136, v136, v140\n" \
  "v_add_f32 v137, v137, v141\n" \
  "v_mfma_f32_16x16x32_bf16 v[92:95], a[100:103], v[36:39], v[92:95]\n" \
  "v_add_f32 v138, v138, v142\n" \
  "v_add_f32 v139, v139, v143\n" \
  "v_mfma_f32_16x16x32_bf16 v[96:99], a[104:107], v[36:39], v[96:99]\n" \
  "ds_write_b128 v191, v[80:83] offset:4096\n" \
  "v_mfma_f32_16x16x32_bf16 v[100:103], a[108:111], v[36:39], v[100:103]\n" \
  "v_add_f32 v128, v128, v136\n" \
  "v_add_f32 v129, v129, v137\n" \
  "v_mfma_f32_16x16x32_bf16 v[104:107], a[112:115], v[36:39], v[104:107]\n" \
  "v_add_f32 v130, v130, v138\n" \
  "v_add_f32 v131, v131, v139\n" \
  "v_mfma_f32_16x16x32_bf16 v[108:111], a[116:119], v[36:39], v[108:111]\n" \
  "buffer_load_dwordx4 v[36:39], v190, s[40:43], s78 offen offset:1024 sc1\n" \
  "v_mfma_f32_16x16x32_bf16 v[88:91], a[120:123], v[44:47], v[88:91]\n" \
  "v_cvt_pk_bf16_f32 v114, v128, v129\n" \
  "v_cvt_pk_bf16_f32 v115, v130, v131\n" \
  "v_mfma_f32_16x16x32_bf16 v[92:95], a[124:127], v[44:47], v[92:95]\n" \
  "buffer_store_dwordx4 v[112:115], v190, s[40:43], s53 offen sc1\n" \
  "v_mfma_f32_16x16x32_bf16 v[96:99], a[128:131], v[44:47], v[96:99]\n" \
  "ds_write_b128 v191, v[84:87] offset:5120\n" \
  "v_mfma_f32_16x16x32_bf16 v[100:103], a[132:135], v[44:47], v[100:103]\n" \
  "s_waitcnt lgkmcnt(7)\n" \
  "v_add_f32 v144, v144, v148\n" \
  "v_add_f32 v145, v145, v149\n" \
  "v_mfma_f32_16x16x32_bf16 v[104:107], a[136:139], v[44:47], v[104:107]\n" \
  "v_add_f32 v146, v146, v150\n" \
  "v_add_f32 v147, v147, v151\n" \
  "v_mfma_f32_16x16x32_bf16 v[108:111], a[140:143], v[44:47], v[108:111]\n" \
  "buffer_load_dwordx4 v[44:47], v190, s[40:43], s78 offen offset:3072 sc1\n" \
  "v_mfma_f32_16x16x32_bf16 v[88:91], a[144:147], v[52:55], v[88:91]\n" \
  "s_waitcnt lgkmcnt(5)\n" \
  "s_add_u32 s78, s80, 16384\n" \
  "v_add_f32 v152, v152, v156\n" \
  "v_add_f32 v153, v153, v157\n" \
  "v_mfma_f32_16x16x32_bf16 v[92:95], a[148:151], v[52:55], v[92:95]\n" \
  "v_add_f32 v154, v154, v158\n" \
  "v_add_f32 v155, v155, v159\n" \
  "v_mfma_f32_16x16x32_bf16 v[96:99], a[152:155], v[52:55], v[96:99]\n" \
  "s_cmp_lt_u32 s94, s55\n" \
  "s_cselect_b32 s94, s94, -1\n" \
  "s_waitcnt vmcnt(16)\n" \
  "v_cmp_ne_u32 vcc, %14, v189\n" \
  "v_mfma_f32_16x16x32_bf16 v[100:103], a[156:159], v[52:55], v[100:103]\n" \
  "v_add_f32 v144, v144, v152\n" \
  "v_add_f32 v145, v145, v153\n" \
  "v_mfma_f32_16x16x32_bf16 v[104:107], a[160:163], v[52:55], v[104:107]\n" \
  "v_add_f32 v146, v146, v154\n" \
  "v_add_f32 v147, v147, v155\n" \
  "v_mfma_f32_16x16x32_bf16 v[108:111], a[164:167], v[52:55], v[108:111]\n" \
  "buffer_load_dwordx4 v[52:55], v190, s[40:43], s81 offen offset:1024 sc1\n" \
  "v_mfma_f32_16x16x32_bf16 v[88:91], a[168:171], v[60:63], v[88:91]\n" \
  "v_cvt_pk_bf16_f32 v116, v144, v145\n" \
  "v_cvt_pk_bf16_f32 v117, v146, v147\n" \
  "v_mfma_f32_16x16x32_bf16 v[92:95], a[172:175], v[60:63], v[92:95]\n" \
  "buffer_store_dwordx2 v[116:117], v198, s[40:43], s53 offen offset:1024 sc1\n" \
  "v_mfma_f32_16x16x32_bf16 v[96:99], a[176:179], v[60:63], v[96:99]\n" \
  "s_lshr_b32 s53, s50, 8\n" \
  "s_and_b32 s53, s53, 8192\n" \
  "v_cndmask_b32 v188, v188, 0, vcc\n" \
  "v_mfma_f32_16x16x32_bf16 v[100:103], a[180:183], v[60:63], v[100:103]\n" \
  "s_xor_b32 s53, s53, s50\n" \
  "s_add_u32 s50, s50, 393216\n" \
  "v_mfma_f32_16x16x32_bf16 v[104:107], a[184:187], v[60:63], v[104:107]\n" \
  "s_cmp_eq_u32 s54, 0\n" \
  "s_cselect_b32 s60, s48, 99\n" \
  "s_cmp_lt_u32 s60, s55\n" \
  "v_xor_b32 v192, 65536, v192\n" \
  "v_cmp_ge_i32 vcc, s94, v188\n" \
  "v_mfma_f32_16x16x32_bf16 v[108:111], a[188:191], v[60:63], v[108:111]\n" \
  "buffer_load_dwordx4 v[60:63], v190, s[40:43], s81 offen offset:3072 sc1\n" \
  "s_cbranch_scc1 .LO_loop_%=\n" \
  "s_cmp_lg_u32 s54, 0\n" \
  "s_cbranch_scc1 .LO_ft_%=\n" \
  ".LO_fl_%=:\n" \
  "s_nop 7\n" \
  "ds_write_b128 v191, v[88:91] offset:6144\n" \
  "ds_write_b128 v191, v[92:95] offset:7168\n" \
  "ds_write_b128 v191, v[96:99] offset:8192\n" \
  "ds_write_b128 v191, v[100:103] offset:9216\n" \
  "ds_write_b128 v191, v[104:107] offset:10240\n" \
  "ds_write_b128 v191, v[108:111] offset:11264\n" \
  "s_waitcnt lgkmcnt(0)\n" \
  "s_barrier\n" \
  "ds_read_b128 v[120:123], v192\n" \
  "ds_read_b128 v[112:115], v192 offset:12288\n" \
  "ds_read_b128 v[116:119], v192 offset:24576\n" \
  "ds_read_b128 v[124:127], v192 offset:36864\n" \
  "ds_read_b128 v[128:131], v192 offset:1024\n" \
  "ds_read_b128 v[132:135], v192 offset:13312\n" \
  "ds_read_b128 v[136:139], v192 offset:25600\n" \
  "ds_read_b128 v[140:143], v192 offset:37888\n" \
  "ds_read_b128 v[144:147], v192 offset:2048\n" \
  "ds_read_b128 v[148:151], v192 offset:14336\n" \
  "ds_read_b128 v[152:155], v192 offset:26624\n" \
  "ds_read_b128 v[156:159], v192 offset:38912\n" \
  "s_waitcnt lgkmcnt(10)\n" \
  "v_add_f32 v120, v120, v112\n" \
  "v_add_f32 v121, v121, v113\n" \
  "v_add_f32 v122, v122, v114\n" \
  "v_add_f32 v123, v123, v115\n" \
  "s_waitcnt lgkmcnt(8)\n" \
  "v_add_f32 v116, v116, v124\n" \
  "v_add_f32 v117, v117, v125\n" \
  "v_add_f32 v118, v118, v126\n" \
  "v_add_f32 v119, v119, v127\n" \
  "v_add_f32 v120, v120, v116\n" \
  "v_add_f32 v121, v121, v117\n" \
  "v_add_f32 v122, v122, v118\n" \
  "v_add_f32 v123, v123, v119\n" \
  "v_cvt_pk_bf16_f32 v112, v120, v121\n" \
  "v_cvt_pk_bf16_f32 v113, v122, v123\n" \
  "s_waitcnt lgkmcnt(6)\n" \
  "v_add_f32 v128, v128, v132\n" \
  "v_add_f32 v129, v129, v133\n" \
  "v_add_f32 v130, v130, v134\n" \
  "v_add_f32 v131, v131, v135\n" \
  "s_waitcnt lgkmcnt(4)\n" \
  "v_add_f32 v136, v136, v140\n" \
  "v_add_f32 v137, v137, v141\n" \
  "v_add_f32 v138, v138, v142\n" \
  "v_add_f32 v139, v139, v143\n" \
  "v_add_f32 v128, v128, v136\n" \
  "v_add_f32 v129, v129, v137\n" \
  "v_add_f32 v130, v130, v138\n" \
  "v_add_f32 v131, v131, v139\n" \
  "v_cvt_pk_bf16_f32 v114, v128, v129\n" \
  "v_cvt_pk_bf16_f32 v115, v130, v131\n" \
  "buffer_store_dwordx4 v[112:115], v190, s[40:43], s53 offen sc1\n" \
  "s_waitcnt lgkmcnt(2)\n" \
  "v_add_f32 v144, v144, v148\n" \
  "v_add_f32 v145, v145, v149\n" \
  "v_add_f32 v146, v146, v150\n" \
  "v_add_f32 v147, v147, v151\n" \
  "s_waitcnt lgkmcnt(0)\n" \
  "v_add_f32 v152, v152, v156\n" \
  "v_add_f32 v153, v153, v157\n" \
  "v_add_f32 v154, v154, v158\n" \
  "v_add_f32 v155, v155, v159\n" \
  "v_add_f32 v144, v144, v152\n" \
  "v_add_f32 v145, v145, v153\n" \
  "v_add_f32 v146, v146, v154\n" \
  "v_add_f32 v147, v147, v155\n" \
  "v_cvt_pk_bf16_f32 v116, v144, v145\n" \
  "v_cvt_pk_bf16_f32 v117, v146, v147\n" \
  "buffer_store_dwordx2 v[116:117], v198, s[40:43], s53 offen offset:1024 sc1\n" \
  "s_waitcnt vmcnt(0)\n" \
  "s_barrier\n" \
  "s_add_i32 s60, s48, 0\n" \
  "v_mov_b32 v186, s60\n" \
  "buffer_store_dwordx2 v[186:187], v197, s[40:43], %18 offen sc1\n" \
  "s_cmp_lg_u32 s56, 0\n" \
  "s_cbranch_scc1 .LO_fl_b_%=\n" \
  "s_branch .LO_end_%=\n" \
  ".LO_pl_%=:\n" \
  "s_add_u32 s60, s48, 2\n" \
  "s_cmp_lg_u32 s60, s55\n" \
  "s_cbranch_scc1 .LO_pw_%=\n" \
  "s_mov_b32 s54, s76\n" \
  "s_mov_b32 s76, 2130706432\n" \
  "s_mov_b32 s77, 2130706432\n" \
  "s_mov_b32 s78, 2130706432\n" \
  "s_mov_b32 s79, 2130706432\n" \
  "s_branch .LO_pl_b_%=\n" \
  ".LO_ft_%=:\n" \
  "s_mov_b32 s94, s48\n" \
  ".LO_pw_%=:\n" \
  "s_mov_b32 s69, 0\n" \
  ".LO_p1_%=:\n" \
  "buffer_load_dwordx2 v[188:189], v193, s[40:43], 0 offen sc1\n" \
  "s_waitcnt vmcnt(0)\n" \
  "v_cmp_ne_u32 vcc, %14, v189\n" \
  "s_nop 1\n" \
  "v_cndmask_b32 v188, v188, 0, vcc\n" \
  "v_cmp_ge_i32 vcc, s94, v188\n" \
  "s_cbranch_vccz .LO_p1_x_%=\n" \
  "s_sleep 2\n" \
  "s_add_u32 s69, s69, 1\n" \
  "s_and_b32 s63, s69, 1023\n" \
  "s_cbranch_scc1 .LO_p1_%=\n" \
  "buffer_load_dwordx2 v[188:189], off, s[40:43], %19 offset:128 sc1\n" \
  "s_waitcnt vmcnt(0)\n" \
  "v_readfirstlane_b32 s63, v189\n" \
  "s_cmp_eq_u32 s63, %14\n" \
  "s_cbranch_scc1 .LO_p1_x_%=\n" \
  "s_cmp_le_u32 s69, 2097152\n" \
  "s_cbranch_scc1 .LO_p1_%=\n" \
  "v_mov_b32 v188, 1\n" \
  "v_mov_b32 v189, %14\n" \
  "buffer_store_dwordx2 v[188:189], off, s[40:43], %19 offset:128 sc1\n" \
  "s_waitcnt vmcnt(0)\n" \
  ".LO_p1_x_%=:\n" \
  "s_cmp_eq_u32 s54, 0\n" \
  "s_cbranch_scc1 .LO_pl_b_%=\n" \
  "s_mov_b32 s76, s54\n" \
  "s_mov_b32 s54, 0\n" \
  "s_add_u32 s77, s76, 4096\n" \
  "s_add_u32 s78, s76, 16384\n" \
  "s_add_u32 s79, s76, 20480\n" \
  "buffer_load_dwordx4 v[0:3], v190, s[40:43], s76 offen sc1\n" \
  "buffer_load_dwordx4 v[8:11], v190, s[40:43], s76 offen offset:2048 sc1\n" \
  "buffer_load_dwordx4 v[16:19], v190, s[40:43], s77 offen sc1\n" \
  "buffer_load_dwordx4 v[24:27], v190, s[40:43], s77 offen offset:2048 sc1\n" \
  "buffer_load_dwordx4 v[32:35], v190, s[40:43], s78 offen sc1\n" \
  "buffer_load_dwordx4 v[40:43], v190, s[40:43], s78 offen offset:2048 sc1\n" \
  "buffer_load_dwordx4 v[48:51], v190, s[40:43], s79 offen sc1\n" \
  "buffer_load_dwordx4 v[56:59], v190, s[40:43], s79 offen offset:2048 sc1\n" \
  "buffer_load_dwordx4 v[4:7], v190, s[40:43], s76 offen offset:1024 sc1\n" \
  "buffer_load_dwordx4 v[12:15], v190, s[40:43], s76 offen offset:3072 sc1\n" \
  "buffer_load_dwordx4 v[20:23], v190, s[40:43], s77 offen offset:1024 sc1\n" \
  "buffer_load_dwordx4 v[28:31], v190, s[40:43], s77 offen offset:3072 sc1\n" \
  "buffer_load_dwordx4 v[36:39], v190, s[40:43], s78 offen offset:1024 sc1\n" \
  "buffer_load_dwordx4 v[44:47], v190, s[40:43], s78 offen offset:3072 sc1\n" \
  "buffer_load_dwordx4 v[52:55], v190, s[40:43], s79 offen offset:1024 sc1\n" \
  "buffer_load_dwordx4 v[60:63], v190, s[40:43], s79 offen offset:3072 sc1\n" \
  "s_waitcnt vmcnt(8)\n" \
  "s_branch .LO_pl_b_%=\n" \
  ".LO_end_%=:\n"
#define O_ASM_VOPS "+{v190}"(oi.l16), "+{v191}"(oi.rw), "+{v192}"(oi.rr), "+{v193}"(oi.fo), "+{v194}"(oi.wv0), "+{v195}"(oi.wv1), "+{v196}"(oi.wv2), "+{v197}"(oi.tro), "+{v198}"(oi.l8), "+{v199}"(oi.rw0), "+{v200}"(oi.rr0)
#define O_ASM_SOPS "s"(oi.ws), "s"(oi.Wo), "s"(oi.w), "s"(oi.tag), "s"(oi.o_w), "s"(oi.o_o), "s"(oi.o_a), "s"(oi.f_a), "s"(oi.o_ctl), "s"(oi.ns)
#define O_ASM_CLOB "v0", "v1", "v2", "v3", "v4", "v5", "v6", "v7", "v8", "v9", "v10", "v11", "v12", "v13", "v14", "v15", "v16", "v17", "v18", "v19", "v20", "v21", "v22", "v23", "v24", "v25", "v26", "v27", "v28", "v29", "v30", "v31", "v32", "v33", "v34", "v35", "v36", "v37", "v38", "v39", "v40", "v41", "v42", "v43", "v44", "v45", "v46", "v47", "v48", "v49", "v50", "v51", "v52", "v53", "v54", "v55", "v56", "v57", "v58", "v59", "v60", "v61", "v62", "v63", "v64", "v65", "v66", "v67", "v68", "v69", "v70", "v71", "v72", "v73", "v74", "v75", "v76", "v77", "v78", "v79", "v80", "v81", "v82", "v83", "v84", "v85", "v86", "v87", "v88", "v89", "v90", "v91", "v92", "v93", "v94", "v95", "v96", "v97", "v98", "v99", "v100", "v101", "v102", "v103", "v104", "v105", "v106", "v107", "v108", "v109", "v110", "v111", "v112", "v113", "v114", "v115", "v116", "v117", "v118", "v119", "v120", "v121", "v122", "v123", "v124", "v125", "v126", "v127", "v128", "v129", "v130", "v131", "v132", "v133", "v134", "v135", "v136", "v137", "v138", "v139", "v140", "v141", "v142", "v143", "v144", "v145", "v146", "v147", "v148", "v149", "v150", "v151", "v152", "v153", "v154", "v155", "v156", "v157", "v158", "v159", "v160", "v161", "v162", "v163", "v164", "v165", "v166", "v167", "v168", "v169", "v170", "v171", "v172", "v173", "v174", "v175", "v176", "v177", "v178", "v179", "v180", "v181", "v182", "v183", "v184", "v185", "v186", "v187", "v188", "v189", "a0", "a1", "a2", "a3", "a4", "a5", "a6", "a7", "a8", "a9", "a10", "a11", "a12", "a13", "a14", "a15", "a16", "a17", "a18", "a19", "a20", "a21", "a22", "a23", "a24", "a25", "a26", "a27", "a28", "a29", "a30", "a31", "a32", "a33", "a34", "a35", "a36", "a37", "a38", "a39", "a40", "a41", "a42", "a43", "a44", "a45", "a46", "a47", "a48", "a49", "a50", "a51", "a52", "a53", "a54", "a55", "a56", "a57", "a58", "a59", "a60", "a61", "a62", "a63", "a64", "a65", "a66", "a67", "a68", "a69", "a70", "a71", "a72", "a73", "a74", "a75", "a76", "a77", "a78", "a79", "a80", "a81", "a82", "a83", "a84", "a85", "a86", "a87", "a88", "a89", "a90", "a91", "a92", "a93", "a94", "a95", "a96", "a97", "a98", "a99", "a100", "a101", "a102", "a103", "a104", "a105", "a106", "a107", "a108", "a109", "a110", "a111", "a112", "a113", "a114", "a115", "a116", "a117", "a118", "a119", "a120", "a121", "a122", "a123", "a124", "a125", "a126", "a127", "a128", "a129", "a130", "a131", "a132", "a133", "a134", "a135", "a136", "a137", "a138", "a139", "a140", "a141", "a142", "a143", "a144", "a145", "a146", "a147", "a148", "a149", "a150", "a151", "a152", "a153", "a154", "a155", "a156", "a157", "a158", "a159", "a160", "a161", "a162", "a163", "a164", "a165", "a166", "a167", "a168", "a169", "a170", "a171", "a172", "a173", "a174", "a175", "a176", "a177", "a178", "a179", "a180", "a181", "a182", "a183", "a184", "a185", "a186", "a187", "a188", "a189", "a190", "a191", "s40", "s41", "s42", "s43", "s44", "s45", "s46", "s47", "s48", "s49", "s50", "s51", "s52", "s53", "s54", "s55", "s56", "s57", "s58", "s59", "s60", "s61", "s62", "s63", "s64", "s65", "s66", "s67", "s68", "s69", "s70", "s71", "s72", "s73", "s74", "s75", "s76", "s77", "s78", "s79", "s80", "s81", "s82", "s83", "s84", "s85", "s86", "s87", "s88", "s89", "s90", "s91", "s92", "s93", "s94", "s95", "s96", "s97", "s98", "s99", "vcc", "scc", "memory"
#define F_ASM_TEXT \
  "s_mov_b64 s[40:41], %30\n" \
  "s_and_b32 s41, s41, 0xffff\n" \
  "s_mov_b32 s42, 255852544\n" \
  "s_mov_b32 s43, 0x00020000\n" \
  "s_mov_b64 s[44:45], %31\n" \
  "s_and_b32 s45, s45, 0xffff\n" \
  "s_mov_b32 s46, 2359296\n" \
  "s_mov_b32 s47, 0x00020000\n" \
  "s_mov_b32 s48, 0\n" \
  "s_mov_b32 s58, 0\n" \
  "s_mov_b32 s59, 0\n" \
  "s_mov_b32 s49, %36\n" \
  "s_add_u32 s53, s48, 59\n" \
  "s_min_u32 s53, s53, 63\n" \
  "s_and_b32 s63, s53, 3\n" \
  "s_lshl_b32 s50, s53, 5\n" \
  "s_lshl_b32 s63, s63, 4\n" \
  "s_sub_u32 s50, s50, s63\n" \
  "s_add_u32 s50, s50, %37\n" \
  "s_mov_b32 s51, %35\n" \
  "s_mov_b32 s65, 0\n" \
  "s_mov_b32 s64, %41\n" \
  "v_not_b32 v197, %32\n" \
  ".LF_pre_%=:\n" \
  "v_readfirstlane_b32 s60, v197\n" \
  "s_cmp_eq_u32 s60, %32\n" \
  "s_cbranch_scc1 .LF_fu_x_%=\n" \
  ".LF_fu_%=:\n" \
  "buffer_load_dwordx2 v[196:197], off, s[40:43], s64 sc1\n" \
  "buffer_load_dwordx4 v[60:63], off, s[40:43], s50 sc1\n" \
  "s_memrealtime s[76:77]\n" \
  "s_waitcnt vmcnt(0) lgkmcnt(0)\n" \
  "v_readfirstlane_b32 s60, v197\n" \
  "s_cmp_eq_u32 s60, %32\n" \
  "s_cbranch_scc1 .LF_fu_x_%=\n" \
  "v_readfirstlane_b32 s62, v60\n" \
  "v_readfirstlane_b32 s61, v62\n" \
  "v_readfirstlane_b32 s60, v63\n" \
  "s_cmp_eq_u32 s62, s53\n" \
  "s_cselect_b32 s60, s60, 0\n" \
  "s_max_u32 s65, s65, s60\n" \
  "s_cmp_eq_u32 s48, 5\n" \
  "s_cselect_b32 s54, 16, 0\n" \
  "s_lshr_b32 s54, %39, s54\n" \
  "s_and_b32 s54, s54, 0x7fff\n" \
  "s_add_u32 s55, s61, s54\n" \
  "s_add_u32 s55, s55, s65\n" \
  "s_cmp_eq_u32 s48, 4\n" \
  "s_cselect_b32 s54, 20, 0\n" \
  "s_sub_u32 s55, s55, s54\n" \
  "s_sub_u32 s60, s55, s76\n" \
  "s_cmp_eq_u32 s62, s53\n" \
  "s_cselect_b32 s60, s60, 999\n" \
  "s_cmp_lt_u32 s48, s58\n" \
  "s_cselect_b32 s60, s60, 999\n" \
  "s_cmp_lt_i32 s60, 60\n" \
  "s_cbranch_scc1 .LF_loop_%=\n" \
  "s_sleep 6\n" \
  "s_add_u32 s59, s59, 1\n" \
  "s_cmp_lt_u32 s59, 131072\n" \
  "s_cbranch_scc1 .LF_fu_%=\n" \
  "v_mov_b32 v32, 1\n" \
  "v_mov_b32 v33, %32\n" \
  "buffer_store_dwordx2 v[32:33], off, s[40:43], %38 offset:128 sc1\n" \
  "s_waitcnt vmcnt(0)\n" \
  ".LF_fu_x_%=:\n" \
  "s_bitcmp1_b32 %39, 15\n" \
  "s_cbranch_scc1 .LF_fl_%=\n" \
  "s_add_u32 s61, s58, 58\n" \
  "s_and_b32 s61, s61, 15\n" \
  "s_lshl_b32 s61, s61, 16\n" \
  "s_xor_b32 s61, s61, %33\n" \
  "s_lshl_b32 s62, s58, 17\n" \
  "s_add_u32 s62, s62, %34\n" \
  "s_xor_b32 s60, s61, 0\n" \
  "buffer_load_dwordx4 v[32:35], v202, s[40:43], s60 offen sc1\n" \
  "s_sleep 1\n" \
  "buffer_load_dwordx4 v[64:67], v203, s[40:43], s62 offen sc1\n" \
  "s_sleep 1\n" \
  "buffer_load_dwordx4 v[68:71], v203, s[40:43], s62 offen offset:1024 sc1\n" \
  "s_sleep 1\n" \
  "s_xor_b32 s60, s61, 1024\n" \
  "buffer_load_dwordx4 v[36:39], v202, s[40:43], s60 offen sc1\n" \
  "s_sleep 1\n" \
  "buffer_load_dwordx4 v[72:75], v203, s[40:43], s62 offen offset:2048 sc1\n" \
  "s_sleep 1\n" \
  "buffer_load_dwordx4 v[76:79], v203, s[40:43], s62 offen offset:3072 sc1\n" \
  "s_sleep 1\n" \
  "s_xor_b32 s60, s61, 2048\n" \
  "buffer_load_dwordx4 v[40:43], v202, s[40:43], s60 offen sc1\n" \
  "s_sleep 1\n" \
  "buffer_load_dwordx4 v[80:83], v204, s[40:43], s62 offen sc1\n" \
  "s_sleep 1\n" \
  "buffer_load_dwordx4 v[84:87], v204, s[40:43], s62 offen offset:1024 sc1\n" \
  "s_sleep 1\n" \
  "s_xor_b32 s60, s61, 3072\n" \
  "buffer_load_dwordx4 v[44:47], v202, s[40:43], s60 offen sc1\n" \
  "s_sleep 1\n" \
  "buffer_load_dwordx4 v[88:91], v204, s[40:43], s62 offen offset:2048 sc1\n" \
  "s_sleep 1\n" \
  "buffer_load_dwordx4 v[92:95], v204, s[40:43], s62 offen offset:3072 sc1\n" \
  "s_sleep 1\n" \
  "s_xor_b32 s60, s61, 4096\n" \
  "buffer_load_dwordx4 v[48:51], v202, s[40:43], s60 offen sc1\n" \
  "s_sleep 1\n" \
  "buffer_load_dwordx4 v[96:99], v205, s[40:43], s62 offen sc1\n" \
  "s_sleep 1\n" \
  "buffer_load_dwordx4 v[100:103], v205, s[40:43], s62 offen offset:1024 sc1\n" \
  "s_sleep 1\n" \
  "s_xor_b32 s60, s61, 5120\n" \
  "buffer_load_dwordx4 v[52:55], v202, s[40:43], s60 offen sc1\n" \
  "s_sleep 1\n" \
  "buffer_load_dwordx4 v[104:107], v205, s[40:43], s62 offen offset:2048 sc1\n" \
  "s_sleep 1\n" \
  "buffer_load_dwordx4 v[108:111], v205, s[40:43], s62 offen offset:3072 sc1\n" \
  "s_sleep 1\n" \
  "s_xor_b32 s60, s61, 6144\n" \
  "buffer_load_dwordx4 v[56:59], v202, s[40:43], s60 offen sc1\n" \
  "s_sleep 1\n" \
  "buffer_load_dwordx4 v[112:115], v206, s[40:43], s62 offen sc1\n" \
  "s_sleep 1\n" \
  "buffer_load_dwordx4 v[116:119], v206, s[40:43], s62 offen offset:1024 sc1\n" \
  "s_sleep 1\n" \
  "s_xor_b32 s60, s61, 7168\n" \
  "buffer_load_dwordx4 v[60:63], v202, s[40:43], s60 offen sc1\n" \
  "s_sleep 1\n" \
  "buffer_load_dwordx4 v[120:123], v206, s[40:43], s62 offen offset:2048 sc1\n" \
  "s_sleep 1\n" \
  "buffer_load_dwordx4 v[124:127], v206, s[40:43], s62 offen offset:3072 sc1\n" \
  "s_sleep 1\n" \
  "s_add_u32 s64, s64, 128\n" \
  ".LF_fl_b_%=:\n" \
  "v_mov_b64 v[128:129], 0\n" \
  "v_mov_b64 v[130:131], 0\n" \
  "v_mov_b64 v[132:133], 0\n" \
  "v_mov_b64 v[134:135], 0\n" \
  "v_mov_b64 v[136:137], 0\n" \
  "v_mov_b64 v[138:139], 0\n" \
  "v_mov_b64 v[140:141], 0\n" \
  "v_mov_b64 v[142:143], 0\n" \
  "v_mov_b64 v[144:145], 0\n" \
  "v_mov_b64 v[146:147], 0\n" \
  "v_mov_b64 v[148:149], 0\n" \
  "v_mov_b64 v[150:151], 0\n" \
  "v_mov_b64 v[152:153], 0\n" \
  "v_mov_b64 v[154:155], 0\n" \
  "v_mov_b64 v[156:157], 0\n" \
  "v_mov_b64 v[158:159], 0\n" \
  "v_mov_b64 v[160:161], 0\n" \
  "v_mov_b64 v[162:163], 0\n" \
  "v_mov_b64 v[164:165], 0\n" \
  "v_mov_b64 v[166:167], 0\n" \
  "v_mov_b64 v[168:169], 0\n" \
  "v_mov_b64 v[170:171], 0\n" \
  "v_mov_b64 v[172:173], 0\n" \
  "v_mov_b64 v[174:175], 0\n" \
  "v_mov_b64 v[176:177], 0\n" \
  "v_mov_b64 v[178:179], 0\n" \
  "v_mov_b64 v[180:181], 0\n" \
  "v_mov_b64 v[182:183], 0\n" \
  "v_mov_b64 v[184:185], 0\n" \
  "v_mov_b64 v[186:187], 0\n" \
  "v_mov_b64 v[188:189], 0\n" \
  "v_mov_b64 v[190:191], 0\n" \
  "s_waitcnt vmcnt(0)\n" \
  "v_mov_b32 v0, v32\n" \
  "v_mov_b32 v1, v36\n" \
  "v_mov_b32 v2, v33\n" \
  "v_mov_b32 v3, v37\n" \
  "v_mov_b32 v4, v34\n" \
  "v_mov_b32 v5, v38\n" \
  "v_mov_b32 v6, v35\n" \
  "v_mov_b32 v7, v39\n" \
  "v_mov_b32 v8, v40\n" \
  "v_mov_b32 v9, v44\n" \
  "v_mov_b32 v10, v41\n" \
  "v_mov_b32 v11, v45\n" \
  "v_mov_b32 v12, v42\n" \
  "v_mov_b32 v13, v46\n" \
  "v_mov_b32 v14, v43\n" \
  "v_mov_b32 v15, v47\n" \
  "v_mov_b32 v16, v48\n" \
  "v_mov_b32 v17, v52\n" \
  "v_mov_b32 v18, v49\n" \
  "v_mov_b32 v19, v53\n" \
  "v_mov_b32 v20, v50\n" \
  "v_mov_b32 v21, v54\n" \
  "v_mov_b32 v22, v51\n" \
  "v_mov_b32 v23, v55\n" \
  "v_mov_b32 v24, v56\n" \
  "v_mov_b32 v25, v60\n" \
  "v_mov_b32 v26, v57\n" \
  "v_mov_b32 v27, v61\n" \
  "v_mov_b32 v28, v58\n" \
  "v_mov_b32 v29, v62\n" \
  "v_mov_b32 v30, v59\n" \
  "v_mov_b32 v31, v63\n" \
  "s_nop 1\n" \
  "v_smfmac_f32_16x16x64_bf16 v[128:131], v[64:67], v[0:7], v200\n" \
  "v_smfmac_f32_16x16x64_bf16 v[132:135], v[68:71], v[8:15], v200\n" \
  "v_smfmac_f32_16x16x64_bf16 v[136:139], v[72:75], v[16:23], v200\n" \
  "v_smfmac_f32_16x16x64_bf16 v[140:143], v[76:79], v[24:31], v200\n" \
  "v_smfmac_f32_16x16x64_bf16 v[144:147], v[80:83], v[0:7], v200\n" \
  "v_smfmac_f32_16x16x64_bf16 v[148:151], v[84:87], v[8:15], v200\n" \
  "v_smfmac_f32_16x16x64_bf16 v[152:155], v[88:91], v[16:23], v200\n" \
  "v_smfmac_f32_16x16x64_bf16 v[156:159], v[92:95], v[24:31], v200\n" \
  "v_smfmac_f32_16x16x64_bf16 v[160:163], v[96:99], v[0:7], v200\n" \
  "v_smfmac_f32_16x16x64_bf16 v[164:167], v[100:103], v[8:15], v200\n" \
  "v_smfmac_f32_16x16x64_bf16 v[168:171], v[104:107], v[16:23], v200\n" \
  "v_smfmac_f32_16x16x64_bf16 v[172:175], v[108:111], v[24:31], v200\n" \
  "v_smfmac_f32_16x16x64_bf16 v[176:179], v[112:115], v[0:7], v200\n" \
  "v_smfmac_f32_16x16x64_bf16 v[180:183], v[116:119], v[8:15], v200\n" \
  "v_smfmac_f32_16x16x64_bf16 v[184:187], v[120:123], v[16:23], v200\n" \
  "v_smfmac_f32_16x16x64_bf16 v[188:191], v[124:127], v[24:31], v200\n" \
  "s_add_u32 s58, s58, 1\n" \
  "buffer_load_dwordx2 v[196:197], off, s[40:43], s64 sc1\n" \
  "s_cmp_eq_u32 s58, 6\n" \
  "s_cbranch_scc1 .LF_ne_%=\n" \
  ".LF_ne_%=:\n" \
  "s_nop 7\n" \
  "v_cvt_pk_bf16_f32 v128, v128, v129\n" \
  "v_cvt_pk_bf16_f32 v129, v130, v131\n" \
  "v_cvt_pk_bf16_f32 v130, v132, v133\n" \
  "v_cvt_pk_bf16_f32 v131, v134, v135\n" \
  "v_cvt_pk_bf16_f32 v132, v136, v137\n" \
  "v_cvt_pk_bf16_f32 v133, v138, v139\n" \
  "v_cvt_pk_bf16_f32 v134, v140, v141\n" \
  "v_cvt_pk_bf16_f32 v135, v142, v143\n" \
  "v_cvt_pk_bf16_f32 v136, v144, v145\n" \
  "v_cvt_pk_bf16_f32 v137, v146, v147\n" \
  "v_cvt_pk_bf16_f32 v138, v148, v149\n" \
  "v_cvt_pk_bf16_f32 v139, v150, v151\n" \
  "v_cvt_pk_bf16_f32 v140, v152, v153\n" \
  "v_cvt_pk_bf16_f32 v141, v154, v155\n" \
  "v_cvt_pk_bf16_f32 v142, v156, v157\n" \
  "v_cvt_pk_bf16_f32 v143, v158, v159\n" \
  "v_cvt_pk_bf16_f32 v144, v160, v161\n" \
  "v_cvt_pk_bf16_f32 v145, v162, v163\n" \
  "v_cvt_pk_bf16_f32 v146, v164, v165\n" \
  "v_cvt_pk_bf16_f32 v147, v166, v167\n" \
  "v_cvt_pk_bf16_f32 v148, v168, v169\n" \
  "v_cvt_pk_bf16_f32 v149, v170, v171\n" \
  "v_cvt_pk_bf16_f32 v150, v172, v173\n" \
  "v_cvt_pk_bf16_f32 v151, v174, v175\n" \
  "v_cvt_pk_bf16_f32 v152, v176, v177\n" \
  "v_cvt_pk_bf16_f32 v153, v178, v179\n" \
  "v_cvt_pk_bf16_f32 v154, v180, v181\n" \
  "v_cvt_pk_bf16_f32 v155, v182, v183\n" \
  "v_cvt_pk_bf16_f32 v156, v184, v185\n" \
  "v_cvt_pk_bf16_f32 v157, v186, v187\n" \
  "v_cvt_pk_bf16_f32 v158, v188, v189\n" \
  "v_cvt_pk_bf16_f32 v159, v190, v191\n" \
  "s_cmp_eq_u32 s58, 1\n" \
  "s_cbranch_scc1 .LF_w1_%=\n" \
  "s_cmp_eq_u32 s58, 2\n" \
  "s_cbranch_scc1 .LF_w2_%=\n" \
  "s_cmp_eq_u32 s58, 3\n" \
  "s_cbranch_scc1 .LF_w3_%=\n" \
  "s_cmp_eq_u32 s58, 4\n" \
  "s_cbranch_scc1 .LF_w4_%=\n" \
  "s_cmp_eq_u32 s58, 5\n" \
  "s_cbranch_scc1 .LF_w5_%=\n" \
  "v_accvgpr_write_b32 a160, v128\n" \
  "v_accvgpr_write_b32 a161, v129\n" \
  "v_accvgpr_write_b32 a162, v130\n" \
  "v_accvgpr_write_b32 a163, v131\n" \
  "v_accvgpr_write_b32 a164, v132\n" \
  "v_accvgpr_write_b32 a165, v133\n" \
  "v_accvgpr_write_b32 a166, v134\n" \
  "v_accvgpr_write_b32 a167, v135\n" \
  "v_accvgpr_write_b32 a168, v136\n" \
  "v_accvgpr_write_b32 a169, v137\n" \
  "v_accvgpr_write_b32 a170, v138\n" \
  "v_accvgpr_write_b32 a171, v139\n" \
  "v_accvgpr_write_b32 a172, v140\n" \
  "v_accvgpr_write_b32 a173, v141\n" \
  "v_accvgpr_write_b32 a174, v142\n" \
  "v_accvgpr_write_b32 a175, v143\n" \
  "v_accvgpr_write_b32 a176, v144\n" \
  "v_accvgpr_write_b32 a177, v145\n" \
  "v_accvgpr_write_b32 a178, v146\n" \
  "v_accvgpr_write_b32 a179, v147\n" \
  "v_accvgpr_write_b32 a180, v148\n" \
  "v_accvgpr_write_b32 a181, v149\n" \
  "v_accvgpr_write_b32 a182, v150\n" \
  "v_accvgpr_write_b32 a183, v151\n" \
  "v_accvgpr_write_b32 a184, v152\n" \
  "v_accvgpr_write_b32 a185, v153\n" \
  "v_accvgpr_write_b32 a186, v154\n" \
  "v_accvgpr_write_b32 a187, v155\n" \
  "v_accvgpr_write_b32 a188, v156\n" \
  "v_accvgpr_write_b32 a189, v157\n" \
  "v_accvgpr_write_b32 a190, v158\n" \
  "v_accvgpr_write_b32 a191, v159\n" \
  ".LF_w_b_%=:\n" \
  "s_cmp_lt_u32 s58, 6\n" \
  "s_waitcnt vmcnt(0)\n" \
  "s_cbranch_scc1 .LF_pre_%=\n" \
  "s_waitcnt vmcnt(0)\n" \
  ".LF_loop_%=:\n" \
  "v_mov_b64 v[40:41], 0\n" \
  "v_mov_b64 v[42:43], 0\n" \
  "v_mov_b64 v[44:45], 0\n" \
  "v_mov_b64 v[46:47], 0\n" \
  "v_mov_b64 v[48:49], 0\n" \
  "v_mov_b64 v[50:51], 0\n" \
  "v_mov_b64 v[52:53], 0\n" \
  "v_mov_b64 v[54:55], 0\n" \
  "s_cmp_lg_u32 %40, 0\n" \
  "s_cbranch_scc1 .LF_go_%=\n" \
  "s_mov_b32 s69, 0\n" \
  "s_cmp_eq_u32 s48, 5\n" \
  "s_cbranch_scc1 .LF_tw1_%=\n" \
  ".LF_tw_%=:\n" \
  "buffer_load_dwordx4 v[60:63], off, s[40:43], s50 sc1\n" \
  ".LF_tw1_%=:\n" \
  "s_memrealtime s[76:77]\n" \
  "s_waitcnt vmcnt(0) lgkmcnt(0)\n" \
  "v_readfirstlane_b32 s62, v60\n" \
  "v_readfirstlane_b32 s61, v62\n" \
  "v_readfirstlane_b32 s60, v63\n" \
  "s_cmp_eq_u32 s62, s53\n" \
  "s_cselect_b32 s60, s60, 0\n" \
  "s_max_u32 s65, s65, s60\n" \
  "s_cmp_eq_u32 s48, 5\n" \
  "s_cselect_b32 s54, 16, 0\n" \
  "s_lshr_b32 s54, %39, s54\n" \
  "s_and_b32 s54, s54, 0x7fff\n" \
  "s_add_u32 s55, s61, s54\n" \
  "s_add_u32 s55, s55, s65\n" \
  "s_cmp_eq_u32 s48, 4\n" \
  "s_cselect_b32 s54, 20, 0\n" \
  "s_sub_u32 s55, s55, s54\n" \
  "s_sub_u32 s60, s55, s76\n" \
  "s_cmp_eq_u32 s62, s53\n" \
  "s_cselect_b32 s60, s60, 999\n" \
  "s_cmp_lt_i32 s60, 900\n" \
  "s_cbranch_scc1 .LF_clk_%=\n" \
  "s_sleep 8\n" \
  "s_add_u32 s69, s69, 1\n" \
  "s_cmp_lt_u32 s69, 20000\n" \
  "s_cbranch_scc1 .LF_tw_%=\n" \
  "s_branch .LF_go_%=\n" \
  ".LF_clk_%=:\n" \
  "s_cmp_le_i32 s60, 3\n" \
  "s_cbranch_scc1 .LF_go_%=\n" \
  "s_sleep 1\n" \
  "s_cmp_le_i32 s60, 30\n" \
  "s_cbranch_scc1 .LF_cl2_%=\n" \
  "s_sleep 6\n" \
  ".LF_cl2_%=:\n" \
  "s_memrealtime s[76:77]\n" \
  "s_waitcnt lgkmcnt(0)\n" \
  "s_sub_u32 s60, s55, s76\n" \
  "s_branch .LF_clk_%=\n" \
  ".LF_go_%=:\n" \
  "s_barrier\n" \
  "s_mov_b32 s69, 0\n" \
  "s_lshr_b32 s52, s49, 8\n" \
  "s_and_b32 s52, s52, 8192\n" \
  "s_xor_b32 s52, s52, s49\n" \
  ".LF_rd_%=:\n" \
  "buffer_load_dwordx4 v[0:3], v202, s[40:43], s52 offen sc1\n" \
  "buffer_load_dwordx4 v[4:7], v202, s[40:43], s52 offen offset:1024 sc1\n" \
  "s_mov_b32 s60, s51\n" \
  "buffer_load_dwordx4 v[8:11], v207, s[40:43], s60 offen sc1\n" \
  "s_add_u32 s60, s60, 4096\n" \
  "buffer_load_dwordx4 v[24:27], v207, s[40:43], s60 offen sc1\n" \
  "s_add_u32 s60, s60, 12288\n" \
  "buffer_load_dwordx4 v[12:15], v207, s[40:43], s60 offen sc1\n" \
  "s_add_u32 s60, s60, 4096\n" \
  "buffer_load_dwordx4 v[28:31], v207, s[40:43], s60 offen sc1\n" \
  "s_add_u32 s60, s60, 12288\n" \
  "buffer_load_dwordx4 v[16:19], v207, s[40:43], s60 offen sc1\n" \
  "s_add_u32 s60, s60, 4096\n" \
  "buffer_load_dwordx4 v[32:35], v207, s[40:43], s60 offen sc1\n" \
  "s_add_u32 s60, s60, 12288\n" \
  "buffer_load_dwordx4 v[20:23], v207, s[40:43], s60 offen sc1\n" \
  "s_add_u32 s60, s60, 4096\n" \
  "buffer_load_dwordx4 v[36:39], v207, s[40:43], s60 offen sc1\n" \
  "s_cmp_eq_u32 s48, 1\n" \
  "s_cbranch_scc1 .LF_r1_%=\n" \
  "s_cmp_eq_u32 s48, 2\n" \
  "s_cbranch_scc1 .LF_r2_%=\n" \
  "s_cmp_eq_u32 s48, 3\n" \
  "s_cbranch_scc1 .LF_r3_%=\n" \
  "s_cmp_eq_u32 s48, 4\n" \
  "s_cbranch_scc1 .LF_r4_%=\n" \
  "s_cmp_eq_u32 s48, 5\n" \
  "s_cbranch_scc1 .LF_r5_%=\n" \
  "v_mov_b32 v192, v212\n" \
  "v_mov_b32 v193, v213\n" \
  "v_mov_b32 v194, v214\n" \
  "v_accvgpr_read_b32 v128, a0\n" \
  "v_accvgpr_read_b32 v129, a1\n" \
  "v_accvgpr_read_b32 v130, a2\n" \
  "v_accvgpr_read_b32 v131, a3\n" \
  "v_accvgpr_read_b32 v132, a4\n" \
  "v_accvgpr_read_b32 v133, a5\n" \
  "v_accvgpr_read_b32 v134, a6\n" \
  "v_accvgpr_read_b32 v135, a7\n" \
  "v_accvgpr_read_b32 v136, a8\n" \
  "v_accvgpr_read_b32 v137, a9\n" \
  "v_accvgpr_read_b32 v138, a10\n" \
  "v_accvgpr_read_b32 v139, a11\n" \
  "v_accvgpr_read_b32 v140, a12\n" \
  "v_accvgpr_read_b32 v141, a13\n" \
  "v_accvgpr_read_b32 v142, a14\n" \
  "v_accvgpr_read_b32 v143, a15\n" \
  "v_accvgpr_read_b32 v144, a16\n" \
  "v_accvgpr_read_b32 v145, a17\n" \
  "v_accvgpr_read_b32 v146, a18\n" \
  "v_accvgpr_read_b32 v147, a19\n" \
  "v_accvgpr_read_b32 v148, a20\n" \
  "v_accvgpr_read_b32 v149, a21\n" \
  "v_accvgpr_read_b32 v150, a22\n" \
  "v_accvgpr_read_b32 v151, a23\n" \
  "v_accvgpr_read_b32 v152, a24\n" \
  "v_accvgpr_read_b32 v153, a25\n" \
  "v_accvgpr_read_b32 v154, a26\n" \
  "v_accvgpr_read_b32 v155, a27\n" \
  "v_accvgpr_read_b32 v156, a28\n" \
  "v_accvgpr_read_b32 v157, a29\n" \
  "v_accvgpr_read_b32 v158, a30\n" \
  "v_accvgpr_read_b32 v159, a31\n" \
  ".LF_r_b_%=:\n" \
  "s_waitcnt vmcnt(8)\n" \
  "v_permlane16_swap_b32 v0, v1\n" \
  "v_permlane16_swap_b32 v2, v3\n" \
  "v_permlane16_swap_b32 v4, v5\n" \
  "v_permlane16_swap_b32 v6, v7\n" \
  "s_mov_b64 s[78:79], 0\n" \
  "s_waitcnt vmcnt(7)\n" \
  "v_smfmac_f32_16x16x64_bf16 v[40:43], v[8:11], v[0:7], v200\n" \
  "s_waitcnt vmcnt(6)\n" \
  "v_smfmac_f32_16x16x64_bf16 v[40:43], v[24:27], v[128:135], v201\n" \
  "s_waitcnt vmcnt(5)\n" \
  "v_smfmac_f32_16x16x64_bf16 v[44:47], v[12:15], v[0:7], v200\n" \
  "s_waitcnt vmcnt(4)\n" \
  "v_smfmac_f32_16x16x64_bf16 v[44:47], v[28:31], v[136:143], v201\n" \
  "s_waitcnt vmcnt(3)\n" \
  "v_smfmac_f32_16x16x64_bf16 v[48:51], v[16:19], v[0:7], v200\n" \
  "s_waitcnt vmcnt(2)\n" \
  "v_smfmac_f32_16x16x64_bf16 v[48:51], v[32:35], v[144:151], v201\n" \
  "s_waitcnt vmcnt(1)\n" \
  "v_smfmac_f32_16x16x64_bf16 v[52:55], v[20:23], v[0:7], v200\n" \
  "s_waitcnt vmcnt(0)\n" \
  "v_smfmac_f32_16x16x64_bf16 v[52:55], v[36:39], v[152:159], v201\n" \
  "s_nop 1\n" \
  "v_permlane32_swap_b32 v40, v42\n" \
  "v_permlane32_swap_b32 v41, v43\n" \
  "v_add_f32 v40, v40, v42\n" \
  "v_add_f32 v41, v41, v43\n" \
  "v_cmp_u_f32 vcc, v40, v41\n" \
  "ds_write2st64_b32 v208, v40, v41 offset0:0 offset1:4\n" \
  "s_or_b64 s[78:79], s[78:79], vcc\n" \
  "v_permlane32_swap_b32 v44, v46\n" \
  "v_permlane32_swap_b32 v45, v47\n" \
  "v_add_f32 v44, v44, v46\n" \
  "v_add_f32 v45, v45, v47\n" \
  "v_cmp_u_f32 vcc, v44, v45\n" \
  "ds_write2st64_b32 v208, v44, v45 offset0:8 offset1:12\n" \
  "s_or_b64 s[78:79], s[78:79], vcc\n" \
  "v_permlane32_swap_b32 v48, v50\n" \
  "v_permlane32_swap_b32 v49, v51\n" \
  "v_add_f32 v48, v48, v50\n" \
  "v_add_f32 v49, v49, v51\n" \
  "v_cmp_u_f32 vcc, v48, v49\n" \
  "ds_write2st64_b32 v208, v48, v49 offset0:16 offset1:20\n" \
  "s_or_b64 s[78:79], s[78:79], vcc\n" \
  "v_permlane32_swap_b32 v52, v54\n" \
  "v_permlane32_swap_b32 v53, v55\n" \
  "v_add_f32 v52, v52, v54\n" \
  "v_add_f32 v53, v53, v55\n" \
  "v_cmp_u_f32 vcc, v52, v53\n" \
  "ds_write2st64_b32 v208, v52, v53 offset0:24 offset1:28\n" \
  "s_or_b64 s[78:79], s[78:79], vcc\n" \
  "s_cbranch_scc1 .LF_bad_%=\n" \
  ".LF_ok_%=:\n" \
  "s_waitcnt lgkmcnt(0)\n" \
  "s_barrier\n" \
  "ds_read_b128 v[40:43], v209\n" \
  "ds_read_b128 v[44:47], v209 offset:512\n" \
  "s_waitcnt lgkmcnt(1)\n" \
  "v_add_f32 v40, v40, v42\n" \
  "v_add_f32 v41, v41, v43\n" \
  "s_waitcnt lgkmcnt(0)\n" \
  "v_add_f32 v44, v44, v46\n" \
  "v_add_f32 v45, v45, v47\n" \
  "v_add_f32 v40, v40, v44\n" \
  "v_add_f32 v41, v41, v45\n" \
  "v_cvt_pk_bf16_f32 v40, v40, v41\n" \
  "v_and_b32 v41, 0xffff0000, v40\n" \
  "v_lshlrev_b32 v40, 16, v40\n" \
  "v_mul_f32 v40, v192, v40\n" \
  "v_mul_f32 v41, v192, v41\n" \
  "v_cvt_pk_bf16_f32 v40, v40, v41\n" \
  "v_and_b32 v41, 0xffff0000, v40\n" \
  "v_lshlrev_b32 v40, 16, v40\n" \
  "v_add_f32 v40, v40, v193\n" \
  "v_add_f32 v41, v41, v194\n" \
  "v_cvt_pk_bf16_f32 v237, v40, v41\n" \
  "s_cmp_eq_u32 s48, 5\n" \
  "s_cbranch_scc1 .LF_out_%=\n" \
  "v_mov_b32 v232, v233\n" \
  "v_mov_b32 v233, v234\n" \
  "v_mov_b32 v234, v235\n" \
  "v_mov_b32 v235, v236\n" \
  "v_mov_b32 v236, v237\n" \
  "v_xor_b32 v208, 8192, v208\n" \
  "v_xor_b32 v209, 8192, v209\n" \
  "s_add_u32 s49, s49, 1048576\n" \
  "s_add_u32 s51, s51, 65536\n" \
  "s_add_u32 s48, s48, 1\n" \
  "s_add_u32 s53, s48, 59\n" \
  "s_min_u32 s53, s53, 63\n" \
  "s_and_b32 s63, s53, 3\n" \
  "s_lshl_b32 s50, s53, 5\n" \
  "s_lshl_b32 s63, s63, 4\n" \
  "s_sub_u32 s50, s50, s63\n" \
  "s_add_u32 s50, s50, %37\n" \
  "s_cmp_lt_u32 s58, 6\n" \
  "s_cbranch_scc1 .LF_fu_%=\n" \
  "s_branch .LF_loop_%=\n" \
  ".LF_out_%=:\n" \
  "s_mov_b32 s60, 184320\n" \
  "buffer_store_dword v237, v210, s[44:47], s60 offen\n" \
  "s_mov_b32 s60, 147456\n" \
  "buffer_store_dword v236, v210, s[44:47], s60 offen\n" \
  "s_mov_b32 s60, 110592\n" \
  "buffer_store_dword v235, v210, s[44:47], s60 offen\n" \
  "s_mov_b32 s60, 73728\n" \
  "buffer_store_dword v234, v210, s[44:47], s60 offen\n" \
  "s_mov_b32 s60, 36864\n" \
  "buffer_store_dword v233, v210, s[44:47], s60 offen\n" \
  "s_mov_b32 s60, 0\n" \
  "buffer_store_dword v232, v210, s[44:47], s60 offen\n" \
  "s_branch .LF_end_%=\n" \
  ".LF_fl_%=:\n" \
  "s_add_u32 s61, s58, 58\n" \
  "s_and_b32 s61, s61, 15\n" \
  "s_lshl_b32 s61, s61, 16\n" \
  "s_xor_b32 s61, s61, %33\n" \
  "s_lshl_b32 s62, s58, 17\n" \
  "s_add_u32 s62, s62, %34\n" \
  "s_xor_b32 s60, s61, 0\n" \
  "buffer_load_dwordx4 v[32:35], v202, s[40:43], s60 offen sc1\n" \
  "buffer_load_dwordx4 v[64:67], v203, s[40:43], s62 offen sc1\n" \
  "buffer_load_dwordx4 v[68:71], v203, s[40:43], s62 offen offset:1024 sc1\n" \
  "s_xor_b32 s60, s61, 1024\n" \
  "buffer_load_dwordx4 v[36:39], v202, s[40:43], s60 offen sc1\n" \
  "buffer_load_dwordx4 v[72:75], v203, s[40:43], s62 offen offset:2048 sc1\n" \
  "buffer_load_dwordx4 v[76:79], v203, s[40:43], s62 offen offset:3072 sc1\n" \
  "s_xor_b32 s60, s61, 2048\n" \
  "buffer_load_dwordx4 v[40:43], v202, s[40:43], s60 offen sc1\n" \
  "buffer_load_dwordx4 v[80:83], v204, s[40:43], s62 offen sc1\n" \
  "buffer_load_dwordx4 v[84:87], v204, s[40:43], s62 offen offset:1024 sc1\n" \
  "s_xor_b32 s60, s61, 3072\n" \
  "buffer_load_dwordx4 v[44:47], v202, s[40:43], s60 offen sc1\n" \
  "buffer_load_dwordx4 v[88:91], v204, s[40:43], s62 offen offset:2048 sc1\n" \
  "buffer_load_dwordx4 v[92:95], v204, s[40:43], s62 offen offset:3072 sc1\n" \
  "s_xor_b32 s60, s61, 4096\n" \
  "buffer_load_dwordx4 v[48:51], v202, s[40:43], s60 offen sc1\n" \
  "buffer_load_dwordx4 v[96:99], v205, s[40:43], s62 offen sc1\n" \
  "buffer_load_dwordx4 v[100:103], v205, s[40:43], s62 offen offset:1024 sc1\n" \
  "s_xor_b32 s60, s61, 5120\n" \
  "buffer_load_dwordx4 v[52:55], v202, s[40:43], s60 offen sc1\n" \
  "buffer_load_dwordx4 v[104:107], v205, s[40:43], s62 offen offset:2048 sc1\n" \
  "buffer_load_dwordx4 v[108:111], v205, s[40:43], s62 offen offset:3072 sc1\n" \
  "s_xor_b32 s60, s61, 6144\n" \
  "buffer_load_dwordx4 v[56:59], v202, s[40:43], s60 offen sc1\n" \
  "buffer_load_dwordx4 v[112:115], v206, s[40:43], s62 offen sc1\n" \
  "buffer_load_dwordx4 v[116:119], v206, s[40:43], s62 offen offset:1024 sc1\n" \
  "s_xor_b32 s60, s61, 7168\n" \
  "buffer_load_dwordx4 v[60:63], v202, s[40:43], s60 offen sc1\n" \
  "buffer_load_dwordx4 v[120:123], v206, s[40:43], s62 offen offset:2048 sc1\n" \
  "buffer_load_dwordx4 v[124:127], v206, s[40:43], s62 offen offset:3072 sc1\n" \
  "s_add_u32 s64, s64, 128\n" \
  "s_branch .LF_fl_b_%=\n" \
  ".LF_w1_%=:\n" \
  "v_accvgpr_write_b32 a0, v128\n" \
  "v_accvgpr_write_b32 a1, v129\n" \
  "v_accvgpr_write_b32 a2, v130\n" \
  "v_accvgpr_write_b32 a3, v131\n" \
  "v_accvgpr_write_b32 a4, v132\n" \
  "v_accvgpr_write_b32 a5, v133\n" \
  "v_accvgpr_write_b32 a6, v134\n" \
  "v_accvgpr_write_b32 a7, v135\n" \
  "v_accvgpr_write_b32 a8, v136\n" \
  "v_accvgpr_write_b32 a9, v137\n" \
  "v_accvgpr_write_b32 a10, v138\n" \
  "v_accvgpr_write_b32 a11, v139\n" \
  "v_accvgpr_write_b32 a12, v140\n" \
  "v_accvgpr_write_b32 a13, v141\n" \
  "v_accvgpr_write_b32 a14, v142\n" \
  "v_accvgpr_write_b32 a15, v143\n" \
  "v_accvgpr_write_b32 a16, v144\n" \
  "v_accvgpr_write_b32 a17, v145\n" \
  "v_accvgpr_write_b32 a18, v146\n" \
  "v_accvgpr_write_b32 a19, v147\n" \
  "v_accvgpr_write_b32 a20, v148\n" \
  "v_accvgpr_write_b32 a21, v149\n" \
  "v_accvgpr_write_b32 a22, v150\n" \
  "v_accvgpr_write_b32 a23, v151\n" \
  "v_accvgpr_write_b32 a24, v152\n" \
  "v_accvgpr_write_b32 a25, v153\n" \
  "v_accvgpr_write_b32 a26, v154\n" \
  "v_accvgpr_write_b32 a27, v155\n" \
  "v_accvgpr_write_b32 a28, v156\n" \
  "v_accvgpr_write_b32 a29, v157\n" \
  "v_accvgpr_write_b32 a30, v158\n" \
  "v_accvgpr_write_b32 a31, v159\n" \
  "s_branch .LF_w_b_%=\n" \
  ".LF_w2_%=:\n" \
  "v_accvgpr_write_b32 a32, v128\n" \
  "v_accvgpr_write_b32 a33, v129\n" \
  "v_accvgpr_write_b32 a34, v130\n" \
  "v_accvgpr_write_b32 a35, v131\n" \
  "v_accvgpr_write_b32 a36, v132\n" \
  "v_accvgpr_write_b32 a37, v133\n" \
  "v_accvgpr_write_b32 a38, v134\n" \
  "v_accvgpr_write_b32 a39, v135\n" \
  "v_accvgpr_write_b32 a40, v136\n" \
  "v_accvgpr_write_b32 a41, v137\n" \
  "v_accvgpr_write_b32 a42, v138\n" \
  "v_accvgpr_write_b32 a43, v139\n" \
  "v_accvgpr_write_b32 a44, v140\n" \
  "v_accvgpr_write_b32 a45, v141\n" \
  "v_accvgpr_write_b32 a46, v142\n" \
  "v_accvgpr_write_b32 a47, v143\n" \
  "v_accvgpr_write_b32 a48, v144\n" \
  "v_accvgpr_write_b32 a49, v145\n" \
  "v_accvgpr_write_b32 a50, v146\n" \
  "v_accvgpr_write_b32 a51, v147\n" \
  "v_accvgpr_write_b32 a52, v148\n" \
  "v_accvgpr_write_b32 a53, v149\n" \
  "v_accvgpr_write_b32 a54, v150\n" \
  "v_accvgpr_write_b32 a55, v151\n" \
  "v_accvgpr_write_b32 a56, v152\n" \
  "v_accvgpr_write_b32 a57, v153\n" \
  "v_accvgpr_write_b32 a58, v154\n" \
  "v_accvgpr_write_b32 a59, v155\n" \
  "v_accvgpr_write_b32 a60, v156\n" \
  "v_accvgpr_write_b32 a61, v157\n" \
  "v_accvgpr_write_b32 a62, v158\n" \
  "v_accvgpr_write_b32 a63, v159\n" \
  "s_branch .LF_w_b_%=\n" \
  ".LF_w3_%=:\n" \
  "v_accvgpr_write_b32 a64, v128\n" \
  "v_accvgpr_write_b32 a65, v129\n" \
  "v_accvgpr_write_b32 a66, v130\n" \
  "v_accvgpr_write_b32 a67, v131\n" \
  "v_accvgpr_write_b32 a68, v132\n" \
  "v_accvgpr_write_b32 a69, v133\n" \
  "v_accvgpr_write_b32 a70, v134\n" \
  "v_accvgpr_write_b32 a71, v135\n" \
  "v_accvgpr_write_b32 a72, v136\n" \
  "v_accvgpr_write_b32 a73, v137\n" \
  "v_accvgpr_write_b32 a74, v138\n" \
  "v_accvgpr_write_b32 a75, v139\n" \
  "v_accvgpr_write_b32 a76, v140\n" \
  "v_accvgpr_write_b32 a77, v141\n" \
  "v_accvgpr_write_b32 a78, v142\n" \
  "v_accvgpr_write_b32 a79, v143\n" \
  "v_accvgpr_write_b32 a80, v144\n" \
  "v_accvgpr_write_b32 a81, v145\n" \
  "v_accvgpr_write_b32 a82, v146\n" \
  "v_accvgpr_write_b32 a83, v147\n" \
  "v_accvgpr_write_b32 a84, v148\n" \
  "v_accvgpr_write_b32 a85, v149\n" \
  "v_accvgpr_write_b32 a86, v150\n" \
  "v_accvgpr_write_b32 a87, v151\n" \
  "v_accvgpr_write_b32 a88, v152\n" \
  "v_accvgpr_write_b32 a89, v153\n" \
  "v_accvgpr_write_b32 a90, v154\n" \
  "v_accvgpr_write_b32 a91, v155\n" \
  "v_accvgpr_write_b32 a92, v156\n" \
  "v_accvgpr_write_b32 a93, v157\n" \
  "v_accvgpr_write_b32 a94, v158\n" \
  "v_accvgpr_write_b32 a95, v159\n" \
  "s_branch .LF_w_b_%=\n" \
  ".LF_w4_%=:\n" \
  "v_accvgpr_write_b32 a96, v128\n" \
  "v_accvgpr_write_b32 a97, v129\n" \
  "v_accvgpr_write_b32 a98, v130\n" \
  "v_accvgpr_write_b32 a99, v131\n" \
  "v_accvgpr_write_b32 a100, v132\n" \
  "v_accvgpr_write_b32 a101, v133\n" \
  "v_accvgpr_write_b32 a102, v134\n" \
  "v_accvgpr_write_b32 a103, v135\n" \
  "v_accvgpr_write_b32 a104, v136\n" \
  "v_accvgpr_write_b32 a105, v137\n" \
  "v_accvgpr_write_b32 a106, v138\n" \
  "v_accvgpr_write_b32 a107, v139\n" \
  "v_accvgpr_write_b32 a108, v140\n" \
  "v_accvgpr_write_b32 a109, v141\n" \
  "v_accvgpr_write_b32 a110, v142\n" \
  "v_accvgpr_write_b32 a111, v143\n" \
  "v_accvgpr_write_b32 a112, v144\n" \
  "v_accvgpr_write_b32 a113, v145\n" \
  "v_accvgpr_write_b32 a114, v146\n" \
  "v_accvgpr_write_b32 a115, v147\n" \
  "v_accvgpr_write_b32 a116, v148\n" \
  "v_accvgpr_write_b32 a117, v149\n" \
  "v_accvgpr_write_b32 a118, v150\n" \
  "v_accvgpr_write_b32 a119, v151\n" \
  "v_accvgpr_write_b32 a120, v152\n" \
  "v_accvgpr_write_b32 a121, v153\n" \
  "v_accvgpr_write_b32 a122, v154\n" \
  "v_accvgpr_write_b32 a123, v155\n" \
  "v_accvgpr_write_b32 a124, v156\n" \
  "v_accvgpr_write_b32 a125, v157\n" \
  "v_accvgpr_write_b32 a126, v158\n" \
  "v_accvgpr_write_b32 a127, v159\n" \
  "s_branch .LF_w_b_%=\n" \
  ".LF_w5_%=:\n" \
  "v_accvgpr_write_b32 a128, v128\n" \
  "v_accvgpr_write_b32 a129, v129\n" \
  "v_accvgpr_write_b32 a130, v130\n" \
  "v_accvgpr_write_b32 a131, v131\n" \
  "v_accvgpr_write_b32 a132, v132\n" \
  "v_accvgpr_write_b32 a133, v133\n" \
  "v_accvgpr_write_b32 a134, v134\n" \
  "v_accvgpr_write_b32 a135, v135\n" \
  "v_accvgpr_write_b32 a136, v136\n" \
  "v_accvgpr_write_b32 a137, v137\n" \
  "v_accvgpr_write_b32 a138, v138\n" \
  "v_accvgpr_write_b32 a139, v139\n" \
  "v_accvgpr_write_b32 a140, v140\n" \
  "v_accvgpr_write_b32 a141, v141\n" \
  "v_accvgpr_write_b32 a142, v142\n" \
  "v_accvgpr_write_b32 a143, v143\n" \
  "v_accvgpr_write_b32 a144, v144\n" \
  "v_accvgpr_write_b32 a145, v145\n" \
  "v_accvgpr_write_b32 a146, v146\n" \
  "v_accvgpr_write_b32 a147, v147\n" \
  "v_accvgpr_write_b32 a148, v148\n" \
  "v_accvgpr_write_b32 a149, v149\n" \
  "v_accvgpr_write_b32 a150, v150\n" \
  "v_accvgpr_write_b32 a151, v151\n" \
  "v_accvgpr_write_b32 a152, v152\n" \
  "v_accvgpr_write_b32 a153, v153\n" \
  "v_accvgpr_write_b32 a154, v154\n" \
  "v_accvgpr_write_b32 a155, v155\n" \
  "v_accvgpr_write_b32 a156, v156\n" \
  "v_accvgpr_write_b32 a157, v157\n" \
  "v_accvgpr_write_b32 a158, v158\n" \
  "v_accvgpr_write_b32 a159, v159\n" \
  "s_branch .LF_w_b_%=\n" \
  ".LF_r1_%=:\n" \
  "v_mov_b32 v192, v215\n" \
  "v_mov_b32 v193, v216\n" \
  "v_mov_b32 v194, v217\n" \
  "v_accvgpr_read_b32 v128, a32\n" \
  "v_accvgpr_read_b32 v129, a33\n" \
  "v_accvgpr_read_b32 v130, a34\n" \
  "v_accvgpr_read_b32 v131, a35\n" \
  "v_accvgpr_read_b32 v132, a36\n" \
  "v_accvgpr_read_b32 v133, a37\n" \
  "v_accvgpr_read_b32 v134, a38\n" \
  "v_accvgpr_read_b32 v135, a39\n" \
  "v_accvgpr_read_b32 v136, a40\n" \
  "v_accvgpr_read_b32 v137, a41\n" \
  "v_accvgpr_read_b32 v138, a42\n" \
  "v_accvgpr_read_b32 v139, a43\n" \
  "v_accvgpr_read_b32 v140, a44\n" \
  "v_accvgpr_read_b32 v141, a45\n" \
  "v_accvgpr_read_b32 v142, a46\n" \
  "v_accvgpr_read_b32 v143, a47\n" \
  "v_accvgpr_read_b32 v144, a48\n" \
  "v_accvgpr_read_b32 v145, a49\n" \
  "v_accvgpr_read_b32 v146, a50\n" \
  "v_accvgpr_read_b32 v147, a51\n" \
  "v_accvgpr_read_b32 v148, a52\n" \
  "v_accvgpr_read_b32 v149, a53\n" \
  "v_accvgpr_read_b32 v150, a54\n" \
  "v_accvgpr_read_b32 v151, a55\n" \
  "v_accvgpr_read_b32 v152, a56\n" \
  "v_accvgpr_read_b32 v153, a57\n" \
  "v_accvgpr_read_b32 v154, a58\n" \
  "v_accvgpr_read_b32 v155, a59\n" \
  "v_accvgpr_read_b32 v156, a60\n" \
  "v_accvgpr_read_b32 v157, a61\n" \
  "v_accvgpr_read_b32 v158, a62\n" \
  "v_accvgpr_read_b32 v159, a63\n" \
  "s_branch .LF_r_b_%=\n" \
  ".LF_r2_%=:\n" \
  "v_mov_b32 v192, v218\n" \
  "v_mov_b32 v193, v219\n" \
  "v_mov_b32 v194, v220\n" \
  "v_accvgpr_read_b32 v128, a64\n" \
  "v_accvgpr_read_b32 v129, a65\n" \
  "v_accvgpr_read_b32 v130, a66\n" \
  "v_accvgpr_read_b32 v131, a67\n" \
  "v_accvgpr_read_b32 v132, a68\n" \
  "v_accvgpr_read_b32 v133, a69\n" \
  "v_accvgpr_read_b32 v134, a70\n" \
  "v_accvgpr_read_b32 v135, a71\n" \
  "v_accvgpr_read_b32 v136, a72\n" \
  "v_accvgpr_read_b32 v137, a73\n" \
  "v_accvgpr_read_b32 v138, a74\n" \
  "v_accvgpr_read_b32 v139, a75\n" \
  "v_accvgpr_read_b32 v140, a76\n" \
  "v_accvgpr_read_b32 v141, a77\n" \
  "v_accvgpr_read_b32 v142, a78\n" \
  "v_accvgpr_read_b32 v143, a79\n" \
  "v_accvgpr_read_b32 v144, a80\n" \
  "v_accvgpr_read_b32 v145, a81\n" \
  "v_accvgpr_read_b32 v146, a82\n" \
  "v_accvgpr_read_b32 v147, a83\n" \
  "v_accvgpr_read_b32 v148, a84\n" \
  "v_accvgpr_read_b32 v149, a85\n" \
  "v_accvgpr_read_b32 v150, a86\n" \
  "v_accvgpr_read_b32 v151, a87\n" \
  "v_accvgpr_read_b32 v152, a88\n" \
  "v_accvgpr_read_b32 v153, a89\n" \
  "v_accvgpr_read_b32 v154, a90\n" \
  "v_accvgpr_read_b32 v155, a91\n" \
  "v_accvgpr_read_b32 v156, a92\n" \
  "v_accvgpr_read_b32 v157, a93\n" \
  "v_accvgpr_read_b32 v158, a94\n" \
  "v_accvgpr_read_b32 v159, a95\n" \
  "s_branch .LF_r_b_%=\n" \
  ".LF_r3_%=:\n" \
  "v_mov_b32 v192, v221\n" \
  "v_mov_b32 v193, v222\n" \
  "v_mov_b32 v194, v223\n" \
  "v_accvgpr_read_b32 v128, a96\n" \
  "v_accvgpr_read_b32 v129, a97\n" \
  "v_accvgpr_read_b32 v130, a98\n" \
  "v_accvgpr_read_b32 v131, a99\n" \
  "v_accvgpr_read_b32 v132, a100\n" \
  "v_accvgpr_read_b32 v133, a101\n" \
  "v_accvgpr_read_b32 v134, a102\n" \
  "v_accvgpr_read_b32 v135, a103\n" \
  "v_accvgpr_read_b32 v136, a104\n" \
  "v_accvgpr_read_b32 v137, a105\n" \
  "v_accvgpr_read_b32 v138, a106\n" \
  "v_accvgpr_read_b32 v139, a107\n" \
  "v_accvgpr_read_b32 v140, a108\n" \
  "v_accvgpr_read_b32 v141, a109\n" \
  "v_accvgpr_read_b32 v142, a110\n" \
  "v_accvgpr_read_b32 v143, a111\n" \
  "v_accvgpr_read_b32 v144, a112\n" \
  "v_accvgpr_read_b32 v145, a113\n" \
  "v_accvgpr_read_b32 v146, a114\n" \
  "v_accvgpr_read_b32 v147, a115\n" \
  "v_accvgpr_read_b32 v148, a116\n" \
  "v_accvgpr_read_b32 v149, a117\n" \
  "v_accvgpr_read_b32 v150, a118\n" \
  "v_accvgpr_read_b32 v151, a119\n" \
  "v_accvgpr_read_b32 v152, a120\n" \
  "v_accvgpr_read_b32 v153, a121\n" \
  "v_accvgpr_read_b32 v154, a122\n" \
  "v_accvgpr_read_b32 v155, a123\n" \
  "v_accvgpr_read_b32 v156, a124\n" \
  "v_accvgpr_read_b32 v157, a125\n" \
  "v_accvgpr_read_b32 v158, a126\n" \
  "v_accvgpr_read_b32 v159, a127\n" \
  "s_branch .LF_r_b_%=\n" \
  ".LF_r4_%=:\n" \
  "v_mov_b32 v192, v224\n" \
  "v_mov_b32 v193, v225\n" \
  "v_mov_b32 v194, v226\n" \
  "v_accvgpr_read_b32 v128, a128\n" \
  "v_accvgpr_read_b32 v129, a129\n" \
  "v_accvgpr_read_b32 v130, a130\n" \
  "v_accvgpr_read_b32 v131, a131\n" \
  "v_accvgpr_read_b32 v132, a132\n" \
  "v_accvgpr_read_b32 v133, a133\n" \
  "v_accvgpr_read_b32 v134, a134\n" \
  "v_accvgpr_read_b32 v135, a135\n" \
  "v_accvgpr_read_b32 v136, a136\n" \
  "v_accvgpr_read_b32 v137, a137\n" \
  "v_accvgpr_read_b32 v138, a138\n" \
  "v_accvgpr_read_b32 v139, a139\n" \
  "v_accvgpr_read_b32 v140, a140\n" \
  "v_accvgpr_read_b32 v141, a141\n" \
  "v_accvgpr_read_b32 v142, a142\n" \
  "v_accvgpr_read_b32 v143, a143\n" \
  "v_accvgpr_read_b32 v144, a144\n" \
  "v_accvgpr_read_b32 v145, a145\n" \
  "v_accvgpr_read_b32 v146, a146\n" \
  "v_accvgpr_read_b32 v147, a147\n" \
  "v_accvgpr_read_b32 v148, a148\n" \
  "v_accvgpr_read_b32 v149, a149\n" \
  "v_accvgpr_read_b32 v150, a150\n" \
  "v_accvgpr_read_b32 v151, a151\n" \
  "v_accvgpr_read_b32 v152, a152\n" \
  "v_accvgpr_read_b32 v153, a153\n" \
  "v_accvgpr_read_b32 v154, a154\n" \
  "v_accvgpr_read_b32 v155, a155\n" \
  "v_accvgpr_read_b32 v156, a156\n" \
  "v_accvgpr_read_b32 v157, a157\n" \
  "v_accvgpr_read_b32 v158, a158\n" \
  "v_accvgpr_read_b32 v159, a159\n" \
  "s_branch .LF_r_b_%=\n" \
  ".LF_r5_%=:\n" \
  "v_mov_b32 v192, v227\n" \
  "v_mov_b32 v193, v228\n" \
  "v_mov_b32 v194, v229\n" \
  "v_accvgpr_read_b32 v128, a160\n" \
  "v_accvgpr_read_b32 v129, a161\n" \
  "v_accvgpr_read_b32 v130, a162\n" \
  "v_accvgpr_read_b32 v131, a163\n" \
  "v_accvgpr_read_b32 v132, a164\n" \
  "v_accvgpr_read_b32 v133, a165\n" \
  "v_accvgpr_read_b32 v134, a166\n" \
  "v_accvgpr_read_b32 v135, a167\n" \
  "v_accvgpr_read_b32 v136, a168\n" \
  "v_accvgpr_read_b32 v137, a169\n" \
  "v_accvgpr_read_b32 v138, a170\n" \
  "v_accvgpr_read_b32 v139, a171\n" \
  "v_accvgpr_read_b32 v140, a172\n" \
  "v_accvgpr_read_b32 v141, a173\n" \
  "v_accvgpr_read_b32 v142, a174\n" \
  "v_accvgpr_read_b32 v143, a175\n" \
  "v_accvgpr_read_b32 v144, a176\n" \
  "v_accvgpr_read_b32 v145, a177\n" \
  "v_accvgpr_read_b32 v146, a178\n" \
  "v_accvgpr_read_b32 v147, a179\n" \
  "v_accvgpr_read_b32 v148, a180\n" \
  "v_accvgpr_read_b32 v149, a181\n" \
  "v_accvgpr_read_b32 v150, a182\n" \
  "v_accvgpr_read_b32 v151, a183\n" \
  "v_accvgpr_read_b32 v152, a184\n" \
  "v_accvgpr_read_b32 v153, a185\n" \
  "v_accvgpr_read_b32 v154, a186\n" \
  "v_accvgpr_read_b32 v155, a187\n" \
  "v_accvgpr_read_b32 v156, a188\n" \
  "v_accvgpr_read_b32 v157, a189\n" \
  "v_accvgpr_read_b32 v158, a190\n" \
  "v_accvgpr_read_b32 v159, a191\n" \
  "s_branch .LF_r_b_%=\n" \
  ".LF_bad_%=:\n" \
  "v_max_u32 v58, v0, v1\n" \
  "v_max3_u32 v58, v58, v2, v3\n" \
  "v_max3_u32 v58, v58, v4, v5\n" \
  "v_max3_u32 v58, v58, v6, v7\n" \
  "v_max3_u32 v58, v58, v8, v9\n" \
  "v_max3_u32 v58, v58, v10, v11\n" \
  "v_max3_u32 v58, v58, v12, v13\n" \
  "v_max3_u32 v58, v58, v14, v15\n" \
  "v_max3_u32 v58, v58, v16, v17\n" \
  "v_max3_u32 v58, v58, v18, v19\n" \
  "v_max3_u32 v58, v58, v20, v21\n" \
  "v_max3_u32 v58, v58, v22, v23\n" \
  "v_max3_u32 v58, v58, v24, v25\n" \
  "v_max3_u32 v58, v58, v26, v27\n" \
  "v_max3_u32 v58, v58, v28, v29\n" \
  "v_max3_u32 v58, v58, v30, v31\n" \
  "v_max3_u32 v58, v58, v32, v33\n" \
  "v_max3_u32 v58, v58, v34, v35\n" \
  "v_max3_u32 v58, v58, v36, v37\n" \
  "v_max3_u32 v58, v58, v38, v39\n" \
  "v_cmp_eq_u32 vcc, -1, v58\n" \
  "s_cbranch_vccz .LF_ok_%=\n" \
  "v_mov_b64 v[40:41], 0\n" \
  "v_mov_b64 v[42:43], 0\n" \
  "v_mov_b64 v[44:45], 0\n" \
  "v_mov_b64 v[46:47], 0\n" \
  "v_mov_b64 v[48:49], 0\n" \
  "v_mov_b64 v[50:51], 0\n" \
  "v_mov_b64 v[52:53], 0\n" \
  "v_mov_b64 v[54:55], 0\n" \
  "s_sleep 2\n" \
  "s_cmp_eq_u32 s48, 5\n" \
  "s_cbranch_scc1 .LF_bd1_%=\n" \
  "s_add_u32 s65, s65, %42\n" \
  "s_sleep 4\n" \
  ".LF_bd1_%=:\n" \
  "s_add_u32 s69, s69, 1\n" \
  "s_and_b32 s63, s69, 1023\n" \
  "s_cbranch_scc1 .LF_rd_%=\n" \
  "buffer_load_dwordx2 v[60:61], off, s[40:43], %38 offset:128 sc1\n" \
  "s_waitcnt vmcnt(0)\n" \
  "v_readfirstlane_b32 s63, v61\n" \
  "s_cmp_eq_u32 s63, %32\n" \
  "s_cbranch_scc1 .LF_ok_%=\n" \
  "s_cmp_le_u32 s69, 2097152\n" \
  "s_cbranch_scc1 .LF_rd_%=\n" \
  "v_mov_b32 v60, 1\n" \
  "v_mov_b32 v61, %32\n" \
  "buffer_store_dwordx2 v[60:61], off, s[40:43], %38 offset:128 sc1\n" \
  "s_branch .LF_ok_%=\n" \
  ".LF_end_%=:\n"
#define F_ASM_VOPS "+{v200}"(fi.sidx), "+{v201}"(fi.cidx), "+{v202}"(fi.l16), "+{v203}"(fi.ae0), "+{v204}"(fi.ae1), "+{v205}"(fi.ae2), "+{v206}"(fi.ae3), "+{v207}"(fi.am), "+{v208}"(fi.lw), "+{v209}"(fi.lr), "+{v210}"(fi.oo), "+{v211}"(fi.d0), "+{v212}"(fi.g[0]), "+{v213}"(fi.b[0]), "+{v214}"(fi.c[0]), "+{v215}"(fi.g[1]), "+{v216}"(fi.b[1]), "+{v217}"(fi.c[1]), "+{v218}"(fi.g[2]), "+{v219}"(fi.b[2]), "+{v220}"(fi.c[2]), "+{v221}"(fi.g[3]), "+{v222}"(fi.b[3]), "+{v223}"(fi.c[3]), "+{v224}"(fi.g[4]), "+{v225}"(fi.b[4]), "+{v226}"(fi.c[4]), "+{v227}"(fi.g[5]), "+{v228}"(fi.b[5]), "+{v229}"(fi.c[5])
#define F_ASM_SOPS "s"(fi.ws), "s"(fi.out), "s"(fi.tag), "s"(fi.o_u), "s"(fi.o_e), "s"(fi.o_x), "s"(fi.o_va), "s"(fi.o_tw), "s"(fi.o_ctl), "s"(fi.pa), "s"(fi.w), "s"(fi.f_w), "s"(fi.dret)
#define F_ASM_CLOB "v0", "v1", "v2", "v3", "v4", "v5", "v6", "v7", "v8", "v9", "v10", "v11", "v12", "v13", "v14", "v15", "v16", "v17", "v18", "v19", "v20", "v21", "v22", "v23", "v24", "v25", "v26", "v27", "v28", "v29", "v30", "v31", "v32", "v33", "v34", "v35", "v36", "v37", "v38", "v39", "v40", "v41", "v42", "v43", "v44", "v45", "v46", "v47", "v48", "v49", "v50", "v51", "v52", "v53", "v54", "v55", "v56", "v57", "v58", "v59", "v60", "v61", "v62", "v63", "v64", "v65", "v66", "v67", "v68", "v69", "v70", "v71", "v72", "v73", "v74", "v75", "v76", "v77", "v78", "v79", "v80", "v81", "v82", "v83", "v84", "v85", "v86", "v87", "v88", "v89", "v90", "v91", "v92", "v93", "v94", "v95", "v96", "v97", "v98", "v99", "v100", "v101", "v102", "v103", "v104", "v105", "v106", "v107", "v108", "v109", "v110", "v111", "v112", "v113", "v114", "v115", "v116", "v117", "v118", "v119", "v120", "v121", "v122", "v123", "v124", "v125", "v126", "v127", "v128", "v129", "v130", "v131", "v132", "v133", "v134", "v135", "v136", "v137", "v138", "v139", "v140", "v141", "v142", "v143", "v144", "v145", "v146", "v147", "v148", "v149", "v150", "v151", "v152", "v153", "v154", "v155", "v156", "v157", "v158", "v159", "v160", "v161", "v162", "v163", "v164", "v165", "v166", "v167", "v168", "v169", "v170", "v171", "v172", "v173", "v174", "v175", "v176", "v177", "v178", "v179", "v180", "v181", "v182", "v183", "v184", "v185", "v186", "v187", "v188", "v189", "v190", "v191", "v192", "v193", "v194", "v195", "v196", "v197", "v232", "v233", "v234", "v235", "v236", "v237", "a0", "a1", "a2", "a3", "a4", "a5", "a6", "a7", "a8", "a9", "a10", "a11", "a12", "a13", "a14", "a15", "a16", "a17", "a18", "a19", "a20", "a21", "a22", "a23", "a24", "a25", "a26", "a27", "a28", "a29", "a30", "a31", "a32", "a33", "a34", "a35", "a36", "a37", "a38", "a39", "a40", "a41", "a42", "a43", "a44", "a45", "a46", "a47", "a48", "a49", "a50", "a51", "a52", "a53", "a54", "a55", "a56", "a57", "a58", "a59", "a60", "a61", "a62", "a63", "a64", "a65", "a66", "a67", "a68", "a69", "a70", "a71", "a72", "a73", "a74", "a75", "a76", "a77", "a78", "a79", "a80", "a81", "a82", "a83", "a84", "a85", "a86", "a87", "a88", "a89", "a90", "a91", "a92", "a93", "a94", "a95", "a96", "a97", "a98", "a99", "a100", "a101", "a102", "a103", "a104", "a105", "a106", "a107", "a108", "a109", "a110", "a111", "a112", "a113", "a114", "a115", "a116", "a117", "a118", "a119", "a120", "a121", "a122", "a123", "a124", "a125", "a126", "a127", "a128", "a129", "a130", "a131", "a132", "a133", "a134", "a135", "a136", "a137", "a138", "a139", "a140", "a141", "a142", "a143", "a144", "a145", "a146", "a147", "a148", "a149", "a150", "a151", "a152", "a153", "a154", "a155", "a156", "a157", "a158", "a159", "a160", "a161", "a162", "a163", "a164", "a165", "a166", "a167", "a168", "a169", "a170", "a171", "a172", "a173", "a174", "a175", "a176", "a177", "a178", "a179", "a180", "a181", "a182", "a183", "a184", "a185", "a186", "a187", "a188", "a189", "a190", "a191", "s40", "s41", "s42", "s43", "s44", "s45", "s46", "s47", "s48", "s49", "s50", "s51", "s52", "s53", "s54", "s55", "s56", "s57", "s58", "s59", "s60", "s61", "s62", "s63", "s64", "s65", "s66", "s67", "s68", "s69", "s70", "s71", "s72", "s73", "s74", "s75", "s76", "s77", "s78", "s79", "s80", "s81", "s82", "s83", "s84", "s85", "s86", "s87", "s88", "s89", "s90", "s91", "s92", "s93", "s94", "s95", "s96", "s97", "s98", "s99", "vcc", "scc", "memory"

struct RedIn {
  unsigned sidx, cidx, l16, ax, am, mo, sel, to, fu, p0, p1, p2, p3, d0, d1, d2, d3, d4;
  const void* ws;
  unsigned w, go, tag, o_va, o_vb, lab, o_u, kind, o_ctl, pa, pb, f_d, f_r, ph, f_l;
};
__device__ __forceinline__ void red_role(const Params& p, int w, int T, char* smem) {
  const int grp = T & 3;
  CODE_PAD(PAD_RED);
  char* const ws = p.ws;
  Ctx cx = {ws, 0u};
  const int l = lane_id(), n = l & 15, g = l >> 4;
  comm_init(cx, grp * 9 + (T >> 2), w, smem, p.wl2 & 4);
  Bulk bk;
  bk.init(cx, grp);
  bk.pl.load(cx, grp);
  const Place& pl = bk.pl;
  char* const base = comm_base(ws) - CO;
  const unsigned dl = (unsigned)(ws - base);
  RedIn ri;
  ri.sidx = (n & 8) ? 0xEEEEu : 0x4444u;
  ri.cidx = (n & 3) * 0x5555u;
  ri.l16 = l * 16;
  ri.ax = W_PX * 32768 + (n >> 3) * 512 + g * 128 + (n & 7) * 16;
  ri.am = W_PM * 32768 + l * 16;
  const unsigned mt = (W_MEM + (T >> 4)) * 32768 + (T & 15) * 256;
  ri.mo = mt + (2 * (g & 1) + (g >> 1)) * 64 + (n >> 3) * 32 + (n & 1) * 16 + ((n >> 1) & 3) * 4;
  ri.sel = w ? (g == 1 ? (U_THIRD ? 2 : NSEG) : g >> 1) : (n & 1) ? 0x03020706u : 0x05040100u;
  ri.to = l < 4 ? CO + TW_OFF + (16384u ^ pl.dyn(l)) + 256 : 0x7F000000u;
  ri.fu = CO + pl.near(N_U + min(10 * w - 10 + min(n, 11), w == 3 ? 31 : 10 * w - 1)) + (T < 32 ? (T >> 1) & 3 : T & 3) * 8 + (g == 1 ? 64 : l & 32);
  ri.d0 = 0; ri.d1 = 0; ri.d2 = 0; ri.d3 = 0; ri.d4 = 0;
  ri.ws = base; ri.w = w; ri.go = pl.go; ri.tag = cx.tag; ri.lab = grp; ri.o_ctl = dl;
  ri.o_va = __builtin_amdgcn_readfirstlane(CO + (win_static(W_VO + 2 * (T >> 2)) ^ pl.dyn(grp)));
  ri.o_vb = __builtin_amdgcn_readfirstlane(CO + (win_static(W_VO + 2 * (T >> 2) + 1) ^ pl.dyn(grp)));
  auto dead = [&](int i) { return i < 0 ? CO + (16384u ^ pl.dyn(g)) + mt + n * 16 : (i < 4 ? ri.o_va : ri.o_vb) + (i & 3) * 1024 + l * 16; };
  ri.p0 = w ? dead(3 * w - 4) : T < 8 && l < 4 ? ri.to - 116 : 0x7F000000u;
  ri.p1 = w ? dead(3 * w - 3) : l < 4 ? 0u : 0x7F000000u; ri.p2 = dead(3 * w - 2);
  const int pz = (T >> 2) * 3 + w - 1;
  ri.p3 = pz < 24 ? CO + (16384u ^ pl.dyn(grp)) + (W_PX + (pz >> 2)) * 32768 + (pz & 3) * 1024 + l * 16 : 0x7F000000u;
  ri.o_u = __builtin_amdgcn_readfirstlane(u_tile(T, 0, 0) ^ (~bk.w13 & 8192u));
  ri.kind = __builtin_amdgcn_readfirstlane(T == 35 ? CO + (pl.near(N_K) ^ 4096u) - 32 : 0u);
  ri.pa = p.vlead << 16;
  ri.pb = p.dret | (p.late_r << 16);
  ri.ph = p.hdel | (p.hdl2 << 8) | 0x10000;
  ri.f_d = __builtin_amdgcn_readfirstlane(T < 32 ? CO + pl.near(N_RD + T) : 0x7F000000u);
  ri.f_r = dl + F_R * 128 + T * 8;
  ri.f_l = __builtin_amdgcn_readfirstlane(CO + (pl.near(N_DUR) ^ 4096u));
  asm volatile(R_ASM_TEXT : R_ASM_VOPS : R_ASM_SOPS : R_ASM_CLOB);
}

__device__ __forceinline__ int c_line(int h) { return (h >> 2) * 512 + (h & 1) * 256 + (h & 2) * 64; }
__device__ __forceinline__ void first_role(const Params& p) {
  const int l = lane_id();
  const Ctx cx = {p.ws, __builtin_amdgcn_readfirstlane(gen_load(p.ws)) + 1u};
  Place pl;
  pl.load(cx, 0);
  unsigned spins = 0;
#pragma unroll 1
  for (int s = 0; s < U_WATCH; ++s) {
    GAS u64* const f = (GAS u64*)(comm_base(p.ws) + pl.near(N_U + (l & 31))) + (l >> 5) * 2 + (s & 1) * 4;
    while (__any((int)min(flag_get(cx, f), flag_get(cx, f + 1)) <= s) && !spin_fail(spins, cx)) sleep_n(p.fsl);
    if (l == 0) flag_put(cx, (GAS u64*)(comm_base(p.ws) + (pl.near(N_DUR + s) ^ 4096u)), s + 1);
  }
}
struct ChainIn {
  unsigned sidx, l16, qw, kw, fq, fk, kxo, pst, ps2, lx, sid2, qxo, sto, plx, mk, ck0, ck2, hx, viw, fva, pk3, fvs, uo, po0, po1, po2, co0, co1, co2;
  unsigned vq0, vq1, vq2, vq3, vo0, vo1, vo2, vo3, v50, v51, v52, v53, tro0, tro1, tro2, tro3, tro4, m0o;
  const void *ws, *Wa, *Wb, *mem0;
  unsigned w, o_a, o_b, o_nx, o_exp, lab, f_p, f_m, f_dur, f_go, o_ctl, o_dbg, dret, o_qx, f_s, f_r;
};

template <bool SC>
__device__ __forceinline__ void chain_role(const Params& p, int w, int h, int grp) {
  constexpr unsigned QT = 9216, KT = 13312, SX = 16384, VI = 9216, VX = 16384;
  const int l = lane_id(), n = l & 15, g = l >> 4, q4 = (l >> 2) & 3, p4 = l & 3;
  const int ty = (n >> 1) & 1, row = 2 * (n >> 2) + (n & 1), cid = h * 2 + (SC ? 0 : 1);
  char* const base = comm_base(p.ws) - CO;
  const unsigned dl = (unsigned)(p.ws - base);
  ChainIn ci;
  ci.sidx = (n & 2) ? 0xEEEEu : 0x4444u;
  ci.l16 = l * 16;
  ci.m0o = row * (DM * 2) + g * 32 + ty * 16;
  if (SC) {
    ci.qw = QT + (32 * w + n) * 16 + g * 4;
    ci.kw = KT + (32 * w + n) * 16 + (((g & 1) << 1) | (g >> 1)) * 4;
    ci.fq = QT + (4 * g + q4) * 16 + (p4 & 1) * 8;
    ci.fk = KT + (4 * g + q4) * 16 + (p4 >> 1) * 8;
    ci.kxo = (g * 16 + 8 * (q4 & 1) + 4 * (q4 >> 1) + p4) * 16;
    const unsigned ex = CO + win_static(W_PX + (h >> 3)) + (h & 7) * 512 + (g & 1) * 128 + (n & 7) * 16 + (g >> 1) * 8 + (n >> 3);
    if (w < 2) ci.pst = ex + w * 256;
    else if (g & 1) ci.pst = CO + win_static(W_PC) + c_line(h) + (n & 7) * 16 + (((n >> 2) ^ (g >> 1)) & 1) * 8 + 2 * (w & 1) + (n >> 3);
    else ci.pst = CO + win_static(W_PM) + (h >> 1) * 256 + (VO_SWAP ? (g >> 1) * 8 + (h & 1) * 128 : (g >> 1) * 128 + (h & 1) * 8) + (n & 7) * 16 + 2 * (w & 1) + (n >> 3);
    ci.ps2 = w < 2 ? ci.pst + 2 : ex + 2 * (w & 1);
    ci.mk = (g & 1) ? 0u : 0x3F800000u;
    ci.ck0 = g == 1 && !(h & 2) ? 0x3F800000u : 0u;
    ci.ck2 = g == 1 && (h & 2) ? 0x3F800000u : 0u;
    ci.hx = SX + 2048 + ((w & 1) * 64 + l) * HAND_B;
    ci.lx = SX + l * 8 + (w == 1 ? 4 : 0);
    ci.qxo = (w >> 1) * 1024 + (g * 16 + 8 * (w & 1) + (n & 7)) * 16;
    ci.sto = 0x80000 + h * 256 + (8 * w + (n & 7)) * 8;
    ci.plx = n >= 8 ? 0x7F000000u : w * 16384 + n * 16 + ((g & 1) ? 4096 + c_line(h) + (((n >> 2) ^ (g >> 1)) & 1) * 8 : h * 128 + (g >> 1) * 8);
  } else {
    ci.viw = VI + (32 * w + n) * 16 + g * 4;
    ci.fva = VI + (4 * g + q4) * 16 + (p4 & 1) * 8;
    ci.pk3 = VI + n * 16 + g * 4; ci.fvs = w * 1024 + l * 16;
    ci.uo = w == 1 ? l * 16 : 0x7F000000u;
    auto vo_at = [&](int T0) {
      const int T = T0 + 2 * w + (g >> 1);
      return CO + win_static(W_VO + 2 * (T >> 2) + (h >> 4)) + (h & 15) * 256 + n * 16 + (T & 3);
    };
    ci.po0 = vo_at(8 * (g & 1)); ci.po1 = vo_at(16 + 8 * (g & 1));
    auto co_at = [&](int T0) { return (T0 + 2 * w + (g >> 1)) * 16384 + h * 256 + n * 16; };
    ci.co0 = co_at(8 * (g & 1)); ci.co1 = co_at(16 + 8 * (g & 1)); ci.co2 = co_at(w == 0 ? 16 : w == 1 ? 22 : 28) + (g & 1) * 8;
    if (w == 0) ci.po1 = h < 4 && l < 4 ? CO + TW_OFF + 16384 + 384 + l : 0x7F000000u;
    ci.po2 = vo_at(w == 0 ? 16 : w == 1 ? 22 : 28) + (g & 1) * 8;
  }
  ci.sid2 = (n & 8) ? 0xEEEEu : 0x4444u;
  unsigned vq[4], vo[4], v5[4];
#pragma unroll
  for (int i = 0; i < 4; ++i) {
    const int ch = n ^ (2 * ((g & 1) | (i << 1))), tp = ch >> 1;
    vq[i] = (g + 4 * i) * (HD * 2) + ch * 16 - 1024 * i;
    vo[i] = (g + 4 * i) * (DM * 2) + ch * 16 - 1024 * i;
    v5[i] = (g + 4 * i + 16 - 16 * (tp >> 2)) * (DM * 2) + (32 + (tp & 3)) * 32 + (ch & 1) * 16 - 1024 * i;
  }
  ci.vq0 = vq[0]; ci.vq1 = vq[1]; ci.vq2 = vq[2]; ci.vq3 = vq[3];
  ci.vo0 = vo[0]; ci.vo1 = vo[1]; ci.vo2 = vo[2]; ci.vo3 = vo[3];
  ci.v50 = v5[0]; ci.v51 = v5[1]; ci.v52 = v5[2]; ci.v53 = v5[3];
  {
    const int rowl = 16 * (g >> 1) + 8 * (q4 >> 1) + 4 * (g & 1) + (q4 & 1), rho = (rowl & 1) | (((rowl >> 2) & 3) << 1);
    ci.tro0 = rowl * 256 + (((4 * w + (p4 >> 1)) ^ (2 * rho)) * 16) + (p4 & 1) * 8;
    ci.tro1 = rowl * 256 + (((4 * w + 2 + (p4 >> 1)) ^ (2 * rho)) * 16) + (p4 & 1) * 8;
    const int row2 = 16 * (g & 1) + 4 * (g >> 1) + (q4 & 1), rho2 = (row2 & 1) | (((row2 >> 2) & 3) << 1);
    ci.tro2 = row2 * 256 + (((4 * w + 2 * (q4 >> 1) + (p4 >> 1)) ^ (2 * rho2)) * 16) + (p4 & 1) * 8;
    ci.tro3 = w < 2 ? (ci.tro2 ^ (w * 64)) + 7 * 16384 : ci.tro2 + 9 * 16384;
    ci.tro4 = w < 2 ? ci.tro3 + 16384 : ci.tro3 ^ 128u;
  }
  ci.ws = base; ci.Wa = SC ? p.Wq : p.Wv; ci.Wb = SC ? p.Wk : p.Wo; ci.mem0 = p.mem0;
  ci.w = w;
  ci.o_a = 16 * w * (HD * 2) + h * 256;
  ci.o_b = (h * DH + 32 * w) * (DM * 2);
  ci.o_nx = near_head(B_K, h) + (w & 1) * 4096;
  ci.o_exp = near_head(B_K, h) + (SC ? 0 : 4096);
  ci.o_qx = PB + 2 * B_O + (SC ? h * 16384 : 0); ci.f_s = __builtin_amdgcn_readfirstlane(dl + (F_S + h) * 128);
  ci.f_r = dl + F_R * 128;
  ci.lab = grp;
  ci.f_p = __builtin_amdgcn_readfirstlane(CO + near_static(N_K + h) + (SC ? 0 : 8));
  ci.f_m = SC ? dl + (F_M + cid) * 128 + (w & 1) * 8 : w == 1 ? dl + (F_M + cid) * 128 : 0x7F000000u; ci.f_dur = __builtin_amdgcn_readfirstlane(CO + near_static(N_DUR + h));
  ci.f_go = dl + (F_GO + grp) * 128; ci.o_ctl = dl;
  ci.o_dbg = dl + (unsigned)WS_END + (cid * 4 + w) * 4096;
  ci.dret = p.dret | (p.late_s << 16);
  if (SC) asm volatile(X_ASM_TEXT M_ASM_TEXT : X_ASM_VOPS : X_ASM_SOPS : X_ASM_CLOB);
  else asm volatile(W_ASM_TEXT V_ASM_TEXT : V_ASM_VOPS : V_ASM_SOPS : V_ASM_CLOB);
}

struct ProjIn {
  unsigned l16, xr0, xr1, ew, er, uo, xs0, xs1, xs2, xs3, xs4, xs5, xs6, xs7, xs8;
  unsigned vq0, vq1, vq2, vq3, vo0, vo1, vo2, vo3, v50, v51, v52, v53, tro0, tro1;
  const void *ws, *Wa, *Wb, *x;
  unsigned w, o_a, o_b, o_p, o_q, f_a, f_b, f_go, o_ctl, f_c, u8, rng, f_d;
};
template <bool VU, int QO = 0>
__device__ __forceinline__ unsigned proj_role(const Params& p, int w, int h) {
  const int l = lane_id(), n = l & 15, g = l >> 4, q4 = (l >> 2) & 3, p4 = l & 3;
  char* const base = comm_base(p.ws) - CO;
  const unsigned dl = (unsigned)(p.ws - base);
  ProjIn pi;
  pi.l16 = l * 16; pi.uo = 0;
  unsigned xs[9];
#pragma unroll
  for (int i = 0; i < 9; ++i) {
    const int ci = (9 * w + i) * 64 + l, r = ci / 72, cc = ci - r * 72;
    xs[i] = (r * 72 + (cc ^ ((r >> 1) & 7))) * 16;
  }
  pi.xs0 = xs[0]; pi.xs1 = xs[1]; pi.xs2 = xs[2]; pi.xs3 = xs[3]; pi.xs4 = xs[4]; pi.xs5 = xs[5]; pi.xs6 = xs[6]; pi.xs7 = xs[7]; pi.xs8 = xs[8];
  pi.xr0 = 16384 + n * 1152 + ((g ^ ((n >> 1) & 7)) * 16); pi.xr1 = 16384 + n * 1152 + (((4 + g) ^ ((n >> 1) & 7)) * 16);
  pi.ew = w * 2048 + n * 64 + ((g ^ ((n >> 1) & 1) ^ ((n >> 2) & 2)) * 16);
  pi.er = 8192 + (g >> 1) * 1032 + (8 * (g & 1) + q4) * 64 + ((p4 ^ (q4 >> 1) ^ (2 * (g & 1))) * 16);
  unsigned vq[4], vo[4], v5[4];
#pragma unroll
  for (int i = 0; i < 4; ++i) {
    const int ch = n ^ (2 * (g | ((i >> 1) << 2))), tp = ch >> 1;
    vq[i] = (g + 4 * i) * (HD * 2) + ch * 16 - 1024 * i;
    vo[i] = (g + 4 * i) * (DM * 2) + ch * 16 - 1024 * i;
    v5[i] = (g + 4 * i + 64 * (tp & 1)) * (DM * 2) + (32 + (tp >> 1)) * 32 + (ch & 1) * 16 - 1024 * i;
  }
  pi.vq0 = vq[0]; pi.vq1 = vq[1]; pi.vq2 = vq[2]; pi.vq3 = vq[3];
  pi.vo0 = vo[0]; pi.vo1 = vo[1]; pi.vo2 = vo[2]; pi.vo3 = vo[3];
  pi.v50 = v5[0]; pi.v51 = v5[1]; pi.v52 = v5[2]; pi.v53 = v5[3];
  const int rho = q4 | ((g & 1) << 2);
  pi.tro0 = (8 * g + q4) * 256 + (((4 * w + (p4 >> 1)) ^ (2 * rho)) * 16) + (p4 & 1) * 8;
  pi.tro1 = (8 * g + q4) * 256 + (((4 * w + 2 + (p4 >> 1)) ^ (2 * rho)) * 16) + (p4 & 1) * 8;
  pi.ws = base; pi.Wa = VU ? p.Wv : QO ? p.Wq : p.Wk; pi.Wb = VU ? p.Wo : p.Wq; pi.x = p.x;
  pi.w = w;
  pi.o_a = 16 * w * (HD * 2) + h * 256;
  pi.o_b = (h * DH + 16 * w) * (DM * 2);
  pi.o_p = PB + 2 * (VU ? B_V : QO ? B_Q : B_K) + h * 16384 + w * 2048 + (VU ? QO * 524288 : 0);
  pi.o_q = VU ? u_tile(2 * w, h >> 3, h & 7) ^ 8192u : PB + 2 * B_Q + h * 16384 + w * 2048 + (K_FIRST - 1) * 524288;
  pi.f_a = __builtin_amdgcn_readfirstlane(w || (QO && !VU) || QO == 2 ? 0x7F000000u : CO + near_static(N_K + h) + (VU ? 8 + 8 * QO : 0));
  pi.f_b = __builtin_amdgcn_readfirstlane(VU ? CO + near_static(N_U + h) + w * 8 + 32 * QO : w || QO ? 0x7F000000u : dl + (F_Q + h) * 128);
  pi.f_go = dl + (F_GO + 4 + (h & 3)) * 128; pi.o_ctl = dl;
  pi.f_c = VU ? dl + (F_M + 2 * h + 1) * 128 : QO ? 77 : 78;
  pi.f_d = __builtin_amdgcn_readfirstlane(QO ? 0x7F000000u : dl + (F_U + h) * 128 + w * 8);
  pi.rng = QO | (QO == 2 ? 4 : QO ? 2 * U_HELP + 1 : NSEG) << 8 | U_LEAD << 16;
  pi.u8 = QO && !VU ? 0x7F000000u : (VU ? u_tile(32 + w, h >> 3, h & 7) : near_head(B_K, h) + w * 2048) ^ 8192u;
  unsigned tag;
  if (!VU) sleep_n(QO ? p.q0 : p.pj0);
  if (VU) asm volatile(U_ASM_TEXT : P_ASM_VOPS : P_ASM_SOPS : P_ASM_CLOB);
  else asm volatile(K_ASM_TEXT : P_ASM_VOPS : P_ASM_SOPS : P_ASM_CLOB);
  return tag;
}

constexpr int NS4 = 67;
__device__ __forceinline__ unsigned y_at(const Bulk& bk, int quad, unsigned o) { return bk.at(B_YT + quad * 16384 + o) ^ (quad >= Y_NEAR && quad < Y_FAR ? 8192u : 0u); }
__device__ __forceinline__ void mix_role(const Params& p, unsigned tag, int w, int ct, int s0, int ds, int s1, bool late) {
  const int l = lane_id(), n = l & 15, g = l >> 4;
  const Ctx cx = {p.ws, tag};
  Bulk bk;
  bk.init(cx, 4 + (ct & 3));
  const int tr = (w >> 1) * 16 + n, dc = ct * 96 + (w & 1) * 48 + 4 * g;
  const rsrc_t rx = mk_rsrc(p.x, (size_t)SL * DM * 2), rout = mk_rsrc(p.out, (size_t)SL * DM * 2);
  const int ro = (((w >> 1) * 16 + (l >> 2)) * DM + ct * 96 + (w & 1) * 48 + 4 * (l & 3)) * 2, pin = (4 * n + g) * 4, pout = (16 * (l & 3) + (l >> 2)) * 4;
  auto perm = [](int a, u32x2 v) { return (u32x2){(unsigned)__builtin_amdgcn_ds_bpermute(a, v[0]), (unsigned)__builtin_amdgcn_ds_bpermute(a, v[1])}; };
  u32x2 dv[3];
  unsigned yo[3];
#pragma unroll
  for (int i = 0; i < 3; ++i) { dv[i] = *(const u32x2*)(p.dsk + dc + 16 * i); yo[i] = y_at(bk, (dc + 16 * i) >> 2, tr * 8); }
  struct In { u32x4 a[4]; u32x2 b[4]; u32x2 x[3], y[3]; float g; bool ok; };
  auto ldb = [&](In& in, int sg) {
    in.g = __uint_as_float(__builtin_amdgcn_raw_buffer_load_b32(bk.rs, tr * 4, bk.at(B_G) + sg * 128, 16));
#pragma unroll
    for (int i = 0; i < 3; ++i) {
      in.y[i] = __builtin_amdgcn_raw_buffer_load_b64(bk.rs, yo[i], (sg >> 5) * 16384 + (sg & 31) * 256, 16);
      in.x[i] = __builtin_amdgcn_raw_buffer_load_b64(rx, ro + 32 * i, sg * (32 * DM * 2), 0);
    }
  };
  int seen = 0;
  const int ob0 = late ? NSEG : p.ob0, obn = p.ob1 - p.ob0, fa = F_A + (late ? 24 : 0) + ct * 4;
  auto seg = [=](int i) { return min(i < ob0 ? i : i + obn, NSEG - 1); };
  auto lda = [&](In& in, int sg) {
    in.ok = seen > sg;
    const unsigned l8 = in.ok ? l * 8 : 0x3F000000u;
#pragma unroll
    for (int kq = 0; kq < 4; ++kq) {
      const unsigned o = bk.at(B_AP + ((sg * 6 + ct) * 4 + kq) * 8192 + w * 2048);
      in.a[kq] = ld128(bk.rs, 2 * l8, o);
      in.b[kq] = __builtin_amdgcn_raw_buffer_load_b64(bk.rs, l8, o + 1024, 16);
    }
  };
  auto step = [&](In& in, In& n2, int k) {
    const int sg = seg(k);
    if (!in.ok) {
      wait_ge_n<2>(cx, fa, 1, 4, sg + 1, seen);
      lda(in, sg);
#pragma unroll
      for (int i = 0; i < 4; ++i) asm volatile("" : "+v"(in.a[i]), "+v"(in.b[i]));
    }
    ldb(n2, seg(k + 2 * ds));
    lda(n2, seg(k + 2 * ds));
    SB();
#pragma unroll
    for (int i = 0; i < 3; ++i) {
      float o[4], b[4];
      const u32x2 xv = perm(pin, in.x[i]);
#pragma unroll
      for (int j = 0; j < 4; ++j) {
        auto part = [&](int kq) { const unsigned u = i < 2 ? in.a[kq][2 * (i & 1) + (j >> 1)] : in.b[kq][j >> 1]; return (j & 1) ? hi16(u) : lo16(u); };
        const float av = (part(0) + part(1)) + (part(2) + part(3));
        const float y = (j & 1) ? hi16(in.y[i][j >> 1]) : lo16(in.y[i][j >> 1]);
        const float xf = (j & 1) ? hi16(xv[j >> 1]) : lo16(xv[j >> 1]), df = (j & 1) ? hi16(dv[i][j >> 1]) : lo16(dv[i][j >> 1]);
        o[j] = rbf(in.g * rbf(av));
        b[j] = rbf(1.0f - in.g) * rbf(y + rbf(xf * df));
      }
      u32x2 bp = {pk2(b[0], b[1]), pk2(b[2], b[3])};
      asm volatile("" : "+v"(bp));
#pragma unroll
      for (int j = 0; j < 4; ++j) o[j] += (j & 1) ? hi16(bp[j >> 1]) : lo16(bp[j >> 1]);
      __builtin_amdgcn_raw_buffer_store_b64(perm(pout, (u32x2){pk2(o[0], o[1]), pk2(o[2], o[3])}), rout, ro + 32 * i, sg * (32 * DM * 2), OUT_AUX);
    }
    return seg(k + ds) < s1;
  };
  wait_all<30>(cx, F_W, 1);
  In a, b, c;
  wait_ge_n<2>(cx, fa, 1, 4, 1, seen);
  ldb(a, seg(s0)); lda(a, seg(s0));
  ldb(b, seg(s0 + ds)); lda(b, seg(s0 + ds));
  for (int sg = s0; step(a, c, sg) && step(b, a, sg + ds) && step(c, b, sg + 2 * ds); sg += 3 * ds) {}
}

__device__ __forceinline__ void gate_rows(const Params& p, const Ctx& cx, const Bulk& bk, int blk) {
  const int l = lane_id(), n = l & 15, g = l >> 4;
  const rsrc_t rx = mk_rsrc(p.x, (size_t)SL * DM * 2), rg = mk_rsrc(p.gw, DM * 2);
  const int t = blk * 16 + g;
  u32x4 gw[4], xv[4][4];
  u32x2 gt, xt[4];
#pragma unroll
  for (int k = 0; k < 4; ++k) gw[k] = __builtin_amdgcn_raw_buffer_load_b128(rg, n * 16, k * 256, 0);
  gt = __builtin_amdgcn_raw_buffer_load_b64(rg, n * 8, 1024, 0);
#pragma unroll
  for (int r = 0; r < 4; ++r) {
#pragma unroll
    for (int k = 0; k < 4; ++k) xv[r][k] = __builtin_amdgcn_raw_buffer_load_b128(rx, t * (DM * 2) + n * 16, r * 4 * (DM * 2) + k * 256, 0);
    xt[r] = __builtin_amdgcn_raw_buffer_load_b64(rx, t * (DM * 2) + n * 8, r * 4 * (DM * 2) + 1024, 0);
  }
  SB();
#pragma unroll
  for (int r = 0; r < 4; ++r) {
    float acc = 0.f;
    auto term = [&](unsigned a, unsigned b) { const unsigned u = pk2(lo16(a) * lo16(b), hi16(a) * hi16(b)); acc += lo16(u) + hi16(u); };
#pragma unroll
    for (int k = 0; k < 4; ++k)
#pragma unroll
      for (int j = 0; j < 4; ++j) term(xv[r][k][j], gw[k][j]);
#pragma unroll
    for (int j = 0; j < 2; ++j) term(xt[r][j], gt[j]);
#pragma unroll
    for (int m = 8; m >= 1; m >>= 1) acc += __shfl_xor(acc, m);
    const float gg = rbf(1.0f / (1.0f + expf(-rbf(acc))));
    if (n == 0) __builtin_amdgcn_raw_buffer_store_b32(__float_as_uint(gg), bk.rs, (t + 4 * r) * 4, bk.at(B_G), 16);
  }
  WAIT_VM(0);
  if (l == 0) flag_put(cx, flag_ptr(p.ws, F_G) + blk, 1u);
}

__device__ __forceinline__ void att_role(const Params& p, unsigned tag, int w, int h, int sa, int sb) {
  CODE_PAD(PAD_ATT);
  char* const ws = p.ws;
  const int l = lane_id(), n = l & 15, g = l >> 4;
  const Ctx cx = {ws, tag};
  Bulk bk;
  bk.init(cx, 4 + (h & 3));
  const unsigned l16 = l * 16, vmo = (n + 16 * (((g & 1) << 1) | (g >> 1))) * 16;
  auto soft = [&](const f32x4* st, int qt, u32x4& px, u32x2& pm) {
    const int nv = qt ? 10 : 6;
    float v[10] = {(g & 1) ? st[0][2] : st[0][0], (g & 1) ? st[0][3] : st[0][1]};
#pragma unroll
    for (int j = 0; j < 4; ++j) {
      v[2 + j] = qt || 4 * g + j <= n ? st[1][j] : -__builtin_inff();
      v[6 + j] = 4 * g + j <= n ? st[2][j] : -__builtin_inff();
    }
    float mx = v[0];
#pragma unroll
    for (int j = 1; j < nv; ++j) mx = fmaxf(mx, v[j]);
    const float c = 0.08838834764831845f * 1.4426950408889634f, nx = -red_max_g(mx) * c;
    float sum = 0.f;
#pragma unroll
    for (int j = 0; j < nv; ++j) { v[j] = __builtin_amdgcn_exp2f(__builtin_fmaf(v[j], c, nx)); sum += v[j]; }
    const float inv = __builtin_amdgcn_rcpf(red_sum_g(sum));
    px = (u32x4){pk2(v[2] * inv, v[3] * inv), pk2(v[4] * inv, v[5] * inv), qt ? pk2(v[6] * inv, v[7] * inv) : 0u, qt ? pk2(v[8] * inv, v[9] * inv) : 0u};
    pm = (u32x2){pk2(v[0] * inv, v[1] * inv), 0u};
  };
  int seenp = 0, seenm = 0;
  auto wait_proj = [&](int target) {
    unsigned spins = 0;
    while (seenp < target) {
      const int v = (int)flag_get(cx, bk.proj_flag(cx, min(l, 3), h)), v2 = __builtin_amdgcn_readlane(v, 3);
      seenp = min(min(max(__builtin_amdgcn_readlane(v, 0), K_FIRST), __builtin_amdgcn_readlane(v, 1)), min(__builtin_amdgcn_readlane(v, 2), v2));
      if (seenp < target) { __builtin_amdgcn_s_sleep(4); if (spin_fail(spins, cx)) break; }
    }
    __builtin_amdgcn_fence(__ATOMIC_ACQUIRE, "wavefront");
  };
  GAS u64* const fm = flag_ptr(ws, F_M + 2 * h + ((l >> 1) & 1)) + (l & 1 & ~(l >> 1));
  auto min8 = [&](u64 f) {
    int v = (unsigned)(f >> 32) == cx.tag ? (int)(unsigned)f : 0;
#pragma unroll
    for (int m = 1; m < 4; m <<= 1) v = min(v, __shfl_xor(v, m));
    return __builtin_amdgcn_readfirstlane(v);
  };
  auto wait_m = [&](int target) {
    unsigned spins = 0;
    while (seenm < target) {
      seenm = min8(__hip_atomic_load(fm, RLX_AGENT));
      if (seenm < target) { __builtin_amdgcn_s_sleep(2); if (spin_fail(spins, cx)) break; }
    }
    __builtin_amdgcn_fence(__ATOMIC_ACQUIRE, "wavefront");
  };
  bf16x8 qn[4][2], kx[4][2], vn[8];
  auto ahead = [&](int s) {
    const unsigned so = (s * NH + h) * 8192, oq = bk.at(B_Q + so), ok = bk.at(B_K + so), ov = bk.at(B_V + so);
#pragma unroll
    for (int k = 0; k < 8; ++k) {
      qn[k >> 1][k & 1] = ldfrag(bk.rs, l16, oq + k * 1024);
      kx[k >> 1][k & 1] = ldfrag(bk.rs, l16, ok + k * 1024);
    }
#pragma unroll
    for (int et = 0; et < 8; ++et) vn[et] = ldfrag(bk.rs, l16, ov + et * 1024);
  };
  bf16x8 km[4];
  u32x2 vm[4];
  auto ldm = [&](int s) {
    const unsigned om = near_head(B_K, h) ^ (~bk.w13 & 8192u) ^ ((s ? s - 1 : 8192) << 14);
#pragma unroll
    for (int ks = 0; ks < 4; ++ks) km[ks] = ldfrag(bk.rs, l16, om + ks * 1024);
#pragma unroll
    for (int k = 0; k < 4; ++k) vm[k] = __builtin_amdgcn_raw_buffer_load_b64(bk.rs, vmo, om + 4096 + k * 1024, 16);
  };
  GAS u64* const fo = flag_ptr(ws, F_O + h) + w;
  if (l == 0) flag_put(cx, fo, sa + w);
  wait_proj(sa + w + 1);
  ahead(sa + w);
  wait_m(sa + w + 1);
  ldm(sa + w);
  bool pend = false, fq = false, gate = true;
  u64 fw = 0;
  for (int s = sa + w; s < sb; s += 4) {
    bf16x8 vx[8];
    u32x2 vc[4];
    f32x4 st[2][3] = {};
#pragma unroll
    for (int ks = 0; ks < 4; ++ks) { st[0][1] = MF(kx[ks][0], qn[ks][0], st[0][1]); st[1][1] = MF(kx[ks][0], qn[ks][1], st[1][1]); st[1][2] = MF(kx[ks][1], qn[ks][1], st[1][2]); }
#pragma unroll
    for (int ks = 0; ks < 4; ++ks) { st[0][0] = MF(km[ks], qn[ks][0], st[0][0]); st[1][0] = MF(km[ks], qn[ks][1], st[1][0]); vc[ks] = vm[ks]; vx[ks] = vn[ks]; vx[ks + 4] = vn[ks + 4]; }
    SB();
    if (fq) {
      seenm = max(seenm, min8(fw));
      __builtin_amdgcn_fence(__ATOMIC_ACQUIRE, "wavefront");
    }
    const bool more = s + 4 < sb, havep = more && seenp > s + 4, have = havep && seenm > s + 4;
    fq = have;
    if (have) {
      ldm(s + 4);
      fw = __hip_atomic_load(fm, RLX_AGENT);
    }
    if (havep) ahead(s + 4);
    SB();
    const unsigned oo = bk.at(B_O + (s * NH + h) * 8192);
#pragma unroll
    for (int qt = 0; qt < 2; ++qt) {
      u32x4 px;
      u32x2 pm;
      soft(st[qt], qt, px, pm);
      const u32x4 pxs = {px[2], px[3], px[0], px[1]};
      f32x4 o[8];
#pragma unroll
      for (int et = 0; et < 8; ++et) {
        const f32x4 z = {};
        o[et] = MF(vx[et], __builtin_bit_cast(bf16x8, (et & 1) ? pxs : px), z);
        o[et] = MF(zext8((u32x2){vc[et >> 1][et & 1], 0u}), zext8(pm), o[et]);
      }
      if (qt == 0 && pend) {
        if (havep) WAIT_VM(24); else WAIT_VM(0);
        if (l == 0) flag_put(cx, fo, s);
      }
#pragma unroll
      for (int k = 0; k < 4; ++k) st128(pack8(o[2 * k], o[2 * k + 1]), bk.rs, l16, oo + qt * 1024 + k * 2048);
    }
    SB();
    pend = true;
    if (!have) {
      pend = false;
      WAIT_VM(0);
      if (l == 0) flag_put(cx, fo, s + 4);
      if (gate && (s >= 20 || !more)) { gate_rows(p, cx, bk, 4 * h + w); gate = false; }
      if (more && !havep) { wait_proj(s + 5); ahead(s + 4); }
      if (more) { wait_m(s + 5); ldm(s + 4); }
    }
  }
}

__device__ __forceinline__ void xprep_role(const Params& p, unsigned tag, int w, int h0) {
  const int l = lane_id(), n = l & 15, g = l >> 4, qt = w & 1, par = w >> 1;
  const Ctx cx = {p.ws, tag};
  Bulk bk;
  bk.init(cx, 4 + (h0 & 3));
  const unsigned l16 = l * 16;
  const unsigned xo = X_AT + ((bk.w13 ^ 8192u) & 8192u);
  for (int i = 0; i < NDIR * 9; ++i)
    st128((u32x4){~0u, ~0u, ~0u, ~0u}, bk.rs, g * 16384 + (h0 + 8 * w) * 256 + n * 16, (bk.at(B_O + (i / 9) * 524288) ^ 8192u) + (i % 9) * 65536);
  for (unsigned spins = 0; __any(flag_get(cx, (l & 4) ? flag_ptr(p.ws, F_Q + h0 + 8 * (l & 3)) : flag_ptr(p.ws, F_Y) + 35 + h0 + 8 * (l & 3)) == 0u) && !spin_fail(spins, cx);) __builtin_amdgcn_s_sleep(8);
  __builtin_amdgcn_fence(__ATOMIC_ACQUIRE, "wavefront");
#pragma unroll 1
  for (int h = h0; h < NH; h += 8) {
  bf16x8 qq[NDIR / 2][4], kk[NDIR / 2][8];
#pragma unroll
  for (int i2 = 0; i2 < NDIR; i2 += 2) {
    const unsigned so = ((SDIR + i2 + par) * NH + h) * 8192, oq = bk.at(B_Q + so) + qt * 1024, ok = bk.at(B_K + so);
#pragma unroll
    for (int ks = 0; ks < 4; ++ks) {
      qq[i2 >> 1][ks] = ldfrag(bk.rs, l16, oq + ks * 2048);
      kk[i2 >> 1][2 * ks] = ldfrag(bk.rs, l16, ok + ks * 2048); kk[i2 >> 1][2 * ks + 1] = ldfrag(bk.rs, l16, ok + ks * 2048 + 1024);
    }
  }
  SB();
#pragma unroll
  for (int i2 = 0; i2 < NDIR; i2 += 2) {
    const int i = i2 + par;
    const unsigned oc = (bk.at(B_O + ((SDIR + i) * NH + h) * 8192) ^ 8192u) + qt * 1024;
    f32x4 a = {}, b = {};
#pragma unroll
    for (int ks = 0; ks < 4; ++ks) {
      const bf16x8 q = qq[i2 >> 1][ks];
      st128(__builtin_bit_cast(u32x4, q), bk.rs, l16, oc + ks * 2048);
      a = MF(kk[i2 >> 1][2 * ks], q, a); b = MF(kk[i2 >> 1][2 * ks + 1], q, b);
    }
    float mx = -__builtin_inff(), sum = 0.f;
#pragma unroll
    for (int j = 0; j < 4; ++j) {
      a[j] = 4 * g + j <= 16 * qt + n ? a[j] : -__builtin_inff(); b[j] = 16 + 4 * g + j <= 16 * qt + n ? b[j] : -__builtin_inff();
      mx = fmaxf(mx, fmaxf(a[j], b[j]));
    }
    const float c = 0.08838834764831845f * 1.4426950408889634f, nx = -red_max_g(mx) * c;
#pragma unroll
    for (int j = 0; j < 4; ++j) { a[j] = __builtin_amdgcn_exp2f(__builtin_fmaf(a[j], c, nx)); b[j] = __builtin_amdgcn_exp2f(__builtin_fmaf(b[j], c, nx)); sum += a[j] + b[j]; }
    sum = red_sum_g(sum);
    const unsigned eo = xo + 0x100000 + (i * 4 + 2 * qt + (n >> 3)) * 32768 + (h >> 4) * 16384 + (h & 15) * 512 + (g >> 1) * 128 + (n & 7) * 16 + (g & 1) * 8;
    __builtin_amdgcn_raw_buffer_store_b64((u32x2){pk2(a[0], a[1]), pk2(a[2], a[3])}, bk.rs, eo, 0, 16);
    __builtin_amdgcn_raw_buffer_store_b64((u32x2){pk2(b[0], b[1]), pk2(b[2], b[3])}, bk.rs, eo, 256, 16);
    if (g == 0) __builtin_amdgcn_raw_buffer_store_b64((u32x2){__float_as_uint(nx), __float_as_uint(sum)}, bk.rs, h * 256 + (16 * qt + n) * 8, xo + 0x80000 + i * 65536, 16);
    __builtin_amdgcn_raw_buffer_store_b64((u32x2){0xFFFFFFFFu, 0xFFFFFFFFu}, bk.rs, (2 * qt + (g >> 1)) * 16384 + ((g & 1) ? 4096 + c_line(h) : h * 128) + n * 8, xo + i * 65536, 16);
  }
  }
  WAIT_VM(0);
  BARRIER();
  if (l == 0) flag_put(cx, flag_ptr(p.ws, F_S + h0 + 8 * w), 1u);
}

struct OutIn {
  unsigned l16, rw, rr, fo, wv0, wv1, wv2, tro, l8, rw0, rr0;
  const void *ws, *Wo;
  unsigned w, tag, o_w, o_o, o_a, f_a, o_ctl, ns;
};
__device__ __forceinline__ void op_role(const Params& p, unsigned tag, int w, int ct, int kq, int grp, bool late) {
  const Ctx cx = {p.ws, tag};
  Bulk bk;
  bk.init(cx, grp);
  const int l = lane_id(), g = l >> 4, q4 = (l >> 2) & 3, p4 = l & 3, h0 = kq * 8 + w * 2;
  char* const base = comm_base(p.ws) - CO;
  const unsigned dl = (unsigned)(p.ws - base);
  OutIn oi;
  oi.l16 = l * 16; oi.l8 = l * 8;
  oi.rw = 65536 + w * 12288 + l * 16;
  oi.rr = 65536 + w * 3072 + l * 16;
  oi.fo = dl + (F_O + h0 + ((l >> 2) & 1)) * 128 + (l & 3) * 8;
  auto piece = [&](int i) { const int idx = i * 64 + l, row = idx / 12; return row * (DM * 2) + (idx - row * 12) * 16; };
  oi.wv0 = piece(0); oi.wv1 = piece(1); oi.wv2 = piece(2);
  oi.tro = w * 24576 + (4 * g + q4) * 192 + p4 * 8;
  oi.ws = base; oi.Wo = p.Wo; oi.w = w; oi.tag = __builtin_amdgcn_readfirstlane(tag); oi.o_ctl = dl;
  oi.ns = __builtin_amdgcn_readfirstlane(late ? p.ob0 | p.ob1 << 8 : p.ob0 ? p.ob0 << 8 | p.ob1 << 16 | SDIR << 24 : p.ob1 | SDIR << 8);
  oi.rw0 = oi.rw; oi.rr0 = oi.rr;
  oi.o_w = __builtin_amdgcn_readfirstlane((kq * 1024 + w * 256) * (DM * 2) + ct * 192);
  oi.o_o = __builtin_amdgcn_readfirstlane(bk.w13 + 2 * B_O + h0 * 16384);
  oi.o_a = __builtin_amdgcn_readfirstlane(bk.w13 + 2 * B_AP + (ct * 4 + kq) * 16384 + w * 2048);
  oi.f_a = __builtin_amdgcn_readfirstlane(w ? 0x7F000000u : dl + (F_A + ct * 4 + kq) * 128 + (late ? 24 * 128 : 0));
  asm volatile(O_ASM_TEXT : O_ASM_VOPS : O_ASM_SOPS : O_ASM_CLOB);
}

typedef __attribute__((ext_vector_type(16))) float f32x16;
constexpr int S4_PRM = 0;
constexpr int S4_KD = S4_PRM + 2560;
constexpr int S4_KH = S4_KD + 2624;
constexpr int S4_U = S4_KH + 5200;
constexpr int S4_USTR = 4352 + 64;
constexpr int S4_WSZ = S4_U + 4 * S4_USTR;
constexpr int S4_RAW = 4 * S4_WSZ;

#define S4M(i) do {} while (0)
struct S4In { u32x2 u[8]; unsigned la, ai, cr, ci, dt; };
__device__ __forceinline__ float s4_params(LAS char* my, const S4In& in) {
  const int l = lane_id();
  const u16 pl_[2] = {(u16)in.la, (u16)(in.la >> 16)}, pa_[2] = {(u16)in.ai, (u16)(in.ai >> 16)}, pr_[2] = {(u16)in.cr, (u16)(in.cr >> 16)}, pi_[2] = {(u16)in.ci, (u16)(in.ci >> 16)};
  const float dt = rbf(expf(bf2f((u16)in.dt)));
  f32x4 pr[2];
#pragma unroll
  for (int k = 0; k < 2; ++k) {
    const float a = -rbf(expf(bf2f(pl_[k]))) * dt;
    const float b = bf2f(pa_[k]) * dt;
    const float c_hi = 0.15915494f, c_lo = (float)(0.15915494309189535 - (double)0.15915494f);
    const float bh = b * c_hi;
    const float bl = __builtin_fmaf(b, c_hi, -bh) + b * c_lo;
    const float cr = bf2f(pr_[k]), ci = bf2f(pi_[k]);
    pr[k] = (f32x4){a * 1.4426950408889634f, bh, bl, log2f(hypotf(cr, ci))};
    *(LAS float*)(my + S4_PRM + 2048 + (l + 64 * k) * 4) = -atan2f(ci, cr) * c_hi;
  }
  float top = fmaxf(fmaxf(pr[0][3], pr[1][3]), -100.f);
#pragma unroll
  for (int m = 1; m < 64; m <<= 1) top = fmaxf(top, __shfl_xor(top, m));
  top = ceilf(top);
#pragma unroll
  for (int k = 0; k < 2; ++k) { pr[k][3] += 14.f - top; *(LAS f32x4*)(my + S4_PRM + (l + 64 * k) * 16) = pr[k]; }
  return exp2f(top - 27.f) * dt;
}
typedef __attribute__((ext_vector_type(2))) _Float16 f16x2;
typedef __attribute__((ext_vector_type(8))) _Float16 f16x8;
__device__ __forceinline__ void s4_modes(LAS char* my, int q0, int q1, f32x4 (&D)[3]) {
  const int l = lane_id(), i = l & 15, kk = l >> 4;
  const f32x2 pe[2] = {{(float)(8 * i), (float)(128 * (i >> 3) + (i & 7))},
                       {(float)(i < 8 ? 256 + 128 * (i >> 2) + 2 * (i & 3) : 2048), (float)(i < 8 ? 512 + 128 * (i >> 1) + 4 * (i & 1) : 128 * i)}};
  const f32x2 pp[2] = {{-pe[0][0], pe[0][1]}, pe[1]};
#pragma unroll 1
  for (int q = q0; q < q1; ++q) {
    u32x4 op[4];
    const f32x4 pc = *(LAS f32x4*)(my + S4_PRM + 2048 + (4 * q + kk) * 16);
#pragma unroll
    for (int j = 0; j < 4; ++j) {
      const f32x4 pr = *(LAS f32x4*)(my + S4_PRM + (16 * q + 4 * kk + j) * 16);
#pragma unroll
      for (int m = 0; m < 2; ++m) {
        const f32x2 ea = __builtin_elementwise_fma((f32x2){pr[0], pr[0]}, pe[m], (f32x2){m ? 14.f : pr[3], 14.f});
        const f32x2 t = pp[m] * pr[1];
        f32x2 fr = __builtin_elementwise_fma((f32x2){pr[1], pr[1]}, pp[m], -(f32x2){__builtin_rintf(t[0]), __builtin_rintf(t[1])});
        fr = __builtin_elementwise_fma((f32x2){pr[2], pr[2]}, pp[m], fr);
        if (!m) fr[0] += pc[j];
#pragma unroll
        for (int h = 0; h < 2; ++h) {
          const f32x2 v = (f32x2){__builtin_amdgcn_cosf(fr[h]), __builtin_amdgcn_sinf(fr[h])} * __builtin_amdgcn_exp2f(ea[h]);
          op[2 * m + h][j] = __builtin_bit_cast(unsigned, __builtin_convertvector(v, f16x2));
        }
      }
    }
#pragma unroll
    for (int m = 0; m < 3; ++m) D[m] = __builtin_amdgcn_mfma_f32_16x16x32_f16(__builtin_bit_cast(f16x8, op[m + 1]), __builtin_bit_cast(f16x8, op[0]), D[m], 0, 0, 0);
  }
}
__device__ __forceinline__ void s4d_task(const Ctx& cx, const Bulk& bk, unsigned yflip, int gentle, int w, int task, char* smem, const S4In& in, char* ws, int q0, f32x4 (&D)[3], float ksc) {
  const int l = lane_id(), tid = w * 64 + l;
  LAS char* const lds = (LAS char*)smem;
  LAS char* const my = lds + w * S4_WSZ;
  S4M(2);
  s4_modes(my, q0, 8, D);
  {
    const int i = l & 15, kk = l >> 4;
#pragma unroll
    for (int r = 0; r < 4; ++r) {
      const int row = 4 * kk + r;
      *(LAS float*)(my + S4_KD + (128 * (row >> 3) + (row & 7) + 8 * i) * 4) = ksc * D[0][r];
      *(LAS float*)(my + S4_KD + (row < 8 ? 256 + 64 * kk + r + 4 * i : 640 + i) * 4) = ksc * D[1][r];
      *(LAS float*)(my + S4_KD + (row < 8 ? 384 + 32 * (row >> 1) + (row & 1) + 2 * i : 384 + 16 * row + i) * 4) = ksc * D[2][r];
    }
  }
  S4M(3);
#pragma unroll
  for (int k = 0; k < 2; ++k)
#pragma unroll
    for (int j = 0; j < 8; j += 4) *(LAS u32x4*)(lds + S4_RAW + k * 8208 + tid * 32 + j * 4) = (u32x4){in.u[j][k], in.u[j + 1][k], in.u[j + 2][k], in.u[j + 3][k]};
  BARRIER();
  u32x2 r[11];
#pragma unroll
  for (int k = 0; k < 2; ++k)
#pragma unroll
    for (int j = 0; j < 12; j += 4) {
      const u32x4 v = *(LAS u32x4*)(lds + S4_RAW + k * 8208 + tid * 32 + j * 4);
#pragma unroll
      for (int i = 0; i < 4; ++i) if (j + i < 11) r[j + i][k] = v[i];
    }
#pragma unroll
  for (int ch = 0; ch < 4; ++ch) {
    unsigned pr[10];
#pragma unroll
    for (int j = 0; j < 10; ++j) pr[j] = __builtin_amdgcn_perm(r[j + 1][ch >> 1], r[j][ch >> 1], (ch & 1) ? 0x07060302u : 0x05040100u);
    if (tid == 255) { pr[7] &= 0xFFFFu; pr[8] = 0u; pr[9] = 0u; }
    LAS char* ub = lds + ch * S4_WSZ + S4_U + 64 + tid * 16;
#pragma unroll
    for (int sft = 0; sft < 4; ++sft) *(LAS u32x4*)(ub + sft * S4_USTR) = (u32x4){pr[sft], pr[sft + 2], pr[sft + 4], pr[sft + 6]};
    if (tid < 4) {
#pragma unroll
      for (int sft = 0; sft < 4; ++sft) {
        const u32x4 v = {0u, 0u, sft == 3 ? pr[0] << 16 : 0u, sft == 0 ? 0u : sft == 1 ? pr[0] << 16 : pr[sft - 2]};
        *(LAS u32x4*)(lds + ch * S4_WSZ + S4_U + sft * S4_USTR + (3 - tid) * 16) = tid == 0 ? v : (u32x4){0u, 0u, 0u, 0u};
      }
    }
  }
  {
    if (l < 5) {
      *(LAS u32x4*)(my + S4_KH + l * 16) = (u32x4){0u, 0u, 0u, 0u};
    }
#pragma unroll
    for (int k = 0; k < 4; ++k) {
      const int grp = l + 64 * k;
      float v[8];
#pragma unroll
      for (int j = 0; j < 8; j += 2) {
        const unsigned c = pk2((float)(8 * grp + j), (float)(8 * grp + j + 1)) - 0x42804280u;
        const bool low = k == 0 && grp < 16;
        v[j] = *(LAS float*)(my + S4_KD + (low ? 8 * grp + j : c & 0xFFFFu) * 4);
        v[j + 1] = *(LAS float*)(my + S4_KD + (low ? 8 * grp + j + 1 : c >> 16) * 4);
      }
      *(LAS u32x4*)(my + S4_KH + 80 + (grp >> 2) * 80 + (grp & 3) * 16) = (u32x4){pk2(v[7], v[6]), pk2(v[5], v[4]), pk2(v[3], v[2]), pk2(v[1], v[0])};
    }
  }
  __syncthreads();
  S4M(4);
  {
    const int r = l & 31, hh = l >> 5, sft = (r + 1) & 3;
    unsigned ua = w * S4_WSZ + S4_U + sft * S4_USTR + (r + 25 - sft - 8 * hh - 16) * 2;
    const unsigned kz = w * S4_WSZ + S4_KH + hh * 16;
    unsigned kb1 = kz + (33 + r) * 80;
    unsigned kb0 = kz + (1 + r) * 80;
    f32x16 acc0 = {}, acc1 = {};
    unsigned ub = ua + 72;
    asm volatile("" : "+v"(ub));
    struct Ops { bf16x8 a0, a1, b0, b1; };
    auto ld = [&](int ks) {
      Ops o;
      const unsigned uo = ua + (1 - ks) * 32;
      o.a0 = cat2(*(LAS bf16x4*)(lds + uo), *(LAS bf16x4*)(lds + uo + 8));
      o.a1 = cat2(*(LAS bf16x4*)(lds + uo + 2048), *(LAS bf16x4*)(lds + uo + 2056));
      o.b0 = *(LAS bf16x8*)(lds + kb0 + ks * 32);
      o.b1 = *(LAS bf16x8*)(lds + kb1 + ks * 32);
      return o;
    };
    auto step = [&](const Ops& o, Ops& nx, int ks) {
      const unsigned uo = ua + (1 - ks) * 32, up = ub + (1 - ks) * 32;
      acc1 = __builtin_amdgcn_mfma_f32_32x32x16_bf16(o.a0, o.b1, acc1, 0, 0, 0); SB();
      nx.a0 = cat2(*(LAS bf16x4*)(lds + uo), *(LAS bf16x4*)(lds + up)); nx.b1 = *(LAS bf16x8*)(lds + kb1 + ks * 32); SB();
      acc0 = __builtin_amdgcn_mfma_f32_32x32x16_bf16(o.a0, o.b0, acc0, 0, 0, 0); SB();
      nx.b0 = *(LAS bf16x8*)(lds + kb0 + ks * 32);
      nx.a1 = cat2(*(LAS bf16x4*)(lds + uo + 2048), *(LAS bf16x4*)(lds + up + 2048));
      if (ks) {
        ub += 64;
        ua += 64;
        kb1 -= 80;
        kb0 = max(kb0 - 80, kz);
      }
      SB();
      acc1 = __builtin_amdgcn_mfma_f32_32x32x16_bf16(o.a1, o.b0, acc1, 0, 0, 0); SB();
    };
    Ops e0 = ld(0), e1 = ld(1), o0, o1;
    ua += 64; kb1 -= 80; kb0 = max(kb0 - 80, kz);
#pragma unroll 1
    for (int D = 0; D < 32; D += 2) {
      step(e0, o0, 0);
      step(e1, o1, 1);
      step(o0, e0, 0);
      step(o1, e1, 1);
      sleep_n(gentle);
    }
    S4M(5);
#pragma unroll
    for (int j = 0; j < 4; ++j) {
      const unsigned off = (32 * r + 8 * j + 4 * hh) * 2;
      *(LAS u32x2*)(my + off) = (u32x2){pk2(acc0[4 * j], acc0[4 * j + 1]), pk2(acc0[4 * j + 2], acc0[4 * j + 3])};
      *(LAS u32x2*)(my + off + 2048) = (u32x2){pk2(acc1[4 * j], acc1[4 * j + 1]), pk2(acc1[4 * j + 2], acc1[4 * j + 3])};
    }
    __syncthreads();
#pragma unroll
    for (int j = 0; j < 2; ++j) {
      u32x2 c[4];
#pragma unroll
      for (int k = 0; k < 4; ++k) c[k] = *(LAS u32x2*)(lds + k * S4_WSZ + (j * 256 + tid) * 8);
      const unsigned o = bk.at(B_YT + task * 16384 + j * 8192) ^ yflip;
#pragma unroll
      for (int k = 0; k < 2; ++k)
        st128((u32x4){__builtin_amdgcn_perm(c[1][k], c[0][k], 0x05040100u), __builtin_amdgcn_perm(c[3][k], c[2][k], 0x05040100u),
                      __builtin_amdgcn_perm(c[1][k], c[0][k], 0x07060302u), __builtin_amdgcn_perm(c[3][k], c[2][k], 0x07060302u)}, bk.rs, tid * 32 + k * 16, o);
    }
  }
  __syncthreads();
  S4M(6);
}

__device__ __forceinline__ void warm_l2(const u16* W, unsigned off, bool rows256, int w) {
  const rsrc_t rw = mk_rsrc(W, (size_t)DM * HD * 2);
  const int l = lane_id();
  const unsigned vo = rows256 ? (l >> 4) * (HD * 2) + (l & 15) * 16 : l * 16, st = rows256 ? 4 * HD * 2 : 1024;
  u32x4 v[36];
#pragma unroll
  for (int i = 0; i < 36; ++i) v[i] = __builtin_amdgcn_raw_buffer_load_b128(rw, vo, off + (i * 4 + w) * st, 0);
#pragma unroll
  for (int i = 0; i < 36; ++i) asm volatile("" :: "v"(v[i]));
}

__device__ __forceinline__ void x_ahead(const Params& p, int w, int idx) {
  const rsrc_t rx = mk_rsrc(p.x, (size_t)SL * DM * 2);
  for (int i = 0; i < p.xpre; ++i) dma16(rx, lane_id() * 16, ((((idx - 35) >> 2) * 4 + w) + i * 32) * 1024, LDS_BYTES - 1024);
}
__device__ __forceinline__ unsigned s4_role(const Params& p, int w, int idx, char* smem) {
  CODE_PAD(PAD_S4);
  char* const ws = p.ws;
  const int l = lane_id(), tid = w * 64 + l;
  const bool near = idx >= 8 && idx < 35;
  const int r_ = idx - 8, t0 = 5 * r_ + (r_ + 2) / 3;
  const int t1 = t0 + 5 + (r_ % 3 == 0) - p.s4skip;
  const rsrc_t rx = mk_rsrc(p.x, (size_t)SL * DM * 2);
  S4In in[6];
#pragma unroll
  for (int k = 0; k < 6; ++k) {
    const int d = min(t0 + k, t1 - 1) * 4 + w, o = d * (NST / 2) + l;
    in[k].la = ((const unsigned*)p.lar)[o]; in[k].ai = ((const unsigned*)p.aim)[o]; in[k].cr = ((const unsigned*)p.cre)[o]; in[k].ci = ((const unsigned*)p.cim)[o];
    in[k].dt = __builtin_amdgcn_raw_buffer_load_b16(mk_rsrc(p.ldt, DM * 2), d * 2, 0, 0);
  }
  const unsigned gen = gen_load(ws);
  LAS char* const my = (LAS char*)smem + w * S4_WSZ;
  float ksc = s4_params(my, in[0]);
  f32x4 D[3] = {};
  const int qs = near ? p.s4q : p.s4f;
  s4_modes(my, 0, qs, D);
  const Ctx cx = {ws, __builtin_amdgcn_readfirstlane(gen) + 1u};
  Bulk bk;
  bk.init(cx, idx & 7);
  for (int m = 4 * t0; m < 4 * t1; ++m) st128((u32x4){0u, 0u, 0u, 0u}, bk.rs, tid * 16, bk.at(B_YT + m * 4096) ^ 8192u);
#pragma unroll
  for (int m = 0; m < 6; ++m)
#pragma unroll
    for (int j = 0; j < 8; ++j) in[m].u[j] = __builtin_amdgcn_raw_buffer_load_b64(rx, t0 + m < t1 ? (tid * 8 + j) * (DM * 2) : 0x7F000000u, (t0 + m) * 8, 0);
  {
    sleep_n((r_ % 3 ? r_ % 7 : 0) * p.stag);
  }
  s4_modes(my, qs, 8, D);
#pragma unroll 1
  for (int k = t0; k < t1; ++k) {
    s4d_task(cx, bk, near ? 8192u : 0u, near ? p.gentle : 0, w, k, smem, in[0], ws, k == t0 ? 8 : 0, D, ksc);
    in[0] = in[1]; in[1] = in[2]; in[2] = in[3]; in[3] = in[4]; in[4] = in[5];
    ksc = s4_params(my, in[0]);
    D[0] = D[1] = D[2] = (f32x4){0.f, 0.f, 0.f, 0.f};
  }
  WAIT_VM(0);
  BARRIER();
  if (tid == 0) flag_put(cx, flag_ptr(ws, F_Y) + idx, 1u);
  return cx.tag;
}
struct FinIn {
  unsigned sidx, cidx, l16, ae0, ae1, ae2, ae3, am, lw, lr, oo, d0;
  float g[NDIR], b[NDIR], c[NDIR];
  const void *ws, *out;
  unsigned tag, o_u, o_e, o_x, o_va, o_tw, o_ctl, pa, w, f_w, dret;
};
__device__ __forceinline__ void watch_role(const Params& p, unsigned tag) {
  const int l = lane_id();
  const Ctx cx = {p.ws, tag};
  for (unsigned spins = 0; __any(flag_get(cx, flag_ptr(p.ws, F_Y) + min(8 + l, NS4 - 1)) == 0u) && !spin_fail(spins, cx);) __builtin_amdgcn_s_sleep(30);
  wait_all<30>(cx, F_G, 64);
  wait_all<30>(cx, F_G + 4, 64);
  unsigned spins = 0;
  while (__any(flag_get(cx, flag_ptr(p.ws, F_S + (l & 31))) == 0u) && !spin_fail(spins, cx)) __builtin_amdgcn_s_sleep(30);
  publish(cx, 0, F_W, 1);
#pragma unroll 1
  for (int i = 0; i < NDIR; ++i) {
    GAS u64* const f = flag_ptr(p.ws, F_U + (l & 31)) + (l >> 5) * 2;
    while (__any((int)min(flag_get(cx, f), flag_get(cx, f + 1)) <= SDIR + i) && !spin_fail(spins, cx)) __builtin_amdgcn_s_sleep(8);
    publish(cx, 0, F_W + 1 + i, 1);
  }
}
struct FinX { unsigned x[NDIR], d; };
__device__ __forceinline__ int fin_col(int T) { const int l = lane_id(); return T * 16 + 8 * ((l >> 3) & 1) + 2 * (l & 3); }
__device__ __forceinline__ int fin_row(int w) { const int l = lane_id(); return SDIR * 32 + 8 * w + 2 * (l >> 4) + ((l >> 2) & 1); }
__device__ __forceinline__ FinX fin_x(const Params& p, int w, int T) {
  FinX f;
#pragma unroll
  for (int i = 0; i < NDIR; ++i) f.x[i] = *(const unsigned*)(p.x + (size_t)(fin_row(w) + 32 * i) * DM + fin_col(T));
  f.d = *(const unsigned*)(p.dsk + fin_col(T));
  return f;
}
__device__ __forceinline__ void fin_role(const Params& p, unsigned tag, int w, int T, const FinX& fx) {
  char* const ws = p.ws;
  const int l = lane_id(), n = l & 15, g = l >> 4;
  const Ctx cx = {ws, tag};
  Bulk bk;
  bk.init(cx, T & 7);
  bk.pl.load(cx, T & 7);
  const Place& pl = bk.pl;
  FinIn fi;
  if (w == 0) wait_all<30>(cx, F_W, 1);
  BARRIER();
  const int d = fin_col(T), r0 = fin_row(w);
  float (&gg)[NDIR] = fi.g, (&b0)[NDIR] = fi.b, (&b1)[NDIR] = fi.c;
#pragma unroll
  for (int i = 0; i < NDIR; ++i) {
    const int t = r0 + 32 * i;
    gg[i] = __uint_as_float(__builtin_amdgcn_raw_buffer_load_b32(bk.rs, t * 4, bk.at(B_G), 16));
    const unsigned yv = __builtin_amdgcn_raw_buffer_load_b32(bk.rs, y_at(bk, d >> 2, t * 8 + (d & 2) * 2), 0, 16);
    const unsigned xv = fx.x[i], dv = fx.d;
    b0[i] = rbf(rbf(1.0f - gg[i]) * rbf(lo16(yv) + rbf(lo16(xv) * lo16(dv)))); b1[i] = rbf(rbf(1.0f - gg[i]) * rbf(hi16(yv) + rbf(hi16(xv) * hi16(dv))));
  }
#pragma unroll
  for (int i = 0; i < NDIR; ++i) asm volatile("" : "+v"(b0[i]), "+v"(b1[i]));
  char* const base = comm_base(ws) - CO;
  fi.sidx = (n & 8) ? 0xEEEEu : 0x4444u;
  fi.l16 = l * 16; fi.d0 = 0;
  fi.cidx = (n & 3) * 0x5555u;
  auto ae = [&](int j) { return g <= j ? j * 32768 + (n >> 3) * 512 + g * 128 + (n & 7) * 16 : 0x7F000000u; };
  fi.ae0 = ae(0); fi.ae1 = ae(1); fi.ae2 = ae(2); fi.ae3 = ae(3);
  fi.am = w * 1024 + l * 16;
  fi.lw = (w >> 1) * 512 + (2 * (g & 1) + (g >> 1)) * 128 + (n >> 1) * 16 + (w & 1) * 8 + (n & 1) * 4;
  fi.lr = w * 2048 + ((l >> 2) & 1) * 1024 + g * 128 + (((l >> 3) & 1) * 4 + (l & 3)) * 16;
  fi.oo = (r0 * DM + d) * 2;
  fi.f_w = (unsigned)(ws - base) + (F_W + 1) * 128;
  fi.ws = base; fi.out = p.out; fi.tag = __builtin_amdgcn_readfirstlane(tag); fi.o_ctl = (unsigned)(ws - base);
  fi.o_u = __builtin_amdgcn_readfirstlane(u_tile(T, w, 0) ^ (~bk.w13 & 8192u));
  fi.o_x = X_AT + ((bk.w13 ^ 8192u) & 8192u);
  fi.o_e = __builtin_amdgcn_readfirstlane(fi.o_x + 0x100000 + (w >> 1) * 16384 + (w & 1) * 4096);
  fi.o_va = __builtin_amdgcn_readfirstlane((bk.w13 ^ 8192u) + 2 * B_O + T * 16384 + w * 2048);
  fi.o_tw = __builtin_amdgcn_readfirstlane(CO + TW_OFF + 1024 + (16384u ^ pl.dyn(T & 3)));
  fi.dret = p.dret;
  fi.pa = p.xt0 | (p.xt1 << 16) | (T == 0 || T >= 28 ? 32768 : 0); fi.w = w;
  asm volatile(F_ASM_TEXT : F_ASM_VOPS : F_ASM_SOPS : F_ASM_CLOB);
  if (T == 0 && w == 0 && lane_id() == 0) __hip_atomic_store((GAS unsigned*)flag_ptr(ws, F_GEN), tag, RLX_AGENT);
}

__device__ __forceinline__ void lines_ahead(const Params& p, int w, int h) {
  const int i = h * 4 + w, l = lane_id();
  const rsrc_t ro = mk_rsrc(p.out, (size_t)SL * DM * 2), rc = mk_rsrc(p.ws, CTL_BYTES), rb = mk_rsrc(comm_base(p.ws) - CO, BASE_BYTES);
  const unsigned z0 = __builtin_amdgcn_raw_buffer_load_b32(rc, l * 128, i * 8192, 16);
  const unsigned z1 = __builtin_amdgcn_raw_buffer_load_b32(rb, l * 128, (w & 2 ? PB + 2 * B_G : near_head(B_K, h) ^ (8192u << 14)) ^ ((w & 1) * 8192), 16);
  if (p.wl2 & 2)
    for (int k = 0; k < 18; ++k) st128((u32x4){0u, 0u, 0u, 0u}, ro, l * 16, (i * 18 + k) * 1024);
  asm volatile("" :: "v"(z0), "v"(z1));
}
__device__ __forceinline__ void code_ahead(int i, int n) {
  const char* pc;
  unsigned back, len;
  asm volatile("s_getpc_b64 %0\n.Lcp_%=:\n\ts_mov_b32 %1, .Lcp_%=-_Z6k_mega6Params\n\ts_mov_b32 %2, k_mega_end-_Z6k_mega6Params" : "=s"(pc), "=s"(back), "=s"(len));
  const rsrc_t rc = mk_rsrc(pc - back, len);
  for (unsigned o = i * 1024; o < len; o += n * 1024) dma16(rc, lane_id() * 16, o, LDS_BYTES - 1024);
  WAIT_VM(0);
}

__global__ void __launch_bounds__(256, 1) k_mega(Params p) {
  extern __shared__ __attribute__((aligned(16))) char smem[];
  const int x = blockIdx.x & 7, j = blockIdx.x >> 3;
  const int w = __builtin_amdgcn_readfirstlane(threadIdx.x >> 6);
  if (w == 1 && blockIdx.x != 128 && ((p.tlb >> (x < 4 ? (j < 16 ? 0 : j < 25 ? 1 : 2) : (j & 24) == 16 ? 4 : 3)) & 1)) touch_pages(p.ws, j);
  if (x < 4 && (j < 16 || j > 24)) sleep_n(x ? p.v0 : p.s0);
  if (x < 4 && j < 8) { chain_role<true>(p, w, x + 4 * j, x); return; }
  else if (x < 4 && j < 16) { chain_role<false>(p, w, x + 4 * (j - 8), x); return; }
  else if (x < 4 && j < 25) red_role(p, w, x + 4 * (j - 16), smem);
  else if (x == 0 && j == 25) {
    if (w == 0) keep_role(p);
    else if (w == 1) first_role(p);
    else if (p.cpf & 4) code_ahead(w - 1, 3);
  }
  else {
    unsigned tag;
    int T = -1, opl = -1, mix = -1, ms0 = 0, mds = 1, ms1 = p.mixs;
    FinX fx;
    if (x >= 4 && j < 16) {
      const int h = (x & 3) + 4 * (j & 7);
      tag = j >= 8 ? proj_role<false>(p, w, h) : proj_role<true>(p, w, h);
      if (j >= 8 && h < 24) opl = h, mix = h >> 2, ms0 = p.ob0 + (h & 3), mds = 4, ms1 = p.ob1;
      else if (h < 30) mix = h % 6, ms0 = p.mixs + (j < 8 ? 1 + h / 6 : 0), mds = 6, ms1 = SDIR;
      if (j >= 8 && h >= 30) { T = (h - 30) * 35; fx = fin_x(p, w, T); }
    } else {
      if (x >= 4 && j < 24) {
        const int h = (x & 3) + 4 * (j & 7);
        lines_ahead(p, w, h);
        if (p.cpf & 1) code_ahead((j & 7) * 4 + w, 32);
        if (p.wl2 & 1) warm_l2(p.Wo, h * DH * DM * 2, false, w);
        x_ahead(p, w, 35 + h);
        WAIT_VM(0);
        BARRIER();
        if (U_THIRD) proj_role<true, 2>(p, w, h);
        tag = proj_role<false, 1>(p, w, h);
        if (w == 0 && lane_id() == 0) flag_put({p.ws, tag}, flag_ptr(p.ws, F_Y) + 35 + h, 1u);
        att_role(p, tag, w, h, 0, SDIR);
      } else {
        if (x >= 4) {
          tag = proj_role<true, 1>(p, w, (x & 3) + 4 * (j & 7));
          if (j >= 30) xprep_role(p, tag, w, (x & 3) + 4 * (j & 1));
        }
        if (x >= 4 && j < 30) opl = (j - 24) * 4 + (x & 3);
        else {
          if (x < 4 && j >= 26 && (p.cpf & 2)) code_ahead((j - 26) * 4 + w, 24);
          T = x < 4 ? (j - 25) * 4 + x : (j - 23) * 4 + (x & 3);
          fx = fin_x(p, w, T);
#pragma unroll
          for (int i = 0; i < NDIR; ++i) asm volatile("" : "+v"(fx.x[i]), "+v"(fx.d));
          if (x < 4) tag = s4_role(p, w, 7 + x * 7 + (j - 25), smem);
          if (T == 35) { if (w == 0) watch_role(p, tag); T = -1; }
          if (T > 28 && p.mixs) mix = T - 29;
        }
      }
    }
    if (opl >= 0) op_role(p, tag, w, opl >> 2, opl & 3, x, j < 16);
    if (mix >= 0) mix_role(p, tag, w, mix, ms0, mds, ms1, opl >= 0);
    if (T >= 0) fin_role(p, tag, w, T, fx);
  }
}

extern "C" __device__ __attribute__((used, noinline, visibility("hidden"))) void k_mega_end() {}

extern "C" void kernel_launch(void* const* d_in, const int* in_sizes, int n_in, void* d_out, int out_size, void* d_ws, size_t ws_size,
                              hipStream_t stream) {
  Params p;
  p.x = (const u16*)d_in[0]; p.mem0 = (const u16*)d_in[1];
  p.Wq = (const u16*)d_in[2]; p.Wk = (const u16*)d_in[3]; p.Wv = (const u16*)d_in[4]; p.Wo = (const u16*)d_in[5];
  p.lar = (const u16*)d_in[8]; p.aim = (const u16*)d_in[9]; p.cre = (const u16*)d_in[10]; p.cim = (const u16*)d_in[11];
  p.ldt = (const u16*)d_in[12]; p.dsk = (const u16*)d_in[13]; p.gw = (const u16*)d_in[14];
  p.out = (u16*)d_out; p.ws = (char*)d_ws;
  (void)hipFuncSetAttribute((const void*)k_mega, hipFuncAttributeMaxDynamicSharedMemorySize, LDS_BYTES);
  {
    k_mega<<<256, 256, LDS_BYTES, stream>>>(p);
  }
}
```

```cpp
#include <hip/hip_runtime.h>
#include <hip/hip_bf16.h>
#include <stdint.h>
#include <type_traits>

typedef __attribute__((ext_vector_type(2))) __bf16 bf16x2;
typedef __attribute__((ext_vector_type(4))) __bf16 bf16x4;
typedef __attribute__((ext_vector_type(8))) __bf16 bf16x8;
typedef __attribute__((ext_vector_type(2))) float f32x2;
typedef __attribute__((ext_vector_type(4))) float f32x4;
typedef __attribute__((ext_vector_type(2))) unsigned u32x2;
typedef __attribute__((ext_vector_type(4))) unsigned u32x4;
typedef unsigned short u16;
typedef __amdgpu_buffer_rsrc_t rsrc_t;
#define LAS __attribute__((address_space(3)))
#define GAS __attribute__((address_space(1)))

constexpr int SL = 2048, DM = 576, NH = 32, DH = 128, HD = 4096, NSEG = 64, NST = 128;

constexpr size_t WS_CTL = 0;
constexpr int CTL_BYTES = 65536;
constexpr int F_GEN = 0, F_TMO = 1, F_GO = 2, F_XCC = 10, F_PRB = 14, F_INITW = 16, F_Y = 20;
constexpr int F_U = 320;
constexpr int F_W = 360;
constexpr int F_R = 300;
constexpr int F_M = 65, F_O = 129, F_A = 200, F_Q = 256;
constexpr int W_VO = 1, W_PX = 19, W_PM = 23, W_PC = 24, W_MEM = 25;
constexpr int N_DUR = 0, N_K = 32, N_U = 64, N_RD = 96;
constexpr size_t COMM_BYTES = (size_t)8 << 20;
constexpr unsigned FR_BYTES = SL * HD * 2;
constexpr unsigned B_K = 0;
constexpr unsigned B_V = FR_BYTES;
constexpr unsigned B_U = FR_BYTES;
constexpr unsigned B_O = 3 * FR_BYTES;
constexpr unsigned B_Q = 4 * FR_BYTES;
constexpr unsigned B_AP = 5 * FR_BYTES + NH * 8192;
constexpr unsigned B_YT = B_AP + NSEG * 24 * 16384;
constexpr unsigned B_G = B_YT + DM * SL * 2;
constexpr int SDIR = 58, NDIR = NSEG - SDIR;
constexpr unsigned X_AT = 8u << 20;
constexpr int Y_NEAR = 0, Y_FAR = 144, F_G = 310;
constexpr int F_S = 26;
#define RG_U 3
#define UNEAR 8192u
#define NCOPY 4
#define RG_QKV 63
#define RG_M 63
#define RG_O 63
#define RG_AP 63
#define PB_MB 12u
constexpr unsigned CO = 4u << 20, PB = PB_MB << 20, BASE_BYTES = 244u << 20;
constexpr unsigned U_AT = (128u << 20) - PB;
constexpr size_t WS_END = (size_t)240 << 20;
constexpr int LDS_BYTES = 163840;
constexpr unsigned SPIN_CAP = 1u << 21;

struct Params {
  const u16 *x, *mem0, *Wq, *Wk, *Wv, *Wo, *lar, *aim, *cre, *cim, *ldt, *dsk, *gw;
  u16* out;
  char* ws;
#define TUNE_LIST(X) X(keep_at, "KD", 20) X(t_a, "TA", -6) X(t_first, "TY", 0) X(t_d, "TD", 11) X(rd0, "RD0", 55) X(gentle, "GENTLE", 0) X(dur_dec, "DDEC", 8) X(dur_inc, "DINC", 40) X(dur0, "DP0", 77) X(boost, "BOOST", 3) X(stag, "STAG", 40) X(dret, "DRET", 76) X(sa, "SA", 8) X(sb, "SB", 50) X(sa1, "SA1", 8) X(sa2, "SA2", 25) X(x2, "X2", 2) X(g2, "G2", 4) X(xg0, "XG0", 8) X(xg1, "XG1", 5) X(xtra, "XTRA", 9) X(s4q, "S4Q", 0) X(s4f, "S4F", 8) X(wl2, "WL2", 3) X(s0, "S0", 30) X(v0, "V0", 0) X(xl0, "XL0", 56) X(tlb, "TLB", 0) X(xt0, "XT0", 8) X(xt1, "XT1", 80) X(vlead, "VLEAD", 23) X(xpre, "XPRE", 7) X(pj0, "PJ0", 0) X(xlast, "XLAST", 8) X(hdel, "HDEL", 0) X(hdl2, "HDL2", 0) X(q0, "Q0", 0) X(s4skip, "S4SKIP", 0) X(mixs, "MIXS", 0) X(ob0, "OB0", 0) X(ob1, "OB1", 6) X(fsl, "FSL", 2) X(h0, "H0", 0) X(late_r, "LR", 8) X(late_s, "LS", 8)
#define TUNE_DECL(n, e, v) static constexpr int n = v;
  TUNE_LIST(TUNE_DECL)
};

__device__ __forceinline__ int lane_id() {
  int z = 0;
  asm volatile("" : "+v"(z));
  return __builtin_amdgcn_mbcnt_hi(~0u, __builtin_amdgcn_mbcnt_lo(~0u, z));
}
__device__ __forceinline__ float bf2f(u16 x) { return __uint_as_float((unsigned)x << 16); }
__device__ __forceinline__ float rbf(float x) { return (float)(__bf16)x; }
__device__ __forceinline__ unsigned pk2(float a, float b) {
  f32x2 v = {a, b};
  return __builtin_bit_cast(unsigned, __builtin_convertvector(v, bf16x2));
}
__device__ __forceinline__ float lo16(unsigned p) { return __uint_as_float(p << 16); }
__device__ __forceinline__ float hi16(unsigned p) { return __uint_as_float(p & 0xFFFF0000u); }
__device__ __forceinline__ bf16x8 zext8(u32x2 v) {
  u32x4 z = {v[0], v[1], 0u, 0u};
  return __builtin_bit_cast(bf16x8, z);
}
__device__ __forceinline__ bf16x4 tr_read(const char* smem_base, unsigned off) {
  return __builtin_amdgcn_ds_read_tr16_b64_v4bf16((LAS bf16x4*)((LAS char*)smem_base + off));
}
__device__ __forceinline__ float red_max_g(float x) {
  auto r = __builtin_amdgcn_permlane16_swap(__float_as_uint(x), __float_as_uint(x), false, false);
  x = fmaxf(__uint_as_float(r[0]), __uint_as_float(r[1]));
  auto q = __builtin_amdgcn_permlane32_swap(__float_as_uint(x), __float_as_uint(x), false, false);
  return fmaxf(__uint_as_float(q[0]), __uint_as_float(q[1]));
}
__device__ __forceinline__ float red_sum_g(float x) {
  auto r = __builtin_amdgcn_permlane16_swap(__float_as_uint(x), __float_as_uint(x), false, false);
  x = __uint_as_float(r[0]) + __uint_as_float(r[1]);
  auto q = __builtin_amdgcn_permlane32_swap(__float_as_uint(x), __float_as_uint(x), false, false);
  return __uint_as_float(q[0]) + __uint_as_float(q[1]);
}
__device__ __forceinline__ rsrc_t mk_rsrc(const void* p, size_t bytes) {
  return __builtin_amdgcn_make_buffer_rsrc((void*)p, 0, (int)bytes, 0x00020000);
}

#define STR2(x) #x
#define STR(x) STR2(x)
#define CODE_PAD(N) asm volatile("s_branch .Lpad_%=\n\t.fill " STR(N) ", 4, 0xBF800000\n\t.Lpad_%=:" ::: "memory")
#define PAD_RED 0
#define PAD_ATT 0
#define PAD_OP 0
#define PAD_S4 0
#define PAD_CH 0
#define WAIT_VM(n) asm volatile("s_waitcnt vmcnt(" #n ")" ::: "memory")
#define WAIT_LGKM0() asm volatile("s_waitcnt lgkmcnt(0)" ::: "memory")
#define BARRIER() do { WAIT_LGKM0(); __builtin_amdgcn_s_barrier(); asm volatile("" ::: "memory"); } while (0)
#define SB() __builtin_amdgcn_sched_barrier(0)
#define MF(a, b, c) __builtin_amdgcn_mfma_f32_16x16x32_bf16(a, b, c, 0, 0, 0)
#define RLX_AGENT __ATOMIC_RELAXED, __HIP_MEMORY_SCOPE_AGENT
#define DEV_COUNT(i, cond) do {} while (0)
#define DEV_COUNT_DECL do {} while (0)
#define DEV_COUNT_FLUSH() do {} while (0)
__device__ __forceinline__ bf16x8 cat2(bf16x4 a, bf16x4 b) { return __builtin_shufflevector(a, b, 0, 1, 2, 3, 4, 5, 6, 7); }
__device__ __forceinline__ u32x4 pack8(const f32x4& a, const f32x4& b) {
  return (u32x4){pk2(a[0], a[1]), pk2(a[2], a[3]), pk2(b[0], b[1]), pk2(b[2], b[3])};
}
__device__ __forceinline__ void dma16(rsrc_t rs, unsigned voff, unsigned soff, unsigned lds_addr) {
  unsigned keep;
  asm volatile("s_mov_b32 %0, m0\n\ts_mov_b32 m0, %3\n\ts_nop 0\n\tbuffer_load_dwordx4 %1, %2, %4 offen lds\n\ts_mov_b32 m0, %0"
               : "=&s"(keep) : "v"(voff), "s"(rs), "s"(lds_addr), "s"(soff) : "memory");
}
__device__ __forceinline__ void st128(u32x4 v, rsrc_t rs, unsigned voff, unsigned soff) {
  __builtin_amdgcn_raw_buffer_store_b128(v, rs, voff, soff, 16);
  asm volatile("s_nop 1" ::"v"(v) : "memory");
}
__device__ __forceinline__ u32x4 ld128(rsrc_t rs, unsigned voff, unsigned soff) { return __builtin_amdgcn_raw_buffer_load_b128(rs, voff, soff, 16); }
__device__ __forceinline__ bf16x8 ldfrag(rsrc_t rs, unsigned voff, unsigned soff) { return __builtin_bit_cast(bf16x8, ld128(rs, voff, soff)); }
__device__ __forceinline__ void sleep_n(int n) { for (int i = 0; i < n; ++i) __builtin_amdgcn_s_sleep(1); }

__device__ __forceinline__ char* comm_base(char* ws) {
  return (char*)(((uintptr_t)ws + CTL_BYTES + ((2u << 20) - 1)) & ~(uintptr_t)((2u << 20) - 1));
}
__device__ __forceinline__ unsigned rt_now() {
  unsigned long long t;
  asm volatile("s_memrealtime %0\n\ts_waitcnt lgkmcnt(0)" : "=s"(t) :: "memory");
  return (unsigned)t;
}
__device__ __forceinline__ int wait_rt(unsigned t) {
  int d = (int)(t - rt_now());
  if (d <= 0 || d > 2000) return -d;
  for (d -= 14; d > 0; d -= 3) __builtin_amdgcn_s_sleep(1);
  do { d = (int)(t - rt_now()); } while (d > 0);
  return -d;
}
__device__ __forceinline__ unsigned xcc_id() { return (unsigned)__builtin_amdgcn_s_getreg(20 | (3 << 11)) & 7u; }
typedef unsigned long long u64;
struct Ctx { char* ws; unsigned tag; };
__device__ __forceinline__ GAS u64* flag_ptr(char* ws, int idx) { return (GAS u64*)(ws + WS_CTL + idx * 128); }
__device__ __forceinline__ unsigned gen_load(char* ws) { return __hip_atomic_load((GAS unsigned*)flag_ptr(ws, F_GEN), RLX_AGENT); }
__device__ __forceinline__ unsigned flag_get(const Ctx& cx, GAS u64* p) {
  const u64 v = __hip_atomic_load(p, RLX_AGENT);
  return (unsigned)(v >> 32) == cx.tag ? (unsigned)v : 0u;
}
__device__ __forceinline__ void flag_put(const Ctx& cx, GAS u64* p, unsigned v) { __hip_atomic_store(p, ((u64)cx.tag << 32) | v, RLX_AGENT); }
__device__ __forceinline__ bool spin_fail(unsigned& spins, const Ctx& cx) {
  ++spins;
  if (spins > SPIN_CAP) { flag_put(cx, flag_ptr(cx.ws, F_TMO), 1u); return true; }
  return (spins & 1023u) == 0 && __builtin_amdgcn_readfirstlane(flag_get(cx, flag_ptr(cx.ws, F_TMO)));
}
template <int SLEEP = 1>
__device__ __forceinline__ void wait_ge_n(const Ctx& cx, int idx, int stride, int cnt, int target, int& seen) {
  unsigned spins = 0;
  const int k = lane_id() & 3;
  while (seen < target) {
    int v = (int)flag_get(cx, flag_ptr(cx.ws, idx + (k < cnt ? k : 0) * stride));
    seen = min(min(__builtin_amdgcn_readlane(v, 0), __builtin_amdgcn_readlane(v, 1)), min(__builtin_amdgcn_readlane(v, 2), __builtin_amdgcn_readlane(v, 3)));
    if (seen < target) {
      __builtin_amdgcn_s_sleep(SLEEP);
      if (spin_fail(spins, cx)) break;
    }
  }
  __builtin_amdgcn_fence(__ATOMIC_ACQUIRE, "wavefront");
}
template <int SLEEP>
__device__ __forceinline__ void wait_all(const Ctx& cx, int idx, int cnt) {
  unsigned spins = 0;
  const int k = lane_id();
  while (__any(flag_get(cx, flag_ptr(cx.ws, idx) + (k < cnt ? k : 0)) == 0u)) {
    __builtin_amdgcn_s_sleep(SLEEP);
    if (spin_fail(spins, cx)) break;
  }
  __builtin_amdgcn_fence(__ATOMIC_ACQUIRE, "wavefront");
}
__device__ __forceinline__ void publish(const Ctx& cx, int w, int idx, int value) {
  if (w == 0 && lane_id() == 0) flag_put(cx, flag_ptr(cx.ws, idx), (unsigned)value);
}

constexpr unsigned X1 = 0x101000u, TW_OFF = 27 * 32768 + 1024;
__device__ __forceinline__ unsigned win_static(int win) { return win * 32768 + (win >> 4) * 16384; }
__device__ __forceinline__ unsigned near_static(int i) { return (win_static(28 + (i >> 5)) ^ ((i & 32) << 7) ^ ((i & 64) << 8)) + (i & 31) * 128; }
struct Place {
  unsigned go;
  __device__ __forceinline__ void load(const Ctx& cx, int grp, unsigned need = 0xC0000000u) {
    unsigned spins = 0;
    while ((go = __builtin_amdgcn_readfirstlane(flag_get(cx, flag_ptr(cx.ws, F_GO + grp)))) < need && !spin_fail(spins, cx)) __builtin_amdgcn_s_sleep(8);
    __builtin_amdgcn_fence(__ATOMIC_ACQUIRE, "wavefront");
  }
  __device__ __forceinline__ unsigned dyn(int x) const { return ((go >> (4 * x)) & 7u) << 12; }
  __device__ __forceinline__ unsigned tog() const { return (((go >> 16) & 7u) << 12) | (2u << 20); }
  __device__ __forceinline__ unsigned slot(int s) const { return (((s ^ (s >> 1)) & 1) ? tog() : 0u) ^ ((s & 2) ? X1 : 0u); }
  __device__ __forceinline__ unsigned near(int i) const { return near_static(i) ^ (dyn(0) & 8192u); }
};
#define BULK_FLIP 0u
#define OUT_AUX 0
#define PROBE_N 6
struct Bulk {
  rsrc_t rs;
  unsigned w13;
  Place pl;
  __device__ __forceinline__ GAS u64* proj_flag(const Ctx& cx, int m, int h) const {
    return m == 0 ? flag_ptr(cx.ws, F_Q + h) : (GAS u64*)(comm_base(cx.ws) + pl.near(N_K + h) + (m - 1) * 8);
  }
  __device__ __forceinline__ void init(const Ctx& cx, int grp, u64 early = 0) {
    pl.go = __builtin_amdgcn_readfirstlane((unsigned)(early >> 32) == cx.tag ? (unsigned)early : 0u);
    if (pl.go < 0x80000000u) pl.load(cx, grp, 0x80000000u);
    rs = mk_rsrc(comm_base(cx.ws) - CO, BASE_BYTES);
    w13 = PB + ((~pl.go ^ BULK_FLIP) & 2u) * 4096;
  }
  __device__ __forceinline__ unsigned at(unsigned o) const {
    const unsigned q = w13 + ((o >> 13) << 14) + (o & 8191u);
    return q ^ ((q >> 8) & 8192u);
  }
};
__device__ __forceinline__ unsigned near_head(unsigned b, int h) { return PB + 2 * b + h * (1u << 20) + ((h >> 1) & 1) * 8192 + ((2 * h) & 63) * 16384; }
__device__ __forceinline__ unsigned u_tile(int T, int hb, int h7) {
  const int wv = T < 32 ? (T >> 1) & 3 : T & 3;
  return PB + 2 * B_U + T * (1u << 20) + ((T >> 1) & 1) * 8192 + (((T >> 3) & 3) * 4 + wv) * 65536 + (hb ^ ((T >> 3) & 3)) * 16384 + (h7 ^ (2 * wv + (T < 32 ? T & 1 : 0))) * 1024;
}
__device__ __forceinline__ unsigned xcc_page(unsigned xc) { return ((xc >> 1) & 1u) | ((xc >> 2) << 1) | ((xc & 1u) << 2); }

__device__ __forceinline__ void touch_pages(char* ws, int i) {
  const rsrc_t rs = mk_rsrc(comm_base(ws) - CO, BASE_BYTES);
  const unsigned o = lane_id() * (2u << 20) + (1u << 20) + (i & 63) * 128;
  const unsigned a = __builtin_amdgcn_raw_buffer_load_b32(rs, o, 4u << 20, 0), b = __builtin_amdgcn_raw_buffer_load_b32(rs, o, 132u << 20, 0);
  asm volatile("" :: "v"(a), "v"(b));
}

__device__ __forceinline__ void comm_init(Ctx& cx, int rid, int w, char* smem, bool all) {
  const int l = lane_id(), tid = w * 64 + l;
  const rsrc_t rs = mk_rsrc(comm_base(cx.ws), COMM_BYTES);
  const unsigned gen = gen_load(cx.ws);
  if (rid == 0) {
    LAS unsigned* const best = (LAS unsigned*)smem + 16;
    const unsigned base = w * 1024, r0 = rt_now(), c0 = (unsigned)__builtin_amdgcn_s_memtime();
    best[32] = 0u;
#pragma unroll
    for (int k = 0; k < 8; ++k) __builtin_amdgcn_raw_buffer_store_b128((u32x4){0u, 0u, 0u, 0u}, rs, base + k * 4096 + l * 16, 0, 16);
    WAIT_VM(0);
    BARRIER();
    unsigned mine = 0xFFFFu;
    auto sample = [&](int pg, int k) {
      unsigned long long t0, t1;
      asm volatile("s_memtime %0\n\ts_waitcnt lgkmcnt(0)" : "=s"(t0) :: "memory");
      const unsigned v = __builtin_amdgcn_raw_buffer_load_b32(rs, base + pg * 4096 + k * 128, 0, 16);
      asm volatile("s_waitcnt vmcnt(0)\n\ts_memtime %0\n\ts_waitcnt lgkmcnt(0)" : "=s"(t1) : "v"(v) : "memory");
      if (l == pg) mine = min(mine, (unsigned)(t1 - t0));
      return (unsigned)(t1 - t0);
    };
    auto pages = [&]() {
      if (l < 8) best[w * 8 + l] = mine;
      __syncthreads();
      const int k8 = l & 7;
      const unsigned b = min(min(best[k8], best[8 + k8]), min(best[16 + k8], best[24 + k8]));
      __syncthreads();
      return b;
    };
    auto page = [](int j) { return (j & 1) | ((j & 2) << 1); };
    const unsigned lim = 25u * ((unsigned)__builtin_amdgcn_s_memtime() - c0) / max(rt_now() - r0, 1u);
    cx.tag = __builtin_amdgcn_readfirstlane(gen) + 1u;
    volatile LAS unsigned* const found = best + 32;
    for (int k = 0; k < 40 && !__builtin_amdgcn_readfirstlane(*found); ++k) {
      const int pg = page(w + (k >> 1)) | ((k & 1) << 1);
      if (sample(pg, k >> 3) < lim && l == 0) atomicCAS((unsigned*)found, 0u, 1u | (pg & 2));
    }
    unsigned b = pages();
    b = min(b, (unsigned)__shfl_xor((int)b, 1)); b = min(b, (unsigned)__shfl_xor((int)b, 4));
    const unsigned fd = __builtin_amdgcn_readfirstlane(*found);
    const unsigned half = fd ? fd & 2u : __builtin_amdgcn_readlane(b, 2) < __builtin_amdgcn_readlane(b, 0) ? 2u : 0u;
    if (w == 3 && l < 8) flag_put(cx, flag_ptr(cx.ws, F_GO + l), 0x80000000u | half);
    unsigned xc = 0;
    if (w < 3) {
#pragma unroll
      for (int k = 0; k < PROBE_N; ++k) sample(page(w + k) | half, 5 + (k >> 2));
    } else {
      for (unsigned spins = 0; __any((xc = flag_get(cx, flag_ptr(cx.ws, F_INITW) + min(l, 35))) == 0u) && !spin_fail(spins, cx);) __builtin_amdgcn_s_sleep(2);
    }
    b = (pages() << 3) | (l & 7);
    b = min(b, (unsigned)__shfl_xor((int)b, 1)); b = min(b, (unsigned)__shfl_xor((int)b, 4));
    if (w == 3) {
      const unsigned v = l ? __shfl((int)xc, min(l, 3) * 9 - 1) - 1u : xcc_id();
      const unsigned p0 = (__builtin_amdgcn_readlane(b, half) & 7u) ^ xcc_page(xcc_id());
      unsigned seen = 0, go = 0xC0020000u;
#pragma unroll
      for (int x = 0; x < 4; ++x) seen |= 1u << (__builtin_amdgcn_readlane(v, x) & 7u);
#pragma unroll
      for (int x = 0; x < 4; ++x)
        go |= (seen == 15u || seen == 240u ? (xcc_page(__builtin_amdgcn_readlane(v, x)) ^ p0) & 7u : page(x) | half) << (4 * x);
      if (l < 8) flag_put(cx, flag_ptr(cx.ws, F_GO + l), go);
    }
    return;
  }
  if (all) {
    for (int j = (rid - 1) * 256 + tid; j < 78 * 2048; j += 35 * 256) {
      const int wi = j >> 11, win = wi < 2 ? 25 + wi : wi < 28 ? 31 + wi : wi < 52 ? 37 + wi : 45 + wi;
      __builtin_amdgcn_raw_buffer_store_b128((u32x4){~0u, ~0u, ~0u, ~0u}, rs, win * 32768 + (j & 2047) * 16, 0, 16);
    }
  }
  for (int j = (rid - 1) * 256 + tid; j < 34 * 2048; j += 35 * 256) {
    const int wi = 33 - (j >> 11), ch = j & 2047;
    const int win = wi < 24 ? wi + 1 : wi < 27 ? wi + 65 : wi == 27 ? 59 : wi == 28 ? 123 : wi - 2;
    const unsigned f = wi < 30 ? 0xFFFFFFFFu : 0u;
    __builtin_amdgcn_raw_buffer_store_b128((u32x4){f, f, f, f}, rs, win * 32768 + ch * 16, 0, 16);
  }
  cx.tag = __builtin_amdgcn_readfirstlane(gen) + 1u;
  WAIT_VM(0);
  BARRIER();
  if (tid == 0) flag_put(cx, flag_ptr(cx.ws, F_INITW) + rid - 1, xcc_id() + 1u);
}

__device__ __forceinline__ void keep_role(const Params& p) {
  char* const ws = p.ws;
  const int l = lane_id();
  const Ctx cx = {ws, __builtin_amdgcn_readfirstlane(gen_load(ws)) + 1u};
  if (l == 0) flag_put(cx, flag_ptr(ws, F_INITW) + 35, 1u);
  const rsrc_t crs = mk_rsrc(comm_base(ws), COMM_BYTES);
  Place pl;
  pl.load(cx, 0);
  int dp[2] = {4 * p.dur0, 4 * p.rd0}, boost = 0;
  for (unsigned spins = 0; p.t_first && __any(min(flag_get(cx, flag_ptr(ws, F_U + (l & 31)) + (l >> 5)), flag_get(cx, flag_ptr(ws, F_U + (l & 31)) + 2 + (l >> 5))) == 0u) && !spin_fail(spins, cx);)
    __builtin_amdgcn_s_sleep(1);
  unsigned t = rt_now() + p.t_first - 2 * (p.dur0 + p.t_a + p.xtra) - (p.rd0 + p.t_d + p.xg0), ta = 0, shift = 0;
  auto plan = [&](int b) {
    u32x4 wd = {0u, 0u, 0u, 0u};
#pragma unroll
    for (int k = 0; k < 4; ++k) {
      const int s = 4 * b + k;
      const unsigned t1 = t + ((dp[0] + k) >> 2) + p.t_a + boost + (s < p.sa ? p.xtra : s >= p.sa1 && s < p.sa2 ? p.x2 : s > p.xl0 ? p.xlast : 0);
      t = t1 + ((dp[1] + 3 - k) >> 2) + p.t_d + boost + (s < p.sa ? p.xg0 : s >= p.sa1 && s < p.sa2 ? p.g2 : s >= p.sb ? p.xg1 : 0);
      if ((l & 7) == k) wd = (u32x4){(unsigned)s, t1, t, shift};
      if (k == 1) ta = t;
    }
    st128(wd, crs, l < 32 ? (16384u ^ pl.dyn(l >> 3)) + (l & 7) * 16 : 0x7F000000u, TW_OFF + 1024 + b * 128);
  };
  plan(0);
#pragma unroll 1
  for (int b = 1; b < NSEG / 4; ++b) {
    u32x2 dw;
    const unsigned before = shift;
    for (unsigned spins = 0; !spin_fail(spins, cx); shift += p.dret) {
      const int d = (int)(ta + shift + p.keep_at - rt_now());
      if (d < -20 && d > -30000) continue;
      while ((unsigned)(ta + shift + p.keep_at - rt_now() - 120u) < 2000u) __builtin_amdgcn_s_sleep(24);
      while ((unsigned)(ta + shift + p.keep_at - rt_now() - 1u) < 2000u) __builtin_amdgcn_s_sleep(2);
      const unsigned f = flag_get(cx, (GAS u64*)(comm_base(ws) + (pl.near(N_K + b - 1) ^ 4096u)));
      dw = __builtin_amdgcn_raw_buffer_load_b64(crs, pl.near((l < 32 ? N_DUR : N_RD - 32) + l), 0, 16);
      if (__builtin_amdgcn_readfirstlane(f)) { shift = __builtin_amdgcn_readfirstlane(f) - 1u; break; }
    }
    if (b > 2) boost = shift - before > (unsigned)p.dret ? min(boost + p.boost, 12) : shift == before ? max(boost - 1, 0) : boost;
    unsigned dur = dw[1] == cx.tag && (dw[0] & 1023u) ? (unsigned)((float)(dw[0] >> 10) * 64.f * __builtin_amdgcn_rcpf((float)(dw[0] & 1023u))) : 0u;
    const unsigned long long none = __ballot(dur == 0u);
#pragma unroll
    for (int m = 1; m < 32; m <<= 1) dur = max(dur, (unsigned)__shfl_xor((int)dur, m));
#pragma unroll
    for (int k = 0; k < 2; ++k) {
      const int dm = __builtin_amdgcn_readlane(dur, 32 * k), d = dp[k];
      if ((unsigned)(none >> (32 * k)) == 0u) dp[k] = max(min(dm, d + p.dur_inc), d - p.dur_dec);
    }
    plan(b);
  }
}

#define X_ASM_TEXT \
  "s_cmp_gt_u32 %28, 1\n" \
  "s_cbranch_scc1 .LX_other_%=\n" \
  "s_mov_b64 s[40:41], %24\n" \
  "s_and_b32 s41, s41, 0xffff\n" \
  "s_mov_b32 s42, 255852544\n" \
  "s_mov_b32 s43, 0x00020000\n" \
  "s_mov_b32 s46, 4718592\n" \
  "s_mov_b32 s47, 0x00020000\n" \
  "s_lshl_b32 s72, %28, 11\n" \
  "buffer_load_dword v196, off, s[40:43], %38 sc1\n" \
  "s_mov_b64 s[44:45], %25\n" \
  "s_and_b32 s45, s45, 0xffff\n" \
  "s_add_u32 s60, %29, 0\n" \
  "s_lshl_b32 s61, s72, 1\n" \
  "s_add_u32 m0, s61, 16384\n" \
  "s_nop 0\n" \
  "buffer_load_dwordx4 v200, s[44:47], s60 offen lds\n" \
  "buffer_load_dwordx4 v201, s[44:47], s60 offen offset:1024 lds\n" \
  "buffer_load_dwordx4 v202, s[44:47], s60 offen offset:2048 lds\n" \
  "buffer_load_dwordx4 v203, s[44:47], s60 offen offset:3072 lds\n" \
  "s_add_u32 s60, %29, 524288\n" \
  "s_lshl_b32 s61, s72, 1\n" \
  "s_add_u32 m0, s61, 32768\n" \
  "s_nop 0\n" \
  "buffer_load_dwordx4 v200, s[44:47], s60 offen lds\n" \
  "buffer_load_dwordx4 v201, s[44:47], s60 offen offset:1024 lds\n" \
  "buffer_load_dwordx4 v202, s[44:47], s60 offen offset:2048 lds\n" \
  "buffer_load_dwordx4 v203, s[44:47], s60 offen offset:3072 lds\n" \
  "s_add_u32 s60, %29, 1048576\n" \
  "s_lshl_b32 s61, s72, 1\n" \
  "s_add_u32 m0, s61, 49152\n" \
  "s_nop 0\n" \
  "buffer_load_dwordx4 v200, s[44:47], s60 offen lds\n" \
  "buffer_load_dwordx4 v201, s[44:47], s60 offen offset:1024 lds\n" \
  "buffer_load_dwordx4 v202, s[44:47], s60 offen offset:2048 lds\n" \
  "buffer_load_dwordx4 v203, s[44:47], s60 offen offset:3072 lds\n" \
  "s_add_u32 s60, %29, 1572864\n" \
  "s_lshl_b32 s61, s72, 1\n" \
  "s_add_u32 m0, s61, 65536\n" \
  "s_nop 0\n" \
  "buffer_load_dwordx4 v200, s[44:47], s60 offen lds\n" \
  "buffer_load_dwordx4 v201, s[44:47], s60 offen offset:1024 lds\n" \
  "buffer_load_dwordx4 v202, s[44:47], s60 offen offset:2048 lds\n" \
  "buffer_load_dwordx4 v203, s[44:47], s60 offen offset:3072 lds\n" \
  "s_add_u32 s60, %29, 2097152\n" \
  "s_lshl_b32 s61, s72, 1\n" \
  "s_add_u32 m0, s61, 81920\n" \
  "s_nop 0\n" \
  "buffer_load_dwordx4 v200, s[44:47], s60 offen lds\n" \
  "buffer_load_dwordx4 v201, s[44:47], s60 offen offset:1024 lds\n" \
  "buffer_load_dwordx4 v202, s[44:47], s60 offen offset:2048 lds\n" \
  "buffer_load_dwordx4 v203, s[44:47], s60 offen offset:3072 lds\n" \
  "s_add_u32 s60, %29, 2621440\n" \
  "s_lshl_b32 s61, s72, 1\n" \
  "s_add_u32 m0, s61, 98304\n" \
  "s_nop 0\n" \
  "buffer_load_dwordx4 v200, s[44:47], s60 offen lds\n" \
  "buffer_load_dwordx4 v201, s[44:47], s60 offen offset:1024 lds\n" \
  "buffer_load_dwordx4 v202, s[44:47], s60 offen offset:2048 lds\n" \
  "buffer_load_dwordx4 v203, s[44:47], s60 offen offset:3072 lds\n" \
  "s_add_u32 s60, %29, 3145728\n" \
  "s_lshl_b32 s61, s72, 1\n" \
  "s_add_u32 m0, s61, 114688\n" \
  "s_nop 0\n" \
  "buffer_load_dwordx4 v200, s[44:47], s60 offen lds\n" \
  "buffer_load_dwordx4 v201, s[44:47], s60 offen offset:1024 lds\n" \
  "buffer_load_dwordx4 v202, s[44:47], s60 offen offset:2048 lds\n" \
  "buffer_load_dwordx4 v203, s[44:47], s60 offen offset:3072 lds\n" \
  "s_add_u32 s60, %29, 3670016\n" \
  "s_lshl_b32 s61, s72, 1\n" \
  "s_add_u32 m0, s61, 131072\n" \
  "s_nop 0\n" \
  "buffer_load_dwordx4 v200, s[44:47], s60 offen lds\n" \
  "buffer_load_dwordx4 v201, s[44:47], s60 offen offset:1024 lds\n" \
  "buffer_load_dwordx4 v202, s[44:47], s60 offen offset:2048 lds\n" \
  "buffer_load_dwordx4 v203, s[44:47], s60 offen offset:3072 lds\n" \
  "s_add_u32 s60, %29, 4194304\n" \
  "s_lshl_b32 s61, s72, 1\n" \
  "s_add_u32 m0, s61, 147456\n" \
  "s_nop 0\n" \
  "buffer_load_dwordx4 v200, s[44:47], s60 offen lds\n" \
  "buffer_load_dwordx4 v201, s[44:47], s60 offen offset:1024 lds\n" \
  "buffer_load_dwordx4 v202, s[44:47], s60 offen offset:2048 lds\n" \
  "buffer_load_dwordx4 v203, s[44:47], s60 offen offset:3072 lds\n" \
  "s_mov_b64 s[44:45], %27\n" \
  "s_and_b32 s45, s45, 0xffff\n" \
  "s_lshr_b32 s60, s72, 3\n" \
  "s_mov_b32 m0, s72\n" \
  "s_nop 0\n" \
  "buffer_load_dwordx4 v206, s[44:47], s60 offen lds\n" \
  "s_add_u32 s60, s60, 128\n" \
  "s_add_u32 m0, s72, 1024\n" \
  "s_nop 0\n" \
  "buffer_load_dwordx4 v206, s[44:47], s60 offen lds\n" \
  "s_movk_i32 s60, 1024\n" \
  "s_movk_i32 m0, 8192\n" \
  "s_nop 0\n" \
  "buffer_load_dwordx4 v206, s[44:47], s60 offen lds\n" \
  "s_waitcnt vmcnt(27)\n" \
  "s_barrier\n" \
  "v_add_u32 v194, 16384, v204\n" \
  "ds_read_b64_tr_b16 a[0:1], v194\n" \
  "ds_read_b64_tr_b16 a[2:3], v194 offset:512\n" \
  "ds_read_b64_tr_b16 a[4:5], v194 offset:8192\n" \
  "ds_read_b64_tr_b16 a[6:7], v194 offset:8704\n" \
  "v_add_u32 v195, 16384, v205\n" \
  "ds_read_b64_tr_b16 a[72:73], v195\n" \
  "ds_read_b64_tr_b16 a[74:75], v195 offset:512\n" \
  "ds_read_b64_tr_b16 a[76:77], v195 offset:8192\n" \
  "ds_read_b64_tr_b16 a[78:79], v195 offset:8704\n" \
  "v_add_u32 v194, 32768, v204\n" \
  "ds_read_b64_tr_b16 a[8:9], v194\n" \
  "ds_read_b64_tr_b16 a[10:11], v194 offset:512\n" \
  "ds_read_b64_tr_b16 a[12:13], v194 offset:8192\n" \
  "ds_read_b64_tr_b16 a[14:15], v194 offset:8704\n" \
  "v_add_u32 v195, 32768, v205\n" \
  "ds_read_b64_tr_b16 a[80:81], v195\n" \
  "ds_read_b64_tr_b16 a[82:83], v195 offset:512\n" \
  "ds_read_b64_tr_b16 a[84:85], v195 offset:8192\n" \
  "ds_read_b64_tr_b16 a[86:87], v195 offset:8704\n" \
  "v_add_u32 v194, 49152, v204\n" \
  "ds_read_b64_tr_b16 a[16:17], v194\n" \
  "ds_read_b64_tr_b16 a[18:19], v194 offset:512\n" \
  "ds_read_b64_tr_b16 a[20:21], v194 offset:8192\n" \
  "ds_read_b64_tr_b16 a[22:23], v194 offset:8704\n" \
  "v_add_u32 v195, 49152, v205\n" \
  "ds_read_b64_tr_b16 a[88:89], v195\n" \
  "ds_read_b64_tr_b16 a[90:91], v195 offset:512\n" \
  "ds_read_b64_tr_b16 a[92:93], v195 offset:8192\n" \
  "ds_read_b64_tr_b16 a[94:95], v195 offset:8704\n" \
  "s_waitcnt lgkmcnt(0)\n" \
  "s_barrier\n" \
  "s_mov_b64 s[44:45], %26\n" \
  "s_and_b32 s45, s45, 0xffff\n" \
  "s_add_u32 s60, %29, 0\n" \
  "s_lshl_b32 s61, s72, 1\n" \
  "s_add_u32 m0, s61, 16384\n" \
  "s_nop 0\n" \
  "buffer_load_dwordx4 v200, s[44:47], s60 offen lds\n" \
  "buffer_load_dwordx4 v201, s[44:47], s60 offen offset:1024 lds\n" \
  "buffer_load_dwordx4 v202, s[44:47], s60 offen offset:2048 lds\n" \
  "buffer_load_dwordx4 v203, s[44:47], s60 offen offset:3072 lds\n" \
  "s_add_u32 s60, %29, 524288\n" \
  "s_lshl_b32 s61, s72, 1\n" \
  "s_add_u32 m0, s61, 32768\n" \
  "s_nop 0\n" \
  "buffer_load_dwordx4 v200, s[44:47], s60 offen lds\n" \
  "buffer_load_dwordx4 v201, s[44:47], s60 offen offset:1024 lds\n" \
  "buffer_load_dwordx4 v202, s[44:47], s60 offen offset:2048 lds\n" \
  "buffer_load_dwordx4 v203, s[44:47], s60 offen offset:3072 lds\n" \
  "s_add_u32 s60, %29, 1048576\n" \
  "s_lshl_b32 s61, s72, 1\n" \
  "s_add_u32 m0, s61, 49152\n" \
  "s_nop 0\n" \
  "buffer_load_dwordx4 v200, s[44:47], s60 offen lds\n" \
  "buffer_load_dwordx4 v201, s[44:47], s60 offen offset:1024 lds\n" \
  "buffer_load_dwordx4 v202, s[44:47], s60 offen offset:2048 lds\n" \
  "buffer_load_dwordx4 v203, s[44:47], s60 offen offset:3072 lds\n" \
  "v_readfirstlane_b32 s55, v196\n" \
  "s_add_u32 s55, s55, 1\n" \
  "s_waitcnt vmcnt(27)\n" \
  "s_barrier\n" \
  "v_add_u32 v194, 65536, v204\n" \
  "ds_read_b64_tr_b16 a[24:25], v194\n" \
  "ds_read_b64_tr_b16 a[26:27], v194 offset:512\n" \
  "ds_read_b64_tr_b16 a[28:29], v194 offset:8192\n" \
  "ds_read_b64_tr_b16 a[30:31], v194 offset:8704\n" \
  "v_add_u32 v195, 65536, v205\n" \
  "ds_read_b64_tr_b16 a[96:97], v195\n" \
  "ds_read_b64_tr_b16 a[98:99], v195 offset:512\n" \
  "ds_read_b64_tr_b16 a[100:101], v195 offset:8192\n" \
  "ds_read_b64_tr_b16 a[102:103], v195 offset:8704\n" \
  "v_add_u32 v194, 81920, v204\n" \
  "ds_read_b64_tr_b16 a[32:33], v194\n" \
  "ds_read_b64_tr_b16 a[34:35], v194 offset:512\n" \
  "ds_read_b64_tr_b16 a[36:37], v194 offset:8192\n" \
  "ds_read_b64_tr_b16 a[38:39], v194 offset:8704\n" \
  "v_add_u32 v195, 81920, v205\n" \
  "ds_read_b64_tr_b16 a[104:105], v195\n" \
  "ds_read_b64_tr_b16 a[106:107], v195 offset:512\n" \
  "ds_read_b64_tr_b16 a[108:109], v195 offset:8192\n" \
  "ds_read_b64_tr_b16 a[110:111], v195 offset:8704\n" \
  "v_add_u32 v194, 98304, v204\n" \
  "ds_read_b64_tr_b16 a[40:41], v194\n" \
  "ds_read_b64_tr_b16 a[42:43], v194 offset:512\n" \
  "ds_read_b64_tr_b16 a[44:45], v194 offset:8192\n" \
  "ds_read_b64_tr_b16 a[46:47], v194 offset:8704\n" \
  "v_add_u32 v195, 98304, v205\n" \
  "ds_read_b64_tr_b16 a[112:113], v195\n" \
  "ds_read_b64_tr_b16 a[114:115], v195 offset:512\n" \
  "ds_read_b64_tr_b16 a[116:117], v195 offset:8192\n" \
  "ds_read_b64_tr_b16 a[118:119], v195 offset:8704\n" \
  "s_waitcnt lgkmcnt(0)\n" \
  "s_barrier\n" \
  "s_add_u32 s60, %29, 1572864\n" \
  "s_lshl_b32 s61, s72, 1\n" \
  "s_add_u32 m0, s61, 65536\n" \
  "s_nop 0\n" \
  "buffer_load_dwordx4 v200, s[44:47], s60 offen lds\n" \
  "buffer_load_dwordx4 v201, s[44:47], s60 offen offset:1024 lds\n" \
  "buffer_load_dwordx4 v202, s[44:47], s60 offen offset:2048 lds\n" \
  "buffer_load_dwordx4 v203, s[44:47], s60 offen offset:3072 lds\n" \
  "s_add_u32 s60, %29, 2097152\n" \
  "s_lshl_b32 s61, s72, 1\n" \
  "s_add_u32 m0, s61, 81920\n" \
  "s_nop 0\n" \
  "buffer_load_dwordx4 v200, s[44:47], s60 offen lds\n" \
  "buffer_load_dwordx4 v201, s[44:47], s60 offen offset:1024 lds\n" \
  "buffer_load_dwordx4 v202, s[44:47], s60 offen offset:2048 lds\n" \
  "buffer_load_dwordx4 v203, s[44:47], s60 offen offset:3072 lds\n" \
  "s_add_u32 s60, %29, 2621440\n" \
  "s_lshl_b32 s61, s72, 1\n" \
  "s_add_u32 m0, s61, 98304\n" \
  "s_nop 0\n" \
  "buffer_load_dwordx4 v200, s[44:47], s60 offen lds\n" \
  "buffer_load_dwordx4 v201, s[44:47], s60 offen offset:1024 lds\n" \
  "buffer_load_dwordx4 v202, s[44:47], s60 offen offset:2048 lds\n" \
  "buffer_load_dwordx4 v203, s[44:47], s60 offen offset:3072 lds\n" \
  "s_waitcnt vmcnt(27)\n" \
  "s_barrier\n" \
  "v_add_u32 v194, 114688, v204\n" \
  "ds_read_b64_tr_b16 a[48:49], v194\n" \
  "ds_read_b64_tr_b16 a[50:51], v194 offset:512\n" \
  "ds_read_b64_tr_b16 a[52:53], v194 offset:8192\n" \
  "ds_read_b64_tr_b16 a[54:55], v194 offset:8704\n" \
  "v_add_u32 v195, 114688, v205\n" \
  "ds_read_b64_tr_b16 a[120:121], v195\n" \
  "ds_read_b64_tr_b16 a[122:123], v195 offset:512\n" \
  "ds_read_b64_tr_b16 a[124:125], v195 offset:8192\n" \
  "ds_read_b64_tr_b16 a[126:127], v195 offset:8704\n" \
  "v_add_u32 v194, 131072, v204\n" \
  "ds_read_b64_tr_b16 a[56:57], v194\n" \
  "ds_read_b64_tr_b16 a[58:59], v194 offset:512\n" \
  "ds_read_b64_tr_b16 a[60:61], v194 offset:8192\n" \
  "ds_read_b64_tr_b16 a[62:63], v194 offset:8704\n" \
  "v_add_u32 v195, 131072, v205\n" \
  "ds_read_b64_tr_b16 a[128:129], v195\n" \
  "ds_read_b64_tr_b16 a[130:131], v195 offset:512\n" \
  "ds_read_b64_tr_b16 a[132:133], v195 offset:8192\n" \
  "ds_read_b64_tr_b16 a[134:135], v195 offset:8704\n" \
  "v_add_u32 v194, 147456, v204\n" \
  "ds_read_b64_tr_b16 a[64:65], v194\n" \
  "ds_read_b64_tr_b16 a[66:67], v194 offset:512\n" \
  "ds_read_b64_tr_b16 a[68:69], v194 offset:8192\n" \
  "ds_read_b64_tr_b16 a[70:71], v194 offset:8704\n" \
  "v_add_u32 v195, 147456, v205\n" \
  "ds_read_b64_tr_b16 a[136:137], v195\n" \
  "ds_read_b64_tr_b16 a[138:139], v195 offset:512\n" \
  "ds_read_b64_tr_b16 a[140:141], v195 offset:8192\n" \
  "ds_read_b64_tr_b16 a[142:143], v195 offset:8704\n" \
  "s_waitcnt lgkmcnt(0)\n" \
  "s_barrier\n" \
  "s_add_u32 s60, %29, 3145728\n" \
  "s_lshl_b32 s61, s72, 1\n" \
  "s_add_u32 m0, s61, 114688\n" \
  "s_nop 0\n" \
  "buffer_load_dwordx4 v200, s[44:47], s60 offen lds\n" \
  "buffer_load_dwordx4 v201, s[44:47], s60 offen offset:1024 lds\n" \
  "buffer_load_dwordx4 v202, s[44:47], s60 offen offset:2048 lds\n" \
  "buffer_load_dwordx4 v203, s[44:47], s60 offen offset:3072 lds\n" \
  "s_add_u32 s60, %29, 3670016\n" \
  "s_lshl_b32 s61, s72, 1\n" \
  "s_add_u32 m0, s61, 131072\n" \
  "s_nop 0\n" \
  "buffer_load_dwordx4 v200, s[44:47], s60 offen lds\n" \
  "buffer_load_dwordx4 v201, s[44:47], s60 offen offset:1024 lds\n" \
  "buffer_load_dwordx4 v202, s[44:47], s60 offen offset:2048 lds\n" \
  "buffer_load_dwordx4 v203, s[44:47], s60 offen offset:3072 lds\n" \
  "s_add_u32 s60, %29, 4194304\n" \
  "s_lshl_b32 s61, s72, 1\n" \
  "s_add_u32 m0, s61, 147456\n" \
  "s_nop 0\n" \
  "buffer_load_dwordx4 v200, s[44:47], s60 offen lds\n" \
  "buffer_load_dwordx4 v201, s[44:47], s60 offen offset:1024 lds\n" \
  "buffer_load_dwordx4 v202, s[44:47], s60 offen offset:2048 lds\n" \
  "buffer_load_dwordx4 v203, s[44:47], s60 offen offset:3072 lds\n" \
  "buffer_load_dwordx2 v[198:199], off, s[40:43], %37 sc1\n" \
  "s_waitcnt vmcnt(25)\n" \
  "s_barrier\n" \
  "v_add_u32 v195, 16384, v205\n" \
  "ds_read_b64_tr_b16 a[216:217], v195\n" \
  "ds_read_b64_tr_b16 a[218:219], v195 offset:512\n" \
  "ds_read_b64_tr_b16 a[220:221], v195 offset:8192\n" \
  "ds_read_b64_tr_b16 a[222:223], v195 offset:8704\n" \
  "v_add_u32 v195, 32768, v205\n" \
  "ds_read_b64_tr_b16 a[224:225], v195\n" \
  "ds_read_b64_tr_b16 a[226:227], v195 offset:512\n" \
  "ds_read_b64_tr_b16 a[228:229], v195 offset:8192\n" \
  "ds_read_b64_tr_b16 a[230:231], v195 offset:8704\n" \
  "v_add_u32 v195, 49152, v205\n" \
  "ds_read_b64_tr_b16 a[232:233], v195\n" \
  "ds_read_b64_tr_b16 a[234:235], v195 offset:512\n" \
  "ds_read_b64_tr_b16 a[236:237], v195 offset:8192\n" \
  "ds_read_b64_tr_b16 a[238:239], v195 offset:8704\n" \
  "s_waitcnt lgkmcnt(0)\n" \
  "s_waitcnt vmcnt(13)\n" \
  "s_barrier\n" \
  "v_add_u32 v195, 65536, v205\n" \
  "ds_read_b64_tr_b16 a[240:241], v195\n" \
  "ds_read_b64_tr_b16 a[242:243], v195 offset:512\n" \
  "ds_read_b64_tr_b16 a[244:245], v195 offset:8192\n" \
  "ds_read_b64_tr_b16 a[246:247], v195 offset:8704\n" \
  "v_add_u32 v194, 81920, v204\n" \
  "ds_read_b64_tr_b16 a[176:177], v194\n" \
  "ds_read_b64_tr_b16 a[178:179], v194 offset:512\n" \
  "ds_read_b64_tr_b16 a[180:181], v194 offset:8192\n" \
  "ds_read_b64_tr_b16 a[182:183], v194 offset:8704\n" \
  "v_add_u32 v195, 81920, v205\n" \
  "ds_read_b64_tr_b16 a[248:249], v195\n" \
  "ds_read_b64_tr_b16 a[250:251], v195 offset:512\n" \
  "ds_read_b64_tr_b16 a[252:253], v195 offset:8192\n" \
  "ds_read_b64_tr_b16 a[254:255], v195 offset:8704\n" \
  "v_add_u32 v194, 98304, v204\n" \
  "ds_read_b64_tr_b16 a[184:185], v194\n" \
  "ds_read_b64_tr_b16 a[186:187], v194 offset:512\n" \
  "ds_read_b64_tr_b16 a[188:189], v194 offset:8192\n" \
  "ds_read_b64_tr_b16 a[190:191], v194 offset:8704\n" \
  "v_add_u32 v195, 98304, v205\n" \
  "ds_read_b64_tr_b16 v[0:1], v195\n" \
  "ds_read_b64_tr_b16 v[2:3], v195 offset:512\n" \
  "ds_read_b64_tr_b16 v[4:5], v195 offset:8192\n" \
  "ds_read_b64_tr_b16 v[6:7], v195 offset:8704\n" \
  "s_waitcnt lgkmcnt(0)\n" \
  "s_waitcnt vmcnt(1)\n" \
  "s_barrier\n" \
  "v_add_u32 v194, 114688, v204\n" \
  "ds_read_b64_tr_b16 a[192:193], v194\n" \
  "ds_read_b64_tr_b16 a[194:195], v194 offset:512\n" \
  "ds_read_b64_tr_b16 a[196:197], v194 offset:8192\n" \
  "ds_read_b64_tr_b16 a[198:199], v194 offset:8704\n" \
  "v_add_u32 v195, 114688, v205\n" \
  "ds_read_b64_tr_b16 v[8:9], v195\n" \
  "ds_read_b64_tr_b16 v[10:11], v195 offset:512\n" \
  "ds_read_b64_tr_b16 v[12:13], v195 offset:8192\n" \
  "ds_read_b64_tr_b16 v[14:15], v195 offset:8704\n" \
  "v_add_u32 v194, 131072, v204\n" \
  "ds_read_b64_tr_b16 a[200:201], v194\n" \
  "ds_read_b64_tr_b16 a[202:203], v194 offset:512\n" \
  "ds_read_b64_tr_b16 a[204:205], v194 offset:8192\n" \
  "ds_read_b64_tr_b16 a[206:207], v194 offset:8704\n" \
  "v_add_u32 v195, 131072, v205\n" \
  "ds_read_b64_tr_b16 v[16:17], v195\n" \
  "ds_read_b64_tr_b16 v[18:19], v195 offset:512\n" \
  "ds_read_b64_tr_b16 v[20:21], v195 offset:8192\n" \
  "ds_read_b64_tr_b16 v[22:23], v195 offset:8704\n" \
  "v_add_u32 v194, 147456, v204\n" \
  "ds_read_b64_tr_b16 a[208:209], v194\n" \
  "ds_read_b64_tr_b16 a[210:211], v194 offset:512\n" \
  "ds_read_b64_tr_b16 a[212:213], v194 offset:8192\n" \
  "ds_read_b64_tr_b16 a[214:215], v194 offset:8704\n" \
  "v_add_u32 v195, 147456, v205\n" \
  "ds_read_b64_tr_b16 v[24:25], v195\n" \
  "ds_read_b64_tr_b16 v[26:27], v195 offset:512\n" \
  "ds_read_b64_tr_b16 v[28:29], v195 offset:8192\n" \
  "ds_read_b64_tr_b16 v[30:31], v195 offset:8704\n" \
  "s_waitcnt lgkmcnt(0)\n" \
  "s_waitcnt vmcnt(0)\n" \
  "v_readfirstlane_b32 s96, v198\n" \
  "v_readfirstlane_b32 s63, v199\n" \
  "s_cmp_eq_u32 s63, s55\n" \
  "s_cselect_b32 s96, s96, 0\n" \
  "s_cmp_ge_u32 s96, 0xc0000000\n" \
  "s_cbranch_scc1 .LX_go_x_%=\n" \
  "s_mov_b32 s69, 0\n" \
  ".LX_go_%=:\n" \
  "buffer_load_dwordx2 v[196:197], off, s[40:43], %37 sc1\n" \
  "s_waitcnt vmcnt(0)\n" \
  "v_readfirstlane_b32 s96, v196\n" \
  "v_readfirstlane_b32 s63, v197\n" \
  "s_cmp_eq_u32 s63, s55\n" \
  "s_cselect_b32 s96, s96, 0\n" \
  "s_cmp_ge_u32 s96, 0xc0000000\n" \
  "s_cbranch_scc1 .LX_go_x_%=\n" \
  "s_sleep 8\n" \
  "s_add_u32 s69, s69, 1\n" \
  "s_and_b32 s63, s69, 1023\n" \
  "s_cbranch_scc1 .LX_go_%=\n" \
  "s_cmp_gt_u32 s69, 2097152\n" \
  "s_cbranch_scc1 .LX_go_set_%=\n" \
  "buffer_load_dwordx2 v[196:197], off, s[40:43], %38 offset:128 sc1\n" \
  "s_waitcnt vmcnt(0)\n" \
  "v_readfirstlane_b32 s63, v197\n" \
  "s_cmp_eq_u32 s63, s55\n" \
  "s_cbranch_scc0 .LX_go_%=\n" \
  "s_branch .LX_go_x_%=\n" \
  ".LX_go_set_%=:\n" \
  "v_mov_b32 v196, 1\n" \
  "v_mov_b32 v197, s55\n" \
  "buffer_store_dwordx2 v[196:197], off, s[40:43], %38 offset:128 sc1\n" \
  "s_waitcnt vmcnt(0)\n" \
  ".LX_go_x_%=:\n" \
  "s_and_b32 s60, s96, 2\n" \
  "s_lshl_b32 s60, s60, 12\n" \
  "s_xor_b32 s74, %34, s60\n" \
  "s_xor_b32 s75, %36, s60\n" \
  "s_bfe_u32 s56, s96, 0x30010\n" \
  "s_lshl_b32 s56, s56, 12\n" \
  "s_or_b32 s56, s56, 0x200000\n" \
  "s_mov_b32 s57, 1052672\n" \
  "s_lshl_b32 s61, %33, 2\n" \
  "s_lshr_b32 s51, s96, s61\n" \
  "s_and_b32 s51, s51, 7\n" \
  "s_lshl_b32 s51, s51, 12\n" \
  "s_xor_b32 s51, s51, 4210688\n" \
  "s_mov_b32 s71, 0\n" \
  "s_add_u32 s45, s51, 886784\n" \
  "s_mov_b32 s46, -1\n" \
  "v_mov_b64 v[32:33], -1\n" \
  "v_mov_b64 v[34:35], -1\n" \
  "v_and_b32 v194, 3, v246\n" \
  "v_lshlrev_b32 v194, 2, v194\n" \
  "v_lshrrev_b32 v194, v194, s96\n" \
  "v_and_b32 v194, 7, v194\n" \
  "v_lshlrev_b32 v194, 12, v194\n" \
  "v_and_b32 v246, -4, v246\n" \
  "v_xor_b32 v246, v246, v194\n" \
  "v_and_b32 v194, 3, v247\n" \
  "v_lshlrev_b32 v194, 2, v194\n" \
  "v_lshrrev_b32 v194, v194, s96\n" \
  "v_and_b32 v194, 7, v194\n" \
  "v_lshlrev_b32 v194, 12, v194\n" \
  "v_and_b32 v247, -4, v247\n" \
  "v_xor_b32 v247, v247, v194\n" \
  "s_xor_b32 s62, s56, s57\n" \
  "s_nop 1\n" \
  "s_andn2_b32 s60, 2, s96\n" \
  "s_lshl_b32 s60, s60, 12\n" \
  "s_xor_b32 s60, s60, 8192\n" \
  "s_xor_b32 s58, %31, s60\n" \
  "s_mov_b32 s49, 0\n" \
  "s_add_u32 s47, %41, s60\n" \
  "s_add_u32 s97, s60, 8388608\n" \
  "s_xor_b32 s50, %32, s60\n" \
  "s_mov_b32 s48, 0\n" \
  "s_mov_b32 s54, 0\n" \
  "s_cmp_eq_u32 %28, 0\n" \
  "s_cselect_b64 s[88:89], -1, 0\n" \
  "s_mov_b32 s68, 0\n" \
  "s_mov_b32 s70, 1\n" \
  "s_mov_b32 s73, 0\n" \
  "s_mov_b32 s44, 0\n" \
  "s_mov_b32 s53, 0\n" \
  "s_memrealtime s[86:87]\n" \
  "s_memtime s[76:77]\n" \
  "s_waitcnt lgkmcnt(0)\n" \
  "s_mov_b32 s90, s76\n" \
  "s_mov_b32 s87, 340\n" \
  "s_mov_b32 s64, 0x3e0293ee\n" \
  "s_mov_b32 s65, 0xbe0293ee\n" \
  "v_mov_b64 v[44:45], 0\n" \
  "v_mov_b64 v[46:47], 0\n" \
  "v_mov_b64 v[48:49], 0\n" \
  "v_mov_b64 v[50:51], 0\n" \
  "v_mov_b64 v[52:53], 0\n" \
  "v_mov_b64 v[54:55], 0\n" \
  "v_mov_b64 v[56:57], 0\n" \
  "v_mov_b64 v[58:59], 0\n" \
  "v_mov_b32 v40, 0\n" \
  "v_mov_b32 v41, s55\n" \
  "v_min_u32 v42, 1, v240\n" \
  "v_lshlrev_b32 v42, 30, v42\n" \
  "s_mov_b64 s[92:93], 0\n" \
  "s_mov_b32 s92, 0xffff0000\n" \
  "s_lshr_b32 s60, s72, 1\n" \
  "s_add_u32 s60, s60, 32768\n" \
  "v_add_u32 v178, s60, v240\n" \
  "v_xor_b32 v246, 1073741824, v246\n" \
  "v_xor_b32 v247, 1073741824, v247\n" \
  "v_mov_b32 v182, 1065369472\n" \
  "v_mov_b32 v183, 1065369472\n" \
  "s_and_b32 s60, s72, 2048\n" \
  "s_add_u32 s50, s50, s60\n" \
  "s_lshr_b32 s60, s60, 1\n" \
  "v_add_u32 v179, s60, v244\n" \
  "s_lshr_b32 s60, s72, 9\n" \
  "s_add_u32 s60, s60, 17408\n" \
  "v_mov_b32 v184, s60\n" \
  "s_mov_b32 s52, 0\n" \
  "s_barrier\n" \
  "ds_write_b32 v184, v40\n" \
  "s_branch .LX_xr_%=\n" \
  ".LX_dry_b_%=:\n" \
  "s_waitcnt vmcnt(0)\n" \
  "s_barrier\n" \
  "ds_read_b64 v[64:65], v178\n" \
  "ds_read_b64 v[66:67], v178 offset:2048\n" \
  "ds_read_b64 v[68:69], v178 offset:4096\n" \
  "ds_read_b64 v[70:71], v178 offset:6144\n" \
  "ds_read_b64 v[72:73], v178 offset:8\n" \
  "ds_read_b64 v[74:75], v178 offset:2056\n" \
  "ds_read_b64 v[76:77], v178 offset:4104\n" \
  "ds_read_b64 v[78:79], v178 offset:6152\n" \
  "v_xor_b32 v178, 8192, v178\n" \
  "s_mov_b32 s67, 2130706432\n" \
  "s_waitcnt vmcnt(0) lgkmcnt(0)\n" \
  "s_barrier\n" \
  "s_branch .LX_p_%=\n" \
  "s_nop 7\n" \
  ".p2align 6\n" \
  ".fill (((40 - (.LX_p_%= - .LX_rot_%=)) & 63) >> 2), 4, 0xBF800000\n" \
  ".LX_rot_%=:\n" \
  "s_xor_b32 s51, s51, s56\n" \
  "s_xor_b32 s71, s71, s56\n" \
  "v_xor_b32 v246, s56, v246\n" \
  "v_xor_b32 v247, s56, v247\n" \
  "s_mov_b32 s60, s56\n" \
  "s_mov_b32 s56, s57\n" \
  "s_mov_b32 s57, s60\n" \
  "s_add_u32 s59, s51, 819200\n" \
  "s_mov_b32 m0, 0\n" \
  "s_mov_b32 s70, 0\n" \
  "s_mov_b32 s68, 0\n" \
  "s_waitcnt lgkmcnt(0)\n" \
  "s_cmp_lg_u32 %28, 0\n" \
  "s_cbranch_scc1 .LX_g3_%=\n" \
  "s_mov_b32 s69, 0\n" \
  ".LX_tw_%=:\n" \
  "v_readfirstlane_b32 s63, v36\n" \
  "s_cmp_lg_u32 s63, s48\n" \
  "s_cbranch_scc1 .LX_tw_re_%=\n" \
  "v_readfirstlane_b32 s53, v38\n" \
  ".LX_rt_%=:\n" \
  "s_memrealtime s[60:61]\n" \
  "s_add_u32 s73, s53, s44\n" \
  "s_waitcnt lgkmcnt(0)\n" \
  "s_sub_u32 s62, s73, s60\n" \
  "s_cmp_ge_u32 s62, 2000\n" \
  "s_cbranch_scc1 .LX_lat_%=\n" \
  "s_sub_u32 s62, s62, 30\n" \
  ".LX_slp_%=:\n" \
  "s_cmp_le_i32 s62, 0\n" \
  "s_cbranch_scc1 .LX_clk_%=\n" \
  "s_sleep 1\n" \
  "s_sub_u32 s62, s62, 3\n" \
  "s_branch .LX_slp_%=\n" \
  ".LX_clk_%=:\n" \
  "s_memrealtime s[60:61]\n" \
  "s_waitcnt lgkmcnt(0)\n" \
  "s_sub_u32 s62, s73, s60\n" \
  "s_cmp_gt_i32 s62, 21\n" \
  "s_cbranch_scc1 .LX_clk_%=\n" \
  "s_cmp_le_i32 s62, 11\n" \
  "s_cbranch_scc1 .LX_clk2_%=\n" \
  "s_memrealtime s[76:77]\n" \
  "s_nop 4\n" \
  "s_memrealtime s[78:79]\n" \
  "s_nop 4\n" \
  "s_memrealtime s[80:81]\n" \
  "s_nop 4\n" \
  "s_memrealtime s[82:83]\n" \
  "s_waitcnt lgkmcnt(0)\n" \
  "s_mov_b32 s77, 1\n" \
  "s_cmp_eq_u32 s78, s76\n" \
  "s_addc_u32 s77, s77, 0\n" \
  "s_cmp_eq_u32 s80, s76\n" \
  "s_addc_u32 s77, s77, 0\n" \
  "s_cmp_eq_u32 s82, s76\n" \
  "s_addc_u32 s77, s77, 0\n" \
  "s_mul_i32 s77, s77, 24\n" \
  "s_lshr_b32 s78, s87, 2\n" \
  "s_sub_u32 s77, s78, s77\n" \
  "s_max_i32 s77, s77, 0\n" \
  "s_sub_u32 s76, s73, s76\n" \
  "s_mul_i32 s76, s76, s87\n" \
  "s_ashr_i32 s76, s76, 4\n" \
  "s_sub_u32 s76, s76, s77\n" \
  "s_sub_u32 s76, s76, 200\n" \
  "s_max_i32 s76, s76, 0\n" \
  "s_min_i32 s76, s76, 320\n" \
  "s_lshr_b32 s76, s76, 3\n" \
  "s_lshl_b32 s76, s76, 2\n" \
  "s_mov_b32 s68, 1\n" \
  "s_getpc_b64 s[78:79]\n" \
  "s_sub_u32 s76, 180, s76\n" \
  "s_add_u32 s78, s78, s76\n" \
  "s_addc_u32 s79, s79, 0\n" \
  "s_setpc_b64 s[78:79]\n" \
  "s_nop 1\n" \
  "s_nop 1\n" \
  "s_nop 1\n" \
  "s_nop 1\n" \
  "s_nop 1\n" \
  "s_nop 1\n" \
  "s_nop 1\n" \
  "s_nop 1\n" \
  "s_nop 1\n" \
  "s_nop 1\n" \
  "s_nop 1\n" \
  "s_nop 1\n" \
  "s_nop 1\n" \
  "s_nop 1\n" \
  "s_nop 1\n" \
  "s_nop 1\n" \
  "s_nop 1\n" \
  "s_nop 1\n" \
  "s_nop 1\n" \
  "s_nop 1\n" \
  "s_nop 1\n" \
  "s_nop 1\n" \
  "s_nop 1\n" \
  "s_nop 1\n" \
  "s_nop 1\n" \
  "s_nop 1\n" \
  "s_nop 1\n" \
  "s_nop 1\n" \
  "s_nop 1\n" \
  "s_nop 1\n" \
  "s_nop 1\n" \
  "s_nop 1\n" \
  "s_nop 1\n" \
  "s_nop 1\n" \
  "s_nop 1\n" \
  "s_nop 1\n" \
  "s_nop 1\n" \
  "s_nop 1\n" \
  "s_nop 1\n" \
  "s_nop 1\n" \
  ".LX_noclk_%=:\n" \
  "buffer_load_dwordx4 v240, s[40:43], s59 offen sc1 lds\n" \
  "s_memtime s[66:67]\n" \
  "buffer_load_dwordx4 v240, s[40:43], s59 offen offset:1024 sc1 lds\n" \
  "buffer_load_dwordx4 v240, s[40:43], s59 offen offset:2048 sc1 lds\n" \
  "buffer_load_dwordx4 v240, s[40:43], s59 offen offset:3072 sc1 lds\n" \
  "s_add_u32 s60, s59, 32768\n" \
  "s_movk_i32 m0, 4096\n" \
  "s_nop 0\n" \
  "buffer_load_dwordx4 v240, s[40:43], s60 offen sc1 lds\n" \
  "buffer_load_dwordx4 v240, s[40:43], s60 offen offset:1024 sc1 lds\n" \
  "buffer_load_dwordx4 v240, s[40:43], s60 offen offset:2048 sc1 lds\n" \
  "buffer_load_dwordx4 v240, s[40:43], s60 offen offset:3072 sc1 lds\n" \
  "s_add_u32 s60, s59, 65536\n" \
  "s_movk_i32 m0, 8192\n" \
  "s_nop 0\n" \
  "buffer_load_dwordx4 v240, s[40:43], s60 offen sc1 lds\n" \
  ".LX_g3_%=:\n" \
  "s_waitcnt vmcnt(6)\n" \
  "s_barrier\n" \
  ".LX_p_%=:\n" \
  "ds_read_b128 v[100:103], v240\n" \
  "ds_read_b128 v[104:107], v240 offset:1024\n" \
  "ds_read_b128 v[108:111], v240 offset:2048\n" \
  "s_waitcnt lgkmcnt(2)\n" \
  "v_smfmac_f32_16x16x64_bf16 v[44:47], v[100:103], a[0:7], v239\n" \
  "s_waitcnt vmcnt(0)\n" \
  "s_barrier\n" \
  "s_waitcnt lgkmcnt(1)\n" \
  "v_smfmac_f32_16x16x64_bf16 v[44:47], v[104:107], a[8:15], v239\n" \
  "ds_read_b128 v[112:115], v240 offset:3072\n" \
  "v_smfmac_f32_16x16x64_bf16 v[48:51], v[100:103], a[72:79], v239\n" \
  "ds_read_b128 v[116:119], v240 offset:6144\n" \
  "s_waitcnt lgkmcnt(2)\n" \
  "v_smfmac_f32_16x16x64_bf16 v[44:47], v[108:111], a[16:23], v239\n" \
  "ds_read_b128 v[120:123], v240 offset:4096\n" \
  "v_smfmac_f32_16x16x64_bf16 v[48:51], v[104:107], a[80:87], v239\n" \
  "ds_read_b128 v[124:127], v240 offset:5120\n" \
  "v_smfmac_f32_16x16x64_bf16 v[48:51], v[108:111], a[88:95], v239\n" \
  "ds_read_b128 v[128:131], v240 offset:7168\n" \
  "s_waitcnt lgkmcnt(4)\n" \
  "v_smfmac_f32_16x16x64_bf16 v[44:47], v[112:115], a[24:31], v239\n" \
  "ds_read_b128 v[132:135], v240 offset:8192\n" \
  "v_smfmac_f32_16x16x64_bf16 v[48:51], v[112:115], a[96:103], v239\n" \
  "s_waitcnt lgkmcnt(3)\n" \
  "v_smfmac_f32_16x16x64_bf16 v[44:47], v[120:123], a[32:39], v239\n" \
  "s_waitcnt lgkmcnt(2)\n" \
  "v_smfmac_f32_16x16x64_bf16 v[44:47], v[124:127], a[40:47], v239\n" \
  "v_smfmac_f32_16x16x64_bf16 v[44:47], v[116:119], a[48:55], v239\n" \
  "s_waitcnt lgkmcnt(1)\n" \
  "v_smfmac_f32_16x16x64_bf16 v[44:47], v[128:131], a[56:63], v239\n" \
  "s_waitcnt lgkmcnt(0)\n" \
  "v_smfmac_f32_16x16x64_bf16 v[44:47], v[132:135], a[64:71], v239\n" \
  "s_nop 2\n" \
  "v_smfmac_f32_16x16x64_bf16 v[48:51], v[120:123], a[104:111], v239\n" \
  "s_nop 2\n" \
  "v_smfmac_f32_16x16x64_bf16 v[48:51], v[124:127], a[112:119], v239\n" \
  "v_add_f32 v45, v45, v47\n" \
  "v_smfmac_f32_16x16x64_bf16 v[48:51], v[116:119], a[120:127], v239\n" \
  "v_add_f32 v44, v44, v46\n" \
  "v_cmp_u_f32 vcc, v44, v45\n" \
  "v_smfmac_f32_16x16x64_bf16 v[48:51], v[128:131], a[128:135], v239\n" \
  "v_mul_f32 v45, s64, v45\n" \
  "v_smfmac_f32_16x16x64_bf16 v[48:51], v[132:135], a[136:143], v239\n" \
  "s_cbranch_vccnz .LX_slow_%=\n" \
  ".LX_back_%=:\n" \
  "s_cmp_eq_u32 s48, 63\n" \
  "v_mul_f32 v44, s64, v44\n" \
  "v_cvt_pk_bf16_f32 v44, v44, v45\n" \
  "v_smfmac_f32_16x16x64_bf16 v[52:55], v[120:123], a[176:183], v239\n" \
  "ds_write_b32 v241, v44\n" \
  "s_nop 1\n" \
  "v_add_f32 v48, v48, v50\n" \
  "v_add_f32 v49, v49, v51\n" \
  "v_mul_f32 v49, s64, v49\n" \
  "v_mul_f32 v48, s64, v48\n" \
  "v_cvt_pk_bf16_f32 v48, v48, v49\n" \
  "v_smfmac_f32_16x16x64_bf16 v[52:55], v[124:127], a[184:191], v239\n" \
  "ds_write_b32 v241, v48 offset:256\n" \
  "v_smfmac_f32_16x16x64_bf16 v[52:55], v[116:119], a[192:199], v239\n" \
  "v_smfmac_f32_16x16x64_bf16 v[56:59], v[100:103], a[216:223], v239\n" \
  "s_waitcnt lgkmcnt(1)\n" \
  "s_barrier\n" \
  "v_smfmac_f32_16x16x64_bf16 v[52:55], v[128:131], a[200:207], v239\n" \
  "ds_read_b64_tr_b16 v[102:103], v243 offset:1536\n" \
  "ds_read_b64_tr_b16 v[100:101], v243 offset:1024\n" \
  "v_smfmac_f32_16x16x64_bf16 v[52:55], v[132:135], a[208:215], v239\n" \
  "ds_read_b64_tr_b16 v[138:139], v243 offset:512\n" \
  "ds_read_b64_tr_b16 v[136:137], v243\n" \
  "v_smfmac_f32_16x16x64_bf16 v[56:59], v[104:107], a[224:231], v239\n" \
  "s_waitcnt lgkmcnt(4)\n" \
  "s_barrier\n" \
  "v_smfmac_f32_16x16x64_bf16 v[56:59], v[108:111], a[232:239], v239\n" \
  "ds_read_b64_tr_b16 v[104:105], v243 offset:256\n" \
  "ds_read_b64_tr_b16 v[106:107], v243 offset:768\n" \
  "ds_read_b64_tr_b16 v[110:111], v243 offset:1792\n" \
  "v_smfmac_f32_16x16x64_bf16 v[56:59], v[112:115], a[240:247], v239\n" \
  "ds_read_b64_tr_b16 v[108:109], v243 offset:1280\n" \
  "s_waitcnt lgkmcnt(4)\n" \
  "v_mfma_f32_16x16x32_bf16 v[112:115], v[64:67], v[136:139], 0\n" \
  "v_add_f32 v52, v52, v54\n" \
  "v_mfma_f32_16x16x32_bf16 v[112:115], v[68:71], v[100:103], v[112:115]\n" \
  "s_waitcnt lgkmcnt(2)\n" \
  "v_mfma_f32_16x16x32_bf16 v[112:115], v[72:75], v[104:107], v[112:115]\n" \
  "s_waitcnt lgkmcnt(0)\n" \
  "v_mfma_f32_16x16x32_bf16 v[112:115], v[76:79], v[108:111], v[112:115]\n" \
  "s_nop 1\n" \
  "v_add_f32 v53, v53, v55\n" \
  "v_smfmac_f32_16x16x64_bf16 v[56:59], v[120:123], a[248:255], v239\n" \
  "s_barrier\n" \
  "ds_read_b64 v[188:189], v255\n" \
  "s_nop 1\n" \
  "v_exp_f32 v100, v112\n" \
  "v_exp_f32 v101, v113\n" \
  "v_exp_f32 v103, v115\n" \
  "v_exp_f32 v102, v114\n" \
  "v_cvt_pk_bf16_f32 v186, v100, v101\n" \
  "v_cvt_pk_bf16_f32 v187, v102, v103\n" \
  "v_smfmac_f32_16x16x64_bf16 v[56:59], v[124:127], v[0:7], v239\n" \
  "buffer_store_dwordx2 v[186:187], v246, s[40:43], 0 offen sc1\n" \
  "buffer_store_dwordx2 v[186:187], v247, s[40:43], 0 offen sc1\n" \
  "v_mfma_f32_16x16x16_bf16 v[100:103], v[182:183], v[186:187], 0\n" \
  "s_nop 0\n" \
  "s_waitcnt lgkmcnt(0)\n" \
  "v_add_f32 v53, v53, v189\n" \
  "v_smfmac_f32_16x16x64_bf16 v[56:59], v[116:119], v[8:15], v239\n" \
  "v_add_f32 v52, v52, v188\n" \
  "v_smfmac_f32_16x16x64_bf16 v[56:59], v[128:131], v[16:23], v239\n" \
  "v_cvt_pk_bf16_f32 v52, v52, v53\n" \
  "v_smfmac_f32_16x16x64_bf16 v[56:59], v[132:135], v[24:31], v239\n" \
  "ds_write_b32 v248, v100\n" \
  "ds_write_b32 v242, v52\n" \
  "s_nop 5\n" \
  "v_add_f32 v57, v57, v59\n" \
  "v_add_f32 v56, v56, v58\n" \
  "v_cvt_pk_bf16_f32 v56, v56, v57\n" \
  "ds_write_b32 v242, v56 offset:256\n" \
  "s_waitcnt lgkmcnt(1)\n" \
  "s_barrier\n" \
  "s_waitcnt lgkmcnt(0)\n" \
  "s_barrier\n" \
  "s_cbranch_scc1 .LX_xr_%=\n" \
  "s_memrealtime s[98:99]\n" \
  "s_memtime s[84:85]\n" \
  ".LX_loop_%=:\n" \
  "s_cmp_ge_u32 s48, 55\n" \
  "s_cbranch_scc1 .LX_xl_%=\n" \
  ".LX_xr_b_%=:\n" \
  "s_add_u32 s95, s48, 1\n" \
  "s_and_b32 s63, s95, 3\n" \
  "s_lshl_b32 s95, s95, 5\n" \
  "s_lshl_b32 s63, s63, 4\n" \
  "s_sub_u32 s95, s95, s63\n" \
  "s_add_u32 s95, s95, s45\n" \
  "s_mov_b64 exec, s[88:89]\n" \
  "buffer_load_dwordx4 v[36:39], off, s[40:43], s95 sc1\n" \
  "s_mov_b64 exec, -1\n" \
  "v_and_b32 v246, 3221225471, v246\n" \
  "v_and_b32 v247, 3221225471, v247\n" \
  "s_waitcnt lgkmcnt(0)\n" \
  "s_lshl_b32 s60, s66, 10\n" \
  "s_or_b32 s60, s60, s87\n" \
  "s_cmp_lg_u32 s68, 0\n" \
  "s_cselect_b32 s60, s60, 0\n" \
  "v_mov_b32 v187, s60\n" \
  "s_sub_u32 s76, s84, s90\n" \
  "s_sub_u32 s77, s98, s86\n" \
  "v_cvt_f32_u32 v188, s76\n" \
  "v_cvt_f32_u32 v189, s77\n" \
  "v_rcp_f32 v189, v189\n" \
  "v_mul_f32 v188, 16.0, v188\n" \
  "v_fma_f32 v188, v188, v189, 0.5\n" \
  "v_cvt_u32_f32 v188, v188\n" \
  "s_nop 0\n" \
  "v_readfirstlane_b32 s76, v188\n" \
  "s_max_u32 s76, s76, 160\n" \
  "s_min_u32 s76, s76, 400\n" \
  "s_add_u32 s77, s48, 0\n" \
  "s_and_b32 s77, s77, 3\n" \
  "s_cselect_b32 s87, s87, s76\n" \
  "s_cselect_b32 s86, s86, s98\n" \
  "s_cselect_b32 s90, s90, s84\n" \
  "s_add_u32 s48, s48, 1\n" \
  "ds_read_b64 v[64:65], v178\n" \
  "ds_read_b64 v[66:67], v178 offset:2048\n" \
  "ds_read_b64 v[68:69], v178 offset:4096\n" \
  "ds_read_b64 v[70:71], v178 offset:6144\n" \
  "ds_read_b64 v[72:73], v178 offset:8\n" \
  "ds_read_b64 v[74:75], v178 offset:2056\n" \
  "ds_read_b64 v[76:77], v178 offset:4104\n" \
  "ds_read_b64 v[78:79], v178 offset:6152\n" \
  "v_xor_b32 v178, 8192, v178\n" \
  "v_mov_b64 v[44:45], 0\n" \
  "v_mov_b64 v[46:47], 0\n" \
  "v_mov_b64 v[48:49], 0\n" \
  "v_mov_b64 v[50:51], 0\n" \
  "v_mov_b64 v[52:53], 0\n" \
  "v_mov_b64 v[54:55], 0\n" \
  "v_mov_b64 v[56:57], 0\n" \
  "v_mov_b64 v[58:59], 0\n" \
  "v_mov_b32 v40, s48\n" \
  "s_waitcnt vmcnt(0)\n" \
  "ds_write2_b32 v184, v40, v187 offset1:2\n" \
  "s_cmp_eq_u32 s48, 58\n" \
  "s_cbranch_scc1 .LX_ef_%=\n" \
  ".LX_ef_b_%=:\n" \
  "s_branch .LX_rot_%=\n" \
  "s_nop 7\n" \
  ".LX_slow_%=:\n" \
  "s_bitcmp1_b32 s70, 0\n" \
  "s_cbranch_scc1 .LX_back_%=\n" \
  "s_waitcnt lgkmcnt(0)\n" \
  "s_cmp_lt_u32 s70, 10\n" \
  "s_nop 3\n" \
  "s_cbranch_scc1 .LX_nol_%=\n" \
  "v_max_u32 v189, v100, v101\n" \
  "v_max3_u32 v189, v189, v102, v103\n" \
  "v_max3_u32 v189, v189, v104, v105\n" \
  "v_max3_u32 v189, v189, v106, v107\n" \
  "v_max3_u32 v189, v189, v108, v109\n" \
  "v_max3_u32 v189, v189, v110, v111\n" \
  "v_max3_u32 v189, v189, v112, v113\n" \
  "v_max3_u32 v189, v189, v114, v115\n" \
  "v_max3_u32 v189, v189, v120, v121\n" \
  "v_max3_u32 v189, v189, v122, v123\n" \
  "v_max3_u32 v189, v189, v124, v125\n" \
  "v_max3_u32 v189, v189, v126, v127\n" \
  "v_max3_u32 v189, v189, v116, v117\n" \
  "v_max3_u32 v189, v189, v118, v119\n" \
  "v_max3_u32 v189, v189, v128, v129\n" \
  "v_max3_u32 v189, v189, v130, v131\n" \
  "v_max3_u32 v189, v189, v132, v133\n" \
  "v_max3_u32 v189, v189, v134, v135\n" \
  "v_cmp_eq_u32 vcc, -1, v189\n" \
  "s_cbranch_vccz .LX_back_%=\n" \
  ".LX_nol_%=:\n" \
  "s_add_u32 s70, s70, 2\n" \
  "s_cmp_gt_u32 s70, 65536\n" \
  "s_cbranch_scc1 .LX_dset_%=\n" \
  "s_barrier\n" \
  "v_mov_b64 v[44:45], 0\n" \
  "v_mov_b64 v[46:47], 0\n" \
  "v_mov_b64 v[48:49], 0\n" \
  "v_mov_b64 v[50:51], 0\n" \
  "v_mov_b64 v[52:53], 0\n" \
  "v_mov_b64 v[54:55], 0\n" \
  "v_mov_b64 v[56:57], 0\n" \
  "v_mov_b64 v[58:59], 0\n" \
  "s_cmp_lg_u32 %28, 0\n" \
  "s_cbranch_scc1 .LX_g3_%=\n" \
  "s_and_b32 s62, %40, 0xffff\n" \
  "s_add_u32 s44, s44, s62\n" \
  "s_xor_b32 s59, s59, 0\n" \
  "s_mov_b32 s68, 0\n" \
  "s_mov_b32 m0, 0\n" \
  "s_branch .LX_rt_%=\n" \
  ".LX_dset_%=:\n" \
  "v_mov_b32 v124, 1\n" \
  "v_mov_b32 v125, s55\n" \
  "buffer_store_dwordx2 v[124:125], off, s[40:43], %38 offset:128 sc1\n" \
  "s_mov_b32 s70, 1\n" \
  "v_mov_b64 v[44:45], 0\n" \
  "v_mov_b64 v[46:47], 0\n" \
  "v_mov_b64 v[48:49], 0\n" \
  "v_mov_b64 v[50:51], 0\n" \
  "v_mov_b64 v[52:53], 0\n" \
  "v_mov_b64 v[54:55], 0\n" \
  "v_mov_b64 v[56:57], 0\n" \
  "v_mov_b64 v[58:59], 0\n" \
  "s_waitcnt vmcnt(0) lgkmcnt(0)\n" \
  "s_barrier\n" \
  "s_branch .LX_p_%=\n" \
  ".LX_xr_%=:\n" \
  "s_waitcnt lgkmcnt(0)\n" \
  "s_nop 7\n" \
  "ds_read_b64_tr_b16 v[132:133], v244\n" \
  "ds_read_b64_tr_b16 v[134:135], v244 offset:256\n" \
  "ds_read_b64_tr_b16 v[136:137], v244 offset:512\n" \
  "ds_read_b64_tr_b16 v[138:139], v244 offset:768\n" \
  "ds_read_b64_tr_b16 v[140:141], v244 offset:1024\n" \
  "ds_read_b64_tr_b16 v[142:143], v244 offset:1280\n" \
  "ds_read_b64_tr_b16 v[144:145], v244 offset:1536\n" \
  "ds_read_b64_tr_b16 v[146:147], v244 offset:1792\n" \
  "s_waitcnt lgkmcnt(6)\n" \
  "v_mfma_f32_16x16x32_bf16 v[148:151], v[132:135], v[84:87], 0\n" \
  "s_waitcnt lgkmcnt(4)\n" \
  "v_mfma_f32_16x16x32_bf16 v[148:151], v[136:139], v[88:91], v[148:151]\n" \
  "s_waitcnt lgkmcnt(2)\n" \
  "v_mfma_f32_16x16x32_bf16 v[148:151], v[140:143], v[92:95], v[148:151]\n" \
  "s_waitcnt lgkmcnt(0)\n" \
  "v_mfma_f32_16x16x32_bf16 v[148:151], v[144:147], v[96:99], v[148:151]\n" \
  "s_sub_u32 s60, s48, 58\n" \
  "s_lshl_b32 s60, s60, 16\n" \
  "s_add_u32 s60, s60, s97\n" \
  "s_cmp_eq_u32 s48, 0\n" \
  "s_cselect_b32 s60, 2130706432, s60\n" \
  "s_nop 2\n" \
  "v_max_f32 v158, v148, v149\n" \
  "v_max3_f32 v158, v150, v151, v158\n" \
  "v_mov_b32 v159, v158\n" \
  "s_nop 1\n" \
  "v_permlane32_swap_b32 v158, v159\n" \
  "v_max_f32 v158, v158, v159\n" \
  "v_mul_f32 v152, s65, v158\n" \
  "v_min_f32 v152, v152, v180\n" \
  "v_fma_f32 v148, v148, s64, v152\n" \
  "v_fma_f32 v149, v149, s64, v152\n" \
  "v_fma_f32 v150, v150, s64, v152\n" \
  "v_fma_f32 v151, v151, s64, v152\n" \
  "v_sub_f32 v160, v152, v180\n" \
  "v_exp_f32 v148, v148\n" \
  "v_exp_f32 v149, v149\n" \
  "v_exp_f32 v150, v150\n" \
  "v_exp_f32 v151, v151\n" \
  "v_exp_f32 v160, v160\n" \
  "v_add_f32 v153, v148, v149\n" \
  "v_add_f32 v158, v150, v151\n" \
  "v_add_f32 v153, v153, v158\n" \
  "v_mov_b32 v159, v153\n" \
  "s_nop 1\n" \
  "v_permlane32_swap_b32 v153, v159\n" \
  "v_add_f32 v153, v153, v159\n" \
  "v_fmac_f32 v153, v160, v181\n" \
  "v_rcp_f32 v154, v153\n" \
  "s_nop 0\n" \
  "v_mul_f32 v160, v160, v154\n" \
  "v_mul_f32 v154, v154, v252\n" \
  "v_mul_f32 v148, v148, v154\n" \
  "v_mul_f32 v149, v149, v154\n" \
  "v_mul_f32 v150, v150, v154\n" \
  "v_mul_f32 v151, v151, v154\n" \
  "v_fmac_f32 v148, v160, v253\n" \
  "v_fmac_f32 v150, v160, v254\n" \
  "v_cvt_pk_bf16_f32 v156, v148, v149\n" \
  "v_cvt_pk_bf16_f32 v157, v150, v151\n" \
  "buffer_store_dwordx2 v[156:157], v251, s[40:43], s60 offen sc1\n" \
  "s_cmp_eq_u32 s48, 63\n" \
  "s_cbranch_scc1 .LX_end_%=\n" \
  ".LX_xq_%=:\n" \
  "s_add_u32 s61, s48, 1\n" \
  "s_lshl_b32 s60, s61, 19\n" \
  "s_add_u32 s60, s60, s47\n" \
  "s_lshr_b32 s62, s60, 8\n" \
  "s_and_b32 s62, s62, 8192\n" \
  "s_xor_b32 s62, s62, s60\n" \
  "s_cmp_eq_u32 s48, 0\n" \
  "s_cselect_b32 s62, 2130706432, s62\n" \
  "s_add_u32 s63, s62, 4096\n" \
  "buffer_load_dwordx4 v[84:87], v249, s[40:43], s62 offen sc1\n" \
  "buffer_load_dwordx4 v[88:91], v249, s[40:43], s62 offen offset:2048 sc1\n" \
  "buffer_load_dwordx4 v[92:95], v249, s[40:43], s63 offen sc1\n" \
  "buffer_load_dwordx4 v[96:99], v249, s[40:43], s63 offen offset:2048 sc1\n" \
  "s_sub_u32 s60, s61, 58\n" \
  "s_lshl_b32 s60, s60, 16\n" \
  "s_add_u32 s60, s60, s97\n" \
  "s_cmp_eq_u32 s48, 0\n" \
  "s_cselect_b32 s60, 2130706432, s60\n" \
  "buffer_load_dwordx2 v[180:181], v250, s[40:43], s60 offen sc1\n" \
  "s_cbranch_scc1 .LX_dry_b_%=\n" \
  "s_branch .LX_xr_b_%=\n" \
  ".LX_xl_%=:\n" \
  "s_cmp_eq_u32 s48, 57\n" \
  "s_cbranch_scc1 .LX_xq_%=\n" \
  "s_cmp_eq_u32 s48, 56\n" \
  "s_cbranch_scc1 .LX_xc_%=\n" \
  "s_cmp_lg_u32 s48, 55\n" \
  "s_cbranch_scc1 .LX_xr_%=\n" \
  "buffer_load_dwordx2 v[180:181], off, s[40:43], %42 sc1\n" \
  "s_branch .LX_xr_b_%=\n" \
  ".LX_xc_%=:\n" \
  "v_readfirstlane_b32 s63, v181\n" \
  "s_cmp_eq_u32 s63, s55\n" \
  "s_cbranch_scc1 .LX_xr_b_%=\n" \
  "s_mov_b32 s69, 0\n" \
  ".LX_xs_%=:\n" \
  "buffer_load_dwordx2 v[180:181], off, s[40:43], %42 sc1\n" \
  "s_waitcnt vmcnt(0)\n" \
  "v_readfirstlane_b32 s62, v180\n" \
  "v_readfirstlane_b32 s63, v181\n" \
  "s_cmp_eq_u32 s63, s55\n" \
  "s_cselect_b32 s62, s62, 0\n" \
  "s_cmp_lg_u32 s62, 0\n" \
  "s_cbranch_scc1 .LX_xs_x_%=\n" \
  "s_sleep 2\n" \
  "s_add_u32 s69, s69, 1\n" \
  "s_and_b32 s63, s69, 1023\n" \
  "s_cbranch_scc1 .LX_xs_%=\n" \
  "s_cmp_gt_u32 s69, 2097152\n" \
  "s_cbranch_scc1 .LX_xs_set_%=\n" \
  "buffer_load_dwordx2 v[180:181], off, s[40:43], %38 offset:128 sc1\n" \
  "s_waitcnt vmcnt(0)\n" \
  "v_readfirstlane_b32 s63, v181\n" \
  "s_cmp_eq_u32 s63, s55\n" \
  "s_cbranch_scc0 .LX_xs_%=\n" \
  "s_branch .LX_xs_x_%=\n" \
  ".LX_xs_set_%=:\n" \
  "v_mov_b32 v180, 1\n" \
  "v_mov_b32 v181, s55\n" \
  "buffer_store_dwordx2 v[180:181], off, s[40:43], %38 offset:128 sc1\n" \
  "s_waitcnt vmcnt(0)\n" \
  ".LX_xs_x_%=:\n" \
  "s_branch .LX_xr_b_%=\n" \
  ".LX_ef_%=:\n" \
  "s_waitcnt vmcnt(0) lgkmcnt(0)\n" \
  "s_branch .LX_ef_b_%=\n" \
  ".LX_tw_re_%=:\n" \
  "s_add_u32 s69, s69, 1\n" \
  "s_cmp_gt_u32 s69, 200\n" \
  "s_cbranch_scc1 .LX_noclk_%=\n" \
  "s_sleep 2\n" \
  "buffer_load_dwordx4 v[36:39], off, s[40:43], s95 sc1\n" \
  "s_waitcnt vmcnt(0)\n" \
  "s_branch .LX_tw_%=\n" \
  ".LX_lat_%=:\n" \
  "s_lshr_b32 s63, %40, 16\n" \
  "s_add_u32 s62, s62, s63\n" \
  "s_cmp_lt_u32 s62, 4294937296\n" \
  "s_cbranch_scc1 .LX_noclk_%=\n" \
  "s_and_b32 s63, %40, 0xffff\n" \
  ".LX_lat1_%=:\n" \
  "s_add_u32 s44, s44, s63\n" \
  "s_add_u32 s62, s62, s63\n" \
  "s_cbranch_scc0 .LX_lat1_%=\n" \
  "s_branch .LX_rt_%=\n" \
  ".LX_clk2_%=:\n" \
  "s_cmp_le_i32 s62, 3\n" \
  "s_cbranch_scc1 .LX_clk_done_%=\n" \
  "s_memrealtime s[60:61]\n" \
  "s_waitcnt lgkmcnt(0)\n" \
  "s_sub_u32 s62, s73, s60\n" \
  "s_branch .LX_clk2_%=\n" \
  ".LX_clk_done_%=:\n" \
  "s_branch .LX_noclk_%=\n" \
  ".LX_end_%=:\n" \
  "s_waitcnt vmcnt(0)\n" \
  "s_waitcnt vmcnt(0)\n" \
  "s_nop 7\n" \
  "s_endpgm\n" \
  ".LX_other_%=:\n"
#define X_ASM_VOPS "+{v239}"(ci.sidx), "+{v240}"(ci.l16), "+{v241}"(ci.qw), "+{v242}"(ci.kw), "+{v243}"(ci.fq), "+{v244}"(ci.fk), "+{v245}"(ci.kxo), "+{v246}"(ci.pst), "+{v247}"(ci.ps2), "+{v248}"(ci.lx), "+{v249}"(ci.qxo), "+{v250}"(ci.sto), "+{v251}"(ci.plx), "+{v252}"(ci.mk), "+{v253}"(ci.ck0), "+{v254}"(ci.ck2), "+{v255}"(ci.hx), "+{v200}"(ci.vq0), "+{v201}"(ci.vq1), "+{v202}"(ci.vq2), "+{v203}"(ci.vq3), "+{v204}"(ci.tro0), "+{v205}"(ci.tro1), "+{v206}"(ci.m0o)
#define X_ASM_SOPS "s"(ci.ws), "s"(ci.Wa), "s"(ci.Wb), "s"(ci.mem0), "s"(ci.w), "s"(ci.o_a), "s"(ci.o_b), "s"(ci.o_nx), "s"(ci.o_exp), "s"(ci.lab), "s"(ci.f_p), "s"(ci.f_m), "s"(ci.f_dur), "s"(ci.f_go), "s"(ci.o_ctl), "s"(ci.o_dbg), "s"(ci.dret), "s"(ci.o_qx), "s"(ci.f_s), "s"(ci.f_r)
#define X_ASM_CLOB "v0", "v1", "v2", "v3", "v4", "v5", "v6", "v7", "v8", "v9", "v10", "v11", "v12", "v13", "v14", "v15", "v16", "v17", "v18", "v19", "v20", "v21", "v22", "v23", "v24", "v25", "v26", "v27", "v28", "v29", "v30", "v31", "v32", "v33", "v34", "v35", "v36", "v37", "v38", "v39", "v40", "v41", "v42", "v43", "v44", "v45", "v46", "v47", "v48", "v49", "v50", "v51", "v52", "v53", "v54", "v55", "v56", "v57", "v58", "v59", "v60", "v61", "v62", "v63", "v64", "v65", "v66", "v67", "v68", "v69", "v70", "v71", "v72", "v73", "v74", "v75", "v76", "v77", "v78", "v79", "v80", "v81", "v82", "v83", "v84", "v85", "v86", "v87", "v88", "v89", "v90", "v91", "v92", "v93", "v94", "v95", "v96", "v97", "v98", "v99", "v100", "v101", "v102", "v103", "v104", "v105", "v106", "v107", "v108", "v109", "v110", "v111", "v112", "v113", "v114", "v115", "v116", "v117", "v118", "v119", "v120", "v121", "v122", "v123", "v124", "v125", "v126", "v127", "v128", "v129", "v130", "v131", "v132", "v133", "v134", "v135", "v136", "v137", "v138", "v139", "v140", "v141", "v142", "v143", "v144", "v145", "v146", "v147", "v148", "v149", "v150", "v151", "v152", "v153", "v154", "v155", "v156", "v157", "v158", "v159", "v160", "v161", "v162", "v163", "v164", "v165", "v166", "v167", "v168", "v169", "v170", "v171", "v172", "v173", "v174", "v175", "v176", "v177", "v178", "v179", "v180", "v181", "v182", "v183", "v184", "v185", "v186", "v187", "v188", "v189", "v190", "v191", "v192", "v193", "v194", "v195", "v196", "v197", "v198", "v199", "a0", "a1", "a2", "a3", "a4", "a5", "a6", "a7", "a8", "a9", "a10", "a11", "a12", "a13", "a14", "a15", "a16", "a17", "a18", "a19", "a20", "a21", "a22", "a23", "a24", "a25", "a26", "a27", "a28", "a29", "a30", "a31", "a32", "a33", "a34", "a35", "a36", "a37", "a38", "a39", "a40", "a41", "a42", "a43", "a44", "a45", "a46", "a47", "a48", "a49", "a50", "a51", "a52", "a53", "a54", "a55", "a56", "a57", "a58", "a59", "a60", "a61", "a62", "a63", "a64", "a65", "a66", "a67", "a68", "a69", "a70", "a71", "a72", "a73", "a74", "a75", "a76", "a77", "a78", "a79", "a80", "a81", "a82", "a83", "a84", "a85", "a86", "a87", "a88", "a89", "a90", "a91", "a92", "a93", "a94", "a95", "a96", "a97", "a98", "a99", "a100", "a101", "a102", "a103", "a104", "a105", "a106", "a107", "a108", "a109", "a110", "a111", "a112", "a113", "a114", "a115", "a116", "a117", "a118", "a119", "a120", "a121", "a122", "a123", "a124", "a125", "a126", "a127", "a128", "a129", "a130", "a131", "a132", "a133", "a134", "a135", "a136", "a137", "a138", "a139", "a140", "a141", "a142", "a143", "a144", "a145", "a146", "a147", "a148", "a149", "a150", "a151", "a152", "a153", "a154", "a155", "a156", "a157", "a158", "a159", "a160", "a161", "a162", "a163", "a164", "a165", "a166", "a167", "a168", "a169", "a170", "a171", "a172", "a173", "a174", "a175", "a176", "a177", "a178", "a179", "a180", "a181", "a182", "a183", "a184", "a185", "a186", "a187", "a188", "a189", "a190", "a191", "a192", "a193", "a194", "a195", "a196", "a197", "a198", "a199", "a200", "a201", "a202", "a203", "a204", "a205", "a206", "a207", "a208", "a209", "a210", "a211", "a212", "a213", "a214", "a215", "a216", "a217", "a218", "a219", "a220", "a221", "a222", "a223", "a224", "a225", "a226", "a227", "a228", "a229", "a230", "a231", "a232", "a233", "a234", "a235", "a236", "a237", "a238", "a239", "a240", "a241", "a242", "a243", "a244", "a245", "a246", "a247", "a248", "a249", "a250", "a251", "a252", "a253", "a254", "a255", "s40", "s41", "s42", "s43", "s44", "s45", "s46", "s47", "s48", "s49", "s50", "s51", "s52", "s53", "s54", "s55", "s56", "s57", "s58", "s59", "s60", "s61", "s62", "s63", "s64", "s65", "s66", "s67", "s68", "s69", "s70", "s71", "s72", "s73", "s74", "s75", "s76", "s77", "s78", "s79", "s80", "s81", "s82", "s83", "s84", "s85", "s86", "s87", "s88", "s89", "s90", "s91", "s92", "s93", "s94", "s95", "s96", "s97", "s98", "s99", "vcc", "scc", "memory"
#define M_ASM_TEXT \
  "s_mov_b64 s[40:41], %24\n" \
  "s_and_b32 s41, s41, 0xffff\n" \
  "s_mov_b32 s42, 255852544\n" \
  "s_mov_b32 s43, 0x00020000\n" \
  "s_mov_b32 s46, 4718592\n" \
  "s_mov_b32 s47, 0x00020000\n" \
  "s_lshl_b32 s72, %28, 11\n" \
  "buffer_load_dword v196, off, s[40:43], %38 sc1\n" \
  "s_mov_b64 s[44:45], %25\n" \
  "s_and_b32 s45, s45, 0xffff\n" \
  "s_add_u32 s60, %29, 0\n" \
  "s_lshl_b32 s61, s72, 1\n" \
  "s_add_u32 m0, s61, 16384\n" \
  "s_nop 0\n" \
  "buffer_load_dwordx4 v200, s[44:47], s60 offen lds\n" \
  "buffer_load_dwordx4 v201, s[44:47], s60 offen offset:1024 lds\n" \
  "buffer_load_dwordx4 v202, s[44:47], s60 offen offset:2048 lds\n" \
  "buffer_load_dwordx4 v203, s[44:47], s60 offen offset:3072 lds\n" \
  "s_add_u32 s60, %29, 524288\n" \
  "s_lshl_b32 s61, s72, 1\n" \
  "s_add_u32 m0, s61, 32768\n" \
  "s_nop 0\n" \
  "buffer_load_dwordx4 v200, s[44:47], s60 offen lds\n" \
  "buffer_load_dwordx4 v201, s[44:47], s60 offen offset:1024 lds\n" \
  "buffer_load_dwordx4 v202, s[44:47], s60 offen offset:2048 lds\n" \
  "buffer_load_dwordx4 v203, s[44:47], s60 offen offset:3072 lds\n" \
  "s_add_u32 s60, %29, 1048576\n" \
  "s_lshl_b32 s61, s72, 1\n" \
  "s_add_u32 m0, s61, 49152\n" \
  "s_nop 0\n" \
  "buffer_load_dwordx4 v200, s[44:47], s60 offen lds\n" \
  "buffer_load_dwordx4 v201, s[44:47], s60 offen offset:1024 lds\n" \
  "buffer_load_dwordx4 v202, s[44:47], s60 offen offset:2048 lds\n" \
  "buffer_load_dwordx4 v203, s[44:47], s60 offen offset:3072 lds\n" \
  "s_add_u32 s60, %29, 1572864\n" \
  "s_lshl_b32 s61, s72, 1\n" \
  "s_add_u32 m0, s61, 65536\n" \
  "s_nop 0\n" \
  "buffer_load_dwordx4 v200, s[44:47], s60 offen lds\n" \
  "buffer_load_dwordx4 v201, s[44:47], s60 offen offset:1024 lds\n" \
  "buffer_load_dwordx4 v202, s[44:47], s60 offen offset:2048 lds\n" \
  "buffer_load_dwordx4 v203, s[44:47], s60 offen offset:3072 lds\n" \
  "s_add_u32 s60, %29, 2097152\n" \
  "s_lshl_b32 s61, s72, 1\n" \
  "s_add_u32 m0, s61, 81920\n" \
  "s_nop 0\n" \
  "buffer_load_dwordx4 v200, s[44:47], s60 offen lds\n" \
  "buffer_load_dwordx4 v201, s[44:47], s60 offen offset:1024 lds\n" \
  "buffer_load_dwordx4 v202, s[44:47], s60 offen offset:2048 lds\n" \
  "buffer_load_dwordx4 v203, s[44:47], s60 offen offset:3072 lds\n" \
  "s_add_u32 s60, %29, 2621440\n" \
  "s_lshl_b32 s61, s72, 1\n" \
  "s_add_u32 m0, s61, 98304\n" \
  "s_nop 0\n" \
  "buffer_load_dwordx4 v200, s[44:47], s60 offen lds\n" \
  "buffer_load_dwordx4 v201, s[44:47], s60 offen offset:1024 lds\n" \
  "buffer_load_dwordx4 v202, s[44:47], s60 offen offset:2048 lds\n" \
  "buffer_load_dwordx4 v203, s[44:47], s60 offen offset:3072 lds\n" \
  "s_add_u32 s60, %29, 3145728\n" \
  "s_lshl_b32 s61, s72, 1\n" \
  "s_add_u32 m0, s61, 114688\n" \
  "s_nop 0\n" \
  "buffer_load_dwordx4 v200, s[44:47], s60 offen lds\n" \
  "buffer_load_dwordx4 v201, s[44:47], s60 offen offset:1024 lds\n" \
  "buffer_load_dwordx4 v202, s[44:47], s60 offen offset:2048 lds\n" \
  "buffer_load_dwordx4 v203, s[44:47], s60 offen offset:3072 lds\n" \
  "s_add_u32 s60, %29, 3670016\n" \
  "s_lshl_b32 s61, s72, 1\n" \
  "s_add_u32 m0, s61, 131072\n" \
  "s_nop 0\n" \
  "buffer_load_dwordx4 v200, s[44:47], s60 offen lds\n" \
  "buffer_load_dwordx4 v201, s[44:47], s60 offen offset:1024 lds\n" \
  "buffer_load_dwordx4 v202, s[44:47], s60 offen offset:2048 lds\n" \
  "buffer_load_dwordx4 v203, s[44:47], s60 offen offset:3072 lds\n" \
  "s_add_u32 s60, %29, 4194304\n" \
  "s_lshl_b32 s61, s72, 1\n" \
  "s_add_u32 m0, s61, 147456\n" \
  "s_nop 0\n" \
  "buffer_load_dwordx4 v200, s[44:47], s60 offen lds\n" \
  "buffer_load_dwordx4 v201, s[44:47], s60 offen offset:1024 lds\n" \
  "buffer_load_dwordx4 v202, s[44:47], s60 offen offset:2048 lds\n" \
  "buffer_load_dwordx4 v203, s[44:47], s60 offen offset:3072 lds\n" \
  "s_mov_b64 s[44:45], %27\n" \
  "s_and_b32 s45, s45, 0xffff\n" \
  "s_lshr_b32 s60, s72, 3\n" \
  "s_mov_b32 m0, s72\n" \
  "s_nop 0\n" \
  "buffer_load_dwordx4 v206, s[44:47], s60 offen lds\n" \
  "s_add_u32 s60, s60, 128\n" \
  "s_add_u32 m0, s72, 1024\n" \
  "s_nop 0\n" \
  "buffer_load_dwordx4 v206, s[44:47], s60 offen lds\n" \
  "s_movk_i32 s60, 1024\n" \
  "s_movk_i32 m0, 8192\n" \
  "s_nop 0\n" \
  "buffer_load_dwordx4 v206, s[44:47], s60 offen lds\n" \
  "s_waitcnt vmcnt(27)\n" \
  "s_barrier\n" \
  "v_add_u32 v194, 16384, v204\n" \
  "ds_read_b64_tr_b16 a[0:1], v194\n" \
  "ds_read_b64_tr_b16 a[2:3], v194 offset:512\n" \
  "ds_read_b64_tr_b16 a[4:5], v194 offset:8192\n" \
  "ds_read_b64_tr_b16 a[6:7], v194 offset:8704\n" \
  "v_add_u32 v195, 16384, v205\n" \
  "ds_read_b64_tr_b16 a[72:73], v195\n" \
  "ds_read_b64_tr_b16 a[74:75], v195 offset:512\n" \
  "ds_read_b64_tr_b16 a[76:77], v195 offset:8192\n" \
  "ds_read_b64_tr_b16 a[78:79], v195 offset:8704\n" \
  "v_add_u32 v194, 32768, v204\n" \
  "ds_read_b64_tr_b16 a[8:9], v194\n" \
  "ds_read_b64_tr_b16 a[10:11], v194 offset:512\n" \
  "ds_read_b64_tr_b16 a[12:13], v194 offset:8192\n" \
  "ds_read_b64_tr_b16 a[14:15], v194 offset:8704\n" \
  "v_add_u32 v195, 32768, v205\n" \
  "ds_read_b64_tr_b16 a[80:81], v195\n" \
  "ds_read_b64_tr_b16 a[82:83], v195 offset:512\n" \
  "ds_read_b64_tr_b16 a[84:85], v195 offset:8192\n" \
  "ds_read_b64_tr_b16 a[86:87], v195 offset:8704\n" \
  "v_add_u32 v194, 49152, v204\n" \
  "ds_read_b64_tr_b16 a[16:17], v194\n" \
  "ds_read_b64_tr_b16 a[18:19], v194 offset:512\n" \
  "ds_read_b64_tr_b16 a[20:21], v194 offset:8192\n" \
  "ds_read_b64_tr_b16 a[22:23], v194 offset:8704\n" \
  "v_add_u32 v195, 49152, v205\n" \
  "ds_read_b64_tr_b16 a[88:89], v195\n" \
  "ds_read_b64_tr_b16 a[90:91], v195 offset:512\n" \
  "ds_read_b64_tr_b16 a[92:93], v195 offset:8192\n" \
  "ds_read_b64_tr_b16 a[94:95], v195 offset:8704\n" \
  "s_waitcnt lgkmcnt(0)\n" \
  "s_barrier\n" \
  "s_mov_b64 s[44:45], %26\n" \
  "s_and_b32 s45, s45, 0xffff\n" \
  "s_add_u32 s60, %29, 0\n" \
  "s_lshl_b32 s61, s72, 1\n" \
  "s_add_u32 m0, s61, 16384\n" \
  "s_nop 0\n" \
  "buffer_load_dwordx4 v200, s[44:47], s60 offen lds\n" \
  "buffer_load_dwordx4 v201, s[44:47], s60 offen offset:1024 lds\n" \
  "buffer_load_dwordx4 v202, s[44:47], s60 offen offset:2048 lds\n" \
  "buffer_load_dwordx4 v203, s[44:47], s60 offen offset:3072 lds\n" \
  "s_add_u32 s60, %29, 524288\n" \
  "s_lshl_b32 s61, s72, 1\n" \
  "s_add_u32 m0, s61, 32768\n" \
  "s_nop 0\n" \
  "buffer_load_dwordx4 v200, s[44:47], s60 offen lds\n" \
  "buffer_load_dwordx4 v201, s[44:47], s60 offen offset:1024 lds\n" \
  "buffer_load_dwordx4 v202, s[44:47], s60 offen offset:2048 lds\n" \
  "buffer_load_dwordx4 v203, s[44:47], s60 offen offset:3072 lds\n" \
  "s_add_u32 s60, %29, 1048576\n" \
  "s_lshl_b32 s61, s72, 1\n" \
  "s_add_u32 m0, s61, 49152\n" \
  "s_nop 0\n" \
  "buffer_load_dwordx4 v200, s[44:47], s60 offen lds\n" \
  "buffer_load_dwordx4 v201, s[44:47], s60 offen offset:1024 lds\n" \
  "buffer_load_dwordx4 v202, s[44:47], s60 offen offset:2048 lds\n" \
  "buffer_load_dwordx4 v203, s[44:47], s60 offen offset:3072 lds\n" \
  "v_readfirstlane_b32 s55, v196\n" \
  "s_add_u32 s55, s55, 1\n" \
  "s_waitcnt vmcnt(27)\n" \
  "s_barrier\n" \
  "v_add_u32 v194, 65536, v204\n" \
  "ds_read_b64_tr_b16 a[24:25], v194\n" \
  "ds_read_b64_tr_b16 a[26:27], v194 offset:512\n" \
  "ds_read_b64_tr_b16 a[28:29], v194 offset:8192\n" \
  "ds_read_b64_tr_b16 a[30:31], v194 offset:8704\n" \
  "v_add_u32 v195, 65536, v205\n" \
  "ds_read_b64_tr_b16 a[96:97], v195\n" \
  "ds_read_b64_tr_b16 a[98:99], v195 offset:512\n" \
  "ds_read_b64_tr_b16 a[100:101], v195 offset:8192\n" \
  "ds_read_b64_tr_b16 a[102:103], v195 offset:8704\n" \
  "v_add_u32 v194, 81920, v204\n" \
  "ds_read_b64_tr_b16 a[32:33], v194\n" \
  "ds_read_b64_tr_b16 a[34:35], v194 offset:512\n" \
  "ds_read_b64_tr_b16 a[36:37], v194 offset:8192\n" \
  "ds_read_b64_tr_b16 a[38:39], v194 offset:8704\n" \
  "v_add_u32 v195, 81920, v205\n" \
  "ds_read_b64_tr_b16 a[104:105], v195\n" \
  "ds_read_b64_tr_b16 a[106:107], v195 offset:512\n" \
  "ds_read_b64_tr_b16 a[108:109], v195 offset:8192\n" \
  "ds_read_b64_tr_b16 a[110:111], v195 offset:8704\n" \
  "v_add_u32 v194, 98304, v204\n" \
  "ds_read_b64_tr_b16 a[40:41], v194\n" \
  "ds_read_b64_tr_b16 a[42:43], v194 offset:512\n" \
  "ds_read_b64_tr_b16 a[44:45], v194 offset:8192\n" \
  "ds_read_b64_tr_b16 a[46:47], v194 offset:8704\n" \
  "v_add_u32 v195, 98304, v205\n" \
  "ds_read_b64_tr_b16 a[112:113], v195\n" \
  "ds_read_b64_tr_b16 a[114:115], v195 offset:512\n" \
  "ds_read_b64_tr_b16 a[116:117], v195 offset:8192\n" \
  "ds_read_b64_tr_b16 a[118:119], v195 offset:8704\n" \
  "s_waitcnt lgkmcnt(0)\n" \
  "s_barrier\n" \
  "s_add_u32 s60, %29, 1572864\n" \
  "s_lshl_b32 s61, s72, 1\n" \
  "s_add_u32 m0, s61, 65536\n" \
  "s_nop 0\n" \
  "buffer_load_dwordx4 v200, s[44:47], s60 offen lds\n" \
  "buffer_load_dwordx4 v201, s[44:47], s60 offen offset:1024 lds\n" \
  "buffer_load_dwordx4 v202, s[44:47], s60 offen offset:2048 lds\n" \
  "buffer_load_dwordx4 v203, s[44:47], s60 offen offset:3072 lds\n" \
  "s_add_u32 s60, %29, 2097152\n" \
  "s_lshl_b32 s61, s72, 1\n" \
  "s_add_u32 m0, s61, 81920\n" \
  "s_nop 0\n" \
  "buffer_load_dwordx4 v200, s[44:47], s60 offen lds\n" \
  "buffer_load_dwordx4 v201, s[44:47], s60 offen offset:1024 lds\n" \
  "buffer_load_dwordx4 v202, s[44:47], s60 offen offset:2048 lds\n" \
  "buffer_load_dwordx4 v203, s[44:47], s60 offen offset:3072 lds\n" \
  "s_add_u32 s60, %29, 2621440\n" \
  "s_lshl_b32 s61, s72, 1\n" \
  "s_add_u32 m0, s61, 98304\n" \
  "s_nop 0\n" \
  "buffer_load_dwordx4 v200, s[44:47], s60 offen lds\n" \
  "buffer_load_dwordx4 v201, s[44:47], s60 offen offset:1024 lds\n" \
  "buffer_load_dwordx4 v202, s[44:47], s60 offen offset:2048 lds\n" \
  "buffer_load_dwordx4 v203, s[44:47], s60 offen offset:3072 lds\n" \
  "s_waitcnt vmcnt(27)\n" \
  "s_barrier\n" \
  "v_add_u32 v194, 114688, v204\n" \
  "ds_read_b64_tr_b16 a[48:49], v194\n" \
  "ds_read_b64_tr_b16 a[50:51], v194 offset:512\n" \
  "ds_read_b64_tr_b16 a[52:53], v194 offset:8192\n" \
  "ds_read_b64_tr_b16 a[54:55], v194 offset:8704\n" \
  "v_add_u32 v195, 114688, v205\n" \
  "ds_read_b64_tr_b16 a[120:121], v195\n" \
  "ds_read_b64_tr_b16 a[122:123], v195 offset:512\n" \
  "ds_read_b64_tr_b16 a[124:125], v195 offset:8192\n" \
  "ds_read_b64_tr_b16 a[126:127], v195 offset:8704\n" \
  "v_add_u32 v194, 131072, v204\n" \
  "ds_read_b64_tr_b16 a[56:57], v194\n" \
  "ds_read_b64_tr_b16 a[58:59], v194 offset:512\n" \
  "ds_read_b64_tr_b16 a[60:61], v194 offset:8192\n" \
  "ds_read_b64_tr_b16 a[62:63], v194 offset:8704\n" \
  "v_add_u32 v195, 131072, v205\n" \
  "ds_read_b64_tr_b16 a[128:129], v195\n" \
  "ds_read_b64_tr_b16 a[130:131], v195 offset:512\n" \
  "ds_read_b64_tr_b16 a[132:133], v195 offset:8192\n" \
  "ds_read_b64_tr_b16 a[134:135], v195 offset:8704\n" \
  "v_add_u32 v194, 147456, v204\n" \
  "ds_read_b64_tr_b16 a[64:65], v194\n" \
  "ds_read_b64_tr_b16 a[66:67], v194 offset:512\n" \
  "ds_read_b64_tr_b16 a[68:69], v194 offset:8192\n" \
  "ds_read_b64_tr_b16 a[70:71], v194 offset:8704\n" \
  "v_add_u32 v195, 147456, v205\n" \
  "ds_read_b64_tr_b16 a[136:137], v195\n" \
  "ds_read_b64_tr_b16 a[138:139], v195 offset:512\n" \
  "ds_read_b64_tr_b16 a[140:141], v195 offset:8192\n" \
  "ds_read_b64_tr_b16 a[142:143], v195 offset:8704\n" \
  "s_waitcnt lgkmcnt(0)\n" \
  "s_barrier\n" \
  "s_add_u32 s60, %29, 3145728\n" \
  "s_lshl_b32 s61, s72, 1\n" \
  "s_add_u32 m0, s61, 114688\n" \
  "s_nop 0\n" \
  "buffer_load_dwordx4 v200, s[44:47], s60 offen lds\n" \
  "buffer_load_dwordx4 v201, s[44:47], s60 offen offset:1024 lds\n" \
  "buffer_load_dwordx4 v202, s[44:47], s60 offen offset:2048 lds\n" \
  "buffer_load_dwordx4 v203, s[44:47], s60 offen offset:3072 lds\n" \
  "s_add_u32 s60, %29, 3670016\n" \
  "s_lshl_b32 s61, s72, 1\n" \
  "s_add_u32 m0, s61, 131072\n" \
  "s_nop 0\n" \
  "buffer_load_dwordx4 v200, s[44:47], s60 offen lds\n" \
  "buffer_load_dwordx4 v201, s[44:47], s60 offen offset:1024 lds\n" \
  "buffer_load_dwordx4 v202, s[44:47], s60 offen offset:2048 lds\n" \
  "buffer_load_dwordx4 v203, s[44:47], s60 offen offset:3072 lds\n" \
  "s_add_u32 s60, %29, 4194304\n" \
  "s_lshl_b32 s61, s72, 1\n" \
  "s_add_u32 m0, s61, 147456\n" \
  "s_nop 0\n" \
  "buffer_load_dwordx4 v200, s[44:47], s60 offen lds\n" \
  "buffer_load_dwordx4 v201, s[44:47], s60 offen offset:1024 lds\n" \
  "buffer_load_dwordx4 v202, s[44:47], s60 offen offset:2048 lds\n" \
  "buffer_load_dwordx4 v203, s[44:47], s60 offen offset:3072 lds\n" \
  "buffer_load_dwordx2 v[198:199], off, s[40:43], %37 sc1\n" \
  "s_waitcnt vmcnt(25)\n" \
  "s_barrier\n" \
  "v_add_u32 v194, 16384, v204\n" \
  "ds_read_b64_tr_b16 a[144:145], v194\n" \
  "ds_read_b64_tr_b16 a[146:147], v194 offset:512\n" \
  "ds_read_b64_tr_b16 a[148:149], v194 offset:8192\n" \
  "ds_read_b64_tr_b16 a[150:151], v194 offset:8704\n" \
  "v_add_u32 v195, 16384, v205\n" \
  "ds_read_b64_tr_b16 a[216:217], v195\n" \
  "ds_read_b64_tr_b16 a[218:219], v195 offset:512\n" \
  "ds_read_b64_tr_b16 a[220:221], v195 offset:8192\n" \
  "ds_read_b64_tr_b16 a[222:223], v195 offset:8704\n" \
  "v_xor_b32 v195, 128, v194\n" \
  "ds_read_b64_tr_b16 v[32:33], v195\n" \
  "ds_read_b64_tr_b16 v[34:35], v195 offset:512\n" \
  "ds_read_b64_tr_b16 v[36:37], v195 offset:8192\n" \
  "ds_read_b64_tr_b16 v[38:39], v195 offset:8704\n" \
  "v_add_u32 v194, 32768, v204\n" \
  "ds_read_b64_tr_b16 a[152:153], v194\n" \
  "ds_read_b64_tr_b16 a[154:155], v194 offset:512\n" \
  "ds_read_b64_tr_b16 a[156:157], v194 offset:8192\n" \
  "ds_read_b64_tr_b16 a[158:159], v194 offset:8704\n" \
  "v_add_u32 v195, 32768, v205\n" \
  "ds_read_b64_tr_b16 a[224:225], v195\n" \
  "ds_read_b64_tr_b16 a[226:227], v195 offset:512\n" \
  "ds_read_b64_tr_b16 a[228:229], v195 offset:8192\n" \
  "ds_read_b64_tr_b16 a[230:231], v195 offset:8704\n" \
  "v_xor_b32 v195, 128, v194\n" \
  "ds_read_b64_tr_b16 v[40:41], v195\n" \
  "ds_read_b64_tr_b16 v[42:43], v195 offset:512\n" \
  "ds_read_b64_tr_b16 v[44:45], v195 offset:8192\n" \
  "ds_read_b64_tr_b16 v[46:47], v195 offset:8704\n" \
  "v_add_u32 v194, 49152, v204\n" \
  "ds_read_b64_tr_b16 a[160:161], v194\n" \
  "ds_read_b64_tr_b16 a[162:163], v194 offset:512\n" \
  "ds_read_b64_tr_b16 a[164:165], v194 offset:8192\n" \
  "ds_read_b64_tr_b16 a[166:167], v194 offset:8704\n" \
  "v_add_u32 v195, 49152, v205\n" \
  "ds_read_b64_tr_b16 a[232:233], v195\n" \
  "ds_read_b64_tr_b16 a[234:235], v195 offset:512\n" \
  "ds_read_b64_tr_b16 a[236:237], v195 offset:8192\n" \
  "ds_read_b64_tr_b16 a[238:239], v195 offset:8704\n" \
  "v_xor_b32 v195, 128, v194\n" \
  "ds_read_b64_tr_b16 v[48:49], v195\n" \
  "ds_read_b64_tr_b16 v[50:51], v195 offset:512\n" \
  "ds_read_b64_tr_b16 v[52:53], v195 offset:8192\n" \
  "ds_read_b64_tr_b16 v[54:55], v195 offset:8704\n" \
  "s_waitcnt lgkmcnt(0)\n" \
  "s_waitcnt vmcnt(13)\n" \
  "s_barrier\n" \
  "v_add_u32 v194, 65536, v204\n" \
  "ds_read_b64_tr_b16 a[168:169], v194\n" \
  "ds_read_b64_tr_b16 a[170:171], v194 offset:512\n" \
  "ds_read_b64_tr_b16 a[172:173], v194 offset:8192\n" \
  "ds_read_b64_tr_b16 a[174:175], v194 offset:8704\n" \
  "v_add_u32 v195, 65536, v205\n" \
  "ds_read_b64_tr_b16 a[240:241], v195\n" \
  "ds_read_b64_tr_b16 a[242:243], v195 offset:512\n" \
  "ds_read_b64_tr_b16 a[244:245], v195 offset:8192\n" \
  "ds_read_b64_tr_b16 a[246:247], v195 offset:8704\n" \
  "v_xor_b32 v195, 128, v194\n" \
  "ds_read_b64_tr_b16 v[56:57], v195\n" \
  "ds_read_b64_tr_b16 v[58:59], v195 offset:512\n" \
  "ds_read_b64_tr_b16 v[60:61], v195 offset:8192\n" \
  "ds_read_b64_tr_b16 v[62:63], v195 offset:8704\n" \
  "v_add_u32 v194, 81920, v204\n" \
  "ds_read_b64_tr_b16 a[176:177], v194\n" \
  "ds_read_b64_tr_b16 a[178:179], v194 offset:512\n" \
  "ds_read_b64_tr_b16 a[180:181], v194 offset:8192\n" \
  "ds_read_b64_tr_b16 a[182:183], v194 offset:8704\n" \
  "v_add_u32 v195, 81920, v205\n" \
  "ds_read_b64_tr_b16 a[248:249], v195\n" \
  "ds_read_b64_tr_b16 a[250:251], v195 offset:512\n" \
  "ds_read_b64_tr_b16 a[252:253], v195 offset:8192\n" \
  "ds_read_b64_tr_b16 a[254:255], v195 offset:8704\n" \
  "v_add_u32 v194, 98304, v204\n" \
  "ds_read_b64_tr_b16 a[184:185], v194\n" \
  "ds_read_b64_tr_b16 a[186:187], v194 offset:512\n" \
  "ds_read_b64_tr_b16 a[188:189], v194 offset:8192\n" \
  "ds_read_b64_tr_b16 a[190:191], v194 offset:8704\n" \
  "v_add_u32 v195, 98304, v205\n" \
  "ds_read_b64_tr_b16 v[0:1], v195\n" \
  "ds_read_b64_tr_b16 v[2:3], v195 offset:512\n" \
  "ds_read_b64_tr_b16 v[4:5], v195 offset:8192\n" \
  "ds_read_b64_tr_b16 v[6:7], v195 offset:8704\n" \
  "s_waitcnt lgkmcnt(0)\n" \
  "s_waitcnt vmcnt(1)\n" \
  "s_barrier\n" \
  "v_add_u32 v194, 114688, v204\n" \
  "ds_read_b64_tr_b16 a[192:193], v194\n" \
  "ds_read_b64_tr_b16 a[194:195], v194 offset:512\n" \
  "ds_read_b64_tr_b16 a[196:197], v194 offset:8192\n" \
  "ds_read_b64_tr_b16 a[198:199], v194 offset:8704\n" \
  "v_add_u32 v195, 114688, v205\n" \
  "ds_read_b64_tr_b16 v[8:9], v195\n" \
  "ds_read_b64_tr_b16 v[10:11], v195 offset:512\n" \
  "ds_read_b64_tr_b16 v[12:13], v195 offset:8192\n" \
  "ds_read_b64_tr_b16 v[14:15], v195 offset:8704\n" \
  "v_add_u32 v194, 131072, v204\n" \
  "ds_read_b64_tr_b16 a[200:201], v194\n" \
  "ds_read_b64_tr_b16 a[202:203], v194 offset:512\n" \
  "ds_read_b64_tr_b16 a[204:205], v194 offset:8192\n" \
  "ds_read_b64_tr_b16 a[206:207], v194 offset:8704\n" \
  "v_add_u32 v195, 131072, v205\n" \
  "ds_read_b64_tr_b16 v[16:17], v195\n" \
  "ds_read_b64_tr_b16 v[18:19], v195 offset:512\n" \
  "ds_read_b64_tr_b16 v[20:21], v195 offset:8192\n" \
  "ds_read_b64_tr_b16 v[22:23], v195 offset:8704\n" \
  "v_add_u32 v194, 147456, v204\n" \
  "ds_read_b64_tr_b16 a[208:209], v194\n" \
  "ds_read_b64_tr_b16 a[210:211], v194 offset:512\n" \
  "ds_read_b64_tr_b16 a[212:213], v194 offset:8192\n" \
  "ds_read_b64_tr_b16 a[214:215], v194 offset:8704\n" \
  "v_add_u32 v195, 147456, v205\n" \
  "ds_read_b64_tr_b16 v[24:25], v195\n" \
  "ds_read_b64_tr_b16 v[26:27], v195 offset:512\n" \
  "ds_read_b64_tr_b16 v[28:29], v195 offset:8192\n" \
  "ds_read_b64_tr_b16 v[30:31], v195 offset:8704\n" \
  "s_waitcnt lgkmcnt(0)\n" \
  "s_waitcnt vmcnt(0)\n" \
  "v_readfirstlane_b32 s96, v198\n" \
  "v_readfirstlane_b32 s63, v199\n" \
  "s_cmp_eq_u32 s63, s55\n" \
  "s_cselect_b32 s96, s96, 0\n" \
  "s_cmp_ge_u32 s96, 0xc0000000\n" \
  "s_cbranch_scc1 .LM_go_x_%=\n" \
  "s_mov_b32 s69, 0\n" \
  ".LM_go_%=:\n" \
  "buffer_load_dwordx2 v[196:197], off, s[40:43], %37 sc1\n" \
  "s_waitcnt vmcnt(0)\n" \
  "v_readfirstlane_b32 s96, v196\n" \
  "v_readfirstlane_b32 s63, v197\n" \
  "s_cmp_eq_u32 s63, s55\n" \
  "s_cselect_b32 s96, s96, 0\n" \
  "s_cmp_ge_u32 s96, 0xc0000000\n" \
  "s_cbranch_scc1 .LM_go_x_%=\n" \
  "s_sleep 8\n" \
  "s_add_u32 s69, s69, 1\n" \
  "s_and_b32 s63, s69, 1023\n" \
  "s_cbranch_scc1 .LM_go_%=\n" \
  "s_cmp_gt_u32 s69, 2097152\n" \
  "s_cbranch_scc1 .LM_go_set_%=\n" \
  "buffer_load_dwordx2 v[196:197], off, s[40:43], %38 offset:128 sc1\n" \
  "s_waitcnt vmcnt(0)\n" \
  "v_readfirstlane_b32 s63, v197\n" \
  "s_cmp_eq_u32 s63, s55\n" \
  "s_cbranch_scc0 .LM_go_%=\n" \
  "s_branch .LM_go_x_%=\n" \
  ".LM_go_set_%=:\n" \
  "v_mov_b32 v196, 1\n" \
  "v_mov_b32 v197, s55\n" \
  "buffer_store_dwordx2 v[196:197], off, s[40:43], %38 offset:128 sc1\n" \
  "s_waitcnt vmcnt(0)\n" \
  ".LM_go_x_%=:\n" \
  "s_and_b32 s60, s96, 2\n" \
  "s_lshl_b32 s60, s60, 12\n" \
  "s_xor_b32 s74, %34, s60\n" \
  "s_xor_b32 s75, %36, s60\n" \
  "s_bfe_u32 s56, s96, 0x30010\n" \
  "s_lshl_b32 s56, s56, 12\n" \
  "s_or_b32 s56, s56, 0x200000\n" \
  "s_mov_b32 s57, 1052672\n" \
  "s_lshl_b32 s61, %33, 2\n" \
  "s_lshr_b32 s51, s96, s61\n" \
  "s_and_b32 s51, s51, 7\n" \
  "s_lshl_b32 s51, s51, 12\n" \
  "s_xor_b32 s51, s51, 4210688\n" \
  "s_mov_b32 s71, 0\n" \
  "s_add_u32 s45, s51, 886784\n" \
  "s_mov_b32 s46, -1\n" \
  "v_mov_b64 v[64:65], -1\n" \
  "v_mov_b64 v[66:67], -1\n" \
  "v_and_b32 v194, 3, v246\n" \
  "v_lshlrev_b32 v194, 2, v194\n" \
  "v_lshrrev_b32 v194, v194, s96\n" \
  "v_and_b32 v194, 7, v194\n" \
  "v_lshlrev_b32 v194, 12, v194\n" \
  "v_and_b32 v246, -4, v246\n" \
  "v_xor_b32 v246, v246, v194\n" \
  "v_and_b32 v194, 3, v247\n" \
  "v_lshlrev_b32 v194, 2, v194\n" \
  "v_lshrrev_b32 v194, v194, s96\n" \
  "v_and_b32 v194, 7, v194\n" \
  "v_lshlrev_b32 v194, 12, v194\n" \
  "v_and_b32 v247, -4, v247\n" \
  "v_xor_b32 v247, v247, v194\n" \
  "v_xor_b32 v190, s56, v246\n" \
  "buffer_store_dwordx2 v[64:65], v190, s[40:43], 0 offen sc1\n" \
  "v_xor_b32 v191, s56, v247\n" \
  "buffer_store_dwordx2 v[64:65], v191, s[40:43], 0 offen sc1\n" \
  "buffer_store_dwordx2 v[64:65], v191, s[40:43], 0 offen offset:256 sc1\n" \
  "s_xor_b32 s62, s56, s57\n" \
  "s_nop 1\n" \
  "v_xor_b32 v190, s62, v246\n" \
  "buffer_store_dwordx2 v[64:65], v190, s[40:43], 0 offen sc1\n" \
  "v_xor_b32 v191, s62, v247\n" \
  "buffer_store_dwordx2 v[64:65], v191, s[40:43], 0 offen sc1\n" \
  "buffer_store_dwordx2 v[64:65], v191, s[40:43], 0 offen offset:256 sc1\n" \
  "s_andn2_b32 s60, 2, s96\n" \
  "s_lshl_b32 s60, s60, 12\n" \
  "s_xor_b32 s60, s60, 8192\n" \
  "s_xor_b32 s58, %31, s60\n" \
  "s_mov_b32 s49, 0\n" \
  "s_add_u32 s47, %41, s60\n" \
  "s_add_u32 s97, s60, 8388608\n" \
  "s_xor_b32 s50, %32, s60\n" \
  "s_mov_b32 s48, 0\n" \
  "s_mov_b32 s54, 0\n" \
  "s_cmp_eq_u32 %28, 0\n" \
  "s_cselect_b64 s[88:89], -1, 0\n" \
  "s_mov_b32 s68, 0\n" \
  "s_mov_b32 s70, 1\n" \
  "s_mov_b32 s73, 0\n" \
  "s_mov_b32 s44, 0\n" \
  "s_mov_b32 s53, 0\n" \
  "s_memrealtime s[86:87]\n" \
  "s_memtime s[76:77]\n" \
  "s_waitcnt lgkmcnt(0)\n" \
  "s_mov_b32 s90, s76\n" \
  "s_mov_b32 s87, 340\n" \
  "s_mov_b32 s64, 0x3e0293ee\n" \
  "s_mov_b32 s65, 0xbe0293ee\n" \
  "v_mov_b64 v[76:77], 0\n" \
  "v_mov_b64 v[78:79], 0\n" \
  "v_mov_b64 v[80:81], 0\n" \
  "v_mov_b64 v[82:83], 0\n" \
  "v_mov_b64 v[84:85], 0\n" \
  "v_mov_b64 v[86:87], 0\n" \
  "v_mov_b64 v[88:89], 0\n" \
  "v_mov_b64 v[90:91], 0\n" \
  "v_mov_b64 v[92:93], 0\n" \
  "v_mov_b64 v[94:95], 0\n" \
  "v_mov_b32 v72, 0\n" \
  "v_mov_b32 v73, s55\n" \
  "v_min_u32 v74, 1, v240\n" \
  "v_lshlrev_b32 v74, 30, v74\n" \
  "s_mov_b64 s[92:93], 0\n" \
  "s_mov_b32 s92, 0xffff0000\n" \
  "s_mov_b32 s60, 8192\n" \
  "s_add_u32 s60, s60, 32768\n" \
  "v_add_u32 v176, s60, v240\n" \
  "v_mov_b32 v180, v252\n" \
  "v_mov_b64 v[96:97], 0\n" \
  "v_mov_b64 v[98:99], 0\n" \
  "v_mov_b32 v96, v253\n" \
  "v_mov_b32 v98, v254\n" \
  "s_and_b32 s60, s72, 2048\n" \
  "s_add_u32 s50, s50, s60\n" \
  "s_lshr_b32 s60, s60, 1\n" \
  "v_add_u32 v177, s60, v244\n" \
  "s_lshr_b32 s60, s72, 9\n" \
  "s_add_u32 s60, s60, 17408\n" \
  "v_mov_b32 v184, s60\n" \
  "s_mov_b32 s52, 0\n" \
  "s_barrier\n" \
  "ds_write_b32 v184, v72\n" \
  "s_branch .LM_xr_%=\n" \
  ".LM_dry_b_%=:\n" \
  "s_and_b32 s60, s72, 2048\n" \
  "s_lshl_b32 s91, s60, 1\n" \
  "s_add_u32 s91, s91, 40960\n" \
  "v_mov_b64 v[182:183], 0\n" \
  "s_mov_b32 s69, 0\n" \
  ".LM_k0_%=:\n" \
  "buffer_load_dwordx2 v[196:197], off, s[40:43], s74 sc1\n" \
  "s_waitcnt vmcnt(0)\n" \
  "v_readfirstlane_b32 s54, v196\n" \
  "v_readfirstlane_b32 s63, v197\n" \
  "s_cmp_eq_u32 s63, s55\n" \
  "s_cselect_b32 s54, s54, 0\n" \
  "s_cmp_gt_i32 s54, 1\n" \
  "s_cbranch_scc1 .LM_k0_x_%=\n" \
  "s_sleep 2\n" \
  "s_add_u32 s69, s69, 1\n" \
  "s_and_b32 s63, s69, 1023\n" \
  "s_cbranch_scc1 .LM_k0_%=\n" \
  "s_cmp_gt_u32 s69, 2097152\n" \
  "s_cbranch_scc1 .LM_k0_set_%=\n" \
  "buffer_load_dwordx2 v[196:197], off, s[40:43], %38 offset:128 sc1\n" \
  "s_waitcnt vmcnt(0)\n" \
  "v_readfirstlane_b32 s63, v197\n" \
  "s_cmp_eq_u32 s63, s55\n" \
  "s_cbranch_scc0 .LM_k0_%=\n" \
  "s_branch .LM_k0_x_%=\n" \
  ".LM_k0_set_%=:\n" \
  "v_mov_b32 v196, 1\n" \
  "v_mov_b32 v197, s55\n" \
  "buffer_store_dwordx2 v[196:197], off, s[40:43], %38 offset:128 sc1\n" \
  "s_waitcnt vmcnt(0)\n" \
  ".LM_k0_x_%=:\n" \
  "s_xor_b32 s60, s49, s58\n" \
  "s_add_u32 s49, s49, 16384\n" \
  "s_xor_b32 s91, s91, 8192\n" \
  "s_mov_b32 m0, s91\n" \
  "s_nop 0\n" \
  "buffer_load_dwordx4 v245, s[40:43], s60 offen sc1 lds\n" \
  "buffer_load_dwordx4 v245, s[40:43], s60 offen offset:1024 sc1 lds\n" \
  "buffer_load_dwordx4 v245, s[40:43], s60 offen offset:2048 sc1 lds\n" \
  "buffer_load_dwordx4 v245, s[40:43], s60 offen offset:3072 sc1 lds\n" \
  "s_xor_b32 s60, s49, s58\n" \
  "s_add_u32 s49, s49, 16384\n" \
  "s_xor_b32 s91, s91, 8192\n" \
  "s_mov_b32 m0, s91\n" \
  "s_nop 0\n" \
  "buffer_load_dwordx4 v245, s[40:43], s60 offen sc1 lds\n" \
  "buffer_load_dwordx4 v245, s[40:43], s60 offen offset:1024 sc1 lds\n" \
  "buffer_load_dwordx4 v245, s[40:43], s60 offen offset:2048 sc1 lds\n" \
  "buffer_load_dwordx4 v245, s[40:43], s60 offen offset:3072 sc1 lds\n" \
  "s_waitcnt vmcnt(0)\n" \
  "s_barrier\n" \
  "v_xor_b32 v176, 8192, v176\n" \
  "s_mov_b32 s67, 2130706432\n" \
  "s_waitcnt vmcnt(0) lgkmcnt(0)\n" \
  "s_barrier\n" \
  "s_branch .LM_p_%=\n" \
  "s_nop 7\n" \
  ".p2align 6\n" \
  ".fill (((24 - (.LM_p_%= - .LM_rot_%=)) & 63) >> 2), 4, 0xBF800000\n" \
  ".LM_rot_%=:\n" \
  "s_xor_b32 s51, s51, s56\n" \
  "s_xor_b32 s71, s71, s56\n" \
  "v_xor_b32 v246, s56, v246\n" \
  "v_xor_b32 v247, s56, v247\n" \
  "s_mov_b32 s60, s56\n" \
  "s_mov_b32 s56, s57\n" \
  "s_mov_b32 s57, s60\n" \
  "s_add_u32 s59, s51, 819200\n" \
  "s_mov_b32 m0, 0\n" \
  "s_mov_b32 s70, 0\n" \
  "s_waitcnt lgkmcnt(0)\n" \
  ".LM_rt_%=:\n" \
  "s_barrier\n" \
  ".LM_p_%=:\n" \
  "ds_read_b128 v[116:119], v240\n" \
  "ds_read_b128 v[120:123], v240 offset:1024\n" \
  "ds_read_b128 v[124:127], v240 offset:2048\n" \
  "s_waitcnt lgkmcnt(2)\n" \
  "v_smfmac_f32_16x16x64_bf16 v[80:83], v[116:119], a[72:79], v239\n" \
  "s_barrier\n" \
  "v_smfmac_f32_16x16x64_bf16 v[76:79], v[116:119], a[0:7], v239\n" \
  "ds_read_b128 v[128:131], v240 offset:3072\n" \
  "s_waitcnt lgkmcnt(2)\n" \
  "v_smfmac_f32_16x16x64_bf16 v[80:83], v[120:123], a[80:87], v239\n" \
  "ds_read_b128 v[132:135], v240 offset:4096\n" \
  "v_smfmac_f32_16x16x64_bf16 v[76:79], v[120:123], a[8:15], v239\n" \
  "ds_read_b128 v[136:139], v240 offset:5120\n" \
  "s_waitcnt lgkmcnt(3)\n" \
  "v_smfmac_f32_16x16x64_bf16 v[76:79], v[124:127], a[16:23], v239\n" \
  "ds_read_b128 v[140:143], v240 offset:6144\n" \
  "ds_read_b128 v[144:147], v240 offset:7168\n" \
  "v_smfmac_f32_16x16x64_bf16 v[80:83], v[124:127], a[88:95], v239\n" \
  "s_waitcnt lgkmcnt(4)\n" \
  "v_smfmac_f32_16x16x64_bf16 v[76:79], v[128:131], a[24:31], v239\n" \
  "ds_read_b128 v[148:151], v240 offset:8192\n" \
  "s_waitcnt lgkmcnt(4)\n" \
  "v_smfmac_f32_16x16x64_bf16 v[76:79], v[132:135], a[32:39], v239\n" \
  "s_waitcnt lgkmcnt(3)\n" \
  "v_smfmac_f32_16x16x64_bf16 v[76:79], v[136:139], a[40:47], v239\n" \
  "s_waitcnt lgkmcnt(2)\n" \
  "v_smfmac_f32_16x16x64_bf16 v[76:79], v[140:143], a[48:55], v239\n" \
  "s_waitcnt lgkmcnt(1)\n" \
  "v_smfmac_f32_16x16x64_bf16 v[76:79], v[144:147], a[56:63], v239\n" \
  "s_waitcnt lgkmcnt(0)\n" \
  "v_smfmac_f32_16x16x64_bf16 v[76:79], v[148:151], a[64:71], v239\n" \
  "s_nop 2\n" \
  "v_smfmac_f32_16x16x64_bf16 v[80:83], v[128:131], a[96:103], v239\n" \
  "s_nop 2\n" \
  "v_smfmac_f32_16x16x64_bf16 v[80:83], v[132:135], a[104:111], v239\n" \
  "v_add_f32 v76, v76, v78\n" \
  "v_add_f32 v77, v77, v79\n" \
  "v_smfmac_f32_16x16x64_bf16 v[80:83], v[136:139], a[112:119], v239\n" \
  "v_cmp_u_f32 vcc, v76, v77\n" \
  "v_smfmac_f32_16x16x64_bf16 v[80:83], v[140:143], a[120:127], v239\n" \
  "v_mul_f32 v77, s64, v77\n" \
  "v_smfmac_f32_16x16x64_bf16 v[80:83], v[144:147], a[128:135], v239\n" \
  "v_mul_f32 v76, s64, v76\n" \
  "v_smfmac_f32_16x16x64_bf16 v[80:83], v[148:151], a[136:143], v239\n" \
  "s_cbranch_vccnz .LM_slow_%=\n" \
  ".LM_back_%=:\n" \
  "s_cmp_eq_u32 s48, 63\n" \
  "v_cvt_pk_bf16_f32 v76, v76, v77\n" \
  "v_smfmac_f32_16x16x64_bf16 v[92:95], v[116:119], v[32:39], v239\n" \
  "ds_write_b32 v241, v76\n" \
  "s_nop 2\n" \
  "v_add_f32 v80, v80, v82\n" \
  "v_mul_f32 v80, s64, v80\n" \
  "v_add_f32 v81, v81, v83\n" \
  "v_mul_f32 v81, s64, v81\n" \
  "v_cvt_pk_bf16_f32 v80, v80, v81\n" \
  "v_smfmac_f32_16x16x64_bf16 v[92:95], v[120:123], v[40:47], v239\n" \
  "ds_write_b32 v241, v80 offset:256\n" \
  "v_smfmac_f32_16x16x64_bf16 v[92:95], v[124:127], v[48:55], v239\n" \
  "v_smfmac_f32_16x16x64_bf16 v[92:95], v[128:131], v[56:63], v239\n" \
  "s_waitcnt lgkmcnt(1)\n" \
  "s_barrier\n" \
  "v_smfmac_f32_16x16x64_bf16 v[84:87], v[116:119], a[144:151], v239\n" \
  "s_nop 0\n" \
  "ds_read_b64_tr_b16 v[152:153], v243 offset:1024\n" \
  "v_smfmac_f32_16x16x64_bf16 v[88:91], v[116:119], a[216:223], v239\n" \
  "s_nop 1\n" \
  "v_add_f32 v93, v93, v95\n" \
  "s_waitcnt lgkmcnt(1)\n" \
  "s_barrier\n" \
  "v_add_f32 v92, v92, v94\n" \
  "v_smfmac_f32_16x16x64_bf16 v[84:87], v[120:123], a[152:159], v239\n" \
  "ds_read_b64_tr_b16 v[116:117], v243\n" \
  "v_smfmac_f32_16x16x64_bf16 v[84:87], v[124:127], a[160:167], v239\n" \
  "ds_write_b64 v255, v[92:93]\n" \
  "v_smfmac_f32_16x16x64_bf16 v[84:87], v[128:131], a[168:175], v239\n" \
  "v_smfmac_f32_16x16x64_bf16 v[84:87], v[132:135], a[176:183], v239\n" \
  "s_waitcnt vmcnt(0) \n" \
  "buffer_store_dwordx2 v[72:73], v74, s[40:43], %35 offen sc1\n" \
  "v_smfmac_f32_16x16x64_bf16 v[84:87], v[136:139], a[184:191], v239\n" \
  "v_smfmac_f32_16x16x64_bf16 v[84:87], v[140:143], a[192:199], v239\n" \
  "s_waitcnt lgkmcnt(0)\n" \
  "s_barrier\n" \
  "v_smfmac_f32_16x16x64_bf16 v[84:87], v[144:147], a[200:207], v239\n" \
  "v_smfmac_f32_16x16x64_bf16 v[84:87], v[148:151], a[208:215], v239\n" \
  "ds_read_b64_tr_b16 v[154:155], v243 offset:1536\n" \
  "ds_read_b64_tr_b16 v[158:159], v243 offset:1792\n" \
  "v_smfmac_f32_16x16x64_bf16 v[88:91], v[120:123], a[224:231], v239\n" \
  "s_nop 2\n" \
  "v_smfmac_f32_16x16x64_bf16 v[88:91], v[124:127], a[232:239], v239\n" \
  "s_nop 0\n" \
  "v_add_f32 v84, v84, v86\n" \
  "v_smfmac_f32_16x16x64_bf16 v[88:91], v[128:131], a[240:247], v239\n" \
  "ds_read_b64_tr_b16 v[120:121], v243 offset:256\n" \
  "v_add_f32 v85, v85, v87\n" \
  "v_smfmac_f32_16x16x64_bf16 v[88:91], v[132:135], a[248:255], v239\n" \
  "ds_read_b64_tr_b16 v[122:123], v243 offset:768\n" \
  "v_cvt_pk_bf16_f32 v84, v84, v85\n" \
  "v_smfmac_f32_16x16x64_bf16 v[88:91], v[136:139], v[0:7], v239\n" \
  "v_smfmac_f32_16x16x64_bf16 v[88:91], v[140:143], v[8:15], v239\n" \
  "ds_write_b32 v242, v84\n" \
  "v_smfmac_f32_16x16x64_bf16 v[88:91], v[144:147], v[16:23], v239\n" \
  "v_smfmac_f32_16x16x64_bf16 v[88:91], v[148:151], v[24:31], v239\n" \
  "ds_read_b64_tr_b16 v[118:119], v243 offset:512\n" \
  "s_nop 6\n" \
  "v_add_f32 v88, v88, v90\n" \
  "v_add_f32 v89, v89, v91\n" \
  "v_cvt_pk_bf16_f32 v88, v88, v89\n" \
  "ds_write_b32 v242, v88 offset:256\n" \
  "s_waitcnt lgkmcnt(2)\n" \
  "s_barrier\n" \
  "ds_read_b64_tr_b16 v[124:125], v244 offset:1024\n" \
  "ds_read_b64_tr_b16 v[126:127], v244 offset:1536\n" \
  "ds_read_b64_tr_b16 v[130:131], v244 offset:512\n" \
  "ds_read_b64_tr_b16 v[128:129], v244\n" \
  "s_waitcnt lgkmcnt(4)\n" \
  "s_barrier\n" \
  "s_cbranch_scc1 .LM_xr_%=\n" \
  "ds_read_b64_tr_b16 v[134:135], v244 offset:768\n" \
  "ds_read_b64_tr_b16 v[132:133], v244 offset:256\n" \
  "ds_read_b64_tr_b16 v[156:157], v243 offset:1280\n" \
  "ds_read_b64_tr_b16 v[136:137], v244 offset:1280\n" \
  "ds_read_b64 v[188:189], v248\n" \
  "ds_read_b64_tr_b16 v[138:139], v244 offset:1792\n" \
  "s_waitcnt lgkmcnt(6)\n" \
  "v_mfma_f32_16x16x32_bf16 v[140:143], v[128:131], v[116:119], 0\n" \
  "v_mfma_f32_16x16x32_bf16 v[140:143], v[124:127], v[152:155], v[140:143]\n" \
  "s_waitcnt lgkmcnt(4)\n" \
  "v_mfma_f32_16x16x32_bf16 v[140:143], v[132:135], v[120:123], v[140:143]\n" \
  "s_waitcnt lgkmcnt(0)\n" \
  "v_mfma_f32_16x16x32_bf16 v[140:143], v[136:139], v[156:159], v[140:143]\n" \
  "s_nop 7\n" \
  "v_exp_f32 v119, v143\n" \
  "v_exp_f32 v117, v141\n" \
  "v_exp_f32 v118, v142\n" \
  "v_exp_f32 v116, v140\n" \
  "v_add_f32 v188, v188, v189\n" \
  "v_pk_add_f32 v[186:187], v[116:117], v[118:119]\n" \
  "s_nop 0\n" \
  "v_pk_add_f32 v[174:175], v[186:187], v[186:187] op_sel:[0,1] op_sel_hi:[1,0]\n" \
  "v_pk_fma_f32 v[116:117], v[116:117], v[180:181], v[96:97] op_sel_hi:[1,0,1]\n" \
  "v_pk_fma_f32 v[118:119], v[118:119], v[180:181], v[98:99] op_sel_hi:[1,0,1]\n" \
  "v_permlane32_swap_b32 v174, v175\n" \
  "v_add_f32 v174, v174, v175\n" \
  "v_add_f32 v174, v174, v188\n" \
  "v_rcp_f32 v188, v174\n" \
  "v_add_u32 v187, 3766484992, v174\n" \
  "v_pk_mul_f32 v[116:117], v[116:117], v[188:189] op_sel_hi:[1,0]\n" \
  "v_pk_mul_f32 v[118:119], v[118:119], v[188:189] op_sel_hi:[1,0]\n" \
  "v_cvt_pk_bf16_f32 v188, v116, v117\n" \
  "v_cvt_pk_bf16_f32 v189, v118, v119\n" \
  "v_cmp_le_u32 vcc, s52, v187\n" \
  "s_cbranch_vccnz .LM_ex_%=\n" \
  ".LM_ex_b_%=:\n" \
  "buffer_store_dwordx2 v[188:189], v246, s[40:43], 0 offen sc1\n" \
  "s_memrealtime s[98:99]\n" \
  "s_memtime s[84:85]\n" \
  ".LM_loop_%=:\n" \
  "s_cmp_ge_u32 s48, 55\n" \
  "s_cbranch_scc1 .LM_xl_%=\n" \
  ".LM_xr_b_%=:\n" \
  "ds_read_b64_tr_b16 v[116:117], v177\n" \
  "ds_read_b64_tr_b16 v[118:119], v177 offset:256\n" \
  "ds_read_b64_tr_b16 v[120:121], v177 offset:512\n" \
  "ds_read_b64_tr_b16 v[122:123], v177 offset:768\n" \
  "ds_read_b32 v188, v184\n" \
  "v_readfirstlane_b32 s63, v183\n" \
  "v_readfirstlane_b32 s62, v182\n" \
  "s_cmp_eq_u32 s63, s55\n" \
  "s_cselect_b32 s62, s62, 0\n" \
  "s_max_i32 s54, s54, s62\n" \
  "s_add_u32 s48, s48, 1\n" \
  "s_add_u32 s62, s48, 1\n" \
  "s_min_u32 s62, s62, 63\n" \
  "s_cmp_le_i32 s54, s62\n" \
  "s_cbranch_scc1 .LM_pk_go_%=\n" \
  ".LM_pk_ok_%=:\n" \
  "s_xor_b32 s60, s49, s58\n" \
  "s_add_u32 s49, s49, 16384\n" \
  "s_xor_b32 s91, s91, 8192\n" \
  "s_mov_b32 m0, s91\n" \
  "s_nop 0\n" \
  "buffer_load_dwordx4 v245, s[40:43], s60 offen sc1 lds\n" \
  "buffer_load_dwordx4 v245, s[40:43], s60 offen offset:1024 sc1 lds\n" \
  "buffer_load_dwordx4 v245, s[40:43], s60 offen offset:2048 sc1 lds\n" \
  "buffer_load_dwordx4 v245, s[40:43], s60 offen offset:3072 sc1 lds\n" \
  "s_add_u32 s62, s48, 2\n" \
  "s_cmp_le_i32 s54, s62\n" \
  "s_cselect_b32 s62, s74, 2130706432\n" \
  "buffer_load_dwordx2 v[182:183], off, s[40:43], s62 sc1\n" \
  "v_xor_b32 v176, 8192, v176\n" \
  "v_mov_b64 v[76:77], 0\n" \
  "v_mov_b64 v[78:79], 0\n" \
  "v_mov_b64 v[80:81], 0\n" \
  "v_mov_b64 v[82:83], 0\n" \
  "v_mov_b64 v[84:85], 0\n" \
  "v_mov_b64 v[86:87], 0\n" \
  "v_mov_b64 v[88:89], 0\n" \
  "v_mov_b64 v[90:91], 0\n" \
  "v_mov_b64 v[92:93], 0\n" \
  "v_mov_b64 v[94:95], 0\n" \
  "v_mov_b32 v72, s48\n" \
  "s_sub_u32 s60, s48, 2\n" \
  "s_cselect_b32 s60, 8192, s60\n" \
  "s_lshl_b32 s60, s60, 14\n" \
  "s_xor_b32 s60, s60, s50\n" \
  "s_cmp_le_u32 s48, 58\n" \
  "s_cselect_b32 s60, s60, 2130706432\n" \
  "s_waitcnt lgkmcnt(3)\n" \
  "buffer_store_dwordx4 v[116:119], v240, s[40:43], s60 offen sc1\n" \
  "s_waitcnt lgkmcnt(1)\n" \
  "buffer_store_dwordx4 v[120:123], v240, s[40:43], s60 offen offset:1024 sc1\n" \
  "s_waitcnt lgkmcnt(0)\n" \
  "s_waitcnt lgkmcnt(0)\n" \
  "v_readfirstlane_b32 s60, v188\n" \
  "v_mov_b32 v189, s55\n" \
  "s_lshl_b32 s66, s66, 10\n" \
  "s_and_b32 s61, s60, 1023\n" \
  "s_cselect_b32 s62, 4096000, 0\n" \
  "s_andn2_b32 s60, s60, 1023\n" \
  "s_sub_u32 s60, s66, s60\n" \
  "s_mov_b32 s66, s84\n" \
  "s_or_b32 s60, s60, s61\n" \
  "s_cmp_lt_u32 s60, s62\n" \
  "s_cselect_b32 s60, s60, -1\n" \
  "s_min_u32 s46, s46, s60\n" \
  "v_mov_b32 v188, s46\n" \
  "s_cmp_lg_u32 s46, -1\n" \
  "s_cselect_b32 s61, s75, 2130706432\n" \
  "s_and_b32 s60, s48, 3\n" \
  "s_cmp_eq_u32 s60, 1\n" \
  "s_cselect_b32 s61, s61, 2130706432\n" \
  "s_cselect_b32 s46, -1, s46\n" \
  "buffer_store_dwordx2 v[188:189], v74, s[40:43], s61 offen sc1\n" \
  "s_cmp_eq_u32 s48, 58\n" \
  "s_cbranch_scc1 .LM_ef_%=\n" \
  ".LM_ef_b_%=:\n" \
  "s_branch .LM_rot_%=\n" \
  "s_nop 7\n" \
  ".LM_slow_%=:\n" \
  "s_bitcmp1_b32 s70, 0\n" \
  "s_cbranch_scc1 .LM_back_%=\n" \
  "s_waitcnt lgkmcnt(0)\n" \
  "s_cmp_lt_u32 s70, 10\n" \
  "s_nop 3\n" \
  "s_cbranch_scc1 .LM_nol_%=\n" \
  "v_max_u32 v189, v116, v117\n" \
  "v_max3_u32 v189, v189, v118, v119\n" \
  "v_max3_u32 v189, v189, v120, v121\n" \
  "v_max3_u32 v189, v189, v122, v123\n" \
  "v_max3_u32 v189, v189, v124, v125\n" \
  "v_max3_u32 v189, v189, v126, v127\n" \
  "v_max3_u32 v189, v189, v128, v129\n" \
  "v_max3_u32 v189, v189, v130, v131\n" \
  "v_max3_u32 v189, v189, v132, v133\n" \
  "v_max3_u32 v189, v189, v134, v135\n" \
  "v_max3_u32 v189, v189, v136, v137\n" \
  "v_max3_u32 v189, v189, v138, v139\n" \
  "v_max3_u32 v189, v189, v140, v141\n" \
  "v_max3_u32 v189, v189, v142, v143\n" \
  "v_max3_u32 v189, v189, v144, v145\n" \
  "v_max3_u32 v189, v189, v146, v147\n" \
  "v_max3_u32 v189, v189, v148, v149\n" \
  "v_max3_u32 v189, v189, v150, v151\n" \
  "v_cmp_eq_u32 vcc, -1, v189\n" \
  "s_cbranch_vccz .LM_back_%=\n" \
  ".LM_nol_%=:\n" \
  "s_add_u32 s70, s70, 2\n" \
  "s_cmp_gt_u32 s70, 65536\n" \
  "s_cbranch_scc1 .LM_dset_%=\n" \
  "s_barrier\n" \
  "v_mov_b64 v[76:77], 0\n" \
  "v_mov_b64 v[78:79], 0\n" \
  "v_mov_b64 v[80:81], 0\n" \
  "v_mov_b64 v[82:83], 0\n" \
  "v_mov_b64 v[84:85], 0\n" \
  "v_mov_b64 v[86:87], 0\n" \
  "v_mov_b64 v[88:89], 0\n" \
  "v_mov_b64 v[90:91], 0\n" \
  "v_mov_b64 v[92:93], 0\n" \
  "v_mov_b64 v[94:95], 0\n" \
  "s_branch .LM_rt_%=\n" \
  ".LM_dset_%=:\n" \
  "v_mov_b32 v136, 1\n" \
  "v_mov_b32 v137, s55\n" \
  "buffer_store_dwordx2 v[136:137], off, s[40:43], %38 offset:128 sc1\n" \
  "s_mov_b32 s70, 1\n" \
  "v_mov_b64 v[76:77], 0\n" \
  "v_mov_b64 v[78:79], 0\n" \
  "v_mov_b64 v[80:81], 0\n" \
  "v_mov_b64 v[82:83], 0\n" \
  "v_mov_b64 v[84:85], 0\n" \
  "v_mov_b64 v[86:87], 0\n" \
  "v_mov_b64 v[88:89], 0\n" \
  "v_mov_b64 v[90:91], 0\n" \
  "v_mov_b64 v[92:93], 0\n" \
  "v_mov_b64 v[94:95], 0\n" \
  "s_waitcnt vmcnt(0) lgkmcnt(0)\n" \
  "s_barrier\n" \
  "s_branch .LM_p_%=\n" \
  ".LM_xr_%=:\n" \
  "s_waitcnt lgkmcnt(0)\n" \
  "s_nop 7\n" \
  "ds_read_b64_tr_b16 v[132:133], v244\n" \
  "ds_read_b64_tr_b16 v[134:135], v244 offset:256\n" \
  "ds_read_b64_tr_b16 v[136:137], v244 offset:512\n" \
  "ds_read_b64_tr_b16 v[138:139], v244 offset:768\n" \
  "ds_read_b64_tr_b16 v[140:141], v244 offset:1024\n" \
  "ds_read_b64_tr_b16 v[142:143], v244 offset:1280\n" \
  "ds_read_b64_tr_b16 v[144:145], v244 offset:1536\n" \
  "ds_read_b64_tr_b16 v[146:147], v244 offset:1792\n" \
  "s_waitcnt lgkmcnt(6)\n" \
  "v_mfma_f32_16x16x32_bf16 v[148:151], v[132:135], v[100:103], 0\n" \
  "s_waitcnt lgkmcnt(4)\n" \
  "v_mfma_f32_16x16x32_bf16 v[148:151], v[136:139], v[104:107], v[148:151]\n" \
  "s_waitcnt lgkmcnt(2)\n" \
  "v_mfma_f32_16x16x32_bf16 v[148:151], v[140:143], v[108:111], v[148:151]\n" \
  "s_waitcnt lgkmcnt(0)\n" \
  "v_mfma_f32_16x16x32_bf16 v[148:151], v[144:147], v[112:115], v[148:151]\n" \
  "s_sub_u32 s60, s48, 58\n" \
  "s_lshl_b32 s60, s60, 16\n" \
  "s_add_u32 s60, s60, s97\n" \
  "s_cmp_eq_u32 s48, 0\n" \
  "s_cselect_b32 s60, 2130706432, s60\n" \
  "s_nop 2\n" \
  "v_max_f32 v158, v148, v149\n" \
  "v_max3_f32 v158, v150, v151, v158\n" \
  "v_mov_b32 v159, v158\n" \
  "s_nop 1\n" \
  "v_permlane32_swap_b32 v158, v159\n" \
  "v_max_f32 v158, v158, v159\n" \
  "v_mul_f32 v152, s65, v158\n" \
  "v_min_f32 v152, v152, v178\n" \
  "v_fma_f32 v148, v148, s64, v152\n" \
  "v_fma_f32 v149, v149, s64, v152\n" \
  "v_fma_f32 v150, v150, s64, v152\n" \
  "v_fma_f32 v151, v151, s64, v152\n" \
  "v_sub_f32 v160, v152, v178\n" \
  "v_exp_f32 v148, v148\n" \
  "v_exp_f32 v149, v149\n" \
  "v_exp_f32 v150, v150\n" \
  "v_exp_f32 v151, v151\n" \
  "v_exp_f32 v160, v160\n" \
  "v_add_f32 v153, v148, v149\n" \
  "v_add_f32 v158, v150, v151\n" \
  "v_add_f32 v153, v153, v158\n" \
  "v_mov_b32 v159, v153\n" \
  "s_nop 1\n" \
  "v_permlane32_swap_b32 v153, v159\n" \
  "v_add_f32 v153, v153, v159\n" \
  "v_fmac_f32 v153, v160, v179\n" \
  "v_rcp_f32 v154, v153\n" \
  "s_nop 0\n" \
  "v_mul_f32 v160, v160, v154\n" \
  "v_mul_f32 v154, v154, v252\n" \
  "v_mul_f32 v148, v148, v154\n" \
  "v_mul_f32 v149, v149, v154\n" \
  "v_mul_f32 v150, v150, v154\n" \
  "v_mul_f32 v151, v151, v154\n" \
  "v_fmac_f32 v148, v160, v253\n" \
  "v_fmac_f32 v150, v160, v254\n" \
  "v_cvt_pk_bf16_f32 v156, v148, v149\n" \
  "v_cvt_pk_bf16_f32 v157, v150, v151\n" \
  "buffer_store_dwordx2 v[156:157], v251, s[40:43], s60 offen sc1\n" \
  "s_cmp_eq_u32 s48, 63\n" \
  "s_cbranch_scc1 .LM_end_%=\n" \
  ".LM_xq_%=:\n" \
  "s_add_u32 s61, s48, 1\n" \
  "s_lshl_b32 s60, s61, 19\n" \
  "s_add_u32 s60, s60, s47\n" \
  "s_lshr_b32 s62, s60, 8\n" \
  "s_and_b32 s62, s62, 8192\n" \
  "s_xor_b32 s62, s62, s60\n" \
  "s_cmp_eq_u32 s48, 0\n" \
  "s_cselect_b32 s62, 2130706432, s62\n" \
  "s_add_u32 s63, s62, 4096\n" \
  "buffer_load_dwordx4 v[100:103], v249, s[40:43], s62 offen sc1\n" \
  "buffer_load_dwordx4 v[104:107], v249, s[40:43], s62 offen offset:2048 sc1\n" \
  "buffer_load_dwordx4 v[108:111], v249, s[40:43], s63 offen sc1\n" \
  "buffer_load_dwordx4 v[112:115], v249, s[40:43], s63 offen offset:2048 sc1\n" \
  "s_sub_u32 s60, s61, 58\n" \
  "s_lshl_b32 s60, s60, 16\n" \
  "s_add_u32 s60, s60, s97\n" \
  "s_cmp_eq_u32 s48, 0\n" \
  "s_cselect_b32 s60, 2130706432, s60\n" \
  "buffer_load_dwordx2 v[178:179], v250, s[40:43], s60 offen sc1\n" \
  "s_cbranch_scc1 .LM_dry_b_%=\n" \
  "s_branch .LM_xr_b_%=\n" \
  ".LM_xl_%=:\n" \
  "s_cmp_eq_u32 s48, 57\n" \
  "s_cbranch_scc1 .LM_xq_%=\n" \
  "s_cmp_eq_u32 s48, 56\n" \
  "s_cbranch_scc1 .LM_xc_%=\n" \
  "s_cmp_lg_u32 s48, 55\n" \
  "s_cbranch_scc1 .LM_xr_%=\n" \
  "buffer_load_dwordx2 v[178:179], off, s[40:43], %42 sc1\n" \
  "s_branch .LM_xr_b_%=\n" \
  ".LM_xc_%=:\n" \
  "v_readfirstlane_b32 s63, v179\n" \
  "s_cmp_eq_u32 s63, s55\n" \
  "s_cbranch_scc1 .LM_xr_b_%=\n" \
  "s_mov_b32 s69, 0\n" \
  ".LM_xs_%=:\n" \
  "buffer_load_dwordx2 v[178:179], off, s[40:43], %42 sc1\n" \
  "s_waitcnt vmcnt(0)\n" \
  "v_readfirstlane_b32 s62, v178\n" \
  "v_readfirstlane_b32 s63, v179\n" \
  "s_cmp_eq_u32 s63, s55\n" \
  "s_cselect_b32 s62, s62, 0\n" \
  "s_cmp_lg_u32 s62, 0\n" \
  "s_cbranch_scc1 .LM_xs_x_%=\n" \
  "s_sleep 2\n" \
  "s_add_u32 s69, s69, 1\n" \
  "s_and_b32 s63, s69, 1023\n" \
  "s_cbranch_scc1 .LM_xs_%=\n" \
  "s_cmp_gt_u32 s69, 2097152\n" \
  "s_cbranch_scc1 .LM_xs_set_%=\n" \
  "buffer_load_dwordx2 v[178:179], off, s[40:43], %38 offset:128 sc1\n" \
  "s_waitcnt vmcnt(0)\n" \
  "v_readfirstlane_b32 s63, v179\n" \
  "s_cmp_eq_u32 s63, s55\n" \
  "s_cbranch_scc0 .LM_xs_%=\n" \
  "s_branch .LM_xs_x_%=\n" \
  ".LM_xs_set_%=:\n" \
  "v_mov_b32 v178, 1\n" \
  "v_mov_b32 v179, s55\n" \
  "buffer_store_dwordx2 v[178:179], off, s[40:43], %38 offset:128 sc1\n" \
  "s_waitcnt vmcnt(0)\n" \
  ".LM_xs_x_%=:\n" \
  "s_branch .LM_xr_b_%=\n" \
  ".LM_pk_go_%=:\n" \
  "s_mov_b32 s94, s62\n" \
  "s_mov_b32 s69, 0\n" \
  ".LM_pk_%=:\n" \
  "buffer_load_dwordx2 v[186:187], off, s[40:43], s74 sc1\n" \
  "s_waitcnt vmcnt(0)\n" \
  "v_readfirstlane_b32 s54, v186\n" \
  "v_readfirstlane_b32 s63, v187\n" \
  "s_cmp_eq_u32 s63, s55\n" \
  "s_cselect_b32 s54, s54, 0\n" \
  "s_cmp_gt_i32 s54, s94\n" \
  "s_cbranch_scc1 .LM_pk_x_%=\n" \
  "s_sleep 2\n" \
  "s_add_u32 s69, s69, 1\n" \
  "s_and_b32 s63, s69, 1023\n" \
  "s_cbranch_scc1 .LM_pk_%=\n" \
  "s_cmp_gt_u32 s69, 2097152\n" \
  "s_cbranch_scc1 .LM_pk_set_%=\n" \
  "buffer_load_dwordx2 v[186:187], off, s[40:43], %38 offset:128 sc1\n" \
  "s_waitcnt vmcnt(0)\n" \
  "v_readfirstlane_b32 s63, v187\n" \
  "s_cmp_eq_u32 s63, s55\n" \
  "s_cbranch_scc0 .LM_pk_%=\n" \
  "s_branch .LM_pk_x_%=\n" \
  ".LM_pk_set_%=:\n" \
  "v_mov_b32 v186, 1\n" \
  "v_mov_b32 v187, s55\n" \
  "buffer_store_dwordx2 v[186:187], off, s[40:43], %38 offset:128 sc1\n" \
  "s_waitcnt vmcnt(0)\n" \
  ".LM_pk_x_%=:\n" \
  "s_branch .LM_pk_ok_%=\n" \
  ".LM_ef_%=:\n" \
  "s_waitcnt vmcnt(0) lgkmcnt(0)\n" \
  "buffer_store_dwordx2 v[72:73], v74, s[40:43], %35 offen sc1\n" \
  "s_branch .LM_ef_b_%=\n" \
  ".LM_ex_%=:\n" \
  "s_waitcnt vmcnt(0) lgkmcnt(0)\n" \
  "s_nop 7\n" \
  "ds_read_b64_tr_b16 v[120:121], v243\n" \
  "ds_read_b64_tr_b16 v[122:123], v243 offset:256\n" \
  "ds_read_b64_tr_b16 v[124:125], v243 offset:512\n" \
  "ds_read_b64_tr_b16 v[126:127], v243 offset:768\n" \
  "ds_read_b64_tr_b16 v[128:129], v243 offset:1024\n" \
  "ds_read_b64_tr_b16 v[130:131], v243 offset:1280\n" \
  "ds_read_b64_tr_b16 v[132:133], v243 offset:1536\n" \
  "ds_read_b64_tr_b16 v[134:135], v243 offset:1792\n" \
  "ds_read_b128 v[136:139], v176\n" \
  "ds_read_b128 v[144:147], v176 offset:2048\n" \
  "ds_read_b128 v[148:151], v176 offset:4096\n" \
  "ds_read_b128 v[152:155], v176 offset:6144\n" \
  "s_waitcnt lgkmcnt(3)\n" \
  "v_mfma_f32_16x16x32_bf16 v[156:159], v[136:139], v[120:123], 0\n" \
  "s_waitcnt lgkmcnt(2)\n" \
  "v_mfma_f32_16x16x32_bf16 v[156:159], v[144:147], v[124:127], v[156:159]\n" \
  "s_waitcnt lgkmcnt(1)\n" \
  "v_mfma_f32_16x16x32_bf16 v[156:159], v[148:151], v[128:131], v[156:159]\n" \
  "s_waitcnt lgkmcnt(0)\n" \
  "v_mfma_f32_16x16x32_bf16 v[156:159], v[152:155], v[132:135], v[156:159]\n" \
  "s_nop 7\n" \
  "ds_read_b128 v[136:139], v176 offset:1024\n" \
  "ds_read_b128 v[144:147], v176 offset:3072\n" \
  "ds_read_b128 v[148:151], v176 offset:5120\n" \
  "ds_read_b128 v[152:155], v176 offset:7168\n" \
  "s_waitcnt lgkmcnt(3)\n" \
  "v_mfma_f32_16x16x32_bf16 v[160:163], v[136:139], v[120:123], 0\n" \
  "s_waitcnt lgkmcnt(2)\n" \
  "v_mfma_f32_16x16x32_bf16 v[160:163], v[144:147], v[124:127], v[160:163]\n" \
  "s_waitcnt lgkmcnt(1)\n" \
  "v_mfma_f32_16x16x32_bf16 v[160:163], v[148:151], v[128:131], v[160:163]\n" \
  "s_waitcnt lgkmcnt(0)\n" \
  "v_mfma_f32_16x16x32_bf16 v[160:163], v[152:155], v[132:135], v[160:163]\n" \
  "s_nop 7\n" \
  "v_max_f32 v164, v140, v141\n" \
  "v_max3_f32 v164, v164, v142, v143\n" \
  "v_max3_f32 v164, v164, v156, v157\n" \
  "v_max3_f32 v164, v164, v158, v159\n" \
  "s_nop 2\n" \
  "v_max3_f32 v164, v164, v160, v161\n" \
  "v_max3_f32 v164, v164, v162, v163\n" \
  "v_mov_b32 v165, v164\n" \
  "s_nop 1\n" \
  "v_permlane32_swap_b32 v164, v165\n" \
  "v_max_f32 v164, v164, v165\n" \
  "v_mov_b32 v165, v164\n" \
  "s_nop 1\n" \
  "v_permlane16_swap_b32 v164, v165\n" \
  "v_max_f32 v164, v164, v165\n" \
  "v_sub_f32 v116, v140, v164\n" \
  "v_sub_f32 v117, v141, v164\n" \
  "v_sub_f32 v118, v142, v164\n" \
  "v_sub_f32 v119, v143, v164\n" \
  "v_sub_f32 v156, v156, v164\n" \
  "v_sub_f32 v157, v157, v164\n" \
  "v_sub_f32 v158, v158, v164\n" \
  "v_sub_f32 v159, v159, v164\n" \
  "v_sub_f32 v160, v160, v164\n" \
  "v_sub_f32 v161, v161, v164\n" \
  "v_sub_f32 v162, v162, v164\n" \
  "v_sub_f32 v163, v163, v164\n" \
  "v_exp_f32 v116, v116\n" \
  "v_exp_f32 v117, v117\n" \
  "v_exp_f32 v118, v118\n" \
  "v_exp_f32 v119, v119\n" \
  "v_exp_f32 v156, v156\n" \
  "v_exp_f32 v157, v157\n" \
  "v_exp_f32 v158, v158\n" \
  "v_exp_f32 v159, v159\n" \
  "v_exp_f32 v160, v160\n" \
  "v_exp_f32 v161, v161\n" \
  "v_exp_f32 v162, v162\n" \
  "v_exp_f32 v163, v163\n" \
  "v_add_f32 v164, v116, v117\n" \
  "v_add_f32 v164, v164, v118\n" \
  "v_add_f32 v164, v164, v119\n" \
  "v_mul_f32 v164, 0.5, v164\n" \
  "v_add_f32 v164, v164, v156\n" \
  "v_add_f32 v164, v164, v157\n" \
  "v_add_f32 v164, v164, v158\n" \
  "v_add_f32 v164, v164, v159\n" \
  "v_add_f32 v164, v164, v160\n" \
  "v_add_f32 v164, v164, v161\n" \
  "v_add_f32 v164, v164, v162\n" \
  "v_add_f32 v164, v164, v163\n" \
  "v_mov_b32 v165, v164\n" \
  "s_nop 1\n" \
  "v_permlane32_swap_b32 v164, v165\n" \
  "v_add_f32 v164, v164, v165\n" \
  "v_mov_b32 v165, v164\n" \
  "s_nop 1\n" \
  "v_permlane16_swap_b32 v164, v165\n" \
  "v_add_f32 v164, v164, v165\n" \
  "v_rcp_f32 v188, v164\n" \
  "v_cvt_pk_bf16_f32 v156, v156, v157\n" \
  "v_cvt_pk_bf16_f32 v157, v158, v159\n" \
  "v_cvt_pk_bf16_f32 v160, v160, v161\n" \
  "v_cvt_pk_bf16_f32 v161, v162, v163\n" \
  "v_mov_b32 v165, 17408\n" \
  "s_cmp_eq_u32 s48, 0\n" \
  "s_cbranch_scc1 .LM_ex2_%=\n" \
  ".LM_ex1_%=:\n" \
  "ds_read_b64 v[166:167], v165\n" \
  "s_waitcnt lgkmcnt(0)\n" \
  "v_min_u32 v166, v166, v167\n" \
  "v_cmp_ge_u32 vcc, s48, v166\n" \
  "s_cbranch_vccnz .LM_ex1_%=\n" \
  ".LM_ex2_%=:\n" \
  "buffer_store_dwordx2 v[156:157], v247, s[40:43], 0 offen sc1\n" \
  "buffer_store_dwordx2 v[160:161], v247, s[40:43], 0 offen offset:256 sc1\n" \
  "v_pk_fma_f32 v[116:117], v[116:117], v[180:181], v[96:97] op_sel_hi:[1,0,1]\n" \
  "v_pk_fma_f32 v[118:119], v[118:119], v[180:181], v[98:99] op_sel_hi:[1,0,1]\n" \
  "v_pk_mul_f32 v[116:117], v[116:117], v[188:189] op_sel_hi:[1,0]\n" \
  "v_pk_mul_f32 v[118:119], v[118:119], v[188:189] op_sel_hi:[1,0]\n" \
  "v_cvt_pk_bf16_f32 v188, v116, v117\n" \
  "v_cvt_pk_bf16_f32 v189, v118, v119\n" \
  "s_waitcnt vmcnt(0)\n" \
  "s_mov_b32 s52, 2.0\n" \
  "s_cmp_eq_u32 s48, 0\n" \
  "s_cbranch_scc1 .LM_ex_b_%=\n" \
  "v_lshrrev_b32 v165, 1, v240\n" \
  "v_min_u32 v165, 280, v165\n" \
  "buffer_load_dwordx2 v[166:167], v165, s[40:43], %43 offen sc1\n" \
  "s_waitcnt vmcnt(0)\n" \
  "v_cmp_eq_u32 vcc, s55, v167\n" \
  "s_nop 1\n" \
  "v_cndmask_b32 v166, 0, v166, vcc\n" \
  "v_add_u32 v164, 2, v166\n" \
  "s_mov_b32 s69, 0\n" \
  ".LM_ex3_%=:\n" \
  "s_sleep 8\n" \
  "buffer_load_dwordx2 v[166:167], v165, s[40:43], %43 offen sc1\n" \
  "s_waitcnt vmcnt(0)\n" \
  "v_cmp_eq_u32 vcc, s55, v167\n" \
  "s_nop 1\n" \
  "v_cndmask_b32 v166, 0, v166, vcc\n" \
  "v_cmp_gt_u32 vcc, v164, v166\n" \
  "s_cbranch_vccz .LM_ex_b_%=\n" \
  "s_add_u32 s69, s69, 1\n" \
  "s_and_b32 s63, s69, 1023\n" \
  "s_cbranch_scc1 .LM_ex3_%=\n" \
  "s_cmp_gt_u32 s69, 2097152\n" \
  "s_cbranch_scc1 .LM_ex3_set_%=\n" \
  "buffer_load_dwordx2 v[166:167], off, s[40:43], %38 offset:128 sc1\n" \
  "s_waitcnt vmcnt(0)\n" \
  "v_readfirstlane_b32 s63, v167\n" \
  "s_cmp_eq_u32 s63, s55\n" \
  "s_cbranch_scc0 .LM_ex3_%=\n" \
  "s_branch .LM_ex_b_%=\n" \
  ".LM_ex3_set_%=:\n" \
  "v_mov_b32 v166, 1\n" \
  "v_mov_b32 v167, s55\n" \
  "buffer_store_dwordx2 v[166:167], off, s[40:43], %38 offset:128 sc1\n" \
  "s_waitcnt vmcnt(0)\n" \
  "s_branch .LM_ex_b_%=\n" \
  ".LM_end_%=:\n" \
  "s_waitcnt vmcnt(0)\n" \
  "s_waitcnt vmcnt(0)\n" \
  "s_nop 7\n" \
  "s_endpgm\n"
#define M_ASM_VOPS "+{v239}"(ci.sidx), "+{v240}"(ci.l16), "+{v241}"(ci.qw), "+{v242}"(ci.kw), "+{v243}"(ci.fq), "+{v244}"(ci.fk), "+{v245}"(ci.kxo), "+{v246}"(ci.pst), "+{v247}"(ci.ps2), "+{v248}"(ci.lx), "+{v249}"(ci.qxo), "+{v250}"(ci.sto), "+{v251}"(ci.plx), "+{v252}"(ci.mk), "+{v253}"(ci.ck0), "+{v254}"(ci.ck2), "+{v255}"(ci.hx), "+{v200}"(ci.vq0), "+{v201}"(ci.vq1), "+{v202}"(ci.vq2), "+{v203}"(ci.vq3), "+{v204}"(ci.tro0), "+{v205}"(ci.tro1), "+{v206}"(ci.m0o)
#define M_ASM_SOPS "s"(ci.ws), "s"(ci.Wa), "s"(ci.Wb), "s"(ci.mem0), "s"(ci.w), "s"(ci.o_a), "s"(ci.o_b), "s"(ci.o_nx), "s"(ci.o_exp), "s"(ci.lab), "s"(ci.f_p), "s"(ci.f_m), "s"(ci.f_dur), "s"(ci.f_go), "s"(ci.o_ctl), "s"(ci.o_dbg), "s"(ci.dret), "s"(ci.o_qx), "s"(ci.f_s), "s"(ci.f_r)
#define M_ASM_CLOB "v0", "v1", "v2", "v3", "v4", "v5", "v6", "v7", "v8", "v9", "v10", "v11", "v12", "v13", "v14", "v15", "v16", "v17", "v18", "v19", "v20", "v21", "v22", "v23", "v24", "v25", "v26", "v27", "v28", "v29", "v30", "v31", "v32", "v33", "v34", "v35", "v36", "v37", "v38", "v39", "v40", "v41", "v42", "v43", "v44", "v45", "v46", "v47", "v48", "v49", "v50", "v51", "v52", "v53", "v54", "v55", "v56", "v57", "v58", "v59", "v60", "v61", "v62", "v63", "v64", "v65", "v66", "v67", "v68", "v69", "v70", "v71", "v72", "v73", "v74", "v75", "v76", "v77", "v78", "v79", "v80", "v81", "v82", "v83", "v84", "v85", "v86", "v87", "v88", "v89", "v90", "v91", "v92", "v93", "v94", "v95", "v96", "v97", "v98", "v99", "v100", "v101", "v102", "v103", "v104", "v105", "v106", "v107", "v108", "v109", "v110", "v111", "v112", "v113", "v114", "v115", "v116", "v117", "v118", "v119", "v120", "v121", "v122", "v123", "v124", "v125", "v126", "v127", "v128", "v129", "v130", "v131", "v132", "v133", "v134", "v135", "v136", "v137", "v138", "v139", "v140", "v141", "v142", "v143", "v144", "v145", "v146", "v147", "v148", "v149", "v150", "v151", "v152", "v153", "v154", "v155", "v156", "v157", "v158", "v159", "v160", "v161", "v162", "v163", "v164", "v165", "v166", "v167", "v168", "v169", "v170", "v171", "v172", "v173", "v174", "v175", "v176", "v177", "v178", "v179", "v180", "v181", "v182", "v183", "v184", "v185", "v186", "v187", "v188", "v189", "v190", "v191", "v192", "v193", "v194", "v195", "v196", "v197", "v198", "v199", "a0", "a1", "a2", "a3", "a4", "a5", "a6", "a7", "a8", "a9", "a10", "a11", "a12", "a13", "a14", "a15", "a16", "a17", "a18", "a19", "a20", "a21", "a22", "a23", "a24", "a25", "a26", "a27", "a28", "a29", "a30", "a31", "a32", "a33", "a34", "a35", "a36", "a37", "a38", "a39", "a40", "a41", "a42", "a43", "a44", "a45", "a46", "a47", "a48", "a49", "a50", "a51", "a52", "a53", "a54", "a55", "a56", "a57", "a58", "a59", "a60", "a61", "a62", "a63", "a64", "a65", "a66", "a67", "a68", "a69", "a70", "a71", "a72", "a73", "a74", "a75", "a76", "a77", "a78", "a79", "a80", "a81", "a82", "a83", "a84", "a85", "a86", "a87", "a88", "a89", "a90", "a91", "a92", "a93", "a94", "a95", "a96", "a97", "a98", "a99", "a100", "a101", "a102", "a103", "a104", "a105", "a106", "a107", "a108", "a109", "a110", "a111", "a112", "a113", "a114", "a115", "a116", "a117", "a118", "a119", "a120", "a121", "a122", "a123", "a124", "a125", "a126", "a127", "a128", "a129", "a130", "a131", "a132", "a133", "a134", "a135", "a136", "a137", "a138", "a139", "a140", "a141", "a142", "a143", "a144", "a145", "a146", "a147", "a148", "a149", "a150", "a151", "a152", "a153", "a154", "a155", "a156", "a157", "a158", "a159", "a160", "a161", "a162", "a163", "a164", "a165", "a166", "a167", "a168", "a169", "a170", "a171", "a172", "a173", "a174", "a175", "a176", "a177", "a178", "a179", "a180", "a181", "a182", "a183", "a184", "a185", "a186", "a187", "a188", "a189", "a190", "a191", "a192", "a193", "a194", "a195", "a196", "a197", "a198", "a199", "a200", "a201", "a202", "a203", "a204", "a205", "a206", "a207", "a208", "a209", "a210", "a211", "a212", "a213", "a214", "a215", "a216", "a217", "a218", "a219", "a220", "a221", "a222", "a223", "a224", "a225", "a226", "a227", "a228", "a229", "a230", "a231", "a232", "a233", "a234", "a235", "a236", "a237", "a238", "a239", "a240", "a241", "a242", "a243", "a244", "a245", "a246", "a247", "a248", "a249", "a250", "a251", "a252", "a253", "a254", "a255", "s40", "s41", "s42", "s43", "s44", "s45", "s46", "s47", "s48", "s49", "s50", "s51", "s52", "s53", "s54", "s55", "s56", "s57", "s58", "s59", "s60", "s61", "s62", "s63", "s64", "s65", "s66", "s67", "s68", "s69", "s70", "s71", "s72", "s73", "s74", "s75", "s76", "s77", "s78", "s79", "s80", "s81", "s82", "s83", "s84", "s85", "s86", "s87", "s88", "s89", "s90", "s91", "s92", "s93", "s94", "s95", "s96", "s97", "s98", "s99", "vcc", "scc", "memory"
#define V_ASM_TEXT \
  "s_mov_b64 s[40:41], %32\n" \
  "s_and_b32 s41, s41, 0xffff\n" \
  "s_mov_b32 s42, 255852544\n" \
  "s_mov_b32 s43, 0x00020000\n" \
  "s_mov_b32 s46, 4718592\n" \
  "s_mov_b32 s47, 0x00020000\n" \
  "s_lshl_b32 s72, %36, 11\n" \
  "buffer_load_dword v196, off, s[40:43], %46 sc1\n" \
  "s_mov_b64 s[44:45], %34\n" \
  "s_and_b32 s45, s45, 0xffff\n" \
  "s_add_u32 s60, %38, 0\n" \
  "s_lshl_b32 s61, s72, 1\n" \
  "s_add_u32 m0, s61, 16384\n" \
  "s_nop 0\n" \
  "buffer_load_dwordx4 v204, s[44:47], s60 offen lds\n" \
  "buffer_load_dwordx4 v205, s[44:47], s60 offen offset:1024 lds\n" \
  "buffer_load_dwordx4 v206, s[44:47], s60 offen offset:2048 lds\n" \
  "buffer_load_dwordx4 v207, s[44:47], s60 offen offset:3072 lds\n" \
  "s_add_u32 s60, %38, 18432\n" \
  "s_lshl_b32 s61, s72, 1\n" \
  "s_add_u32 m0, s61, 32768\n" \
  "s_nop 0\n" \
  "buffer_load_dwordx4 v204, s[44:47], s60 offen lds\n" \
  "buffer_load_dwordx4 v205, s[44:47], s60 offen offset:1024 lds\n" \
  "buffer_load_dwordx4 v206, s[44:47], s60 offen offset:2048 lds\n" \
  "buffer_load_dwordx4 v207, s[44:47], s60 offen offset:3072 lds\n" \
  "s_add_u32 s60, %38, 256\n" \
  "s_lshl_b32 s61, s72, 1\n" \
  "s_add_u32 m0, s61, 49152\n" \
  "s_nop 0\n" \
  "buffer_load_dwordx4 v204, s[44:47], s60 offen lds\n" \
  "buffer_load_dwordx4 v205, s[44:47], s60 offen offset:1024 lds\n" \
  "buffer_load_dwordx4 v206, s[44:47], s60 offen offset:2048 lds\n" \
  "buffer_load_dwordx4 v207, s[44:47], s60 offen offset:3072 lds\n" \
  "s_add_u32 s60, %38, 18688\n" \
  "s_lshl_b32 s61, s72, 1\n" \
  "s_add_u32 m0, s61, 65536\n" \
  "s_nop 0\n" \
  "buffer_load_dwordx4 v204, s[44:47], s60 offen lds\n" \
  "buffer_load_dwordx4 v205, s[44:47], s60 offen offset:1024 lds\n" \
  "buffer_load_dwordx4 v206, s[44:47], s60 offen offset:2048 lds\n" \
  "buffer_load_dwordx4 v207, s[44:47], s60 offen offset:3072 lds\n" \
  "s_add_u32 s60, %38, 512\n" \
  "s_lshl_b32 s61, s72, 1\n" \
  "s_add_u32 m0, s61, 81920\n" \
  "s_nop 0\n" \
  "buffer_load_dwordx4 v204, s[44:47], s60 offen lds\n" \
  "buffer_load_dwordx4 v205, s[44:47], s60 offen offset:1024 lds\n" \
  "buffer_load_dwordx4 v206, s[44:47], s60 offen offset:2048 lds\n" \
  "buffer_load_dwordx4 v207, s[44:47], s60 offen offset:3072 lds\n" \
  "s_add_u32 s60, %38, 18944\n" \
  "s_lshl_b32 s61, s72, 1\n" \
  "s_add_u32 m0, s61, 98304\n" \
  "s_nop 0\n" \
  "buffer_load_dwordx4 v204, s[44:47], s60 offen lds\n" \
  "buffer_load_dwordx4 v205, s[44:47], s60 offen offset:1024 lds\n" \
  "buffer_load_dwordx4 v206, s[44:47], s60 offen offset:2048 lds\n" \
  "buffer_load_dwordx4 v207, s[44:47], s60 offen offset:3072 lds\n" \
  "s_add_u32 s60, %38, 768\n" \
  "s_lshl_b32 s61, s72, 1\n" \
  "s_add_u32 m0, s61, 114688\n" \
  "s_nop 0\n" \
  "buffer_load_dwordx4 v204, s[44:47], s60 offen lds\n" \
  "buffer_load_dwordx4 v205, s[44:47], s60 offen offset:1024 lds\n" \
  "buffer_load_dwordx4 v206, s[44:47], s60 offen offset:2048 lds\n" \
  "buffer_load_dwordx4 v207, s[44:47], s60 offen offset:3072 lds\n" \
  "s_add_u32 s60, %38, 19200\n" \
  "s_lshl_b32 s61, s72, 1\n" \
  "s_add_u32 m0, s61, 131072\n" \
  "s_nop 0\n" \
  "buffer_load_dwordx4 v204, s[44:47], s60 offen lds\n" \
  "buffer_load_dwordx4 v205, s[44:47], s60 offen offset:1024 lds\n" \
  "buffer_load_dwordx4 v206, s[44:47], s60 offen offset:2048 lds\n" \
  "buffer_load_dwordx4 v207, s[44:47], s60 offen offset:3072 lds\n" \
  "s_mov_b32 s60, %38\n" \
  "s_lshl_b32 s61, s72, 1\n" \
  "s_add_u32 m0, s61, 147456\n" \
  "s_nop 0\n" \
  "buffer_load_dwordx4 v208, s[44:47], s60 offen lds\n" \
  "buffer_load_dwordx4 v209, s[44:47], s60 offen offset:1024 lds\n" \
  "buffer_load_dwordx4 v210, s[44:47], s60 offen offset:2048 lds\n" \
  "buffer_load_dwordx4 v211, s[44:47], s60 offen offset:3072 lds\n" \
  "s_mov_b64 s[44:45], %35\n" \
  "s_and_b32 s45, s45, 0xffff\n" \
  "s_lshr_b32 s60, s72, 3\n" \
  "s_mov_b32 m0, s72\n" \
  "s_nop 0\n" \
  "buffer_load_dwordx4 v214, s[44:47], s60 offen lds\n" \
  "s_add_u32 s60, s60, 128\n" \
  "s_add_u32 m0, s72, 1024\n" \
  "s_nop 0\n" \
  "buffer_load_dwordx4 v214, s[44:47], s60 offen lds\n" \
  "s_movk_i32 s60, 1024\n" \
  "s_movk_i32 m0, 8192\n" \
  "s_nop 0\n" \
  "buffer_load_dwordx4 v214, s[44:47], s60 offen lds\n" \
  "s_waitcnt vmcnt(27)\n" \
  "s_barrier\n" \
  "v_add_u32 v194, 16384, v215\n" \
  "v_xor_b32 v195, 2176, v194\n" \
  "ds_read_b64_tr_b16 a[144:145], v194\n" \
  "ds_read_b64_tr_b16 a[146:147], v194 offset:512\n" \
  "ds_read_b64_tr_b16 a[148:149], v195\n" \
  "ds_read_b64_tr_b16 a[150:151], v195 offset:512\n" \
  "ds_read_b64_tr_b16 a[152:153], v194 offset:8192\n" \
  "ds_read_b64_tr_b16 a[154:155], v194 offset:8704\n" \
  "ds_read_b64_tr_b16 a[156:157], v195 offset:8192\n" \
  "ds_read_b64_tr_b16 a[158:159], v195 offset:8704\n" \
  "v_add_u32 v194, 32768, v215\n" \
  "v_xor_b32 v195, 2176, v194\n" \
  "ds_read_b64_tr_b16 a[160:161], v194\n" \
  "ds_read_b64_tr_b16 a[162:163], v194 offset:512\n" \
  "ds_read_b64_tr_b16 a[164:165], v195\n" \
  "ds_read_b64_tr_b16 a[166:167], v195 offset:512\n" \
  "ds_read_b64_tr_b16 a[168:169], v194 offset:8192\n" \
  "ds_read_b64_tr_b16 a[170:171], v194 offset:8704\n" \
  "ds_read_b64_tr_b16 a[172:173], v195 offset:8192\n" \
  "ds_read_b64_tr_b16 a[174:175], v195 offset:8704\n" \
  "v_add_u32 v194, 49152, v215\n" \
  "v_xor_b32 v195, 2176, v194\n" \
  "ds_read_b64_tr_b16 a[176:177], v194\n" \
  "ds_read_b64_tr_b16 a[178:179], v194 offset:512\n" \
  "ds_read_b64_tr_b16 a[180:181], v195\n" \
  "ds_read_b64_tr_b16 a[182:183], v195 offset:512\n" \
  "ds_read_b64_tr_b16 a[184:185], v194 offset:8192\n" \
  "ds_read_b64_tr_b16 a[186:187], v194 offset:8704\n" \
  "ds_read_b64_tr_b16 a[188:189], v195 offset:8192\n" \
  "ds_read_b64_tr_b16 a[190:191], v195 offset:8704\n" \
  "s_waitcnt lgkmcnt(0)\n" \
  "s_barrier\n" \
  "s_mov_b64 s[44:45], %33\n" \
  "s_and_b32 s45, s45, 0xffff\n" \
  "s_add_u32 s60, %37, 0\n" \
  "s_lshl_b32 s61, s72, 1\n" \
  "s_add_u32 m0, s61, 16384\n" \
  "s_nop 0\n" \
  "buffer_load_dwordx4 v200, s[44:47], s60 offen lds\n" \
  "buffer_load_dwordx4 v201, s[44:47], s60 offen offset:1024 lds\n" \
  "buffer_load_dwordx4 v202, s[44:47], s60 offen offset:2048 lds\n" \
  "buffer_load_dwordx4 v203, s[44:47], s60 offen offset:3072 lds\n" \
  "s_add_u32 s60, %37, 524288\n" \
  "s_lshl_b32 s61, s72, 1\n" \
  "s_add_u32 m0, s61, 32768\n" \
  "s_nop 0\n" \
  "buffer_load_dwordx4 v200, s[44:47], s60 offen lds\n" \
  "buffer_load_dwordx4 v201, s[44:47], s60 offen offset:1024 lds\n" \
  "buffer_load_dwordx4 v202, s[44:47], s60 offen offset:2048 lds\n" \
  "buffer_load_dwordx4 v203, s[44:47], s60 offen offset:3072 lds\n" \
  "s_add_u32 s60, %37, 1048576\n" \
  "s_lshl_b32 s61, s72, 1\n" \
  "s_add_u32 m0, s61, 49152\n" \
  "s_nop 0\n" \
  "buffer_load_dwordx4 v200, s[44:47], s60 offen lds\n" \
  "buffer_load_dwordx4 v201, s[44:47], s60 offen offset:1024 lds\n" \
  "buffer_load_dwordx4 v202, s[44:47], s60 offen offset:2048 lds\n" \
  "buffer_load_dwordx4 v203, s[44:47], s60 offen offset:3072 lds\n" \
  "v_readfirstlane_b32 s55, v196\n" \
  "s_add_u32 s55, s55, 1\n" \
  "s_waitcnt vmcnt(27)\n" \
  "s_barrier\n" \
  "v_add_u32 v194, 65536, v215\n" \
  "v_xor_b32 v195, 2176, v194\n" \
  "ds_read_b64_tr_b16 a[192:193], v194\n" \
  "ds_read_b64_tr_b16 a[194:195], v194 offset:512\n" \
  "ds_read_b64_tr_b16 a[196:197], v195\n" \
  "ds_read_b64_tr_b16 a[198:199], v195 offset:512\n" \
  "ds_read_b64_tr_b16 a[200:201], v194 offset:8192\n" \
  "ds_read_b64_tr_b16 a[202:203], v194 offset:8704\n" \
  "ds_read_b64_tr_b16 a[204:205], v195 offset:8192\n" \
  "ds_read_b64_tr_b16 a[206:207], v195 offset:8704\n" \
  "v_add_u32 v194, 81920, v215\n" \
  "v_xor_b32 v195, 2176, v194\n" \
  "ds_read_b64_tr_b16 a[208:209], v194\n" \
  "ds_read_b64_tr_b16 a[210:211], v194 offset:512\n" \
  "ds_read_b64_tr_b16 a[212:213], v195\n" \
  "ds_read_b64_tr_b16 a[214:215], v195 offset:512\n" \
  "ds_read_b64_tr_b16 a[216:217], v194 offset:8192\n" \
  "ds_read_b64_tr_b16 a[218:219], v194 offset:8704\n" \
  "ds_read_b64_tr_b16 a[220:221], v195 offset:8192\n" \
  "ds_read_b64_tr_b16 a[222:223], v195 offset:8704\n" \
  "v_add_u32 v194, 98304, v215\n" \
  "v_xor_b32 v195, 2176, v194\n" \
  "ds_read_b64_tr_b16 a[224:225], v194\n" \
  "ds_read_b64_tr_b16 a[226:227], v194 offset:512\n" \
  "ds_read_b64_tr_b16 a[228:229], v195\n" \
  "ds_read_b64_tr_b16 a[230:231], v195 offset:512\n" \
  "ds_read_b64_tr_b16 a[232:233], v194 offset:8192\n" \
  "ds_read_b64_tr_b16 a[234:235], v194 offset:8704\n" \
  "ds_read_b64_tr_b16 a[236:237], v195 offset:8192\n" \
  "ds_read_b64_tr_b16 a[238:239], v195 offset:8704\n" \
  "s_waitcnt lgkmcnt(0)\n" \
  "s_barrier\n" \
  "s_add_u32 s60, %37, 1572864\n" \
  "s_lshl_b32 s61, s72, 1\n" \
  "s_add_u32 m0, s61, 65536\n" \
  "s_nop 0\n" \
  "buffer_load_dwordx4 v200, s[44:47], s60 offen lds\n" \
  "buffer_load_dwordx4 v201, s[44:47], s60 offen offset:1024 lds\n" \
  "buffer_load_dwordx4 v202, s[44:47], s60 offen offset:2048 lds\n" \
  "buffer_load_dwordx4 v203, s[44:47], s60 offen offset:3072 lds\n" \
  "s_add_u32 s60, %37, 2097152\n" \
  "s_lshl_b32 s61, s72, 1\n" \
  "s_add_u32 m0, s61, 81920\n" \
  "s_nop 0\n" \
  "buffer_load_dwordx4 v200, s[44:47], s60 offen lds\n" \
  "buffer_load_dwordx4 v201, s[44:47], s60 offen offset:1024 lds\n" \
  "buffer_load_dwordx4 v202, s[44:47], s60 offen offset:2048 lds\n" \
  "buffer_load_dwordx4 v203, s[44:47], s60 offen offset:3072 lds\n" \
  "s_add_u32 s60, %37, 2621440\n" \
  "s_lshl_b32 s61, s72, 1\n" \
  "s_add_u32 m0, s61, 98304\n" \
  "s_nop 0\n" \
  "buffer_load_dwordx4 v200, s[44:47], s60 offen lds\n" \
  "buffer_load_dwordx4 v201, s[44:47], s60 offen offset:1024 lds\n" \
  "buffer_load_dwordx4 v202, s[44:47], s60 offen offset:2048 lds\n" \
  "buffer_load_dwordx4 v203, s[44:47], s60 offen offset:3072 lds\n" \
  "s_waitcnt vmcnt(27)\n" \
  "s_barrier\n" \
  "v_add_u32 v194, 114688, v215\n" \
  "v_xor_b32 v195, 2176, v194\n" \
  "ds_read_b64_tr_b16 a[240:241], v194\n" \
  "ds_read_b64_tr_b16 a[242:243], v194 offset:512\n" \
  "ds_read_b64_tr_b16 a[244:245], v195\n" \
  "ds_read_b64_tr_b16 a[246:247], v195 offset:512\n" \
  "ds_read_b64_tr_b16 a[248:249], v194 offset:8192\n" \
  "ds_read_b64_tr_b16 a[250:251], v194 offset:8704\n" \
  "ds_read_b64_tr_b16 a[252:253], v195 offset:8192\n" \
  "ds_read_b64_tr_b16 a[254:255], v195 offset:8704\n" \
  "v_add_u32 v194, 131072, v215\n" \
  "v_xor_b32 v195, 2176, v194\n" \
  "ds_read_b64_tr_b16 v[0:1], v194\n" \
  "ds_read_b64_tr_b16 v[2:3], v194 offset:512\n" \
  "ds_read_b64_tr_b16 v[4:5], v195\n" \
  "ds_read_b64_tr_b16 v[6:7], v195 offset:512\n" \
  "ds_read_b64_tr_b16 v[8:9], v194 offset:8192\n" \
  "ds_read_b64_tr_b16 v[10:11], v194 offset:8704\n" \
  "ds_read_b64_tr_b16 v[12:13], v195 offset:8192\n" \
  "ds_read_b64_tr_b16 v[14:15], v195 offset:8704\n" \
  "v_mov_b32 v194, v216\n" \
  "v_xor_b32 v195, 2176, v194\n" \
  "ds_read_b64_tr_b16 v[16:17], v194\n" \
  "ds_read_b64_tr_b16 v[18:19], v194 offset:512\n" \
  "ds_read_b64_tr_b16 v[20:21], v195\n" \
  "ds_read_b64_tr_b16 v[22:23], v195 offset:512\n" \
  "ds_read_b64_tr_b16 v[24:25], v194 offset:8192\n" \
  "ds_read_b64_tr_b16 v[26:27], v194 offset:8704\n" \
  "ds_read_b64_tr_b16 v[28:29], v195 offset:8192\n" \
  "ds_read_b64_tr_b16 v[30:31], v195 offset:8704\n" \
  "v_mov_b32 v194, v217\n" \
  "v_xor_b32 v195, 2176, v194\n" \
  "ds_read_b64_tr_b16 v[32:33], v194\n" \
  "ds_read_b64_tr_b16 v[34:35], v194 offset:512\n" \
  "ds_read_b64_tr_b16 v[36:37], v195\n" \
  "ds_read_b64_tr_b16 v[38:39], v195 offset:512\n" \
  "ds_read_b64_tr_b16 v[40:41], v194 offset:8192\n" \
  "ds_read_b64_tr_b16 v[42:43], v194 offset:8704\n" \
  "ds_read_b64_tr_b16 v[44:45], v195 offset:8192\n" \
  "ds_read_b64_tr_b16 v[46:47], v195 offset:8704\n" \
  "s_waitcnt lgkmcnt(0)\n" \
  "s_barrier\n" \
  "s_add_u32 s60, %37, 3145728\n" \
  "s_lshl_b32 s61, s72, 1\n" \
  "s_add_u32 m0, s61, 114688\n" \
  "s_nop 0\n" \
  "buffer_load_dwordx4 v200, s[44:47], s60 offen lds\n" \
  "buffer_load_dwordx4 v201, s[44:47], s60 offen offset:1024 lds\n" \
  "buffer_load_dwordx4 v202, s[44:47], s60 offen offset:2048 lds\n" \
  "buffer_load_dwordx4 v203, s[44:47], s60 offen offset:3072 lds\n" \
  "s_add_u32 s60, %37, 3670016\n" \
  "s_lshl_b32 s61, s72, 1\n" \
  "s_add_u32 m0, s61, 131072\n" \
  "s_nop 0\n" \
  "buffer_load_dwordx4 v200, s[44:47], s60 offen lds\n" \
  "buffer_load_dwordx4 v201, s[44:47], s60 offen offset:1024 lds\n" \
  "buffer_load_dwordx4 v202, s[44:47], s60 offen offset:2048 lds\n" \
  "buffer_load_dwordx4 v203, s[44:47], s60 offen offset:3072 lds\n" \
  "s_add_u32 s60, %37, 4194304\n" \
  "s_lshl_b32 s61, s72, 1\n" \
  "s_add_u32 m0, s61, 147456\n" \
  "s_nop 0\n" \
  "buffer_load_dwordx4 v200, s[44:47], s60 offen lds\n" \
  "buffer_load_dwordx4 v201, s[44:47], s60 offen offset:1024 lds\n" \
  "buffer_load_dwordx4 v202, s[44:47], s60 offen offset:2048 lds\n" \
  "buffer_load_dwordx4 v203, s[44:47], s60 offen offset:3072 lds\n" \
  "buffer_load_dwordx2 v[198:199], off, s[40:43], %45 sc1\n" \
  "s_waitcnt vmcnt(25)\n" \
  "s_barrier\n" \
  "v_add_u32 v194, 16384, v212\n" \
  "ds_read_b64_tr_b16 a[0:1], v194\n" \
  "ds_read_b64_tr_b16 a[2:3], v194 offset:512\n" \
  "ds_read_b64_tr_b16 a[4:5], v194 offset:8192\n" \
  "ds_read_b64_tr_b16 a[6:7], v194 offset:8704\n" \
  "v_add_u32 v195, 16384, v213\n" \
  "ds_read_b64_tr_b16 a[72:73], v195\n" \
  "ds_read_b64_tr_b16 a[74:75], v195 offset:512\n" \
  "ds_read_b64_tr_b16 a[76:77], v195 offset:8192\n" \
  "ds_read_b64_tr_b16 a[78:79], v195 offset:8704\n" \
  "v_add_u32 v194, 32768, v212\n" \
  "ds_read_b64_tr_b16 a[8:9], v194\n" \
  "ds_read_b64_tr_b16 a[10:11], v194 offset:512\n" \
  "ds_read_b64_tr_b16 a[12:13], v194 offset:8192\n" \
  "ds_read_b64_tr_b16 a[14:15], v194 offset:8704\n" \
  "v_add_u32 v195, 32768, v213\n" \
  "ds_read_b64_tr_b16 a[80:81], v195\n" \
  "ds_read_b64_tr_b16 a[82:83], v195 offset:512\n" \
  "ds_read_b64_tr_b16 a[84:85], v195 offset:8192\n" \
  "ds_read_b64_tr_b16 a[86:87], v195 offset:8704\n" \
  "v_add_u32 v194, 49152, v212\n" \
  "ds_read_b64_tr_b16 a[16:17], v194\n" \
  "ds_read_b64_tr_b16 a[18:19], v194 offset:512\n" \
  "ds_read_b64_tr_b16 a[20:21], v194 offset:8192\n" \
  "ds_read_b64_tr_b16 a[22:23], v194 offset:8704\n" \
  "v_add_u32 v195, 49152, v213\n" \
  "ds_read_b64_tr_b16 a[88:89], v195\n" \
  "ds_read_b64_tr_b16 a[90:91], v195 offset:512\n" \
  "ds_read_b64_tr_b16 a[92:93], v195 offset:8192\n" \
  "ds_read_b64_tr_b16 a[94:95], v195 offset:8704\n" \
  "s_waitcnt lgkmcnt(0)\n" \
  "s_waitcnt vmcnt(13)\n" \
  "s_barrier\n" \
  "v_add_u32 v194, 65536, v212\n" \
  "ds_read_b64_tr_b16 a[24:25], v194\n" \
  "ds_read_b64_tr_b16 a[26:27], v194 offset:512\n" \
  "ds_read_b64_tr_b16 a[28:29], v194 offset:8192\n" \
  "ds_read_b64_tr_b16 a[30:31], v194 offset:8704\n" \
  "v_add_u32 v195, 65536, v213\n" \
  "ds_read_b64_tr_b16 a[96:97], v195\n" \
  "ds_read_b64_tr_b16 a[98:99], v195 offset:512\n" \
  "ds_read_b64_tr_b16 a[100:101], v195 offset:8192\n" \
  "ds_read_b64_tr_b16 a[102:103], v195 offset:8704\n" \
  "v_add_u32 v194, 81920, v212\n" \
  "ds_read_b64_tr_b16 a[32:33], v194\n" \
  "ds_read_b64_tr_b16 a[34:35], v194 offset:512\n" \
  "ds_read_b64_tr_b16 a[36:37], v194 offset:8192\n" \
  "ds_read_b64_tr_b16 a[38:39], v194 offset:8704\n" \
  "v_add_u32 v195, 81920, v213\n" \
  "ds_read_b64_tr_b16 a[104:105], v195\n" \
  "ds_read_b64_tr_b16 a[106:107], v195 offset:512\n" \
  "ds_read_b64_tr_b16 a[108:109], v195 offset:8192\n" \
  "ds_read_b64_tr_b16 a[110:111], v195 offset:8704\n" \
  "v_add_u32 v194, 98304, v212\n" \
  "ds_read_b64_tr_b16 a[40:41], v194\n" \
  "ds_read_b64_tr_b16 a[42:43], v194 offset:512\n" \
  "ds_read_b64_tr_b16 a[44:45], v194 offset:8192\n" \
  "ds_read_b64_tr_b16 a[46:47], v194 offset:8704\n" \
  "v_add_u32 v195, 98304, v213\n" \
  "ds_read_b64_tr_b16 a[112:113], v195\n" \
  "ds_read_b64_tr_b16 a[114:115], v195 offset:512\n" \
  "ds_read_b64_tr_b16 a[116:117], v195 offset:8192\n" \
  "ds_read_b64_tr_b16 a[118:119], v195 offset:8704\n" \
  "s_waitcnt lgkmcnt(0)\n" \
  "s_waitcnt vmcnt(1)\n" \
  "s_barrier\n" \
  "v_add_u32 v194, 114688, v212\n" \
  "ds_read_b64_tr_b16 a[48:49], v194\n" \
  "ds_read_b64_tr_b16 a[50:51], v194 offset:512\n" \
  "ds_read_b64_tr_b16 a[52:53], v194 offset:8192\n" \
  "ds_read_b64_tr_b16 a[54:55], v194 offset:8704\n" \
  "v_add_u32 v195, 114688, v213\n" \
  "ds_read_b64_tr_b16 a[120:121], v195\n" \
  "ds_read_b64_tr_b16 a[122:123], v195 offset:512\n" \
  "ds_read_b64_tr_b16 a[124:125], v195 offset:8192\n" \
  "ds_read_b64_tr_b16 a[126:127], v195 offset:8704\n" \
  "v_add_u32 v194, 131072, v212\n" \
  "ds_read_b64_tr_b16 a[56:57], v194\n" \
  "ds_read_b64_tr_b16 a[58:59], v194 offset:512\n" \
  "ds_read_b64_tr_b16 a[60:61], v194 offset:8192\n" \
  "ds_read_b64_tr_b16 a[62:63], v194 offset:8704\n" \
  "v_add_u32 v195, 131072, v213\n" \
  "ds_read_b64_tr_b16 a[128:129], v195\n" \
  "ds_read_b64_tr_b16 a[130:131], v195 offset:512\n" \
  "ds_read_b64_tr_b16 a[132:133], v195 offset:8192\n" \
  "ds_read_b64_tr_b16 a[134:135], v195 offset:8704\n" \
  "v_add_u32 v194, 147456, v212\n" \
  "ds_read_b64_tr_b16 a[64:65], v194\n" \
  "ds_read_b64_tr_b16 a[66:67], v194 offset:512\n" \
  "ds_read_b64_tr_b16 a[68:69], v194 offset:8192\n" \
  "ds_read_b64_tr_b16 a[70:71], v194 offset:8704\n" \
  "v_add_u32 v195, 147456, v213\n" \
  "ds_read_b64_tr_b16 a[136:137], v195\n" \
  "ds_read_b64_tr_b16 a[138:139], v195 offset:512\n" \
  "ds_read_b64_tr_b16 a[140:141], v195 offset:8192\n" \
  "ds_read_b64_tr_b16 a[142:143], v195 offset:8704\n" \
  "s_waitcnt lgkmcnt(0)\n" \
  "s_waitcnt vmcnt(0)\n" \
  "v_readfirstlane_b32 s96, v198\n" \
  "v_readfirstlane_b32 s63, v199\n" \
  "s_cmp_eq_u32 s63, s55\n" \
  "s_cselect_b32 s96, s96, 0\n" \
  "s_cmp_ge_u32 s96, 0xc0000000\n" \
  "s_cbranch_scc1 .LV_go_x_%=\n" \
  "s_mov_b32 s69, 0\n" \
  ".LV_go_%=:\n" \
  "buffer_load_dwordx2 v[196:197], off, s[40:43], %45 sc1\n" \
  "s_waitcnt vmcnt(0)\n" \
  "v_readfirstlane_b32 s96, v196\n" \
  "v_readfirstlane_b32 s63, v197\n" \
  "s_cmp_eq_u32 s63, s55\n" \
  "s_cselect_b32 s96, s96, 0\n" \
  "s_cmp_ge_u32 s96, 0xc0000000\n" \
  "s_cbranch_scc1 .LV_go_x_%=\n" \
  "s_sleep 8\n" \
  "s_add_u32 s69, s69, 1\n" \
  "s_and_b32 s63, s69, 1023\n" \
  "s_cbranch_scc1 .LV_go_%=\n" \
  "s_cmp_gt_u32 s69, 2097152\n" \
  "s_cbranch_scc1 .LV_go_set_%=\n" \
  "buffer_load_dwordx2 v[196:197], off, s[40:43], %46 offset:128 sc1\n" \
  "s_waitcnt vmcnt(0)\n" \
  "v_readfirstlane_b32 s63, v197\n" \
  "s_cmp_eq_u32 s63, s55\n" \
  "s_cbranch_scc0 .LV_go_%=\n" \
  "s_branch .LV_go_x_%=\n" \
  ".LV_go_set_%=:\n" \
  "v_mov_b32 v196, 1\n" \
  "v_mov_b32 v197, s55\n" \
  "buffer_store_dwordx2 v[196:197], off, s[40:43], %46 offset:128 sc1\n" \
  "s_waitcnt vmcnt(0)\n" \
  ".LV_go_x_%=:\n" \
  "s_and_b32 s60, s96, 2\n" \
  "s_lshl_b32 s60, s60, 12\n" \
  "s_xor_b32 s74, %42, s60\n" \
  "s_xor_b32 s75, %44, s60\n" \
  "s_bfe_u32 s56, s96, 0x30010\n" \
  "s_lshl_b32 s56, s56, 12\n" \
  "s_or_b32 s56, s56, 0x200000\n" \
  "s_mov_b32 s57, 1052672\n" \
  "s_lshl_b32 s61, %41, 2\n" \
  "s_lshr_b32 s51, s96, s61\n" \
  "s_and_b32 s51, s51, 7\n" \
  "s_lshl_b32 s51, s51, 12\n" \
  "s_xor_b32 s51, s51, 4210688\n" \
  "s_mov_b32 s71, 0\n" \
  "s_add_u32 s45, s51, 886784\n" \
  "s_mov_b32 s46, -1\n" \
  "v_mov_b64 v[48:49], -1\n" \
  "v_mov_b64 v[50:51], -1\n" \
  "v_and_b32 v194, 3, v250\n" \
  "v_lshlrev_b32 v194, 2, v194\n" \
  "v_lshrrev_b32 v194, v194, s96\n" \
  "v_and_b32 v194, 7, v194\n" \
  "v_lshlrev_b32 v194, 12, v194\n" \
  "v_and_b32 v250, -4, v250\n" \
  "v_xor_b32 v250, v250, v194\n" \
  "v_and_b32 v194, 3, v251\n" \
  "v_lshlrev_b32 v194, 2, v194\n" \
  "v_lshrrev_b32 v194, v194, s96\n" \
  "v_and_b32 v194, 7, v194\n" \
  "v_lshlrev_b32 v194, 12, v194\n" \
  "v_and_b32 v251, -4, v251\n" \
  "v_xor_b32 v251, v251, v194\n" \
  "v_and_b32 v194, 3, v252\n" \
  "v_lshlrev_b32 v194, 2, v194\n" \
  "v_lshrrev_b32 v194, v194, s96\n" \
  "v_and_b32 v194, 7, v194\n" \
  "v_lshlrev_b32 v194, 12, v194\n" \
  "v_and_b32 v252, -4, v252\n" \
  "v_xor_b32 v252, v252, v194\n" \
  "v_xor_b32 v185, s56, v250\n" \
  "buffer_store_dwordx4 v[48:51], v185, s[40:43], 0 offen sc1\n" \
  "v_xor_b32 v186, s56, v251\n" \
  "buffer_store_dwordx4 v[48:51], v186, s[40:43], 0 offen sc1\n" \
  "v_xor_b32 v187, s56, v252\n" \
  "buffer_store_dwordx2 v[48:49], v187, s[40:43], 0 offen sc1\n" \
  "s_xor_b32 s62, s56, s57\n" \
  "s_nop 1\n" \
  "v_xor_b32 v185, s62, v250\n" \
  "buffer_store_dwordx4 v[48:51], v185, s[40:43], 0 offen sc1\n" \
  "v_xor_b32 v186, s62, v251\n" \
  "buffer_store_dwordx4 v[48:51], v186, s[40:43], 0 offen sc1\n" \
  "v_xor_b32 v187, s62, v252\n" \
  "buffer_store_dwordx2 v[48:49], v187, s[40:43], 0 offen sc1\n" \
  "s_andn2_b32 s60, 2, s96\n" \
  "s_lshl_b32 s60, s60, 12\n" \
  "s_xor_b32 s60, s60, 8192\n" \
  "s_xor_b32 s58, %39, s60\n" \
  "s_mov_b32 s49, 0\n" \
  "s_add_u32 s47, %49, s60\n" \
  "s_add_u32 s97, s60, 8388608\n" \
  "s_xor_b32 s50, %40, s60\n" \
  "s_mov_b32 s48, 0\n" \
  "s_mov_b32 s54, 0\n" \
  "s_cmp_eq_u32 %36, 0\n" \
  "s_cselect_b64 s[88:89], -1, 0\n" \
  "s_mov_b32 s68, 0\n" \
  "s_mov_b32 s70, 1\n" \
  "s_mov_b32 s73, 0\n" \
  "s_mov_b32 s44, 0\n" \
  "s_mov_b32 s53, 0\n" \
  "s_memrealtime s[86:87]\n" \
  "s_memtime s[76:77]\n" \
  "s_waitcnt lgkmcnt(0)\n" \
  "s_mov_b32 s90, s76\n" \
  "s_mov_b32 s87, 340\n" \
  "s_mov_b32 s64, 0x3e0293ee\n" \
  "s_mov_b32 s65, 0xbe0293ee\n" \
  "v_mov_b64 v[60:61], 0\n" \
  "v_mov_b64 v[62:63], 0\n" \
  "v_mov_b64 v[64:65], 0\n" \
  "v_mov_b64 v[66:67], 0\n" \
  "v_mov_b64 v[68:69], 0\n" \
  "v_mov_b64 v[70:71], 0\n" \
  "v_mov_b64 v[72:73], 0\n" \
  "v_mov_b64 v[74:75], 0\n" \
  "v_mov_b64 v[76:77], 0\n" \
  "v_mov_b64 v[78:79], 0\n" \
  "v_mov_b64 v[80:81], 0\n" \
  "v_mov_b64 v[82:83], 0\n" \
  "v_mov_b64 v[84:85], 0\n" \
  "v_mov_b64 v[86:87], 0\n" \
  "v_mov_b32 v56, 0\n" \
  "v_mov_b32 v57, s55\n" \
  "v_min_u32 v58, 1, v243\n" \
  "v_lshlrev_b32 v58, 30, v58\n" \
  "v_mov_b64 v[90:91], 0\n" \
  "s_mov_b32 s67, 2130706432\n" \
  "s_waitcnt vmcnt(0) lgkmcnt(0)\n" \
  "s_barrier\n" \
  "s_branch .LV_p_%=\n" \
  "s_nop 7\n" \
  ".p2align 6\n" \
  ".fill (((20 - (.LV_p_%= - .LV_rot_%=)) & 63) >> 2), 4, 0xBF800000\n" \
  ".LV_rot_%=:\n" \
  "s_xor_b32 s51, s51, s56\n" \
  "s_xor_b32 s71, s71, s56\n" \
  "v_xor_b32 v250, s56, v250\n" \
  "v_xor_b32 v251, s56, v251\n" \
  "v_xor_b32 v252, s56, v252\n" \
  "s_mov_b32 s60, s56\n" \
  "s_mov_b32 s56, s57\n" \
  "s_mov_b32 s57, s60\n" \
  "s_add_u32 s59, s51, 819200\n" \
  "s_mov_b32 m0, 0\n" \
  "s_mov_b32 s70, 0\n" \
  "s_waitcnt lgkmcnt(0)\n" \
  ".LV_rt_%=:\n" \
  "s_barrier\n" \
  ".LV_p_%=:\n" \
  "ds_read_b128 v[92:95], v243\n" \
  "ds_read_b128 v[96:99], v243 offset:3072\n" \
  "ds_read_b128 v[100:103], v243 offset:1024\n" \
  "ds_read_b128 v[104:107], v243 offset:2048\n" \
  "s_waitcnt lgkmcnt(3)\n" \
  "v_smfmac_f32_16x16x64_bf16 v[60:63], v[92:95], a[0:7], v242\n" \
  "s_barrier\n" \
  "ds_read_b128 v[108:111], v243 offset:4096\n" \
  "v_smfmac_f32_16x16x64_bf16 v[64:67], v[92:95], a[72:79], v242\n" \
  "ds_read_b128 v[112:115], v243 offset:5120\n" \
  "s_waitcnt lgkmcnt(3)\n" \
  "v_smfmac_f32_16x16x64_bf16 v[60:63], v[100:103], a[8:15], v242\n" \
  "ds_read_b128 v[116:119], v243 offset:6144\n" \
  "s_waitcnt lgkmcnt(3)\n" \
  "v_smfmac_f32_16x16x64_bf16 v[60:63], v[104:107], a[16:23], v242\n" \
  "ds_read_b128 v[120:123], v243 offset:7168\n" \
  "v_smfmac_f32_16x16x64_bf16 v[60:63], v[96:99], a[24:31], v242\n" \
  "ds_read_b128 v[124:127], v243 offset:8192\n" \
  "s_waitcnt lgkmcnt(4)\n" \
  "v_smfmac_f32_16x16x64_bf16 v[60:63], v[108:111], a[32:39], v242\n" \
  "s_waitcnt lgkmcnt(3)\n" \
  "v_smfmac_f32_16x16x64_bf16 v[60:63], v[112:115], a[40:47], v242\n" \
  "s_waitcnt lgkmcnt(2)\n" \
  "v_smfmac_f32_16x16x64_bf16 v[60:63], v[116:119], a[48:55], v242\n" \
  "s_waitcnt lgkmcnt(1)\n" \
  "v_smfmac_f32_16x16x64_bf16 v[60:63], v[120:123], a[56:63], v242\n" \
  "s_waitcnt lgkmcnt(0)\n" \
  "v_smfmac_f32_16x16x64_bf16 v[60:63], v[124:127], a[64:71], v242\n" \
  "s_nop 2\n" \
  "v_smfmac_f32_16x16x64_bf16 v[64:67], v[100:103], a[80:87], v242\n" \
  "s_nop 2\n" \
  "v_smfmac_f32_16x16x64_bf16 v[64:67], v[104:107], a[88:95], v242\n" \
  "v_add_f32 v60, v60, v62\n" \
  "v_add_f32 v61, v61, v63\n" \
  "v_cvt_pk_bf16_f32 v88, v60, v61\n" \
  "v_cmp_u_f32 vcc, v60, v61\n" \
  "v_smfmac_f32_16x16x64_bf16 v[64:67], v[96:99], a[96:103], v242\n" \
  "s_cbranch_vccnz .LV_slow_%=\n" \
  ".LV_back_%=:\n" \
  "ds_write_b32 v244, v88\n" \
  "v_smfmac_f32_16x16x64_bf16 v[64:67], v[108:111], a[104:111], v242\n" \
  "v_smfmac_f32_16x16x64_bf16 v[64:67], v[112:115], a[112:119], v242\n" \
  "v_smfmac_f32_16x16x64_bf16 v[64:67], v[116:119], a[120:127], v242\n" \
  "v_smfmac_f32_16x16x64_bf16 v[64:67], v[120:123], a[128:135], v242\n" \
  "s_waitcnt lgkmcnt(0)\n" \
  "s_waitcnt vmcnt(0) \n" \
  "s_barrier\n" \
  "ds_read_b64_tr_b16 v[92:93], v245\n" \
  "ds_read_b64_tr_b16 v[94:95], v245 offset:512\n" \
  "v_smfmac_f32_16x16x64_bf16 v[64:67], v[124:127], a[136:143], v242\n" \
  "ds_read_b64_tr_b16 v[96:97], v245 offset:1024\n" \
  "ds_read_b64_tr_b16 v[98:99], v245 offset:1536\n" \
  "s_nop 5\n" \
  "v_add_f32 v64, v64, v66\n" \
  "v_add_f32 v65, v65, v67\n" \
  "v_cvt_pk_bf16_f32 v89, v64, v65\n" \
  "s_waitcnt lgkmcnt(2)\n" \
  "v_smfmac_f32_16x16x64_bf16 v[76:79], v[92:95], a[208:215], v248\n" \
  "ds_write_b32 v244, v89 offset:256\n" \
  "s_waitcnt lgkmcnt(1)\n" \
  "v_smfmac_f32_16x16x64_bf16 v[76:79], v[96:99], a[216:223], v248\n" \
  "v_smfmac_f32_16x16x64_bf16 v[68:71], v[92:95], a[144:151], v248\n" \
  "v_smfmac_f32_16x16x64_bf16 v[72:75], v[92:95], a[176:183], v248\n" \
  "buffer_store_dwordx2 v[56:57], v58, s[40:43], %43 offen sc1\n" \
  "v_smfmac_f32_16x16x64_bf16 v[68:71], v[96:99], a[152:159], v248\n" \
  "s_waitcnt lgkmcnt(0)\n" \
  "s_barrier\n" \
  "v_smfmac_f32_16x16x64_bf16 v[80:83], v[92:95], a[240:247], v248\n" \
  "ds_read_b64_tr_b16 v[102:103], v245 offset:768\n" \
  "ds_read_b64_tr_b16 v[100:101], v245 offset:256\n" \
  "v_smfmac_f32_16x16x64_bf16 v[84:87], v[92:95], v[16:23], v248\n" \
  "ds_read_b64_tr_b16 v[94:95], v245 offset:1792\n" \
  "ds_read_b64_tr_b16 v[92:93], v245 offset:1280\n" \
  "v_smfmac_f32_16x16x64_bf16 v[80:83], v[96:99], a[248:255], v248\n" \
  "v_smfmac_f32_16x16x64_bf16 v[72:75], v[96:99], a[184:191], v248\n" \
  "s_waitcnt lgkmcnt(2)\n" \
  "v_smfmac_f32_16x16x64_bf16 v[68:71], v[100:103], a[160:167], v248\n" \
  "s_waitcnt lgkmcnt(0)\n" \
  "v_smfmac_f32_16x16x64_bf16 v[68:71], v[92:95], a[168:175], v248\n" \
  "s_nop 2\n" \
  "v_smfmac_f32_16x16x64_bf16 v[72:75], v[100:103], a[192:199], v248\n" \
  "s_nop 2\n" \
  "v_smfmac_f32_16x16x64_bf16 v[72:75], v[92:95], a[200:207], v248\n" \
  "s_nop 1\n" \
  "v_cvt_pk_bf16_f32 v68, v68, v69\n" \
  "v_smfmac_f32_16x16x64_bf16 v[84:87], v[96:99], v[24:31], v248\n" \
  "s_nop 0\n" \
  "v_cvt_pk_bf16_f32 v69, v70, v71\n" \
  "v_smfmac_f32_16x16x64_bf16 v[76:79], v[100:103], a[224:231], v248\n" \
  "v_smfmac_f32_16x16x64_bf16 v[80:83], v[100:103], v[0:7], v248\n" \
  "v_cvt_pk_bf16_f32 v71, v74, v75\n" \
  "v_smfmac_f32_16x16x64_bf16 v[84:87], v[100:103], v[32:39], v248\n" \
  "v_cvt_pk_bf16_f32 v70, v72, v73\n" \
  "v_smfmac_f32_16x16x64_bf16 v[76:79], v[92:95], a[232:239], v248\n" \
  "s_nop 1\n" \
  "v_permlane16_swap_b32 v69, v71\n" \
  "v_smfmac_f32_16x16x64_bf16 v[80:83], v[92:95], v[8:15], v248\n" \
  "s_nop 0\n" \
  "s_nop 0\n" \
  "v_permlane16_swap_b32 v68, v70\n" \
  "v_smfmac_f32_16x16x64_bf16 v[84:87], v[92:95], v[40:47], v248\n" \
  "buffer_store_dwordx4 v[68:71], v250, s[40:43], 0 offen sc1\n" \
  "v_cvt_pk_bf16_f32 v76, v76, v77\n" \
  "v_cvt_pk_bf16_f32 v77, v78, v79\n" \
  "s_nop 0\n" \
  "v_cvt_pk_bf16_f32 v78, v80, v81\n" \
  "v_cvt_pk_bf16_f32 v79, v82, v83\n" \
  "s_nop 0\n" \
  "v_permlane16_swap_b32 v76, v78\n" \
  "v_cvt_pk_bf16_f32 v84, v84, v85\n" \
  "v_cvt_pk_bf16_f32 v85, v86, v87\n" \
  "v_permlane16_swap_b32 v77, v79\n" \
  "buffer_store_dwordx4 v[76:79], v251, s[40:43], 0 offen sc1\n" \
  "buffer_store_dwordx2 v[84:85], v252, s[40:43], 0 offen sc1\n" \
  ".LV_loop_%=:\n" \
  "s_cmp_ge_u32 s48, 58\n" \
  "s_cbranch_scc1 .LV_cp_%=\n" \
  ".LV_cp_b_%=:\n" \
  "v_mov_b64 v[60:61], 0\n" \
  "v_mov_b64 v[62:63], 0\n" \
  "v_mov_b64 v[64:65], 0\n" \
  "v_mov_b64 v[66:67], 0\n" \
  "v_mov_b64 v[68:69], 0\n" \
  "v_mov_b64 v[70:71], 0\n" \
  "v_mov_b64 v[72:73], 0\n" \
  "v_mov_b64 v[74:75], 0\n" \
  "v_mov_b64 v[76:77], 0\n" \
  "v_mov_b64 v[78:79], 0\n" \
  "v_mov_b64 v[80:81], 0\n" \
  "v_mov_b64 v[82:83], 0\n" \
  "v_mov_b64 v[84:85], 0\n" \
  "v_mov_b64 v[86:87], 0\n" \
  "ds_read_b32 v92, v246\n" \
  "ds_read_b32 v93, v246 offset:256\n" \
  "v_mov_b64 v[94:95], 0\n" \
  "s_sub_u32 s60, s48, 1\n" \
  "s_cselect_b32 s60, 8192, s60\n" \
  "s_lshl_b32 s60, s60, 14\n" \
  "s_xor_b32 s60, s60, s50\n" \
  "buffer_store_dwordx4 v[88:91], v247, s[40:43], s60 offen sc1\n" \
  "s_waitcnt lgkmcnt(0)\n" \
  "buffer_store_dwordx4 v[92:95], v249, s[40:43], s60 offen sc1\n" \
  "s_add_u32 s48, s48, 1\n" \
  "v_mov_b32 v56, s48\n" \
  "s_waitcnt lgkmcnt(0)\n" \
  "s_cmp_gt_u32 s48, 57\n" \
  "s_cbranch_scc1 .LV_ef_%=\n" \
  ".LV_ef_b_%=:\n" \
  "s_branch .LV_rot_%=\n" \
  "s_nop 7\n" \
  ".LV_slow_%=:\n" \
  "s_bitcmp1_b32 s70, 0\n" \
  "s_cbranch_scc1 .LV_back_%=\n" \
  "s_waitcnt lgkmcnt(0)\n" \
  "s_cmp_lt_u32 s70, 10\n" \
  "s_nop 3\n" \
  "s_cbranch_scc1 .LV_nol_%=\n" \
  "v_max_u32 v217, v92, v93\n" \
  "v_max3_u32 v217, v217, v94, v95\n" \
  "v_max3_u32 v217, v217, v100, v101\n" \
  "v_max3_u32 v217, v217, v102, v103\n" \
  "v_max3_u32 v217, v217, v104, v105\n" \
  "v_max3_u32 v217, v217, v106, v107\n" \
  "v_max3_u32 v217, v217, v96, v97\n" \
  "v_max3_u32 v217, v217, v98, v99\n" \
  "v_max3_u32 v217, v217, v108, v109\n" \
  "v_max3_u32 v217, v217, v110, v111\n" \
  "v_max3_u32 v217, v217, v112, v113\n" \
  "v_max3_u32 v217, v217, v114, v115\n" \
  "v_max3_u32 v217, v217, v116, v117\n" \
  "v_max3_u32 v217, v217, v118, v119\n" \
  "v_max3_u32 v217, v217, v120, v121\n" \
  "v_max3_u32 v217, v217, v122, v123\n" \
  "v_max3_u32 v217, v217, v124, v125\n" \
  "v_max3_u32 v217, v217, v126, v127\n" \
  "v_cmp_eq_u32 vcc, -1, v217\n" \
  "s_cbranch_vccz .LV_back_%=\n" \
  ".LV_nol_%=:\n" \
  "s_add_u32 s70, s70, 2\n" \
  "s_cmp_gt_u32 s70, 65536\n" \
  "s_cbranch_scc1 .LV_dset_%=\n" \
  "s_barrier\n" \
  "v_mov_b64 v[60:61], 0\n" \
  "v_mov_b64 v[62:63], 0\n" \
  "v_mov_b64 v[64:65], 0\n" \
  "v_mov_b64 v[66:67], 0\n" \
  "v_mov_b64 v[68:69], 0\n" \
  "v_mov_b64 v[70:71], 0\n" \
  "v_mov_b64 v[72:73], 0\n" \
  "v_mov_b64 v[74:75], 0\n" \
  "v_mov_b64 v[76:77], 0\n" \
  "v_mov_b64 v[78:79], 0\n" \
  "v_mov_b64 v[80:81], 0\n" \
  "v_mov_b64 v[82:83], 0\n" \
  "v_mov_b64 v[84:85], 0\n" \
  "v_mov_b64 v[86:87], 0\n" \
  "s_branch .LV_rt_%=\n" \
  ".LV_dset_%=:\n" \
  "v_mov_b32 v112, 1\n" \
  "v_mov_b32 v113, s55\n" \
  "buffer_store_dwordx2 v[112:113], off, s[40:43], %46 offset:128 sc1\n" \
  "s_mov_b32 s70, 1\n" \
  "v_mov_b64 v[60:61], 0\n" \
  "v_mov_b64 v[62:63], 0\n" \
  "v_mov_b64 v[64:65], 0\n" \
  "v_mov_b64 v[66:67], 0\n" \
  "v_mov_b64 v[68:69], 0\n" \
  "v_mov_b64 v[70:71], 0\n" \
  "v_mov_b64 v[72:73], 0\n" \
  "v_mov_b64 v[74:75], 0\n" \
  "v_mov_b64 v[76:77], 0\n" \
  "v_mov_b64 v[78:79], 0\n" \
  "v_mov_b64 v[80:81], 0\n" \
  "v_mov_b64 v[82:83], 0\n" \
  "v_mov_b64 v[84:85], 0\n" \
  "v_mov_b64 v[86:87], 0\n" \
  "s_waitcnt vmcnt(0) lgkmcnt(0)\n" \
  "s_barrier\n" \
  "s_branch .LV_p_%=\n" \
  ".LV_cp_%=:\n" \
  "s_sub_u32 s60, s48, 58\n" \
  "s_lshl_b32 s60, s60, 20\n" \
  "s_add_u32 s60, s60, s47\n" \
  "s_lshr_b32 s62, s60, 8\n" \
  "s_and_b32 s62, s62, 8192\n" \
  "s_xor_b32 s62, s62, s60\n" \
  "buffer_store_dwordx4 v[68:71], v253, s[40:43], s62 offen sc1\n" \
  "buffer_store_dwordx4 v[76:79], v254, s[40:43], s62 offen sc1\n" \
  "buffer_store_dwordx2 v[84:85], v255, s[40:43], s62 offen sc1\n" \
  "s_nop 1\n" \
  "s_branch .LV_cp_b_%=\n" \
  ".LV_ef_%=:\n" \
  "s_cmp_eq_u32 s48, 64\n" \
  "s_cbranch_scc1 .LV_end_%=\n" \
  "s_waitcnt vmcnt(0)\n" \
  "s_barrier\n" \
  "buffer_store_dwordx2 v[56:57], v58, s[40:43], %43 offen sc1\n" \
  "s_branch .LV_ef_b_%=\n" \
  ".LV_end_%=:\n" \
  "s_waitcnt vmcnt(0)\n" \
  "s_waitcnt vmcnt(0)\n" \
  "s_nop 7\n" \
  "s_endpgm\n"
#define V_ASM_VOPS "+{v242}"(ci.sidx), "+{v243}"(ci.l16), "+{v244}"(ci.viw), "+{v245}"(ci.fva), "+{v246}"(ci.pk3), "+{v247}"(ci.fvs), "+{v248}"(ci.sid2), "+{v249}"(ci.uo), "+{v250}"(ci.po0), "+{v251}"(ci.po1), "+{v252}"(ci.po2), "+{v253}"(ci.co0), "+{v254}"(ci.co1), "+{v255}"(ci.co2), "+{v200}"(ci.vq0), "+{v201}"(ci.vq1), "+{v202}"(ci.vq2), "+{v203}"(ci.vq3), "+{v204}"(ci.vo0), "+{v205}"(ci.vo1), "+{v206}"(ci.vo2), "+{v207}"(ci.vo3), "+{v208}"(ci.v50), "+{v209}"(ci.v51), "+{v210}"(ci.v52), "+{v211}"(ci.v53), "+{v212}"(ci.tro0), "+{v213}"(ci.tro1), "+{v214}"(ci.m0o), "+{v215}"(ci.tro2), "+{v216}"(ci.tro3), "+{v217}"(ci.tro4)
#define V_ASM_SOPS "s"(ci.ws), "s"(ci.Wa), "s"(ci.Wb), "s"(ci.mem0), "s"(ci.w), "s"(ci.o_a), "s"(ci.o_b), "s"(ci.o_nx), "s"(ci.o_exp), "s"(ci.lab), "s"(ci.f_p), "s"(ci.f_m), "s"(ci.f_dur), "s"(ci.f_go), "s"(ci.o_ctl), "s"(ci.o_dbg), "s"(ci.dret), "s"(ci.o_qx), "s"(ci.f_s), "s"(ci.f_r)
#define V_ASM_CLOB "v0", "v1", "v2", "v3", "v4", "v5", "v6", "v7", "v8", "v9", "v10", "v11", "v12", "v13", "v14", "v15", "v16", "v17", "v18", "v19", "v20", "v21", "v22", "v23", "v24", "v25", "v26", "v27", "v28", "v29", "v30", "v31", "v32", "v33", "v34", "v35", "v36", "v37", "v38", "v39", "v40", "v41", "v42", "v43", "v44", "v45", "v46", "v47", "v48", "v49", "v50", "v51", "v52", "v53", "v54", "v55", "v56", "v57", "v58", "v59", "v60", "v61", "v62", "v63", "v64", "v65", "v66", "v67", "v68", "v69", "v70", "v71", "v72", "v73", "v74", "v75", "v76", "v77", "v78", "v79", "v80", "v81", "v82", "v83", "v84", "v85", "v86", "v87", "v88", "v89", "v90", "v91", "v92", "v93", "v94", "v95", "v96", "v97", "v98", "v99", "v100", "v101", "v102", "v103", "v104", "v105", "v106", "v107", "v108", "v109", "v110", "v111", "v112", "v113", "v114", "v115", "v116", "v117", "v118", "v119", "v120", "v121", "v122", "v123", "v124", "v125", "v126", "v127", "v128", "v129", "v130", "v131", "v132", "v133", "v134", "v135", "v136", "v137", "v138", "v139", "v140", "v141", "v142", "v143", "v144", "v145", "v146", "v147", "v148", "v149", "v150", "v151", "v152", "v153", "v154", "v155", "v156", "v157", "v158", "v159", "v160", "v161", "v162", "v163", "v164", "v165", "v166", "v167", "v168", "v169", "v170", "v171", "v172", "v173", "v174", "v175", "v176", "v177", "v178", "v179", "v180", "v181", "v182", "v183", "v184", "v185", "v186", "v187", "v188", "v189", "v190", "v191", "v192", "v193", "v194", "v195", "v196", "v197", "v198", "v199", "a0", "a1", "a2", "a3", "a4", "a5", "a6", "a7", "a8", "a9", "a10", "a11", "a12", "a13", "a14", "a15", "a16", "a17", "a18", "a19", "a20", "a21", "a22", "a23", "a24", "a25", "a26", "a27", "a28", "a29", "a30", "a31", "a32", "a33", "a34", "a35", "a36", "a37", "a38", "a39", "a40", "a41", "a42", "a43", "a44", "a45", "a46", "a47", "a48", "a49", "a50", "a51", "a52", "a53", "a54", "a55", "a56", "a57", "a58", "a59", "a60", "a61", "a62", "a63", "a64", "a65", "a66", "a67", "a68", "a69", "a70", "a71", "a72", "a73", "a74", "a75", "a76", "a77", "a78", "a79", "a80", "a81", "a82", "a83", "a84", "a85", "a86", "a87", "a88", "a89", "a90", "a91", "a92", "a93", "a94", "a95", "a96", "a97", "a98", "a99", "a100", "a101", "a102", "a103", "a104", "a105", "a106", "a107", "a108", "a109", "a110", "a111", "a112", "a113", "a114", "a115", "a116", "a117", "a118", "a119", "a120", "a121", "a122", "a123", "a124", "a125", "a126", "a127", "a128", "a129", "a130", "a131", "a132", "a133", "a134", "a135", "a136", "a137", "a138", "a139", "a140", "a141", "a142", "a143", "a144", "a145", "a146", "a147", "a148", "a149", "a150", "a151", "a152", "a153", "a154", "a155", "a156", "a157", "a158", "a159", "a160", "a161", "a162", "a163", "a164", "a165", "a166", "a167", "a168", "a169", "a170", "a171", "a172", "a173", "a174", "a175", "a176", "a177", "a178", "a179", "a180", "a181", "a182", "a183", "a184", "a185", "a186", "a187", "a188", "a189", "a190", "a191", "a192", "a193", "a194", "a195", "a196", "a197", "a198", "a199", "a200", "a201", "a202", "a203", "a204", "a205", "a206", "a207", "a208", "a209", "a210", "a211", "a212", "a213", "a214", "a215", "a216", "a217", "a218", "a219", "a220", "a221", "a222", "a223", "a224", "a225", "a226", "a227", "a228", "a229", "a230", "a231", "a232", "a233", "a234", "a235", "a236", "a237", "a238", "a239", "a240", "a241", "a242", "a243", "a244", "a245", "a246", "a247", "a248", "a249", "a250", "a251", "a252", "a253", "a254", "a255", "s40", "s41", "s42", "s43", "s44", "s45", "s46", "s47", "s48", "s49", "s50", "s51", "s52", "s53", "s54", "s55", "s56", "s57", "s58", "s59", "s60", "s61", "s62", "s63", "s64", "s65", "s66", "s67", "s68", "s69", "s70", "s71", "s72", "s73", "s74", "s75", "s76", "s77", "s78", "s79", "s80", "s81", "s82", "s83", "s84", "s85", "s86", "s87", "s88", "s89", "s90", "s91", "s92", "s93", "s94", "s95", "s96", "s97", "s98", "s99", "vcc", "scc", "memory"
#define W_ASM_TEXT \
  "s_cmp_gt_u32 %36, 0\n" \
  "s_cbranch_scc1 .LW_other_%=\n" \
  "s_mov_b64 s[40:41], %32\n" \
  "s_and_b32 s41, s41, 0xffff\n" \
  "s_mov_b32 s42, 255852544\n" \
  "s_mov_b32 s43, 0x00020000\n" \
  "s_mov_b32 s46, 4718592\n" \
  "s_mov_b32 s47, 0x00020000\n" \
  "s_lshl_b32 s72, %36, 11\n" \
  "buffer_load_dword v196, off, s[40:43], %46 sc1\n" \
  "s_mov_b64 s[44:45], %34\n" \
  "s_and_b32 s45, s45, 0xffff\n" \
  "s_add_u32 s60, %38, 0\n" \
  "s_lshl_b32 s61, s72, 1\n" \
  "s_add_u32 m0, s61, 16384\n" \
  "s_nop 0\n" \
  "buffer_load_dwordx4 v204, s[44:47], s60 offen lds\n" \
  "buffer_load_dwordx4 v205, s[44:47], s60 offen offset:1024 lds\n" \
  "buffer_load_dwordx4 v206, s[44:47], s60 offen offset:2048 lds\n" \
  "buffer_load_dwordx4 v207, s[44:47], s60 offen offset:3072 lds\n" \
  "s_add_u32 s60, %38, 18432\n" \
  "s_lshl_b32 s61, s72, 1\n" \
  "s_add_u32 m0, s61, 32768\n" \
  "s_nop 0\n" \
  "buffer_load_dwordx4 v204, s[44:47], s60 offen lds\n" \
  "buffer_load_dwordx4 v205, s[44:47], s60 offen offset:1024 lds\n" \
  "buffer_load_dwordx4 v206, s[44:47], s60 offen offset:2048 lds\n" \
  "buffer_load_dwordx4 v207, s[44:47], s60 offen offset:3072 lds\n" \
  "s_add_u32 s60, %38, 256\n" \
  "s_lshl_b32 s61, s72, 1\n" \
  "s_add_u32 m0, s61, 49152\n" \
  "s_nop 0\n" \
  "buffer_load_dwordx4 v204, s[44:47], s60 offen lds\n" \
  "buffer_load_dwordx4 v205, s[44:47], s60 offen offset:1024 lds\n" \
  "buffer_load_dwordx4 v206, s[44:47], s60 offen offset:2048 lds\n" \
  "buffer_load_dwordx4 v207, s[44:47], s60 offen offset:3072 lds\n" \
  "s_add_u32 s60, %38, 18688\n" \
  "s_lshl_b32 s61, s72, 1\n" \
  "s_add_u32 m0, s61, 65536\n" \
  "s_nop 0\n" \
  "buffer_load_dwordx4 v204, s[44:47], s60 offen lds\n" \
  "buffer_load_dwordx4 v205, s[44:47], s60 offen offset:1024 lds\n" \
  "buffer_load_dwordx4 v206, s[44:47], s60 offen offset:2048 lds\n" \
  "buffer_load_dwordx4 v207, s[44:47], s60 offen offset:3072 lds\n" \
  "s_add_u32 s60, %38, 512\n" \
  "s_lshl_b32 s61, s72, 1\n" \
  "s_add_u32 m0, s61, 81920\n" \
  "s_nop 0\n" \
  "buffer_load_dwordx4 v204, s[44:47], s60 offen lds\n" \
  "buffer_load_dwordx4 v205, s[44:47], s60 offen offset:1024 lds\n" \
  "buffer_load_dwordx4 v206, s[44:47], s60 offen offset:2048 lds\n" \
  "buffer_load_dwordx4 v207, s[44:47], s60 offen offset:3072 lds\n" \
  "s_add_u32 s60, %38, 18944\n" \
  "s_lshl_b32 s61, s72, 1\n" \
  "s_add_u32 m0, s61, 98304\n" \
  "s_nop 0\n" \
  "buffer_load_dwordx4 v204, s[44:47], s60 offen lds\n" \
  "buffer_load_dwordx4 v205, s[44:47], s60 offen offset:1024 lds\n" \
  "buffer_load_dwordx4 v206, s[44:47], s60 offen offset:2048 lds\n" \
  "buffer_load_dwordx4 v207, s[44:47], s60 offen offset:3072 lds\n" \
  "s_add_u32 s60, %38, 768\n" \
  "s_lshl_b32 s61, s72, 1\n" \
  "s_add_u32 m0, s61, 114688\n" \
  "s_nop 0\n" \
  "buffer_load_dwordx4 v204, s[44:47], s60 offen lds\n" \
  "buffer_load_dwordx4 v205, s[44:47], s60 offen offset:1024 lds\n" \
  "buffer_load_dwordx4 v206, s[44:47], s60 offen offset:2048 lds\n" \
  "buffer_load_dwordx4 v207, s[44:47], s60 offen offset:3072 lds\n" \
  "s_add_u32 s60, %38, 19200\n" \
  "s_lshl_b32 s61, s72, 1\n" \
  "s_add_u32 m0, s61, 131072\n" \
  "s_nop 0\n" \
  "buffer_load_dwordx4 v204, s[44:47], s60 offen lds\n" \
  "buffer_load_dwordx4 v205, s[44:47], s60 offen offset:1024 lds\n" \
  "buffer_load_dwordx4 v206, s[44:47], s60 offen offset:2048 lds\n" \
  "buffer_load_dwordx4 v207, s[44:47], s60 offen offset:3072 lds\n" \
  "s_mov_b32 s60, %38\n" \
  "s_lshl_b32 s61, s72, 1\n" \
  "s_add_u32 m0, s61, 147456\n" \
  "s_nop 0\n" \
  "buffer_load_dwordx4 v208, s[44:47], s60 offen lds\n" \
  "buffer_load_dwordx4 v209, s[44:47], s60 offen offset:1024 lds\n" \
  "buffer_load_dwordx4 v210, s[44:47], s60 offen offset:2048 lds\n" \
  "buffer_load_dwordx4 v211, s[44:47], s60 offen offset:3072 lds\n" \
  "s_mov_b64 s[44:45], %35\n" \
  "s_and_b32 s45, s45, 0xffff\n" \
  "s_lshr_b32 s60, s72, 3\n" \
  "s_mov_b32 m0, s72\n" \
  "s_nop 0\n" \
  "buffer_load_dwordx4 v214, s[44:47], s60 offen lds\n" \
  "s_add_u32 s60, s60, 128\n" \
  "s_add_u32 m0, s72, 1024\n" \
  "s_nop 0\n" \
  "buffer_load_dwordx4 v214, s[44:47], s60 offen lds\n" \
  "s_movk_i32 s60, 1024\n" \
  "s_movk_i32 m0, 8192\n" \
  "s_nop 0\n" \
  "buffer_load_dwordx4 v214, s[44:47], s60 offen lds\n" \
  "s_waitcnt vmcnt(27)\n" \
  "s_barrier\n" \
  "v_add_u32 v194, 16384, v215\n" \
  "v_xor_b32 v195, 2176, v194\n" \
  "ds_read_b64_tr_b16 a[144:145], v194\n" \
  "ds_read_b64_tr_b16 a[146:147], v194 offset:512\n" \
  "ds_read_b64_tr_b16 a[148:149], v195\n" \
  "ds_read_b64_tr_b16 a[150:151], v195 offset:512\n" \
  "ds_read_b64_tr_b16 a[152:153], v194 offset:8192\n" \
  "ds_read_b64_tr_b16 a[154:155], v194 offset:8704\n" \
  "ds_read_b64_tr_b16 a[156:157], v195 offset:8192\n" \
  "ds_read_b64_tr_b16 a[158:159], v195 offset:8704\n" \
  "v_add_u32 v194, 32768, v215\n" \
  "v_xor_b32 v195, 2176, v194\n" \
  "ds_read_b64_tr_b16 a[160:161], v194\n" \
  "ds_read_b64_tr_b16 a[162:163], v194 offset:512\n" \
  "ds_read_b64_tr_b16 a[164:165], v195\n" \
  "ds_read_b64_tr_b16 a[166:167], v195 offset:512\n" \
  "ds_read_b64_tr_b16 a[168:169], v194 offset:8192\n" \
  "ds_read_b64_tr_b16 a[170:171], v194 offset:8704\n" \
  "ds_read_b64_tr_b16 a[172:173], v195 offset:8192\n" \
  "ds_read_b64_tr_b16 a[174:175], v195 offset:8704\n" \
  "v_add_u32 v194, 49152, v215\n" \
  "v_xor_b32 v195, 2176, v194\n" \
  "ds_read_b64_tr_b16 a[176:177], v194\n" \
  "ds_read_b64_tr_b16 a[178:179], v194 offset:512\n" \
  "ds_read_b64_tr_b16 a[180:181], v195\n" \
  "ds_read_b64_tr_b16 a[182:183], v195 offset:512\n" \
  "ds_read_b64_tr_b16 a[184:185], v194 offset:8192\n" \
  "ds_read_b64_tr_b16 a[186:187], v194 offset:8704\n" \
  "ds_read_b64_tr_b16 a[188:189], v195 offset:8192\n" \
  "ds_read_b64_tr_b16 a[190:191], v195 offset:8704\n" \
  "s_waitcnt lgkmcnt(0)\n" \
  "s_barrier\n" \
  "s_mov_b64 s[44:45], %33\n" \
  "s_and_b32 s45, s45, 0xffff\n" \
  "s_add_u32 s60, %37, 0\n" \
  "s_lshl_b32 s61, s72, 1\n" \
  "s_add_u32 m0, s61, 16384\n" \
  "s_nop 0\n" \
  "buffer_load_dwordx4 v200, s[44:47], s60 offen lds\n" \
  "buffer_load_dwordx4 v201, s[44:47], s60 offen offset:1024 lds\n" \
  "buffer_load_dwordx4 v202, s[44:47], s60 offen offset:2048 lds\n" \
  "buffer_load_dwordx4 v203, s[44:47], s60 offen offset:3072 lds\n" \
  "s_add_u32 s60, %37, 524288\n" \
  "s_lshl_b32 s61, s72, 1\n" \
  "s_add_u32 m0, s61, 32768\n" \
  "s_nop 0\n" \
  "buffer_load_dwordx4 v200, s[44:47], s60 offen lds\n" \
  "buffer_load_dwordx4 v201, s[44:47], s60 offen offset:1024 lds\n" \
  "buffer_load_dwordx4 v202, s[44:47], s60 offen offset:2048 lds\n" \
  "buffer_load_dwordx4 v203, s[44:47], s60 offen offset:3072 lds\n" \
  "s_add_u32 s60, %37, 1048576\n" \
  "s_lshl_b32 s61, s72, 1\n" \
  "s_add_u32 m0, s61, 49152\n" \
  "s_nop 0\n" \
  "buffer_load_dwordx4 v200, s[44:47], s60 offen lds\n" \
  "buffer_load_dwordx4 v201, s[44:47], s60 offen offset:1024 lds\n" \
  "buffer_load_dwordx4 v202, s[44:47], s60 offen offset:2048 lds\n" \
  "buffer_load_dwordx4 v203, s[44:47], s60 offen offset:3072 lds\n" \
  "v_readfirstlane_b32 s55, v196\n" \
  "s_add_u32 s55, s55, 1\n" \
  "s_waitcnt vmcnt(27)\n" \
  "s_barrier\n" \
  "v_add_u32 v194, 65536, v215\n" \
  "v_xor_b32 v195, 2176, v194\n" \
  "ds_read_b64_tr_b16 a[192:193], v194\n" \
  "ds_read_b64_tr_b16 a[194:195], v194 offset:512\n" \
  "ds_read_b64_tr_b16 a[196:197], v195\n" \
  "ds_read_b64_tr_b16 a[198:199], v195 offset:512\n" \
  "ds_read_b64_tr_b16 a[200:201], v194 offset:8192\n" \
  "ds_read_b64_tr_b16 a[202:203], v194 offset:8704\n" \
  "ds_read_b64_tr_b16 a[204:205], v195 offset:8192\n" \
  "ds_read_b64_tr_b16 a[206:207], v195 offset:8704\n" \
  "v_add_u32 v194, 81920, v215\n" \
  "v_xor_b32 v195, 2176, v194\n" \
  "ds_read_b64_tr_b16 a[208:209], v194\n" \
  "ds_read_b64_tr_b16 a[210:211], v194 offset:512\n" \
  "ds_read_b64_tr_b16 a[212:213], v195\n" \
  "ds_read_b64_tr_b16 a[214:215], v195 offset:512\n" \
  "ds_read_b64_tr_b16 a[216:217], v194 offset:8192\n" \
  "ds_read_b64_tr_b16 a[218:219], v194 offset:8704\n" \
  "ds_read_b64_tr_b16 a[220:221], v195 offset:8192\n" \
  "ds_read_b64_tr_b16 a[222:223], v195 offset:8704\n" \
  "v_add_u32 v194, 98304, v215\n" \
  "v_xor_b32 v195, 2176, v194\n" \
  "ds_read_b64_tr_b16 a[224:225], v194\n" \
  "ds_read_b64_tr_b16 a[226:227], v194 offset:512\n" \
  "ds_read_b64_tr_b16 a[228:229], v195\n" \
  "ds_read_b64_tr_b16 a[230:231], v195 offset:512\n" \
  "ds_read_b64_tr_b16 a[232:233], v194 offset:8192\n" \
  "ds_read_b64_tr_b16 a[234:235], v194 offset:8704\n" \
  "ds_read_b64_tr_b16 a[236:237], v195 offset:8192\n" \
  "ds_read_b64_tr_b16 a[238:239], v195 offset:8704\n" \
  "s_waitcnt lgkmcnt(0)\n" \
  "s_barrier\n" \
  "s_add_u32 s60, %37, 1572864\n" \
  "s_lshl_b32 s61, s72, 1\n" \
  "s_add_u32 m0, s61, 65536\n" \
  "s_nop 0\n" \
  "buffer_load_dwordx4 v200, s[44:47], s60 offen lds\n" \
  "buffer_load_dwordx4 v201, s[44:47], s60 offen offset:1024 lds\n" \
  "buffer_load_dwordx4 v202, s[44:47], s60 offen offset:2048 lds\n" \
  "buffer_load_dwordx4 v203, s[44:47], s60 offen offset:3072 lds\n" \
  "s_add_u32 s60, %37, 2097152\n" \
  "s_lshl_b32 s61, s72, 1\n" \
  "s_add_u32 m0, s61, 81920\n" \
  "s_nop 0\n" \
  "buffer_load_dwordx4 v200, s[44:47], s60 offen lds\n" \
  "buffer_load_dwordx4 v201, s[44:47], s60 offen offset:1024 lds\n" \
  "buffer_load_dwordx4 v202, s[44:47], s60 offen offset:2048 lds\n" \
  "buffer_load_dwordx4 v203, s[44:47], s60 offen offset:3072 lds\n" \
  "s_add_u32 s60, %37, 2621440\n" \
  "s_lshl_b32 s61, s72, 1\n" \
  "s_add_u32 m0, s61, 98304\n" \
  "s_nop 0\n" \
  "buffer_load_dwordx4 v200, s[44:47], s60 offen lds\n" \
  "buffer_load_dwordx4 v201, s[44:47], s60 offen offset:1024 lds\n" \
  "buffer_load_dwordx4 v202, s[44:47], s60 offen offset:2048 lds\n" \
  "buffer_load_dwordx4 v203, s[44:47], s60 offen offset:3072 lds\n" \
  "s_waitcnt vmcnt(27)\n" \
  "s_barrier\n" \
  "s_waitcnt lgkmcnt(0)\n" \
  "s_barrier\n" \
  "s_add_u32 s60, %37, 3145728\n" \
  "s_lshl_b32 s61, s72, 1\n" \
  "s_add_u32 m0, s61, 114688\n" \
  "s_nop 0\n" \
  "buffer_load_dwordx4 v200, s[44:47], s60 offen lds\n" \
  "buffer_load_dwordx4 v201, s[44:47], s60 offen offset:1024 lds\n" \
  "buffer_load_dwordx4 v202, s[44:47], s60 offen offset:2048 lds\n" \
  "buffer_load_dwordx4 v203, s[44:47], s60 offen offset:3072 lds\n" \
  "s_add_u32 s60, %37, 3670016\n" \
  "s_lshl_b32 s61, s72, 1\n" \
  "s_add_u32 m0, s61, 131072\n" \
  "s_nop 0\n" \
  "buffer_load_dwordx4 v200, s[44:47], s60 offen lds\n" \
  "buffer_load_dwordx4 v201, s[44:47], s60 offen offset:1024 lds\n" \
  "buffer_load_dwordx4 v202, s[44:47], s60 offen offset:2048 lds\n" \
  "buffer_load_dwordx4 v203, s[44:47], s60 offen offset:3072 lds\n" \
  "s_add_u32 s60, %37, 4194304\n" \
  "s_lshl_b32 s61, s72, 1\n" \
  "s_add_u32 m0, s61, 147456\n" \
  "s_nop 0\n" \
  "buffer_load_dwordx4 v200, s[44:47], s60 offen lds\n" \
  "buffer_load_dwordx4 v201, s[44:47], s60 offen offset:1024 lds\n" \
  "buffer_load_dwordx4 v202, s[44:47], s60 offen offset:2048 lds\n" \
  "buffer_load_dwordx4 v203, s[44:47], s60 offen offset:3072 lds\n" \
  "buffer_load_dwordx2 v[198:199], off, s[40:43], %45 sc1\n" \
  "s_waitcnt vmcnt(25)\n" \
  "s_barrier\n" \
  "v_add_u32 v194, 16384, v212\n" \
  "ds_read_b64_tr_b16 a[0:1], v194\n" \
  "ds_read_b64_tr_b16 a[2:3], v194 offset:512\n" \
  "ds_read_b64_tr_b16 a[4:5], v194 offset:8192\n" \
  "ds_read_b64_tr_b16 a[6:7], v194 offset:8704\n" \
  "v_add_u32 v195, 16384, v213\n" \
  "ds_read_b64_tr_b16 a[72:73], v195\n" \
  "ds_read_b64_tr_b16 a[74:75], v195 offset:512\n" \
  "ds_read_b64_tr_b16 a[76:77], v195 offset:8192\n" \
  "ds_read_b64_tr_b16 a[78:79], v195 offset:8704\n" \
  "v_add_u32 v194, 32768, v212\n" \
  "ds_read_b64_tr_b16 a[8:9], v194\n" \
  "ds_read_b64_tr_b16 a[10:11], v194 offset:512\n" \
  "ds_read_b64_tr_b16 a[12:13], v194 offset:8192\n" \
  "ds_read_b64_tr_b16 a[14:15], v194 offset:8704\n" \
  "v_add_u32 v195, 32768, v213\n" \
  "ds_read_b64_tr_b16 a[80:81], v195\n" \
  "ds_read_b64_tr_b16 a[82:83], v195 offset:512\n" \
  "ds_read_b64_tr_b16 a[84:85], v195 offset:8192\n" \
  "ds_read_b64_tr_b16 a[86:87], v195 offset:8704\n" \
  "v_add_u32 v194, 49152, v212\n" \
  "ds_read_b64_tr_b16 a[16:17], v194\n" \
  "ds_read_b64_tr_b16 a[18:19], v194 offset:512\n" \
  "ds_read_b64_tr_b16 a[20:21], v194 offset:8192\n" \
  "ds_read_b64_tr_b16 a[22:23], v194 offset:8704\n" \
  "v_add_u32 v195, 49152, v213\n" \
  "ds_read_b64_tr_b16 a[88:89], v195\n" \
  "ds_read_b64_tr_b16 a[90:91], v195 offset:512\n" \
  "ds_read_b64_tr_b16 a[92:93], v195 offset:8192\n" \
  "ds_read_b64_tr_b16 a[94:95], v195 offset:8704\n" \
  "s_waitcnt lgkmcnt(0)\n" \
  "s_waitcnt vmcnt(13)\n" \
  "s_barrier\n" \
  "v_add_u32 v194, 65536, v212\n" \
  "ds_read_b64_tr_b16 a[24:25], v194\n" \
  "ds_read_b64_tr_b16 a[26:27], v194 offset:512\n" \
  "ds_read_b64_tr_b16 a[28:29], v194 offset:8192\n" \
  "ds_read_b64_tr_b16 a[30:31], v194 offset:8704\n" \
  "v_add_u32 v195, 65536, v213\n" \
  "ds_read_b64_tr_b16 a[96:97], v195\n" \
  "ds_read_b64_tr_b16 a[98:99], v195 offset:512\n" \
  "ds_read_b64_tr_b16 a[100:101], v195 offset:8192\n" \
  "ds_read_b64_tr_b16 a[102:103], v195 offset:8704\n" \
  "v_add_u32 v194, 81920, v212\n" \
  "ds_read_b64_tr_b16 a[32:33], v194\n" \
  "ds_read_b64_tr_b16 a[34:35], v194 offset:512\n" \
  "ds_read_b64_tr_b16 a[36:37], v194 offset:8192\n" \
  "ds_read_b64_tr_b16 a[38:39], v194 offset:8704\n" \
  "v_add_u32 v195, 81920, v213\n" \
  "ds_read_b64_tr_b16 a[104:105], v195\n" \
  "ds_read_b64_tr_b16 a[106:107], v195 offset:512\n" \
  "ds_read_b64_tr_b16 a[108:109], v195 offset:8192\n" \
  "ds_read_b64_tr_b16 a[110:111], v195 offset:8704\n" \
  "v_add_u32 v194, 98304, v212\n" \
  "ds_read_b64_tr_b16 a[40:41], v194\n" \
  "ds_read_b64_tr_b16 a[42:43], v194 offset:512\n" \
  "ds_read_b64_tr_b16 a[44:45], v194 offset:8192\n" \
  "ds_read_b64_tr_b16 a[46:47], v194 offset:8704\n" \
  "v_add_u32 v195, 98304, v213\n" \
  "ds_read_b64_tr_b16 a[112:113], v195\n" \
  "ds_read_b64_tr_b16 a[114:115], v195 offset:512\n" \
  "ds_read_b64_tr_b16 a[116:117], v195 offset:8192\n" \
  "ds_read_b64_tr_b16 a[118:119], v195 offset:8704\n" \
  "s_waitcnt lgkmcnt(0)\n" \
  "s_waitcnt vmcnt(1)\n" \
  "s_barrier\n" \
  "v_add_u32 v194, 114688, v212\n" \
  "ds_read_b64_tr_b16 a[48:49], v194\n" \
  "ds_read_b64_tr_b16 a[50:51], v194 offset:512\n" \
  "ds_read_b64_tr_b16 a[52:53], v194 offset:8192\n" \
  "ds_read_b64_tr_b16 a[54:55], v194 offset:8704\n" \
  "v_add_u32 v195, 114688, v213\n" \
  "ds_read_b64_tr_b16 a[120:121], v195\n" \
  "ds_read_b64_tr_b16 a[122:123], v195 offset:512\n" \
  "ds_read_b64_tr_b16 a[124:125], v195 offset:8192\n" \
  "ds_read_b64_tr_b16 a[126:127], v195 offset:8704\n" \
  "v_add_u32 v194, 131072, v212\n" \
  "ds_read_b64_tr_b16 a[56:57], v194\n" \
  "ds_read_b64_tr_b16 a[58:59], v194 offset:512\n" \
  "ds_read_b64_tr_b16 a[60:61], v194 offset:8192\n" \
  "ds_read_b64_tr_b16 a[62:63], v194 offset:8704\n" \
  "v_add_u32 v195, 131072, v213\n" \
  "ds_read_b64_tr_b16 a[128:129], v195\n" \
  "ds_read_b64_tr_b16 a[130:131], v195 offset:512\n" \
  "ds_read_b64_tr_b16 a[132:133], v195 offset:8192\n" \
  "ds_read_b64_tr_b16 a[134:135], v195 offset:8704\n" \
  "v_add_u32 v194, 147456, v212\n" \
  "ds_read_b64_tr_b16 a[64:65], v194\n" \
  "ds_read_b64_tr_b16 a[66:67], v194 offset:512\n" \
  "ds_read_b64_tr_b16 a[68:69], v194 offset:8192\n" \
  "ds_read_b64_tr_b16 a[70:71], v194 offset:8704\n" \
  "v_add_u32 v195, 147456, v213\n" \
  "ds_read_b64_tr_b16 a[136:137], v195\n" \
  "ds_read_b64_tr_b16 a[138:139], v195 offset:512\n" \
  "ds_read_b64_tr_b16 a[140:141], v195 offset:8192\n" \
  "ds_read_b64_tr_b16 a[142:143], v195 offset:8704\n" \
  "s_waitcnt lgkmcnt(0)\n" \
  "s_waitcnt vmcnt(0)\n" \
  "v_readfirstlane_b32 s96, v198\n" \
  "v_readfirstlane_b32 s63, v199\n" \
  "s_cmp_eq_u32 s63, s55\n" \
  "s_cselect_b32 s96, s96, 0\n" \
  "s_cmp_ge_u32 s96, 0xc0000000\n" \
  "s_cbranch_scc1 .LW_go_x_%=\n" \
  "s_mov_b32 s69, 0\n" \
  ".LW_go_%=:\n" \
  "buffer_load_dwordx2 v[196:197], off, s[40:43], %45 sc1\n" \
  "s_waitcnt vmcnt(0)\n" \
  "v_readfirstlane_b32 s96, v196\n" \
  "v_readfirstlane_b32 s63, v197\n" \
  "s_cmp_eq_u32 s63, s55\n" \
  "s_cselect_b32 s96, s96, 0\n" \
  "s_cmp_ge_u32 s96, 0xc0000000\n" \
  "s_cbranch_scc1 .LW_go_x_%=\n" \
  "s_sleep 8\n" \
  "s_add_u32 s69, s69, 1\n" \
  "s_and_b32 s63, s69, 1023\n" \
  "s_cbranch_scc1 .LW_go_%=\n" \
  "s_cmp_gt_u32 s69, 2097152\n" \
  "s_cbranch_scc1 .LW_go_set_%=\n" \
  "buffer_load_dwordx2 v[196:197], off, s[40:43], %46 offset:128 sc1\n" \
  "s_waitcnt vmcnt(0)\n" \
  "v_readfirstlane_b32 s63, v197\n" \
  "s_cmp_eq_u32 s63, s55\n" \
  "s_cbranch_scc0 .LW_go_%=\n" \
  "s_branch .LW_go_x_%=\n" \
  ".LW_go_set_%=:\n" \
  "v_mov_b32 v196, 1\n" \
  "v_mov_b32 v197, s55\n" \
  "buffer_store_dwordx2 v[196:197], off, s[40:43], %46 offset:128 sc1\n" \
  "s_waitcnt vmcnt(0)\n" \
  ".LW_go_x_%=:\n" \
  "s_and_b32 s60, s96, 2\n" \
  "s_lshl_b32 s60, s60, 12\n" \
  "s_xor_b32 s74, %42, s60\n" \
  "s_xor_b32 s75, %44, s60\n" \
  "s_bfe_u32 s56, s96, 0x30010\n" \
  "s_lshl_b32 s56, s56, 12\n" \
  "s_or_b32 s56, s56, 0x200000\n" \
  "s_mov_b32 s57, 1052672\n" \
  "s_lshl_b32 s61, %41, 2\n" \
  "s_lshr_b32 s51, s96, s61\n" \
  "s_and_b32 s51, s51, 7\n" \
  "s_lshl_b32 s51, s51, 12\n" \
  "s_xor_b32 s51, s51, 4210688\n" \
  "s_mov_b32 s71, 0\n" \
  "s_add_u32 s45, s51, 886784\n" \
  "s_mov_b32 s46, -1\n" \
  "v_mov_b64 v[48:49], -1\n" \
  "v_mov_b64 v[50:51], -1\n" \
  "v_and_b32 v194, 3, v250\n" \
  "v_lshlrev_b32 v194, 2, v194\n" \
  "v_lshrrev_b32 v194, v194, s96\n" \
  "v_and_b32 v194, 7, v194\n" \
  "v_lshlrev_b32 v194, 12, v194\n" \
  "v_and_b32 v250, -4, v250\n" \
  "v_xor_b32 v250, v250, v194\n" \
  "v_and_b32 v194, 3, v252\n" \
  "v_lshlrev_b32 v194, 2, v194\n" \
  "v_lshrrev_b32 v194, v194, s96\n" \
  "v_and_b32 v194, 7, v194\n" \
  "v_lshlrev_b32 v194, 12, v194\n" \
  "v_and_b32 v252, -4, v252\n" \
  "v_xor_b32 v252, v252, v194\n" \
  "v_and_b32 v194, 3, v251\n" \
  "v_lshlrev_b32 v194, 2, v194\n" \
  "v_lshrrev_b32 v194, v194, s96\n" \
  "v_and_b32 v194, 7, v194\n" \
  "v_lshlrev_b32 v194, 12, v194\n" \
  "v_and_b32 v251, -4, v251\n" \
  "v_xor_b32 v251, v251, v194\n" \
  "v_xor_b32 v185, s56, v250\n" \
  "buffer_store_dwordx4 v[48:51], v185, s[40:43], 0 offen sc1\n" \
  "v_xor_b32 v186, s56, v252\n" \
  "buffer_store_dwordx2 v[48:49], v186, s[40:43], 0 offen sc1\n" \
  "s_xor_b32 s62, s56, s57\n" \
  "s_nop 1\n" \
  "v_xor_b32 v185, s62, v250\n" \
  "buffer_store_dwordx4 v[48:51], v185, s[40:43], 0 offen sc1\n" \
  "v_xor_b32 v186, s62, v252\n" \
  "buffer_store_dwordx2 v[48:49], v186, s[40:43], 0 offen sc1\n" \
  "s_andn2_b32 s60, 2, s96\n" \
  "s_lshl_b32 s60, s60, 12\n" \
  "s_xor_b32 s60, s60, 8192\n" \
  "s_xor_b32 s58, %39, s60\n" \
  "s_mov_b32 s49, 0\n" \
  "s_add_u32 s47, %49, s60\n" \
  "s_add_u32 s97, s60, 8388608\n" \
  "s_xor_b32 s50, %40, s60\n" \
  "s_mov_b32 s48, 0\n" \
  "s_mov_b32 s54, 0\n" \
  "s_cmp_eq_u32 %36, 0\n" \
  "s_cselect_b64 s[88:89], -1, 0\n" \
  "s_mov_b32 s68, 0\n" \
  "s_mov_b32 s70, 1\n" \
  "s_mov_b32 s73, 0\n" \
  "s_mov_b32 s44, 0\n" \
  "s_mov_b32 s53, 0\n" \
  "s_memrealtime s[86:87]\n" \
  "s_memtime s[76:77]\n" \
  "s_waitcnt lgkmcnt(0)\n" \
  "s_mov_b32 s90, s76\n" \
  "s_mov_b32 s87, 340\n" \
  "s_mov_b32 s64, 0x3e0293ee\n" \
  "s_mov_b32 s65, 0xbe0293ee\n" \
  "v_mov_b64 v[60:61], 0\n" \
  "v_mov_b64 v[62:63], 0\n" \
  "v_mov_b64 v[64:65], 0\n" \
  "v_mov_b64 v[66:67], 0\n" \
  "v_mov_b64 v[68:69], 0\n" \
  "v_mov_b64 v[70:71], 0\n" \
  "v_mov_b64 v[72:73], 0\n" \
  "v_mov_b64 v[74:75], 0\n" \
  "v_mov_b64 v[76:77], 0\n" \
  "v_mov_b64 v[78:79], 0\n" \
  "v_mov_b32 v56, 0\n" \
  "v_mov_b32 v57, s55\n" \
  "v_min_u32 v58, 1, v243\n" \
  "v_lshlrev_b32 v58, 30, v58\n" \
  "v_mov_b64 v[82:83], 0\n" \
  "s_mov_b32 s67, 2130706432\n" \
  "s_waitcnt vmcnt(0) lgkmcnt(0)\n" \
  "s_barrier\n" \
  "s_branch .LW_p_%=\n" \
  "s_nop 7\n" \
  ".p2align 6\n" \
  ".fill (((40 - (.LW_p_%= - .LW_rot_%=)) & 63) >> 2), 4, 0xBF800000\n" \
  ".LW_rot_%=:\n" \
  "s_xor_b32 s51, s51, s56\n" \
  "s_xor_b32 s71, s71, s56\n" \
  "v_xor_b32 v250, s56, v250\n" \
  "v_xor_b32 v252, s56, v252\n" \
  "s_mov_b32 s60, s56\n" \
  "s_mov_b32 s56, s57\n" \
  "s_mov_b32 s57, s60\n" \
  "s_add_u32 s59, s51, 819200\n" \
  "s_mov_b32 m0, 0\n" \
  "s_mov_b32 s70, 0\n" \
  "s_mov_b32 s68, 0\n" \
  "s_waitcnt lgkmcnt(0)\n" \
  "s_mov_b32 s69, 0\n" \
  ".LW_tw_%=:\n" \
  "v_readfirstlane_b32 s63, v52\n" \
  "s_cmp_lg_u32 s63, s48\n" \
  "s_cbranch_scc1 .LW_tw_re_%=\n" \
  "v_readfirstlane_b32 s53, v54\n" \
  ".LW_rt_%=:\n" \
  "s_memrealtime s[60:61]\n" \
  "s_add_u32 s73, s53, s44\n" \
  "s_waitcnt lgkmcnt(0)\n" \
  "s_sub_u32 s62, s73, s60\n" \
  "s_cmp_ge_u32 s62, 2000\n" \
  "s_cbranch_scc1 .LW_lat_%=\n" \
  "s_sub_u32 s62, s62, 30\n" \
  ".LW_slp_%=:\n" \
  "s_cmp_le_i32 s62, 0\n" \
  "s_cbranch_scc1 .LW_clk_%=\n" \
  "s_sleep 1\n" \
  "s_sub_u32 s62, s62, 3\n" \
  "s_branch .LW_slp_%=\n" \
  ".LW_clk_%=:\n" \
  "s_memrealtime s[60:61]\n" \
  "s_waitcnt lgkmcnt(0)\n" \
  "s_sub_u32 s62, s73, s60\n" \
  "s_cmp_gt_i32 s62, 21\n" \
  "s_cbranch_scc1 .LW_clk_%=\n" \
  "s_cmp_le_i32 s62, 11\n" \
  "s_cbranch_scc1 .LW_clk2_%=\n" \
  "s_memrealtime s[76:77]\n" \
  "s_nop 4\n" \
  "s_memrealtime s[78:79]\n" \
  "s_nop 4\n" \
  "s_memrealtime s[80:81]\n" \
  "s_nop 4\n" \
  "s_memrealtime s[82:83]\n" \
  "s_waitcnt lgkmcnt(0)\n" \
  "s_mov_b32 s77, 1\n" \
  "s_cmp_eq_u32 s78, s76\n" \
  "s_addc_u32 s77, s77, 0\n" \
  "s_cmp_eq_u32 s80, s76\n" \
  "s_addc_u32 s77, s77, 0\n" \
  "s_cmp_eq_u32 s82, s76\n" \
  "s_addc_u32 s77, s77, 0\n" \
  "s_mul_i32 s77, s77, 24\n" \
  "s_lshr_b32 s78, s87, 2\n" \
  "s_sub_u32 s77, s78, s77\n" \
  "s_max_i32 s77, s77, 0\n" \
  "s_sub_u32 s76, s73, s76\n" \
  "s_mul_i32 s76, s76, s87\n" \
  "s_ashr_i32 s76, s76, 4\n" \
  "s_sub_u32 s76, s76, s77\n" \
  "s_sub_u32 s76, s76, 200\n" \
  "s_max_i32 s76, s76, 0\n" \
  "s_min_i32 s76, s76, 320\n" \
  "s_lshr_b32 s76, s76, 3\n" \
  "s_lshl_b32 s76, s76, 2\n" \
  "s_mov_b32 s68, 1\n" \
  "s_getpc_b64 s[78:79]\n" \
  "s_sub_u32 s76, 180, s76\n" \
  "s_add_u32 s78, s78, s76\n" \
  "s_addc_u32 s79, s79, 0\n" \
  "s_setpc_b64 s[78:79]\n" \
  "s_nop 1\n" \
  "s_nop 1\n" \
  "s_nop 1\n" \
  "s_nop 1\n" \
  "s_nop 1\n" \
  "s_nop 1\n" \
  "s_nop 1\n" \
  "s_nop 1\n" \
  "s_nop 1\n" \
  "s_nop 1\n" \
  "s_nop 1\n" \
  "s_nop 1\n" \
  "s_nop 1\n" \
  "s_nop 1\n" \
  "s_nop 1\n" \
  "s_nop 1\n" \
  "s_nop 1\n" \
  "s_nop 1\n" \
  "s_nop 1\n" \
  "s_nop 1\n" \
  "s_nop 1\n" \
  "s_nop 1\n" \
  "s_nop 1\n" \
  "s_nop 1\n" \
  "s_nop 1\n" \
  "s_nop 1\n" \
  "s_nop 1\n" \
  "s_nop 1\n" \
  "s_nop 1\n" \
  "s_nop 1\n" \
  "s_nop 1\n" \
  "s_nop 1\n" \
  "s_nop 1\n" \
  "s_nop 1\n" \
  "s_nop 1\n" \
  "s_nop 1\n" \
  "s_nop 1\n" \
  "s_nop 1\n" \
  "s_nop 1\n" \
  "s_nop 1\n" \
  ".LW_noclk_%=:\n" \
  "buffer_load_dwordx4 v243, s[40:43], s59 offen sc1 lds\n" \
  "buffer_load_dwordx4 v243, s[40:43], s59 offen offset:1024 sc1 lds\n" \
  "buffer_load_dwordx4 v243, s[40:43], s59 offen offset:2048 sc1 lds\n" \
  "buffer_load_dwordx4 v243, s[40:43], s59 offen offset:3072 sc1 lds\n" \
  "s_add_u32 s60, s59, 32768\n" \
  "s_movk_i32 m0, 4096\n" \
  "s_nop 0\n" \
  "buffer_load_dwordx4 v243, s[40:43], s60 offen sc1 lds\n" \
  "buffer_load_dwordx4 v243, s[40:43], s60 offen offset:1024 sc1 lds\n" \
  "buffer_load_dwordx4 v243, s[40:43], s60 offen offset:2048 sc1 lds\n" \
  "buffer_load_dwordx4 v243, s[40:43], s60 offen offset:3072 sc1 lds\n" \
  "s_add_u32 s60, s59, 65536\n" \
  "s_movk_i32 m0, 8192\n" \
  "s_nop 0\n" \
  "buffer_load_dwordx4 v243, s[40:43], s60 offen sc1 lds\n" \
  ".LW_g3_%=:\n" \
  "s_waitcnt vmcnt(5)\n" \
  "s_barrier\n" \
  ".LW_p_%=:\n" \
  "ds_read_b128 v[84:87], v243\n" \
  "ds_read_b128 v[88:91], v243 offset:1024\n" \
  "ds_read_b128 v[92:95], v243 offset:2048\n" \
  "ds_read_b128 v[96:99], v243 offset:3072\n" \
  "s_waitcnt lgkmcnt(3)\n" \
  "v_smfmac_f32_16x16x64_bf16 v[60:63], v[84:87], a[0:7], v242\n" \
  "s_waitcnt vmcnt(0)\n" \
  "s_barrier\n" \
  "ds_read_b128 v[100:103], v243 offset:5120\n" \
  "ds_read_b128 v[104:107], v243 offset:4096\n" \
  "s_waitcnt lgkmcnt(4)\n" \
  "v_smfmac_f32_16x16x64_bf16 v[60:63], v[88:91], a[8:15], v242\n" \
  "ds_read_b128 v[108:111], v243 offset:7168\n" \
  "s_waitcnt lgkmcnt(4)\n" \
  "v_smfmac_f32_16x16x64_bf16 v[60:63], v[92:95], a[16:23], v242\n" \
  "ds_read_b128 v[112:115], v243 offset:6144\n" \
  "v_smfmac_f32_16x16x64_bf16 v[64:67], v[84:87], a[72:79], v242\n" \
  "ds_read_b128 v[116:119], v243 offset:8192\n" \
  "s_waitcnt lgkmcnt(5)\n" \
  "v_smfmac_f32_16x16x64_bf16 v[60:63], v[96:99], a[24:31], v242\n" \
  "s_waitcnt lgkmcnt(3)\n" \
  "v_smfmac_f32_16x16x64_bf16 v[60:63], v[104:107], a[32:39], v242\n" \
  "v_smfmac_f32_16x16x64_bf16 v[60:63], v[100:103], a[40:47], v242\n" \
  "s_waitcnt lgkmcnt(1)\n" \
  "v_smfmac_f32_16x16x64_bf16 v[60:63], v[112:115], a[48:55], v242\n" \
  "v_smfmac_f32_16x16x64_bf16 v[60:63], v[108:111], a[56:63], v242\n" \
  "s_waitcnt lgkmcnt(0)\n" \
  "v_smfmac_f32_16x16x64_bf16 v[60:63], v[116:119], a[64:71], v242\n" \
  "s_nop 2\n" \
  "v_smfmac_f32_16x16x64_bf16 v[64:67], v[88:91], a[80:87], v242\n" \
  "s_nop 3\n" \
  "v_add_f32 v60, v60, v62\n" \
  "v_add_f32 v61, v61, v63\n" \
  "v_cmp_u_f32 vcc, v60, v61\n" \
  "v_cvt_pk_bf16_f32 v80, v60, v61\n" \
  "v_smfmac_f32_16x16x64_bf16 v[64:67], v[92:95], a[88:95], v242\n" \
  "s_cbranch_vccnz .LW_slow_%=\n" \
  ".LW_back_%=:\n" \
  "ds_write_b32 v244, v80\n" \
  "v_smfmac_f32_16x16x64_bf16 v[64:67], v[96:99], a[96:103], v242\n" \
  "v_smfmac_f32_16x16x64_bf16 v[64:67], v[104:107], a[104:111], v242\n" \
  "v_smfmac_f32_16x16x64_bf16 v[64:67], v[100:103], a[112:119], v242\n" \
  "v_smfmac_f32_16x16x64_bf16 v[64:67], v[112:115], a[120:127], v242\n" \
  "s_waitcnt lgkmcnt(0)\n" \
  "s_barrier\n" \
  "v_smfmac_f32_16x16x64_bf16 v[64:67], v[108:111], a[128:135], v242\n" \
  "ds_read_b64_tr_b16 v[86:87], v245 offset:1536\n" \
  "ds_read_b64_tr_b16 v[90:91], v245 offset:512\n" \
  "v_smfmac_f32_16x16x64_bf16 v[64:67], v[116:119], a[136:143], v242\n" \
  "ds_read_b64_tr_b16 v[88:89], v245\n" \
  "s_nop 6\n" \
  "v_add_f32 v65, v65, v67\n" \
  "v_add_f32 v64, v64, v66\n" \
  "v_cvt_pk_bf16_f32 v81, v64, v65\n" \
  "ds_write_b32 v244, v81 offset:256\n" \
  "s_waitcnt lgkmcnt(1)\n" \
  "v_smfmac_f32_16x16x64_bf16 v[76:79], v[88:91], a[208:215], v248\n" \
  "v_smfmac_f32_16x16x64_bf16 v[72:75], v[88:91], a[176:183], v248\n" \
  "ds_read_b64_tr_b16 v[84:85], v245 offset:1024\n" \
  "v_smfmac_f32_16x16x64_bf16 v[68:71], v[88:91], a[144:151], v248\n" \
  "s_waitcnt lgkmcnt(1)\n" \
  "s_barrier\n" \
  "ds_read_b64_tr_b16 v[88:89], v245 offset:256\n" \
  "ds_read_b64_tr_b16 v[90:91], v245 offset:768\n" \
  "ds_read_b64_tr_b16 v[94:95], v245 offset:1792\n" \
  "s_waitcnt lgkmcnt(3)\n" \
  "v_smfmac_f32_16x16x64_bf16 v[68:71], v[84:87], a[152:159], v248\n" \
  "ds_read_b64_tr_b16 v[92:93], v245 offset:1280\n" \
  "v_smfmac_f32_16x16x64_bf16 v[72:75], v[84:87], a[184:191], v248\n" \
  "v_smfmac_f32_16x16x64_bf16 v[76:79], v[84:87], a[216:223], v248\n" \
  "s_waitcnt lgkmcnt(2)\n" \
  "v_smfmac_f32_16x16x64_bf16 v[68:71], v[88:91], a[160:167], v248\n" \
  "s_waitcnt lgkmcnt(0)\n" \
  "v_smfmac_f32_16x16x64_bf16 v[68:71], v[92:95], a[168:175], v248\n" \
  "s_nop 2\n" \
  "v_smfmac_f32_16x16x64_bf16 v[72:75], v[88:91], a[192:199], v248\n" \
  "s_nop 0\n" \
  "v_smfmac_f32_16x16x64_bf16 v[72:75], v[92:95], a[200:207], v248\n" \
  "s_nop 2\n" \
  "v_smfmac_f32_16x16x64_bf16 v[76:79], v[88:91], a[224:231], v248\n" \
  "s_nop 0\n" \
  "v_smfmac_f32_16x16x64_bf16 v[76:79], v[92:95], a[232:239], v248\n" \
  "v_cvt_pk_bf16_f32 v68, v68, v69\n" \
  "v_cvt_pk_bf16_f32 v69, v70, v71\n" \
  "v_cvt_pk_bf16_f32 v71, v74, v75\n" \
  "v_cvt_pk_bf16_f32 v70, v72, v73\n" \
  "s_nop 1\n" \
  "v_permlane16_swap_b32 v68, v70\n" \
  "v_permlane16_swap_b32 v69, v71\n" \
  "v_cvt_pk_bf16_f32 v76, v76, v77\n" \
  "v_cvt_pk_bf16_f32 v77, v78, v79\n" \
  "buffer_store_dwordx4 v[68:71], v250, s[40:43], 0 offen sc1\n" \
  "buffer_store_dwordx2 v[76:77], v252, s[40:43], 0 offen sc1\n" \
  ".LW_loop_%=:\n" \
  "s_memrealtime s[98:99]\n" \
  "s_memtime s[84:85]\n" \
  "s_cmp_ge_u32 s48, 58\n" \
  "s_cbranch_scc1 .LW_cp_%=\n" \
  ".LW_cp_b_%=:\n" \
  "v_mov_b64 v[60:61], 0\n" \
  "v_mov_b64 v[62:63], 0\n" \
  "v_mov_b64 v[64:65], 0\n" \
  "v_mov_b64 v[66:67], 0\n" \
  "v_mov_b64 v[68:69], 0\n" \
  "v_mov_b64 v[70:71], 0\n" \
  "v_mov_b64 v[72:73], 0\n" \
  "v_mov_b64 v[74:75], 0\n" \
  "v_mov_b64 v[76:77], 0\n" \
  "v_mov_b64 v[78:79], 0\n" \
  "s_add_u32 s95, s48, 1\n" \
  "s_and_b32 s63, s95, 3\n" \
  "s_lshl_b32 s95, s95, 5\n" \
  "s_lshl_b32 s63, s63, 4\n" \
  "s_sub_u32 s95, s95, s63\n" \
  "s_add_u32 s95, s95, s45\n" \
  "buffer_load_dwordx4 v[52:55], off, s[40:43], s95 sc1\n" \
  "s_add_u32 s48, s48, 1\n" \
  "v_mov_b32 v56, s48\n" \
  "s_waitcnt lgkmcnt(0)\n" \
  "s_cmp_gt_u32 s48, 57\n" \
  "s_cbranch_scc1 .LW_ef_%=\n" \
  ".LW_ef_b_%=:\n" \
  "s_sub_u32 s76, s84, s90\n" \
  "s_sub_u32 s77, s98, s86\n" \
  "v_cvt_f32_u32 v216, s76\n" \
  "v_cvt_f32_u32 v217, s77\n" \
  "v_rcp_f32 v217, v217\n" \
  "v_mul_f32 v216, 16.0, v216\n" \
  "v_fma_f32 v216, v216, v217, 0.5\n" \
  "v_cvt_u32_f32 v216, v216\n" \
  "s_nop 0\n" \
  "v_readfirstlane_b32 s76, v216\n" \
  "s_max_u32 s76, s76, 160\n" \
  "s_min_u32 s76, s76, 400\n" \
  "s_add_u32 s77, s48, 3\n" \
  "s_and_b32 s77, s77, 3\n" \
  "s_cselect_b32 s87, s87, s76\n" \
  "s_cselect_b32 s86, s86, s98\n" \
  "s_cselect_b32 s90, s90, s84\n" \
  "s_waitcnt vmcnt(0)\n" \
  "s_branch .LW_rot_%=\n" \
  "s_nop 7\n" \
  ".LW_slow_%=:\n" \
  "s_bitcmp1_b32 s70, 0\n" \
  "s_cbranch_scc1 .LW_back_%=\n" \
  "s_waitcnt lgkmcnt(0)\n" \
  "s_cmp_lt_u32 s70, 10\n" \
  "s_nop 3\n" \
  "s_cbranch_scc1 .LW_nol_%=\n" \
  "v_max_u32 v217, v84, v85\n" \
  "v_max3_u32 v217, v217, v86, v87\n" \
  "v_max3_u32 v217, v217, v88, v89\n" \
  "v_max3_u32 v217, v217, v90, v91\n" \
  "v_max3_u32 v217, v217, v92, v93\n" \
  "v_max3_u32 v217, v217, v94, v95\n" \
  "v_max3_u32 v217, v217, v96, v97\n" \
  "v_max3_u32 v217, v217, v98, v99\n" \
  "v_max3_u32 v217, v217, v104, v105\n" \
  "v_max3_u32 v217, v217, v106, v107\n" \
  "v_max3_u32 v217, v217, v100, v101\n" \
  "v_max3_u32 v217, v217, v102, v103\n" \
  "v_max3_u32 v217, v217, v112, v113\n" \
  "v_max3_u32 v217, v217, v114, v115\n" \
  "v_max3_u32 v217, v217, v108, v109\n" \
  "v_max3_u32 v217, v217, v110, v111\n" \
  "v_max3_u32 v217, v217, v116, v117\n" \
  "v_max3_u32 v217, v217, v118, v119\n" \
  "v_cmp_eq_u32 vcc, -1, v217\n" \
  "s_cbranch_vccz .LW_back_%=\n" \
  ".LW_nol_%=:\n" \
  "s_add_u32 s70, s70, 2\n" \
  "s_cmp_gt_u32 s70, 65536\n" \
  "s_cbranch_scc1 .LW_dset_%=\n" \
  "s_barrier\n" \
  "v_mov_b64 v[60:61], 0\n" \
  "v_mov_b64 v[62:63], 0\n" \
  "v_mov_b64 v[64:65], 0\n" \
  "v_mov_b64 v[66:67], 0\n" \
  "v_mov_b64 v[68:69], 0\n" \
  "v_mov_b64 v[70:71], 0\n" \
  "v_mov_b64 v[72:73], 0\n" \
  "v_mov_b64 v[74:75], 0\n" \
  "v_mov_b64 v[76:77], 0\n" \
  "v_mov_b64 v[78:79], 0\n" \
  "s_and_b32 s62, %48, 0xffff\n" \
  "s_add_u32 s44, s44, s62\n" \
  "s_xor_b32 s59, s59, 0\n" \
  "s_mov_b32 s68, 0\n" \
  "s_mov_b32 m0, 0\n" \
  "s_branch .LW_rt_%=\n" \
  ".LW_dset_%=:\n" \
  "v_mov_b32 v100, 1\n" \
  "v_mov_b32 v101, s55\n" \
  "buffer_store_dwordx2 v[100:101], off, s[40:43], %46 offset:128 sc1\n" \
  "s_mov_b32 s70, 1\n" \
  "v_mov_b64 v[60:61], 0\n" \
  "v_mov_b64 v[62:63], 0\n" \
  "v_mov_b64 v[64:65], 0\n" \
  "v_mov_b64 v[66:67], 0\n" \
  "v_mov_b64 v[68:69], 0\n" \
  "v_mov_b64 v[70:71], 0\n" \
  "v_mov_b64 v[72:73], 0\n" \
  "v_mov_b64 v[74:75], 0\n" \
  "v_mov_b64 v[76:77], 0\n" \
  "v_mov_b64 v[78:79], 0\n" \
  "s_waitcnt vmcnt(0) lgkmcnt(0)\n" \
  "s_barrier\n" \
  "s_branch .LW_p_%=\n" \
  ".LW_cp_%=:\n" \
  "s_sub_u32 s60, s48, 58\n" \
  "s_lshl_b32 s60, s60, 20\n" \
  "s_add_u32 s60, s60, s47\n" \
  "s_lshr_b32 s62, s60, 8\n" \
  "s_and_b32 s62, s62, 8192\n" \
  "s_xor_b32 s62, s62, s60\n" \
  "buffer_store_dwordx4 v[68:71], v253, s[40:43], s62 offen sc1\n" \
  "buffer_store_dwordx2 v[76:77], v255, s[40:43], s62 offen sc1\n" \
  "s_nop 1\n" \
  "s_branch .LW_cp_b_%=\n" \
  ".LW_ef_%=:\n" \
  "s_cmp_eq_u32 s48, 64\n" \
  "s_cbranch_scc1 .LW_end_%=\n" \
  "s_waitcnt vmcnt(0)\n" \
  "s_barrier\n" \
  "s_branch .LW_ef_b_%=\n" \
  ".LW_tw_re_%=:\n" \
  "s_add_u32 s69, s69, 1\n" \
  "s_cmp_gt_u32 s69, 200\n" \
  "s_cbranch_scc1 .LW_noclk_%=\n" \
  "s_sleep 2\n" \
  "buffer_load_dwordx4 v[52:55], off, s[40:43], s95 sc1\n" \
  "s_waitcnt vmcnt(0)\n" \
  "s_branch .LW_tw_%=\n" \
  ".LW_lat_%=:\n" \
  "s_lshr_b32 s63, %48, 16\n" \
  "s_add_u32 s62, s62, s63\n" \
  "s_cmp_lt_u32 s62, 4294937296\n" \
  "s_cbranch_scc1 .LW_noclk_%=\n" \
  "s_and_b32 s63, %48, 0xffff\n" \
  ".LW_lat1_%=:\n" \
  "s_add_u32 s44, s44, s63\n" \
  "s_add_u32 s62, s62, s63\n" \
  "s_cbranch_scc0 .LW_lat1_%=\n" \
  "s_branch .LW_rt_%=\n" \
  ".LW_clk2_%=:\n" \
  "s_cmp_le_i32 s62, 3\n" \
  "s_cbranch_scc1 .LW_clk_done_%=\n" \
  "s_memrealtime s[60:61]\n" \
  "s_waitcnt lgkmcnt(0)\n" \
  "s_sub_u32 s62, s73, s60\n" \
  "s_branch .LW_clk2_%=\n" \
  ".LW_clk_done_%=:\n" \
  "s_branch .LW_noclk_%=\n" \
  ".LW_end_%=:\n" \
  "s_waitcnt vmcnt(0)\n" \
  "s_waitcnt vmcnt(0)\n" \
  "s_nop 7\n" \
  "s_endpgm\n" \
  ".LW_other_%=:\n"
#define W_ASM_VOPS "+{v242}"(ci.sidx), "+{v243}"(ci.l16), "+{v244}"(ci.viw), "+{v245}"(ci.fva), "+{v246}"(ci.pk3), "+{v247}"(ci.fvs), "+{v248}"(ci.sid2), "+{v249}"(ci.uo), "+{v250}"(ci.po0), "+{v251}"(ci.po1), "+{v252}"(ci.po2), "+{v253}"(ci.co0), "+{v254}"(ci.co1), "+{v255}"(ci.co2), "+{v200}"(ci.vq0), "+{v201}"(ci.vq1), "+{v202}"(ci.vq2), "+{v203}"(ci.vq3), "+{v204}"(ci.vo0), "+{v205}"(ci.vo1), "+{v206}"(ci.vo2), "+{v207}"(ci.vo3), "+{v208}"(ci.v50), "+{v209}"(ci.v51), "+{v210}"(ci.v52), "+{v211}"(ci.v53), "+{v212}"(ci.tro0), "+{v213}"(ci.tro1), "+{v214}"(ci.m0o), "+{v215}"(ci.tro2), "+{v216}"(ci.tro3), "+{v217}"(ci.tro4)
#define W_ASM_SOPS "s"(ci.ws), "s"(ci.Wa), "s"(ci.Wb), "s"(ci.mem0), "s"(ci.w), "s"(ci.o_a), "s"(ci.o_b), "s"(ci.o_nx), "s"(ci.o_exp), "s"(ci.lab), "s"(ci.f_p), "s"(ci.f_m), "s"(ci.f_dur), "s"(ci.f_go), "s"(ci.o_ctl), "s"(ci.o_dbg), "s"(ci.dret), "s"(ci.o_qx), "s"(ci.f_s), "s"(ci.f_r)
#define W_ASM_CLOB "v0", "v1", "v2", "v3", "v4", "v5", "v6", "v7", "v8", "v9", "v10", "v11", "v12", "v13", "v14", "v15", "v16", "v17", "v18", "v19", "v20", "v21", "v22", "v23", "v24", "v25", "v26", "v27", "v28", "v29", "v30", "v31", "v32", "v33", "v34", "v35", "v36", "v37", "v38", "v39", "v40", "v41", "v42", "v43", "v44", "v45", "v46", "v47", "v48", "v49", "v50", "v51", "v52", "v53", "v54", "v55", "v56", "v57", "v58", "v59", "v60", "v61", "v62", "v63", "v64", "v65", "v66", "v67", "v68", "v69", "v70", "v71", "v72", "v73", "v74", "v75", "v76", "v77", "v78", "v79", "v80", "v81", "v82", "v83", "v84", "v85", "v86", "v87", "v88", "v89", "v90", "v91", "v92", "v93", "v94", "v95", "v96", "v97", "v98", "v99", "v100", "v101", "v102", "v103", "v104", "v105", "v106", "v107", "v108", "v109", "v110", "v111", "v112", "v113", "v114", "v115", "v116", "v117", "v118", "v119", "v120", "v121", "v122", "v123", "v124", "v125", "v126", "v127", "v128", "v129", "v130", "v131", "v132", "v133", "v134", "v135", "v136", "v137", "v138", "v139", "v140", "v141", "v142", "v143", "v144", "v145", "v146", "v147", "v148", "v149", "v150", "v151", "v152", "v153", "v154", "v155", "v156", "v157", "v158", "v159", "v160", "v161", "v162", "v163", "v164", "v165", "v166", "v167", "v168", "v169", "v170", "v171", "v172", "v173", "v174", "v175", "v176", "v177", "v178", "v179", "v180", "v181", "v182", "v183", "v184", "v185", "v186", "v187", "v188", "v189", "v190", "v191", "v192", "v193", "v194", "v195", "v196", "v197", "v198", "v199", "a0", "a1", "a2", "a3", "a4", "a5", "a6", "a7", "a8", "a9", "a10", "a11", "a12", "a13", "a14", "a15", "a16", "a17", "a18", "a19", "a20", "a21", "a22", "a23", "a24", "a25", "a26", "a27", "a28", "a29", "a30", "a31", "a32", "a33", "a34", "a35", "a36", "a37", "a38", "a39", "a40", "a41", "a42", "a43", "a44", "a45", "a46", "a47", "a48", "a49", "a50", "a51", "a52", "a53", "a54", "a55", "a56", "a57", "a58", "a59", "a60", "a61", "a62", "a63", "a64", "a65", "a66", "a67", "a68", "a69", "a70", "a71", "a72", "a73", "a74", "a75", "a76", "a77", "a78", "a79", "a80", "a81", "a82", "a83", "a84", "a85", "a86", "a87", "a88", "a89", "a90", "a91", "a92", "a93", "a94", "a95", "a96", "a97", "a98", "a99", "a100", "a101", "a102", "a103", "a104", "a105", "a106", "a107", "a108", "a109", "a110", "a111", "a112", "a113", "a114", "a115", "a116", "a117", "a118", "a119", "a120", "a121", "a122", "a123", "a124", "a125", "a126", "a127", "a128", "a129", "a130", "a131", "a132", "a133", "a134", "a135", "a136", "a137", "a138", "a139", "a140", "a141", "a142", "a143", "a144", "a145", "a146", "a147", "a148", "a149", "a150", "a151", "a152", "a153", "a154", "a155", "a156", "a157", "a158", "a159", "a160", "a161", "a162", "a163", "a164", "a165", "a166", "a167", "a168", "a169", "a170", "a171", "a172", "a173", "a174", "a175", "a176", "a177", "a178", "a179", "a180", "a181", "a182", "a183", "a184", "a185", "a186", "a187", "a188", "a189", "a190", "a191", "a192", "a193", "a194", "a195", "a196", "a197", "a198", "a199", "a200", "a201", "a202", "a203", "a204", "a205", "a206", "a207", "a208", "a209", "a210", "a211", "a212", "a213", "a214", "a215", "a216", "a217", "a218", "a219", "a220", "a221", "a222", "a223", "a224", "a225", "a226", "a227", "a228", "a229", "a230", "a231", "a232", "a233", "a234", "a235", "a236", "a237", "a238", "a239", "a240", "a241", "a242", "a243", "a244", "a245", "a246", "a247", "a248", "a249", "a250", "a251", "a252", "a253", "a254", "a255", "s40", "s41", "s42", "s43", "s44", "s45", "s46", "s47", "s48", "s49", "s50", "s51", "s52", "s53", "s54", "s55", "s56", "s57", "s58", "s59", "s60", "s61", "s62", "s63", "s64", "s65", "s66", "s67", "s68", "s69", "s70", "s71", "s72", "s73", "s74", "s75", "s76", "s77", "s78", "s79", "s80", "s81", "s82", "s83", "s84", "s85", "s86", "s87", "s88", "s89", "s90", "s91", "s92", "s93", "s94", "s95", "s96", "s97", "s98", "s99", "vcc", "scc", "memory"
#define R_ASM_TEXT \
  "s_mov_b64 s[40:41], %18\n" \
  "s_and_b32 s41, s41, 0xffff\n" \
  "s_mov_b32 s42, 255852544\n" \
  "s_mov_b32 s43, 0x00020000\n" \
  "s_bfe_u32 s56, %20, 0x30010\n" \
  "s_lshl_b32 s56, s56, 12\n" \
  "s_or_b32 s56, s56, 0x200000\n" \
  "s_mov_b32 s57, 1052672\n" \
  "s_mov_b32 s48, 0\n" \
  "s_cmp_lg_u32 %19, 0\n" \
  "s_cbranch_scc1 .LR_help_%=\n" \
  "s_lshl_b32 s61, %24, 2\n" \
  "s_lshr_b32 s51, %20, s61\n" \
  "s_and_b32 s51, s51, 7\n" \
  "s_lshl_b32 s51, s51, 12\n" \
  "s_xor_b32 s51, s51, 4210688\n" \
  "s_mov_b32 s71, s56\n" \
  "s_add_u32 s47, s51, 886784\n" \
  "s_lshr_b32 s52, %20, 0\n" \
  "s_and_b32 s52, s52, 7\n" \
  "s_lshl_b32 s52, s52, 12\n" \
  "s_xor_b32 s52, s52, 4210688\n" \
  "s_xor_b32 s52, s52, s56\n" \
  "s_lshr_b32 s64, %20, 4\n" \
  "s_and_b32 s64, s64, 7\n" \
  "s_lshl_b32 s64, s64, 12\n" \
  "s_xor_b32 s64, s64, 4210688\n" \
  "s_xor_b32 s64, s64, s56\n" \
  "s_lshr_b32 s65, %20, 8\n" \
  "s_and_b32 s65, s65, 7\n" \
  "s_lshl_b32 s65, s65, 12\n" \
  "s_xor_b32 s65, s65, 4210688\n" \
  "s_xor_b32 s65, s65, s56\n" \
  "s_lshr_b32 s67, %20, 12\n" \
  "s_and_b32 s67, s67, 7\n" \
  "s_lshl_b32 s67, s67, 12\n" \
  "s_xor_b32 s67, s67, 4210688\n" \
  "s_xor_b32 s67, s67, s56\n" \
  "v_add_u32 v208, 32768, v215\n" \
  "v_add_u32 v209, 65536, v215\n" \
  "v_add_u32 v210, 98304, v215\n" \
  "v_add_u32 v211, 40960, v214\n" \
  "s_mov_b32 s49, %22\n" \
  "s_mov_b32 s58, %23\n" \
  "s_memrealtime s[86:87]\n" \
  "s_memtime s[90:91]\n" \
  "s_waitcnt lgkmcnt(0)\n" \
  "s_mov_b32 s87, 900\n" \
  "s_mov_b32 s50, 0\n" \
  "s_mov_b32 s44, 0\n" \
  "s_mov_b32 s88, 0\n" \
  "s_mov_b32 s45, 0\n" \
  "s_mov_b32 s72, 2130706432\n" \
  "s_mov_b32 s46, 4096\n" \
  "v_mov_b32 v199, %21\n" \
  "s_mov_b32 s89, 2130706432\n" \
  "s_xor_b32 s51, s51, 0x40000000\n" \
  "s_xor_b32 s49, s49, 0x40000000\n" \
  "s_xor_b32 s58, s58, 0x40000000\n" \
  "s_xor_b32 s47, s47, 0x40000000\n" \
  "s_xor_b32 s52, s52, 0x40000000\n" \
  "s_xor_b32 s64, s64, 0x40000000\n" \
  "s_xor_b32 s65, s65, 0x40000000\n" \
  "s_xor_b32 s67, s67, 0x40000000\n" \
  ".LR_loop_%=:\n" \
  "s_add_u32 s95, s48, 0\n" \
  "s_and_b32 s63, s95, 3\n" \
  "s_lshl_b32 s95, s95, 5\n" \
  "s_lshl_b32 s63, s63, 4\n" \
  "s_sub_u32 s95, s95, s63\n" \
  "s_add_u32 s95, s95, s47\n" \
  "s_mov_b32 s54, s51\n" \
  "s_add_u32 s94, s51, 32768\n" \
  "s_mov_b32 s70, 0\n" \
  "s_mov_b32 s96, 0\n" \
  "buffer_load_dwordx4 v[200:203], off, s[40:43], s95 sc1\n" \
  "s_sub_u32 s76, s84, s90\n" \
  "s_sub_u32 s77, s98, s86\n" \
  "v_cvt_f32_u32 v196, s76\n" \
  "v_cvt_f32_u32 v197, s77\n" \
  "v_rcp_f32 v197, v197\n" \
  "v_mul_f32 v196, 16.0, v196\n" \
  "v_fma_f32 v196, v196, v197, 0.5\n" \
  "v_cvt_u32_f32 v196, v196\n" \
  "s_nop 0\n" \
  "v_readfirstlane_b32 s76, v196\n" \
  "s_max_u32 s76, s76, 160\n" \
  "s_min_u32 s76, s76, 400\n" \
  "s_add_u32 s77, s48, 2\n" \
  "s_and_b32 s77, s77, 3\n" \
  "s_cselect_b32 s87, s87, s76\n" \
  "s_cselect_b32 s86, s86, s98\n" \
  "s_cselect_b32 s90, s90, s84\n" \
  "v_mov_b64 v[64:65], 0\n" \
  "v_mov_b64 v[66:67], 0\n" \
  "v_mov_b64 v[68:69], 0\n" \
  "v_mov_b64 v[70:71], 0\n" \
  "v_mov_b64 v[72:73], 0\n" \
  "v_mov_b64 v[74:75], 0\n" \
  "v_mov_b64 v[76:77], 0\n" \
  "v_mov_b64 v[78:79], 0\n" \
  "v_mov_b64 v[80:81], 0\n" \
  "v_mov_b64 v[82:83], 0\n" \
  "v_mov_b64 v[84:85], 0\n" \
  "v_mov_b64 v[86:87], 0\n" \
  "v_mov_b64 v[88:89], 0\n" \
  "v_mov_b64 v[90:91], 0\n" \
  "v_mov_b64 v[92:93], 0\n" \
  "v_mov_b64 v[94:95], 0\n" \
  "v_mov_b64 v[96:97], 0\n" \
  "v_mov_b64 v[98:99], 0\n" \
  "v_mov_b64 v[100:101], 0\n" \
  "v_mov_b64 v[102:103], 0\n" \
  "v_mov_b64 v[104:105], 0\n" \
  "v_mov_b64 v[106:107], 0\n" \
  "v_mov_b64 v[108:109], 0\n" \
  "v_mov_b64 v[110:111], 0\n" \
  "v_mov_b64 v[112:113], 0\n" \
  "v_mov_b64 v[114:115], 0\n" \
  "v_mov_b64 v[116:117], 0\n" \
  "v_mov_b64 v[118:119], 0\n" \
  "v_mov_b64 v[120:121], 0\n" \
  "v_mov_b64 v[122:123], 0\n" \
  "v_mov_b64 v[124:125], 0\n" \
  "v_mov_b64 v[126:127], 0\n" \
  "v_mov_b64 v[192:193], 0\n" \
  "v_mov_b64 v[194:195], 0\n" \
  "s_mov_b64 exec, 1\n" \
  "buffer_store_dwordx2 v[198:199], off, s[40:43], s89 sc1\n" \
  "s_mov_b64 exec, -1\n" \
  "s_barrier\n" \
  "ds_read_b128 a[0:3], v211 offset:0\n" \
  "ds_read_b128 a[4:7], v211 offset:1024\n" \
  "ds_read_b128 a[8:11], v211 offset:2048\n" \
  "ds_read_b128 a[12:15], v211 offset:3072\n" \
  "ds_read_b128 a[16:19], v211 offset:4096\n" \
  "ds_read_b128 a[20:23], v211 offset:5120\n" \
  "ds_read_b128 a[24:27], v211 offset:6144\n" \
  "ds_read_b128 a[28:31], v211 offset:7168\n" \
  "ds_read_b128 a[32:35], v211 offset:8192\n" \
  "ds_read_b128 a[36:39], v211 offset:9216\n" \
  "ds_read_b128 a[40:43], v211 offset:10240\n" \
  "ds_read_b128 a[44:47], v211 offset:11264\n" \
  "ds_read_b128 a[48:51], v211 offset:12288\n" \
  "ds_read_b128 a[52:55], v211 offset:13312\n" \
  "ds_read_b128 a[56:59], v211 offset:14336\n" \
  "ds_read_b128 a[60:63], v211 offset:15360\n" \
  "ds_read_b128 a[64:67], v211 offset:16384\n" \
  "ds_read_b128 a[68:71], v211 offset:17408\n" \
  "ds_read_b128 a[72:75], v211 offset:18432\n" \
  "ds_read_b128 a[76:79], v211 offset:19456\n" \
  "ds_read_b128 a[80:83], v211 offset:20480\n" \
  "ds_read_b128 a[84:87], v211 offset:21504\n" \
  "ds_read_b128 a[88:91], v211 offset:22528\n" \
  "ds_read_b128 a[92:95], v211 offset:23552\n" \
  "ds_read_b128 a[96:99], v211 offset:24576\n" \
  "ds_read_b128 a[100:103], v211 offset:25600\n" \
  "ds_read_b128 a[104:107], v211 offset:26624\n" \
  "ds_read_b128 a[108:111], v211 offset:27648\n" \
  "ds_read_b128 a[112:115], v211 offset:28672\n" \
  "ds_read_b128 a[116:119], v211 offset:29696\n" \
  "ds_read_b128 a[120:123], v211 offset:30720\n" \
  "ds_read_b128 a[124:127], v211 offset:31744\n" \
  "v_xor_b32 v211, 32768, v211\n" \
  "s_mov_b32 s69, 0\n" \
  "s_waitcnt vmcnt(1)\n" \
  ".LR_tw_%=:\n" \
  "v_readfirstlane_b32 s60, v200\n" \
  "s_cmp_lg_u32 s60, s48\n" \
  "s_cbranch_scc1 .LR_tw_re_%=\n" \
  "v_readfirstlane_b32 s50, v201\n" \
  "s_cmp_eq_u32 s48, 0\n" \
  "s_cselect_b32 s50, s86, s50\n" \
  "s_cmp_lg_u32 s44, s45\n" \
  "s_cselect_b32 s60, s46, 0\n" \
  "s_mov_b32 s45, s44\n" \
  "s_xor_b32 s54, s54, s60\n" \
  "s_xor_b32 s94, s94, s60\n" \
  ".LR_again_%=:\n" \
  "s_memrealtime s[60:61]\n" \
  "s_add_u32 s53, s50, s44\n" \
  "s_bfe_u32 s63, %28, 0x80010\n" \
  "s_waitcnt lgkmcnt(0)\n" \
  "s_sub_u32 s62, s53, s60\n" \
  "s_sub_u32 s61, s62, 14\n" \
  "s_cmp_ge_u32 s61, 2000\n" \
  "s_cbranch_scc1 .LR_near_%=\n" \
  "s_sub_u32 s62, s62, s63\n" \
  "s_cmp_le_i32 s62, 0\n" \
  "s_cbranch_scc1 .LR_fin_%=\n" \
  "s_sub_u32 s62, s62, 9\n" \
  ".LR_slp_%=:\n" \
  "s_cmp_le_i32 s62, 0\n" \
  "s_cbranch_scc1 .LR_clk_%=\n" \
  "s_sleep 1\n" \
  "s_sub_u32 s62, s62, 3\n" \
  "s_branch .LR_slp_%=\n" \
  ".LR_clk_%=:\n" \
  "s_memrealtime s[60:61]\n" \
  "s_waitcnt lgkmcnt(0)\n" \
  "s_sub_u32 s62, s53, s60\n" \
  "s_cmp_gt_i32 s62, s63\n" \
  "s_cbranch_scc1 .LR_clk_%=\n" \
  ".LR_fin_%=:\n" \
  "s_memrealtime s[76:77]\n" \
  "s_nop 4\n" \
  "s_memrealtime s[78:79]\n" \
  "s_nop 4\n" \
  "s_memrealtime s[80:81]\n" \
  "s_nop 4\n" \
  "s_memrealtime s[82:83]\n" \
  "s_waitcnt lgkmcnt(0)\n" \
  "s_mov_b32 s77, 1\n" \
  "s_cmp_eq_u32 s78, s76\n" \
  "s_addc_u32 s77, s77, 0\n" \
  "s_cmp_eq_u32 s80, s76\n" \
  "s_addc_u32 s77, s77, 0\n" \
  "s_cmp_eq_u32 s82, s76\n" \
  "s_addc_u32 s77, s77, 0\n" \
  "s_mul_i32 s77, s77, 24\n" \
  "s_lshr_b32 s78, s87, 2\n" \
  "s_sub_u32 s77, s78, s77\n" \
  "s_max_i32 s77, s77, 0\n" \
  "s_sub_u32 s76, s53, s76\n" \
  "s_mul_i32 s76, s76, s87\n" \
  "s_ashr_i32 s76, s76, 4\n" \
  "s_sub_u32 s76, s76, s77\n" \
  "s_sub_u32 s76, s76, 220\n" \
  "s_max_i32 s76, s76, 0\n" \
  "s_min_i32 s76, s76, 400\n" \
  "s_lshr_b32 s76, s76, 3\n" \
  "s_lshl_b32 s76, s76, 2\n" \
  "s_getpc_b64 s[78:79]\n" \
  "s_sub_u32 s76, 220, s76\n" \
  "s_add_u32 s78, s78, s76\n" \
  "s_addc_u32 s79, s79, 0\n" \
  "s_setpc_b64 s[78:79]\n" \
  "s_nop 1\n" \
  "s_nop 1\n" \
  "s_nop 1\n" \
  "s_nop 1\n" \
  "s_nop 1\n" \
  "s_nop 1\n" \
  "s_nop 1\n" \
  "s_nop 1\n" \
  "s_nop 1\n" \
  "s_nop 1\n" \
  "s_nop 1\n" \
  "s_nop 1\n" \
  "s_nop 1\n" \
  "s_nop 1\n" \
  "s_nop 1\n" \
  "s_nop 1\n" \
  "s_nop 1\n" \
  "s_nop 1\n" \
  "s_nop 1\n" \
  "s_nop 1\n" \
  "s_nop 1\n" \
  "s_nop 1\n" \
  "s_nop 1\n" \
  "s_nop 1\n" \
  "s_nop 1\n" \
  "s_nop 1\n" \
  "s_nop 1\n" \
  "s_nop 1\n" \
  "s_nop 1\n" \
  "s_nop 1\n" \
  "s_nop 1\n" \
  "s_nop 1\n" \
  "s_nop 1\n" \
  "s_nop 1\n" \
  "s_nop 1\n" \
  "s_nop 1\n" \
  "s_nop 1\n" \
  "s_nop 1\n" \
  "s_nop 1\n" \
  "s_nop 1\n" \
  "s_nop 1\n" \
  "s_nop 1\n" \
  "s_nop 1\n" \
  "s_nop 1\n" \
  "s_nop 1\n" \
  "s_nop 1\n" \
  "s_nop 1\n" \
  "s_nop 1\n" \
  "s_nop 1\n" \
  "s_nop 1\n" \
  ".LR_b2_%=:\n" \
  "buffer_load_dwordx4 v[0:3], v215, s[40:43], s54 offen sc1\n" \
  "s_memtime s[92:93]\n" \
  "s_cmp_lg_u32 s96, 0\n" \
  "buffer_load_dwordx4 v[4:7], v215, s[40:43], s54 offen offset:1024 sc1\n" \
  "buffer_load_dwordx4 v[8:11], v215, s[40:43], s54 offen offset:2048 sc1\n" \
  "buffer_load_dwordx4 v[12:15], v215, s[40:43], s54 offen offset:3072 sc1\n" \
  "buffer_load_dwordx4 v[16:19], v208, s[40:43], s54 offen sc1\n" \
  "buffer_load_dwordx4 v[20:23], v208, s[40:43], s54 offen offset:1024 sc1\n" \
  "buffer_load_dwordx4 v[24:27], v208, s[40:43], s54 offen offset:2048 sc1\n" \
  "buffer_load_dwordx4 v[28:31], v208, s[40:43], s54 offen offset:3072 sc1\n" \
  "buffer_load_dwordx4 v[32:35], v209, s[40:43], s54 offen sc1\n" \
  "buffer_load_dwordx4 v[36:39], v209, s[40:43], s54 offen offset:1024 sc1\n" \
  "buffer_load_dwordx4 v[40:43], v209, s[40:43], s54 offen offset:2048 sc1\n" \
  "buffer_load_dwordx4 v[44:47], v209, s[40:43], s54 offen offset:3072 sc1\n" \
  "buffer_load_dwordx4 v[48:51], v210, s[40:43], s54 offen sc1\n" \
  "buffer_load_dwordx4 v[52:55], v210, s[40:43], s54 offen offset:1024 sc1\n" \
  "buffer_load_dwordx4 v[56:59], v210, s[40:43], s54 offen offset:2048 sc1\n" \
  "buffer_load_dwordx4 v[60:63], v210, s[40:43], s54 offen offset:3072 sc1\n" \
  "buffer_load_dwordx4 v[128:131], v214, s[40:43], s49 offen sc1\n" \
  "buffer_load_dwordx4 v[132:135], v214, s[40:43], s49 offen offset:1024 sc1\n" \
  "buffer_load_dwordx4 v[136:139], v214, s[40:43], s49 offen offset:2048 sc1\n" \
  "buffer_load_dwordx4 v[140:143], v214, s[40:43], s49 offen offset:3072 sc1\n" \
  "buffer_load_dwordx4 v[144:147], v214, s[40:43], s58 offen sc1\n" \
  "buffer_load_dwordx4 v[148:151], v214, s[40:43], s58 offen offset:1024 sc1\n" \
  "buffer_load_dwordx4 v[152:155], v214, s[40:43], s58 offen offset:2048 sc1\n" \
  "buffer_load_dwordx4 v[156:159], v214, s[40:43], s58 offen offset:3072 sc1\n" \
  "buffer_load_dwordx4 v[204:207], v215, s[40:43], s54 offen sc1\n" \
  "buffer_load_dwordx4 v[204:207], v215, s[40:43], s54 offen sc1\n" \
  "buffer_load_dwordx4 v[204:207], v215, s[40:43], s54 offen sc1\n" \
  "buffer_load_dwordx4 v[160:163], v216, s[40:43], s54 offen sc1\n" \
  "buffer_load_dwordx4 v[164:167], v216, s[40:43], s54 offen offset:1024 sc1\n" \
  "buffer_load_dwordx4 v[168:171], v216, s[40:43], s54 offen offset:2048 sc1\n" \
  "buffer_load_dwordx4 v[172:175], v216, s[40:43], s54 offen offset:3072 sc1\n" \
  "buffer_load_dwordx4 v[176:179], v216, s[40:43], s94 offen sc1\n" \
  "buffer_load_dwordx4 v[180:183], v216, s[40:43], s94 offen offset:1024 sc1\n" \
  "buffer_load_dwordx4 v[184:187], v216, s[40:43], s94 offen offset:2048 sc1\n" \
  "buffer_load_dwordx4 v[188:191], v216, s[40:43], s94 offen offset:3072 sc1\n" \
  "s_cbranch_scc1 .LR_nb_%=\n" \
  "s_barrier\n" \
  ".LR_nb_%=:\n" \
  "s_waitcnt vmcnt(34)\n" \
  "v_smfmac_f32_16x16x64_bf16 v[64:67], v[0:3], a[0:7], v212\n" \
  "s_waitcnt vmcnt(33)\n" \
  "s_nop 1\n" \
  "v_smfmac_f32_16x16x64_bf16 v[68:71], v[4:7], a[8:15], v212\n" \
  "s_waitcnt vmcnt(32)\n" \
  "s_nop 1\n" \
  "v_smfmac_f32_16x16x64_bf16 v[72:75], v[8:11], a[16:23], v212\n" \
  "s_waitcnt vmcnt(31)\n" \
  "v_cvt_pk_bf16_f32 v64, v64, v65\n" \
  "v_cvt_pk_bf16_f32 v65, v66, v67\n" \
  "v_smfmac_f32_16x16x64_bf16 v[76:79], v[12:15], a[24:31], v212\n" \
  "s_waitcnt vmcnt(30)\n" \
  "v_cvt_pk_bf16_f32 v66, v68, v69\n" \
  "v_cvt_pk_bf16_f32 v67, v70, v71\n" \
  "v_smfmac_f32_16x16x64_bf16 v[80:83], v[16:19], a[32:39], v212\n" \
  "s_waitcnt vmcnt(29)\n" \
  "v_cvt_pk_bf16_f32 v68, v72, v73\n" \
  "v_cvt_pk_bf16_f32 v69, v74, v75\n" \
  "v_smfmac_f32_16x16x64_bf16 v[84:87], v[20:23], a[40:47], v212\n" \
  "s_waitcnt vmcnt(28)\n" \
  "v_cvt_pk_bf16_f32 v70, v76, v77\n" \
  "v_cvt_pk_bf16_f32 v71, v78, v79\n" \
  "v_smfmac_f32_16x16x64_bf16 v[88:91], v[24:27], a[48:55], v212\n" \
  "s_waitcnt vmcnt(27)\n" \
  "v_cvt_pk_bf16_f32 v80, v80, v81\n" \
  "v_cvt_pk_bf16_f32 v81, v82, v83\n" \
  "v_smfmac_f32_16x16x64_bf16 v[92:95], v[28:31], a[56:63], v212\n" \
  "s_waitcnt vmcnt(26)\n" \
  "v_cvt_pk_bf16_f32 v82, v84, v85\n" \
  "v_cvt_pk_bf16_f32 v83, v86, v87\n" \
  "v_smfmac_f32_16x16x64_bf16 v[96:99], v[32:35], a[64:71], v212\n" \
  "s_waitcnt vmcnt(25)\n" \
  "v_cvt_pk_bf16_f32 v84, v88, v89\n" \
  "v_cvt_pk_bf16_f32 v85, v90, v91\n" \
  "v_smfmac_f32_16x16x64_bf16 v[100:103], v[36:39], a[72:79], v212\n" \
  "s_waitcnt vmcnt(24)\n" \
  "v_cvt_pk_bf16_f32 v86, v92, v93\n" \
  "v_cvt_pk_bf16_f32 v87, v94, v95\n" \
  "v_smfmac_f32_16x16x64_bf16 v[104:107], v[40:43], a[80:87], v212\n" \
  "s_waitcnt vmcnt(23)\n" \
  "v_cvt_pk_bf16_f32 v96, v96, v97\n" \
  "v_cvt_pk_bf16_f32 v97, v98, v99\n" \
  "v_smfmac_f32_16x16x64_bf16 v[108:111], v[44:47], a[88:95], v212\n" \
  "s_waitcnt vmcnt(22)\n" \
  "v_cvt_pk_bf16_f32 v98, v100, v101\n" \
  "v_cvt_pk_bf16_f32 v99, v102, v103\n" \
  "v_smfmac_f32_16x16x64_bf16 v[112:115], v[48:51], a[96:103], v212\n" \
  "s_waitcnt vmcnt(21)\n" \
  "v_cvt_pk_bf16_f32 v100, v104, v105\n" \
  "v_cvt_pk_bf16_f32 v101, v106, v107\n" \
  "v_smfmac_f32_16x16x64_bf16 v[116:119], v[52:55], a[104:111], v212\n" \
  "s_waitcnt vmcnt(20)\n" \
  "v_cvt_pk_bf16_f32 v102, v108, v109\n" \
  "v_cvt_pk_bf16_f32 v103, v110, v111\n" \
  "v_smfmac_f32_16x16x64_bf16 v[120:123], v[56:59], a[112:119], v212\n" \
  "s_waitcnt vmcnt(19)\n" \
  "v_cvt_pk_bf16_f32 v112, v112, v113\n" \
  "v_cvt_pk_bf16_f32 v113, v114, v115\n" \
  "v_smfmac_f32_16x16x64_bf16 v[124:127], v[60:63], a[120:127], v212\n" \
  "s_mov_b32 s96, 1\n" \
  "v_cvt_pk_bf16_f32 v114, v116, v117\n" \
  "v_cvt_pk_bf16_f32 v115, v118, v119\n" \
  "s_nop 0\n" \
  "v_cvt_pk_bf16_f32 v116, v120, v121\n" \
  "v_cvt_pk_bf16_f32 v117, v122, v123\n" \
  "s_nop 1\n" \
  "v_cvt_pk_bf16_f32 v118, v124, v125\n" \
  "v_cvt_pk_bf16_f32 v119, v126, v127\n" \
  "s_waitcnt vmcnt(18)\n" \
  "v_permlane16_swap_b32 v128, v129\n" \
  "v_permlane16_swap_b32 v130, v131\n" \
  "s_waitcnt vmcnt(17)\n" \
  "v_permlane16_swap_b32 v132, v133\n" \
  "v_permlane16_swap_b32 v134, v135\n" \
  "s_waitcnt vmcnt(16)\n" \
  "v_permlane16_swap_b32 v136, v137\n" \
  "v_permlane16_swap_b32 v138, v139\n" \
  "s_waitcnt vmcnt(15)\n" \
  "v_permlane16_swap_b32 v140, v141\n" \
  "v_permlane16_swap_b32 v142, v143\n" \
  "s_waitcnt vmcnt(14)\n" \
  "v_permlane16_swap_b32 v144, v145\n" \
  "v_permlane16_swap_b32 v146, v147\n" \
  "s_waitcnt vmcnt(13)\n" \
  "v_permlane16_swap_b32 v148, v149\n" \
  "v_permlane16_swap_b32 v150, v151\n" \
  "s_waitcnt vmcnt(12)\n" \
  "v_permlane16_swap_b32 v152, v153\n" \
  "v_permlane16_swap_b32 v154, v155\n" \
  "s_waitcnt vmcnt(11)\n" \
  "v_permlane16_swap_b32 v156, v157\n" \
  "v_permlane16_swap_b32 v158, v159\n" \
  "s_nop 0\n" \
  "s_waitcnt vmcnt(7)\n" \
  "v_smfmac_f32_16x16x64_bf16 v[192:195], v[160:163], v[128:135], v212\n" \
  "s_waitcnt vmcnt(6)\n" \
  "v_smfmac_f32_16x16x64_bf16 v[192:195], v[164:167], v[136:143], v212\n" \
  "s_waitcnt vmcnt(5)\n" \
  "v_smfmac_f32_16x16x64_bf16 v[192:195], v[168:171], v[144:151], v212\n" \
  "s_waitcnt vmcnt(4)\n" \
  "v_smfmac_f32_16x16x64_bf16 v[192:195], v[172:175], v[152:159], v212\n" \
  "s_waitcnt vmcnt(3)\n" \
  "v_smfmac_f32_16x16x64_bf16 v[192:195], v[176:179], v[64:71], v213\n" \
  "s_waitcnt vmcnt(2)\n" \
  "v_smfmac_f32_16x16x64_bf16 v[192:195], v[180:183], v[80:87], v213\n" \
  "s_waitcnt vmcnt(1)\n" \
  "v_smfmac_f32_16x16x64_bf16 v[192:195], v[184:187], v[96:103], v213\n" \
  "s_waitcnt vmcnt(0)\n" \
  "v_smfmac_f32_16x16x64_bf16 v[192:195], v[188:191], v[112:119], v213\n" \
  "s_nop 7\n" \
  "v_permlane32_swap_b32 v192, v194\n" \
  "v_permlane32_swap_b32 v193, v195\n" \
  "v_add_f32 v192, v192, v194\n" \
  "v_add_f32 v193, v193, v195\n" \
  "v_cvt_pk_bf16_f32 v194, v192, v193\n" \
  "v_cmp_u_f32 vcc, v192, v193\n" \
  "s_nop 0\n" \
  "v_mov_b32_dpp v195, v194 quad_perm:[1,0,3,2] row_mask:0xf bank_mask:0xf\n" \
  "v_perm_b32 v194, v195, v194, v218\n" \
  "s_cbranch_vccnz .LR_bad_%=\n" \
  ".LR_good_%=:\n" \
  "buffer_store_dword v194, v217, s[40:43], s52 offen sc1\n" \
  "buffer_store_dword v194, v217, s[40:43], s64 offen sc1\n" \
  "buffer_store_dword v194, v217, s[40:43], s65 offen sc1\n" \
  "buffer_store_dword v194, v217, s[40:43], s67 offen sc1\n" \
  "s_memrealtime s[98:99]\n" \
  "s_memtime s[84:85]\n" \
  "s_waitcnt lgkmcnt(0)\n" \
  "s_sub_u32 s60, s84, s92\n" \
  "s_cmp_lt_u32 s60, 4000\n" \
  "s_cselect_b32 s60, s60, 4194304\n" \
  "s_cmp_eq_u32 s70, 0\n" \
  "s_cselect_b32 s60, s60, 4194304\n" \
  "s_min_u32 s66, s66, s60\n" \
  "s_lshl_b32 s60, s66, 10\n" \
  "s_cselect_b32 s89, %30, 2130706432\n" \
  "s_or_b32 s60, s60, s87\n" \
  "v_mov_b32 v198, s60\n" \
  "s_and_b32 s61, s48, 3\n" \
  "s_cmp_eq_u32 s61, 0\n" \
  "s_cselect_b32 s89, s89, 2130706432\n" \
  "s_cselect_b32 s66, 4194304, s66\n" \
  "s_cmp_eq_u32 s48, 0\n" \
  "s_cbranch_scc1 .LR_go0_%=\n" \
  "s_cmp_lg_u32 %26, 0\n" \
  "s_cbranch_scc1 .LR_extra_%=\n" \
  ".LR_extra_b_%=:\n" \
  "s_xor_b32 s51, s51, s56\n" \
  "s_xor_b32 s49, s49, s56\n" \
  "s_xor_b32 s58, s58, s56\n" \
  "s_xor_b32 s52, s52, s57\n" \
  "s_xor_b32 s64, s64, s57\n" \
  "s_xor_b32 s65, s65, s57\n" \
  "s_xor_b32 s67, s67, s57\n" \
  "s_xor_b32 s71, s71, s57\n" \
  "s_mov_b32 s60, s56\n" \
  "s_mov_b32 s56, s57\n" \
  "s_mov_b32 s57, s60\n" \
  ".LR_go_%=:\n" \
  "s_add_u32 s48, s48, 1\n" \
  "s_cmp_lt_u32 s48, 64\n" \
  "s_cbranch_scc1 .LR_loop_%=\n" \
  "s_branch .LR_end_%=\n" \
  ".LR_tw_re_%=:\n" \
  "s_add_u32 s69, s69, 1\n" \
  "s_cmp_gt_u32 s69, 64\n" \
  "s_cbranch_scc1 .LR_now_%=\n" \
  "buffer_load_dwordx4 v[200:203], off, s[40:43], s95 sc1\n" \
  "s_waitcnt vmcnt(0)\n" \
  "s_branch .LR_tw_%=\n" \
  ".LR_now_%=:\n" \
  "s_waitcnt lgkmcnt(0)\n" \
  "s_branch .LR_b2_%=\n" \
  ".LR_near_%=:\n" \
  "s_cmp_ge_u32 s62, 14\n" \
  "s_cbranch_scc1 .LR_lat_%=\n" \
  "s_cmp_le_u32 s62, 4\n" \
  "s_cbranch_scc1 .LR_b2_%=\n" \
  "s_memrealtime s[60:61]\n" \
  "s_waitcnt lgkmcnt(0)\n" \
  "s_sub_u32 s62, s53, s60\n" \
  "s_branch .LR_near_%=\n" \
  ".LR_lat_%=:\n" \
  "s_lshr_b32 s63, %29, 16\n" \
  "s_add_u32 s62, s62, s63\n" \
  "s_cmp_lt_u32 s62, 4294937296\n" \
  "s_cbranch_scc1 .LR_b2_%=\n" \
  "s_and_b32 s63, %29, 0xffff\n" \
  ".LR_lat1_%=:\n" \
  "s_add_u32 s44, s44, s63\n" \
  "s_add_u32 s62, s62, s63\n" \
  "s_cbranch_scc0 .LR_lat1_%=\n" \
  "s_branch .LR_again_%=\n" \
  ".LR_mv_%=:\n" \
  "s_sub_u32 s76, s77, s44\n" \
  "s_cmp_gt_u32 s76, 2000\n" \
  "s_cbranch_scc1 .LR_b2_%=\n" \
  "s_mov_b32 s44, s77\n" \
  "s_branch .LR_again_%=\n" \
  ".LR_go0_%=:\n" \
  "s_xor_b32 s51, s51, 0x40000000\n" \
  "s_xor_b32 s49, s49, 0x40000000\n" \
  "s_xor_b32 s58, s58, 0x40000000\n" \
  "s_xor_b32 s47, s47, 0x40000000\n" \
  "s_xor_b32 s52, s52, 0x40000000\n" \
  "s_xor_b32 s64, s64, 0x40000000\n" \
  "s_xor_b32 s65, s65, 0x40000000\n" \
  "s_xor_b32 s67, s67, 0x40000000\n" \
  "s_mov_b32 s44, 0\n" \
  "s_mov_b32 s89, 2130706432\n" \
  "s_mov_b32 s87, 340\n" \
  "s_mov_b32 s66, 4194304\n" \
  "s_add_u32 s95, s47, 16\n" \
  "s_mov_b32 s69, 0\n" \
  ".LR_go1_%=:\n" \
  "buffer_load_dwordx4 v[200:203], off, s[40:43], s95 sc1\n" \
  "s_add_u32 s69, s69, 1\n" \
  "s_waitcnt vmcnt(0)\n" \
  "v_readfirstlane_b32 s60, v200\n" \
  "s_sleep 1\n" \
  "s_cmp_gt_u32 s69, 200\n" \
  "s_cselect_b32 s60, 1, s60\n" \
  "s_cmp_lg_u32 s60, 1\n" \
  "s_cbranch_scc1 .LR_go1_%=\n" \
  "s_branch .LR_go_%=\n" \
  ".LR_extra_%=:\n" \
  "s_cmp_lg_u32 s61, 1\n" \
  "s_cbranch_scc1 .LR_extra_b_%=\n" \
  "s_lshl_b32 s62, s48, 5\n" \
  "s_add_u32 s62, s62, %26\n" \
  "s_add_u32 s60, s44, 1\n" \
  "v_mov_b32 v196, s60\n" \
  "v_mov_b32 v197, %21\n" \
  "s_mov_b64 exec, 1\n" \
  "buffer_store_dwordx2 v[196:197], off, s[40:43], s62 sc1\n" \
  "s_mov_b64 exec, -1\n" \
  "s_branch .LR_extra_b_%=\n" \
  ".LR_bad_%=:\n" \
  "s_cmp_eq_u32 s48, 0\n" \
  "s_cbranch_scc1 .LR_good_%=\n" \
  "s_add_u32 s70, s70, 1\n" \
  "s_cmp_gt_u32 s70, 5\n" \
  "s_cbranch_scc1 .LR_bad1_%=\n" \
  ".LR_redo_%=:\n" \
  "s_and_b32 s62, %29, 0xffff\n" \
  "s_add_u32 s44, s44, s62\n" \
  "v_mov_b32 v196, s44\n" \
  "v_xor_b32 v197, s71, v221\n" \
  "s_xor_b32 s54, s54, s46\n" \
  "s_xor_b32 s94, s94, s46\n" \
  "v_mov_b64 v[64:65], 0\n" \
  "v_mov_b64 v[66:67], 0\n" \
  "v_mov_b64 v[68:69], 0\n" \
  "v_mov_b64 v[70:71], 0\n" \
  "v_mov_b64 v[72:73], 0\n" \
  "v_mov_b64 v[74:75], 0\n" \
  "v_mov_b64 v[76:77], 0\n" \
  "v_mov_b64 v[78:79], 0\n" \
  "v_mov_b64 v[80:81], 0\n" \
  "v_mov_b64 v[82:83], 0\n" \
  "v_mov_b64 v[84:85], 0\n" \
  "v_mov_b64 v[86:87], 0\n" \
  "v_mov_b64 v[88:89], 0\n" \
  "v_mov_b64 v[90:91], 0\n" \
  "v_mov_b64 v[92:93], 0\n" \
  "v_mov_b64 v[94:95], 0\n" \
  "v_mov_b64 v[96:97], 0\n" \
  "v_mov_b64 v[98:99], 0\n" \
  "v_mov_b64 v[100:101], 0\n" \
  "v_mov_b64 v[102:103], 0\n" \
  "v_mov_b64 v[104:105], 0\n" \
  "v_mov_b64 v[106:107], 0\n" \
  "v_mov_b64 v[108:109], 0\n" \
  "v_mov_b64 v[110:111], 0\n" \
  "v_mov_b64 v[112:113], 0\n" \
  "v_mov_b64 v[114:115], 0\n" \
  "v_mov_b64 v[116:117], 0\n" \
  "v_mov_b64 v[118:119], 0\n" \
  "v_mov_b64 v[120:121], 0\n" \
  "v_mov_b64 v[122:123], 0\n" \
  "v_mov_b64 v[124:125], 0\n" \
  "v_mov_b64 v[126:127], 0\n" \
  "v_mov_b64 v[192:193], 0\n" \
  "v_mov_b64 v[194:195], 0\n" \
  "s_branch .LR_again_%=\n" \
  ".LR_bad1_%=:\n" \
  "v_max_u32 v196, v0, v1\n" \
  "v_max3_u32 v196, v196, v2, v3\n" \
  "v_max3_u32 v196, v196, v4, v5\n" \
  "v_max3_u32 v196, v196, v6, v7\n" \
  "v_max3_u32 v196, v196, v8, v9\n" \
  "v_max3_u32 v196, v196, v10, v11\n" \
  "v_max3_u32 v196, v196, v12, v13\n" \
  "v_max3_u32 v196, v196, v14, v15\n" \
  "v_max3_u32 v196, v196, v16, v17\n" \
  "v_max3_u32 v196, v196, v18, v19\n" \
  "v_max3_u32 v196, v196, v20, v21\n" \
  "v_max3_u32 v196, v196, v22, v23\n" \
  "v_max3_u32 v196, v196, v24, v25\n" \
  "v_max3_u32 v196, v196, v26, v27\n" \
  "v_max3_u32 v196, v196, v28, v29\n" \
  "v_max3_u32 v196, v196, v30, v31\n" \
  "v_max3_u32 v196, v196, v32, v33\n" \
  "v_max3_u32 v196, v196, v34, v35\n" \
  "v_max3_u32 v196, v196, v36, v37\n" \
  "v_max3_u32 v196, v196, v38, v39\n" \
  "v_max3_u32 v196, v196, v40, v41\n" \
  "v_max3_u32 v196, v196, v42, v43\n" \
  "v_max3_u32 v196, v196, v44, v45\n" \
  "v_max3_u32 v196, v196, v46, v47\n" \
  "v_max3_u32 v196, v196, v48, v49\n" \
  "v_max3_u32 v196, v196, v50, v51\n" \
  "v_max3_u32 v196, v196, v52, v53\n" \
  "v_max3_u32 v196, v196, v54, v55\n" \
  "v_max3_u32 v196, v196, v56, v57\n" \
  "v_max3_u32 v196, v196, v58, v59\n" \
  "v_max3_u32 v196, v196, v60, v61\n" \
  "v_max3_u32 v196, v196, v62, v63\n" \
  "v_max_u32 v197, v128, v129\n" \
  "v_max3_u32 v197, v197, v130, v131\n" \
  "v_max3_u32 v197, v197, v132, v133\n" \
  "v_max3_u32 v197, v197, v134, v135\n" \
  "v_max3_u32 v197, v197, v136, v137\n" \
  "v_max3_u32 v197, v197, v138, v139\n" \
  "v_max3_u32 v197, v197, v140, v141\n" \
  "v_max3_u32 v197, v197, v142, v143\n" \
  "v_max3_u32 v197, v197, v144, v145\n" \
  "v_max3_u32 v197, v197, v146, v147\n" \
  "v_max3_u32 v197, v197, v148, v149\n" \
  "v_max3_u32 v197, v197, v150, v151\n" \
  "v_max3_u32 v197, v197, v152, v153\n" \
  "v_max3_u32 v197, v197, v154, v155\n" \
  "v_max3_u32 v197, v197, v156, v157\n" \
  "v_max3_u32 v197, v197, v158, v159\n" \
  "v_max_u32 v204, v160, v161\n" \
  "v_max3_u32 v204, v204, v162, v163\n" \
  "v_max3_u32 v204, v204, v164, v165\n" \
  "v_max3_u32 v204, v204, v166, v167\n" \
  "v_max3_u32 v204, v204, v168, v169\n" \
  "v_max3_u32 v204, v204, v170, v171\n" \
  "v_max3_u32 v204, v204, v172, v173\n" \
  "v_max3_u32 v204, v204, v174, v175\n" \
  "v_max3_u32 v204, v204, v176, v177\n" \
  "v_max3_u32 v204, v204, v178, v179\n" \
  "v_max3_u32 v204, v204, v180, v181\n" \
  "v_max3_u32 v204, v204, v182, v183\n" \
  "v_max3_u32 v204, v204, v184, v185\n" \
  "v_max3_u32 v204, v204, v186, v187\n" \
  "v_max3_u32 v204, v204, v188, v189\n" \
  "v_max3_u32 v204, v204, v190, v191\n" \
  "v_max3_u32 v196, v196, v197, v204\n" \
  "v_cmp_eq_u32 vcc, -1, v196\n" \
  "s_cbranch_vccz .LR_nan_%=\n" \
  "s_add_u32 s88, s88, 1\n" \
  "v_mov_b32 v196, s88\n" \
  "v_mov_b32 v197, %21\n" \
  "buffer_store_dwordx2 v[196:197], off, s[40:43], %31 sc1\n" \
  "s_waitcnt vmcnt(0)\n" \
  "s_and_b32 s63, s70, 255\n" \
  "s_cbranch_scc1 .LR_redo_%=\n" \
  "buffer_load_dwordx2 v[196:197], off, s[40:43], %27 offset:128 sc1\n" \
  "s_waitcnt vmcnt(0)\n" \
  "v_readfirstlane_b32 s63, v197\n" \
  "s_cmp_eq_u32 s63, %21\n" \
  "s_cbranch_scc1 .LR_nan_%=\n" \
  "s_cmp_lt_u32 s70, 262144\n" \
  "s_cbranch_scc1 .LR_redo_%=\n" \
  "v_mov_b32 v196, 1\n" \
  "v_mov_b32 v197, %21\n" \
  "buffer_store_dwordx2 v[196:197], off, s[40:43], %27 offset:128 sc1\n" \
  ".LR_nan_%=:\n" \
  "v_min_u32 v194, -2, v194\n" \
  "s_branch .LR_good_%=\n" \
  ".LR_fu_%=:\n" \
  "s_add_u32 s60, s48, 0\n" \
  "s_lshl_b32 s62, s60, 7\n" \
  "s_add_u32 s62, s62, %33\n" \
  "v_mov_b32 v146, s62\n" \
  "s_sub_u32 s60, s60, 0\n" \
  "s_cmp_lt_u32 s60, 10\n" \
  "s_cselect_b64 s[76:77], -1, 0\n" \
  "v_cndmask_b32 v146, v220, v146, s[76:77]\n" \
  "buffer_load_dwordx2 v[140:141], v146, s[40:43], 0 offen sc1\n" \
  "s_waitcnt vmcnt(0)\n" \
  "v_cmp_ne_u32 vcc, %21, v141\n" \
  "s_nop 1\n" \
  "v_cndmask_b32 v140, v140, 0, vcc\n" \
  "v_max_i32 v140, v140, v218\n" \
  "v_cmp_ge_i32 vcc, s48, v140\n" \
  "s_cbranch_vccnz .LR_fu0_%=\n" \
  "s_cmp_lg_u32 %19, 3\n" \
  "s_cbranch_scc1 .LR_h8_%=\n" \
  "s_xor_b32 s60, s61, s75\n" \
  "buffer_load_dwordx4 v[40:43], v214, s[40:43], s60 offen sc1\n" \
  "s_xor_b32 s60, s60, 1024\n" \
  "buffer_load_dwordx4 v[44:47], v214, s[40:43], s60 offen sc1\n" \
  ".LR_h8_%=:\n" \
  "s_xor_b32 s60, s61, s64\n" \
  "buffer_load_dwordx4 v[0:3], v214, s[40:43], s60 offen sc1\n" \
  "s_xor_b32 s60, s60, 1024\n" \
  "buffer_load_dwordx4 v[4:7], v214, s[40:43], s60 offen sc1\n" \
  "s_xor_b32 s60, s61, s65\n" \
  "buffer_load_dwordx4 v[8:11], v214, s[40:43], s60 offen sc1\n" \
  "s_xor_b32 s60, s60, 1024\n" \
  "buffer_load_dwordx4 v[12:15], v214, s[40:43], s60 offen sc1\n" \
  "s_xor_b32 s60, s61, s67\n" \
  "buffer_load_dwordx4 v[16:19], v214, s[40:43], s60 offen sc1\n" \
  "s_xor_b32 s60, s60, 1024\n" \
  "buffer_load_dwordx4 v[20:23], v214, s[40:43], s60 offen sc1\n" \
  "s_cmp_lt_u32 s48, 62\n" \
  "s_cselect_b32 s60, s71, 2130706432\n" \
  "v_xor_b32 v142, s60, v221\n" \
  "v_xor_b32 v143, s60, v222\n" \
  "v_xor_b32 v144, s60, v223\n" \
  "buffer_store_dwordx4 v[136:139], v142, s[40:43], 0 offen sc1\n" \
  "buffer_store_dwordx4 v[136:139], v143, s[40:43], 0 offen sc1\n" \
  "buffer_store_dwordx4 v[136:139], v144, s[40:43], 0 offen sc1\n" \
  "s_barrier\n" \
  "s_xor_b32 s60, s61, s68\n" \
  "buffer_load_dwordx4 v[24:27], v214, s[40:43], s60 offen sc1\n" \
  "s_xor_b32 s60, s60, 1024\n" \
  "buffer_load_dwordx4 v[28:31], v214, s[40:43], s60 offen sc1\n" \
  "s_xor_b32 s60, s61, s74\n" \
  "buffer_load_dwordx4 v[32:35], v214, s[40:43], s60 offen sc1\n" \
  "s_xor_b32 s60, s60, 1024\n" \
  "buffer_load_dwordx4 v[36:39], v214, s[40:43], s60 offen sc1\n" \
  "s_add_u32 s60, s48, 1\n" \
  "s_lshl_b32 s62, s60, 7\n" \
  "s_add_u32 s62, s62, %33\n" \
  "v_mov_b32 v146, s62\n" \
  "s_sub_u32 s60, s60, 0\n" \
  "s_cmp_lt_u32 s60, 10\n" \
  "s_cselect_b64 s[76:77], -1, 0\n" \
  "v_cndmask_b32 v146, v220, v146, s[76:77]\n" \
  "buffer_load_dwordx2 v[140:141], v146, s[40:43], 0 offen sc1\n" \
  "s_waitcnt vmcnt(0)\n" \
  "v_mov_b32 v72, v24\n" \
  "v_mov_b32 v73, v28\n" \
  "v_mov_b32 v74, v25\n" \
  "v_mov_b32 v75, v29\n" \
  "v_mov_b32 v76, v26\n" \
  "v_mov_b32 v77, v30\n" \
  "v_mov_b32 v78, v27\n" \
  "v_mov_b32 v79, v31\n" \
  "ds_write_b128 v211, v[72:75] offset:6144\n" \
  "ds_write_b128 v211, v[76:79] offset:7168\n" \
  "v_mov_b32 v80, v32\n" \
  "v_mov_b32 v81, v36\n" \
  "v_mov_b32 v82, v33\n" \
  "v_mov_b32 v83, v37\n" \
  "v_mov_b32 v84, v34\n" \
  "v_mov_b32 v85, v38\n" \
  "v_mov_b32 v86, v35\n" \
  "v_mov_b32 v87, v39\n" \
  "ds_write_b128 v211, v[80:83] offset:8192\n" \
  "ds_write_b128 v211, v[84:87] offset:9216\n" \
  "s_branch .LR_fu_b_%=\n" \
  ".LR_fu0_%=:\n" \
  "s_cmp_lt_u32 s48, 62\n" \
  "s_cselect_b32 s60, s71, 2130706432\n" \
  "v_xor_b32 v142, s60, v221\n" \
  "v_xor_b32 v143, s60, v222\n" \
  "v_xor_b32 v144, s60, v223\n" \
  "buffer_store_dwordx4 v[136:139], v142, s[40:43], 0 offen sc1\n" \
  "buffer_store_dwordx4 v[136:139], v143, s[40:43], 0 offen sc1\n" \
  "buffer_store_dwordx4 v[136:139], v144, s[40:43], 0 offen sc1\n" \
  "s_barrier\n" \
  "s_sleep 0\n" \
  "s_mov_b32 s69, 0\n" \
  ".LR_fu1_%=:\n" \
  "s_add_u32 s60, s48, 0\n" \
  "s_lshl_b32 s62, s60, 7\n" \
  "s_add_u32 s62, s62, %33\n" \
  "v_mov_b32 v146, s62\n" \
  "s_sub_u32 s60, s60, 0\n" \
  "s_cmp_lt_u32 s60, 10\n" \
  "s_cselect_b64 s[76:77], -1, 0\n" \
  "v_cndmask_b32 v146, v220, v146, s[76:77]\n" \
  "buffer_load_dwordx2 v[140:141], v146, s[40:43], 0 offen sc1\n" \
  "s_waitcnt vmcnt(0)\n" \
  "v_cmp_ne_u32 vcc, %21, v141\n" \
  "s_nop 1\n" \
  "v_cndmask_b32 v140, v140, 0, vcc\n" \
  "v_max_i32 v140, v140, v218\n" \
  "v_cmp_ge_i32 vcc, s48, v140\n" \
  "s_cbranch_vccz .LR_fu_x_%=\n" \
  "s_sleep 2\n" \
  "s_add_u32 s69, s69, 1\n" \
  "s_and_b32 s63, s69, 1023\n" \
  "s_cbranch_scc1 .LR_fu1_%=\n" \
  "s_cmp_gt_u32 s69, 2097152\n" \
  "s_cbranch_scc1 .LR_fu1_set_%=\n" \
  "buffer_load_dwordx2 v[196:197], off, s[40:43], %27 offset:128 sc1\n" \
  "s_waitcnt vmcnt(0)\n" \
  "v_readfirstlane_b32 s63, v197\n" \
  "s_cmp_eq_u32 s63, %21\n" \
  "s_cbranch_scc0 .LR_fu1_%=\n" \
  "s_branch .LR_fu_x_%=\n" \
  ".LR_fu1_set_%=:\n" \
  "v_mov_b32 v196, 1\n" \
  "v_mov_b32 v197, %21\n" \
  "buffer_store_dwordx2 v[196:197], off, s[40:43], %27 offset:128 sc1\n" \
  "s_waitcnt vmcnt(0)\n" \
  "s_branch .LR_fu_x_%=\n" \
  ".LR_fu_x_%=:\n" \
  "s_xor_b32 s60, s61, s68\n" \
  "buffer_load_dwordx4 v[24:27], v214, s[40:43], s60 offen sc1\n" \
  "s_xor_b32 s60, s60, 1024\n" \
  "buffer_load_dwordx4 v[28:31], v214, s[40:43], s60 offen sc1\n" \
  "s_xor_b32 s60, s61, s74\n" \
  "buffer_load_dwordx4 v[32:35], v214, s[40:43], s60 offen sc1\n" \
  "s_xor_b32 s60, s60, 1024\n" \
  "buffer_load_dwordx4 v[36:39], v214, s[40:43], s60 offen sc1\n" \
  "s_cmp_lg_u32 %19, 3\n" \
  "s_cbranch_scc1 .LR_h7_%=\n" \
  "s_xor_b32 s60, s61, s75\n" \
  "buffer_load_dwordx4 v[40:43], v214, s[40:43], s60 offen sc1\n" \
  "s_xor_b32 s60, s60, 1024\n" \
  "buffer_load_dwordx4 v[44:47], v214, s[40:43], s60 offen sc1\n" \
  ".LR_h7_%=:\n" \
  "s_xor_b32 s60, s61, s64\n" \
  "buffer_load_dwordx4 v[0:3], v214, s[40:43], s60 offen sc1\n" \
  "s_xor_b32 s60, s60, 1024\n" \
  "buffer_load_dwordx4 v[4:7], v214, s[40:43], s60 offen sc1\n" \
  "s_xor_b32 s60, s61, s65\n" \
  "buffer_load_dwordx4 v[8:11], v214, s[40:43], s60 offen sc1\n" \
  "s_xor_b32 s60, s60, 1024\n" \
  "buffer_load_dwordx4 v[12:15], v214, s[40:43], s60 offen sc1\n" \
  "s_xor_b32 s60, s61, s67\n" \
  "buffer_load_dwordx4 v[16:19], v214, s[40:43], s60 offen sc1\n" \
  "s_xor_b32 s60, s60, 1024\n" \
  "buffer_load_dwordx4 v[20:23], v214, s[40:43], s60 offen sc1\n" \
  "s_add_u32 s60, s48, 1\n" \
  "s_lshl_b32 s62, s60, 7\n" \
  "s_add_u32 s62, s62, %33\n" \
  "v_mov_b32 v146, s62\n" \
  "s_sub_u32 s60, s60, 0\n" \
  "s_cmp_lt_u32 s60, 10\n" \
  "s_cselect_b64 s[76:77], -1, 0\n" \
  "v_cndmask_b32 v146, v220, v146, s[76:77]\n" \
  "buffer_load_dwordx2 v[140:141], v146, s[40:43], 0 offen sc1\n" \
  "s_waitcnt vmcnt(7)\n" \
  "v_mov_b32 v72, v24\n" \
  "v_mov_b32 v73, v28\n" \
  "v_mov_b32 v74, v25\n" \
  "v_mov_b32 v75, v29\n" \
  "v_mov_b32 v76, v26\n" \
  "v_mov_b32 v77, v30\n" \
  "v_mov_b32 v78, v27\n" \
  "v_mov_b32 v79, v31\n" \
  "ds_write_b128 v211, v[72:75] offset:6144\n" \
  "ds_write_b128 v211, v[76:79] offset:7168\n" \
  "v_mov_b32 v80, v32\n" \
  "v_mov_b32 v81, v36\n" \
  "v_mov_b32 v82, v33\n" \
  "v_mov_b32 v83, v37\n" \
  "v_mov_b32 v84, v34\n" \
  "v_mov_b32 v85, v38\n" \
  "v_mov_b32 v86, v35\n" \
  "v_mov_b32 v87, v39\n" \
  "ds_write_b128 v211, v[80:83] offset:8192\n" \
  "ds_write_b128 v211, v[84:87] offset:9216\n" \
  "s_waitcnt vmcnt(0)\n" \
  "s_branch .LR_fu_b_%=\n" \
  ".LR_nb0_%=:\n" \
  "s_waitcnt vmcnt(0)\n" \
  "s_branch .LR_nb1_%=\n" \
  ".LR_help_%=:\n" \
  "s_mul_i32 s60, %19, 5\n" \
  "s_sub_u32 s60, s60, 5\n" \
  "s_lshl_b32 s61, s60, 11\n" \
  "s_add_u32 s61, s61, 8192\n" \
  "v_add_u32 v211, s61, v214\n" \
  "s_lshr_b32 s61, s60, 2\n" \
  "s_lshl_b32 s61, s61, 14\n" \
  "s_and_b32 s62, s60, 3\n" \
  "s_lshl_b32 s62, s62, 11\n" \
  "s_xor_b32 s61, s61, s62\n" \
  "s_xor_b32 s64, s61, %25\n" \
  "s_add_u32 s60, s60, 1\n" \
  "s_lshr_b32 s61, s60, 2\n" \
  "s_lshl_b32 s61, s61, 14\n" \
  "s_and_b32 s62, s60, 3\n" \
  "s_lshl_b32 s62, s62, 11\n" \
  "s_xor_b32 s61, s61, s62\n" \
  "s_xor_b32 s65, s61, %25\n" \
  "s_add_u32 s60, s60, 1\n" \
  "s_lshr_b32 s61, s60, 2\n" \
  "s_lshl_b32 s61, s61, 14\n" \
  "s_and_b32 s62, s60, 3\n" \
  "s_lshl_b32 s62, s62, 11\n" \
  "s_xor_b32 s61, s61, s62\n" \
  "s_xor_b32 s67, s61, %25\n" \
  "s_add_u32 s60, s60, 1\n" \
  "s_lshr_b32 s61, s60, 2\n" \
  "s_lshl_b32 s61, s61, 14\n" \
  "s_and_b32 s62, s60, 3\n" \
  "s_lshl_b32 s62, s62, 11\n" \
  "s_xor_b32 s61, s61, s62\n" \
  "s_xor_b32 s68, s61, %25\n" \
  "s_add_u32 s60, s60, 1\n" \
  "s_lshr_b32 s61, s60, 2\n" \
  "s_lshl_b32 s61, s61, 14\n" \
  "s_and_b32 s62, s60, 3\n" \
  "s_lshl_b32 s62, s62, 11\n" \
  "s_xor_b32 s61, s61, s62\n" \
  "s_xor_b32 s74, s61, %25\n" \
  "s_add_u32 s60, s60, 1\n" \
  "s_lshr_b32 s61, s60, 2\n" \
  "s_lshl_b32 s61, s61, 14\n" \
  "s_and_b32 s62, s60, 3\n" \
  "s_lshl_b32 s62, s62, 11\n" \
  "s_xor_b32 s61, s61, s62\n" \
  "s_xor_b32 s75, s61, %25\n" \
  "s_add_u32 s60, s60, 1\n" \
  "s_xor_b32 s71, s56, s57\n" \
  "s_movk_i32 s88, -32768\n" \
  "v_mov_b64 v[136:137], -1\n" \
  "v_mov_b64 v[138:139], -1\n" \
  "v_mov_b64 v[140:141], 0\n" \
  "s_mov_b64 vcc, -1\n" \
  "s_barrier\n" \
  ".LR_hl_%=:\n" \
  "s_mul_i32 s61, s48, 8454144\n" \
  "s_and_b32 s61, s61, 983040\n" \
  "s_bfe_u32 s60, %32, 524288\n" \
  ".LR_hs_%=:\n" \
  "s_cmp_eq_u32 s60, 0\n" \
  "s_cbranch_scc1 .LR_hs_x_%=\n" \
  "s_sleep 1\n" \
  "s_sub_u32 s60, s60, 1\n" \
  "s_branch .LR_hs_%=\n" \
  ".LR_hs_x_%=:\n" \
  "s_xor_b32 s60, s71, s57\n" \
  "v_xor_b32 v145, s60, v224\n" \
  "buffer_store_dwordx4 v[136:139], v145, s[40:43], 0 offen sc1\n" \
  "s_cbranch_vccnz .LR_fu_%=\n" \
  "s_cmp_lg_u32 %19, 3\n" \
  "s_cbranch_scc1 .LR_h5_%=\n" \
  "s_xor_b32 s60, s61, s75\n" \
  "buffer_load_dwordx4 v[40:43], v214, s[40:43], s60 offen sc1\n" \
  "s_xor_b32 s60, s60, 1024\n" \
  "buffer_load_dwordx4 v[44:47], v214, s[40:43], s60 offen sc1\n" \
  ".LR_h5_%=:\n" \
  "s_xor_b32 s60, s61, s64\n" \
  "buffer_load_dwordx4 v[0:3], v214, s[40:43], s60 offen sc1\n" \
  "s_xor_b32 s60, s60, 1024\n" \
  "buffer_load_dwordx4 v[4:7], v214, s[40:43], s60 offen sc1\n" \
  "s_xor_b32 s60, s61, s65\n" \
  "buffer_load_dwordx4 v[8:11], v214, s[40:43], s60 offen sc1\n" \
  "s_xor_b32 s60, s60, 1024\n" \
  "buffer_load_dwordx4 v[12:15], v214, s[40:43], s60 offen sc1\n" \
  "s_xor_b32 s60, s61, s67\n" \
  "buffer_load_dwordx4 v[16:19], v214, s[40:43], s60 offen sc1\n" \
  "s_xor_b32 s60, s60, 1024\n" \
  "buffer_load_dwordx4 v[20:23], v214, s[40:43], s60 offen sc1\n" \
  "s_cmp_lt_u32 s48, 62\n" \
  "s_cselect_b32 s60, s71, 2130706432\n" \
  "v_xor_b32 v142, s60, v221\n" \
  "v_xor_b32 v143, s60, v222\n" \
  "v_xor_b32 v144, s60, v223\n" \
  "buffer_store_dwordx4 v[136:139], v142, s[40:43], 0 offen sc1\n" \
  "buffer_store_dwordx4 v[136:139], v143, s[40:43], 0 offen sc1\n" \
  "buffer_store_dwordx4 v[136:139], v144, s[40:43], 0 offen sc1\n" \
  "s_waitcnt vmcnt(9)\n" \
  "v_mov_b32 v72, v24\n" \
  "v_mov_b32 v73, v28\n" \
  "v_mov_b32 v74, v25\n" \
  "v_mov_b32 v75, v29\n" \
  "v_mov_b32 v76, v26\n" \
  "v_mov_b32 v77, v30\n" \
  "v_mov_b32 v78, v27\n" \
  "v_mov_b32 v79, v31\n" \
  "ds_write_b128 v211, v[72:75] offset:6144\n" \
  "ds_write_b128 v211, v[76:79] offset:7168\n" \
  "v_mov_b32 v80, v32\n" \
  "v_mov_b32 v81, v36\n" \
  "v_mov_b32 v82, v33\n" \
  "v_mov_b32 v83, v37\n" \
  "v_mov_b32 v84, v34\n" \
  "v_mov_b32 v85, v38\n" \
  "v_mov_b32 v86, v35\n" \
  "v_mov_b32 v87, v39\n" \
  "ds_write_b128 v211, v[80:83] offset:8192\n" \
  "ds_write_b128 v211, v[84:87] offset:9216\n" \
  "s_barrier\n" \
  ".LR_fu_b_%=:\n" \
  "s_add_u32 s48, s48, 1\n" \
  "v_cmp_ne_u32 vcc, %21, v141\n" \
  "s_nop 1\n" \
  "v_cndmask_b32 v140, v140, 0, vcc\n" \
  "v_max_i32 v140, v140, v218\n" \
  "v_cmp_ge_i32 vcc, s48, v140\n" \
  "s_mul_i32 s61, s48, 8454144\n" \
  "s_and_b32 s61, s61, 983040\n" \
  "s_cbranch_vccnz .LR_nb0_%=\n" \
  "s_bfe_u32 s60, %32, 524296\n" \
  ".LR_ht_%=:\n" \
  "s_cmp_eq_u32 s60, 0\n" \
  "s_cbranch_scc1 .LR_ht_x_%=\n" \
  "s_sleep 1\n" \
  "s_sub_u32 s60, s60, 1\n" \
  "s_branch .LR_ht_%=\n" \
  ".LR_ht_x_%=:\n" \
  "s_xor_b32 s60, s61, s68\n" \
  "buffer_load_dwordx4 v[24:27], v214, s[40:43], s60 offen sc1\n" \
  "s_xor_b32 s60, s60, 1024\n" \
  "buffer_load_dwordx4 v[28:31], v214, s[40:43], s60 offen sc1\n" \
  "s_xor_b32 s60, s61, s74\n" \
  "buffer_load_dwordx4 v[32:35], v214, s[40:43], s60 offen sc1\n" \
  "s_xor_b32 s60, s60, 1024\n" \
  "buffer_load_dwordx4 v[36:39], v214, s[40:43], s60 offen sc1\n" \
  "s_waitcnt vmcnt(4)\n" \
  ".LR_nb1_%=:\n" \
  "s_add_u32 s60, s48, 3\n" \
  "s_min_u32 s60, s60, 63\n" \
  "v_cmp_ge_i32 s[76:77], s60, v140\n" \
  "s_nop 1\n" \
  "s_cmp_eq_u64 s[76:77], 0\n" \
  "s_cbranch_scc1 .LR_nf_%=\n" \
  "s_add_u32 s60, s48, 1\n" \
  "s_lshl_b32 s62, s60, 7\n" \
  "s_add_u32 s62, s62, %33\n" \
  "v_mov_b32 v146, s62\n" \
  "s_sub_u32 s60, s60, 0\n" \
  "s_cmp_lt_u32 s60, 10\n" \
  "s_cselect_b64 s[76:77], -1, 0\n" \
  "v_cndmask_b32 v146, v220, v146, s[76:77]\n" \
  "buffer_load_dwordx2 v[140:141], v146, s[40:43], 0 offen sc1\n" \
  ".LR_nf_%=:\n" \
  "v_mov_b32 v48, v0\n" \
  "v_mov_b32 v49, v4\n" \
  "v_mov_b32 v50, v1\n" \
  "v_mov_b32 v51, v5\n" \
  "v_mov_b32 v52, v2\n" \
  "v_mov_b32 v53, v6\n" \
  "v_mov_b32 v54, v3\n" \
  "v_mov_b32 v55, v7\n" \
  "ds_write_b128 v211, v[48:51] offset:0\n" \
  "ds_write_b128 v211, v[52:55] offset:1024\n" \
  "v_mov_b32 v56, v8\n" \
  "v_mov_b32 v57, v12\n" \
  "v_mov_b32 v58, v9\n" \
  "v_mov_b32 v59, v13\n" \
  "v_mov_b32 v60, v10\n" \
  "v_mov_b32 v61, v14\n" \
  "v_mov_b32 v62, v11\n" \
  "v_mov_b32 v63, v15\n" \
  "ds_write_b128 v211, v[56:59] offset:2048\n" \
  "ds_write_b128 v211, v[60:63] offset:3072\n" \
  "v_mov_b32 v64, v16\n" \
  "v_mov_b32 v65, v20\n" \
  "v_mov_b32 v66, v17\n" \
  "v_mov_b32 v67, v21\n" \
  "v_mov_b32 v68, v18\n" \
  "v_mov_b32 v69, v22\n" \
  "v_mov_b32 v70, v19\n" \
  "v_mov_b32 v71, v23\n" \
  "ds_write_b128 v211, v[64:67] offset:4096\n" \
  "ds_write_b128 v211, v[68:71] offset:5120\n" \
  "s_cmp_lg_u32 %19, 3\n" \
  "s_cbranch_scc1 .LR_h6_%=\n" \
  "v_mov_b32 v88, v40\n" \
  "v_mov_b32 v89, v44\n" \
  "v_mov_b32 v90, v41\n" \
  "v_mov_b32 v91, v45\n" \
  "v_mov_b32 v92, v42\n" \
  "v_mov_b32 v93, v46\n" \
  "v_mov_b32 v94, v43\n" \
  "v_mov_b32 v95, v47\n" \
  "ds_write_b128 v211, v[88:91] offset:10240\n" \
  "ds_write_b128 v211, v[92:95] offset:11264\n" \
  ".LR_h6_%=:\n" \
  "s_sub_u32 s88, 0, s88\n" \
  "s_xor_b32 s71, s71, s56\n" \
  "s_mov_b32 s61, s56\n" \
  "s_mov_b32 s56, s57\n" \
  "s_mov_b32 s57, s61\n" \
  "s_waitcnt lgkmcnt(0)\n" \
  "v_add_u32 v211, s88, v211\n" \
  "s_barrier\n" \
  "s_cmp_lt_u32 s48, 63\n" \
  "s_cbranch_scc1 .LR_hl_%=\n" \
  "s_barrier\n" \
  ".LR_end_%=:\n" \
  "s_waitcnt vmcnt(0)\n"
#define R_ASM_VOPS "+{v212}"(ri.sidx), "+{v213}"(ri.cidx), "+{v214}"(ri.l16), "+{v215}"(ri.ax), "+{v216}"(ri.am), "+{v217}"(ri.mo), "+{v218}"(ri.sel), "+{v219}"(ri.to), "+{v220}"(ri.fu), "+{v221}"(ri.p0), "+{v222}"(ri.p1), "+{v223}"(ri.p2), "+{v224}"(ri.p3), "+{v225}"(ri.d0), "+{v226}"(ri.d1), "+{v227}"(ri.d2), "+{v228}"(ri.d3), "+{v229}"(ri.d4)
#define R_ASM_SOPS "s"(ri.ws), "s"(ri.w), "s"(ri.go), "s"(ri.tag), "s"(ri.o_va), "s"(ri.o_vb), "s"(ri.lab), "s"(ri.o_u), "s"(ri.kind), "s"(ri.o_ctl), "s"(ri.pa), "s"(ri.pb), "s"(ri.f_d), "s"(ri.f_r), "s"(ri.ph), "s"(ri.f_l)
#define R_ASM_CLOB "v0", "v1", "v2", "v3", "v4", "v5", "v6", "v7", "v8", "v9", "v10", "v11", "v12", "v13", "v14", "v15", "v16", "v17", "v18", "v19", "v20", "v21", "v22", "v23", "v24", "v25", "v26", "v27", "v28", "v29", "v30", "v31", "v32", "v33", "v34", "v35", "v36", "v37", "v38", "v39", "v40", "v41", "v42", "v43", "v44", "v45", "v46", "v47", "v48", "v49", "v50", "v51", "v52", "v53", "v54", "v55", "v56", "v57", "v58", "v59", "v60", "v61", "v62", "v63", "v64", "v65", "v66", "v67", "v68", "v69", "v70", "v71", "v72", "v73", "v74", "v75", "v76", "v77", "v78", "v79", "v80", "v81", "v82", "v83", "v84", "v85", "v86", "v87", "v88", "v89", "v90", "v91", "v92", "v93", "v94", "v95", "v96", "v97", "v98", "v99", "v100", "v101", "v102", "v103", "v104", "v105", "v106", "v107", "v108", "v109", "v110", "v111", "v112", "v113", "v114", "v115", "v116", "v117", "v118", "v119", "v120", "v121", "v122", "v123", "v124", "v125", "v126", "v127", "v128", "v129", "v130", "v131", "v132", "v133", "v134", "v135", "v136", "v137", "v138", "v139", "v140", "v141", "v142", "v143", "v144", "v145", "v146", "v147", "v148", "v149", "v150", "v151", "v152", "v153", "v154", "v155", "v156", "v157", "v158", "v159", "v160", "v161", "v162", "v163", "v164", "v165", "v166", "v167", "v168", "v169", "v170", "v171", "v172", "v173", "v174", "v175", "v176", "v177", "v178", "v179", "v180", "v181", "v182", "v183", "v184", "v185", "v186", "v187", "v188", "v189", "v190", "v191", "v192", "v193", "v194", "v195", "v196", "v197", "v198", "v199", "v200", "v201", "v202", "v203", "v204", "v205", "v206", "v207", "v208", "v209", "v210", "v211", "a0", "a1", "a2", "a3", "a4", "a5", "a6", "a7", "a8", "a9", "a10", "a11", "a12", "a13", "a14", "a15", "a16", "a17", "a18", "a19", "a20", "a21", "a22", "a23", "a24", "a25", "a26", "a27", "a28", "a29", "a30", "a31", "a32", "a33", "a34", "a35", "a36", "a37", "a38", "a39", "a40", "a41", "a42", "a43", "a44", "a45", "a46", "a47", "a48", "a49", "a50", "a51", "a52", "a53", "a54", "a55", "a56", "a57", "a58", "a59", "a60", "a61", "a62", "a63", "a64", "a65", "a66", "a67", "a68", "a69", "a70", "a71", "a72", "a73", "a74", "a75", "a76", "a77", "a78", "a79", "a80", "a81", "a82", "a83", "a84", "a85", "a86", "a87", "a88", "a89", "a90", "a91", "a92", "a93", "a94", "a95", "a96", "a97", "a98", "a99", "a100", "a101", "a102", "a103", "a104", "a105", "a106", "a107", "a108", "a109", "a110", "a111", "a112", "a113", "a114", "a115", "a116", "a117", "a118", "a119", "a120", "a121", "a122", "a123", "a124", "a125", "a126", "a127", "s40", "s41", "s42", "s43", "s44", "s45", "s46", "s47", "s48", "s49", "s50", "s51", "s52", "s53", "s54", "s55", "s56", "s57", "s58", "s59", "s60", "s61", "s62", "s63", "s64", "s65", "s66", "s67", "s68", "s69", "s70", "s71", "s72", "s73", "s74", "s75", "s76", "s77", "s78", "s79", "s80", "s81", "s82", "s83", "s84", "s85", "s86", "s87", "s88", "s89", "s90", "s91", "s92", "s93", "s94", "s95", "s96", "s97", "s98", "s99", "vcc", "scc", "memory"
#define K_ASM_TEXT \
  "s_mov_b64 s[40:41], %30\n" \
  "s_and_b32 s41, s41, 0xffff\n" \
  "s_mov_b32 s42, 255852544\n" \
  "s_mov_b32 s43, 0x00020000\n" \
  "s_mov_b32 s47, 0x00020000\n" \
  "s_lshl_b32 s72, %34, 12\n" \
  "buffer_load_dword v184, off, s[40:43], %42 sc1\n" \
  "s_mov_b64 s[44:45], %31\n" \
  "s_and_b32 s45, s45, 0xffff\n" \
  "s_mov_b32 s46, 4718592\n" \
  "s_add_u32 s60, %35, 0\n" \
  "s_add_u32 m0, s72, 16384\n" \
  "s_nop 0\n" \
  "buffer_load_dwordx4 v170, s[44:47], s60 offen lds\n" \
  "buffer_load_dwordx4 v171, s[44:47], s60 offen offset:1024 lds\n" \
  "buffer_load_dwordx4 v172, s[44:47], s60 offen offset:2048 lds\n" \
  "buffer_load_dwordx4 v173, s[44:47], s60 offen offset:3072 lds\n" \
  "s_add_u32 s60, %35, 524288\n" \
  "s_add_u32 m0, s72, 32768\n" \
  "s_nop 0\n" \
  "buffer_load_dwordx4 v170, s[44:47], s60 offen lds\n" \
  "buffer_load_dwordx4 v171, s[44:47], s60 offen offset:1024 lds\n" \
  "buffer_load_dwordx4 v172, s[44:47], s60 offen offset:2048 lds\n" \
  "buffer_load_dwordx4 v173, s[44:47], s60 offen offset:3072 lds\n" \
  "s_add_u32 s60, %35, 1048576\n" \
  "s_add_u32 m0, s72, 49152\n" \
  "s_nop 0\n" \
  "buffer_load_dwordx4 v170, s[44:47], s60 offen lds\n" \
  "buffer_load_dwordx4 v171, s[44:47], s60 offen offset:1024 lds\n" \
  "buffer_load_dwordx4 v172, s[44:47], s60 offen offset:2048 lds\n" \
  "buffer_load_dwordx4 v173, s[44:47], s60 offen offset:3072 lds\n" \
  "s_add_u32 s60, %35, 1572864\n" \
  "s_add_u32 m0, s72, 65536\n" \
  "s_nop 0\n" \
  "buffer_load_dwordx4 v170, s[44:47], s60 offen lds\n" \
  "buffer_load_dwordx4 v171, s[44:47], s60 offen offset:1024 lds\n" \
  "buffer_load_dwordx4 v172, s[44:47], s60 offen offset:2048 lds\n" \
  "buffer_load_dwordx4 v173, s[44:47], s60 offen offset:3072 lds\n" \
  "s_add_u32 s60, %35, 2097152\n" \
  "s_add_u32 m0, s72, 81920\n" \
  "s_nop 0\n" \
  "buffer_load_dwordx4 v170, s[44:47], s60 offen lds\n" \
  "buffer_load_dwordx4 v171, s[44:47], s60 offen offset:1024 lds\n" \
  "buffer_load_dwordx4 v172, s[44:47], s60 offen offset:2048 lds\n" \
  "buffer_load_dwordx4 v173, s[44:47], s60 offen offset:3072 lds\n" \
  "s_add_u32 s60, %35, 2621440\n" \
  "s_add_u32 m0, s72, 98304\n" \
  "s_nop 0\n" \
  "buffer_load_dwordx4 v170, s[44:47], s60 offen lds\n" \
  "buffer_load_dwordx4 v171, s[44:47], s60 offen offset:1024 lds\n" \
  "buffer_load_dwordx4 v172, s[44:47], s60 offen offset:2048 lds\n" \
  "buffer_load_dwordx4 v173, s[44:47], s60 offen offset:3072 lds\n" \
  "s_add_u32 s60, %35, 3145728\n" \
  "s_add_u32 m0, s72, 114688\n" \
  "s_nop 0\n" \
  "buffer_load_dwordx4 v170, s[44:47], s60 offen lds\n" \
  "buffer_load_dwordx4 v171, s[44:47], s60 offen offset:1024 lds\n" \
  "buffer_load_dwordx4 v172, s[44:47], s60 offen offset:2048 lds\n" \
  "buffer_load_dwordx4 v173, s[44:47], s60 offen offset:3072 lds\n" \
  "s_add_u32 s60, %35, 3670016\n" \
  "s_add_u32 m0, s72, 131072\n" \
  "s_nop 0\n" \
  "buffer_load_dwordx4 v170, s[44:47], s60 offen lds\n" \
  "buffer_load_dwordx4 v171, s[44:47], s60 offen offset:1024 lds\n" \
  "buffer_load_dwordx4 v172, s[44:47], s60 offen offset:2048 lds\n" \
  "buffer_load_dwordx4 v173, s[44:47], s60 offen offset:3072 lds\n" \
  "s_add_u32 s60, %35, 4194304\n" \
  "s_add_u32 m0, s72, 147456\n" \
  "s_nop 0\n" \
  "buffer_load_dwordx4 v170, s[44:47], s60 offen lds\n" \
  "buffer_load_dwordx4 v171, s[44:47], s60 offen offset:1024 lds\n" \
  "buffer_load_dwordx4 v172, s[44:47], s60 offen offset:2048 lds\n" \
  "buffer_load_dwordx4 v173, s[44:47], s60 offen offset:3072 lds\n" \
  "s_waitcnt vmcnt(24)\n" \
  "s_barrier\n" \
  "v_add_u32 v186, 16384, v182\n" \
  "ds_read_b64_tr_b16 a[0:1], v186\n" \
  "ds_read_b64_tr_b16 a[2:3], v186 offset:1024\n" \
  "ds_read_b64_tr_b16 a[4:5], v186 offset:8192\n" \
  "ds_read_b64_tr_b16 a[6:7], v186 offset:9216\n" \
  "v_add_u32 v187, 16384, v183\n" \
  "ds_read_b64_tr_b16 a[72:73], v187\n" \
  "ds_read_b64_tr_b16 a[74:75], v187 offset:1024\n" \
  "ds_read_b64_tr_b16 a[76:77], v187 offset:8192\n" \
  "ds_read_b64_tr_b16 a[78:79], v187 offset:9216\n" \
  "v_add_u32 v186, 32768, v182\n" \
  "ds_read_b64_tr_b16 a[8:9], v186\n" \
  "ds_read_b64_tr_b16 a[10:11], v186 offset:1024\n" \
  "ds_read_b64_tr_b16 a[12:13], v186 offset:8192\n" \
  "ds_read_b64_tr_b16 a[14:15], v186 offset:9216\n" \
  "v_add_u32 v187, 32768, v183\n" \
  "ds_read_b64_tr_b16 a[80:81], v187\n" \
  "ds_read_b64_tr_b16 a[82:83], v187 offset:1024\n" \
  "ds_read_b64_tr_b16 a[84:85], v187 offset:8192\n" \
  "ds_read_b64_tr_b16 a[86:87], v187 offset:9216\n" \
  "v_add_u32 v186, 49152, v182\n" \
  "ds_read_b64_tr_b16 a[16:17], v186\n" \
  "ds_read_b64_tr_b16 a[18:19], v186 offset:1024\n" \
  "ds_read_b64_tr_b16 a[20:21], v186 offset:8192\n" \
  "ds_read_b64_tr_b16 a[22:23], v186 offset:9216\n" \
  "v_add_u32 v187, 49152, v183\n" \
  "ds_read_b64_tr_b16 a[88:89], v187\n" \
  "ds_read_b64_tr_b16 a[90:91], v187 offset:1024\n" \
  "ds_read_b64_tr_b16 a[92:93], v187 offset:8192\n" \
  "ds_read_b64_tr_b16 a[94:95], v187 offset:9216\n" \
  "s_waitcnt lgkmcnt(0)\n" \
  "s_barrier\n" \
  "v_readfirstlane_b32 s55, v184\n" \
  "s_add_u32 s55, s55, 1\n" \
  "s_mov_b64 s[44:45], %33\n" \
  "s_and_b32 s45, s45, 0xffff\n" \
  "s_mov_b32 s46, 2359296\n" \
  "s_mov_b32 s51, 0\n" \
  "s_mul_i32 s52, %34, 9216\n" \
  "s_add_u32 s52, s52, 16384\n" \
  "s_add_u32 m0, s52, 0\n" \
  "s_nop 0\n" \
  "buffer_load_dwordx4 v196, s[44:47], s51 offen lds\n" \
  "s_add_u32 m0, s52, 1024\n" \
  "s_nop 0\n" \
  "buffer_load_dwordx4 v197, s[44:47], s51 offen lds\n" \
  "s_add_u32 m0, s52, 2048\n" \
  "s_nop 0\n" \
  "buffer_load_dwordx4 v198, s[44:47], s51 offen lds\n" \
  "s_add_u32 m0, s52, 3072\n" \
  "s_nop 0\n" \
  "buffer_load_dwordx4 v199, s[44:47], s51 offen lds\n" \
  "s_add_u32 m0, s52, 4096\n" \
  "s_nop 0\n" \
  "buffer_load_dwordx4 v200, s[44:47], s51 offen lds\n" \
  "s_add_u32 m0, s52, 5120\n" \
  "s_nop 0\n" \
  "buffer_load_dwordx4 v201, s[44:47], s51 offen lds\n" \
  "s_add_u32 m0, s52, 6144\n" \
  "s_nop 0\n" \
  "buffer_load_dwordx4 v202, s[44:47], s51 offen lds\n" \
  "s_add_u32 m0, s52, 7168\n" \
  "s_nop 0\n" \
  "buffer_load_dwordx4 v203, s[44:47], s51 offen lds\n" \
  "s_add_u32 m0, s52, 8192\n" \
  "s_nop 0\n" \
  "buffer_load_dwordx4 v204, s[44:47], s51 offen lds\n" \
  "s_add_u32 s51, s51, 36864\n" \
  "s_min_u32 s51, s51, 2322432\n" \
  "s_add_u32 s52, s52, 36864\n" \
  "s_cmp_ge_u32 s52, 163840\n" \
  "s_cselect_b32 s60, 147456, 0\n" \
  "s_sub_u32 s52, s52, s60\n" \
  "s_waitcnt vmcnt(21)\n" \
  "s_barrier\n" \
  "v_add_u32 v186, 65536, v182\n" \
  "ds_read_b64_tr_b16 a[24:25], v186\n" \
  "ds_read_b64_tr_b16 a[26:27], v186 offset:1024\n" \
  "ds_read_b64_tr_b16 a[28:29], v186 offset:8192\n" \
  "ds_read_b64_tr_b16 a[30:31], v186 offset:9216\n" \
  "v_add_u32 v187, 65536, v183\n" \
  "ds_read_b64_tr_b16 a[96:97], v187\n" \
  "ds_read_b64_tr_b16 a[98:99], v187 offset:1024\n" \
  "ds_read_b64_tr_b16 a[100:101], v187 offset:8192\n" \
  "ds_read_b64_tr_b16 a[102:103], v187 offset:9216\n" \
  "v_add_u32 v186, 81920, v182\n" \
  "ds_read_b64_tr_b16 a[32:33], v186\n" \
  "ds_read_b64_tr_b16 a[34:35], v186 offset:1024\n" \
  "ds_read_b64_tr_b16 a[36:37], v186 offset:8192\n" \
  "ds_read_b64_tr_b16 a[38:39], v186 offset:9216\n" \
  "v_add_u32 v187, 81920, v183\n" \
  "ds_read_b64_tr_b16 a[104:105], v187\n" \
  "ds_read_b64_tr_b16 a[106:107], v187 offset:1024\n" \
  "ds_read_b64_tr_b16 a[108:109], v187 offset:8192\n" \
  "ds_read_b64_tr_b16 a[110:111], v187 offset:9216\n" \
  "v_add_u32 v186, 98304, v182\n" \
  "ds_read_b64_tr_b16 a[40:41], v186\n" \
  "ds_read_b64_tr_b16 a[42:43], v186 offset:1024\n" \
  "ds_read_b64_tr_b16 a[44:45], v186 offset:8192\n" \
  "ds_read_b64_tr_b16 a[46:47], v186 offset:9216\n" \
  "v_add_u32 v187, 98304, v183\n" \
  "ds_read_b64_tr_b16 a[112:113], v187\n" \
  "ds_read_b64_tr_b16 a[114:115], v187 offset:1024\n" \
  "ds_read_b64_tr_b16 a[116:117], v187 offset:8192\n" \
  "ds_read_b64_tr_b16 a[118:119], v187 offset:9216\n" \
  "s_waitcnt lgkmcnt(0)\n" \
  "s_barrier\n" \
  "buffer_load_dwordx2 v[184:185], off, s[40:43], %41 sc1\n" \
  "s_add_u32 m0, s52, 0\n" \
  "s_nop 0\n" \
  "buffer_load_dwordx4 v196, s[44:47], s51 offen lds\n" \
  "s_add_u32 m0, s52, 1024\n" \
  "s_nop 0\n" \
  "buffer_load_dwordx4 v197, s[44:47], s51 offen lds\n" \
  "s_add_u32 m0, s52, 2048\n" \
  "s_nop 0\n" \
  "buffer_load_dwordx4 v198, s[44:47], s51 offen lds\n" \
  "s_add_u32 m0, s52, 3072\n" \
  "s_nop 0\n" \
  "buffer_load_dwordx4 v199, s[44:47], s51 offen lds\n" \
  "s_add_u32 m0, s52, 4096\n" \
  "s_nop 0\n" \
  "buffer_load_dwordx4 v200, s[44:47], s51 offen lds\n" \
  "s_add_u32 m0, s52, 5120\n" \
  "s_nop 0\n" \
  "buffer_load_dwordx4 v201, s[44:47], s51 offen lds\n" \
  "s_add_u32 m0, s52, 6144\n" \
  "s_nop 0\n" \
  "buffer_load_dwordx4 v202, s[44:47], s51 offen lds\n" \
  "s_add_u32 m0, s52, 7168\n" \
  "s_nop 0\n" \
  "buffer_load_dwordx4 v203, s[44:47], s51 offen lds\n" \
  "s_add_u32 m0, s52, 8192\n" \
  "s_nop 0\n" \
  "buffer_load_dwordx4 v204, s[44:47], s51 offen lds\n" \
  "s_add_u32 s51, s51, 36864\n" \
  "s_min_u32 s51, s51, 2322432\n" \
  "s_add_u32 s52, s52, 36864\n" \
  "s_cmp_ge_u32 s52, 163840\n" \
  "s_cselect_b32 s60, 147456, 0\n" \
  "s_sub_u32 s52, s52, s60\n" \
  "s_waitcnt vmcnt(19)\n" \
  "s_barrier\n" \
  "v_add_u32 v186, 114688, v182\n" \
  "ds_read_b64_tr_b16 a[48:49], v186\n" \
  "ds_read_b64_tr_b16 a[50:51], v186 offset:1024\n" \
  "ds_read_b64_tr_b16 a[52:53], v186 offset:8192\n" \
  "ds_read_b64_tr_b16 a[54:55], v186 offset:9216\n" \
  "v_add_u32 v187, 114688, v183\n" \
  "ds_read_b64_tr_b16 a[120:121], v187\n" \
  "ds_read_b64_tr_b16 a[122:123], v187 offset:1024\n" \
  "ds_read_b64_tr_b16 a[124:125], v187 offset:8192\n" \
  "ds_read_b64_tr_b16 a[126:127], v187 offset:9216\n" \
  "v_add_u32 v186, 131072, v182\n" \
  "ds_read_b64_tr_b16 a[56:57], v186\n" \
  "ds_read_b64_tr_b16 a[58:59], v186 offset:1024\n" \
  "ds_read_b64_tr_b16 a[60:61], v186 offset:8192\n" \
  "ds_read_b64_tr_b16 a[62:63], v186 offset:9216\n" \
  "v_add_u32 v187, 131072, v183\n" \
  "ds_read_b64_tr_b16 a[128:129], v187\n" \
  "ds_read_b64_tr_b16 a[130:131], v187 offset:1024\n" \
  "ds_read_b64_tr_b16 a[132:133], v187 offset:8192\n" \
  "ds_read_b64_tr_b16 a[134:135], v187 offset:9216\n" \
  "v_add_u32 v186, 147456, v182\n" \
  "ds_read_b64_tr_b16 a[64:65], v186\n" \
  "ds_read_b64_tr_b16 a[66:67], v186 offset:1024\n" \
  "ds_read_b64_tr_b16 a[68:69], v186 offset:8192\n" \
  "ds_read_b64_tr_b16 a[70:71], v186 offset:9216\n" \
  "v_add_u32 v187, 147456, v183\n" \
  "ds_read_b64_tr_b16 a[136:137], v187\n" \
  "ds_read_b64_tr_b16 a[138:139], v187 offset:1024\n" \
  "ds_read_b64_tr_b16 a[140:141], v187 offset:8192\n" \
  "ds_read_b64_tr_b16 a[142:143], v187 offset:9216\n" \
  "s_waitcnt lgkmcnt(0)\n" \
  "s_mov_b32 s69, 0\n" \
  "s_waitcnt vmcnt(9)\n" \
  "s_branch .LK_go1_%=\n" \
  ".LK_go_%=:\n" \
  "buffer_load_dwordx2 v[184:185], off, s[40:43], %41 sc1\n" \
  "s_waitcnt vmcnt(0)\n" \
  ".LK_go1_%=:\n" \
  "v_readfirstlane_b32 s96, v184\n" \
  "v_readfirstlane_b32 s63, v185\n" \
  "s_cmp_eq_u32 s63, s55\n" \
  "s_cselect_b32 s96, s96, 0\n" \
  "s_cmp_ge_u32 s96, 0x80000000\n" \
  "s_cbranch_scc1 .LK_go_x_%=\n" \
  "s_sleep 4\n" \
  "s_add_u32 s69, s69, 1\n" \
  "s_cmp_lt_u32 s69, 2097152\n" \
  "s_cbranch_scc1 .LK_go_%=\n" \
  "v_mov_b32 v184, 1\n" \
  "v_mov_b32 v185, s55\n" \
  "buffer_store_dwordx2 v[184:185], off, s[40:43], %42 offset:128 sc1\n" \
  ".LK_go_x_%=:\n" \
  "s_and_b32 s60, s96, 2\n" \
  "s_lshl_b32 s60, s60, 12\n" \
  "s_xor_b32 s53, %39, s60\n" \
  "s_mov_b32 s54, %40\n" \
  "s_andn2_b32 s60, 2, s96\n" \
  "s_lshl_b32 s60, s60, 12\n" \
  "s_add_u32 s49, %37, s60\n" \
  "s_add_u32 s50, %38, s60\n" \
  "s_xor_b32 s66, %38, s60\n" \
  "s_xor_b32 s59, %44, s60\n" \
  "s_or_b32 s50, s50, 0x40000000\n" \
  "s_mov_b32 s57, 2130706432\n" \
  "s_mov_b32 s48, 0\n" \
  "v_mov_b32 v169, s55\n" \
  "v_mov_b32 v167, 2130706432\n" \
  "v_mov_b32 v163, 2130706432\n" \
  "v_mov_b32 v166, -1\n" \
  "s_mov_b64 exec, 3\n" \
  "v_mov_b32 v167, s54\n" \
  "s_mov_b64 exec, 1\n" \
  "v_mov_b32 v167, s53\n" \
  "v_mov_b32 v163, s53\n" \
  "v_mov_b32 v166, 0\n" \
  "s_mov_b64 exec, -1\n" \
  "s_barrier\n" \
  "ds_read_b128 v[32:35], v191\n" \
  "ds_read_b128 v[36:39], v191 offset:18432\n" \
  "ds_read_b128 v[40:43], v192\n" \
  "ds_read_b128 v[44:47], v192 offset:18432\n" \
  "ds_read_b128 v[48:51], v191 offset:128\n" \
  "ds_read_b128 v[52:55], v191 offset:18560\n" \
  "s_waitcnt lgkmcnt(0)\n" \
  "s_sub_u32 s49, s49, 524288\n" \
  "s_or_b32 s49, s49, 0x40000000\n" \
  "s_mov_b32 s56, -1\n" \
  "s_mov_b32 s57, 0x10000\n" \
  "s_mov_b32 s71, 36864\n" \
  ".LK_ma_%=:\n" \
  "s_waitcnt lgkmcnt(5)\n" \
  "v_mfma_f32_16x16x32_bf16 v[64:67], a[0:3], v[32:35], 0\n" \
  "ds_read_b128 v[56:59], v192 offset:128\n" \
  "s_waitcnt lgkmcnt(5)\n" \
  "v_mfma_f32_16x16x32_bf16 v[72:75], a[0:3], v[36:39], 0\n" \
  "ds_read_b128 v[60:63], v192 offset:18560\n" \
  "s_lshr_b32 s62, s49, 8\n" \
  "v_mfma_f32_16x16x32_bf16 v[68:71], a[72:75], v[32:35], 0\n" \
  "s_add_u32 m0, s52, 0\n" \
  "s_and_b32 s62, s62, 8192\n" \
  "s_xor_b32 s62, s62, s49\n" \
  "v_mfma_f32_16x16x32_bf16 v[76:79], a[72:75], v[36:39], 0\n" \
  "s_waitcnt lgkmcnt(5)\n" \
  "buffer_load_dwordx4 v196, s[44:47], s51 offen lds\n" \
  "v_mfma_f32_16x16x32_bf16 v[64:67], a[4:7], v[40:43], v[64:67]\n" \
  "ds_read_b128 v[32:35], v191 offset:256\n" \
  "s_waitcnt lgkmcnt(5)\n" \
  "v_cvt_pk_bf16_f32 v96, v80, v81\n" \
  "v_mfma_f32_16x16x32_bf16 v[72:75], a[4:7], v[44:47], v[72:75]\n" \
  "ds_read_b128 v[36:39], v191 offset:18688\n" \
  "v_cvt_pk_bf16_f32 v97, v82, v83\n" \
  "v_mfma_f32_16x16x32_bf16 v[68:71], a[76:79], v[40:43], v[68:71]\n" \
  "s_lshl_b32 s63, s57, 14\n" \
  "v_cvt_pk_bf16_f32 v98, v84, v85\n" \
  "v_cvt_pk_bf16_f32 v99, v86, v87\n" \
  "v_mfma_f32_16x16x32_bf16 v[76:79], a[76:79], v[44:47], v[76:79]\n" \
  "s_xor_b32 s63, s63, s59\n" \
  "s_waitcnt lgkmcnt(5)\n" \
  "v_mfma_f32_16x16x32_bf16 v[64:67], a[8:11], v[48:51], v[64:67]\n" \
  "ds_read_b128 v[40:43], v192 offset:256\n" \
  "s_waitcnt lgkmcnt(5)\n" \
  "v_mfma_f32_16x16x32_bf16 v[72:75], a[8:11], v[52:55], v[72:75]\n" \
  "ds_read_b128 v[44:47], v192 offset:18688\n" \
  "v_mfma_f32_16x16x32_bf16 v[68:71], a[80:83], v[48:51], v[68:71]\n" \
  "s_add_u32 m0, s52, 1024\n" \
  "s_waitcnt lgkmcnt(5)\n" \
  "v_mfma_f32_16x16x32_bf16 v[76:79], a[80:83], v[52:55], v[76:79]\n" \
  "buffer_load_dwordx4 v197, s[44:47], s51 offen lds\n" \
  "v_mfma_f32_16x16x32_bf16 v[64:67], a[12:15], v[56:59], v[64:67]\n" \
  "ds_read_b128 v[48:51], v191 offset:384\n" \
  "s_waitcnt lgkmcnt(5)\n" \
  "v_mfma_f32_16x16x32_bf16 v[72:75], a[12:15], v[60:63], v[72:75]\n" \
  "ds_read_b128 v[52:55], v191 offset:18816\n" \
  "v_mfma_f32_16x16x32_bf16 v[68:71], a[84:87], v[56:59], v[68:71]\n" \
  "buffer_store_dwordx4 v[96:99], v190, s[40:43], s62 offen sc1\n" \
  "v_mfma_f32_16x16x32_bf16 v[76:79], a[84:87], v[60:63], v[76:79]\n" \
  "s_waitcnt lgkmcnt(5)\n" \
  "buffer_store_dwordx4 v[96:99], v190, s[40:43], s63 offen sc1\n" \
  "v_mfma_f32_16x16x32_bf16 v[64:67], a[16:19], v[32:35], v[64:67]\n" \
  "ds_read_b128 v[56:59], v192 offset:384\n" \
  "s_waitcnt lgkmcnt(5)\n" \
  "v_cvt_pk_bf16_f32 v100, v88, v89\n" \
  "v_mfma_f32_16x16x32_bf16 v[72:75], a[16:19], v[36:39], v[72:75]\n" \
  "ds_read_b128 v[60:63], v192 offset:18816\n" \
  "v_cvt_pk_bf16_f32 v101, v90, v91\n" \
  "v_mfma_f32_16x16x32_bf16 v[68:71], a[88:91], v[32:35], v[68:71]\n" \
  "s_add_u32 m0, s52, 2048\n" \
  "v_cvt_pk_bf16_f32 v102, v92, v93\n" \
  "v_cvt_pk_bf16_f32 v103, v94, v95\n" \
  "v_mfma_f32_16x16x32_bf16 v[76:79], a[88:91], v[36:39], v[76:79]\n" \
  "s_waitcnt lgkmcnt(5)\n" \
  "buffer_load_dwordx4 v198, s[44:47], s51 offen lds\n" \
  "v_mfma_f32_16x16x32_bf16 v[64:67], a[20:23], v[40:43], v[64:67]\n" \
  "ds_read_b128 v[32:35], v191 offset:512\n" \
  "s_waitcnt lgkmcnt(5)\n" \
  "v_mfma_f32_16x16x32_bf16 v[72:75], a[20:23], v[44:47], v[72:75]\n" \
  "ds_read_b128 v[36:39], v191 offset:18944\n" \
  "v_mfma_f32_16x16x32_bf16 v[68:71], a[92:95], v[40:43], v[68:71]\n" \
  "buffer_store_dwordx4 v[100:103], v190, s[40:43], s62 offen offset:1024 sc1\n" \
  "v_mfma_f32_16x16x32_bf16 v[76:79], a[92:95], v[44:47], v[76:79]\n" \
  "s_waitcnt lgkmcnt(5)\n" \
  "buffer_store_dwordx4 v[100:103], v190, s[40:43], s63 offen offset:1024 sc1\n" \
  "v_mfma_f32_16x16x32_bf16 v[64:67], a[24:27], v[48:51], v[64:67]\n" \
  "ds_read_b128 v[40:43], v192 offset:512\n" \
  "s_add_u32 s49, s49, 524288\n" \
  "s_waitcnt lgkmcnt(5)\n" \
  "v_mfma_f32_16x16x32_bf16 v[72:75], a[24:27], v[52:55], v[72:75]\n" \
  "ds_read_b128 v[44:47], v192 offset:18944\n" \
  "s_bitset0_b32 s49, 30\n" \
  "v_mfma_f32_16x16x32_bf16 v[68:71], a[96:99], v[48:51], v[68:71]\n" \
  "s_add_u32 m0, s52, 3072\n" \
  "s_waitcnt lgkmcnt(5)\n" \
  "v_mfma_f32_16x16x32_bf16 v[76:79], a[96:99], v[52:55], v[76:79]\n" \
  "buffer_load_dwordx4 v199, s[44:47], s51 offen lds\n" \
  "v_mfma_f32_16x16x32_bf16 v[64:67], a[28:31], v[56:59], v[64:67]\n" \
  "ds_read_b128 v[48:51], v191 offset:640\n" \
  "s_waitcnt lgkmcnt(5)\n" \
  "v_mfma_f32_16x16x32_bf16 v[72:75], a[28:31], v[60:63], v[72:75]\n" \
  "ds_read_b128 v[52:55], v191 offset:19072\n" \
  "v_mfma_f32_16x16x32_bf16 v[68:71], a[100:103], v[56:59], v[68:71]\n" \
  "s_cmp_eq_u32 s48, 20\n" \
  "s_cselect_b32 s60, 19922944, 0\n" \
  "s_add_u32 s49, s49, s60\n" \
  "v_mfma_f32_16x16x32_bf16 v[76:79], a[100:103], v[60:63], v[76:79]\n" \
  "s_add_u32 s58, s48, 1\n" \
  "s_and_b32 s58, s58, 3\n" \
  "s_cselect_b32 s58, 0, 147456\n" \
  "s_waitcnt lgkmcnt(5)\n" \
  "v_mfma_f32_16x16x32_bf16 v[64:67], a[32:35], v[32:35], v[64:67]\n" \
  "ds_read_b128 v[56:59], v192 offset:640\n" \
  "s_sub_u32 s58, 36864, s58\n" \
  "s_waitcnt lgkmcnt(5)\n" \
  "v_mfma_f32_16x16x32_bf16 v[72:75], a[32:35], v[36:39], v[72:75]\n" \
  "ds_read_b128 v[60:63], v192 offset:19072\n" \
  "v_add_u32 v165, s58, v191\n" \
  "v_mfma_f32_16x16x32_bf16 v[68:71], a[104:107], v[32:35], v[68:71]\n" \
  "s_add_u32 m0, s52, 4096\n" \
  "v_add_u32 v164, s58, v192\n" \
  "v_mfma_f32_16x16x32_bf16 v[76:79], a[104:107], v[36:39], v[76:79]\n" \
  "s_waitcnt lgkmcnt(5)\n" \
  "buffer_load_dwordx4 v200, s[44:47], s51 offen lds\n" \
  "v_mfma_f32_16x16x32_bf16 v[64:67], a[36:39], v[40:43], v[64:67]\n" \
  "ds_read_b128 v[32:35], v191 offset:768\n" \
  "s_waitcnt lgkmcnt(5)\n" \
  "v_mfma_f32_16x16x32_bf16 v[72:75], a[36:39], v[44:47], v[72:75]\n" \
  "ds_read_b128 v[36:39], v191 offset:19200\n" \
  "v_mfma_f32_16x16x32_bf16 v[68:71], a[108:111], v[40:43], v[68:71]\n" \
  "v_mfma_f32_16x16x32_bf16 v[76:79], a[108:111], v[44:47], v[76:79]\n" \
  "s_waitcnt lgkmcnt(5)\n" \
  "v_mfma_f32_16x16x32_bf16 v[64:67], a[40:43], v[48:51], v[64:67]\n" \
  "ds_read_b128 v[40:43], v192 offset:768\n" \
  "s_waitcnt lgkmcnt(5)\n" \
  "v_mfma_f32_16x16x32_bf16 v[72:75], a[40:43], v[52:55], v[72:75]\n" \
  "ds_read_b128 v[44:47], v192 offset:19200\n" \
  "v_mfma_f32_16x16x32_bf16 v[68:71], a[112:115], v[48:51], v[68:71]\n" \
  "s_add_u32 m0, s52, 5120\n" \
  "s_waitcnt lgkmcnt(5)\n" \
  "v_mfma_f32_16x16x32_bf16 v[76:79], a[112:115], v[52:55], v[76:79]\n" \
  "buffer_load_dwordx4 v201, s[44:47], s51 offen lds\n" \
  "v_mfma_f32_16x16x32_bf16 v[64:67], a[44:47], v[56:59], v[64:67]\n" \
  "ds_read_b128 v[48:51], v191 offset:896\n" \
  "s_waitcnt lgkmcnt(5)\n" \
  "v_mfma_f32_16x16x32_bf16 v[72:75], a[44:47], v[60:63], v[72:75]\n" \
  "ds_read_b128 v[52:55], v191 offset:19328\n" \
  "v_mfma_f32_16x16x32_bf16 v[68:71], a[116:119], v[56:59], v[68:71]\n" \
  "v_mfma_f32_16x16x32_bf16 v[76:79], a[116:119], v[60:63], v[76:79]\n" \
  "s_waitcnt lgkmcnt(5)\n" \
  "v_mfma_f32_16x16x32_bf16 v[64:67], a[48:51], v[32:35], v[64:67]\n" \
  "ds_read_b128 v[56:59], v192 offset:896\n" \
  "s_waitcnt lgkmcnt(5)\n" \
  "v_mfma_f32_16x16x32_bf16 v[72:75], a[48:51], v[36:39], v[72:75]\n" \
  "ds_read_b128 v[60:63], v192 offset:19328\n" \
  "v_mfma_f32_16x16x32_bf16 v[68:71], a[120:123], v[32:35], v[68:71]\n" \
  "s_add_u32 m0, s52, 6144\n" \
  "v_mov_b32 v168, s56\n" \
  "v_mfma_f32_16x16x32_bf16 v[76:79], a[120:123], v[36:39], v[76:79]\n" \
  "s_waitcnt lgkmcnt(5)\n" \
  "buffer_load_dwordx4 v202, s[44:47], s51 offen lds\n" \
  "v_mfma_f32_16x16x32_bf16 v[64:67], a[52:55], v[40:43], v[64:67]\n" \
  "ds_read_b128 v[32:35], v191 offset:1024\n" \
  "s_waitcnt lgkmcnt(5)\n" \
  "v_mfma_f32_16x16x32_bf16 v[72:75], a[52:55], v[44:47], v[72:75]\n" \
  "ds_read_b128 v[36:39], v191 offset:19456\n" \
  "v_mfma_f32_16x16x32_bf16 v[68:71], a[124:127], v[40:43], v[68:71]\n" \
  "s_waitcnt vmcnt(11)\n" \
  "s_barrier\n" \
  "v_mfma_f32_16x16x32_bf16 v[76:79], a[124:127], v[44:47], v[76:79]\n" \
  "s_waitcnt lgkmcnt(5)\n" \
  "buffer_store_dwordx2 v[168:169], v163, s[40:43], 0 offen sc1\n" \
  "v_mfma_f32_16x16x32_bf16 v[64:67], a[56:59], v[48:51], v[64:67]\n" \
  "ds_read_b128 v[40:43], v192 offset:1024\n" \
  "s_waitcnt lgkmcnt(5)\n" \
  "v_mfma_f32_16x16x32_bf16 v[72:75], a[56:59], v[52:55], v[72:75]\n" \
  "ds_read_b128 v[44:47], v192 offset:19456\n" \
  "v_mfma_f32_16x16x32_bf16 v[68:71], a[128:131], v[48:51], v[68:71]\n" \
  "s_add_u32 m0, s52, 7168\n" \
  "s_waitcnt lgkmcnt(5)\n" \
  "v_mfma_f32_16x16x32_bf16 v[76:79], a[128:131], v[52:55], v[76:79]\n" \
  "buffer_load_dwordx4 v203, s[44:47], s51 offen lds\n" \
  "v_mfma_f32_16x16x32_bf16 v[64:67], a[60:63], v[56:59], v[64:67]\n" \
  "ds_read_b128 v[48:51], v165\n" \
  "s_waitcnt lgkmcnt(5)\n" \
  "v_mfma_f32_16x16x32_bf16 v[72:75], a[60:63], v[60:63], v[72:75]\n" \
  "ds_read_b128 v[52:55], v165 offset:18432\n" \
  "v_mfma_f32_16x16x32_bf16 v[68:71], a[132:135], v[56:59], v[68:71]\n" \
  "s_min_u32 s56, s48, 20\n" \
  "s_cselect_b32 s57, 0, 38\n" \
  "s_add_u32 s57, s57, s48\n" \
  "v_mfma_f32_16x16x32_bf16 v[76:79], a[132:135], v[60:63], v[76:79]\n" \
  "s_add_u32 s48, s48, 1\n" \
  "v_mov_b32 v191, v165\n" \
  "v_mov_b32 v192, v164\n" \
  "s_waitcnt lgkmcnt(5)\n" \
  "v_mfma_f32_16x16x32_bf16 v[64:67], a[64:67], v[32:35], v[64:67]\n" \
  "ds_read_b128 v[56:59], v164\n" \
  "s_waitcnt lgkmcnt(5)\n" \
  "v_mfma_f32_16x16x32_bf16 v[72:75], a[64:67], v[36:39], v[72:75]\n" \
  "ds_read_b128 v[60:63], v164 offset:18432\n" \
  "v_mfma_f32_16x16x32_bf16 v[68:71], a[136:139], v[32:35], v[68:71]\n" \
  "s_add_u32 m0, s52, 8192\n" \
  "s_cmp_eq_u32 s51, 700416\n" \
  "s_cselect_b32 s70, 1437696, s71\n" \
  "v_mfma_f32_16x16x32_bf16 v[76:79], a[136:139], v[36:39], v[76:79]\n" \
  "s_waitcnt lgkmcnt(5)\n" \
  "buffer_load_dwordx4 v204, s[44:47], s51 offen lds\n" \
  "v_mfma_f32_16x16x32_bf16 v[64:67], a[68:71], v[40:43], v[64:67]\n" \
  "ds_read_b128 v[32:35], v165 offset:128\n" \
  "s_add_u32 s51, s51, s70\n" \
  "s_waitcnt lgkmcnt(5)\n" \
  "v_mfma_f32_16x16x32_bf16 v[72:75], a[68:71], v[44:47], v[72:75]\n" \
  "ds_read_b128 v[36:39], v165 offset:18560\n" \
  "s_min_u32 s51, s51, 2322432\n" \
  "v_mfma_f32_16x16x32_bf16 v[68:71], a[140:143], v[40:43], v[68:71]\n" \
  "s_add_u32 s52, s52, 36864\n" \
  "v_mfma_f32_16x16x32_bf16 v[76:79], a[140:143], v[44:47], v[76:79]\n" \
  "s_cmp_ge_u32 s52, 163840\n" \
  "s_cselect_b32 s60, 147456, 0\n" \
  "s_sub_u32 s52, s52, s60\n" \
  "s_cmp_le_u32 s48, 4\n" \
  "s_cbranch_scc1 .LK_mea_%=\n" \
  ".LK_mea_b_%=:\n" \
  "s_waitcnt lgkmcnt(5)\n" \
  "v_mfma_f32_16x16x32_bf16 v[80:83], a[0:3], v[48:51], 0\n" \
  "ds_read_b128 v[40:43], v192 offset:128\n" \
  "s_waitcnt lgkmcnt(5)\n" \
  "v_mfma_f32_16x16x32_bf16 v[88:91], a[0:3], v[52:55], 0\n" \
  "ds_read_b128 v[44:47], v192 offset:18560\n" \
  "s_lshr_b32 s62, s49, 8\n" \
  "v_mfma_f32_16x16x32_bf16 v[84:87], a[72:75], v[48:51], 0\n" \
  "s_add_u32 m0, s52, 0\n" \
  "s_and_b32 s62, s62, 8192\n" \
  "s_xor_b32 s62, s62, s49\n" \
  "v_mfma_f32_16x16x32_bf16 v[92:95], a[72:75], v[52:55], 0\n" \
  "s_waitcnt lgkmcnt(5)\n" \
  "buffer_load_dwordx4 v196, s[44:47], s51 offen lds\n" \
  "v_mfma_f32_16x16x32_bf16 v[80:83], a[4:7], v[56:59], v[80:83]\n" \
  "ds_read_b128 v[48:51], v191 offset:256\n" \
  "s_waitcnt lgkmcnt(5)\n" \
  "v_cvt_pk_bf16_f32 v96, v64, v65\n" \
  "v_mfma_f32_16x16x32_bf16 v[88:91], a[4:7], v[60:63], v[88:91]\n" \
  "ds_read_b128 v[52:55], v191 offset:18688\n" \
  "v_cvt_pk_bf16_f32 v97, v66, v67\n" \
  "v_mfma_f32_16x16x32_bf16 v[84:87], a[76:79], v[56:59], v[84:87]\n" \
  "s_lshl_b32 s63, s57, 14\n" \
  "v_cvt_pk_bf16_f32 v98, v68, v69\n" \
  "v_cvt_pk_bf16_f32 v99, v70, v71\n" \
  "v_mfma_f32_16x16x32_bf16 v[92:95], a[76:79], v[60:63], v[92:95]\n" \
  "s_xor_b32 s63, s63, s59\n" \
  "s_waitcnt lgkmcnt(5)\n" \
  "v_mfma_f32_16x16x32_bf16 v[80:83], a[8:11], v[32:35], v[80:83]\n" \
  "ds_read_b128 v[56:59], v192 offset:256\n" \
  "s_waitcnt lgkmcnt(5)\n" \
  "v_mfma_f32_16x16x32_bf16 v[88:91], a[8:11], v[36:39], v[88:91]\n" \
  "ds_read_b128 v[60:63], v192 offset:18688\n" \
  "v_mfma_f32_16x16x32_bf16 v[84:87], a[80:83], v[32:35], v[84:87]\n" \
  "s_add_u32 m0, s52, 1024\n" \
  "s_waitcnt lgkmcnt(5)\n" \
  "v_mfma_f32_16x16x32_bf16 v[92:95], a[80:83], v[36:39], v[92:95]\n" \
  "buffer_load_dwordx4 v197, s[44:47], s51 offen lds\n" \
  "v_mfma_f32_16x16x32_bf16 v[80:83], a[12:15], v[40:43], v[80:83]\n" \
  "ds_read_b128 v[32:35], v191 offset:384\n" \
  "s_waitcnt lgkmcnt(5)\n" \
  "v_mfma_f32_16x16x32_bf16 v[88:91], a[12:15], v[44:47], v[88:91]\n" \
  "ds_read_b128 v[36:39], v191 offset:18816\n" \
  "v_mfma_f32_16x16x32_bf16 v[84:87], a[84:87], v[40:43], v[84:87]\n" \
  "buffer_store_dwordx4 v[96:99], v190, s[40:43], s62 offen sc1\n" \
  "v_mfma_f32_16x16x32_bf16 v[92:95], a[84:87], v[44:47], v[92:95]\n" \
  "s_waitcnt lgkmcnt(5)\n" \
  "buffer_store_dwordx4 v[96:99], v190, s[40:43], s63 offen sc1\n" \
  "v_mfma_f32_16x16x32_bf16 v[80:83], a[16:19], v[48:51], v[80:83]\n" \
  "ds_read_b128 v[40:43], v192 offset:384\n" \
  "s_waitcnt lgkmcnt(5)\n" \
  "v_cvt_pk_bf16_f32 v100, v72, v73\n" \
  "v_mfma_f32_16x16x32_bf16 v[88:91], a[16:19], v[52:55], v[88:91]\n" \
  "ds_read_b128 v[44:47], v192 offset:18816\n" \
  "v_cvt_pk_bf16_f32 v101, v74, v75\n" \
  "v_mfma_f32_16x16x32_bf16 v[84:87], a[88:91], v[48:51], v[84:87]\n" \
  "s_add_u32 m0, s52, 2048\n" \
  "v_cvt_pk_bf16_f32 v102, v76, v77\n" \
  "v_cvt_pk_bf16_f32 v103, v78, v79\n" \
  "v_mfma_f32_16x16x32_bf16 v[92:95], a[88:91], v[52:55], v[92:95]\n" \
  "s_waitcnt lgkmcnt(5)\n" \
  "buffer_load_dwordx4 v198, s[44:47], s51 offen lds\n" \
  "v_mfma_f32_16x16x32_bf16 v[80:83], a[20:23], v[56:59], v[80:83]\n" \
  "ds_read_b128 v[48:51], v191 offset:512\n" \
  "s_waitcnt lgkmcnt(5)\n" \
  "v_mfma_f32_16x16x32_bf16 v[88:91], a[20:23], v[60:63], v[88:91]\n" \
  "ds_read_b128 v[52:55], v191 offset:18944\n" \
  "v_mfma_f32_16x16x32_bf16 v[84:87], a[92:95], v[56:59], v[84:87]\n" \
  "buffer_store_dwordx4 v[100:103], v190, s[40:43], s62 offen offset:1024 sc1\n" \
  "v_mfma_f32_16x16x32_bf16 v[92:95], a[92:95], v[60:63], v[92:95]\n" \
  "s_waitcnt lgkmcnt(5)\n" \
  "buffer_store_dwordx4 v[100:103], v190, s[40:43], s63 offen offset:1024 sc1\n" \
  "v_mfma_f32_16x16x32_bf16 v[80:83], a[24:27], v[32:35], v[80:83]\n" \
  "ds_read_b128 v[56:59], v192 offset:512\n" \
  "s_add_u32 s49, s49, 524288\n" \
  "s_waitcnt lgkmcnt(5)\n" \
  "v_mfma_f32_16x16x32_bf16 v[88:91], a[24:27], v[36:39], v[88:91]\n" \
  "ds_read_b128 v[60:63], v192 offset:18944\n" \
  "s_bitset0_b32 s49, 30\n" \
  "v_mfma_f32_16x16x32_bf16 v[84:87], a[96:99], v[32:35], v[84:87]\n" \
  "s_add_u32 m0, s52, 3072\n" \
  "s_waitcnt lgkmcnt(5)\n" \
  "v_mfma_f32_16x16x32_bf16 v[92:95], a[96:99], v[36:39], v[92:95]\n" \
  "buffer_load_dwordx4 v199, s[44:47], s51 offen lds\n" \
  "v_mfma_f32_16x16x32_bf16 v[80:83], a[28:31], v[40:43], v[80:83]\n" \
  "ds_read_b128 v[32:35], v191 offset:640\n" \
  "s_waitcnt lgkmcnt(5)\n" \
  "v_mfma_f32_16x16x32_bf16 v[88:91], a[28:31], v[44:47], v[88:91]\n" \
  "ds_read_b128 v[36:39], v191 offset:19072\n" \
  "v_mfma_f32_16x16x32_bf16 v[84:87], a[100:103], v[40:43], v[84:87]\n" \
  "s_cmp_eq_u32 s48, 20\n" \
  "s_cselect_b32 s60, 19922944, 0\n" \
  "s_add_u32 s49, s49, s60\n" \
  "v_mfma_f32_16x16x32_bf16 v[92:95], a[100:103], v[44:47], v[92:95]\n" \
  "s_add_u32 s58, s48, 1\n" \
  "s_and_b32 s58, s58, 3\n" \
  "s_cselect_b32 s58, 0, 147456\n" \
  "s_waitcnt lgkmcnt(5)\n" \
  "v_mfma_f32_16x16x32_bf16 v[80:83], a[32:35], v[48:51], v[80:83]\n" \
  "ds_read_b128 v[40:43], v192 offset:640\n" \
  "s_sub_u32 s58, 36864, s58\n" \
  "s_waitcnt lgkmcnt(5)\n" \
  "v_mfma_f32_16x16x32_bf16 v[88:91], a[32:35], v[52:55], v[88:91]\n" \
  "ds_read_b128 v[44:47], v192 offset:19072\n" \
  "v_add_u32 v165, s58, v191\n" \
  "v_mfma_f32_16x16x32_bf16 v[84:87], a[104:107], v[48:51], v[84:87]\n" \
  "s_add_u32 m0, s52, 4096\n" \
  "v_add_u32 v164, s58, v192\n" \
  "v_mfma_f32_16x16x32_bf16 v[92:95], a[104:107], v[52:55], v[92:95]\n" \
  "s_waitcnt lgkmcnt(5)\n" \
  "buffer_load_dwordx4 v200, s[44:47], s51 offen lds\n" \
  "v_mfma_f32_16x16x32_bf16 v[80:83], a[36:39], v[56:59], v[80:83]\n" \
  "ds_read_b128 v[48:51], v191 offset:768\n" \
  "s_waitcnt lgkmcnt(5)\n" \
  "v_mfma_f32_16x16x32_bf16 v[88:91], a[36:39], v[60:63], v[88:91]\n" \
  "ds_read_b128 v[52:55], v191 offset:19200\n" \
  "v_mfma_f32_16x16x32_bf16 v[84:87], a[108:111], v[56:59], v[84:87]\n" \
  "v_mfma_f32_16x16x32_bf16 v[92:95], a[108:111], v[60:63], v[92:95]\n" \
  "s_waitcnt lgkmcnt(5)\n" \
  "v_mfma_f32_16x16x32_bf16 v[80:83], a[40:43], v[32:35], v[80:83]\n" \
  "ds_read_b128 v[56:59], v192 offset:768\n" \
  "s_waitcnt lgkmcnt(5)\n" \
  "v_mfma_f32_16x16x32_bf16 v[88:91], a[40:43], v[36:39], v[88:91]\n" \
  "ds_read_b128 v[60:63], v192 offset:19200\n" \
  "v_mfma_f32_16x16x32_bf16 v[84:87], a[112:115], v[32:35], v[84:87]\n" \
  "s_add_u32 m0, s52, 5120\n" \
  "s_waitcnt lgkmcnt(5)\n" \
  "v_mfma_f32_16x16x32_bf16 v[92:95], a[112:115], v[36:39], v[92:95]\n" \
  "buffer_load_dwordx4 v201, s[44:47], s51 offen lds\n" \
  "v_mfma_f32_16x16x32_bf16 v[80:83], a[44:47], v[40:43], v[80:83]\n" \
  "ds_read_b128 v[32:35], v191 offset:896\n" \
  "s_waitcnt lgkmcnt(5)\n" \
  "v_mfma_f32_16x16x32_bf16 v[88:91], a[44:47], v[44:47], v[88:91]\n" \
  "ds_read_b128 v[36:39], v191 offset:19328\n" \
  "v_mfma_f32_16x16x32_bf16 v[84:87], a[116:119], v[40:43], v[84:87]\n" \
  "v_mfma_f32_16x16x32_bf16 v[92:95], a[116:119], v[44:47], v[92:95]\n" \
  "s_waitcnt lgkmcnt(5)\n" \
  "v_mfma_f32_16x16x32_bf16 v[80:83], a[48:51], v[48:51], v[80:83]\n" \
  "ds_read_b128 v[40:43], v192 offset:896\n" \
  "s_waitcnt lgkmcnt(5)\n" \
  "v_mfma_f32_16x16x32_bf16 v[88:91], a[48:51], v[52:55], v[88:91]\n" \
  "ds_read_b128 v[44:47], v192 offset:19328\n" \
  "v_mfma_f32_16x16x32_bf16 v[84:87], a[120:123], v[48:51], v[84:87]\n" \
  "s_add_u32 m0, s52, 6144\n" \
  "v_mov_b32 v168, s56\n" \
  "v_mfma_f32_16x16x32_bf16 v[92:95], a[120:123], v[52:55], v[92:95]\n" \
  "s_waitcnt lgkmcnt(5)\n" \
  "buffer_load_dwordx4 v202, s[44:47], s51 offen lds\n" \
  "v_mfma_f32_16x16x32_bf16 v[80:83], a[52:55], v[56:59], v[80:83]\n" \
  "ds_read_b128 v[48:51], v191 offset:1024\n" \
  "s_waitcnt lgkmcnt(5)\n" \
  "v_mfma_f32_16x16x32_bf16 v[88:91], a[52:55], v[60:63], v[88:91]\n" \
  "ds_read_b128 v[52:55], v191 offset:19456\n" \
  "v_mfma_f32_16x16x32_bf16 v[84:87], a[124:127], v[56:59], v[84:87]\n" \
  "s_waitcnt vmcnt(11)\n" \
  "s_barrier\n" \
  "v_mfma_f32_16x16x32_bf16 v[92:95], a[124:127], v[60:63], v[92:95]\n" \
  "s_waitcnt lgkmcnt(5)\n" \
  "buffer_store_dwordx2 v[168:169], v163, s[40:43], 0 offen sc1\n" \
  "v_mfma_f32_16x16x32_bf16 v[80:83], a[56:59], v[32:35], v[80:83]\n" \
  "ds_read_b128 v[56:59], v192 offset:1024\n" \
  "s_waitcnt lgkmcnt(5)\n" \
  "v_mfma_f32_16x16x32_bf16 v[88:91], a[56:59], v[36:39], v[88:91]\n" \
  "ds_read_b128 v[60:63], v192 offset:19456\n" \
  "v_mfma_f32_16x16x32_bf16 v[84:87], a[128:131], v[32:35], v[84:87]\n" \
  "s_add_u32 m0, s52, 7168\n" \
  "s_waitcnt lgkmcnt(5)\n" \
  "v_mfma_f32_16x16x32_bf16 v[92:95], a[128:131], v[36:39], v[92:95]\n" \
  "buffer_load_dwordx4 v203, s[44:47], s51 offen lds\n" \
  "v_mfma_f32_16x16x32_bf16 v[80:83], a[60:63], v[40:43], v[80:83]\n" \
  "ds_read_b128 v[32:35], v165\n" \
  "s_waitcnt lgkmcnt(5)\n" \
  "v_mfma_f32_16x16x32_bf16 v[88:91], a[60:63], v[44:47], v[88:91]\n" \
  "ds_read_b128 v[36:39], v165 offset:18432\n" \
  "v_mfma_f32_16x16x32_bf16 v[84:87], a[132:135], v[40:43], v[84:87]\n" \
  "s_min_u32 s56, s48, 20\n" \
  "s_cselect_b32 s57, 0, 38\n" \
  "s_add_u32 s57, s57, s48\n" \
  "v_mfma_f32_16x16x32_bf16 v[92:95], a[132:135], v[44:47], v[92:95]\n" \
  "s_add_u32 s48, s48, 1\n" \
  "v_mov_b32 v191, v165\n" \
  "v_mov_b32 v192, v164\n" \
  "s_waitcnt lgkmcnt(5)\n" \
  "v_mfma_f32_16x16x32_bf16 v[80:83], a[64:67], v[48:51], v[80:83]\n" \
  "ds_read_b128 v[40:43], v164\n" \
  "s_waitcnt lgkmcnt(5)\n" \
  "v_mfma_f32_16x16x32_bf16 v[88:91], a[64:67], v[52:55], v[88:91]\n" \
  "ds_read_b128 v[44:47], v164 offset:18432\n" \
  "v_mfma_f32_16x16x32_bf16 v[84:87], a[136:139], v[48:51], v[84:87]\n" \
  "s_add_u32 m0, s52, 8192\n" \
  "s_cmp_eq_u32 s51, 700416\n" \
  "s_cselect_b32 s70, 1437696, s71\n" \
  "v_mfma_f32_16x16x32_bf16 v[92:95], a[136:139], v[52:55], v[92:95]\n" \
  "s_waitcnt lgkmcnt(5)\n" \
  "buffer_load_dwordx4 v204, s[44:47], s51 offen lds\n" \
  "v_mfma_f32_16x16x32_bf16 v[80:83], a[68:71], v[56:59], v[80:83]\n" \
  "ds_read_b128 v[48:51], v165 offset:128\n" \
  "s_add_u32 s51, s51, s70\n" \
  "s_waitcnt lgkmcnt(5)\n" \
  "v_mfma_f32_16x16x32_bf16 v[88:91], a[68:71], v[60:63], v[88:91]\n" \
  "ds_read_b128 v[52:55], v165 offset:18560\n" \
  "s_min_u32 s51, s51, 2322432\n" \
  "v_mfma_f32_16x16x32_bf16 v[84:87], a[140:143], v[56:59], v[84:87]\n" \
  "s_add_u32 s52, s52, 36864\n" \
  "v_mfma_f32_16x16x32_bf16 v[92:95], a[140:143], v[60:63], v[92:95]\n" \
  "s_cmp_ge_u32 s52, 163840\n" \
  "s_cselect_b32 s60, 147456, 0\n" \
  "s_sub_u32 s52, s52, s60\n" \
  "s_cmp_le_u32 s48, 4\n" \
  "s_cbranch_scc1 .LK_meb_%=\n" \
  ".LK_meb_b_%=:\n" \
  "s_cmp_lt_u32 s48, 26\n" \
  "s_cbranch_scc1 .LK_ma_%=\n" \
  "s_waitcnt lgkmcnt(0)\n" \
  "s_nop 7\n" \
  "s_lshr_b32 s62, s49, 8\n" \
  "s_and_b32 s62, s62, 8192\n" \
  "s_xor_b32 s62, s62, s49\n" \
  "s_lshl_b32 s63, s57, 14\n" \
  "s_xor_b32 s63, s63, s59\n" \
  "v_cvt_pk_bf16_f32 v96, v80, v81\n" \
  "v_cvt_pk_bf16_f32 v97, v82, v83\n" \
  "v_cvt_pk_bf16_f32 v98, v84, v85\n" \
  "v_cvt_pk_bf16_f32 v99, v86, v87\n" \
  "buffer_store_dwordx4 v[96:99], v190, s[40:43], s62 offen sc1\n" \
  "buffer_store_dwordx4 v[96:99], v190, s[40:43], s63 offen sc1\n" \
  "v_cvt_pk_bf16_f32 v100, v88, v89\n" \
  "v_cvt_pk_bf16_f32 v101, v90, v91\n" \
  "v_cvt_pk_bf16_f32 v102, v92, v93\n" \
  "v_cvt_pk_bf16_f32 v103, v94, v95\n" \
  "buffer_store_dwordx4 v[100:103], v190, s[40:43], s62 offen offset:1024 sc1\n" \
  "buffer_store_dwordx4 v[100:103], v190, s[40:43], s63 offen offset:1024 sc1\n" \
  "s_sub_u32 s49, s49, 22544384\n" \
  "s_mov_b32 s48, 20\n" \
  "s_waitcnt vmcnt(0)\n" \
  "s_barrier\n" \
  "v_mov_b32 v168, 1\n" \
  "s_mov_b64 exec, 2\n" \
  "buffer_store_dwordx2 v[168:169], v167, s[40:43], 0 offen sc1\n" \
  "s_mov_b64 exec, -1\n" \
  "v_subrev_u32 v191, 73728, v191\n" \
  "v_subrev_u32 v192, 73728, v192\n" \
  "s_waitcnt vmcnt(0)\n" \
  "s_cmp_eq_u32 %43, 77\n" \
  "s_cbranch_scc1 .LK_end_%=\n" \
  "s_mov_b64 s[44:45], %32\n" \
  "s_and_b32 s45, s45, 0xffff\n" \
  "s_mov_b32 s46, 4718592\n" \
  "s_add_u32 s60, %35, 0\n" \
  "s_add_u32 m0, s72, 16384\n" \
  "s_nop 0\n" \
  "buffer_load_dwordx4 v170, s[44:47], s60 offen lds\n" \
  "buffer_load_dwordx4 v171, s[44:47], s60 offen offset:1024 lds\n" \
  "buffer_load_dwordx4 v172, s[44:47], s60 offen offset:2048 lds\n" \
  "buffer_load_dwordx4 v173, s[44:47], s60 offen offset:3072 lds\n" \
  "s_add_u32 s60, %35, 524288\n" \
  "s_add_u32 m0, s72, 32768\n" \
  "s_nop 0\n" \
  "buffer_load_dwordx4 v170, s[44:47], s60 offen lds\n" \
  "buffer_load_dwordx4 v171, s[44:47], s60 offen offset:1024 lds\n" \
  "buffer_load_dwordx4 v172, s[44:47], s60 offen offset:2048 lds\n" \
  "buffer_load_dwordx4 v173, s[44:47], s60 offen offset:3072 lds\n" \
  "s_add_u32 s60, %35, 1048576\n" \
  "s_add_u32 m0, s72, 49152\n" \
  "s_nop 0\n" \
  "buffer_load_dwordx4 v170, s[44:47], s60 offen lds\n" \
  "buffer_load_dwordx4 v171, s[44:47], s60 offen offset:1024 lds\n" \
  "buffer_load_dwordx4 v172, s[44:47], s60 offen offset:2048 lds\n" \
  "buffer_load_dwordx4 v173, s[44:47], s60 offen offset:3072 lds\n" \
  "s_add_u32 s60, %35, 1572864\n" \
  "s_add_u32 m0, s72, 65536\n" \
  "s_nop 0\n" \
  "buffer_load_dwordx4 v170, s[44:47], s60 offen lds\n" \
  "buffer_load_dwordx4 v171, s[44:47], s60 offen offset:1024 lds\n" \
  "buffer_load_dwordx4 v172, s[44:47], s60 offen offset:2048 lds\n" \
  "buffer_load_dwordx4 v173, s[44:47], s60 offen offset:3072 lds\n" \
  "s_add_u32 s60, %35, 2097152\n" \
  "s_add_u32 m0, s72, 81920\n" \
  "s_nop 0\n" \
  "buffer_load_dwordx4 v170, s[44:47], s60 offen lds\n" \
  "buffer_load_dwordx4 v171, s[44:47], s60 offen offset:1024 lds\n" \
  "buffer_load_dwordx4 v172, s[44:47], s60 offen offset:2048 lds\n" \
  "buffer_load_dwordx4 v173, s[44:47], s60 offen offset:3072 lds\n" \
  "s_add_u32 s60, %35, 2621440\n" \
  "s_add_u32 m0, s72, 98304\n" \
  "s_nop 0\n" \
  "buffer_load_dwordx4 v170, s[44:47], s60 offen lds\n" \
  "buffer_load_dwordx4 v171, s[44:47], s60 offen offset:1024 lds\n" \
  "buffer_load_dwordx4 v172, s[44:47], s60 offen offset:2048 lds\n" \
  "buffer_load_dwordx4 v173, s[44:47], s60 offen offset:3072 lds\n" \
  "s_add_u32 s60, %35, 3145728\n" \
  "s_add_u32 m0, s72, 114688\n" \
  "s_nop 0\n" \
  "buffer_load_dwordx4 v170, s[44:47], s60 offen lds\n" \
  "buffer_load_dwordx4 v171, s[44:47], s60 offen offset:1024 lds\n" \
  "buffer_load_dwordx4 v172, s[44:47], s60 offen offset:2048 lds\n" \
  "buffer_load_dwordx4 v173, s[44:47], s60 offen offset:3072 lds\n" \
  "s_add_u32 s60, %35, 3670016\n" \
  "s_add_u32 m0, s72, 131072\n" \
  "s_nop 0\n" \
  "buffer_load_dwordx4 v170, s[44:47], s60 offen lds\n" \
  "buffer_load_dwordx4 v171, s[44:47], s60 offen offset:1024 lds\n" \
  "buffer_load_dwordx4 v172, s[44:47], s60 offen offset:2048 lds\n" \
  "buffer_load_dwordx4 v173, s[44:47], s60 offen offset:3072 lds\n" \
  "s_add_u32 s60, %35, 4194304\n" \
  "s_add_u32 m0, s72, 147456\n" \
  "s_nop 0\n" \
  "buffer_load_dwordx4 v170, s[44:47], s60 offen lds\n" \
  "buffer_load_dwordx4 v171, s[44:47], s60 offen offset:1024 lds\n" \
  "buffer_load_dwordx4 v172, s[44:47], s60 offen offset:2048 lds\n" \
  "buffer_load_dwordx4 v173, s[44:47], s60 offen offset:3072 lds\n" \
  "s_waitcnt vmcnt(24)\n" \
  "s_barrier\n" \
  "v_add_u32 v186, 16384, v182\n" \
  "ds_read_b64_tr_b16 a[144:145], v186\n" \
  "ds_read_b64_tr_b16 a[146:147], v186 offset:1024\n" \
  "ds_read_b64_tr_b16 a[148:149], v186 offset:8192\n" \
  "ds_read_b64_tr_b16 a[150:151], v186 offset:9216\n" \
  "v_add_u32 v187, 16384, v183\n" \
  "ds_read_b64_tr_b16 a[216:217], v187\n" \
  "ds_read_b64_tr_b16 a[218:219], v187 offset:1024\n" \
  "ds_read_b64_tr_b16 a[220:221], v187 offset:8192\n" \
  "ds_read_b64_tr_b16 a[222:223], v187 offset:9216\n" \
  "v_add_u32 v186, 32768, v182\n" \
  "ds_read_b64_tr_b16 a[152:153], v186\n" \
  "ds_read_b64_tr_b16 a[154:155], v186 offset:1024\n" \
  "ds_read_b64_tr_b16 a[156:157], v186 offset:8192\n" \
  "ds_read_b64_tr_b16 a[158:159], v186 offset:9216\n" \
  "v_add_u32 v187, 32768, v183\n" \
  "ds_read_b64_tr_b16 a[224:225], v187\n" \
  "ds_read_b64_tr_b16 a[226:227], v187 offset:1024\n" \
  "ds_read_b64_tr_b16 a[228:229], v187 offset:8192\n" \
  "ds_read_b64_tr_b16 a[230:231], v187 offset:9216\n" \
  "v_add_u32 v186, 49152, v182\n" \
  "ds_read_b64_tr_b16 a[160:161], v186\n" \
  "ds_read_b64_tr_b16 a[162:163], v186 offset:1024\n" \
  "ds_read_b64_tr_b16 a[164:165], v186 offset:8192\n" \
  "ds_read_b64_tr_b16 a[166:167], v186 offset:9216\n" \
  "v_add_u32 v187, 49152, v183\n" \
  "ds_read_b64_tr_b16 a[232:233], v187\n" \
  "ds_read_b64_tr_b16 a[234:235], v187 offset:1024\n" \
  "ds_read_b64_tr_b16 a[236:237], v187 offset:8192\n" \
  "ds_read_b64_tr_b16 a[238:239], v187 offset:9216\n" \
  "s_waitcnt lgkmcnt(0)\n" \
  "s_barrier\n" \
  "s_mov_b64 s[44:45], %33\n" \
  "s_and_b32 s45, s45, 0xffff\n" \
  "s_mov_b32 s46, 2359296\n" \
  "s_mov_b32 s51, 737280\n" \
  "s_mul_i32 s52, %34, 9216\n" \
  "s_add_u32 s52, s52, 16384\n" \
  "s_add_u32 m0, s52, 0\n" \
  "s_nop 0\n" \
  "buffer_load_dwordx4 v196, s[44:47], s51 offen lds\n" \
  "s_add_u32 m0, s52, 1024\n" \
  "s_nop 0\n" \
  "buffer_load_dwordx4 v197, s[44:47], s51 offen lds\n" \
  "s_add_u32 m0, s52, 2048\n" \
  "s_nop 0\n" \
  "buffer_load_dwordx4 v198, s[44:47], s51 offen lds\n" \
  "s_add_u32 m0, s52, 3072\n" \
  "s_nop 0\n" \
  "buffer_load_dwordx4 v199, s[44:47], s51 offen lds\n" \
  "s_add_u32 m0, s52, 4096\n" \
  "s_nop 0\n" \
  "buffer_load_dwordx4 v200, s[44:47], s51 offen lds\n" \
  "s_add_u32 m0, s52, 5120\n" \
  "s_nop 0\n" \
  "buffer_load_dwordx4 v201, s[44:47], s51 offen lds\n" \
  "s_add_u32 m0, s52, 6144\n" \
  "s_nop 0\n" \
  "buffer_load_dwordx4 v202, s[44:47], s51 offen lds\n" \
  "s_add_u32 m0, s52, 7168\n" \
  "s_nop 0\n" \
  "buffer_load_dwordx4 v203, s[44:47], s51 offen lds\n" \
  "s_add_u32 m0, s52, 8192\n" \
  "s_nop 0\n" \
  "buffer_load_dwordx4 v204, s[44:47], s51 offen lds\n" \
  "s_add_u32 s51, s51, 36864\n" \
  "s_min_u32 s51, s51, 2322432\n" \
  "s_add_u32 s52, s52, 36864\n" \
  "s_cmp_ge_u32 s52, 163840\n" \
  "s_cselect_b32 s60, 147456, 0\n" \
  "s_sub_u32 s52, s52, s60\n" \
  "s_waitcnt vmcnt(21)\n" \
  "s_barrier\n" \
  "v_add_u32 v186, 65536, v182\n" \
  "ds_read_b64_tr_b16 a[168:169], v186\n" \
  "ds_read_b64_tr_b16 a[170:171], v186 offset:1024\n" \
  "ds_read_b64_tr_b16 a[172:173], v186 offset:8192\n" \
  "ds_read_b64_tr_b16 a[174:175], v186 offset:9216\n" \
  "v_add_u32 v187, 65536, v183\n" \
  "ds_read_b64_tr_b16 a[240:241], v187\n" \
  "ds_read_b64_tr_b16 a[242:243], v187 offset:1024\n" \
  "ds_read_b64_tr_b16 a[244:245], v187 offset:8192\n" \
  "ds_read_b64_tr_b16 a[246:247], v187 offset:9216\n" \
  "v_add_u32 v186, 81920, v182\n" \
  "ds_read_b64_tr_b16 a[176:177], v186\n" \
  "ds_read_b64_tr_b16 a[178:179], v186 offset:1024\n" \
  "ds_read_b64_tr_b16 a[180:181], v186 offset:8192\n" \
  "ds_read_b64_tr_b16 a[182:183], v186 offset:9216\n" \
  "v_add_u32 v187, 81920, v183\n" \
  "ds_read_b64_tr_b16 a[248:249], v187\n" \
  "ds_read_b64_tr_b16 a[250:251], v187 offset:1024\n" \
  "ds_read_b64_tr_b16 a[252:253], v187 offset:8192\n" \
  "ds_read_b64_tr_b16 a[254:255], v187 offset:9216\n" \
  "v_add_u32 v186, 98304, v182\n" \
  "ds_read_b64_tr_b16 a[184:185], v186\n" \
  "ds_read_b64_tr_b16 a[186:187], v186 offset:1024\n" \
  "ds_read_b64_tr_b16 a[188:189], v186 offset:8192\n" \
  "ds_read_b64_tr_b16 a[190:191], v186 offset:9216\n" \
  "v_add_u32 v187, 98304, v183\n" \
  "ds_read_b64_tr_b16 v[0:1], v187\n" \
  "ds_read_b64_tr_b16 v[2:3], v187 offset:1024\n" \
  "ds_read_b64_tr_b16 v[4:5], v187 offset:8192\n" \
  "ds_read_b64_tr_b16 v[6:7], v187 offset:9216\n" \
  "s_waitcnt lgkmcnt(0)\n" \
  "s_barrier\n" \
  "s_add_u32 m0, s52, 0\n" \
  "s_nop 0\n" \
  "buffer_load_dwordx4 v196, s[44:47], s51 offen lds\n" \
  "s_add_u32 m0, s52, 1024\n" \
  "s_nop 0\n" \
  "buffer_load_dwordx4 v197, s[44:47], s51 offen lds\n" \
  "s_add_u32 m0, s52, 2048\n" \
  "s_nop 0\n" \
  "buffer_load_dwordx4 v198, s[44:47], s51 offen lds\n" \
  "s_add_u32 m0, s52, 3072\n" \
  "s_nop 0\n" \
  "buffer_load_dwordx4 v199, s[44:47], s51 offen lds\n" \
  "s_add_u32 m0, s52, 4096\n" \
  "s_nop 0\n" \
  "buffer_load_dwordx4 v200, s[44:47], s51 offen lds\n" \
  "s_add_u32 m0, s52, 5120\n" \
  "s_nop 0\n" \
  "buffer_load_dwordx4 v201, s[44:47], s51 offen lds\n" \
  "s_add_u32 m0, s52, 6144\n" \
  "s_nop 0\n" \
  "buffer_load_dwordx4 v202, s[44:47], s51 offen lds\n" \
  "s_add_u32 m0, s52, 7168\n" \
  "s_nop 0\n" \
  "buffer_load_dwordx4 v203, s[44:47], s51 offen lds\n" \
  "s_add_u32 m0, s52, 8192\n" \
  "s_nop 0\n" \
  "buffer_load_dwordx4 v204, s[44:47], s51 offen lds\n" \
  "s_add_u32 s51, s51, 36864\n" \
  "s_min_u32 s51, s51, 2322432\n" \
  "s_add_u32 s52, s52, 36864\n" \
  "s_cmp_ge_u32 s52, 163840\n" \
  "s_cselect_b32 s60, 147456, 0\n" \
  "s_sub_u32 s52, s52, s60\n" \
  "s_waitcnt vmcnt(18)\n" \
  "s_barrier\n" \
  "v_add_u32 v186, 114688, v182\n" \
  "ds_read_b64_tr_b16 a[192:193], v186\n" \
  "ds_read_b64_tr_b16 a[194:195], v186 offset:1024\n" \
  "ds_read_b64_tr_b16 a[196:197], v186 offset:8192\n" \
  "ds_read_b64_tr_b16 a[198:199], v186 offset:9216\n" \
  "v_add_u32 v187, 114688, v183\n" \
  "ds_read_b64_tr_b16 v[8:9], v187\n" \
  "ds_read_b64_tr_b16 v[10:11], v187 offset:1024\n" \
  "ds_read_b64_tr_b16 v[12:13], v187 offset:8192\n" \
  "ds_read_b64_tr_b16 v[14:15], v187 offset:9216\n" \
  "v_add_u32 v186, 131072, v182\n" \
  "ds_read_b64_tr_b16 a[200:201], v186\n" \
  "ds_read_b64_tr_b16 a[202:203], v186 offset:1024\n" \
  "ds_read_b64_tr_b16 a[204:205], v186 offset:8192\n" \
  "ds_read_b64_tr_b16 a[206:207], v186 offset:9216\n" \
  "v_add_u32 v187, 131072, v183\n" \
  "ds_read_b64_tr_b16 v[16:17], v187\n" \
  "ds_read_b64_tr_b16 v[18:19], v187 offset:1024\n" \
  "ds_read_b64_tr_b16 v[20:21], v187 offset:8192\n" \
  "ds_read_b64_tr_b16 v[22:23], v187 offset:9216\n" \
  "v_add_u32 v186, 147456, v182\n" \
  "ds_read_b64_tr_b16 a[208:209], v186\n" \
  "ds_read_b64_tr_b16 a[210:211], v186 offset:1024\n" \
  "ds_read_b64_tr_b16 a[212:213], v186 offset:8192\n" \
  "ds_read_b64_tr_b16 a[214:215], v186 offset:9216\n" \
  "v_add_u32 v187, 147456, v183\n" \
  "ds_read_b64_tr_b16 v[24:25], v187\n" \
  "ds_read_b64_tr_b16 v[26:27], v187 offset:1024\n" \
  "ds_read_b64_tr_b16 v[28:29], v187 offset:8192\n" \
  "ds_read_b64_tr_b16 v[30:31], v187 offset:9216\n" \
  "s_waitcnt lgkmcnt(0)\n" \
  "s_waitcnt vmcnt(9)\n" \
  "s_barrier\n" \
  "ds_read_b128 v[32:35], v191\n" \
  "ds_read_b128 v[36:39], v191 offset:18432\n" \
  "ds_read_b128 v[40:43], v192\n" \
  "ds_read_b128 v[44:47], v192 offset:18432\n" \
  "ds_read_b128 v[48:51], v191 offset:128\n" \
  "ds_read_b128 v[52:55], v191 offset:18560\n" \
  "s_waitcnt lgkmcnt(0)\n" \
  ".LK_loop_%=:\n" \
  "s_waitcnt lgkmcnt(5)\n" \
  "v_mfma_f32_16x16x32_bf16 v[64:67], a[0:3], v[32:35], 0\n" \
  "ds_read_b128 v[56:59], v192 offset:128\n" \
  "s_waitcnt lgkmcnt(5)\n" \
  "v_mfma_f32_16x16x32_bf16 v[72:75], a[0:3], v[36:39], 0\n" \
  "ds_read_b128 v[60:63], v192 offset:18560\n" \
  "s_lshr_b32 s62, s50, 8\n" \
  "v_mfma_f32_16x16x32_bf16 v[68:71], a[72:75], v[32:35], 0\n" \
  "s_and_b32 s62, s62, 8192\n" \
  "s_xor_b32 s62, s62, s50\n" \
  "v_cvt_pk_bf16_f32 v96, v80, v81\n" \
  "v_mfma_f32_16x16x32_bf16 v[76:79], a[72:75], v[36:39], 0\n" \
  "s_waitcnt lgkmcnt(5)\n" \
  "v_cvt_pk_bf16_f32 v97, v82, v83\n" \
  "v_cvt_pk_bf16_f32 v98, v84, v85\n" \
  "v_mfma_f32_16x16x32_bf16 v[64:67], a[4:7], v[40:43], v[64:67]\n" \
  "ds_read_b128 v[32:35], v191 offset:256\n" \
  "s_waitcnt lgkmcnt(5)\n" \
  "v_cvt_pk_bf16_f32 v99, v86, v87\n" \
  "v_mfma_f32_16x16x32_bf16 v[72:75], a[4:7], v[44:47], v[72:75]\n" \
  "ds_read_b128 v[36:39], v191 offset:18688\n" \
  "v_mfma_f32_16x16x32_bf16 v[68:71], a[76:79], v[40:43], v[68:71]\n" \
  "buffer_store_dwordx4 v[96:99], v190, s[40:43], s62 offen sc1\n" \
  "v_mfma_f32_16x16x32_bf16 v[76:79], a[76:79], v[44:47], v[76:79]\n" \
  "s_waitcnt lgkmcnt(5)\n" \
  "v_cvt_pk_bf16_f32 v100, v88, v89\n" \
  "v_cvt_pk_bf16_f32 v101, v90, v91\n" \
  "v_mfma_f32_16x16x32_bf16 v[64:67], a[8:11], v[48:51], v[64:67]\n" \
  "ds_read_b128 v[40:43], v192 offset:256\n" \
  "s_waitcnt lgkmcnt(5)\n" \
  "v_cvt_pk_bf16_f32 v102, v92, v93\n" \
  "v_mfma_f32_16x16x32_bf16 v[72:75], a[8:11], v[52:55], v[72:75]\n" \
  "ds_read_b128 v[44:47], v192 offset:18688\n" \
  "v_cvt_pk_bf16_f32 v103, v94, v95\n" \
  "v_mfma_f32_16x16x32_bf16 v[68:71], a[80:83], v[48:51], v[68:71]\n" \
  "buffer_store_dwordx4 v[100:103], v190, s[40:43], s62 offen offset:1024 sc1\n" \
  "v_mfma_f32_16x16x32_bf16 v[76:79], a[80:83], v[52:55], v[76:79]\n" \
  "s_add_u32 s50, s50, 524288\n" \
  "s_bitset0_b32 s50, 30\n" \
  "s_waitcnt lgkmcnt(5)\n" \
  "v_mfma_f32_16x16x32_bf16 v[64:67], a[12:15], v[56:59], v[64:67]\n" \
  "ds_read_b128 v[48:51], v191 offset:384\n" \
  "s_waitcnt lgkmcnt(5)\n" \
  "v_mfma_f32_16x16x32_bf16 v[72:75], a[12:15], v[60:63], v[72:75]\n" \
  "ds_read_b128 v[52:55], v191 offset:18816\n" \
  "v_mfma_f32_16x16x32_bf16 v[68:71], a[84:87], v[56:59], v[68:71]\n" \
  "v_mfma_f32_16x16x32_bf16 v[76:79], a[84:87], v[60:63], v[76:79]\n" \
  "s_waitcnt lgkmcnt(5)\n" \
  "v_mfma_f32_16x16x32_bf16 v[64:67], a[16:19], v[32:35], v[64:67]\n" \
  "ds_read_b128 v[56:59], v192 offset:384\n" \
  "s_waitcnt lgkmcnt(5)\n" \
  "v_mfma_f32_16x16x32_bf16 v[72:75], a[16:19], v[36:39], v[72:75]\n" \
  "ds_read_b128 v[60:63], v192 offset:18816\n" \
  "v_mfma_f32_16x16x32_bf16 v[68:71], a[88:91], v[32:35], v[68:71]\n" \
  "v_mfma_f32_16x16x32_bf16 v[76:79], a[88:91], v[36:39], v[76:79]\n" \
  "s_waitcnt lgkmcnt(5)\n" \
  "v_mfma_f32_16x16x32_bf16 v[64:67], a[20:23], v[40:43], v[64:67]\n" \
  "ds_read_b128 v[32:35], v191 offset:512\n" \
  "s_waitcnt lgkmcnt(5)\n" \
  "v_mfma_f32_16x16x32_bf16 v[72:75], a[20:23], v[44:47], v[72:75]\n" \
  "ds_read_b128 v[36:39], v191 offset:18944\n" \
  "v_mfma_f32_16x16x32_bf16 v[68:71], a[92:95], v[40:43], v[68:71]\n" \
  "v_mfma_f32_16x16x32_bf16 v[76:79], a[92:95], v[44:47], v[76:79]\n" \
  "s_waitcnt lgkmcnt(5)\n" \
  "v_mfma_f32_16x16x32_bf16 v[64:67], a[24:27], v[48:51], v[64:67]\n" \
  "ds_read_b128 v[40:43], v192 offset:512\n" \
  "s_waitcnt lgkmcnt(5)\n" \
  "v_mfma_f32_16x16x32_bf16 v[72:75], a[24:27], v[52:55], v[72:75]\n" \
  "ds_read_b128 v[44:47], v192 offset:18944\n" \
  "v_mfma_f32_16x16x32_bf16 v[68:71], a[96:99], v[48:51], v[68:71]\n" \
  "v_mfma_f32_16x16x32_bf16 v[76:79], a[96:99], v[52:55], v[76:79]\n" \
  "s_waitcnt lgkmcnt(5)\n" \
  "v_mfma_f32_16x16x32_bf16 v[64:67], a[28:31], v[56:59], v[64:67]\n" \
  "ds_read_b128 v[48:51], v191 offset:640\n" \
  "s_waitcnt lgkmcnt(5)\n" \
  "v_mfma_f32_16x16x32_bf16 v[72:75], a[28:31], v[60:63], v[72:75]\n" \
  "ds_read_b128 v[52:55], v191 offset:19072\n" \
  "v_mfma_f32_16x16x32_bf16 v[68:71], a[100:103], v[56:59], v[68:71]\n" \
  "s_add_u32 m0, s52, 0\n" \
  "s_waitcnt lgkmcnt(5)\n" \
  "v_mfma_f32_16x16x32_bf16 v[76:79], a[100:103], v[60:63], v[76:79]\n" \
  "buffer_load_dwordx4 v196, s[44:47], s51 offen lds\n" \
  "v_mfma_f32_16x16x32_bf16 v[64:67], a[32:35], v[32:35], v[64:67]\n" \
  "ds_read_b128 v[56:59], v192 offset:640\n" \
  "s_waitcnt lgkmcnt(5)\n" \
  "v_mfma_f32_16x16x32_bf16 v[72:75], a[32:35], v[36:39], v[72:75]\n" \
  "ds_read_b128 v[60:63], v192 offset:19072\n" \
  "v_mfma_f32_16x16x32_bf16 v[68:71], a[104:107], v[32:35], v[68:71]\n" \
  "v_mfma_f32_16x16x32_bf16 v[76:79], a[104:107], v[36:39], v[76:79]\n" \
  "s_waitcnt lgkmcnt(5)\n" \
  "v_mfma_f32_16x16x32_bf16 v[64:67], a[36:39], v[40:43], v[64:67]\n" \
  "ds_read_b128 v[32:35], v191 offset:768\n" \
  "s_waitcnt lgkmcnt(5)\n" \
  "v_mfma_f32_16x16x32_bf16 v[72:75], a[36:39], v[44:47], v[72:75]\n" \
  "ds_read_b128 v[36:39], v191 offset:19200\n" \
  "v_mfma_f32_16x16x32_bf16 v[68:71], a[108:111], v[40:43], v[68:71]\n" \
  "s_add_u32 m0, s52, 1024\n" \
  "s_waitcnt lgkmcnt(5)\n" \
  "v_mfma_f32_16x16x32_bf16 v[76:79], a[108:111], v[44:47], v[76:79]\n" \
  "buffer_load_dwordx4 v197, s[44:47], s51 offen lds\n" \
  "v_mfma_f32_16x16x32_bf16 v[64:67], a[40:43], v[48:51], v[64:67]\n" \
  "ds_read_b128 v[40:43], v192 offset:768\n" \
  "s_waitcnt lgkmcnt(5)\n" \
  "v_mfma_f32_16x16x32_bf16 v[72:75], a[40:43], v[52:55], v[72:75]\n" \
  "ds_read_b128 v[44:47], v192 offset:19200\n" \
  "v_mfma_f32_16x16x32_bf16 v[68:71], a[112:115], v[48:51], v[68:71]\n" \
  "v_mfma_f32_16x16x32_bf16 v[76:79], a[112:115], v[52:55], v[76:79]\n" \
  "s_waitcnt lgkmcnt(5)\n" \
  "v_mfma_f32_16x16x32_bf16 v[64:67], a[44:47], v[56:59], v[64:67]\n" \
  "ds_read_b128 v[48:51], v191 offset:896\n" \
  "s_waitcnt lgkmcnt(5)\n" \
  "v_mfma_f32_16x16x32_bf16 v[72:75], a[44:47], v[60:63], v[72:75]\n" \
  "ds_read_b128 v[52:55], v191 offset:19328\n" \
  "v_mfma_f32_16x16x32_bf16 v[68:71], a[116:119], v[56:59], v[68:71]\n" \
  "s_add_u32 m0, s52, 2048\n" \
  "v_add_u32 v168, s48, v166\n" \
  "v_mfma_f32_16x16x32_bf16 v[76:79], a[116:119], v[60:63], v[76:79]\n" \
  "s_waitcnt lgkmcnt(5)\n" \
  "buffer_load_dwordx4 v198, s[44:47], s51 offen lds\n" \
  "v_mfma_f32_16x16x32_bf16 v[64:67], a[48:51], v[32:35], v[64:67]\n" \
  "ds_read_b128 v[56:59], v192 offset:896\n" \
  "s_waitcnt lgkmcnt(5)\n" \
  "v_mfma_f32_16x16x32_bf16 v[72:75], a[48:51], v[36:39], v[72:75]\n" \
  "ds_read_b128 v[60:63], v192 offset:19328\n" \
  "v_mfma_f32_16x16x32_bf16 v[68:71], a[120:123], v[32:35], v[68:71]\n" \
  "s_waitcnt vmcnt(5)\n" \
  "s_barrier\n" \
  "v_mfma_f32_16x16x32_bf16 v[76:79], a[120:123], v[36:39], v[76:79]\n" \
  "s_waitcnt lgkmcnt(5)\n" \
  "buffer_store_dwordx2 v[168:169], v167, s[40:43], 0 offen sc1\n" \
  "v_mfma_f32_16x16x32_bf16 v[64:67], a[52:55], v[40:43], v[64:67]\n" \
  "ds_read_b128 v[32:35], v191 offset:1024\n" \
  "s_waitcnt lgkmcnt(5)\n" \
  "v_mfma_f32_16x16x32_bf16 v[72:75], a[52:55], v[44:47], v[72:75]\n" \
  "ds_read_b128 v[36:39], v191 offset:19456\n" \
  "v_mfma_f32_16x16x32_bf16 v[68:71], a[124:127], v[40:43], v[68:71]\n" \
  "s_add_u32 m0, s52, 3072\n" \
  "s_waitcnt lgkmcnt(5)\n" \
  "v_mfma_f32_16x16x32_bf16 v[76:79], a[124:127], v[44:47], v[76:79]\n" \
  "buffer_load_dwordx4 v199, s[44:47], s51 offen lds\n" \
  "v_mfma_f32_16x16x32_bf16 v[64:67], a[56:59], v[48:51], v[64:67]\n" \
  "ds_read_b128 v[40:43], v192 offset:1024\n" \
  "s_waitcnt lgkmcnt(5)\n" \
  "v_mfma_f32_16x16x32_bf16 v[72:75], a[56:59], v[52:55], v[72:75]\n" \
  "ds_read_b128 v[44:47], v192 offset:19456\n" \
  "v_mfma_f32_16x16x32_bf16 v[68:71], a[128:131], v[48:51], v[68:71]\n" \
  "v_mfma_f32_16x16x32_bf16 v[76:79], a[128:131], v[52:55], v[76:79]\n" \
  "s_waitcnt lgkmcnt(5)\n" \
  "v_mfma_f32_16x16x32_bf16 v[64:67], a[60:63], v[56:59], v[64:67]\n" \
  "ds_read_b128 v[48:51], v191\n" \
  "s_waitcnt lgkmcnt(5)\n" \
  "v_mfma_f32_16x16x32_bf16 v[72:75], a[60:63], v[60:63], v[72:75]\n" \
  "ds_read_b128 v[52:55], v191 offset:18432\n" \
  "v_mfma_f32_16x16x32_bf16 v[68:71], a[132:135], v[56:59], v[68:71]\n" \
  "s_add_u32 m0, s52, 4096\n" \
  "s_waitcnt lgkmcnt(5)\n" \
  "v_mfma_f32_16x16x32_bf16 v[76:79], a[132:135], v[60:63], v[76:79]\n" \
  "buffer_load_dwordx4 v200, s[44:47], s51 offen lds\n" \
  "v_mfma_f32_16x16x32_bf16 v[64:67], a[64:67], v[32:35], v[64:67]\n" \
  "ds_read_b128 v[56:59], v192\n" \
  "s_waitcnt lgkmcnt(5)\n" \
  "v_mfma_f32_16x16x32_bf16 v[72:75], a[64:67], v[36:39], v[72:75]\n" \
  "ds_read_b128 v[60:63], v192 offset:18432\n" \
  "v_mfma_f32_16x16x32_bf16 v[68:71], a[136:139], v[32:35], v[68:71]\n" \
  "v_mfma_f32_16x16x32_bf16 v[76:79], a[136:139], v[36:39], v[76:79]\n" \
  "s_waitcnt lgkmcnt(5)\n" \
  "v_mfma_f32_16x16x32_bf16 v[64:67], a[68:71], v[40:43], v[64:67]\n" \
  "ds_read_b128 v[32:35], v191 offset:128\n" \
  "s_waitcnt lgkmcnt(5)\n" \
  "v_mfma_f32_16x16x32_bf16 v[72:75], a[68:71], v[44:47], v[72:75]\n" \
  "ds_read_b128 v[36:39], v191 offset:18560\n" \
  "v_mfma_f32_16x16x32_bf16 v[68:71], a[140:143], v[40:43], v[68:71]\n" \
  "v_mfma_f32_16x16x32_bf16 v[76:79], a[140:143], v[44:47], v[76:79]\n" \
  "s_waitcnt lgkmcnt(5)\n" \
  "v_mfma_f32_16x16x32_bf16 v[80:83], a[144:147], v[48:51], 0\n" \
  "ds_read_b128 v[40:43], v192 offset:128\n" \
  "s_waitcnt lgkmcnt(5)\n" \
  "v_mfma_f32_16x16x32_bf16 v[88:91], a[144:147], v[52:55], 0\n" \
  "ds_read_b128 v[44:47], v192 offset:18560\n" \
  "s_lshr_b32 s62, s49, 8\n" \
  "v_mfma_f32_16x16x32_bf16 v[84:87], a[216:219], v[48:51], 0\n" \
  "s_and_b32 s62, s62, 8192\n" \
  "s_xor_b32 s62, s62, s49\n" \
  "v_cvt_pk_bf16_f32 v96, v64, v65\n" \
  "v_mfma_f32_16x16x32_bf16 v[92:95], a[216:219], v[52:55], 0\n" \
  "s_waitcnt lgkmcnt(5)\n" \
  "v_cvt_pk_bf16_f32 v97, v66, v67\n" \
  "v_cvt_pk_bf16_f32 v98, v68, v69\n" \
  "v_mfma_f32_16x16x32_bf16 v[80:83], a[148:151], v[56:59], v[80:83]\n" \
  "ds_read_b128 v[48:51], v191 offset:256\n" \
  "s_waitcnt lgkmcnt(5)\n" \
  "v_cvt_pk_bf16_f32 v99, v70, v71\n" \
  "v_mfma_f32_16x16x32_bf16 v[88:91], a[148:151], v[60:63], v[88:91]\n" \
  "ds_read_b128 v[52:55], v191 offset:18688\n" \
  "s_lshl_b32 s63, s48, 14\n" \
  "v_mfma_f32_16x16x32_bf16 v[84:87], a[220:223], v[56:59], v[84:87]\n" \
  "s_xor_b32 s63, s63, s59\n" \
  "s_waitcnt lgkmcnt(5)\n" \
  "v_mfma_f32_16x16x32_bf16 v[92:95], a[220:223], v[60:63], v[92:95]\n" \
  "buffer_store_dwordx4 v[96:99], v190, s[40:43], s62 offen sc1\n" \
  "v_mfma_f32_16x16x32_bf16 v[80:83], a[152:155], v[32:35], v[80:83]\n" \
  "ds_read_b128 v[56:59], v192 offset:256\n" \
  "s_waitcnt lgkmcnt(5)\n" \
  "v_mfma_f32_16x16x32_bf16 v[88:91], a[152:155], v[36:39], v[88:91]\n" \
  "ds_read_b128 v[60:63], v192 offset:18688\n" \
  "v_mfma_f32_16x16x32_bf16 v[84:87], a[224:227], v[32:35], v[84:87]\n" \
  "buffer_store_dwordx4 v[96:99], v190, s[40:43], s63 offen sc1\n" \
  "v_mfma_f32_16x16x32_bf16 v[92:95], a[224:227], v[36:39], v[92:95]\n" \
  "s_waitcnt lgkmcnt(5)\n" \
  "v_cvt_pk_bf16_f32 v100, v72, v73\n" \
  "v_cvt_pk_bf16_f32 v101, v74, v75\n" \
  "v_mfma_f32_16x16x32_bf16 v[80:83], a[156:159], v[40:43], v[80:83]\n" \
  "ds_read_b128 v[32:35], v191 offset:384\n" \
  "s_waitcnt lgkmcnt(5)\n" \
  "v_cvt_pk_bf16_f32 v102, v76, v77\n" \
  "v_mfma_f32_16x16x32_bf16 v[88:91], a[156:159], v[44:47], v[88:91]\n" \
  "ds_read_b128 v[36:39], v191 offset:18816\n" \
  "v_cvt_pk_bf16_f32 v103, v78, v79\n" \
  "v_mfma_f32_16x16x32_bf16 v[84:87], a[228:231], v[40:43], v[84:87]\n" \
  "buffer_store_dwordx4 v[100:103], v190, s[40:43], s62 offen offset:1024 sc1\n" \
  "v_mfma_f32_16x16x32_bf16 v[92:95], a[228:231], v[44:47], v[92:95]\n" \
  "s_waitcnt lgkmcnt(5)\n" \
  "buffer_store_dwordx4 v[100:103], v190, s[40:43], s63 offen offset:1024 sc1\n" \
  "v_mfma_f32_16x16x32_bf16 v[80:83], a[160:163], v[48:51], v[80:83]\n" \
  "ds_read_b128 v[40:43], v192 offset:384\n" \
  "s_add_u32 s49, s49, 524288\n" \
  "s_waitcnt lgkmcnt(5)\n" \
  "v_mfma_f32_16x16x32_bf16 v[88:91], a[160:163], v[52:55], v[88:91]\n" \
  "ds_read_b128 v[44:47], v192 offset:18816\n" \
  "s_add_u32 s58, s48, 1\n" \
  "v_mfma_f32_16x16x32_bf16 v[84:87], a[232:235], v[48:51], v[84:87]\n" \
  "s_and_b32 s58, s58, 3\n" \
  "s_cselect_b32 s58, 0, 147456\n" \
  "s_sub_u32 s58, 36864, s58\n" \
  "v_mfma_f32_16x16x32_bf16 v[92:95], a[232:235], v[52:55], v[92:95]\n" \
  "s_waitcnt lgkmcnt(5)\n" \
  "v_add_u32 v165, s58, v191\n" \
  "v_add_u32 v164, s58, v192\n" \
  "v_mfma_f32_16x16x32_bf16 v[80:83], a[164:167], v[56:59], v[80:83]\n" \
  "ds_read_b128 v[48:51], v191 offset:512\n" \
  "s_waitcnt lgkmcnt(5)\n" \
  "v_mfma_f32_16x16x32_bf16 v[88:91], a[164:167], v[60:63], v[88:91]\n" \
  "ds_read_b128 v[52:55], v191 offset:18944\n" \
  "v_mfma_f32_16x16x32_bf16 v[84:87], a[236:239], v[56:59], v[84:87]\n" \
  "v_mfma_f32_16x16x32_bf16 v[92:95], a[236:239], v[60:63], v[92:95]\n" \
  "s_waitcnt lgkmcnt(5)\n" \
  "v_mfma_f32_16x16x32_bf16 v[80:83], a[168:171], v[32:35], v[80:83]\n" \
  "ds_read_b128 v[56:59], v192 offset:512\n" \
  "s_waitcnt lgkmcnt(5)\n" \
  "v_mfma_f32_16x16x32_bf16 v[88:91], a[168:171], v[36:39], v[88:91]\n" \
  "ds_read_b128 v[60:63], v192 offset:18944\n" \
  "v_mfma_f32_16x16x32_bf16 v[84:87], a[240:243], v[32:35], v[84:87]\n" \
  "v_mfma_f32_16x16x32_bf16 v[92:95], a[240:243], v[36:39], v[92:95]\n" \
  "s_waitcnt lgkmcnt(5)\n" \
  "v_mfma_f32_16x16x32_bf16 v[80:83], a[172:175], v[40:43], v[80:83]\n" \
  "ds_read_b128 v[32:35], v191 offset:640\n" \
  "s_waitcnt lgkmcnt(5)\n" \
  "v_mfma_f32_16x16x32_bf16 v[88:91], a[172:175], v[44:47], v[88:91]\n" \
  "ds_read_b128 v[36:39], v191 offset:19072\n" \
  "v_mfma_f32_16x16x32_bf16 v[84:87], a[244:247], v[40:43], v[84:87]\n" \
  "v_mfma_f32_16x16x32_bf16 v[92:95], a[244:247], v[44:47], v[92:95]\n" \
  "s_waitcnt lgkmcnt(5)\n" \
  "v_mfma_f32_16x16x32_bf16 v[80:83], a[176:179], v[48:51], v[80:83]\n" \
  "ds_read_b128 v[40:43], v192 offset:640\n" \
  "s_waitcnt lgkmcnt(5)\n" \
  "v_mfma_f32_16x16x32_bf16 v[88:91], a[176:179], v[52:55], v[88:91]\n" \
  "ds_read_b128 v[44:47], v192 offset:19072\n" \
  "v_mfma_f32_16x16x32_bf16 v[84:87], a[248:251], v[48:51], v[84:87]\n" \
  "s_add_u32 m0, s52, 5120\n" \
  "s_waitcnt lgkmcnt(5)\n" \
  "v_mfma_f32_16x16x32_bf16 v[92:95], a[248:251], v[52:55], v[92:95]\n" \
  "buffer_load_dwordx4 v201, s[44:47], s51 offen lds\n" \
  "v_mfma_f32_16x16x32_bf16 v[80:83], a[180:183], v[56:59], v[80:83]\n" \
  "ds_read_b128 v[48:51], v191 offset:768\n" \
  "s_waitcnt lgkmcnt(5)\n" \
  "v_mfma_f32_16x16x32_bf16 v[88:91], a[180:183], v[60:63], v[88:91]\n" \
  "ds_read_b128 v[52:55], v191 offset:19200\n" \
  "v_mfma_f32_16x16x32_bf16 v[84:87], a[252:255], v[56:59], v[84:87]\n" \
  "v_mfma_f32_16x16x32_bf16 v[92:95], a[252:255], v[60:63], v[92:95]\n" \
  "s_waitcnt lgkmcnt(5)\n" \
  "v_mfma_f32_16x16x32_bf16 v[80:83], a[184:187], v[32:35], v[80:83]\n" \
  "ds_read_b128 v[56:59], v192 offset:768\n" \
  "s_waitcnt lgkmcnt(5)\n" \
  "v_mfma_f32_16x16x32_bf16 v[88:91], a[184:187], v[36:39], v[88:91]\n" \
  "ds_read_b128 v[60:63], v192 offset:19200\n" \
  "v_mfma_f32_16x16x32_bf16 v[84:87], v[0:3], v[32:35], v[84:87]\n" \
  "s_add_u32 m0, s52, 6144\n" \
  "s_waitcnt lgkmcnt(5)\n" \
  "v_mfma_f32_16x16x32_bf16 v[92:95], v[0:3], v[36:39], v[92:95]\n" \
  "buffer_load_dwordx4 v202, s[44:47], s51 offen lds\n" \
  "v_mfma_f32_16x16x32_bf16 v[80:83], a[188:191], v[40:43], v[80:83]\n" \
  "ds_read_b128 v[32:35], v191 offset:896\n" \
  "s_waitcnt lgkmcnt(5)\n" \
  "v_mfma_f32_16x16x32_bf16 v[88:91], a[188:191], v[44:47], v[88:91]\n" \
  "ds_read_b128 v[36:39], v191 offset:19328\n" \
  "v_mfma_f32_16x16x32_bf16 v[84:87], v[4:7], v[40:43], v[84:87]\n" \
  "v_mfma_f32_16x16x32_bf16 v[92:95], v[4:7], v[44:47], v[92:95]\n" \
  "s_waitcnt lgkmcnt(5)\n" \
  "v_mfma_f32_16x16x32_bf16 v[80:83], a[192:195], v[48:51], v[80:83]\n" \
  "ds_read_b128 v[40:43], v192 offset:896\n" \
  "s_waitcnt lgkmcnt(5)\n" \
  "v_mfma_f32_16x16x32_bf16 v[88:91], a[192:195], v[52:55], v[88:91]\n" \
  "ds_read_b128 v[44:47], v192 offset:19328\n" \
  "v_mfma_f32_16x16x32_bf16 v[84:87], v[8:11], v[48:51], v[84:87]\n" \
  "s_add_u32 m0, s52, 7168\n" \
  "s_waitcnt lgkmcnt(5)\n" \
  "v_mfma_f32_16x16x32_bf16 v[92:95], v[8:11], v[52:55], v[92:95]\n" \
  "buffer_load_dwordx4 v203, s[44:47], s51 offen lds\n" \
  "v_mfma_f32_16x16x32_bf16 v[80:83], a[196:199], v[56:59], v[80:83]\n" \
  "ds_read_b128 v[48:51], v191 offset:1024\n" \
  "s_waitcnt lgkmcnt(5)\n" \
  "v_mfma_f32_16x16x32_bf16 v[88:91], a[196:199], v[60:63], v[88:91]\n" \
  "ds_read_b128 v[52:55], v191 offset:19456\n" \
  "v_mfma_f32_16x16x32_bf16 v[84:87], v[12:15], v[56:59], v[84:87]\n" \
  "v_mfma_f32_16x16x32_bf16 v[92:95], v[12:15], v[60:63], v[92:95]\n" \
  "s_waitcnt lgkmcnt(5)\n" \
  "v_mfma_f32_16x16x32_bf16 v[80:83], a[200:203], v[32:35], v[80:83]\n" \
  "ds_read_b128 v[56:59], v192 offset:1024\n" \
  "s_waitcnt lgkmcnt(5)\n" \
  "v_mfma_f32_16x16x32_bf16 v[88:91], a[200:203], v[36:39], v[88:91]\n" \
  "ds_read_b128 v[60:63], v192 offset:19456\n" \
  "v_mfma_f32_16x16x32_bf16 v[84:87], v[16:19], v[32:35], v[84:87]\n" \
  "s_add_u32 m0, s52, 8192\n" \
  "s_waitcnt lgkmcnt(5)\n" \
  "v_mfma_f32_16x16x32_bf16 v[92:95], v[16:19], v[36:39], v[92:95]\n" \
  "buffer_load_dwordx4 v204, s[44:47], s51 offen lds\n" \
  "v_mfma_f32_16x16x32_bf16 v[80:83], a[204:207], v[40:43], v[80:83]\n" \
  "ds_read_b128 v[32:35], v165\n" \
  "s_add_u32 s51, s51, 36864\n" \
  "s_waitcnt lgkmcnt(5)\n" \
  "v_mfma_f32_16x16x32_bf16 v[88:91], a[204:207], v[44:47], v[88:91]\n" \
  "ds_read_b128 v[36:39], v165 offset:18432\n" \
  "s_min_u32 s51, s51, 2322432\n" \
  "v_mfma_f32_16x16x32_bf16 v[84:87], v[20:23], v[40:43], v[84:87]\n" \
  "s_add_u32 s52, s52, 36864\n" \
  "v_mfma_f32_16x16x32_bf16 v[92:95], v[20:23], v[44:47], v[92:95]\n" \
  "s_cmp_ge_u32 s52, 163840\n" \
  "s_cselect_b32 s60, 147456, 0\n" \
  "s_sub_u32 s52, s52, s60\n" \
  "s_waitcnt lgkmcnt(5)\n" \
  "v_mfma_f32_16x16x32_bf16 v[80:83], a[208:211], v[48:51], v[80:83]\n" \
  "ds_read_b128 v[40:43], v164\n" \
  "s_waitcnt lgkmcnt(5)\n" \
  "v_mfma_f32_16x16x32_bf16 v[88:91], a[208:211], v[52:55], v[88:91]\n" \
  "ds_read_b128 v[44:47], v164 offset:18432\n" \
  "v_mfma_f32_16x16x32_bf16 v[84:87], v[24:27], v[48:51], v[84:87]\n" \
  "v_mov_b32 v191, v165\n" \
  "v_mov_b32 v192, v164\n" \
  "v_mfma_f32_16x16x32_bf16 v[92:95], v[24:27], v[52:55], v[92:95]\n" \
  "s_waitcnt lgkmcnt(5)\n" \
  "v_mfma_f32_16x16x32_bf16 v[80:83], a[212:215], v[56:59], v[80:83]\n" \
  "ds_read_b128 v[48:51], v165 offset:128\n" \
  "s_waitcnt lgkmcnt(5)\n" \
  "v_mfma_f32_16x16x32_bf16 v[88:91], a[212:215], v[60:63], v[88:91]\n" \
  "ds_read_b128 v[52:55], v165 offset:18560\n" \
  "v_mfma_f32_16x16x32_bf16 v[84:87], v[28:31], v[56:59], v[84:87]\n" \
  "s_add_u32 s48, s48, 1\n" \
  "s_cmp_lt_u32 s48, 58\n" \
  "v_mfma_f32_16x16x32_bf16 v[92:95], v[28:31], v[60:63], v[92:95]\n" \
  "s_cbranch_scc1 .LK_loop_%=\n" \
  "s_waitcnt lgkmcnt(0)\n" \
  "s_nop 7\n" \
  "s_lshr_b32 s62, s50, 8\n" \
  "s_and_b32 s62, s62, 8192\n" \
  "s_xor_b32 s62, s62, s50\n" \
  "v_cvt_pk_bf16_f32 v96, v80, v81\n" \
  "v_cvt_pk_bf16_f32 v97, v82, v83\n" \
  "v_cvt_pk_bf16_f32 v98, v84, v85\n" \
  "v_cvt_pk_bf16_f32 v99, v86, v87\n" \
  "buffer_store_dwordx4 v[96:99], v190, s[40:43], s62 offen sc1\n" \
  "v_cvt_pk_bf16_f32 v100, v88, v89\n" \
  "v_cvt_pk_bf16_f32 v101, v90, v91\n" \
  "v_cvt_pk_bf16_f32 v102, v92, v93\n" \
  "v_cvt_pk_bf16_f32 v103, v94, v95\n" \
  "buffer_store_dwordx4 v[100:103], v190, s[40:43], s62 offen offset:1024 sc1\n" \
  "s_waitcnt vmcnt(0)\n" \
  "s_barrier\n" \
  "s_mov_b32 s48, 64\n" \
  "s_mov_b32 s69, 0\n" \
  ".LK_ie_%=:\n" \
  "s_cmp_ge_u32 s96, 0xc0000000\n" \
  "s_cbranch_scc1 .LK_ie_x_%=\n" \
  "buffer_load_dwordx2 v[184:185], off, s[40:43], %41 sc1\n" \
  "s_waitcnt vmcnt(0)\n" \
  "s_add_u32 s69, s69, 1\n" \
  "s_cmp_lt_u32 s69, 2097152\n" \
  "v_readfirstlane_b32 s96, v184\n" \
  "s_cbranch_scc1 .LK_ie_%=\n" \
  ".LK_ie_x_%=:\n" \
  "v_mov_b32 v168, s48\n" \
  "buffer_store_dwordx2 v[168:169], v167, s[40:43], 0 offen sc1\n" \
  "s_branch .LK_end_%=\n" \
  ".LK_mea_%=:\n" \
  "s_waitcnt vmcnt(7)\n" \
  "s_barrier\n" \
  "v_mov_b32 v168, s56\n" \
  "s_mov_b64 exec, 1\n" \
  "buffer_store_dwordx2 v[168:169], v167, s[40:43], 0 offen sc1\n" \
  "s_mov_b64 exec, -1\n" \
  "s_branch .LK_mea_b_%=\n" \
  ".LK_meb_%=:\n" \
  "s_waitcnt vmcnt(7)\n" \
  "s_barrier\n" \
  "v_mov_b32 v168, s56\n" \
  "s_mov_b64 exec, 1\n" \
  "buffer_store_dwordx2 v[168:169], v167, s[40:43], 0 offen sc1\n" \
  "s_mov_b64 exec, -1\n" \
  "s_cmp_lg_u32 s48, 4\n" \
  "s_cbranch_scc1 .LK_in_x_%=\n" \
  "s_mov_b32 s69, 0\n" \
  ".LK_in_%=:\n" \
  "s_cmp_ge_u32 s96, 0xc0000000\n" \
  "s_cbranch_scc1 .LK_in_x_%=\n" \
  "buffer_load_dwordx2 v[184:185], off, s[40:43], %41 sc1\n" \
  "s_waitcnt vmcnt(0)\n" \
  "s_add_u32 s69, s69, 1\n" \
  "s_cmp_lt_u32 s69, 2097152\n" \
  "v_readfirstlane_b32 s96, v184\n" \
  "s_cbranch_scc1 .LK_in_%=\n" \
  ".LK_in_x_%=:\n" \
  "s_branch .LK_meb_b_%=\n" \
  ".LK_end_%=:\n" \
  "s_mov_b32 %0, s55\n"
#define K_FIRST 20
#define U_HELP 13
#define U_THIRD 0
#define U_WATCH 10
#define HAND_B 8
#define U_LEAD 14
#define VO_SWAP 1
static_assert(SDIR == 58, "generator");
#define U_ASM_TEXT \
  "s_mov_b64 s[40:41], %30\n" \
  "s_and_b32 s41, s41, 0xffff\n" \
  "s_mov_b32 s42, 255852544\n" \
  "s_mov_b32 s43, 0x00020000\n" \
  "s_mov_b32 s47, 0x00020000\n" \
  "s_lshl_b32 s72, %34, 12\n" \
  "buffer_load_dword v184, off, s[40:43], %42 sc1\n" \
  "s_and_b32 s73, %45, 255\n" \
  "s_bfe_u32 s74, %45, 0x80008\n" \
  "s_lshr_b32 s68, %45, 16\n" \
  "s_mov_b32 s75, s73\n" \
  "s_mov_b32 s70, 2\n" \
  "s_mov_b64 s[44:45], %31\n" \
  "s_and_b32 s45, s45, 0xffff\n" \
  "s_mov_b32 s46, 4718592\n" \
  "s_add_u32 s60, %35, 0\n" \
  "s_add_u32 m0, s72, 16384\n" \
  "s_nop 0\n" \
  "buffer_load_dwordx4 v170, s[44:47], s60 offen lds\n" \
  "buffer_load_dwordx4 v171, s[44:47], s60 offen offset:1024 lds\n" \
  "buffer_load_dwordx4 v172, s[44:47], s60 offen offset:2048 lds\n" \
  "buffer_load_dwordx4 v173, s[44:47], s60 offen offset:3072 lds\n" \
  "s_add_u32 s60, %35, 524288\n" \
  "s_add_u32 m0, s72, 32768\n" \
  "s_nop 0\n" \
  "buffer_load_dwordx4 v170, s[44:47], s60 offen lds\n" \
  "buffer_load_dwordx4 v171, s[44:47], s60 offen offset:1024 lds\n" \
  "buffer_load_dwordx4 v172, s[44:47], s60 offen offset:2048 lds\n" \
  "buffer_load_dwordx4 v173, s[44:47], s60 offen offset:3072 lds\n" \
  "s_add_u32 s60, %35, 1048576\n" \
  "s_add_u32 m0, s72, 49152\n" \
  "s_nop 0\n" \
  "buffer_load_dwordx4 v170, s[44:47], s60 offen lds\n" \
  "buffer_load_dwordx4 v171, s[44:47], s60 offen offset:1024 lds\n" \
  "buffer_load_dwordx4 v172, s[44:47], s60 offen offset:2048 lds\n" \
  "buffer_load_dwordx4 v173, s[44:47], s60 offen offset:3072 lds\n" \
  "s_add_u32 s60, %35, 1572864\n" \
  "s_add_u32 m0, s72, 65536\n" \
  "s_nop 0\n" \
  "buffer_load_dwordx4 v170, s[44:47], s60 offen lds\n" \
  "buffer_load_dwordx4 v171, s[44:47], s60 offen offset:1024 lds\n" \
  "buffer_load_dwordx4 v172, s[44:47], s60 offen offset:2048 lds\n" \
  "buffer_load_dwordx4 v173, s[44:47], s60 offen offset:3072 lds\n" \
  "s_add_u32 s60, %35, 2097152\n" \
  "s_add_u32 m0, s72, 81920\n" \
  "s_nop 0\n" \
  "buffer_load_dwordx4 v170, s[44:47], s60 offen lds\n" \
  "buffer_load_dwordx4 v171, s[44:47], s60 offen offset:1024 lds\n" \
  "buffer_load_dwordx4 v172, s[44:47], s60 offen offset:2048 lds\n" \
  "buffer_load_dwordx4 v173, s[44:47], s60 offen offset:3072 lds\n" \
  "s_add_u32 s60, %35, 2621440\n" \
  "s_add_u32 m0, s72, 98304\n" \
  "s_nop 0\n" \
  "buffer_load_dwordx4 v170, s[44:47], s60 offen lds\n" \
  "buffer_load_dwordx4 v171, s[44:47], s60 offen offset:1024 lds\n" \
  "buffer_load_dwordx4 v172, s[44:47], s60 offen offset:2048 lds\n" \
  "buffer_load_dwordx4 v173, s[44:47], s60 offen offset:3072 lds\n" \
  "s_add_u32 s60, %35, 3145728\n" \
  "s_add_u32 m0, s72, 114688\n" \
  "s_nop 0\n" \
  "buffer_load_dwordx4 v170, s[44:47], s60 offen lds\n" \
  "buffer_load_dwordx4 v171, s[44:47], s60 offen offset:1024 lds\n" \
  "buffer_load_dwordx4 v172, s[44:47], s60 offen offset:2048 lds\n" \
  "buffer_load_dwordx4 v173, s[44:47], s60 offen offset:3072 lds\n" \
  "s_add_u32 s60, %35, 3670016\n" \
  "s_add_u32 m0, s72, 131072\n" \
  "s_nop 0\n" \
  "buffer_load_dwordx4 v170, s[44:47], s60 offen lds\n" \
  "buffer_load_dwordx4 v171, s[44:47], s60 offen offset:1024 lds\n" \
  "buffer_load_dwordx4 v172, s[44:47], s60 offen offset:2048 lds\n" \
  "buffer_load_dwordx4 v173, s[44:47], s60 offen offset:3072 lds\n" \
  "s_add_u32 s60, %35, 4194304\n" \
  "s_add_u32 m0, s72, 147456\n" \
  "s_nop 0\n" \
  "buffer_load_dwordx4 v170, s[44:47], s60 offen lds\n" \
  "buffer_load_dwordx4 v171, s[44:47], s60 offen offset:1024 lds\n" \
  "buffer_load_dwordx4 v172, s[44:47], s60 offen offset:2048 lds\n" \
  "buffer_load_dwordx4 v173, s[44:47], s60 offen offset:3072 lds\n" \
  "s_waitcnt vmcnt(24)\n" \
  "s_barrier\n" \
  "v_add_u32 v186, 16384, v182\n" \
  "ds_read_b64_tr_b16 a[0:1], v186\n" \
  "ds_read_b64_tr_b16 a[2:3], v186 offset:1024\n" \
  "ds_read_b64_tr_b16 a[4:5], v186 offset:8192\n" \
  "ds_read_b64_tr_b16 a[6:7], v186 offset:9216\n" \
  "v_add_u32 v187, 16384, v183\n" \
  "ds_read_b64_tr_b16 a[72:73], v187\n" \
  "ds_read_b64_tr_b16 a[74:75], v187 offset:1024\n" \
  "ds_read_b64_tr_b16 a[76:77], v187 offset:8192\n" \
  "ds_read_b64_tr_b16 a[78:79], v187 offset:9216\n" \
  "v_add_u32 v186, 32768, v182\n" \
  "ds_read_b64_tr_b16 a[8:9], v186\n" \
  "ds_read_b64_tr_b16 a[10:11], v186 offset:1024\n" \
  "ds_read_b64_tr_b16 a[12:13], v186 offset:8192\n" \
  "ds_read_b64_tr_b16 a[14:15], v186 offset:9216\n" \
  "v_add_u32 v187, 32768, v183\n" \
  "ds_read_b64_tr_b16 a[80:81], v187\n" \
  "ds_read_b64_tr_b16 a[82:83], v187 offset:1024\n" \
  "ds_read_b64_tr_b16 a[84:85], v187 offset:8192\n" \
  "ds_read_b64_tr_b16 a[86:87], v187 offset:9216\n" \
  "v_add_u32 v186, 49152, v182\n" \
  "ds_read_b64_tr_b16 a[16:17], v186\n" \
  "ds_read_b64_tr_b16 a[18:19], v186 offset:1024\n" \
  "ds_read_b64_tr_b16 a[20:21], v186 offset:8192\n" \
  "ds_read_b64_tr_b16 a[22:23], v186 offset:9216\n" \
  "v_add_u32 v187, 49152, v183\n" \
  "ds_read_b64_tr_b16 a[88:89], v187\n" \
  "ds_read_b64_tr_b16 a[90:91], v187 offset:1024\n" \
  "ds_read_b64_tr_b16 a[92:93], v187 offset:8192\n" \
  "ds_read_b64_tr_b16 a[94:95], v187 offset:9216\n" \
  "s_waitcnt lgkmcnt(0)\n" \
  "s_barrier\n" \
  "s_mov_b64 s[44:45], %32\n" \
  "s_and_b32 s45, s45, 0xffff\n" \
  "s_mov_b32 s46, 4718592\n" \
  "s_add_u32 s60, %36, 0\n" \
  "s_add_u32 m0, s72, 16384\n" \
  "s_nop 0\n" \
  "buffer_load_dwordx4 v174, s[44:47], s60 offen lds\n" \
  "buffer_load_dwordx4 v175, s[44:47], s60 offen offset:1024 lds\n" \
  "buffer_load_dwordx4 v176, s[44:47], s60 offen offset:2048 lds\n" \
  "buffer_load_dwordx4 v177, s[44:47], s60 offen offset:3072 lds\n" \
  "s_add_u32 s60, %36, 73728\n" \
  "s_add_u32 m0, s72, 32768\n" \
  "s_nop 0\n" \
  "buffer_load_dwordx4 v174, s[44:47], s60 offen lds\n" \
  "buffer_load_dwordx4 v175, s[44:47], s60 offen offset:1024 lds\n" \
  "buffer_load_dwordx4 v176, s[44:47], s60 offen offset:2048 lds\n" \
  "buffer_load_dwordx4 v177, s[44:47], s60 offen offset:3072 lds\n" \
  "s_add_u32 s60, %36, 256\n" \
  "s_add_u32 m0, s72, 49152\n" \
  "s_nop 0\n" \
  "buffer_load_dwordx4 v174, s[44:47], s60 offen lds\n" \
  "buffer_load_dwordx4 v175, s[44:47], s60 offen offset:1024 lds\n" \
  "buffer_load_dwordx4 v176, s[44:47], s60 offen offset:2048 lds\n" \
  "buffer_load_dwordx4 v177, s[44:47], s60 offen offset:3072 lds\n" \
  "v_readfirstlane_b32 s55, v184\n" \
  "s_add_u32 s55, s55, 1\n" \
  "s_waitcnt vmcnt(24)\n" \
  "s_barrier\n" \
  "v_add_u32 v186, 65536, v182\n" \
  "ds_read_b64_tr_b16 a[24:25], v186\n" \
  "ds_read_b64_tr_b16 a[26:27], v186 offset:1024\n" \
  "ds_read_b64_tr_b16 a[28:29], v186 offset:8192\n" \
  "ds_read_b64_tr_b16 a[30:31], v186 offset:9216\n" \
  "v_add_u32 v187, 65536, v183\n" \
  "ds_read_b64_tr_b16 a[96:97], v187\n" \
  "ds_read_b64_tr_b16 a[98:99], v187 offset:1024\n" \
  "ds_read_b64_tr_b16 a[100:101], v187 offset:8192\n" \
  "ds_read_b64_tr_b16 a[102:103], v187 offset:9216\n" \
  "v_add_u32 v186, 81920, v182\n" \
  "ds_read_b64_tr_b16 a[32:33], v186\n" \
  "ds_read_b64_tr_b16 a[34:35], v186 offset:1024\n" \
  "ds_read_b64_tr_b16 a[36:37], v186 offset:8192\n" \
  "ds_read_b64_tr_b16 a[38:39], v186 offset:9216\n" \
  "v_add_u32 v187, 81920, v183\n" \
  "ds_read_b64_tr_b16 a[104:105], v187\n" \
  "ds_read_b64_tr_b16 a[106:107], v187 offset:1024\n" \
  "ds_read_b64_tr_b16 a[108:109], v187 offset:8192\n" \
  "ds_read_b64_tr_b16 a[110:111], v187 offset:9216\n" \
  "v_add_u32 v186, 98304, v182\n" \
  "ds_read_b64_tr_b16 a[40:41], v186\n" \
  "ds_read_b64_tr_b16 a[42:43], v186 offset:1024\n" \
  "ds_read_b64_tr_b16 a[44:45], v186 offset:8192\n" \
  "ds_read_b64_tr_b16 a[46:47], v186 offset:9216\n" \
  "v_add_u32 v187, 98304, v183\n" \
  "ds_read_b64_tr_b16 a[112:113], v187\n" \
  "ds_read_b64_tr_b16 a[114:115], v187 offset:1024\n" \
  "ds_read_b64_tr_b16 a[116:117], v187 offset:8192\n" \
  "ds_read_b64_tr_b16 a[118:119], v187 offset:9216\n" \
  "s_waitcnt lgkmcnt(0)\n" \
  "s_barrier\n" \
  "s_add_u32 s60, %36, 73984\n" \
  "s_add_u32 m0, s72, 65536\n" \
  "s_nop 0\n" \
  "buffer_load_dwordx4 v174, s[44:47], s60 offen lds\n" \
  "buffer_load_dwordx4 v175, s[44:47], s60 offen offset:1024 lds\n" \
  "buffer_load_dwordx4 v176, s[44:47], s60 offen offset:2048 lds\n" \
  "buffer_load_dwordx4 v177, s[44:47], s60 offen offset:3072 lds\n" \
  "s_add_u32 s60, %36, 512\n" \
  "s_add_u32 m0, s72, 81920\n" \
  "s_nop 0\n" \
  "buffer_load_dwordx4 v174, s[44:47], s60 offen lds\n" \
  "buffer_load_dwordx4 v175, s[44:47], s60 offen offset:1024 lds\n" \
  "buffer_load_dwordx4 v176, s[44:47], s60 offen offset:2048 lds\n" \
  "buffer_load_dwordx4 v177, s[44:47], s60 offen offset:3072 lds\n" \
  "s_add_u32 s60, %36, 74240\n" \
  "s_add_u32 m0, s72, 98304\n" \
  "s_nop 0\n" \
  "buffer_load_dwordx4 v174, s[44:47], s60 offen lds\n" \
  "buffer_load_dwordx4 v175, s[44:47], s60 offen offset:1024 lds\n" \
  "buffer_load_dwordx4 v176, s[44:47], s60 offen offset:2048 lds\n" \
  "buffer_load_dwordx4 v177, s[44:47], s60 offen offset:3072 lds\n" \
  "s_waitcnt vmcnt(24)\n" \
  "s_barrier\n" \
  "v_add_u32 v186, 114688, v182\n" \
  "ds_read_b64_tr_b16 a[48:49], v186\n" \
  "ds_read_b64_tr_b16 a[50:51], v186 offset:1024\n" \
  "ds_read_b64_tr_b16 a[52:53], v186 offset:8192\n" \
  "ds_read_b64_tr_b16 a[54:55], v186 offset:9216\n" \
  "v_add_u32 v187, 114688, v183\n" \
  "ds_read_b64_tr_b16 a[120:121], v187\n" \
  "ds_read_b64_tr_b16 a[122:123], v187 offset:1024\n" \
  "ds_read_b64_tr_b16 a[124:125], v187 offset:8192\n" \
  "ds_read_b64_tr_b16 a[126:127], v187 offset:9216\n" \
  "v_add_u32 v186, 131072, v182\n" \
  "ds_read_b64_tr_b16 a[56:57], v186\n" \
  "ds_read_b64_tr_b16 a[58:59], v186 offset:1024\n" \
  "ds_read_b64_tr_b16 a[60:61], v186 offset:8192\n" \
  "ds_read_b64_tr_b16 a[62:63], v186 offset:9216\n" \
  "v_add_u32 v187, 131072, v183\n" \
  "ds_read_b64_tr_b16 a[128:129], v187\n" \
  "ds_read_b64_tr_b16 a[130:131], v187 offset:1024\n" \
  "ds_read_b64_tr_b16 a[132:133], v187 offset:8192\n" \
  "ds_read_b64_tr_b16 a[134:135], v187 offset:9216\n" \
  "v_add_u32 v186, 147456, v182\n" \
  "ds_read_b64_tr_b16 a[64:65], v186\n" \
  "ds_read_b64_tr_b16 a[66:67], v186 offset:1024\n" \
  "ds_read_b64_tr_b16 a[68:69], v186 offset:8192\n" \
  "ds_read_b64_tr_b16 a[70:71], v186 offset:9216\n" \
  "v_add_u32 v187, 147456, v183\n" \
  "ds_read_b64_tr_b16 a[136:137], v187\n" \
  "ds_read_b64_tr_b16 a[138:139], v187 offset:1024\n" \
  "ds_read_b64_tr_b16 a[140:141], v187 offset:8192\n" \
  "ds_read_b64_tr_b16 a[142:143], v187 offset:9216\n" \
  "s_waitcnt lgkmcnt(0)\n" \
  "s_barrier\n" \
  "s_add_u32 s60, %36, 768\n" \
  "s_add_u32 m0, s72, 114688\n" \
  "s_nop 0\n" \
  "buffer_load_dwordx4 v174, s[44:47], s60 offen lds\n" \
  "buffer_load_dwordx4 v175, s[44:47], s60 offen offset:1024 lds\n" \
  "buffer_load_dwordx4 v176, s[44:47], s60 offen offset:2048 lds\n" \
  "buffer_load_dwordx4 v177, s[44:47], s60 offen offset:3072 lds\n" \
  "s_add_u32 s60, %36, 74496\n" \
  "s_add_u32 m0, s72, 131072\n" \
  "s_nop 0\n" \
  "buffer_load_dwordx4 v174, s[44:47], s60 offen lds\n" \
  "buffer_load_dwordx4 v175, s[44:47], s60 offen offset:1024 lds\n" \
  "buffer_load_dwordx4 v176, s[44:47], s60 offen offset:2048 lds\n" \
  "buffer_load_dwordx4 v177, s[44:47], s60 offen offset:3072 lds\n" \
  "s_add_u32 s60, %36, 0\n" \
  "s_add_u32 m0, s72, 147456\n" \
  "s_nop 0\n" \
  "buffer_load_dwordx4 v178, s[44:47], s60 offen lds\n" \
  "buffer_load_dwordx4 v179, s[44:47], s60 offen offset:1024 lds\n" \
  "buffer_load_dwordx4 v180, s[44:47], s60 offen offset:2048 lds\n" \
  "buffer_load_dwordx4 v181, s[44:47], s60 offen offset:3072 lds\n" \
  "s_waitcnt vmcnt(24)\n" \
  "s_barrier\n" \
  "v_add_u32 v186, 16384, v182\n" \
  "ds_read_b64_tr_b16 a[144:145], v186\n" \
  "ds_read_b64_tr_b16 a[146:147], v186 offset:1024\n" \
  "ds_read_b64_tr_b16 a[148:149], v186 offset:8192\n" \
  "ds_read_b64_tr_b16 a[150:151], v186 offset:9216\n" \
  "v_add_u32 v187, 16384, v183\n" \
  "ds_read_b64_tr_b16 a[160:161], v187\n" \
  "ds_read_b64_tr_b16 a[162:163], v187 offset:1024\n" \
  "ds_read_b64_tr_b16 a[164:165], v187 offset:8192\n" \
  "ds_read_b64_tr_b16 a[166:167], v187 offset:9216\n" \
  "v_add_u32 v186, 32768, v182\n" \
  "ds_read_b64_tr_b16 a[152:153], v186\n" \
  "ds_read_b64_tr_b16 a[154:155], v186 offset:1024\n" \
  "ds_read_b64_tr_b16 a[156:157], v186 offset:8192\n" \
  "ds_read_b64_tr_b16 a[158:159], v186 offset:9216\n" \
  "v_add_u32 v187, 32768, v183\n" \
  "ds_read_b64_tr_b16 a[168:169], v187\n" \
  "ds_read_b64_tr_b16 a[170:171], v187 offset:1024\n" \
  "ds_read_b64_tr_b16 a[172:173], v187 offset:8192\n" \
  "ds_read_b64_tr_b16 a[174:175], v187 offset:9216\n" \
  "v_add_u32 v186, 49152, v182\n" \
  "ds_read_b64_tr_b16 a[176:177], v186\n" \
  "ds_read_b64_tr_b16 a[178:179], v186 offset:1024\n" \
  "ds_read_b64_tr_b16 a[180:181], v186 offset:8192\n" \
  "ds_read_b64_tr_b16 a[182:183], v186 offset:9216\n" \
  "v_add_u32 v187, 49152, v183\n" \
  "ds_read_b64_tr_b16 a[192:193], v187\n" \
  "ds_read_b64_tr_b16 a[194:195], v187 offset:1024\n" \
  "ds_read_b64_tr_b16 a[196:197], v187 offset:8192\n" \
  "ds_read_b64_tr_b16 a[198:199], v187 offset:9216\n" \
  "s_waitcnt lgkmcnt(0)\n" \
  "s_barrier\n" \
  "s_mov_b64 s[44:45], %33\n" \
  "s_and_b32 s45, s45, 0xffff\n" \
  "s_mov_b32 s46, 2359296\n" \
  "s_mul_i32 s51, s73, 36864\n" \
  "s_mul_i32 s52, %34, 9216\n" \
  "s_add_u32 s52, s52, 16384\n" \
  "s_add_u32 m0, s52, 0\n" \
  "s_nop 0\n" \
  "buffer_load_dwordx4 v196, s[44:47], s51 offen lds\n" \
  "s_add_u32 m0, s52, 1024\n" \
  "s_nop 0\n" \
  "buffer_load_dwordx4 v197, s[44:47], s51 offen lds\n" \
  "s_add_u32 m0, s52, 2048\n" \
  "s_nop 0\n" \
  "buffer_load_dwordx4 v198, s[44:47], s51 offen lds\n" \
  "s_add_u32 m0, s52, 3072\n" \
  "s_nop 0\n" \
  "buffer_load_dwordx4 v199, s[44:47], s51 offen lds\n" \
  "s_add_u32 m0, s52, 4096\n" \
  "s_nop 0\n" \
  "buffer_load_dwordx4 v200, s[44:47], s51 offen lds\n" \
  "s_add_u32 m0, s52, 5120\n" \
  "s_nop 0\n" \
  "buffer_load_dwordx4 v201, s[44:47], s51 offen lds\n" \
  "s_add_u32 m0, s52, 6144\n" \
  "s_nop 0\n" \
  "buffer_load_dwordx4 v202, s[44:47], s51 offen lds\n" \
  "s_add_u32 m0, s52, 7168\n" \
  "s_nop 0\n" \
  "buffer_load_dwordx4 v203, s[44:47], s51 offen lds\n" \
  "s_add_u32 m0, s52, 8192\n" \
  "s_nop 0\n" \
  "buffer_load_dwordx4 v204, s[44:47], s51 offen lds\n" \
  "s_cmp_lt_u32 s51, 958464\n" \
  "s_cselect_b32 s60, 36864, 0\n" \
  "s_add_u32 s51, s51, s60\n" \
  "s_add_u32 s51, s51, 36864\n" \
  "s_min_u32 s51, s51, 2322432\n" \
  "s_add_u32 s52, s52, 36864\n" \
  "s_cmp_ge_u32 s52, 163840\n" \
  "s_cselect_b32 s60, 147456, 0\n" \
  "s_sub_u32 s52, s52, s60\n" \
  "s_cmp_eq_u32 s73, 0\n" \
  "s_cselect_b32 s51, 73728, s51\n" \
  "s_waitcnt vmcnt(21)\n" \
  "s_barrier\n" \
  "v_add_u32 v186, 65536, v182\n" \
  "ds_read_b64_tr_b16 a[184:185], v186\n" \
  "ds_read_b64_tr_b16 a[186:187], v186 offset:1024\n" \
  "ds_read_b64_tr_b16 a[188:189], v186 offset:8192\n" \
  "ds_read_b64_tr_b16 a[190:191], v186 offset:9216\n" \
  "v_add_u32 v187, 65536, v183\n" \
  "ds_read_b64_tr_b16 a[200:201], v187\n" \
  "ds_read_b64_tr_b16 a[202:203], v187 offset:1024\n" \
  "ds_read_b64_tr_b16 a[204:205], v187 offset:8192\n" \
  "ds_read_b64_tr_b16 a[206:207], v187 offset:9216\n" \
  "v_add_u32 v186, 81920, v182\n" \
  "ds_read_b64_tr_b16 a[208:209], v186\n" \
  "ds_read_b64_tr_b16 a[210:211], v186 offset:1024\n" \
  "ds_read_b64_tr_b16 a[212:213], v186 offset:8192\n" \
  "ds_read_b64_tr_b16 a[214:215], v186 offset:9216\n" \
  "v_add_u32 v187, 81920, v183\n" \
  "ds_read_b64_tr_b16 a[224:225], v187\n" \
  "ds_read_b64_tr_b16 a[226:227], v187 offset:1024\n" \
  "ds_read_b64_tr_b16 a[228:229], v187 offset:8192\n" \
  "ds_read_b64_tr_b16 a[230:231], v187 offset:9216\n" \
  "v_add_u32 v186, 98304, v182\n" \
  "ds_read_b64_tr_b16 a[216:217], v186\n" \
  "ds_read_b64_tr_b16 a[218:219], v186 offset:1024\n" \
  "ds_read_b64_tr_b16 a[220:221], v186 offset:8192\n" \
  "ds_read_b64_tr_b16 a[222:223], v186 offset:9216\n" \
  "v_add_u32 v187, 98304, v183\n" \
  "ds_read_b64_tr_b16 a[232:233], v187\n" \
  "ds_read_b64_tr_b16 a[234:235], v187 offset:1024\n" \
  "ds_read_b64_tr_b16 a[236:237], v187 offset:8192\n" \
  "ds_read_b64_tr_b16 a[238:239], v187 offset:9216\n" \
  "s_waitcnt lgkmcnt(0)\n" \
  "s_barrier\n" \
  "buffer_load_dwordx2 v[184:185], off, s[40:43], %41 sc1\n" \
  "s_add_u32 m0, s52, 0\n" \
  "s_nop 0\n" \
  "buffer_load_dwordx4 v196, s[44:47], s51 offen lds\n" \
  "s_add_u32 m0, s52, 1024\n" \
  "s_nop 0\n" \
  "buffer_load_dwordx4 v197, s[44:47], s51 offen lds\n" \
  "s_add_u32 m0, s52, 2048\n" \
  "s_nop 0\n" \
  "buffer_load_dwordx4 v198, s[44:47], s51 offen lds\n" \
  "s_add_u32 m0, s52, 3072\n" \
  "s_nop 0\n" \
  "buffer_load_dwordx4 v199, s[44:47], s51 offen lds\n" \
  "s_add_u32 m0, s52, 4096\n" \
  "s_nop 0\n" \
  "buffer_load_dwordx4 v200, s[44:47], s51 offen lds\n" \
  "s_add_u32 m0, s52, 5120\n" \
  "s_nop 0\n" \
  "buffer_load_dwordx4 v201, s[44:47], s51 offen lds\n" \
  "s_add_u32 m0, s52, 6144\n" \
  "s_nop 0\n" \
  "buffer_load_dwordx4 v202, s[44:47], s51 offen lds\n" \
  "s_add_u32 m0, s52, 7168\n" \
  "s_nop 0\n" \
  "buffer_load_dwordx4 v203, s[44:47], s51 offen lds\n" \
  "s_add_u32 m0, s52, 8192\n" \
  "s_nop 0\n" \
  "buffer_load_dwordx4 v204, s[44:47], s51 offen lds\n" \
  "s_cmp_lt_u32 s51, 958464\n" \
  "s_cselect_b32 s60, 36864, 0\n" \
  "s_add_u32 s51, s51, s60\n" \
  "s_add_u32 s51, s51, 36864\n" \
  "s_min_u32 s51, s51, 2322432\n" \
  "s_add_u32 s52, s52, 36864\n" \
  "s_cmp_ge_u32 s52, 163840\n" \
  "s_cselect_b32 s60, 147456, 0\n" \
  "s_sub_u32 s52, s52, s60\n" \
  "s_waitcnt vmcnt(19)\n" \
  "s_barrier\n" \
  "v_add_u32 v186, 114688, v182\n" \
  "ds_read_b64_tr_b16 a[240:241], v186\n" \
  "ds_read_b64_tr_b16 a[242:243], v186 offset:1024\n" \
  "ds_read_b64_tr_b16 a[244:245], v186 offset:8192\n" \
  "ds_read_b64_tr_b16 a[246:247], v186 offset:9216\n" \
  "v_add_u32 v187, 114688, v183\n" \
  "ds_read_b64_tr_b16 v[0:1], v187\n" \
  "ds_read_b64_tr_b16 v[2:3], v187 offset:1024\n" \
  "ds_read_b64_tr_b16 v[4:5], v187 offset:8192\n" \
  "ds_read_b64_tr_b16 v[6:7], v187 offset:9216\n" \
  "v_add_u32 v186, 131072, v182\n" \
  "ds_read_b64_tr_b16 a[248:249], v186\n" \
  "ds_read_b64_tr_b16 a[250:251], v186 offset:1024\n" \
  "ds_read_b64_tr_b16 a[252:253], v186 offset:8192\n" \
  "ds_read_b64_tr_b16 a[254:255], v186 offset:9216\n" \
  "v_add_u32 v187, 131072, v183\n" \
  "ds_read_b64_tr_b16 v[8:9], v187\n" \
  "ds_read_b64_tr_b16 v[10:11], v187 offset:1024\n" \
  "ds_read_b64_tr_b16 v[12:13], v187 offset:8192\n" \
  "ds_read_b64_tr_b16 v[14:15], v187 offset:9216\n" \
  "v_add_u32 v186, 147456, v182\n" \
  "ds_read_b64_tr_b16 v[16:17], v186\n" \
  "ds_read_b64_tr_b16 v[18:19], v186 offset:1024\n" \
  "ds_read_b64_tr_b16 v[20:21], v186 offset:8192\n" \
  "ds_read_b64_tr_b16 v[22:23], v186 offset:9216\n" \
  "v_add_u32 v187, 147456, v183\n" \
  "ds_read_b64_tr_b16 v[24:25], v187\n" \
  "ds_read_b64_tr_b16 v[26:27], v187 offset:1024\n" \
  "ds_read_b64_tr_b16 v[28:29], v187 offset:8192\n" \
  "ds_read_b64_tr_b16 v[30:31], v187 offset:9216\n" \
  "s_waitcnt lgkmcnt(0)\n" \
  "s_mov_b32 s69, 0\n" \
  "s_waitcnt vmcnt(9)\n" \
  "s_branch .LU_go1_%=\n" \
  ".LU_go_%=:\n" \
  "buffer_load_dwordx2 v[184:185], off, s[40:43], %41 sc1\n" \
  "s_waitcnt vmcnt(0)\n" \
  ".LU_go1_%=:\n" \
  "v_readfirstlane_b32 s96, v184\n" \
  "v_readfirstlane_b32 s63, v185\n" \
  "s_cmp_eq_u32 s63, s55\n" \
  "s_cselect_b32 s96, s96, 0\n" \
  "s_cmp_ge_u32 s96, 0x80000000\n" \
  "s_cbranch_scc1 .LU_go_x_%=\n" \
  "s_sleep 4\n" \
  "s_add_u32 s69, s69, 1\n" \
  "s_cmp_lt_u32 s69, 2097152\n" \
  "s_cbranch_scc1 .LU_go_%=\n" \
  "v_mov_b32 v184, 1\n" \
  "v_mov_b32 v185, s55\n" \
  "buffer_store_dwordx2 v[184:185], off, s[40:43], %42 offset:128 sc1\n" \
  ".LU_go_x_%=:\n" \
  "s_and_b32 s60, s96, 2\n" \
  "s_lshl_b32 s60, s60, 12\n" \
  "s_xor_b32 s53, %39, s60\n" \
  "s_xor_b32 s54, %40, s60\n" \
  "s_andn2_b32 s60, 2, s96\n" \
  "s_lshl_b32 s60, s60, 12\n" \
  "s_add_u32 s49, %37, s60\n" \
  "s_add_u32 s50, %38, s60\n" \
  "s_xor_b32 s66, %38, s60\n" \
  "s_xor_b32 s59, %44, s60\n" \
  "s_or_b32 s50, s50, 0x40000000\n" \
  "s_mov_b32 s57, 2130706432\n" \
  "s_mov_b32 s48, 0\n" \
  "v_mov_b32 v169, s55\n" \
  "v_mov_b32 v167, 2130706432\n" \
  "v_mov_b32 v161, 2130706432\n" \
  "v_mov_b32 v166, 0\n" \
  "s_mov_b64 exec, 7\n" \
  "v_mov_b32 v167, %46\n" \
  "s_mov_b64 exec, 3\n" \
  "v_mov_b32 v167, s54\n" \
  "s_mov_b64 exec, 1\n" \
  "v_mov_b32 v167, s53\n" \
  "v_mov_b32 v161, s53\n" \
  "v_mov_b32 v166, 0\n" \
  "s_mov_b64 exec, -1\n" \
  "s_barrier\n" \
  "ds_read_b128 v[32:35], v191\n" \
  "ds_read_b128 v[36:39], v191 offset:18432\n" \
  "ds_read_b128 v[40:43], v192\n" \
  "ds_read_b128 v[44:47], v192 offset:18432\n" \
  "ds_read_b128 v[48:51], v191 offset:128\n" \
  "ds_read_b128 v[52:55], v191 offset:18560\n" \
  "s_waitcnt lgkmcnt(0)\n" \
  "v_mov_b64 v[164:165], 0\n" \
  ".LU_loop_%=:\n" \
  "v_mfma_f32_16x16x32_bf16 v[64:67], v[32:35], a[0:3], 0\n" \
  "ds_read_b128 v[56:59], v192 offset:128\n" \
  "v_mfma_f32_16x16x32_bf16 v[72:75], v[36:39], a[0:3], 0\n" \
  "ds_read_b128 v[60:63], v192 offset:18560\n" \
  "v_mfma_f32_16x16x32_bf16 v[68:71], v[32:35], a[72:75], 0\n" \
  "s_mov_b32 s63, s57\n" \
  "v_cvt_pk_bf16_f32 v104, v88, v89\n" \
  "v_cvt_pk_bf16_f32 v105, v90, v91\n" \
  "v_mfma_f32_16x16x32_bf16 v[76:79], v[36:39], a[72:75], 0\n" \
  "v_cvt_pk_bf16_f32 v106, v92, v93\n" \
  "v_cvt_pk_bf16_f32 v107, v94, v95\n" \
  "v_mfma_f32_16x16x32_bf16 v[64:67], v[40:43], a[4:7], v[64:67]\n" \
  "ds_read_b128 v[32:35], v191 offset:256\n" \
  "v_mfma_f32_16x16x32_bf16 v[72:75], v[44:47], a[4:7], v[72:75]\n" \
  "ds_read_b128 v[36:39], v191 offset:18688\n" \
  "v_mfma_f32_16x16x32_bf16 v[68:71], v[40:43], a[76:79], v[68:71]\n" \
  "buffer_store_dwordx4 v[104:107], v190, s[40:43], s63 offen sc1\n" \
  "v_mfma_f32_16x16x32_bf16 v[76:79], v[44:47], a[76:79], v[76:79]\n" \
  "v_mfma_f32_16x16x32_bf16 v[64:67], v[48:51], a[8:11], v[64:67]\n" \
  "ds_read_b128 v[40:43], v192 offset:256\n" \
  "v_mfma_f32_16x16x32_bf16 v[72:75], v[52:55], a[8:11], v[72:75]\n" \
  "ds_read_b128 v[44:47], v192 offset:18688\n" \
  "v_mfma_f32_16x16x32_bf16 v[68:71], v[48:51], a[80:83], v[68:71]\n" \
  "s_add_u32 m0, s52, 0\n" \
  "s_waitcnt lgkmcnt(5)\n" \
  "v_mfma_f32_16x16x32_bf16 v[76:79], v[52:55], a[80:83], v[76:79]\n" \
  "buffer_load_dwordx4 v196, s[44:47], s51 offen lds\n" \
  "v_mfma_f32_16x16x32_bf16 v[64:67], v[56:59], a[12:15], v[64:67]\n" \
  "ds_read_b128 v[48:51], v191 offset:384\n" \
  "s_mov_b32 s58, s75\n" \
  "s_waitcnt lgkmcnt(5)\n" \
  "v_mfma_f32_16x16x32_bf16 v[72:75], v[60:63], a[12:15], v[72:75]\n" \
  "ds_read_b128 v[52:55], v191 offset:18816\n" \
  "s_mul_i32 s58, s58, 8454144\n" \
  "v_mfma_f32_16x16x32_bf16 v[68:71], v[56:59], a[84:87], v[68:71]\n" \
  "s_mov_b32 s64, 8\n" \
  "s_and_b32 s58, s58, 983040\n" \
  "s_xor_b32 s50, s58, s66\n" \
  "v_mfma_f32_16x16x32_bf16 v[76:79], v[60:63], a[84:87], v[76:79]\n" \
  "s_xor_b32 s57, s58, s59\n" \
  "v_mul_i32_i24 v161, s70, v166\n" \
  "s_waitcnt lgkmcnt(5)\n" \
  "v_mfma_f32_16x16x32_bf16 v[64:67], v[32:35], a[16:19], v[64:67]\n" \
  "ds_read_b128 v[56:59], v192 offset:384\n" \
  "s_waitcnt lgkmcnt(5)\n" \
  "v_mfma_f32_16x16x32_bf16 v[72:75], v[36:39], a[16:19], v[72:75]\n" \
  "ds_read_b128 v[60:63], v192 offset:18816\n" \
  "v_mfma_f32_16x16x32_bf16 v[68:71], v[32:35], a[88:91], v[68:71]\n" \
  "s_add_u32 m0, s52, 1024\n" \
  "s_cmp_lt_u32 s73, 26\n" \
  "s_cselect_b32 s70, 2, 1\n" \
  "v_mfma_f32_16x16x32_bf16 v[76:79], v[36:39], a[88:91], v[76:79]\n" \
  "s_waitcnt lgkmcnt(5)\n" \
  "buffer_load_dwordx4 v197, s[44:47], s51 offen lds\n" \
  "v_mfma_f32_16x16x32_bf16 v[64:67], v[40:43], a[20:23], v[64:67]\n" \
  "ds_read_b128 v[32:35], v191 offset:512\n" \
  "s_lshl_b32 s71, s70, 19\n" \
  "s_waitcnt lgkmcnt(5)\n" \
  "v_mfma_f32_16x16x32_bf16 v[72:75], v[44:47], a[20:23], v[72:75]\n" \
  "ds_read_b128 v[36:39], v191 offset:18944\n" \
  "s_mov_b32 s75, s73\n" \
  "v_mfma_f32_16x16x32_bf16 v[68:71], v[40:43], a[92:95], v[68:71]\n" \
  "v_mfma_f32_16x16x32_bf16 v[76:79], v[44:47], a[92:95], v[76:79]\n" \
  "s_waitcnt lgkmcnt(5)\n" \
  "v_mfma_f32_16x16x32_bf16 v[64:67], v[48:51], a[24:27], v[64:67]\n" \
  "ds_read_b128 v[40:43], v192 offset:512\n" \
  "s_waitcnt lgkmcnt(5)\n" \
  "v_xor_b32 v160, 8, v194\n" \
  "v_mfma_f32_16x16x32_bf16 v[72:75], v[52:55], a[24:27], v[72:75]\n" \
  "ds_read_b128 v[44:47], v192 offset:18944\n" \
  "v_mfma_f32_16x16x32_bf16 v[68:71], v[48:51], a[96:99], v[68:71]\n" \
  "s_add_u32 m0, s52, 2048\n" \
  "v_add_u32 v168, s73, v161\n" \
  "v_mfma_f32_16x16x32_bf16 v[76:79], v[52:55], a[96:99], v[76:79]\n" \
  "s_waitcnt lgkmcnt(5)\n" \
  "buffer_load_dwordx4 v198, s[44:47], s51 offen lds\n" \
  "v_mfma_f32_16x16x32_bf16 v[64:67], v[56:59], a[28:31], v[64:67]\n" \
  "ds_read_b128 v[48:51], v191 offset:640\n" \
  "s_waitcnt lgkmcnt(5)\n" \
  "v_mfma_f32_16x16x32_bf16 v[72:75], v[60:63], a[28:31], v[72:75]\n" \
  "ds_read_b128 v[52:55], v191 offset:19072\n" \
  "v_mfma_f32_16x16x32_bf16 v[68:71], v[56:59], a[100:103], v[68:71]\n" \
  "s_waitcnt vmcnt(14)\n" \
  "s_barrier\n" \
  "v_mfma_f32_16x16x32_bf16 v[76:79], v[60:63], a[100:103], v[76:79]\n" \
  "ds_read_b64_tr_b16 v[104:105], v194\n" \
  "s_waitcnt lgkmcnt(6)\n" \
  "v_mfma_f32_16x16x32_bf16 v[64:67], v[32:35], a[32:35], v[64:67]\n" \
  "ds_read_b128 v[56:59], v192 offset:640\n" \
  "s_waitcnt lgkmcnt(6)\n" \
  "v_mfma_f32_16x16x32_bf16 v[72:75], v[36:39], a[32:35], v[72:75]\n" \
  "ds_read_b128 v[60:63], v192 offset:19072\n" \
  "v_mfma_f32_16x16x32_bf16 v[68:71], v[32:35], a[104:107], v[68:71]\n" \
  "s_add_u32 m0, s52, 3072\n" \
  "ds_read_b64_tr_b16 v[106:107], v194 offset:256\n" \
  "s_waitcnt lgkmcnt(7)\n" \
  "v_mfma_f32_16x16x32_bf16 v[76:79], v[36:39], a[104:107], v[76:79]\n" \
  "buffer_load_dwordx4 v199, s[44:47], s51 offen lds\n" \
  "v_mfma_f32_16x16x32_bf16 v[64:67], v[40:43], a[36:39], v[64:67]\n" \
  "ds_read_b128 v[32:35], v191 offset:768\n" \
  "s_waitcnt lgkmcnt(7)\n" \
  "v_mfma_f32_16x16x32_bf16 v[72:75], v[44:47], a[36:39], v[72:75]\n" \
  "ds_read_b128 v[36:39], v191 offset:19200\n" \
  "v_mfma_f32_16x16x32_bf16 v[68:71], v[40:43], a[108:111], v[68:71]\n" \
  "s_cmp_lt_u32 s48, s64\n" \
  "s_cbranch_scc1 .LU_e38_%=\n" \
  ".LU_e38_b_%=:\n" \
  "v_mfma_f32_16x16x32_bf16 v[76:79], v[44:47], a[108:111], v[76:79]\n" \
  "ds_read_b64_tr_b16 v[108:109], v160\n" \
  "s_waitcnt lgkmcnt(8)\n" \
  "v_mfma_f32_16x16x32_bf16 v[64:67], v[48:51], a[40:43], v[64:67]\n" \
  "ds_read_b128 v[40:43], v192 offset:768\n" \
  "s_waitcnt lgkmcnt(8)\n" \
  "v_mfma_f32_16x16x32_bf16 v[72:75], v[52:55], a[40:43], v[72:75]\n" \
  "ds_read_b128 v[44:47], v192 offset:19200\n" \
  "v_mfma_f32_16x16x32_bf16 v[68:71], v[48:51], a[112:115], v[68:71]\n" \
  "s_add_u32 m0, s52, 4096\n" \
  "ds_read_b64_tr_b16 v[110:111], v160 offset:256\n" \
  "s_waitcnt lgkmcnt(8)\n" \
  "v_mfma_f32_16x16x32_bf16 v[76:79], v[52:55], a[112:115], v[76:79]\n" \
  "buffer_load_dwordx4 v200, s[44:47], s51 offen lds\n" \
  "v_mfma_f32_16x16x32_bf16 v[64:67], v[56:59], a[44:47], v[64:67]\n" \
  "ds_read_b128 v[48:51], v191 offset:896\n" \
  "s_waitcnt lgkmcnt(8)\n" \
  "v_mfma_f32_16x16x32_bf16 v[72:75], v[60:63], a[44:47], v[72:75]\n" \
  "ds_read_b128 v[52:55], v191 offset:19328\n" \
  "v_mfma_f32_16x16x32_bf16 v[68:71], v[56:59], a[116:119], v[68:71]\n" \
  "ds_read_b64_tr_b16 v[112:113], v194 offset:2048\n" \
  "v_mfma_f32_16x16x32_bf16 v[76:79], v[60:63], a[116:119], v[76:79]\n" \
  "ds_read_b64_tr_b16 v[114:115], v194 offset:2304\n" \
  "s_waitcnt lgkmcnt(9)\n" \
  "v_mfma_f32_16x16x32_bf16 v[64:67], v[32:35], a[48:51], v[64:67]\n" \
  "ds_read_b128 v[56:59], v192 offset:896\n" \
  "s_waitcnt lgkmcnt(9)\n" \
  "v_mfma_f32_16x16x32_bf16 v[72:75], v[36:39], a[48:51], v[72:75]\n" \
  "ds_read_b128 v[60:63], v192 offset:19328\n" \
  "v_mfma_f32_16x16x32_bf16 v[68:71], v[32:35], a[120:123], v[68:71]\n" \
  "s_add_u32 m0, s52, 5120\n" \
  "ds_read_b64_tr_b16 v[116:117], v160 offset:2048\n" \
  "s_waitcnt lgkmcnt(9)\n" \
  "v_mfma_f32_16x16x32_bf16 v[76:79], v[36:39], a[120:123], v[76:79]\n" \
  "buffer_load_dwordx4 v201, s[44:47], s51 offen lds\n" \
  "v_mfma_f32_16x16x32_bf16 v[64:67], v[40:43], a[52:55], v[64:67]\n" \
  "ds_read_b128 v[32:35], v191 offset:1024\n" \
  "s_waitcnt lgkmcnt(9)\n" \
  "v_mfma_f32_16x16x32_bf16 v[72:75], v[44:47], a[52:55], v[72:75]\n" \
  "ds_read_b128 v[36:39], v191 offset:19456\n" \
  "v_mfma_f32_16x16x32_bf16 v[68:71], v[40:43], a[124:127], v[68:71]\n" \
  "s_cmp_lt_u32 s48, s64\n" \
  "s_cbranch_scc1 .LU_e54_%=\n" \
  ".LU_e54_b_%=:\n" \
  "v_mfma_f32_16x16x32_bf16 v[76:79], v[44:47], a[124:127], v[76:79]\n" \
  "ds_read_b64_tr_b16 v[118:119], v160 offset:2304\n" \
  "s_waitcnt lgkmcnt(9)\n" \
  "v_mfma_f32_16x16x32_bf16 v[64:67], v[48:51], a[56:59], v[64:67]\n" \
  "ds_read_b128 v[40:43], v192 offset:1024\n" \
  "s_waitcnt lgkmcnt(9)\n" \
  "v_mfma_f32_16x16x32_bf16 v[72:75], v[52:55], a[56:59], v[72:75]\n" \
  "ds_read_b128 v[44:47], v192 offset:19456\n" \
  "v_mfma_f32_16x16x32_bf16 v[68:71], v[48:51], a[128:131], v[68:71]\n" \
  "s_add_u32 m0, s52, 6144\n" \
  "ds_read_b64_tr_b16 v[120:121], v194 offset:4096\n" \
  "s_waitcnt lgkmcnt(8)\n" \
  "v_mfma_f32_16x16x32_bf16 v[76:79], v[52:55], a[128:131], v[76:79]\n" \
  "buffer_load_dwordx4 v202, s[44:47], s51 offen lds\n" \
  "v_mfma_f32_16x16x32_bf16 v[64:67], v[56:59], a[60:63], v[64:67]\n" \
  "ds_read_b64_tr_b16 v[122:123], v194 offset:4352\n" \
  "s_waitcnt lgkmcnt(8)\n" \
  "v_mfma_f32_16x16x32_bf16 v[72:75], v[60:63], a[60:63], v[72:75]\n" \
  "ds_read_b64_tr_b16 v[124:125], v160 offset:4096\n" \
  "v_mfma_f32_16x16x32_bf16 v[68:71], v[56:59], a[132:135], v[68:71]\n" \
  "s_add_u32 m0, s52, 7168\n" \
  "ds_read_b64_tr_b16 v[126:127], v160 offset:4352\n" \
  "s_waitcnt lgkmcnt(8)\n" \
  "v_mfma_f32_16x16x32_bf16 v[76:79], v[60:63], a[132:135], v[76:79]\n" \
  "buffer_load_dwordx4 v203, s[44:47], s51 offen lds\n" \
  "v_mfma_f32_16x16x32_bf16 v[64:67], v[32:35], a[64:67], v[64:67]\n" \
  "ds_read_b64_tr_b16 v[128:129], v194 offset:6144\n" \
  "s_waitcnt lgkmcnt(8)\n" \
  "v_mfma_f32_16x16x32_bf16 v[72:75], v[36:39], a[64:67], v[72:75]\n" \
  "ds_read_b64_tr_b16 v[130:131], v194 offset:6400\n" \
  "v_mfma_f32_16x16x32_bf16 v[68:71], v[32:35], a[136:139], v[68:71]\n" \
  "ds_read_b64_tr_b16 v[132:133], v160 offset:6144\n" \
  "v_mfma_f32_16x16x32_bf16 v[76:79], v[36:39], a[136:139], v[76:79]\n" \
  "ds_read_b64_tr_b16 v[134:135], v160 offset:6400\n" \
  "s_waitcnt lgkmcnt(9)\n" \
  "v_mfma_f32_16x16x32_bf16 v[64:67], v[40:43], a[68:71], v[64:67]\n" \
  "s_waitcnt lgkmcnt(8)\n" \
  "v_mfma_f32_16x16x32_bf16 v[72:75], v[44:47], a[68:71], v[72:75]\n" \
  "s_cmp_lt_u32 s48, s64\n" \
  "s_cbranch_scc1 .LU_e69_%=\n" \
  ".LU_e69_b_%=:\n" \
  "v_mfma_f32_16x16x32_bf16 v[68:71], v[40:43], a[140:143], v[68:71]\n" \
  "s_add_u32 m0, s52, 8192\n" \
  "v_mfma_f32_16x16x32_bf16 v[76:79], v[44:47], a[140:143], v[76:79]\n" \
  "buffer_load_dwordx4 v204, s[44:47], s51 offen lds\n" \
  "s_cmp_le_u32 s48, 5\n" \
  "s_cbranch_scc1 .LU_first_%=\n" \
  ".LU_phb_%=:\n" \
  "v_mfma_f32_16x16x32_bf16 v[88:91], v[104:107], a[144:147], 0\n" \
  "v_mfma_f32_16x16x32_bf16 v[92:95], v[108:111], a[144:147], 0\n" \
  "s_lshr_b32 s62, s49, 8\n" \
  "s_and_b32 s62, s62, 8192\n" \
  "s_xor_b32 s62, s62, s49\n" \
  "v_mfma_f32_16x16x32_bf16 v[88:91], v[112:115], a[148:151], v[88:91]\n" \
  "v_cvt_pk_bf16_f32 v80, v64, v65\n" \
  "v_cvt_pk_bf16_f32 v81, v66, v67\n" \
  "v_mfma_f32_16x16x32_bf16 v[92:95], v[116:119], a[148:151], v[92:95]\n" \
  "s_waitcnt lgkmcnt(6)\n" \
  "v_cvt_pk_bf16_f32 v82, v72, v73\n" \
  "v_cvt_pk_bf16_f32 v83, v74, v75\n" \
  "v_mfma_f32_16x16x32_bf16 v[88:91], v[120:123], a[152:155], v[88:91]\n" \
  "s_waitcnt lgkmcnt(4)\n" \
  "buffer_store_dwordx4 v[80:83], v190, s[40:43], s62 offen sc1\n" \
  "v_mfma_f32_16x16x32_bf16 v[92:95], v[124:127], a[152:155], v[92:95]\n" \
  "ds_write_b128 v193, v[80:83]\n" \
  "s_waitcnt lgkmcnt(3)\n" \
  "v_mfma_f32_16x16x32_bf16 v[88:91], v[128:131], a[156:159], v[88:91]\n" \
  "s_waitcnt lgkmcnt(1)\n" \
  "v_cvt_pk_bf16_f32 v84, v76, v77\n" \
  "v_cvt_pk_bf16_f32 v85, v78, v79\n" \
  "v_mfma_f32_16x16x32_bf16 v[92:95], v[132:135], a[156:159], v[92:95]\n" \
  "v_cvt_pk_bf16_f32 v86, v68, v69\n" \
  "v_cvt_pk_bf16_f32 v87, v70, v71\n" \
  "v_mfma_f32_16x16x32_bf16 v[96:99], v[104:107], a[160:163], 0\n" \
  "buffer_store_dwordx4 v[84:87], v190, s[40:43], s62 offen offset:1024 sc1\n" \
  "v_mfma_f32_16x16x32_bf16 v[100:103], v[108:111], a[160:163], 0\n" \
  "ds_write_b128 v193, v[84:87] offset:1024\n" \
  "v_mfma_f32_16x16x32_bf16 v[96:99], v[112:115], a[164:167], v[96:99]\n" \
  "s_xor_b32 s63, s50, 0\n" \
  "v_cvt_pk_bf16_f32 v136, v88, v89\n" \
  "v_cvt_pk_bf16_f32 v137, v90, v91\n" \
  "v_mfma_f32_16x16x32_bf16 v[100:103], v[116:119], a[164:167], v[100:103]\n" \
  "s_add_u32 s63, s63, 0\n" \
  "v_cvt_pk_bf16_f32 v138, v92, v93\n" \
  "v_cvt_pk_bf16_f32 v139, v94, v95\n" \
  "v_mfma_f32_16x16x32_bf16 v[96:99], v[120:123], a[168:171], v[96:99]\n" \
  "buffer_store_dwordx4 v[136:139], v190, s[40:43], s63 offen sc1\n" \
  "v_mfma_f32_16x16x32_bf16 v[100:103], v[124:127], a[168:171], v[100:103]\n" \
  "s_add_u32 s58, s48, 1\n" \
  "s_and_b32 s58, s58, 3\n" \
  "s_cselect_b32 s58, 0, 147456\n" \
  "v_mfma_f32_16x16x32_bf16 v[96:99], v[128:131], a[172:175], v[96:99]\n" \
  "s_sub_u32 s58, 36864, s58\n" \
  "v_add_u32 v163, s58, v191\n" \
  "v_add_u32 v162, s58, v192\n" \
  "v_mfma_f32_16x16x32_bf16 v[100:103], v[132:135], a[172:175], v[100:103]\n" \
  "s_cmp_lt_u32 s51, 958464\n" \
  "s_cselect_b32 s60, 36864, 0\n" \
  "s_add_u32 s51, s51, s60\n" \
  "v_mfma_f32_16x16x32_bf16 v[88:91], v[104:107], a[176:179], 0\n" \
  "s_add_u32 s51, s51, 36864\n" \
  "s_min_u32 s51, s51, 2322432\n" \
  "s_add_u32 s52, s52, 36864\n" \
  "v_mfma_f32_16x16x32_bf16 v[92:95], v[108:111], a[176:179], 0\n" \
  "s_cmp_ge_u32 s52, 163840\n" \
  "s_cselect_b32 s60, 147456, 0\n" \
  "s_sub_u32 s52, s52, s60\n" \
  "v_mfma_f32_16x16x32_bf16 v[88:91], v[112:115], a[180:183], v[88:91]\n" \
  "s_xor_b32 s63, s50, 1024\n" \
  "v_cvt_pk_bf16_f32 v136, v96, v97\n" \
  "v_cvt_pk_bf16_f32 v137, v98, v99\n" \
  "v_mfma_f32_16x16x32_bf16 v[92:95], v[116:119], a[180:183], v[92:95]\n" \
  "s_add_u32 s63, s63, 1048576\n" \
  "v_cvt_pk_bf16_f32 v138, v100, v101\n" \
  "v_cvt_pk_bf16_f32 v139, v102, v103\n" \
  "v_mfma_f32_16x16x32_bf16 v[88:91], v[120:123], a[184:187], v[88:91]\n" \
  "buffer_store_dwordx4 v[136:139], v190, s[40:43], s63 offen sc1\n" \
  "v_mfma_f32_16x16x32_bf16 v[92:95], v[124:127], a[184:187], v[92:95]\n" \
  "v_mfma_f32_16x16x32_bf16 v[88:91], v[128:131], a[188:191], v[88:91]\n" \
  "s_nop 2\n" \
  "v_mfma_f32_16x16x32_bf16 v[92:95], v[132:135], a[188:191], v[92:95]\n" \
  "v_mfma_f32_16x16x32_bf16 v[96:99], v[104:107], a[192:195], 0\n" \
  "v_mfma_f32_16x16x32_bf16 v[100:103], v[108:111], a[192:195], 0\n" \
  "v_mfma_f32_16x16x32_bf16 v[96:99], v[112:115], a[196:199], v[96:99]\n" \
  "s_xor_b32 s63, s50, 278528\n" \
  "v_cvt_pk_bf16_f32 v136, v88, v89\n" \
  "v_cvt_pk_bf16_f32 v137, v90, v91\n" \
  "v_mfma_f32_16x16x32_bf16 v[100:103], v[116:119], a[196:199], v[100:103]\n" \
  "s_add_u32 s63, s63, 8388608\n" \
  "v_cvt_pk_bf16_f32 v138, v92, v93\n" \
  "v_cvt_pk_bf16_f32 v139, v94, v95\n" \
  "v_mfma_f32_16x16x32_bf16 v[96:99], v[120:123], a[200:203], v[96:99]\n" \
  "buffer_store_dwordx4 v[136:139], v190, s[40:43], s63 offen sc1\n" \
  "v_mfma_f32_16x16x32_bf16 v[100:103], v[124:127], a[200:203], v[100:103]\n" \
  "v_mfma_f32_16x16x32_bf16 v[96:99], v[128:131], a[204:207], v[96:99]\n" \
  "v_readfirstlane_b32 s58, v164\n" \
  "v_readfirstlane_b32 s61, v165\n" \
  "v_mfma_f32_16x16x32_bf16 v[100:103], v[132:135], a[204:207], v[100:103]\n" \
  "s_cmp_eq_u32 s61, s55\n" \
  "s_cselect_b32 s58, s58, 0\n" \
  "s_add_u32 s58, s58, s68\n" \
  "v_mfma_f32_16x16x32_bf16 v[88:91], v[104:107], a[208:211], 0\n" \
  "s_cmp_lt_u32 s58, s73\n" \
  "s_cbranch_scc1 .LU_fc_%=\n" \
  ".LU_fc_b_%=:\n" \
  "v_mfma_f32_16x16x32_bf16 v[92:95], v[108:111], a[208:211], 0\n" \
  "buffer_load_dwordx2 v[164:165], off, s[40:43], %43 sc1\n" \
  "v_mfma_f32_16x16x32_bf16 v[88:91], v[112:115], a[212:215], v[88:91]\n" \
  "s_xor_b32 s63, s50, 279552\n" \
  "v_cvt_pk_bf16_f32 v136, v96, v97\n" \
  "v_cvt_pk_bf16_f32 v137, v98, v99\n" \
  "v_mfma_f32_16x16x32_bf16 v[92:95], v[116:119], a[212:215], v[92:95]\n" \
  "s_add_u32 s63, s63, 9437184\n" \
  "v_cvt_pk_bf16_f32 v138, v100, v101\n" \
  "v_cvt_pk_bf16_f32 v139, v102, v103\n" \
  "v_mfma_f32_16x16x32_bf16 v[88:91], v[120:123], a[216:219], v[88:91]\n" \
  "buffer_store_dwordx4 v[136:139], v190, s[40:43], s63 offen sc1\n" \
  "v_mfma_f32_16x16x32_bf16 v[92:95], v[124:127], a[216:219], v[92:95]\n" \
  "v_mfma_f32_16x16x32_bf16 v[88:91], v[128:131], a[220:223], v[88:91]\n" \
  "s_nop 0\n" \
  "s_waitcnt vmcnt(16)\n" \
  "buffer_store_dwordx2 v[168:169], v167, s[40:43], 0 offen sc1\n" \
  "v_mfma_f32_16x16x32_bf16 v[92:95], v[132:135], a[220:223], v[92:95]\n" \
  "v_mfma_f32_16x16x32_bf16 v[96:99], v[104:107], a[224:227], 0\n" \
  "v_mfma_f32_16x16x32_bf16 v[100:103], v[108:111], a[224:227], 0\n" \
  "v_mfma_f32_16x16x32_bf16 v[96:99], v[112:115], a[228:231], v[96:99]\n" \
  "s_xor_b32 s63, s50, 557056\n" \
  "v_cvt_pk_bf16_f32 v136, v88, v89\n" \
  "v_cvt_pk_bf16_f32 v137, v90, v91\n" \
  "v_mfma_f32_16x16x32_bf16 v[100:103], v[116:119], a[228:231], v[100:103]\n" \
  "s_add_u32 s63, s63, 16777216\n" \
  "v_cvt_pk_bf16_f32 v138, v92, v93\n" \
  "v_cvt_pk_bf16_f32 v139, v94, v95\n" \
  "v_mfma_f32_16x16x32_bf16 v[96:99], v[120:123], a[232:235], v[96:99]\n" \
  "buffer_store_dwordx4 v[136:139], v190, s[40:43], s63 offen sc1\n" \
  "v_mfma_f32_16x16x32_bf16 v[100:103], v[124:127], a[232:235], v[100:103]\n" \
  "v_mfma_f32_16x16x32_bf16 v[96:99], v[128:131], a[236:239], v[96:99]\n" \
  "ds_read_b128 v[32:35], v163\n" \
  "v_mfma_f32_16x16x32_bf16 v[100:103], v[132:135], a[236:239], v[100:103]\n" \
  "ds_read_b128 v[36:39], v163 offset:18432\n" \
  "v_mfma_f32_16x16x32_bf16 v[88:91], v[104:107], a[240:243], 0\n" \
  "ds_read_b128 v[40:43], v162\n" \
  "v_mfma_f32_16x16x32_bf16 v[92:95], v[108:111], a[240:243], 0\n" \
  "ds_read_b128 v[44:47], v162 offset:18432\n" \
  "v_mfma_f32_16x16x32_bf16 v[88:91], v[112:115], a[244:247], v[88:91]\n" \
  "s_xor_b32 s63, s50, 558080\n" \
  "v_cvt_pk_bf16_f32 v136, v96, v97\n" \
  "v_cvt_pk_bf16_f32 v137, v98, v99\n" \
  "v_mfma_f32_16x16x32_bf16 v[92:95], v[116:119], a[244:247], v[92:95]\n" \
  "s_add_u32 s63, s63, 17825792\n" \
  "v_cvt_pk_bf16_f32 v138, v100, v101\n" \
  "v_cvt_pk_bf16_f32 v139, v102, v103\n" \
  "v_mfma_f32_16x16x32_bf16 v[88:91], v[120:123], a[248:251], v[88:91]\n" \
  "buffer_store_dwordx4 v[136:139], v190, s[40:43], s63 offen sc1\n" \
  "v_mfma_f32_16x16x32_bf16 v[92:95], v[124:127], a[248:251], v[92:95]\n" \
  "v_mfma_f32_16x16x32_bf16 v[88:91], v[128:131], a[252:255], v[88:91]\n" \
  "ds_read_b128 v[48:51], v163 offset:128\n" \
  "v_mfma_f32_16x16x32_bf16 v[92:95], v[132:135], a[252:255], v[92:95]\n" \
  "ds_read_b128 v[52:55], v163 offset:18560\n" \
  "v_mfma_f32_16x16x32_bf16 v[96:99], v[104:107], v[0:3], 0\n" \
  "v_mov_b32 v191, v163\n" \
  "v_mov_b32 v192, v162\n" \
  "v_mfma_f32_16x16x32_bf16 v[100:103], v[108:111], v[0:3], 0\n" \
  "s_add_u32 s49, s49, s71\n" \
  "v_xor_b32 v193, 8192, v193\n" \
  "v_xor_b32 v194, 8192, v194\n" \
  "v_mfma_f32_16x16x32_bf16 v[96:99], v[112:115], v[4:7], v[96:99]\n" \
  "s_xor_b32 s63, s50, 835584\n" \
  "v_cvt_pk_bf16_f32 v136, v88, v89\n" \
  "v_cvt_pk_bf16_f32 v137, v90, v91\n" \
  "v_mfma_f32_16x16x32_bf16 v[100:103], v[116:119], v[4:7], v[100:103]\n" \
  "s_add_u32 s63, s63, 25165824\n" \
  "v_cvt_pk_bf16_f32 v138, v92, v93\n" \
  "v_cvt_pk_bf16_f32 v139, v94, v95\n" \
  "v_mfma_f32_16x16x32_bf16 v[96:99], v[120:123], v[8:11], v[96:99]\n" \
  "buffer_store_dwordx4 v[136:139], v190, s[40:43], s63 offen sc1\n" \
  "v_mfma_f32_16x16x32_bf16 v[100:103], v[124:127], v[8:11], v[100:103]\n" \
  "s_add_u32 s48, s48, 1\n" \
  "s_add_u32 s73, s73, s70\n" \
  "v_mfma_f32_16x16x32_bf16 v[96:99], v[128:131], v[12:15], v[96:99]\n" \
  "s_cmp_eq_u32 s73, s74\n" \
  "s_cselect_b32 s49, 2130706432, s49\n" \
  "v_mfma_f32_16x16x32_bf16 v[100:103], v[132:135], v[12:15], v[100:103]\n" \
  "s_cmp_le_u32 s48, 5\n" \
  "s_addc_u32 s67, s73, 0\n" \
  "v_mfma_f32_16x16x32_bf16 v[88:91], v[104:107], v[16:19], 0\n" \
  "s_cmp_gt_u32 s67, s74\n" \
  "s_cselect_b32 s67, 2, 0\n" \
  "v_mfma_f32_16x16x32_bf16 v[92:95], v[108:111], v[16:19], 0\n" \
  "s_cmp_le_u32 s48, 0\n" \
  "s_addc_u32 s67, s67, 0\n" \
  "v_mfma_f32_16x16x32_bf16 v[88:91], v[112:115], v[20:23], v[88:91]\n" \
  "s_xor_b32 s63, s50, 836608\n" \
  "v_cvt_pk_bf16_f32 v104, v96, v97\n" \
  "v_cvt_pk_bf16_f32 v105, v98, v99\n" \
  "v_mfma_f32_16x16x32_bf16 v[92:95], v[116:119], v[20:23], v[92:95]\n" \
  "s_add_u32 s63, s63, 26214400\n" \
  "v_cvt_pk_bf16_f32 v106, v100, v101\n" \
  "v_cvt_pk_bf16_f32 v107, v102, v103\n" \
  "v_mfma_f32_16x16x32_bf16 v[88:91], v[120:123], v[24:27], v[88:91]\n" \
  "buffer_store_dwordx4 v[104:107], v190, s[40:43], s63 offen sc1\n" \
  "v_mfma_f32_16x16x32_bf16 v[92:95], v[124:127], v[24:27], v[92:95]\n" \
  "v_mfma_f32_16x16x32_bf16 v[88:91], v[128:131], v[28:31], v[88:91]\n" \
  "v_mfma_f32_16x16x32_bf16 v[92:95], v[132:135], v[28:31], v[92:95]\n" \
  "s_cmp_eq_u32 s67, 0\n" \
  "s_waitcnt lgkmcnt(0)\n" \
  ".LU_first_b_%=:\n" \
  "s_cbranch_scc1 .LU_loop_%=\n" \
  "s_cmp_eq_u32 s67, 1\n" \
  "s_cbranch_scc1 .LU_now_%=\n" \
  "s_waitcnt lgkmcnt(0)\n" \
  "s_nop 7\n" \
  "s_mov_b32 s63, s57\n" \
  "v_cvt_pk_bf16_f32 v104, v88, v89\n" \
  "v_cvt_pk_bf16_f32 v105, v90, v91\n" \
  "v_cvt_pk_bf16_f32 v106, v92, v93\n" \
  "v_cvt_pk_bf16_f32 v107, v94, v95\n" \
  "buffer_store_dwordx4 v[104:107], v190, s[40:43], s63 offen sc1\n" \
  "s_waitcnt vmcnt(0)\n" \
  "s_barrier\n" \
  "s_mov_b32 s48, 64\n" \
  "s_mov_b32 s69, 0\n" \
  ".LU_ie_%=:\n" \
  "s_cmp_ge_u32 s96, 0xc0000000\n" \
  "s_cbranch_scc1 .LU_ie_x_%=\n" \
  "buffer_load_dwordx2 v[184:185], off, s[40:43], %41 sc1\n" \
  "s_waitcnt vmcnt(0)\n" \
  "s_add_u32 s69, s69, 1\n" \
  "s_cmp_lt_u32 s69, 2097152\n" \
  "v_readfirstlane_b32 s96, v184\n" \
  "s_cbranch_scc1 .LU_ie_%=\n" \
  ".LU_ie_x_%=:\n" \
  "v_mov_b32 v168, s48\n" \
  "buffer_store_dwordx2 v[168:169], v167, s[40:43], 0 offen sc1\n" \
  "s_branch .LU_end_%=\n" \
  ".LU_e38_%=:\n" \
  "s_getreg_b32 s60, hwreg(HW_REG_IB_STS, 0, 4)\n" \
  "s_cmp_gt_u32 s60, 4\n" \
  "s_cbranch_scc1 .LU_e38_b_%=\n" \
  "s_waitcnt vmcnt(4)\n" \
  "v_add_u32 v168, s73, v161\n" \
  "s_mov_b64 exec, 2\n" \
  "buffer_store_dwordx2 v[168:169], v167, s[40:43], 0 offen sc1\n" \
  "s_mov_b64 exec, -1\n" \
  "s_mov_b32 s64, 0\n" \
  "s_branch .LU_e38_b_%=\n" \
  ".LU_e54_%=:\n" \
  "s_getreg_b32 s60, hwreg(HW_REG_IB_STS, 0, 4)\n" \
  "s_cmp_gt_u32 s60, 6\n" \
  "s_cbranch_scc1 .LU_e54_b_%=\n" \
  "s_waitcnt vmcnt(6)\n" \
  "v_add_u32 v168, s73, v161\n" \
  "s_mov_b64 exec, 2\n" \
  "buffer_store_dwordx2 v[168:169], v167, s[40:43], 0 offen sc1\n" \
  "s_mov_b64 exec, -1\n" \
  "s_mov_b32 s64, 0\n" \
  "s_branch .LU_e54_b_%=\n" \
  ".LU_e69_%=:\n" \
  "s_getreg_b32 s60, hwreg(HW_REG_IB_STS, 0, 4)\n" \
  "s_cmp_gt_u32 s60, 99\n" \
  "s_cbranch_scc1 .LU_e69_b_%=\n" \
  "s_waitcnt vmcnt(8)\n" \
  "v_add_u32 v168, s73, v161\n" \
  "s_mov_b64 exec, 2\n" \
  "buffer_store_dwordx2 v[168:169], v167, s[40:43], 0 offen sc1\n" \
  "s_mov_b64 exec, -1\n" \
  "s_mov_b32 s64, 0\n" \
  "s_branch .LU_e69_b_%=\n" \
  ".LU_fc_%=:\n" \
  "s_mov_b32 s69, 0\n" \
  ".LU_fc1_%=:\n" \
  "s_sleep 8\n" \
  "buffer_load_dwordx2 v[164:165], off, s[40:43], %43 sc1\n" \
  "s_waitcnt vmcnt(0)\n" \
  "v_readfirstlane_b32 s58, v164\n" \
  "v_readfirstlane_b32 s61, v165\n" \
  "s_cmp_eq_u32 s61, s55\n" \
  "s_cselect_b32 s58, s58, 0\n" \
  "s_add_u32 s58, s58, s68\n" \
  "s_cmp_lt_u32 s58, s73\n" \
  "s_cbranch_scc0 .LU_fc_b_%=\n" \
  "s_add_u32 s69, s69, 1\n" \
  "s_cmp_lt_u32 s69, 524288\n" \
  "s_cbranch_scc1 .LU_fc1_%=\n" \
  "v_mov_b32 v164, 1\n" \
  "v_mov_b32 v165, s55\n" \
  "buffer_store_dwordx2 v[164:165], off, s[40:43], %42 offset:128 sc1\n" \
  "s_waitcnt vmcnt(0)\n" \
  "v_mov_b32 v164, 100\n" \
  "s_branch .LU_fc_b_%=\n" \
  ".LU_first_%=:\n" \
  "s_waitcnt lgkmcnt(0)\n" \
  "s_cmp_eq_u32 s73, 0\n" \
  "s_cselect_b32 s70, 2, s70\n" \
  "s_lshl_b32 s71, s70, 19\n" \
  "s_nop 4\n" \
  "s_lshr_b32 s62, s49, 8\n" \
  "s_and_b32 s62, s62, 8192\n" \
  "s_xor_b32 s62, s62, s49\n" \
  "v_cvt_pk_bf16_f32 v80, v64, v65\n" \
  "v_cvt_pk_bf16_f32 v81, v66, v67\n" \
  "v_cvt_pk_bf16_f32 v82, v72, v73\n" \
  "v_cvt_pk_bf16_f32 v83, v74, v75\n" \
  "ds_write_b128 v193, v[80:83]\n" \
  "v_cvt_pk_bf16_f32 v84, v76, v77\n" \
  "v_cvt_pk_bf16_f32 v85, v78, v79\n" \
  "v_cvt_pk_bf16_f32 v86, v68, v69\n" \
  "v_cvt_pk_bf16_f32 v87, v70, v71\n" \
  "ds_write_b128 v193, v[84:87] offset:1024\n" \
  "s_cmp_eq_u32 s48, 5\n" \
  "s_cbranch_scc1 .LU_skip_%=\n" \
  "v_xor_b32 v160, 8, v194\n" \
  "s_waitcnt lgkmcnt(0)\n" \
  "s_barrier\n" \
  "v_xor_b32 v194, 8192, v194\n" \
  "v_xor_b32 v160, 8192, v160\n" \
  "ds_read_b64_tr_b16 v[104:105], v194\n" \
  "ds_read_b64_tr_b16 v[106:107], v194 offset:256\n" \
  "ds_read_b64_tr_b16 v[108:109], v160\n" \
  "ds_read_b64_tr_b16 v[110:111], v160 offset:256\n" \
  "ds_read_b64_tr_b16 v[112:113], v194 offset:2048\n" \
  "ds_read_b64_tr_b16 v[114:115], v194 offset:2304\n" \
  "ds_read_b64_tr_b16 v[116:117], v160 offset:2048\n" \
  "ds_read_b64_tr_b16 v[118:119], v160 offset:2304\n" \
  "ds_read_b64_tr_b16 v[120:121], v194 offset:4096\n" \
  "ds_read_b64_tr_b16 v[122:123], v194 offset:4352\n" \
  "ds_read_b64_tr_b16 v[124:125], v160 offset:4096\n" \
  "ds_read_b64_tr_b16 v[126:127], v160 offset:4352\n" \
  "ds_read_b64_tr_b16 v[128:129], v194 offset:6144\n" \
  "ds_read_b64_tr_b16 v[130:131], v194 offset:6400\n" \
  "ds_read_b64_tr_b16 v[132:133], v160 offset:6144\n" \
  "ds_read_b64_tr_b16 v[134:135], v160 offset:6400\n" \
  "s_mov_b32 s58, s73\n" \
  "s_mul_i32 s58, s58, 8454144\n" \
  "s_and_b32 s58, s58, 983040\n" \
  "s_xor_b32 s50, s58, s66\n" \
  "s_xor_b32 s57, s58, s59\n" \
  "v_xor_b32 v194, 8192, v194\n" \
  "s_waitcnt lgkmcnt(0)\n" \
  "s_branch .LU_phb_%=\n" \
  ".LU_skip_%=:\n" \
  "buffer_store_dwordx4 v[80:83], v190, s[40:43], s62 offen sc1\n" \
  "buffer_store_dwordx4 v[84:87], v190, s[40:43], s62 offen offset:1024 sc1\n" \
  "s_mov_b64 exec, 6\n" \
  "v_mov_b32 v166, -1\n" \
  "s_mov_b64 exec, -1\n" \
  "s_add_u32 s58, s48, 1\n" \
  "s_and_b32 s58, s58, 3\n" \
  "s_cselect_b32 s58, 0, 147456\n" \
  "s_sub_u32 s58, 36864, s58\n" \
  "s_cmp_lt_u32 s51, 958464\n" \
  "s_cselect_b32 s60, 36864, 0\n" \
  "s_add_u32 s51, s51, s60\n" \
  "s_add_u32 s51, s51, 36864\n" \
  "s_min_u32 s51, s51, 2322432\n" \
  "s_add_u32 s52, s52, 36864\n" \
  "s_cmp_ge_u32 s52, 163840\n" \
  "s_cselect_b32 s60, 147456, 0\n" \
  "s_sub_u32 s52, s52, s60\n" \
  "v_add_u32 v163, s58, v191\n" \
  "v_add_u32 v162, s58, v192\n" \
  "ds_read_b128 v[32:35], v163\n" \
  "ds_read_b128 v[36:39], v163 offset:18432\n" \
  "ds_read_b128 v[40:43], v162\n" \
  "ds_read_b128 v[44:47], v162 offset:18432\n" \
  "ds_read_b128 v[48:51], v163 offset:128\n" \
  "ds_read_b128 v[52:55], v163 offset:18560\n" \
  "s_add_u32 s49, s49, s71\n" \
  "s_mov_b32 s69, 0\n" \
  "v_mov_b32 v191, v163\n" \
  "v_mov_b32 v192, v162\n" \
  "v_xor_b32 v193, 8192, v193\n" \
  "v_xor_b32 v194, 8192, v194\n" \
  ".LU_in_%=:\n" \
  "s_cmp_ge_u32 s96, 0xc0000000\n" \
  "s_cbranch_scc1 .LU_in_x_%=\n" \
  "buffer_load_dwordx2 v[184:185], off, s[40:43], %41 sc1\n" \
  "s_waitcnt vmcnt(0)\n" \
  "s_add_u32 s69, s69, 1\n" \
  "s_cmp_lt_u32 s69, 2097152\n" \
  "v_readfirstlane_b32 s96, v184\n" \
  "s_cbranch_scc1 .LU_in_%=\n" \
  ".LU_in_x_%=:\n" \
  "s_mov_b32 s57, 2130706432\n" \
  "s_mov_b32 s69, 0\n" \
  ".LU_sk1_%=:\n" \
  "buffer_load_dwordx2 v[164:165], off, s[40:43], %43 sc1\n" \
  "s_waitcnt vmcnt(0)\n" \
  "v_readfirstlane_b32 s58, v164\n" \
  "v_readfirstlane_b32 s61, v165\n" \
  "s_cmp_eq_u32 s61, s55\n" \
  "s_cselect_b32 s58, s58, 0\n" \
  "s_add_u32 s58, s58, s68\n" \
  "s_cmp_lt_u32 s58, s73\n" \
  "s_cbranch_scc0 .LU_sk2_%=\n" \
  "v_mov_b32 v168, s73\n" \
  "s_mov_b64 exec, 2\n" \
  "buffer_store_dwordx2 v[168:169], v167, s[40:43], 0 offen sc1\n" \
  "s_mov_b64 exec, -1\n" \
  "s_sleep 16\n" \
  "s_add_u32 s69, s69, 1\n" \
  "s_cmp_lt_u32 s69, 524288\n" \
  "s_cbranch_scc1 .LU_sk1_%=\n" \
  "v_mov_b32 v164, 1\n" \
  "v_mov_b32 v165, s55\n" \
  "buffer_store_dwordx2 v[164:165], off, s[40:43], %42 offset:128 sc1\n" \
  "s_waitcnt vmcnt(0)\n" \
  ".LU_sk2_%=:\n" \
  "s_add_u32 s48, s48, 1\n" \
  "s_add_u32 s73, s73, s70\n" \
  "s_cmp_eq_u32 s73, s74\n" \
  "s_cselect_b32 s49, 2130706432, s49\n" \
  "s_cmp_le_u32 s48, 5\n" \
  "s_addc_u32 s67, s73, 0\n" \
  "s_cmp_gt_u32 s67, s74\n" \
  "s_cselect_b32 s67, 2, 0\n" \
  "s_cmp_le_u32 s48, 0\n" \
  "s_addc_u32 s67, s67, 0\n" \
  "s_cmp_eq_u32 s67, 0\n" \
  "s_waitcnt lgkmcnt(0)\n" \
  "s_branch .LU_first_b_%=\n" \
  ".LU_now_%=:\n" \
  "s_cmp_gt_u32 s48, 0\n" \
  "s_cbranch_scc1 .LU_loop_%=\n" \
  "s_nop 7\n" \
  "s_mov_b32 s63, s57\n" \
  "v_cvt_pk_bf16_f32 v104, v88, v89\n" \
  "v_cvt_pk_bf16_f32 v105, v90, v91\n" \
  "v_cvt_pk_bf16_f32 v106, v92, v93\n" \
  "v_cvt_pk_bf16_f32 v107, v94, v95\n" \
  "buffer_store_dwordx4 v[104:107], v190, s[40:43], s63 offen sc1\n" \
  "v_mov_b32 v168, s73\n" \
  "s_mov_b64 exec, 6\n" \
  "s_waitcnt vmcnt(0)\n" \
  "buffer_store_dwordx2 v[168:169], v167, s[40:43], 0 offen sc1\n" \
  "s_mov_b64 exec, -1\n" \
  "s_branch .LU_loop_%=\n" \
  ".LU_end_%=:\n" \
  "s_mov_b32 %0, s55\n"
#define P_ASM_VOPS "=&s"(tag), "+{v190}"(pi.l16), "+{v191}"(pi.xr0), "+{v192}"(pi.xr1), "+{v193}"(pi.ew), "+{v194}"(pi.er), "+{v195}"(pi.uo), "+{v196}"(pi.xs0), "+{v197}"(pi.xs1), "+{v198}"(pi.xs2), "+{v199}"(pi.xs3), "+{v200}"(pi.xs4), "+{v201}"(pi.xs5), "+{v202}"(pi.xs6), "+{v203}"(pi.xs7), "+{v204}"(pi.xs8), "+{v170}"(pi.vq0), "+{v171}"(pi.vq1), "+{v172}"(pi.vq2), "+{v173}"(pi.vq3), "+{v174}"(pi.vo0), "+{v175}"(pi.vo1), "+{v176}"(pi.vo2), "+{v177}"(pi.vo3), "+{v178}"(pi.v50), "+{v179}"(pi.v51), "+{v180}"(pi.v52), "+{v181}"(pi.v53), "+{v182}"(pi.tro0), "+{v183}"(pi.tro1)
#define P_ASM_SOPS "s"(pi.ws), "s"(pi.Wa), "s"(pi.Wb), "s"(pi.x), "s"(pi.w), "s"(pi.o_a), "s"(pi.o_b), "s"(pi.o_p), "s"(pi.o_q), "s"(pi.f_a), "s"(pi.f_b), "s"(pi.f_go), "s"(pi.o_ctl), "s"(pi.f_c), "s"(pi.u8), "s"(pi.rng), "s"(pi.f_d)
#define P_ASM_CLOB "v0", "v1", "v2", "v3", "v4", "v5", "v6", "v7", "v8", "v9", "v10", "v11", "v12", "v13", "v14", "v15", "v16", "v17", "v18", "v19", "v20", "v21", "v22", "v23", "v24", "v25", "v26", "v27", "v28", "v29", "v30", "v31", "v32", "v33", "v34", "v35", "v36", "v37", "v38", "v39", "v40", "v41", "v42", "v43", "v44", "v45", "v46", "v47", "v48", "v49", "v50", "v51", "v52", "v53", "v54", "v55", "v56", "v57", "v58", "v59", "v60", "v61", "v62", "v63", "v64", "v65", "v66", "v67", "v68", "v69", "v70", "v71", "v72", "v73", "v74", "v75", "v76", "v77", "v78", "v79", "v80", "v81", "v82", "v83", "v84", "v85", "v86", "v87", "v88", "v89", "v90", "v91", "v92", "v93", "v94", "v95", "v96", "v97", "v98", "v99", "v100", "v101", "v102", "v103", "v104", "v105", "v106", "v107", "v108", "v109", "v110", "v111", "v112", "v113", "v114", "v115", "v116", "v117", "v118", "v119", "v120", "v121", "v122", "v123", "v124", "v125", "v126", "v127", "v128", "v129", "v130", "v131", "v132", "v133", "v134", "v135", "v136", "v137", "v138", "v139", "v140", "v141", "v142", "v143", "v144", "v145", "v146", "v147", "v148", "v149", "v150", "v151", "v152", "v153", "v154", "v155", "v156", "v157", "v158", "v159", "v160", "v161", "v162", "v163", "v164", "v165", "v166", "v167", "v168", "v169", "v184", "v185", "v186", "v187", "v188", "v189", "a0", "a1", "a2", "a3", "a4", "a5", "a6", "a7", "a8", "a9", "a10", "a11", "a12", "a13", "a14", "a15", "a16", "a17", "a18", "a19", "a20", "a21", "a22", "a23", "a24", "a25", "a26", "a27", "a28", "a29", "a30", "a31", "a32", "a33", "a34", "a35", "a36", "a37", "a38", "a39", "a40", "a41", "a42", "a43", "a44", "a45", "a46", "a47", "a48", "a49", "a50", "a51", "a52", "a53", "a54", "a55", "a56", "a57", "a58", "a59", "a60", "a61", "a62", "a63", "a64", "a65", "a66", "a67", "a68", "a69", "a70", "a71", "a72", "a73", "a74", "a75", "a76", "a77", "a78", "a79", "a80", "a81", "a82", "a83", "a84", "a85", "a86", "a87", "a88", "a89", "a90", "a91", "a92", "a93", "a94", "a95", "a96", "a97", "a98", "a99", "a100", "a101", "a102", "a103", "a104", "a105", "a106", "a107", "a108", "a109", "a110", "a111", "a112", "a113", "a114", "a115", "a116", "a117", "a118", "a119", "a120", "a121", "a122", "a123", "a124", "a125", "a126", "a127", "a128", "a129", "a130", "a131", "a132", "a133", "a134", "a135", "a136", "a137", "a138", "a139", "a140", "a141", "a142", "a143", "a144", "a145", "a146", "a147", "a148", "a149", "a150", "a151", "a152", "a153", "a154", "a155", "a156", "a157", "a158", "a159", "a160", "a161", "a162", "a163", "a164", "a165", "a166", "a167", "a168", "a169", "a170", "a171", "a172", "a173", "a174", "a175", "a176", "a177", "a178", "a179", "a180", "a181", "a182", "a183", "a184", "a185", "a186", "a187", "a188", "a189", "a190", "a191", "a192", "a193", "a194", "a195", "a196", "a197", "a198", "a199", "a200", "a201", "a202", "a203", "a204", "a205", "a206", "a207", "a208", "a209", "a210", "a211", "a212", "a213", "a214", "a215", "a216", "a217", "a218", "a219", "a220", "a221", "a222", "a223", "a224", "a225", "a226", "a227", "a228", "a229", "a230", "a231", "a232", "a233", "a234", "a235", "a236", "a237", "a238", "a239", "a240", "a241", "a242", "a243", "a244", "a245", "a246", "a247", "a248", "a249", "a250", "a251", "a252", "a253", "a254", "a255", "s40", "s41", "s42", "s43", "s44", "s45", "s46", "s47", "s48", "s49", "s50", "s51", "s52", "s53", "s54", "s55", "s56", "s57", "s58", "s59", "s60", "s61", "s62", "s63", "s64", "s65", "s66", "s67", "s68", "s69", "s70", "s71", "s72", "s73", "s74", "s75", "s76", "s77", "s78", "s79", "s80", "s81", "s82", "s83", "s84", "s85", "s86", "s87", "s88", "s89", "s90", "s91", "s92", "s93", "s94", "s95", "s96", "s97", "s98", "s99", "vcc", "scc", "memory"
#define O_ASM_TEXT \
  "s_mov_b64 s[40:41], %11\n" \
  "s_and_b32 s41, s41, 0xffff\n" \
  "s_mov_b32 s42, 255852544\n" \
  "s_mov_b32 s43, 0x00020000\n" \
  "s_mov_b64 s[44:45], %12\n" \
  "s_and_b32 s45, s45, 0xffff\n" \
  "s_mov_b32 s46, 4718592\n" \
  "s_mov_b32 s47, 0x00020000\n" \
  "s_mul_i32 s62, %13, 24576\n" \
  "s_add_u32 s60, %15, 0\n" \
  "s_add_u32 m0, s62, 0\n" \
  "s_nop 0\n" \
  "buffer_load_dwordx4 v194, s[44:47], s60 offen lds\n" \
  "s_add_u32 m0, s62, 1024\n" \
  "s_nop 0\n" \
  "buffer_load_dwordx4 v195, s[44:47], s60 offen lds\n" \
  "s_add_u32 m0, s62, 2048\n" \
  "s_nop 0\n" \
  "buffer_load_dwordx4 v196, s[44:47], s60 offen lds\n" \
  "s_add_u32 s60, %15, 18432\n" \
  "s_add_u32 m0, s62, 3072\n" \
  "s_nop 0\n" \
  "buffer_load_dwordx4 v194, s[44:47], s60 offen lds\n" \
  "s_add_u32 m0, s62, 4096\n" \
  "s_nop 0\n" \
  "buffer_load_dwordx4 v195, s[44:47], s60 offen lds\n" \
  "s_add_u32 m0, s62, 5120\n" \
  "s_nop 0\n" \
  "buffer_load_dwordx4 v196, s[44:47], s60 offen lds\n" \
  "s_add_u32 s60, %15, 36864\n" \
  "s_add_u32 m0, s62, 6144\n" \
  "s_nop 0\n" \
  "buffer_load_dwordx4 v194, s[44:47], s60 offen lds\n" \
  "s_add_u32 m0, s62, 7168\n" \
  "s_nop 0\n" \
  "buffer_load_dwordx4 v195, s[44:47], s60 offen lds\n" \
  "s_add_u32 m0, s62, 8192\n" \
  "s_nop 0\n" \
  "buffer_load_dwordx4 v196, s[44:47], s60 offen lds\n" \
  "s_add_u32 s60, %15, 55296\n" \
  "s_add_u32 m0, s62, 9216\n" \
  "s_nop 0\n" \
  "buffer_load_dwordx4 v194, s[44:47], s60 offen lds\n" \
  "s_add_u32 m0, s62, 10240\n" \
  "s_nop 0\n" \
  "buffer_load_dwordx4 v195, s[44:47], s60 offen lds\n" \
  "s_add_u32 m0, s62, 11264\n" \
  "s_nop 0\n" \
  "buffer_load_dwordx4 v196, s[44:47], s60 offen lds\n" \
  "s_add_u32 s60, %15, 73728\n" \
  "s_add_u32 m0, s62, 12288\n" \
  "s_nop 0\n" \
  "buffer_load_dwordx4 v194, s[44:47], s60 offen lds\n" \
  "s_add_u32 m0, s62, 13312\n" \
  "s_nop 0\n" \
  "buffer_load_dwordx4 v195, s[44:47], s60 offen lds\n" \
  "s_add_u32 m0, s62, 14336\n" \
  "s_nop 0\n" \
  "buffer_load_dwordx4 v196, s[44:47], s60 offen lds\n" \
  "s_add_u32 s60, %15, 92160\n" \
  "s_add_u32 m0, s62, 15360\n" \
  "s_nop 0\n" \
  "buffer_load_dwordx4 v194, s[44:47], s60 offen lds\n" \
  "s_add_u32 m0, s62, 16384\n" \
  "s_nop 0\n" \
  "buffer_load_dwordx4 v195, s[44:47], s60 offen lds\n" \
  "s_add_u32 m0, s62, 17408\n" \
  "s_nop 0\n" \
  "buffer_load_dwordx4 v196, s[44:47], s60 offen lds\n" \
  "s_add_u32 s60, %15, 110592\n" \
  "s_add_u32 m0, s62, 18432\n" \
  "s_nop 0\n" \
  "buffer_load_dwordx4 v194, s[44:47], s60 offen lds\n" \
  "s_add_u32 m0, s62, 19456\n" \
  "s_nop 0\n" \
  "buffer_load_dwordx4 v195, s[44:47], s60 offen lds\n" \
  "s_add_u32 m0, s62, 20480\n" \
  "s_nop 0\n" \
  "buffer_load_dwordx4 v196, s[44:47], s60 offen lds\n" \
  "s_add_u32 s60, %15, 129024\n" \
  "s_add_u32 m0, s62, 21504\n" \
  "s_nop 0\n" \
  "buffer_load_dwordx4 v194, s[44:47], s60 offen lds\n" \
  "s_add_u32 m0, s62, 22528\n" \
  "s_nop 0\n" \
  "buffer_load_dwordx4 v195, s[44:47], s60 offen lds\n" \
  "s_add_u32 m0, s62, 23552\n" \
  "s_nop 0\n" \
  "buffer_load_dwordx4 v196, s[44:47], s60 offen lds\n" \
  "s_waitcnt vmcnt(0)\n" \
  "ds_read_b64_tr_b16 a[0:1], v197\n" \
  "ds_read_b64_tr_b16 a[2:3], v197 offset:3072\n" \
  "ds_read_b64_tr_b16 a[4:5], v197 offset:32\n" \
  "ds_read_b64_tr_b16 a[6:7], v197 offset:3104\n" \
  "ds_read_b64_tr_b16 a[8:9], v197 offset:64\n" \
  "ds_read_b64_tr_b16 a[10:11], v197 offset:3136\n" \
  "ds_read_b64_tr_b16 a[12:13], v197 offset:96\n" \
  "ds_read_b64_tr_b16 a[14:15], v197 offset:3168\n" \
  "ds_read_b64_tr_b16 a[16:17], v197 offset:128\n" \
  "ds_read_b64_tr_b16 a[18:19], v197 offset:3200\n" \
  "ds_read_b64_tr_b16 a[20:21], v197 offset:160\n" \
  "ds_read_b64_tr_b16 a[22:23], v197 offset:3232\n" \
  "ds_read_b64_tr_b16 a[24:25], v197 offset:6144\n" \
  "ds_read_b64_tr_b16 a[26:27], v197 offset:9216\n" \
  "ds_read_b64_tr_b16 a[28:29], v197 offset:6176\n" \
  "ds_read_b64_tr_b16 a[30:31], v197 offset:9248\n" \
  "ds_read_b64_tr_b16 a[32:33], v197 offset:6208\n" \
  "ds_read_b64_tr_b16 a[34:35], v197 offset:9280\n" \
  "ds_read_b64_tr_b16 a[36:37], v197 offset:6240\n" \
  "ds_read_b64_tr_b16 a[38:39], v197 offset:9312\n" \
  "ds_read_b64_tr_b16 a[40:41], v197 offset:6272\n" \
  "ds_read_b64_tr_b16 a[42:43], v197 offset:9344\n" \
  "ds_read_b64_tr_b16 a[44:45], v197 offset:6304\n" \
  "ds_read_b64_tr_b16 a[46:47], v197 offset:9376\n" \
  "ds_read_b64_tr_b16 a[48:49], v197 offset:12288\n" \
  "ds_read_b64_tr_b16 a[50:51], v197 offset:15360\n" \
  "ds_read_b64_tr_b16 a[52:53], v197 offset:12320\n" \
  "ds_read_b64_tr_b16 a[54:55], v197 offset:15392\n" \
  "ds_read_b64_tr_b16 a[56:57], v197 offset:12352\n" \
  "ds_read_b64_tr_b16 a[58:59], v197 offset:15424\n" \
  "ds_read_b64_tr_b16 a[60:61], v197 offset:12384\n" \
  "ds_read_b64_tr_b16 a[62:63], v197 offset:15456\n" \
  "ds_read_b64_tr_b16 a[64:65], v197 offset:12416\n" \
  "ds_read_b64_tr_b16 a[66:67], v197 offset:15488\n" \
  "ds_read_b64_tr_b16 a[68:69], v197 offset:12448\n" \
  "ds_read_b64_tr_b16 a[70:71], v197 offset:15520\n" \
  "ds_read_b64_tr_b16 a[72:73], v197 offset:18432\n" \
  "ds_read_b64_tr_b16 a[74:75], v197 offset:21504\n" \
  "ds_read_b64_tr_b16 a[76:77], v197 offset:18464\n" \
  "ds_read_b64_tr_b16 a[78:79], v197 offset:21536\n" \
  "ds_read_b64_tr_b16 a[80:81], v197 offset:18496\n" \
  "ds_read_b64_tr_b16 a[82:83], v197 offset:21568\n" \
  "ds_read_b64_tr_b16 a[84:85], v197 offset:18528\n" \
  "ds_read_b64_tr_b16 a[86:87], v197 offset:21600\n" \
  "ds_read_b64_tr_b16 a[88:89], v197 offset:18560\n" \
  "ds_read_b64_tr_b16 a[90:91], v197 offset:21632\n" \
  "ds_read_b64_tr_b16 a[92:93], v197 offset:18592\n" \
  "ds_read_b64_tr_b16 a[94:95], v197 offset:21664\n" \
  "s_waitcnt lgkmcnt(0)\n" \
  "s_add_u32 s60, %15, 147456\n" \
  "s_add_u32 m0, s62, 0\n" \
  "s_nop 0\n" \
  "buffer_load_dwordx4 v194, s[44:47], s60 offen lds\n" \
  "s_add_u32 m0, s62, 1024\n" \
  "s_nop 0\n" \
  "buffer_load_dwordx4 v195, s[44:47], s60 offen lds\n" \
  "s_add_u32 m0, s62, 2048\n" \
  "s_nop 0\n" \
  "buffer_load_dwordx4 v196, s[44:47], s60 offen lds\n" \
  "s_add_u32 s60, %15, 165888\n" \
  "s_add_u32 m0, s62, 3072\n" \
  "s_nop 0\n" \
  "buffer_load_dwordx4 v194, s[44:47], s60 offen lds\n" \
  "s_add_u32 m0, s62, 4096\n" \
  "s_nop 0\n" \
  "buffer_load_dwordx4 v195, s[44:47], s60 offen lds\n" \
  "s_add_u32 m0, s62, 5120\n" \
  "s_nop 0\n" \
  "buffer_load_dwordx4 v196, s[44:47], s60 offen lds\n" \
  "s_add_u32 s60, %15, 184320\n" \
  "s_add_u32 m0, s62, 6144\n" \
  "s_nop 0\n" \
  "buffer_load_dwordx4 v194, s[44:47], s60 offen lds\n" \
  "s_add_u32 m0, s62, 7168\n" \
  "s_nop 0\n" \
  "buffer_load_dwordx4 v195, s[44:47], s60 offen lds\n" \
  "s_add_u32 m0, s62, 8192\n" \
  "s_nop 0\n" \
  "buffer_load_dwordx4 v196, s[44:47], s60 offen lds\n" \
  "s_add_u32 s60, %15, 202752\n" \
  "s_add_u32 m0, s62, 9216\n" \
  "s_nop 0\n" \
  "buffer_load_dwordx4 v194, s[44:47], s60 offen lds\n" \
  "s_add_u32 m0, s62, 10240\n" \
  "s_nop 0\n" \
  "buffer_load_dwordx4 v195, s[44:47], s60 offen lds\n" \
  "s_add_u32 m0, s62, 11264\n" \
  "s_nop 0\n" \
  "buffer_load_dwordx4 v196, s[44:47], s60 offen lds\n" \
  "s_add_u32 s60, %15, 221184\n" \
  "s_add_u32 m0, s62, 12288\n" \
  "s_nop 0\n" \
  "buffer_load_dwordx4 v194, s[44:47], s60 offen lds\n" \
  "s_add_u32 m0, s62, 13312\n" \
  "s_nop 0\n" \
  "buffer_load_dwordx4 v195, s[44:47], s60 offen lds\n" \
  "s_add_u32 m0, s62, 14336\n" \
  "s_nop 0\n" \
  "buffer_load_dwordx4 v196, s[44:47], s60 offen lds\n" \
  "s_add_u32 s60, %15, 239616\n" \
  "s_add_u32 m0, s62, 15360\n" \
  "s_nop 0\n" \
  "buffer_load_dwordx4 v194, s[44:47], s60 offen lds\n" \
  "s_add_u32 m0, s62, 16384\n" \
  "s_nop 0\n" \
  "buffer_load_dwordx4 v195, s[44:47], s60 offen lds\n" \
  "s_add_u32 m0, s62, 17408\n" \
  "s_nop 0\n" \
  "buffer_load_dwordx4 v196, s[44:47], s60 offen lds\n" \
  "s_add_u32 s60, %15, 258048\n" \
  "s_add_u32 m0, s62, 18432\n" \
  "s_nop 0\n" \
  "buffer_load_dwordx4 v194, s[44:47], s60 offen lds\n" \
  "s_add_u32 m0, s62, 19456\n" \
  "s_nop 0\n" \
  "buffer_load_dwordx4 v195, s[44:47], s60 offen lds\n" \
  "s_add_u32 m0, s62, 20480\n" \
  "s_nop 0\n" \
  "buffer_load_dwordx4 v196, s[44:47], s60 offen lds\n" \
  "s_add_u32 s60, %15, 276480\n" \
  "s_add_u32 m0, s62, 21504\n" \
  "s_nop 0\n" \
  "buffer_load_dwordx4 v194, s[44:47], s60 offen lds\n" \
  "s_add_u32 m0, s62, 22528\n" \
  "s_nop 0\n" \
  "buffer_load_dwordx4 v195, s[44:47], s60 offen lds\n" \
  "s_add_u32 m0, s62, 23552\n" \
  "s_nop 0\n" \
  "buffer_load_dwordx4 v196, s[44:47], s60 offen lds\n" \
  "s_waitcnt vmcnt(0)\n" \
  "ds_read_b64_tr_b16 a[96:97], v197\n" \
  "ds_read_b64_tr_b16 a[98:99], v197 offset:3072\n" \
  "ds_read_b64_tr_b16 a[100:101], v197 offset:32\n" \
  "ds_read_b64_tr_b16 a[102:103], v197 offset:3104\n" \
  "ds_read_b64_tr_b16 a[104:105], v197 offset:64\n" \
  "ds_read_b64_tr_b16 a[106:107], v197 offset:3136\n" \
  "ds_read_b64_tr_b16 a[108:109], v197 offset:96\n" \
  "ds_read_b64_tr_b16 a[110:111], v197 offset:3168\n" \
  "ds_read_b64_tr_b16 a[112:113], v197 offset:128\n" \
  "ds_read_b64_tr_b16 a[114:115], v197 offset:3200\n" \
  "ds_read_b64_tr_b16 a[116:117], v197 offset:160\n" \
  "ds_read_b64_tr_b16 a[118:119], v197 offset:3232\n" \
  "ds_read_b64_tr_b16 a[120:121], v197 offset:6144\n" \
  "ds_read_b64_tr_b16 a[122:123], v197 offset:9216\n" \
  "ds_read_b64_tr_b16 a[124:125], v197 offset:6176\n" \
  "ds_read_b64_tr_b16 a[126:127], v197 offset:9248\n" \
  "ds_read_b64_tr_b16 a[128:129], v197 offset:6208\n" \
  "ds_read_b64_tr_b16 a[130:131], v197 offset:9280\n" \
  "ds_read_b64_tr_b16 a[132:133], v197 offset:6240\n" \
  "ds_read_b64_tr_b16 a[134:135], v197 offset:9312\n" \
  "ds_read_b64_tr_b16 a[136:137], v197 offset:6272\n" \
  "ds_read_b64_tr_b16 a[138:139], v197 offset:9344\n" \
  "ds_read_b64_tr_b16 a[140:141], v197 offset:6304\n" \
  "ds_read_b64_tr_b16 a[142:143], v197 offset:9376\n" \
  "ds_read_b64_tr_b16 a[144:145], v197 offset:12288\n" \
  "ds_read_b64_tr_b16 a[146:147], v197 offset:15360\n" \
  "ds_read_b64_tr_b16 a[148:149], v197 offset:12320\n" \
  "ds_read_b64_tr_b16 a[150:151], v197 offset:15392\n" \
  "ds_read_b64_tr_b16 a[152:153], v197 offset:12352\n" \
  "ds_read_b64_tr_b16 a[154:155], v197 offset:15424\n" \
  "ds_read_b64_tr_b16 a[156:157], v197 offset:12384\n" \
  "ds_read_b64_tr_b16 a[158:159], v197 offset:15456\n" \
  "ds_read_b64_tr_b16 a[160:161], v197 offset:12416\n" \
  "ds_read_b64_tr_b16 a[162:163], v197 offset:15488\n" \
  "ds_read_b64_tr_b16 a[164:165], v197 offset:12448\n" \
  "ds_read_b64_tr_b16 a[166:167], v197 offset:15520\n" \
  "ds_read_b64_tr_b16 a[168:169], v197 offset:18432\n" \
  "ds_read_b64_tr_b16 a[170:171], v197 offset:21504\n" \
  "ds_read_b64_tr_b16 a[172:173], v197 offset:18464\n" \
  "ds_read_b64_tr_b16 a[174:175], v197 offset:21536\n" \
  "ds_read_b64_tr_b16 a[176:177], v197 offset:18496\n" \
  "ds_read_b64_tr_b16 a[178:179], v197 offset:21568\n" \
  "ds_read_b64_tr_b16 a[180:181], v197 offset:18528\n" \
  "ds_read_b64_tr_b16 a[182:183], v197 offset:21600\n" \
  "ds_read_b64_tr_b16 a[184:185], v197 offset:18560\n" \
  "ds_read_b64_tr_b16 a[186:187], v197 offset:21632\n" \
  "ds_read_b64_tr_b16 a[188:189], v197 offset:18592\n" \
  "ds_read_b64_tr_b16 a[190:191], v197 offset:21664\n" \
  "s_waitcnt lgkmcnt(0)\n" \
  "s_barrier\n" \
  "s_mov_b32 s56, %20\n" \
  "s_mov_b32 s48, 0\n" \
  "s_mov_b32 s53, 2130706432\n" \
  "v_mov_b32 v187, %14\n" \
  "v_min_u32 v197, 1, v190\n" \
  "v_lshlrev_b32 v197, 30, v197\n" \
  "s_branch .LO_fl_%=\n" \
  ".LO_fl_b_%=:\n" \
  "s_and_b32 s48, s56, 255\n" \
  "s_bfe_u32 s55, s56, 0x80008\n" \
  "s_lshr_b32 s56, s56, 16\n" \
  "s_lshl_b32 s49, s48, 19\n" \
  "s_add_u32 s49, s49, %16\n" \
  "s_mul_i32 s50, s48, 393216\n" \
  "s_add_u32 s50, s50, %17\n" \
  "s_mov_b32 s53, 2130706432\n" \
  "s_mov_b32 s94, s48\n" \
  "v_mov_b32 v191, v199\n" \
  "v_mov_b32 v192, v200\n" \
  "s_mov_b32 s69, 0\n" \
  ".LO_p0_%=:\n" \
  "buffer_load_dwordx2 v[188:189], v193, s[40:43], 0 offen sc1\n" \
  "s_waitcnt vmcnt(0)\n" \
  "v_cmp_ne_u32 vcc, %14, v189\n" \
  "s_nop 1\n" \
  "v_cndmask_b32 v188, v188, 0, vcc\n" \
  "v_cmp_ge_i32 vcc, s94, v188\n" \
  "s_cbranch_vccz .LO_p0_x_%=\n" \
  "s_sleep 2\n" \
  "s_add_u32 s69, s69, 1\n" \
  "s_and_b32 s63, s69, 1023\n" \
  "s_cbranch_scc1 .LO_p0_%=\n" \
  "buffer_load_dwordx2 v[188:189], off, s[40:43], %19 offset:128 sc1\n" \
  "s_waitcnt vmcnt(0)\n" \
  "v_readfirstlane_b32 s63, v189\n" \
  "s_cmp_eq_u32 s63, %14\n" \
  "s_cbranch_scc1 .LO_p0_x_%=\n" \
  "s_cmp_le_u32 s69, 2097152\n" \
  "s_cbranch_scc1 .LO_p0_%=\n" \
  "v_mov_b32 v188, 1\n" \
  "v_mov_b32 v189, %14\n" \
  "buffer_store_dwordx2 v[188:189], off, s[40:43], %19 offset:128 sc1\n" \
  "s_waitcnt vmcnt(0)\n" \
  ".LO_p0_x_%=:\n" \
  "s_lshr_b32 s76, s49, 8\n" \
  "s_and_b32 s76, s76, 8192\n" \
  "s_xor_b32 s76, s76, s49\n" \
  "s_add_u32 s77, s76, 4096\n" \
  "s_add_u32 s78, s76, 16384\n" \
  "s_add_u32 s79, s76, 20480\n" \
  "s_add_u32 s49, s49, 524288\n" \
  "buffer_load_dwordx4 v[0:3], v190, s[40:43], s76 offen sc1\n" \
  "buffer_load_dwordx4 v[8:11], v190, s[40:43], s76 offen offset:2048 sc1\n" \
  "buffer_load_dwordx4 v[16:19], v190, s[40:43], s77 offen sc1\n" \
  "buffer_load_dwordx4 v[24:27], v190, s[40:43], s77 offen offset:2048 sc1\n" \
  "buffer_load_dwordx4 v[32:35], v190, s[40:43], s78 offen sc1\n" \
  "buffer_load_dwordx4 v[40:43], v190, s[40:43], s78 offen offset:2048 sc1\n" \
  "buffer_load_dwordx4 v[48:51], v190, s[40:43], s79 offen sc1\n" \
  "buffer_load_dwordx4 v[56:59], v190, s[40:43], s79 offen offset:2048 sc1\n" \
  "buffer_load_dwordx4 v[4:7], v190, s[40:43], s76 offen offset:1024 sc1\n" \
  "buffer_load_dwordx4 v[12:15], v190, s[40:43], s76 offen offset:3072 sc1\n" \
  "buffer_load_dwordx4 v[20:23], v190, s[40:43], s77 offen offset:1024 sc1\n" \
  "buffer_load_dwordx4 v[28:31], v190, s[40:43], s77 offen offset:3072 sc1\n" \
  "buffer_load_dwordx4 v[36:39], v190, s[40:43], s78 offen offset:1024 sc1\n" \
  "buffer_load_dwordx4 v[44:47], v190, s[40:43], s78 offen offset:3072 sc1\n" \
  "buffer_load_dwordx4 v[52:55], v190, s[40:43], s79 offen offset:1024 sc1\n" \
  "buffer_load_dwordx4 v[60:63], v190, s[40:43], s79 offen offset:3072 sc1\n" \
  "s_lshr_b32 s76, s49, 8\n" \
  "s_and_b32 s76, s76, 8192\n" \
  "s_xor_b32 s76, s76, s49\n" \
  "s_add_u32 s77, s76, 4096\n" \
  "s_add_u32 s78, s76, 16384\n" \
  "s_add_u32 s79, s76, 20480\n" \
  "s_add_u32 s49, s49, 524288\n" \
  "s_add_u32 s94, s94, 1\n" \
  "s_mov_b32 s54, 0\n" \
  "s_waitcnt vmcnt(0)\n" \
  "v_cmp_ne_u32 vcc, %14, v189\n" \
  "s_nop 1\n" \
  "v_cndmask_b32 v188, v188, 0, vcc\n" \
  "v_cmp_ge_i32 vcc, s94, v188\n" \
  ".LO_loop_%=:\n" \
  "s_cbranch_vccnz .LO_pl_%=\n" \
  ".LO_pl_b_%=:\n" \
  "s_waitcnt vmcnt(18)\n" \
  "v_mfma_f32_16x16x32_bf16 v[64:67], a[0:3], v[0:3], 0\n" \
  "s_waitcnt lgkmcnt(4)\n" \
  "buffer_load_dwordx2 v[188:189], v193, s[40:43], 0 offen sc1\n" \
  "v_mfma_f32_16x16x32_bf16 v[68:71], a[4:7], v[0:3], 0\n" \
  "s_waitcnt lgkmcnt(3)\n" \
  "v_mfma_f32_16x16x32_bf16 v[72:75], a[8:11], v[0:3], 0\n" \
  "ds_write_b128 v191, v[88:91] offset:6144\n" \
  "s_waitcnt lgkmcnt(3)\n" \
  "v_mfma_f32_16x16x32_bf16 v[76:79], a[12:15], v[0:3], 0\n" \
  "s_mov_b32 s81, s79\n" \
  "s_waitcnt lgkmcnt(2)\n" \
  "v_mfma_f32_16x16x32_bf16 v[80:83], a[16:19], v[0:3], 0\n" \
  "s_waitcnt lgkmcnt(1)\n" \
  "v_mfma_f32_16x16x32_bf16 v[84:87], a[20:23], v[0:3], 0\n" \
  "buffer_load_dwordx4 v[0:3], v190, s[40:43], s76 offen sc1\n" \
  "s_waitcnt vmcnt(19)\n" \
  "v_mfma_f32_16x16x32_bf16 v[64:67], a[24:27], v[8:11], v[64:67]\n" \
  "v_mfma_f32_16x16x32_bf16 v[68:71], a[28:31], v[8:11], v[68:71]\n" \
  "v_mfma_f32_16x16x32_bf16 v[72:75], a[32:35], v[8:11], v[72:75]\n" \
  "ds_write_b128 v191, v[92:95] offset:7168\n" \
  "v_mfma_f32_16x16x32_bf16 v[76:79], a[36:39], v[8:11], v[76:79]\n" \
  "v_mfma_f32_16x16x32_bf16 v[80:83], a[40:43], v[8:11], v[80:83]\n" \
  "s_waitcnt vmcnt(18)\n" \
  "v_mfma_f32_16x16x32_bf16 v[84:87], a[44:47], v[8:11], v[84:87]\n" \
  "buffer_load_dwordx4 v[8:11], v190, s[40:43], s76 offen offset:2048 sc1\n" \
  "v_mfma_f32_16x16x32_bf16 v[64:67], a[48:51], v[16:19], v[64:67]\n" \
  "v_mfma_f32_16x16x32_bf16 v[68:71], a[52:55], v[16:19], v[68:71]\n" \
  "v_mfma_f32_16x16x32_bf16 v[72:75], a[56:59], v[16:19], v[72:75]\n" \
  "ds_write_b128 v191, v[96:99] offset:8192\n" \
  "v_mfma_f32_16x16x32_bf16 v[76:79], a[60:63], v[16:19], v[76:79]\n" \
  "v_mfma_f32_16x16x32_bf16 v[80:83], a[64:67], v[16:19], v[80:83]\n" \
  "s_waitcnt vmcnt(18)\n" \
  "v_mfma_f32_16x16x32_bf16 v[84:87], a[68:71], v[16:19], v[84:87]\n" \
  "buffer_load_dwordx4 v[16:19], v190, s[40:43], s77 offen sc1\n" \
  "v_mfma_f32_16x16x32_bf16 v[64:67], a[72:75], v[24:27], v[64:67]\n" \
  "v_mfma_f32_16x16x32_bf16 v[68:71], a[76:79], v[24:27], v[68:71]\n" \
  "v_mfma_f32_16x16x32_bf16 v[72:75], a[80:83], v[24:27], v[72:75]\n" \
  "ds_write_b128 v191, v[100:103] offset:9216\n" \
  "v_mfma_f32_16x16x32_bf16 v[76:79], a[84:87], v[24:27], v[76:79]\n" \
  "v_mfma_f32_16x16x32_bf16 v[80:83], a[88:91], v[24:27], v[80:83]\n" \
  "s_waitcnt vmcnt(18)\n" \
  "v_mfma_f32_16x16x32_bf16 v[84:87], a[92:95], v[24:27], v[84:87]\n" \
  "buffer_load_dwordx4 v[24:27], v190, s[40:43], s77 offen offset:2048 sc1\n" \
  "v_mfma_f32_16x16x32_bf16 v[64:67], a[96:99], v[32:35], v[64:67]\n" \
  "v_mfma_f32_16x16x32_bf16 v[68:71], a[100:103], v[32:35], v[68:71]\n" \
  "v_mfma_f32_16x16x32_bf16 v[72:75], a[104:107], v[32:35], v[72:75]\n" \
  "ds_write_b128 v191, v[104:107] offset:10240\n" \
  "v_mfma_f32_16x16x32_bf16 v[76:79], a[108:111], v[32:35], v[76:79]\n" \
  "v_mfma_f32_16x16x32_bf16 v[80:83], a[112:115], v[32:35], v[80:83]\n" \
  "s_waitcnt vmcnt(18)\n" \
  "v_mfma_f32_16x16x32_bf16 v[84:87], a[116:119], v[32:35], v[84:87]\n" \
  "buffer_load_dwordx4 v[32:35], v190, s[40:43], s78 offen sc1\n" \
  "v_mfma_f32_16x16x32_bf16 v[64:67], a[120:123], v[40:43], v[64:67]\n" \
  "v_mfma_f32_16x16x32_bf16 v[68:71], a[124:127], v[40:43], v[68:71]\n" \
  "v_mfma_f32_16x16x32_bf16 v[72:75], a[128:131], v[40:43], v[72:75]\n" \
  "ds_write_b128 v191, v[108:111] offset:11264\n" \
  "v_mfma_f32_16x16x32_bf16 v[76:79], a[132:135], v[40:43], v[76:79]\n" \
  "v_xor_b32 v191, 65536, v191\n" \
  "v_mfma_f32_16x16x32_bf16 v[80:83], a[136:139], v[40:43], v[80:83]\n" \
  "s_waitcnt vmcnt(18)\n" \
  "v_mfma_f32_16x16x32_bf16 v[84:87], a[140:143], v[40:43], v[84:87]\n" \
  "buffer_load_dwordx4 v[40:43], v190, s[40:43], s78 offen offset:2048 sc1\n" \
  "v_mfma_f32_16x16x32_bf16 v[64:67], a[144:147], v[48:51], v[64:67]\n" \
  "v_mfma_f32_16x16x32_bf16 v[68:71], a[148:151], v[48:51], v[68:71]\n" \
  "s_add_i32 s60, s48, -1\n" \
  "v_mov_b32 v186, s60\n" \
  "v_mfma_f32_16x16x32_bf16 v[72:75], a[152:155], v[48:51], v[72:75]\n" \
  "v_mfma_f32_16x16x32_bf16 v[76:79], a[156:159], v[48:51], v[76:79]\n" \
  "s_waitcnt lgkmcnt(0)\n" \
  "s_waitcnt vmcnt(8)\n" \
  "s_barrier\n" \
  "v_mfma_f32_16x16x32_bf16 v[80:83], a[160:163], v[48:51], v[80:83]\n" \
  "ds_read_b128 v[120:123], v192\n" \
  "v_mfma_f32_16x16x32_bf16 v[84:87], a[164:167], v[48:51], v[84:87]\n" \
  "buffer_load_dwordx4 v[48:51], v190, s[40:43], s79 offen sc1\n" \
  "v_mfma_f32_16x16x32_bf16 v[64:67], a[168:171], v[56:59], v[64:67]\n" \
  "ds_read_b128 v[112:115], v192 offset:12288\n" \
  "v_mfma_f32_16x16x32_bf16 v[68:71], a[172:175], v[56:59], v[68:71]\n" \
  "buffer_store_dwordx2 v[186:187], v197, s[40:43], %18 offen sc1\n" \
  "v_mfma_f32_16x16x32_bf16 v[72:75], a[176:179], v[56:59], v[72:75]\n" \
  "ds_read_b128 v[116:119], v192 offset:24576\n" \
  "v_mfma_f32_16x16x32_bf16 v[76:79], a[180:183], v[56:59], v[76:79]\n" \
  "ds_read_b128 v[124:127], v192 offset:36864\n" \
  "v_mfma_f32_16x16x32_bf16 v[80:83], a[184:187], v[56:59], v[80:83]\n" \
  "ds_read_b128 v[128:131], v192 offset:1024\n" \
  "v_mfma_f32_16x16x32_bf16 v[84:87], a[188:191], v[56:59], v[84:87]\n" \
  "buffer_load_dwordx4 v[56:59], v190, s[40:43], s79 offen offset:2048 sc1\n" \
  "v_mfma_f32_16x16x32_bf16 v[88:91], a[0:3], v[4:7], 0\n" \
  "ds_read_b128 v[132:135], v192 offset:13312\n" \
  "ds_read_b128 v[136:139], v192 offset:25600\n" \
  "v_mfma_f32_16x16x32_bf16 v[92:95], a[4:7], v[4:7], 0\n" \
  "ds_read_b128 v[140:143], v192 offset:37888\n" \
  "ds_read_b128 v[144:147], v192 offset:2048\n" \
  "v_mfma_f32_16x16x32_bf16 v[96:99], a[8:11], v[4:7], 0\n" \
  "ds_write_b128 v191, v[64:67]\n" \
  "v_mfma_f32_16x16x32_bf16 v[100:103], a[12:15], v[4:7], 0\n" \
  "s_lshr_b32 s80, s49, 8\n" \
  "s_and_b32 s80, s80, 8192\n" \
  "v_mfma_f32_16x16x32_bf16 v[104:107], a[16:19], v[4:7], 0\n" \
  "s_xor_b32 s80, s80, s49\n" \
  "s_add_u32 s49, s49, 524288\n" \
  "v_mfma_f32_16x16x32_bf16 v[108:111], a[20:23], v[4:7], 0\n" \
  "buffer_load_dwordx4 v[4:7], v190, s[40:43], s76 offen offset:1024 sc1\n" \
  "v_mfma_f32_16x16x32_bf16 v[88:91], a[24:27], v[12:15], v[88:91]\n" \
  "ds_read_b128 v[148:151], v192 offset:14336\n" \
  "v_mfma_f32_16x16x32_bf16 v[92:95], a[28:31], v[12:15], v[92:95]\n" \
  "ds_read_b128 v[152:155], v192 offset:26624\n" \
  "ds_read_b128 v[156:159], v192 offset:38912\n" \
  "v_mfma_f32_16x16x32_bf16 v[96:99], a[32:35], v[12:15], v[96:99]\n" \
  "ds_write_b128 v191, v[68:71] offset:1024\n" \
  "v_mfma_f32_16x16x32_bf16 v[100:103], a[36:39], v[12:15], v[100:103]\n" \
  "s_add_u32 s79, s80, 20480\n" \
  "s_add_u32 s48, s48, 1\n" \
  "v_mfma_f32_16x16x32_bf16 v[104:107], a[40:43], v[12:15], v[104:107]\n" \
  "s_waitcnt lgkmcnt(12)\n" \
  "v_add_f32 v120, v120, v112\n" \
  "v_add_f32 v121, v121, v113\n" \
  "v_mfma_f32_16x16x32_bf16 v[108:111], a[44:47], v[12:15], v[108:111]\n" \
  "buffer_load_dwordx4 v[12:15], v190, s[40:43], s76 offen offset:3072 sc1\n" \
  "v_mfma_f32_16x16x32_bf16 v[88:91], a[48:51], v[20:23], v[88:91]\n" \
  "s_mov_b32 s76, s80\n" \
  "v_add_f32 v122, v122, v114\n" \
  "v_add_f32 v123, v123, v115\n" \
  "v_mfma_f32_16x16x32_bf16 v[92:95], a[52:55], v[20:23], v[92:95]\n" \
  "s_waitcnt lgkmcnt(10)\n" \
  "s_add_u32 s94, s48, 1\n" \
  "v_add_f32 v116, v116, v124\n" \
  "v_add_f32 v117, v117, v125\n" \
  "v_mfma_f32_16x16x32_bf16 v[96:99], a[56:59], v[20:23], v[96:99]\n" \
  "ds_write_b128 v191, v[72:75] offset:2048\n" \
  "v_mfma_f32_16x16x32_bf16 v[100:103], a[60:63], v[20:23], v[100:103]\n" \
  "v_add_f32 v118, v118, v126\n" \
  "v_add_f32 v119, v119, v127\n" \
  "v_mfma_f32_16x16x32_bf16 v[104:107], a[64:67], v[20:23], v[104:107]\n" \
  "v_add_f32 v120, v120, v116\n" \
  "v_add_f32 v121, v121, v117\n" \
  "v_mfma_f32_16x16x32_bf16 v[108:111], a[68:71], v[20:23], v[108:111]\n" \
  "buffer_load_dwordx4 v[20:23], v190, s[40:43], s77 offen offset:1024 sc1\n" \
  "v_mfma_f32_16x16x32_bf16 v[88:91], a[72:75], v[28:31], v[88:91]\n" \
  "v_add_f32 v122, v122, v118\n" \
  "v_add_f32 v123, v123, v119\n" \
  "v_mfma_f32_16x16x32_bf16 v[92:95], a[76:79], v[28:31], v[92:95]\n" \
  "v_cvt_pk_bf16_f32 v112, v120, v121\n" \
  "v_cvt_pk_bf16_f32 v113, v122, v123\n" \
  "v_mfma_f32_16x16x32_bf16 v[96:99], a[80:83], v[28:31], v[96:99]\n" \
  "ds_write_b128 v191, v[76:79] offset:3072\n" \
  "v_mfma_f32_16x16x32_bf16 v[100:103], a[84:87], v[28:31], v[100:103]\n" \
  "s_waitcnt lgkmcnt(10)\n" \
  "v_add_f32 v128, v128, v132\n" \
  "v_add_f32 v129, v129, v133\n" \
  "v_mfma_f32_16x16x32_bf16 v[104:107], a[88:91], v[28:31], v[104:107]\n" \
  "v_add_f32 v130, v130, v134\n" \
  "v_add_f32 v131, v131, v135\n" \
  "v_mfma_f32_16x16x32_bf16 v[108:111], a[92:95], v[28:31], v[108:111]\n" \
  "buffer_load_dwordx4 v[28:31], v190, s[40:43], s77 offen offset:3072 sc1\n" \
  "v_mfma_f32_16x16x32_bf16 v[88:91], a[96:99], v[36:39], v[88:91]\n" \
  "s_waitcnt lgkmcnt(8)\n" \
  "s_add_u32 s77, s80, 4096\n" \
  "v_add_f32 v136, v136, v140\n" \
  "v_add_f32 v137, v137, v141\n" \
  "v_mfma_f32_16x16x32_bf16 v[92:95], a[100:103], v[36:39], v[92:95]\n" \
  "v_add_f32 v138, v138, v142\n" \
  "v_add_f32 v139, v139, v143\n" \
  "v_mfma_f32_16x16x32_bf16 v[96:99], a[104:107], v[36:39], v[96:99]\n" \
  "ds_write_b128 v191, v[80:83] offset:4096\n" \
  "v_mfma_f32_16x16x32_bf16 v[100:103], a[108:111], v[36:39], v[100:103]\n" \
  "v_add_f32 v128, v128, v136\n" \
  "v_add_f32 v129, v129, v137\n" \
  "v_mfma_f32_16x16x32_bf16 v[104:107], a[112:115], v[36:39], v[104:107]\n" \
  "v_add_f32 v130, v130, v138\n" \
  "v_add_f32 v131, v131, v139\n" \
  "v_mfma_f32_16x16x32_bf16 v[108:111], a[116:119], v[36:39], v[108:111]\n" \
  "buffer_load_dwordx4 v[36:39], v190, s[40:43], s78 offen offset:1024 sc1\n" \
  "v_mfma_f32_16x16x32_bf16 v[88:91], a[120:123], v[44:47], v[88:91]\n" \
  "v_cvt_pk_bf16_f32 v114, v128, v129\n" \
  "v_cvt_pk_bf16_f32 v115, v130, v131\n" \
  "v_mfma_f32_16x16x32_bf16 v[92:95], a[124:127], v[44:47], v[92:95]\n" \
  "buffer_store_dwordx4 v[112:115], v190, s[40:43], s53 offen sc1\n" \
  "v_mfma_f32_16x16x32_bf16 v[96:99], a[128:131], v[44:47], v[96:99]\n" \
  "ds_write_b128 v191, v[84:87] offset:5120\n" \
  "v_mfma_f32_16x16x32_bf16 v[100:103], a[132:135], v[44:47], v[100:103]\n" \
  "s_waitcnt lgkmcnt(7)\n" \
  "v_add_f32 v144, v144, v148\n" \
  "v_add_f32 v145, v145, v149\n" \
  "v_mfma_f32_16x16x32_bf16 v[104:107], a[136:139], v[44:47], v[104:107]\n" \
  "v_add_f32 v146, v146, v150\n" \
  "v_add_f32 v147, v147, v151\n" \
  "v_mfma_f32_16x16x32_bf16 v[108:111], a[140:143], v[44:47], v[108:111]\n" \
  "buffer_load_dwordx4 v[44:47], v190, s[40:43], s78 offen offset:3072 sc1\n" \
  "v_mfma_f32_16x16x32_bf16 v[88:91], a[144:147], v[52:55], v[88:91]\n" \
  "s_waitcnt lgkmcnt(5)\n" \
  "s_add_u32 s78, s80, 16384\n" \
  "v_add_f32 v152, v152, v156\n" \
  "v_add_f32 v153, v153, v157\n" \
  "v_mfma_f32_16x16x32_bf16 v[92:95], a[148:151], v[52:55], v[92:95]\n" \
  "v_add_f32 v154, v154, v158\n" \
  "v_add_f32 v155, v155, v159\n" \
  "v_mfma_f32_16x16x32_bf16 v[96:99], a[152:155], v[52:55], v[96:99]\n" \
  "s_cmp_lt_u32 s94, s55\n" \
  "s_cselect_b32 s94, s94, -1\n" \
  "s_waitcnt vmcnt(16)\n" \
  "v_cmp_ne_u32 vcc, %14, v189\n" \
  "v_mfma_f32_16x16x32_bf16 v[100:103], a[156:159], v[52:55], v[100:103]\n" \
  "v_add_f32 v144, v144, v152\n" \
  "v_add_f32 v145, v145, v153\n" \
  "v_mfma_f32_16x16x32_bf16 v[104:107], a[160:163], v[52:55], v[104:107]\n" \
  "v_add_f32 v146, v146, v154\n" \
  "v_add_f32 v147, v147, v155\n" \
  "v_mfma_f32_16x16x32_bf16 v[108:111], a[164:167], v[52:55], v[108:111]\n" \
  "buffer_load_dwordx4 v[52:55], v190, s[40:43], s81 offen offset:1024 sc1\n" \
  "v_mfma_f32_16x16x32_bf16 v[88:91], a[168:171], v[60:63], v[88:91]\n" \
  "v_cvt_pk_bf16_f32 v116, v144, v145\n" \
  "v_cvt_pk_bf16_f32 v117, v146, v147\n" \
  "v_mfma_f32_16x16x32_bf16 v[92:95], a[172:175], v[60:63], v[92:95]\n" \
  "buffer_store_dwordx2 v[116:117], v198, s[40:43], s53 offen offset:1024 sc1\n" \
  "v_mfma_f32_16x16x32_bf16 v[96:99], a[176:179], v[60:63], v[96:99]\n" \
  "s_lshr_b32 s53, s50, 8\n" \
  "s_and_b32 s53, s53, 8192\n" \
  "v_cndmask_b32 v188, v188, 0, vcc\n" \
  "v_mfma_f32_16x16x32_bf16 v[100:103], a[180:183], v[60:63], v[100:103]\n" \
  "s_xor_b32 s53, s53, s50\n" \
  "s_add_u32 s50, s50, 393216\n" \
  "v_mfma_f32_16x16x32_bf16 v[104:107], a[184:187], v[60:63], v[104:107]\n" \
  "s_cmp_eq_u32 s54, 0\n" \
  "s_cselect_b32 s60, s48, 99\n" \
  "s_cmp_lt_u32 s60, s55\n" \
  "v_xor_b32 v192, 65536, v192\n" \
  "v_cmp_ge_i32 vcc, s94, v188\n" \
  "v_mfma_f32_16x16x32_bf16 v[108:111], a[188:191], v[60:63], v[108:111]\n" \
  "buffer_load_dwordx4 v[60:63], v190, s[40:43], s81 offen offset:3072 sc1\n" \
  "s_cbranch_scc1 .LO_loop_%=\n" \
  "s_cmp_lg_u32 s54, 0\n" \
  "s_cbranch_scc1 .LO_ft_%=\n" \
  ".LO_fl_%=:\n" \
  "s_nop 7\n" \
  "ds_write_b128 v191, v[88:91] offset:6144\n" \
  "ds_write_b128 v191, v[92:95] offset:7168\n" \
  "ds_write_b128 v191, v[96:99] offset:8192\n" \
  "ds_write_b128 v191, v[100:103] offset:9216\n" \
  "ds_write_b128 v191, v[104:107] offset:10240\n" \
  "ds_write_b128 v191, v[108:111] offset:11264\n" \
  "s_waitcnt lgkmcnt(0)\n" \
  "s_barrier\n" \
  "ds_read_b128 v[120:123], v192\n" \
  "ds_read_b128 v[112:115], v192 offset:12288\n" \
  "ds_read_b128 v[116:119], v192 offset:24576\n" \
  "ds_read_b128 v[124:127], v192 offset:36864\n" \
  "ds_read_b128 v[128:131], v192 offset:1024\n" \
  "ds_read_b128 v[132:135], v192 offset:13312\n" \
  "ds_read_b128 v[136:139], v192 offset:25600\n" \
  "ds_read_b128 v[140:143], v192 offset:37888\n" \
  "ds_read_b128 v[144:147], v192 offset:2048\n" \
  "ds_read_b128 v[148:151], v192 offset:14336\n" \
  "ds_read_b128 v[152:155], v192 offset:26624\n" \
  "ds_read_b128 v[156:159], v192 offset:38912\n" \
  "s_waitcnt lgkmcnt(10)\n" \
  "v_add_f32 v120, v120, v112\n" \
  "v_add_f32 v121, v121, v113\n" \
  "v_add_f32 v122, v122, v114\n" \
  "v_add_f32 v123, v123, v115\n" \
  "s_waitcnt lgkmcnt(8)\n" \
  "v_add_f32 v116, v116, v124\n" \
  "v_add_f32 v117, v117, v125\n" \
  "v_add_f32 v118, v118, v126\n" \
  "v_add_f32 v119, v119, v127\n" \
  "v_add_f32 v120, v120, v116\n" \
  "v_add_f32 v121, v121, v117\n" \
  "v_add_f32 v122, v122, v118\n" \
  "v_add_f32 v123, v123, v119\n" \
  "v_cvt_pk_bf16_f32 v112, v120, v121\n" \
  "v_cvt_pk_bf16_f32 v113, v122, v123\n" \
  "s_waitcnt lgkmcnt(6)\n" \
  "v_add_f32 v128, v128, v132\n" \
  "v_add_f32 v129, v129, v133\n" \
  "v_add_f32 v130, v130, v134\n" \
  "v_add_f32 v131, v131, v135\n" \
  "s_waitcnt lgkmcnt(4)\n" \
  "v_add_f32 v136, v136, v140\n" \
  "v_add_f32 v137, v137, v141\n" \
  "v_add_f32 v138, v138, v142\n" \
  "v_add_f32 v139, v139, v143\n" \
  "v_add_f32 v128, v128, v136\n" \
  "v_add_f32 v129, v129, v137\n" \
  "v_add_f32 v130, v130, v138\n" \
  "v_add_f32 v131, v131, v139\n" \
  "v_cvt_pk_bf16_f32 v114, v128, v129\n" \
  "v_cvt_pk_bf16_f32 v115, v130, v131\n" \
  "buffer_store_dwordx4 v[112:115], v190, s[40:43], s53 offen sc1\n" \
  "s_waitcnt lgkmcnt(2)\n" \
  "v_add_f32 v144, v144, v148\n" \
  "v_add_f32 v145, v145, v149\n" \
  "v_add_f32 v146, v146, v150\n" \
  "v_add_f32 v147, v147, v151\n" \
  "s_waitcnt lgkmcnt(0)\n" \
  "v_add_f32 v152, v152, v156\n" \
  "v_add_f32 v153, v153, v157\n" \
  "v_add_f32 v154, v154, v158\n" \
  "v_add_f32 v155, v155, v159\n" \
  "v_add_f32 v144, v144, v152\n" \
  "v_add_f32 v145, v145, v153\n" \
  "v_add_f32 v146, v146, v154\n" \
  "v_add_f32 v147, v147, v155\n" \
  "v_cvt_pk_bf16_f32 v116, v144, v145\n" \
  "v_cvt_pk_bf16_f32 v117, v146, v147\n" \
  "buffer_store_dwordx2 v[116:117], v198, s[40:43], s53 offen offset:1024 sc1\n" \
  "s_waitcnt vmcnt(0)\n" \
  "s_barrier\n" \
  "s_add_i32 s60, s48, 0\n" \
  "v_mov_b32 v186, s60\n" \
  "buffer_store_dwordx2 v[186:187], v197, s[40:43], %18 offen sc1\n" \
  "s_cmp_lg_u32 s56, 0\n" \
  "s_cbranch_scc1 .LO_fl_b_%=\n" \
  "s_branch .LO_end_%=\n" \
  ".LO_pl_%=:\n" \
  "s_add_u32 s60, s48, 2\n" \
  "s_cmp_lg_u32 s60, s55\n" \
  "s_cbranch_scc1 .LO_pw_%=\n" \
  "s_mov_b32 s54, s76\n" \
  "s_mov_b32 s76, 2130706432\n" \
  "s_mov_b32 s77, 2130706432\n" \
  "s_mov_b32 s78, 2130706432\n" \
  "s_mov_b32 s79, 2130706432\n" \
  "s_branch .LO_pl_b_%=\n" \
  ".LO_ft_%=:\n" \
  "s_mov_b32 s94, s48\n" \
  ".LO_pw_%=:\n" \
  "s_mov_b32 s69, 0\n" \
  ".LO_p1_%=:\n" \
  "buffer_load_dwordx2 v[188:189], v193, s[40:43], 0 offen sc1\n" \
  "s_waitcnt vmcnt(0)\n" \
  "v_cmp_ne_u32 vcc, %14, v189\n" \
  "s_nop 1\n" \
  "v_cndmask_b32 v188, v188, 0, vcc\n" \
  "v_cmp_ge_i32 vcc, s94, v188\n" \
  "s_cbranch_vccz .LO_p1_x_%=\n" \
  "s_sleep 2\n" \
  "s_add_u32 s69, s69, 1\n" \
  "s_and_b32 s63, s69, 1023\n" \
  "s_cbranch_scc1 .LO_p1_%=\n" \
  "buffer_load_dwordx2 v[188:189], off, s[40:43], %19 offset:128 sc1\n" \
  "s_waitcnt vmcnt(0)\n" \
  "v_readfirstlane_b32 s63, v189\n" \
  "s_cmp_eq_u32 s63, %14\n" \
  "s_cbranch_scc1 .LO_p1_x_%=\n" \
  "s_cmp_le_u32 s69, 2097152\n" \
  "s_cbranch_scc1 .LO_p1_%=\n" \
  "v_mov_b32 v188, 1\n" \
  "v_mov_b32 v189, %14\n" \
  "buffer_store_dwordx2 v[188:189], off, s[40:43], %19 offset:128 sc1\n" \
  "s_waitcnt vmcnt(0)\n" \
  ".LO_p1_x_%=:\n" \
  "s_cmp_eq_u32 s54, 0\n" \
  "s_cbranch_scc1 .LO_pl_b_%=\n" \
  "s_mov_b32 s76, s54\n" \
  "s_mov_b32 s54, 0\n" \
  "s_add_u32 s77, s76, 4096\n" \
  "s_add_u32 s78, s76, 16384\n" \
  "s_add_u32 s79, s76, 20480\n" \
  "buffer_load_dwordx4 v[0:3], v190, s[40:43], s76 offen sc1\n" \
  "buffer_load_dwordx4 v[8:11], v190, s[40:43], s76 offen offset:2048 sc1\n" \
  "buffer_load_dwordx4 v[16:19], v190, s[40:43], s77 offen sc1\n" \
  "buffer_load_dwordx4 v[24:27], v190, s[40:43], s77 offen offset:2048 sc1\n" \
  "buffer_load_dwordx4 v[32:35], v190, s[40:43], s78 offen sc1\n" \
  "buffer_load_dwordx4 v[40:43], v190, s[40:43], s78 offen offset:2048 sc1\n" \
  "buffer_load_dwordx4 v[48:51], v190, s[40:43], s79 offen sc1\n" \
  "buffer_load_dwordx4 v[56:59], v190, s[40:43], s79 offen offset:2048 sc1\n" \
  "buffer_load_dwordx4 v[4:7], v190, s[40:43], s76 offen offset:1024 sc1\n" \
  "buffer_load_dwordx4 v[12:15], v190, s[40:43], s76 offen offset:3072 sc1\n" \
  "buffer_load_dwordx4 v[20:23], v190, s[40:43], s77 offen offset:1024 sc1\n" \
  "buffer_load_dwordx4 v[28:31], v190, s[40:43], s77 offen offset:3072 sc1\n" \
  "buffer_load_dwordx4 v[36:39], v190, s[40:43], s78 offen offset:1024 sc1\n" \
  "buffer_load_dwordx4 v[44:47], v190, s[40:43], s78 offen offset:3072 sc1\n" \
  "buffer_load_dwordx4 v[52:55], v190, s[40:43], s79 offen offset:1024 sc1\n" \
  "buffer_load_dwordx4 v[60:63], v190, s[40:43], s79 offen offset:3072 sc1\n" \
  "s_waitcnt vmcnt(8)\n" \
  "s_branch .LO_pl_b_%=\n" \
  ".LO_end_%=:\n"
#define O_ASM_VOPS "+{v190}"(oi.l16), "+{v191}"(oi.rw), "+{v192}"(oi.rr), "+{v193}"(oi.fo), "+{v194}"(oi.wv0), "+{v195}"(oi.wv1), "+{v196}"(oi.wv2), "+{v197}"(oi.tro), "+{v198}"(oi.l8), "+{v199}"(oi.rw0), "+{v200}"(oi.rr0)
#define O_ASM_SOPS "s"(oi.ws), "s"(oi.Wo), "s"(oi.w), "s"(oi.tag), "s"(oi.o_w), "s"(oi.o_o), "s"(oi.o_a), "s"(oi.f_a), "s"(oi.o_ctl), "s"(oi.ns)
#define O_ASM_CLOB "v0", "v1", "v2", "v3", "v4", "v5", "v6", "v7", "v8", "v9", "v10", "v11", "v12", "v13", "v14", "v15", "v16", "v17", "v18", "v19", "v20", "v21", "v22", "v23", "v24", "v25", "v26", "v27", "v28", "v29", "v30", "v31", "v32", "v33", "v34", "v35", "v36", "v37", "v38", "v39", "v40", "v41", "v42", "v43", "v44", "v45", "v46", "v47", "v48", "v49", "v50", "v51", "v52", "v53", "v54", "v55", "v56", "v57", "v58", "v59", "v60", "v61", "v62", "v63", "v64", "v65", "v66", "v67", "v68", "v69", "v70", "v71", "v72", "v73", "v74", "v75", "v76", "v77", "v78", "v79", "v80", "v81", "v82", "v83", "v84", "v85", "v86", "v87", "v88", "v89", "v90", "v91", "v92", "v93", "v94", "v95", "v96", "v97", "v98", "v99", "v100", "v101", "v102", "v103", "v104", "v105", "v106", "v107", "v108", "v109", "v110", "v111", "v112", "v113", "v114", "v115", "v116", "v117", "v118", "v119", "v120", "v121", "v122", "v123", "v124", "v125", "v126", "v127", "v128", "v129", "v130", "v131", "v132", "v133", "v134", "v135", "v136", "v137", "v138", "v139", "v140", "v141", "v142", "v143", "v144", "v145", "v146", "v147", "v148", "v149", "v150", "v151", "v152", "v153", "v154", "v155", "v156", "v157", "v158", "v159", "v160", "v161", "v162", "v163", "v164", "v165", "v166", "v167", "v168", "v169", "v170", "v171", "v172", "v173", "v174", "v175", "v176", "v177", "v178", "v179", "v180", "v181", "v182", "v183", "v184", "v185", "v186", "v187", "v188", "v189", "a0", "a1", "a2", "a3", "a4", "a5", "a6", "a7", "a8", "a9", "a10", "a11", "a12", "a13", "a14", "a15", "a16", "a17", "a18", "a19", "a20", "a21", "a22", "a23", "a24", "a25", "a26", "a27", "a28", "a29", "a30", "a31", "a32", "a33", "a34", "a35", "a36", "a37", "a38", "a39", "a40", "a41", "a42", "a43", "a44", "a45", "a46", "a47", "a48", "a49", "a50", "a51", "a52", "a53", "a54", "a55", "a56", "a57", "a58", "a59", "a60", "a61", "a62", "a63", "a64", "a65", "a66", "a67", "a68", "a69", "a70", "a71", "a72", "a73", "a74", "a75", "a76", "a77", "a78", "a79", "a80", "a81", "a82", "a83", "a84", "a85", "a86", "a87", "a88", "a89", "a90", "a91", "a92", "a93", "a94", "a95", "a96", "a97", "a98", "a99", "a100", "a101", "a102", "a103", "a104", "a105", "a106", "a107", "a108", "a109", "a110", "a111", "a112", "a113", "a114", "a115", "a116", "a117", "a118", "a119", "a120", "a121", "a122", "a123", "a124", "a125", "a126", "a127", "a128", "a129", "a130", "a131", "a132", "a133", "a134", "a135", "a136", "a137", "a138", "a139", "a140", "a141", "a142", "a143", "a144", "a145", "a146", "a147", "a148", "a149", "a150", "a151", "a152", "a153", "a154", "a155", "a156", "a157", "a158", "a159", "a160", "a161", "a162", "a163", "a164", "a165", "a166", "a167", "a168", "a169", "a170", "a171", "a172", "a173", "a174", "a175", "a176", "a177", "a178", "a179", "a180", "a181", "a182", "a183", "a184", "a185", "a186", "a187", "a188", "a189", "a190", "a191", "s40", "s41", "s42", "s43", "s44", "s45", "s46", "s47", "s48", "s49", "s50", "s51", "s52", "s53", "s54", "s55", "s56", "s57", "s58", "s59", "s60", "s61", "s62", "s63", "s64", "s65", "s66", "s67", "s68", "s69", "s70", "s71", "s72", "s73", "s74", "s75", "s76", "s77", "s78", "s79", "s80", "s81", "s82", "s83", "s84", "s85", "s86", "s87", "s88", "s89", "s90", "s91", "s92", "s93", "s94", "s95", "s96", "s97", "s98", "s99", "vcc", "scc", "memory"
#define F_ASM_TEXT \
  "s_mov_b64 s[40:41], %30\n" \
  "s_and_b32 s41, s41, 0xffff\n" \
  "s_mov_b32 s42, 255852544\n" \
  "s_mov_b32 s43, 0x00020000\n" \
  "s_mov_b64 s[44:45], %31\n" \
  "s_and_b32 s45, s45, 0xffff\n" \
  "s_mov_b32 s46, 2359296\n" \
  "s_mov_b32 s47, 0x00020000\n" \
  "s_mov_b32 s48, 0\n" \
  "s_mov_b32 s58, 0\n" \
  "s_mov_b32 s59, 0\n" \
  "s_mov_b32 s49, %36\n" \
  "s_add_u32 s53, s48, 59\n" \
  "s_min_u32 s53, s53, 63\n" \
  "s_and_b32 s63, s53, 3\n" \
  "s_lshl_b32 s50, s53, 5\n" \
  "s_lshl_b32 s63, s63, 4\n" \
  "s_sub_u32 s50, s50, s63\n" \
  "s_add_u32 s50, s50, %37\n" \
  "s_mov_b32 s51, %35\n" \
  "s_mov_b32 s65, 0\n" \
  "s_mov_b32 s64, %41\n" \
  "v_not_b32 v197, %32\n" \
  ".LF_pre_%=:\n" \
  "v_readfirstlane_b32 s60, v197\n" \
  "s_cmp_eq_u32 s60, %32\n" \
  "s_cbranch_scc1 .LF_fu_x_%=\n" \
  ".LF_fu_%=:\n" \
  "buffer_load_dwordx2 v[196:197], off, s[40:43], s64 sc1\n" \
  "buffer_load_dwordx4 v[60:63], off, s[40:43], s50 sc1\n" \
  "s_memrealtime s[76:77]\n" \
  "s_waitcnt vmcnt(0) lgkmcnt(0)\n" \
  "v_readfirstlane_b32 s60, v197\n" \
  "s_cmp_eq_u32 s60, %32\n" \
  "s_cbranch_scc1 .LF_fu_x_%=\n" \
  "v_readfirstlane_b32 s62, v60\n" \
  "v_readfirstlane_b32 s61, v62\n" \
  "v_readfirstlane_b32 s60, v63\n" \
  "s_cmp_eq_u32 s62, s53\n" \
  "s_cselect_b32 s60, s60, 0\n" \
  "s_max_u32 s65, s65, s60\n" \
  "s_cmp_eq_u32 s48, 5\n" \
  "s_cselect_b32 s54, 16, 0\n" \
  "s_lshr_b32 s54, %39, s54\n" \
  "s_and_b32 s54, s54, 0x7fff\n" \
  "s_add_u32 s55, s61, s54\n" \
  "s_add_u32 s55, s55, s65\n" \
  "s_cmp_eq_u32 s48, 4\n" \
  "s_cselect_b32 s54, 20, 0\n" \
  "s_sub_u32 s55, s55, s54\n" \
  "s_sub_u32 s60, s55, s76\n" \
  "s_cmp_eq_u32 s62, s53\n" \
  "s_cselect_b32 s60, s60, 999\n" \
  "s_cmp_lt_u32 s48, s58\n" \
  "s_cselect_b32 s60, s60, 999\n" \
  "s_cmp_lt_i32 s60, 60\n" \
  "s_cbranch_scc1 .LF_loop_%=\n" \
  "s_sleep 6\n" \
  "s_add_u32 s59, s59, 1\n" \
  "s_cmp_lt_u32 s59, 131072\n" \
  "s_cbranch_scc1 .LF_fu_%=\n" \
  "v_mov_b32 v32, 1\n" \
  "v_mov_b32 v33, %32\n" \
  "buffer_store_dwordx2 v[32:33], off, s[40:43], %38 offset:128 sc1\n" \
  "s_waitcnt vmcnt(0)\n" \
  ".LF_fu_x_%=:\n" \
  "s_bitcmp1_b32 %39, 15\n" \
  "s_cbranch_scc1 .LF_fl_%=\n" \
  "s_add_u32 s61, s58, 58\n" \
  "s_mul_i32 s61, s61, 8454144\n" \
  "s_and_b32 s61, s61, 983040\n" \
  "s_xor_b32 s61, s61, %33\n" \
  "s_lshl_b32 s62, s58, 17\n" \
  "s_add_u32 s62, s62, %34\n" \
  "s_xor_b32 s60, s61, 0\n" \
  "buffer_load_dwordx4 v[32:35], v202, s[40:43], s60 offen sc1\n" \
  "s_sleep 1\n" \
  "buffer_load_dwordx4 v[64:67], v203, s[40:43], s62 offen sc1\n" \
  "s_sleep 1\n" \
  "buffer_load_dwordx4 v[68:71], v203, s[40:43], s62 offen offset:1024 sc1\n" \
  "s_sleep 1\n" \
  "s_xor_b32 s60, s61, 1024\n" \
  "buffer_load_dwordx4 v[36:39], v202, s[40:43], s60 offen sc1\n" \
  "s_sleep 1\n" \
  "buffer_load_dwordx4 v[72:75], v203, s[40:43], s62 offen offset:2048 sc1\n" \
  "s_sleep 1\n" \
  "buffer_load_dwordx4 v[76:79], v203, s[40:43], s62 offen offset:3072 sc1\n" \
  "s_sleep 1\n" \
  "s_xor_b32 s60, s61, 2048\n" \
  "buffer_load_dwordx4 v[40:43], v202, s[40:43], s60 offen sc1\n" \
  "s_sleep 1\n" \
  "buffer_load_dwordx4 v[80:83], v204, s[40:43], s62 offen sc1\n" \
  "s_sleep 1\n" \
  "buffer_load_dwordx4 v[84:87], v204, s[40:43], s62 offen offset:1024 sc1\n" \
  "s_sleep 1\n" \
  "s_xor_b32 s60, s61, 3072\n" \
  "buffer_load_dwordx4 v[44:47], v202, s[40:43], s60 offen sc1\n" \
  "s_sleep 1\n" \
  "buffer_load_dwordx4 v[88:91], v204, s[40:43], s62 offen offset:2048 sc1\n" \
  "s_sleep 1\n" \
  "buffer_load_dwordx4 v[92:95], v204, s[40:43], s62 offen offset:3072 sc1\n" \
  "s_sleep 1\n" \
  "s_xor_b32 s60, s61, 4096\n" \
  "buffer_load_dwordx4 v[48:51], v202, s[40:43], s60 offen sc1\n" \
  "s_sleep 1\n" \
  "buffer_load_dwordx4 v[96:99], v205, s[40:43], s62 offen sc1\n" \
  "s_sleep 1\n" \
  "buffer_load_dwordx4 v[100:103], v205, s[40:43], s62 offen offset:1024 sc1\n" \
  "s_sleep 1\n" \
  "s_xor_b32 s60, s61, 5120\n" \
  "buffer_load_dwordx4 v[52:55], v202, s[40:43], s60 offen sc1\n" \
  "s_sleep 1\n" \
  "buffer_load_dwordx4 v[104:107], v205, s[40:43], s62 offen offset:2048 sc1\n" \
  "s_sleep 1\n" \
  "buffer_load_dwordx4 v[108:111], v205, s[40:43], s62 offen offset:3072 sc1\n" \
  "s_sleep 1\n" \
  "s_xor_b32 s60, s61, 6144\n" \
  "buffer_load_dwordx4 v[56:59], v202, s[40:43], s60 offen sc1\n" \
  "s_sleep 1\n" \
  "buffer_load_dwordx4 v[112:115], v206, s[40:43], s62 offen sc1\n" \
  "s_sleep 1\n" \
  "buffer_load_dwordx4 v[116:119], v206, s[40:43], s62 offen offset:1024 sc1\n" \
  "s_sleep 1\n" \
  "s_xor_b32 s60, s61, 7168\n" \
  "buffer_load_dwordx4 v[60:63], v202, s[40:43], s60 offen sc1\n" \
  "s_sleep 1\n" \
  "buffer_load_dwordx4 v[120:123], v206, s[40:43], s62 offen offset:2048 sc1\n" \
  "s_sleep 1\n" \
  "buffer_load_dwordx4 v[124:127], v206, s[40:43], s62 offen offset:3072 sc1\n" \
  "s_sleep 1\n" \
  "s_add_u32 s64, s64, 128\n" \
  ".LF_fl_b_%=:\n" \
  "v_mov_b64 v[128:129], 0\n" \
  "v_mov_b64 v[130:131], 0\n" \
  "v_mov_b64 v[132:133], 0\n" \
  "v_mov_b64 v[134:135], 0\n" \
  "v_mov_b64 v[136:137], 0\n" \
  "v_mov_b64 v[138:139], 0\n" \
  "v_mov_b64 v[140:141], 0\n" \
  "v_mov_b64 v[142:143], 0\n" \
  "v_mov_b64 v[144:145], 0\n" \
  "v_mov_b64 v[146:147], 0\n" \
  "v_mov_b64 v[148:149], 0\n" \
  "v_mov_b64 v[150:151], 0\n" \
  "v_mov_b64 v[152:153], 0\n" \
  "v_mov_b64 v[154:155], 0\n" \
  "v_mov_b64 v[156:157], 0\n" \
  "v_mov_b64 v[158:159], 0\n" \
  "v_mov_b64 v[160:161], 0\n" \
  "v_mov_b64 v[162:163], 0\n" \
  "v_mov_b64 v[164:165], 0\n" \
  "v_mov_b64 v[166:167], 0\n" \
  "v_mov_b64 v[168:169], 0\n" \
  "v_mov_b64 v[170:171], 0\n" \
  "v_mov_b64 v[172:173], 0\n" \
  "v_mov_b64 v[174:175], 0\n" \
  "v_mov_b64 v[176:177], 0\n" \
  "v_mov_b64 v[178:179], 0\n" \
  "v_mov_b64 v[180:181], 0\n" \
  "v_mov_b64 v[182:183], 0\n" \
  "v_mov_b64 v[184:185], 0\n" \
  "v_mov_b64 v[186:187], 0\n" \
  "v_mov_b64 v[188:189], 0\n" \
  "v_mov_b64 v[190:191], 0\n" \
  "s_waitcnt vmcnt(0)\n" \
  "v_mov_b32 v0, v32\n" \
  "v_mov_b32 v1, v36\n" \
  "v_mov_b32 v2, v33\n" \
  "v_mov_b32 v3, v37\n" \
  "v_mov_b32 v4, v34\n" \
  "v_mov_b32 v5, v38\n" \
  "v_mov_b32 v6, v35\n" \
  "v_mov_b32 v7, v39\n" \
  "v_mov_b32 v8, v40\n" \
  "v_mov_b32 v9, v44\n" \
  "v_mov_b32 v10, v41\n" \
  "v_mov_b32 v11, v45\n" \
  "v_mov_b32 v12, v42\n" \
  "v_mov_b32 v13, v46\n" \
  "v_mov_b32 v14, v43\n" \
  "v_mov_b32 v15, v47\n" \
  "v_mov_b32 v16, v48\n" \
  "v_mov_b32 v17, v52\n" \
  "v_mov_b32 v18, v49\n" \
  "v_mov_b32 v19, v53\n" \
  "v_mov_b32 v20, v50\n" \
  "v_mov_b32 v21, v54\n" \
  "v_mov_b32 v22, v51\n" \
  "v_mov_b32 v23, v55\n" \
  "v_mov_b32 v24, v56\n" \
  "v_mov_b32 v25, v60\n" \
  "v_mov_b32 v26, v57\n" \
  "v_mov_b32 v27, v61\n" \
  "v_mov_b32 v28, v58\n" \
  "v_mov_b32 v29, v62\n" \
  "v_mov_b32 v30, v59\n" \
  "v_mov_b32 v31, v63\n" \
  "s_nop 1\n" \
  "v_smfmac_f32_16x16x64_bf16 v[128:131], v[64:67], v[0:7], v200\n" \
  "v_smfmac_f32_16x16x64_bf16 v[132:135], v[68:71], v[8:15], v200\n" \
  "v_smfmac_f32_16x16x64_bf16 v[136:139], v[72:75], v[16:23], v200\n" \
  "v_smfmac_f32_16x16x64_bf16 v[140:143], v[76:79], v[24:31], v200\n" \
  "v_smfmac_f32_16x16x64_bf16 v[144:147], v[80:83], v[0:7], v200\n" \
  "v_smfmac_f32_16x16x64_bf16 v[148:151], v[84:87], v[8:15], v200\n" \
  "v_smfmac_f32_16x16x64_bf16 v[152:155], v[88:91], v[16:23], v200\n" \
  "v_smfmac_f32_16x16x64_bf16 v[156:159], v[92:95], v[24:31], v200\n" \
  "v_smfmac_f32_16x16x64_bf16 v[160:163], v[96:99], v[0:7], v200\n" \
  "v_smfmac_f32_16x16x64_bf16 v[164:167], v[100:103], v[8:15], v200\n" \
  "v_smfmac_f32_16x16x64_bf16 v[168:171], v[104:107], v[16:23], v200\n" \
  "v_smfmac_f32_16x16x64_bf16 v[172:175], v[108:111], v[24:31], v200\n" \
  "v_smfmac_f32_16x16x64_bf16 v[176:179], v[112:115], v[0:7], v200\n" \
  "v_smfmac_f32_16x16x64_bf16 v[180:183], v[116:119], v[8:15], v200\n" \
  "v_smfmac_f32_16x16x64_bf16 v[184:187], v[120:123], v[16:23], v200\n" \
  "v_smfmac_f32_16x16x64_bf16 v[188:191], v[124:127], v[24:31], v200\n" \
  "s_add_u32 s58, s58, 1\n" \
  "buffer_load_dwordx2 v[196:197], off, s[40:43], s64 sc1\n" \
  "s_cmp_eq_u32 s58, 6\n" \
  "s_cbranch_scc1 .LF_ne_%=\n" \
  ".LF_ne_%=:\n" \
  "s_nop 7\n" \
  "v_cvt_pk_bf16_f32 v128, v128, v129\n" \
  "v_cvt_pk_bf16_f32 v129, v130, v131\n" \
  "v_cvt_pk_bf16_f32 v130, v132, v133\n" \
  "v_cvt_pk_bf16_f32 v131, v134, v135\n" \
  "v_cvt_pk_bf16_f32 v132, v136, v137\n" \
  "v_cvt_pk_bf16_f32 v133, v138, v139\n" \
  "v_cvt_pk_bf16_f32 v134, v140, v141\n" \
  "v_cvt_pk_bf16_f32 v135, v142, v143\n" \
  "v_cvt_pk_bf16_f32 v136, v144, v145\n" \
  "v_cvt_pk_bf16_f32 v137, v146, v147\n" \
  "v_cvt_pk_bf16_f32 v138, v148, v149\n" \
  "v_cvt_pk_bf16_f32 v139, v150, v151\n" \
  "v_cvt_pk_bf16_f32 v140, v152, v153\n" \
  "v_cvt_pk_bf16_f32 v141, v154, v155\n" \
  "v_cvt_pk_bf16_f32 v142, v156, v157\n" \
  "v_cvt_pk_bf16_f32 v143, v158, v159\n" \
  "v_cvt_pk_bf16_f32 v144, v160, v161\n" \
  "v_cvt_pk_bf16_f32 v145, v162, v163\n" \
  "v_cvt_pk_bf16_f32 v146, v164, v165\n" \
  "v_cvt_pk_bf16_f32 v147, v166, v167\n" \
  "v_cvt_pk_bf16_f32 v148, v168, v169\n" \
  "v_cvt_pk_bf16_f32 v149, v170, v171\n" \
  "v_cvt_pk_bf16_f32 v150, v172, v173\n" \
  "v_cvt_pk_bf16_f32 v151, v174, v175\n" \
  "v_cvt_pk_bf16_f32 v152, v176, v177\n" \
  "v_cvt_pk_bf16_f32 v153, v178, v179\n" \
  "v_cvt_pk_bf16_f32 v154, v180, v181\n" \
  "v_cvt_pk_bf16_f32 v155, v182, v183\n" \
  "v_cvt_pk_bf16_f32 v156, v184, v185\n" \
  "v_cvt_pk_bf16_f32 v157, v186, v187\n" \
  "v_cvt_pk_bf16_f32 v158, v188, v189\n" \
  "v_cvt_pk_bf16_f32 v159, v190, v191\n" \
  "s_cmp_eq_u32 s58, 1\n" \
  "s_cbranch_scc1 .LF_w1_%=\n" \
  "s_cmp_eq_u32 s58, 2\n" \
  "s_cbranch_scc1 .LF_w2_%=\n" \
  "s_cmp_eq_u32 s58, 3\n" \
  "s_cbranch_scc1 .LF_w3_%=\n" \
  "s_cmp_eq_u32 s58, 4\n" \
  "s_cbranch_scc1 .LF_w4_%=\n" \
  "s_cmp_eq_u32 s58, 5\n" \
  "s_cbranch_scc1 .LF_w5_%=\n" \
  "v_accvgpr_write_b32 a160, v128\n" \
  "v_accvgpr_write_b32 a161, v129\n" \
  "v_accvgpr_write_b32 a162, v130\n" \
  "v_accvgpr_write_b32 a163, v131\n" \
  "v_accvgpr_write_b32 a164, v132\n" \
  "v_accvgpr_write_b32 a165, v133\n" \
  "v_accvgpr_write_b32 a166, v134\n" \
  "v_accvgpr_write_b32 a167, v135\n" \
  "v_accvgpr_write_b32 a168, v136\n" \
  "v_accvgpr_write_b32 a169, v137\n" \
  "v_accvgpr_write_b32 a170, v138\n" \
  "v_accvgpr_write_b32 a171, v139\n" \
  "v_accvgpr_write_b32 a172, v140\n" \
  "v_accvgpr_write_b32 a173, v141\n" \
  "v_accvgpr_write_b32 a174, v142\n" \
  "v_accvgpr_write_b32 a175, v143\n" \
  "v_accvgpr_write_b32 a176, v144\n" \
  "v_accvgpr_write_b32 a177, v145\n" \
  "v_accvgpr_write_b32 a178, v146\n" \
  "v_accvgpr_write_b32 a179, v147\n" \
  "v_accvgpr_write_b32 a180, v148\n" \
  "v_accvgpr_write_b32 a181, v149\n" \
  "v_accvgpr_write_b32 a182, v150\n" \
  "v_accvgpr_write_b32 a183, v151\n" \
  "v_accvgpr_write_b32 a184, v152\n" \
  "v_accvgpr_write_b32 a185, v153\n" \
  "v_accvgpr_write_b32 a186, v154\n" \
  "v_accvgpr_write_b32 a187, v155\n" \
  "v_accvgpr_write_b32 a188, v156\n" \
  "v_accvgpr_write_b32 a189, v157\n" \
  "v_accvgpr_write_b32 a190, v158\n" \
  "v_accvgpr_write_b32 a191, v159\n" \
  ".LF_w_b_%=:\n" \
  "s_cmp_lt_u32 s58, 6\n" \
  "s_waitcnt vmcnt(0)\n" \
  "s_cbranch_scc1 .LF_pre_%=\n" \
  "s_waitcnt vmcnt(0)\n" \
  ".LF_loop_%=:\n" \
  "v_mov_b64 v[40:41], 0\n" \
  "v_mov_b64 v[42:43], 0\n" \
  "v_mov_b64 v[44:45], 0\n" \
  "v_mov_b64 v[46:47], 0\n" \
  "v_mov_b64 v[48:49], 0\n" \
  "v_mov_b64 v[50:51], 0\n" \
  "v_mov_b64 v[52:53], 0\n" \
  "v_mov_b64 v[54:55], 0\n" \
  "s_cmp_lg_u32 %40, 0\n" \
  "s_cbranch_scc1 .LF_go_%=\n" \
  "s_mov_b32 s69, 0\n" \
  "s_cmp_eq_u32 s48, 5\n" \
  "s_cbranch_scc1 .LF_tw1_%=\n" \
  ".LF_tw_%=:\n" \
  "buffer_load_dwordx4 v[60:63], off, s[40:43], s50 sc1\n" \
  ".LF_tw1_%=:\n" \
  "s_memrealtime s[76:77]\n" \
  "s_waitcnt vmcnt(0) lgkmcnt(0)\n" \
  "v_readfirstlane_b32 s62, v60\n" \
  "v_readfirstlane_b32 s61, v62\n" \
  "v_readfirstlane_b32 s60, v63\n" \
  "s_cmp_eq_u32 s62, s53\n" \
  "s_cselect_b32 s60, s60, 0\n" \
  "s_max_u32 s65, s65, s60\n" \
  "s_cmp_eq_u32 s48, 5\n" \
  "s_cselect_b32 s54, 16, 0\n" \
  "s_lshr_b32 s54, %39, s54\n" \
  "s_and_b32 s54, s54, 0x7fff\n" \
  "s_add_u32 s55, s61, s54\n" \
  "s_add_u32 s55, s55, s65\n" \
  "s_cmp_eq_u32 s48, 4\n" \
  "s_cselect_b32 s54, 20, 0\n" \
  "s_sub_u32 s55, s55, s54\n" \
  "s_sub_u32 s60, s55, s76\n" \
  "s_cmp_eq_u32 s62, s53\n" \
  "s_cselect_b32 s60, s60, 999\n" \
  "s_cmp_lt_i32 s60, 900\n" \
  "s_cbranch_scc1 .LF_clk_%=\n" \
  "s_sleep 8\n" \
  "s_add_u32 s69, s69, 1\n" \
  "s_cmp_lt_u32 s69, 20000\n" \
  "s_cbranch_scc1 .LF_tw_%=\n" \
  "s_branch .LF_go_%=\n" \
  ".LF_clk_%=:\n" \
  "s_cmp_le_i32 s60, 3\n" \
  "s_cbranch_scc1 .LF_go_%=\n" \
  "s_sleep 1\n" \
  "s_cmp_le_i32 s60, 30\n" \
  "s_cbranch_scc1 .LF_cl2_%=\n" \
  "s_sleep 6\n" \
  ".LF_cl2_%=:\n" \
  "s_memrealtime s[76:77]\n" \
  "s_waitcnt lgkmcnt(0)\n" \
  "s_sub_u32 s60, s55, s76\n" \
  "s_branch .LF_clk_%=\n" \
  ".LF_go_%=:\n" \
  "s_barrier\n" \
  "s_mov_b32 s69, 0\n" \
  "s_lshr_b32 s52, s49, 8\n" \
  "s_and_b32 s52, s52, 8192\n" \
  "s_xor_b32 s52, s52, s49\n" \
  ".LF_rd_%=:\n" \
  "buffer_load_dwordx4 v[0:3], v202, s[40:43], s52 offen sc1\n" \
  "buffer_load_dwordx4 v[4:7], v202, s[40:43], s52 offen offset:1024 sc1\n" \
  "s_mov_b32 s60, s51\n" \
  "buffer_load_dwordx4 v[8:11], v207, s[40:43], s60 offen sc1\n" \
  "s_add_u32 s60, s60, 4096\n" \
  "buffer_load_dwordx4 v[24:27], v207, s[40:43], s60 offen sc1\n" \
  "s_add_u32 s60, s60, 12288\n" \
  "buffer_load_dwordx4 v[12:15], v207, s[40:43], s60 offen sc1\n" \
  "s_add_u32 s60, s60, 4096\n" \
  "buffer_load_dwordx4 v[28:31], v207, s[40:43], s60 offen sc1\n" \
  "s_add_u32 s60, s60, 12288\n" \
  "buffer_load_dwordx4 v[16:19], v207, s[40:43], s60 offen sc1\n" \
  "s_add_u32 s60, s60, 4096\n" \
  "buffer_load_dwordx4 v[32:35], v207, s[40:43], s60 offen sc1\n" \
  "s_add_u32 s60, s60, 12288\n" \
  "buffer_load_dwordx4 v[20:23], v207, s[40:43], s60 offen sc1\n" \
  "s_add_u32 s60, s60, 4096\n" \
  "buffer_load_dwordx4 v[36:39], v207, s[40:43], s60 offen sc1\n" \
  "s_cmp_eq_u32 s48, 1\n" \
  "s_cbranch_scc1 .LF_r1_%=\n" \
  "s_cmp_eq_u32 s48, 2\n" \
  "s_cbranch_scc1 .LF_r2_%=\n" \
  "s_cmp_eq_u32 s48, 3\n" \
  "s_cbranch_scc1 .LF_r3_%=\n" \
  "s_cmp_eq_u32 s48, 4\n" \
  "s_cbranch_scc1 .LF_r4_%=\n" \
  "s_cmp_eq_u32 s48, 5\n" \
  "s_cbranch_scc1 .LF_r5_%=\n" \
  "v_mov_b32 v192, v212\n" \
  "v_mov_b32 v193, v213\n" \
  "v_mov_b32 v194, v214\n" \
  "v_accvgpr_read_b32 v128, a0\n" \
  "v_accvgpr_read_b32 v129, a1\n" \
  "v_accvgpr_read_b32 v130, a2\n" \
  "v_accvgpr_read_b32 v131, a3\n" \
  "v_accvgpr_read_b32 v132, a4\n" \
  "v_accvgpr_read_b32 v133, a5\n" \
  "v_accvgpr_read_b32 v134, a6\n" \
  "v_accvgpr_read_b32 v135, a7\n" \
  "v_accvgpr_read_b32 v136, a8\n" \
  "v_accvgpr_read_b32 v137, a9\n" \
  "v_accvgpr_read_b32 v138, a10\n" \
  "v_accvgpr_read_b32 v139, a11\n" \
  "v_accvgpr_read_b32 v140, a12\n" \
  "v_accvgpr_read_b32 v141, a13\n" \
  "v_accvgpr_read_b32 v142, a14\n" \
  "v_accvgpr_read_b32 v143, a15\n" \
  "v_accvgpr_read_b32 v144, a16\n" \
  "v_accvgpr_read_b32 v145, a17\n" \
  "v_accvgpr_read_b32 v146, a18\n" \
  "v_accvgpr_read_b32 v147, a19\n" \
  "v_accvgpr_read_b32 v148, a20\n" \
  "v_accvgpr_read_b32 v149, a21\n" \
  "v_accvgpr_read_b32 v150, a22\n" \
  "v_accvgpr_read_b32 v151, a23\n" \
  "v_accvgpr_read_b32 v152, a24\n" \
  "v_accvgpr_read_b32 v153, a25\n" \
  "v_accvgpr_read_b32 v154, a26\n" \
  "v_accvgpr_read_b32 v155, a27\n" \
  "v_accvgpr_read_b32 v156, a28\n" \
  "v_accvgpr_read_b32 v157, a29\n" \
  "v_accvgpr_read_b32 v158, a30\n" \
  "v_accvgpr_read_b32 v159, a31\n" \
  ".LF_r_b_%=:\n" \
  "s_waitcnt vmcnt(8)\n" \
  "v_permlane16_swap_b32 v0, v1\n" \
  "v_permlane16_swap_b32 v2, v3\n" \
  "v_permlane16_swap_b32 v4, v5\n" \
  "v_permlane16_swap_b32 v6, v7\n" \
  "s_mov_b64 s[78:79], 0\n" \
  "s_waitcnt vmcnt(7)\n" \
  "v_smfmac_f32_16x16x64_bf16 v[40:43], v[8:11], v[0:7], v200\n" \
  "s_waitcnt vmcnt(6)\n" \
  "v_smfmac_f32_16x16x64_bf16 v[40:43], v[24:27], v[128:135], v201\n" \
  "s_waitcnt vmcnt(5)\n" \
  "v_smfmac_f32_16x16x64_bf16 v[44:47], v[12:15], v[0:7], v200\n" \
  "s_waitcnt vmcnt(4)\n" \
  "v_smfmac_f32_16x16x64_bf16 v[44:47], v[28:31], v[136:143], v201\n" \
  "s_waitcnt vmcnt(3)\n" \
  "v_smfmac_f32_16x16x64_bf16 v[48:51], v[16:19], v[0:7], v200\n" \
  "s_waitcnt vmcnt(2)\n" \
  "v_smfmac_f32_16x16x64_bf16 v[48:51], v[32:35], v[144:151], v201\n" \
  "s_waitcnt vmcnt(1)\n" \
  "v_smfmac_f32_16x16x64_bf16 v[52:55], v[20:23], v[0:7], v200\n" \
  "s_waitcnt vmcnt(0)\n" \
  "v_smfmac_f32_16x16x64_bf16 v[52:55], v[36:39], v[152:159], v201\n" \
  "s_nop 1\n" \
  "v_permlane32_swap_b32 v40, v42\n" \
  "v_permlane32_swap_b32 v41, v43\n" \
  "v_add_f32 v40, v40, v42\n" \
  "v_add_f32 v41, v41, v43\n" \
  "v_cmp_u_f32 vcc, v40, v41\n" \
  "ds_write2st64_b32 v208, v40, v41 offset0:0 offset1:4\n" \
  "s_or_b64 s[78:79], s[78:79], vcc\n" \
  "v_permlane32_swap_b32 v44, v46\n" \
  "v_permlane32_swap_b32 v45, v47\n" \
  "v_add_f32 v44, v44, v46\n" \
  "v_add_f32 v45, v45, v47\n" \
  "v_cmp_u_f32 vcc, v44, v45\n" \
  "ds_write2st64_b32 v208, v44, v45 offset0:8 offset1:12\n" \
  "s_or_b64 s[78:79], s[78:79], vcc\n" \
  "v_permlane32_swap_b32 v48, v50\n" \
  "v_permlane32_swap_b32 v49, v51\n" \
  "v_add_f32 v48, v48, v50\n" \
  "v_add_f32 v49, v49, v51\n" \
  "v_cmp_u_f32 vcc, v48, v49\n" \
  "ds_write2st64_b32 v208, v48, v49 offset0:16 offset1:20\n" \
  "s_or_b64 s[78:79], s[78:79], vcc\n" \
  "v_permlane32_swap_b32 v52, v54\n" \
  "v_permlane32_swap_b32 v53, v55\n" \
  "v_add_f32 v52, v52, v54\n" \
  "v_add_f32 v53, v53, v55\n" \
  "v_cmp_u_f32 vcc, v52, v53\n" \
  "ds_write2st64_b32 v208, v52, v53 offset0:24 offset1:28\n" \
  "s_or_b64 s[78:79], s[78:79], vcc\n" \
  "s_cbranch_scc1 .LF_bad_%=\n" \
  ".LF_ok_%=:\n" \
  "s_waitcnt lgkmcnt(0)\n" \
  "s_barrier\n" \
  "ds_read_b128 v[40:43], v209\n" \
  "ds_read_b128 v[44:47], v209 offset:512\n" \
  "s_waitcnt lgkmcnt(1)\n" \
  "v_add_f32 v40, v40, v42\n" \
  "v_add_f32 v41, v41, v43\n" \
  "s_waitcnt lgkmcnt(0)\n" \
  "v_add_f32 v44, v44, v46\n" \
  "v_add_f32 v45, v45, v47\n" \
  "v_add_f32 v40, v40, v44\n" \
  "v_add_f32 v41, v41, v45\n" \
  "v_cvt_pk_bf16_f32 v40, v40, v41\n" \
  "v_and_b32 v41, 0xffff0000, v40\n" \
  "v_lshlrev_b32 v40, 16, v40\n" \
  "v_mul_f32 v40, v192, v40\n" \
  "v_mul_f32 v41, v192, v41\n" \
  "v_cvt_pk_bf16_f32 v40, v40, v41\n" \
  "v_and_b32 v41, 0xffff0000, v40\n" \
  "v_lshlrev_b32 v40, 16, v40\n" \
  "v_add_f32 v40, v40, v193\n" \
  "v_add_f32 v41, v41, v194\n" \
  "v_cvt_pk_bf16_f32 v237, v40, v41\n" \
  "s_cmp_eq_u32 s48, 5\n" \
  "s_cbranch_scc1 .LF_out_%=\n" \
  "v_mov_b32 v232, v233\n" \
  "v_mov_b32 v233, v234\n" \
  "v_mov_b32 v234, v235\n" \
  "v_mov_b32 v235, v236\n" \
  "v_mov_b32 v236, v237\n" \
  "v_xor_b32 v208, 8192, v208\n" \
  "v_xor_b32 v209, 8192, v209\n" \
  "s_add_u32 s49, s49, 1048576\n" \
  "s_add_u32 s51, s51, 65536\n" \
  "s_add_u32 s48, s48, 1\n" \
  "s_add_u32 s53, s48, 59\n" \
  "s_min_u32 s53, s53, 63\n" \
  "s_and_b32 s63, s53, 3\n" \
  "s_lshl_b32 s50, s53, 5\n" \
  "s_lshl_b32 s63, s63, 4\n" \
  "s_sub_u32 s50, s50, s63\n" \
  "s_add_u32 s50, s50, %37\n" \
  "s_cmp_lt_u32 s58, 6\n" \
  "s_cbranch_scc1 .LF_fu_%=\n" \
  "s_branch .LF_loop_%=\n" \
  ".LF_out_%=:\n" \
  "s_mov_b32 s60, 184320\n" \
  "buffer_store_dword v237, v210, s[44:47], s60 offen\n" \
  "s_mov_b32 s60, 147456\n" \
  "buffer_store_dword v236, v210, s[44:47], s60 offen\n" \
  "s_mov_b32 s60, 110592\n" \
  "buffer_store_dword v235, v210, s[44:47], s60 offen\n" \
  "s_mov_b32 s60, 73728\n" \
  "buffer_store_dword v234, v210, s[44:47], s60 offen\n" \
  "s_mov_b32 s60, 36864\n" \
  "buffer_store_dword v233, v210, s[44:47], s60 offen\n" \
  "s_mov_b32 s60, 0\n" \
  "buffer_store_dword v232, v210, s[44:47], s60 offen\n" \
  "s_branch .LF_end_%=\n" \
  ".LF_fl_%=:\n" \
  "s_add_u32 s61, s58, 58\n" \
  "s_mul_i32 s61, s61, 8454144\n" \
  "s_and_b32 s61, s61, 983040\n" \
  "s_xor_b32 s61, s61, %33\n" \
  "s_lshl_b32 s62, s58, 17\n" \
  "s_add_u32 s62, s62, %34\n" \
  "s_xor_b32 s60, s61, 0\n" \
  "buffer_load_dwordx4 v[32:35], v202, s[40:43], s60 offen sc1\n" \
  "buffer_load_dwordx4 v[64:67], v203, s[40:43], s62 offen sc1\n" \
  "buffer_load_dwordx4 v[68:71], v203, s[40:43], s62 offen offset:1024 sc1\n" \
  "s_xor_b32 s60, s61, 1024\n" \
  "buffer_load_dwordx4 v[36:39], v202, s[40:43], s60 offen sc1\n" \
  "buffer_load_dwordx4 v[72:75], v203, s[40:43], s62 offen offset:2048 sc1\n" \
  "buffer_load_dwordx4 v[76:79], v203, s[40:43], s62 offen offset:3072 sc1\n" \
  "s_xor_b32 s60, s61, 2048\n" \
  "buffer_load_dwordx4 v[40:43], v202, s[40:43], s60 offen sc1\n" \
  "buffer_load_dwordx4 v[80:83], v204, s[40:43], s62 offen sc1\n" \
  "buffer_load_dwordx4 v[84:87], v204, s[40:43], s62 offen offset:1024 sc1\n" \
  "s_xor_b32 s60, s61, 3072\n" \
  "buffer_load_dwordx4 v[44:47], v202, s[40:43], s60 offen sc1\n" \
  "buffer_load_dwordx4 v[88:91], v204, s[40:43], s62 offen offset:2048 sc1\n" \
  "buffer_load_dwordx4 v[92:95], v204, s[40:43], s62 offen offset:3072 sc1\n" \
  "s_xor_b32 s60, s61, 4096\n" \
  "buffer_load_dwordx4 v[48:51], v202, s[40:43], s60 offen sc1\n" \
  "buffer_load_dwordx4 v[96:99], v205, s[40:43], s62 offen sc1\n" \
  "buffer_load_dwordx4 v[100:103], v205, s[40:43], s62 offen offset:1024 sc1\n" \
  "s_xor_b32 s60, s61, 5120\n" \
  "buffer_load_dwordx4 v[52:55], v202, s[40:43], s60 offen sc1\n" \
  "buffer_load_dwordx4 v[104:107], v205, s[40:43], s62 offen offset:2048 sc1\n" \
  "buffer_load_dwordx4 v[108:111], v205, s[40:43], s62 offen offset:3072 sc1\n" \
  "s_xor_b32 s60, s61, 6144\n" \
  "buffer_load_dwordx4 v[56:59], v202, s[40:43], s60 offen sc1\n" \
  "buffer_load_dwordx4 v[112:115], v206, s[40:43], s62 offen sc1\n" \
  "buffer_load_dwordx4 v[116:119], v206, s[40:43], s62 offen offset:1024 sc1\n" \
  "s_xor_b32 s60, s61, 7168\n" \
  "buffer_load_dwordx4 v[60:63], v202, s[40:43], s60 offen sc1\n" \
  "buffer_load_dwordx4 v[120:123], v206, s[40:43], s62 offen offset:2048 sc1\n" \
  "buffer_load_dwordx4 v[124:127], v206, s[40:43], s62 offen offset:3072 sc1\n" \
  "s_add_u32 s64, s64, 128\n" \
  "s_branch .LF_fl_b_%=\n" \
  ".LF_w1_%=:\n" \
  "v_accvgpr_write_b32 a0, v128\n" \
  "v_accvgpr_write_b32 a1, v129\n" \
  "v_accvgpr_write_b32 a2, v130\n" \
  "v_accvgpr_write_b32 a3, v131\n" \
  "v_accvgpr_write_b32 a4, v132\n" \
  "v_accvgpr_write_b32 a5, v133\n" \
  "v_accvgpr_write_b32 a6, v134\n" \
  "v_accvgpr_write_b32 a7, v135\n" \
  "v_accvgpr_write_b32 a8, v136\n" \
  "v_accvgpr_write_b32 a9, v137\n" \
  "v_accvgpr_write_b32 a10, v138\n" \
  "v_accvgpr_write_b32 a11, v139\n" \
  "v_accvgpr_write_b32 a12, v140\n" \
  "v_accvgpr_write_b32 a13, v141\n" \
  "v_accvgpr_write_b32 a14, v142\n" \
  "v_accvgpr_write_b32 a15, v143\n" \
  "v_accvgpr_write_b32 a16, v144\n" \
  "v_accvgpr_write_b32 a17, v145\n" \
  "v_accvgpr_write_b32 a18, v146\n" \
  "v_accvgpr_write_b32 a19, v147\n" \
  "v_accvgpr_write_b32 a20, v148\n" \
  "v_accvgpr_write_b32 a21, v149\n" \
  "v_accvgpr_write_b32 a22, v150\n" \
  "v_accvgpr_write_b32 a23, v151\n" \
  "v_accvgpr_write_b32 a24, v152\n" \
  "v_accvgpr_write_b32 a25, v153\n" \
  "v_accvgpr_write_b32 a26, v154\n" \
  "v_accvgpr_write_b32 a27, v155\n" \
  "v_accvgpr_write_b32 a28, v156\n" \
  "v_accvgpr_write_b32 a29, v157\n" \
  "v_accvgpr_write_b32 a30, v158\n" \
  "v_accvgpr_write_b32 a31, v159\n" \
  "s_branch .LF_w_b_%=\n" \
  ".LF_w2_%=:\n" \
  "v_accvgpr_write_b32 a32, v128\n" \
  "v_accvgpr_write_b32 a33, v129\n" \
  "v_accvgpr_write_b32 a34, v130\n" \
  "v_accvgpr_write_b32 a35, v131\n" \
  "v_accvgpr_write_b32 a36, v132\n" \
  "v_accvgpr_write_b32 a37, v133\n" \
  "v_accvgpr_write_b32 a38, v134\n" \
  "v_accvgpr_write_b32 a39, v135\n" \
  "v_accvgpr_write_b32 a40, v136\n" \
  "v_accvgpr_write_b32 a41, v137\n" \
  "v_accvgpr_write_b32 a42, v138\n" \
  "v_accvgpr_write_b32 a43, v139\n" \
  "v_accvgpr_write_b32 a44, v140\n" \
  "v_accvgpr_write_b32 a45, v141\n" \
  "v_accvgpr_write_b32 a46, v142\n" \
  "v_accvgpr_write_b32 a47, v143\n" \
  "v_accvgpr_write_b32 a48, v144\n" \
  "v_accvgpr_write_b32 a49, v145\n" \
  "v_accvgpr_write_b32 a50, v146\n" \
  "v_accvgpr_write_b32 a51, v147\n" \
  "v_accvgpr_write_b32 a52, v148\n" \
  "v_accvgpr_write_b32 a53, v149\n" \
  "v_accvgpr_write_b32 a54, v150\n" \
  "v_accvgpr_write_b32 a55, v151\n" \
  "v_accvgpr_write_b32 a56, v152\n" \
  "v_accvgpr_write_b32 a57, v153\n" \
  "v_accvgpr_write_b32 a58, v154\n" \
  "v_accvgpr_write_b32 a59, v155\n" \
  "v_accvgpr_write_b32 a60, v156\n" \
  "v_accvgpr_write_b32 a61, v157\n" \
  "v_accvgpr_write_b32 a62, v158\n" \
  "v_accvgpr_write_b32 a63, v159\n" \
  "s_branch .LF_w_b_%=\n" \
  ".LF_w3_%=:\n" \
  "v_accvgpr_write_b32 a64, v128\n" \
  "v_accvgpr_write_b32 a65, v129\n" \
  "v_accvgpr_write_b32 a66, v130\n" \
  "v_accvgpr_write_b32 a67, v131\n" \
  "v_accvgpr_write_b32 a68, v132\n" \
  "v_accvgpr_write_b32 a69, v133\n" \
  "v_accvgpr_write_b32 a70, v134\n" \
  "v_accvgpr_write_b32 a71, v135\n" \
  "v_accvgpr_write_b32 a72, v136\n" \
  "v_accvgpr_write_b32 a73, v137\n" \
  "v_accvgpr_write_b32 a74, v138\n" \
  "v_accvgpr_write_b32 a75, v139\n" \
  "v_accvgpr_write_b32 a76, v140\n" \
  "v_accvgpr_write_b32 a77, v141\n" \
  "v_accvgpr_write_b32 a78, v142\n" \
  "v_accvgpr_write_b32 a79, v143\n" \
  "v_accvgpr_write_b32 a80, v144\n" \
  "v_accvgpr_write_b32 a81, v145\n" \
  "v_accvgpr_write_b32 a82, v146\n" \
  "v_accvgpr_write_b32 a83, v147\n" \
  "v_accvgpr_write_b32 a84, v148\n" \
  "v_accvgpr_write_b32 a85, v149\n" \
  "v_accvgpr_write_b32 a86, v150\n" \
  "v_accvgpr_write_b32 a87, v151\n" \
  "v_accvgpr_write_b32 a88, v152\n" \
  "v_accvgpr_write_b32 a89, v153\n" \
  "v_accvgpr_write_b32 a90, v154\n" \
  "v_accvgpr_write_b32 a91, v155\n" \
  "v_accvgpr_write_b32 a92, v156\n" \
  "v_accvgpr_write_b32 a93, v157\n" \
  "v_accvgpr_write_b32 a94, v158\n" \
  "v_accvgpr_write_b32 a95, v159\n" \
  "s_branch .LF_w_b_%=\n" \
  ".LF_w4_%=:\n" \
  "v_accvgpr_write_b32 a96, v128\n" \
  "v_accvgpr_write_b32 a97, v129\n" \
  "v_accvgpr_write_b32 a98, v130\n" \
  "v_accvgpr_write_b32 a99, v131\n" \
  "v_accvgpr_write_b32 a100, v132\n" \
  "v_accvgpr_write_b32 a101, v133\n" \
  "v_accvgpr_write_b32 a102, v134\n" \
  "v_accvgpr_write_b32 a103, v135\n" \
  "v_accvgpr_write_b32 a104, v136\n" \
  "v_accvgpr_write_b32 a105, v137\n" \
  "v_accvgpr_write_b32 a106, v138\n" \
  "v_accvgpr_write_b32 a107, v139\n" \
  "v_accvgpr_write_b32 a108, v140\n" \
  "v_accvgpr_write_b32 a109, v141\n" \
  "v_accvgpr_write_b32 a110, v142\n" \
  "v_accvgpr_write_b32 a111, v143\n" \
  "v_accvgpr_write_b32 a112, v144\n" \
  "v_accvgpr_write_b32 a113, v145\n" \
  "v_accvgpr_write_b32 a114, v146\n" \
  "v_accvgpr_write_b32 a115, v147\n" \
  "v_accvgpr_write_b32 a116, v148\n" \
  "v_accvgpr_write_b32 a117, v149\n" \
  "v_accvgpr_write_b32 a118, v150\n" \
  "v_accvgpr_write_b32 a119, v151\n" \
  "v_accvgpr_write_b32 a120, v152\n" \
  "v_accvgpr_write_b32 a121, v153\n" \
  "v_accvgpr_write_b32 a122, v154\n" \
  "v_accvgpr_write_b32 a123, v155\n" \
  "v_accvgpr_write_b32 a124, v156\n" \
  "v_accvgpr_write_b32 a125, v157\n" \
  "v_accvgpr_write_b32 a126, v158\n" \
  "v_accvgpr_write_b32 a127, v159\n" \
  "s_branch .LF_w_b_%=\n" \
  ".LF_w5_%=:\n" \
  "v_accvgpr_write_b32 a128, v128\n" \
  "v_accvgpr_write_b32 a129, v129\n" \
  "v_accvgpr_write_b32 a130, v130\n" \
  "v_accvgpr_write_b32 a131, v131\n" \
  "v_accvgpr_write_b32 a132, v132\n" \
  "v_accvgpr_write_b32 a133, v133\n" \
  "v_accvgpr_write_b32 a134, v134\n" \
  "v_accvgpr_write_b32 a135, v135\n" \
  "v_accvgpr_write_b32 a136, v136\n" \
  "v_accvgpr_write_b32 a137, v137\n" \
  "v_accvgpr_write_b32 a138, v138\n" \
  "v_accvgpr_write_b32 a139, v139\n" \
  "v_accvgpr_write_b32 a140, v140\n" \
  "v_accvgpr_write_b32 a141, v141\n" \
  "v_accvgpr_write_b32 a142, v142\n" \
  "v_accvgpr_write_b32 a143, v143\n" \
  "v_accvgpr_write_b32 a144, v144\n" \
  "v_accvgpr_write_b32 a145, v145\n" \
  "v_accvgpr_write_b32 a146, v146\n" \
  "v_accvgpr_write_b32 a147, v147\n" \
  "v_accvgpr_write_b32 a148, v148\n" \
  "v_accvgpr_write_b32 a149, v149\n" \
  "v_accvgpr_write_b32 a150, v150\n" \
  "v_accvgpr_write_b32 a151, v151\n" \
  "v_accvgpr_write_b32 a152, v152\n" \
  "v_accvgpr_write_b32 a153, v153\n" \
  "v_accvgpr_write_b32 a154, v154\n" \
  "v_accvgpr_write_b32 a155, v155\n" \
  "v_accvgpr_write_b32 a156, v156\n" \
  "v_accvgpr_write_b32 a157, v157\n" \
  "v_accvgpr_write_b32 a158, v158\n" \
  "v_accvgpr_write_b32 a159, v159\n" \
  "s_branch .LF_w_b_%=\n" \
  ".LF_r1_%=:\n" \
  "v_mov_b32 v192, v215\n" \
  "v_mov_b32 v193, v216\n" \
  "v_mov_b32 v194, v217\n" \
  "v_accvgpr_read_b32 v128, a32\n" \
  "v_accvgpr_read_b32 v129, a33\n" \
  "v_accvgpr_read_b32 v130, a34\n" \
  "v_accvgpr_read_b32 v131, a35\n" \
  "v_accvgpr_read_b32 v132, a36\n" \
  "v_accvgpr_read_b32 v133, a37\n" \
  "v_accvgpr_read_b32 v134, a38\n" \
  "v_accvgpr_read_b32 v135, a39\n" \
  "v_accvgpr_read_b32 v136, a40\n" \
  "v_accvgpr_read_b32 v137, a41\n" \
  "v_accvgpr_read_b32 v138, a42\n" \
  "v_accvgpr_read_b32 v139, a43\n" \
  "v_accvgpr_read_b32 v140, a44\n" \
  "v_accvgpr_read_b32 v141, a45\n" \
  "v_accvgpr_read_b32 v142, a46\n" \
  "v_accvgpr_read_b32 v143, a47\n" \
  "v_accvgpr_read_b32 v144, a48\n" \
  "v_accvgpr_read_b32 v145, a49\n" \
  "v_accvgpr_read_b32 v146, a50\n" \
  "v_accvgpr_read_b32 v147, a51\n" \
  "v_accvgpr_read_b32 v148, a52\n" \
  "v_accvgpr_read_b32 v149, a53\n" \
  "v_accvgpr_read_b32 v150, a54\n" \
  "v_accvgpr_read_b32 v151, a55\n" \
  "v_accvgpr_read_b32 v152, a56\n" \
  "v_accvgpr_read_b32 v153, a57\n" \
  "v_accvgpr_read_b32 v154, a58\n" \
  "v_accvgpr_read_b32 v155, a59\n" \
  "v_accvgpr_read_b32 v156, a60\n" \
  "v_accvgpr_read_b32 v157, a61\n" \
  "v_accvgpr_read_b32 v158, a62\n" \
  "v_accvgpr_read_b32 v159, a63\n" \
  "s_branch .LF_r_b_%=\n" \
  ".LF_r2_%=:\n" \
  "v_mov_b32 v192, v218\n" \
  "v_mov_b32 v193, v219\n" \
  "v_mov_b32 v194, v220\n" \
  "v_accvgpr_read_b32 v128, a64\n" \
  "v_accvgpr_read_b32 v129, a65\n" \
  "v_accvgpr_read_b32 v130, a66\n" \
  "v_accvgpr_read_b32 v131, a67\n" \
  "v_accvgpr_read_b32 v132, a68\n" \
  "v_accvgpr_read_b32 v133, a69\n" \
  "v_accvgpr_read_b32 v134, a70\n" \
  "v_accvgpr_read_b32 v135, a71\n" \
  "v_accvgpr_read_b32 v136, a72\n" \
  "v_accvgpr_read_b32 v137, a73\n" \
  "v_accvgpr_read_b32 v138, a74\n" \
  "v_accvgpr_read_b32 v139, a75\n" \
  "v_accvgpr_read_b32 v140, a76\n" \
  "v_accvgpr_read_b32 v141, a77\n" \
  "v_accvgpr_read_b32 v142, a78\n" \
  "v_accvgpr_read_b32 v143, a79\n" \
  "v_accvgpr_read_b32 v144, a80\n" \
  "v_accvgpr_read_b32 v145, a81\n" \
  "v_accvgpr_read_b32 v146, a82\n" \
  "v_accvgpr_read_b32 v147, a83\n" \
  "v_accvgpr_read_b32 v148, a84\n" \
  "v_accvgpr_read_b32 v149, a85\n" \
  "v_accvgpr_read_b32 v150, a86\n" \
  "v_accvgpr_read_b32 v151, a87\n" \
  "v_accvgpr_read_b32 v152, a88\n" \
  "v_accvgpr_read_b32 v153, a89\n" \
  "v_accvgpr_read_b32 v154, a90\n" \
  "v_accvgpr_read_b32 v155, a91\n" \
  "v_accvgpr_read_b32 v156, a92\n" \
  "v_accvgpr_read_b32 v157, a93\n" \
  "v_accvgpr_read_b32 v158, a94\n" \
  "v_accvgpr_read_b32 v159, a95\n" \
  "s_branch .LF_r_b_%=\n" \
  ".LF_r3_%=:\n" \
  "v_mov_b32 v192, v221\n" \
  "v_mov_b32 v193, v222\n" \
  "v_mov_b32 v194, v223\n" \
  "v_accvgpr_read_b32 v128, a96\n" \
  "v_accvgpr_read_b32 v129, a97\n" \
  "v_accvgpr_read_b32 v130, a98\n" \
  "v_accvgpr_read_b32 v131, a99\n" \
  "v_accvgpr_read_b32 v132, a100\n" \
  "v_accvgpr_read_b32 v133, a101\n" \
  "v_accvgpr_read_b32 v134, a102\n" \
  "v_accvgpr_read_b32 v135, a103\n" \
  "v_accvgpr_read_b32 v136, a104\n" \
  "v_accvgpr_read_b32 v137, a105\n" \
  "v_accvgpr_read_b32 v138, a106\n" \
  "v_accvgpr_read_b32 v139, a107\n" \
  "v_accvgpr_read_b32 v140, a108\n" \
  "v_accvgpr_read_b32 v141, a109\n" \
  "v_accvgpr_read_b32 v142, a110\n" \
  "v_accvgpr_read_b32 v143, a111\n" \
  "v_accvgpr_read_b32 v144, a112\n" \
  "v_accvgpr_read_b32 v145, a113\n" \
  "v_accvgpr_read_b32 v146, a114\n" \
  "v_accvgpr_read_b32 v147, a115\n" \
  "v_accvgpr_read_b32 v148, a116\n" \
  "v_accvgpr_read_b32 v149, a117\n" \
  "v_accvgpr_read_b32 v150, a118\n" \
  "v_accvgpr_read_b32 v151, a119\n" \
  "v_accvgpr_read_b32 v152, a120\n" \
  "v_accvgpr_read_b32 v153, a121\n" \
  "v_accvgpr_read_b32 v154, a122\n" \
  "v_accvgpr_read_b32 v155, a123\n" \
  "v_accvgpr_read_b32 v156, a124\n" \
  "v_accvgpr_read_b32 v157, a125\n" \
  "v_accvgpr_read_b32 v158, a126\n" \
  "v_accvgpr_read_b32 v159, a127\n" \
  "s_branch .LF_r_b_%=\n" \
  ".LF_r4_%=:\n" \
  "v_mov_b32 v192, v224\n" \
  "v_mov_b32 v193, v225\n" \
  "v_mov_b32 v194, v226\n" \
  "v_accvgpr_read_b32 v128, a128\n" \
  "v_accvgpr_read_b32 v129, a129\n" \
  "v_accvgpr_read_b32 v130, a130\n" \
  "v_accvgpr_read_b32 v131, a131\n" \
  "v_accvgpr_read_b32 v132, a132\n" \
  "v_accvgpr_read_b32 v133, a133\n" \
  "v_accvgpr_read_b32 v134, a134\n" \
  "v_accvgpr_read_b32 v135, a135\n" \
  "v_accvgpr_read_b32 v136, a136\n" \
  "v_accvgpr_read_b32 v137, a137\n" \
  "v_accvgpr_read_b32 v138, a138\n" \
  "v_accvgpr_read_b32 v139, a139\n" \
  "v_accvgpr_read_b32 v140, a140\n" \
  "v_accvgpr_read_b32 v141, a141\n" \
  "v_accvgpr_read_b32 v142, a142\n" \
  "v_accvgpr_read_b32 v143, a143\n" \
  "v_accvgpr_read_b32 v144, a144\n" \
  "v_accvgpr_read_b32 v145, a145\n" \
  "v_accvgpr_read_b32 v146, a146\n" \
  "v_accvgpr_read_b32 v147, a147\n" \
  "v_accvgpr_read_b32 v148, a148\n" \
  "v_accvgpr_read_b32 v149, a149\n" \
  "v_accvgpr_read_b32 v150, a150\n" \
  "v_accvgpr_read_b32 v151, a151\n" \
  "v_accvgpr_read_b32 v152, a152\n" \
  "v_accvgpr_read_b32 v153, a153\n" \
  "v_accvgpr_read_b32 v154, a154\n" \
  "v_accvgpr_read_b32 v155, a155\n" \
  "v_accvgpr_read_b32 v156, a156\n" \
  "v_accvgpr_read_b32 v157, a157\n" \
  "v_accvgpr_read_b32 v158, a158\n" \
  "v_accvgpr_read_b32 v159, a159\n" \
  "s_branch .LF_r_b_%=\n" \
  ".LF_r5_%=:\n" \
  "v_mov_b32 v192, v227\n" \
  "v_mov_b32 v193, v228\n" \
  "v_mov_b32 v194, v229\n" \
  "v_accvgpr_read_b32 v128, a160\n" \
  "v_accvgpr_read_b32 v129, a161\n" \
  "v_accvgpr_read_b32 v130, a162\n" \
  "v_accvgpr_read_b32 v131, a163\n" \
  "v_accvgpr_read_b32 v132, a164\n" \
  "v_accvgpr_read_b32 v133, a165\n" \
  "v_accvgpr_read_b32 v134, a166\n" \
  "v_accvgpr_read_b32 v135, a167\n" \
  "v_accvgpr_read_b32 v136, a168\n" \
  "v_accvgpr_read_b32 v137, a169\n" \
  "v_accvgpr_read_b32 v138, a170\n" \
  "v_accvgpr_read_b32 v139, a171\n" \
  "v_accvgpr_read_b32 v140, a172\n" \
  "v_accvgpr_read_b32 v141, a173\n" \
  "v_accvgpr_read_b32 v142, a174\n" \
  "v_accvgpr_read_b32 v143, a175\n" \
  "v_accvgpr_read_b32 v144, a176\n" \
  "v_accvgpr_read_b32 v145, a177\n" \
  "v_accvgpr_read_b32 v146, a178\n" \
  "v_accvgpr_read_b32 v147, a179\n" \
  "v_accvgpr_read_b32 v148, a180\n" \
  "v_accvgpr_read_b32 v149, a181\n" \
  "v_accvgpr_read_b32 v150, a182\n" \
  "v_accvgpr_read_b32 v151, a183\n" \
  "v_accvgpr_read_b32 v152, a184\n" \
  "v_accvgpr_read_b32 v153, a185\n" \
  "v_accvgpr_read_b32 v154, a186\n" \
  "v_accvgpr_read_b32 v155, a187\n" \
  "v_accvgpr_read_b32 v156, a188\n" \
  "v_accvgpr_read_b32 v157, a189\n" \
  "v_accvgpr_read_b32 v158, a190\n" \
  "v_accvgpr_read_b32 v159, a191\n" \
  "s_branch .LF_r_b_%=\n" \
  ".LF_bad_%=:\n" \
  "v_max_u32 v58, v0, v1\n" \
  "v_max3_u32 v58, v58, v2, v3\n" \
  "v_max3_u32 v58, v58, v4, v5\n" \
  "v_max3_u32 v58, v58, v6, v7\n" \
  "v_max3_u32 v58, v58, v8, v9\n" \
  "v_max3_u32 v58, v58, v10, v11\n" \
  "v_max3_u32 v58, v58, v12, v13\n" \
  "v_max3_u32 v58, v58, v14, v15\n" \
  "v_max3_u32 v58, v58, v16, v17\n" \
  "v_max3_u32 v58, v58, v18, v19\n" \
  "v_max3_u32 v58, v58, v20, v21\n" \
  "v_max3_u32 v58, v58, v22, v23\n" \
  "v_max3_u32 v58, v58, v24, v25\n" \
  "v_max3_u32 v58, v58, v26, v27\n" \
  "v_max3_u32 v58, v58, v28, v29\n" \
  "v_max3_u32 v58, v58, v30, v31\n" \
  "v_max3_u32 v58, v58, v32, v33\n" \
  "v_max3_u32 v58, v58, v34, v35\n" \
  "v_max3_u32 v58, v58, v36, v37\n" \
  "v_max3_u32 v58, v58, v38, v39\n" \
  "v_cmp_eq_u32 vcc, -1, v58\n" \
  "s_cbranch_vccz .LF_ok_%=\n" \
  "v_mov_b64 v[40:41], 0\n" \
  "v_mov_b64 v[42:43], 0\n" \
  "v_mov_b64 v[44:45], 0\n" \
  "v_mov_b64 v[46:47], 0\n" \
  "v_mov_b64 v[48:49], 0\n" \
  "v_mov_b64 v[50:51], 0\n" \
  "v_mov_b64 v[52:53], 0\n" \
  "v_mov_b64 v[54:55], 0\n" \
  "s_sleep 2\n" \
  "s_cmp_eq_u32 s48, 5\n" \
  "s_cbranch_scc1 .LF_bd1_%=\n" \
  "s_add_u32 s65, s65, %42\n" \
  "s_sleep 4\n" \
  ".LF_bd1_%=:\n" \
  "s_add_u32 s69, s69, 1\n" \
  "s_and_b32 s63, s69, 1023\n" \
  "s_cbranch_scc1 .LF_rd_%=\n" \
  "buffer_load_dwordx2 v[60:61], off, s[40:43], %38 offset:128 sc1\n" \
  "s_waitcnt vmcnt(0)\n" \
  "v_readfirstlane_b32 s63, v61\n" \
  "s_cmp_eq_u32 s63, %32\n" \
  "s_cbranch_scc1 .LF_ok_%=\n" \
  "s_cmp_le_u32 s69, 2097152\n" \
  "s_cbranch_scc1 .LF_rd_%=\n" \
  "v_mov_b32 v60, 1\n" \
  "v_mov_b32 v61, %32\n" \
  "buffer_store_dwordx2 v[60:61], off, s[40:43], %38 offset:128 sc1\n" \
  "s_branch .LF_ok_%=\n" \
  ".LF_end_%=:\n"
#define F_ASM_VOPS "+{v200}"(fi.sidx), "+{v201}"(fi.cidx), "+{v202}"(fi.l16), "+{v203}"(fi.ae0), "+{v204}"(fi.ae1), "+{v205}"(fi.ae2), "+{v206}"(fi.ae3), "+{v207}"(fi.am), "+{v208}"(fi.lw), "+{v209}"(fi.lr), "+{v210}"(fi.oo), "+{v211}"(fi.d0), "+{v212}"(fi.g[0]), "+{v213}"(fi.b[0]), "+{v214}"(fi.c[0]), "+{v215}"(fi.g[1]), "+{v216}"(fi.b[1]), "+{v217}"(fi.c[1]), "+{v218}"(fi.g[2]), "+{v219}"(fi.b[2]), "+{v220}"(fi.c[2]), "+{v221}"(fi.g[3]), "+{v222}"(fi.b[3]), "+{v223}"(fi.c[3]), "+{v224}"(fi.g[4]), "+{v225}"(fi.b[4]), "+{v226}"(fi.c[4]), "+{v227}"(fi.g[5]), "+{v228}"(fi.b[5]), "+{v229}"(fi.c[5])
#define F_ASM_SOPS "s"(fi.ws), "s"(fi.out), "s"(fi.tag), "s"(fi.o_u), "s"(fi.o_e), "s"(fi.o_x), "s"(fi.o_va), "s"(fi.o_tw), "s"(fi.o_ctl), "s"(fi.pa), "s"(fi.w), "s"(fi.f_w), "s"(fi.dret)
#define F_ASM_CLOB "v0", "v1", "v2", "v3", "v4", "v5", "v6", "v7", "v8", "v9", "v10", "v11", "v12", "v13", "v14", "v15", "v16", "v17", "v18", "v19", "v20", "v21", "v22", "v23", "v24", "v25", "v26", "v27", "v28", "v29", "v30", "v31", "v32", "v33", "v34", "v35", "v36", "v37", "v38", "v39", "v40", "v41", "v42", "v43", "v44", "v45", "v46", "v47", "v48", "v49", "v50", "v51", "v52", "v53", "v54", "v55", "v56", "v57", "v58", "v59", "v60", "v61", "v62", "v63", "v64", "v65", "v66", "v67", "v68", "v69", "v70", "v71", "v72", "v73", "v74", "v75", "v76", "v77", "v78", "v79", "v80", "v81", "v82", "v83", "v84", "v85", "v86", "v87", "v88", "v89", "v90", "v91", "v92", "v93", "v94", "v95", "v96", "v97", "v98", "v99", "v100", "v101", "v102", "v103", "v104", "v105", "v106", "v107", "v108", "v109", "v110", "v111", "v112", "v113", "v114", "v115", "v116", "v117", "v118", "v119", "v120", "v121", "v122", "v123", "v124", "v125", "v126", "v127", "v128", "v129", "v130", "v131", "v132", "v133", "v134", "v135", "v136", "v137", "v138", "v139", "v140", "v141", "v142", "v143", "v144", "v145", "v146", "v147", "v148", "v149", "v150", "v151", "v152", "v153", "v154", "v155", "v156", "v157", "v158", "v159", "v160", "v161", "v162", "v163", "v164", "v165", "v166", "v167", "v168", "v169", "v170", "v171", "v172", "v173", "v174", "v175", "v176", "v177", "v178", "v179", "v180", "v181", "v182", "v183", "v184", "v185", "v186", "v187", "v188", "v189", "v190", "v191", "v192", "v193", "v194", "v195", "v196", "v197", "v232", "v233", "v234", "v235", "v236", "v237", "a0", "a1", "a2", "a3", "a4", "a5", "a6", "a7", "a8", "a9", "a10", "a11", "a12", "a13", "a14", "a15", "a16", "a17", "a18", "a19", "a20", "a21", "a22", "a23", "a24", "a25", "a26", "a27", "a28", "a29", "a30", "a31", "a32", "a33", "a34", "a35", "a36", "a37", "a38", "a39", "a40", "a41", "a42", "a43", "a44", "a45", "a46", "a47", "a48", "a49", "a50", "a51", "a52", "a53", "a54", "a55", "a56", "a57", "a58", "a59", "a60", "a61", "a62", "a63", "a64", "a65", "a66", "a67", "a68", "a69", "a70", "a71", "a72", "a73", "a74", "a75", "a76", "a77", "a78", "a79", "a80", "a81", "a82", "a83", "a84", "a85", "a86", "a87", "a88", "a89", "a90", "a91", "a92", "a93", "a94", "a95", "a96", "a97", "a98", "a99", "a100", "a101", "a102", "a103", "a104", "a105", "a106", "a107", "a108", "a109", "a110", "a111", "a112", "a113", "a114", "a115", "a116", "a117", "a118", "a119", "a120", "a121", "a122", "a123", "a124", "a125", "a126", "a127", "a128", "a129", "a130", "a131", "a132", "a133", "a134", "a135", "a136", "a137", "a138", "a139", "a140", "a141", "a142", "a143", "a144", "a145", "a146", "a147", "a148", "a149", "a150", "a151", "a152", "a153", "a154", "a155", "a156", "a157", "a158", "a159", "a160", "a161", "a162", "a163", "a164", "a165", "a166", "a167", "a168", "a169", "a170", "a171", "a172", "a173", "a174", "a175", "a176", "a177", "a178", "a179", "a180", "a181", "a182", "a183", "a184", "a185", "a186", "a187", "a188", "a189", "a190", "a191", "s40", "s41", "s42", "s43", "s44", "s45", "s46", "s47", "s48", "s49", "s50", "s51", "s52", "s53", "s54", "s55", "s56", "s57", "s58", "s59", "s60", "s61", "s62", "s63", "s64", "s65", "s66", "s67", "s68", "s69", "s70", "s71", "s72", "s73", "s74", "s75", "s76", "s77", "s78", "s79", "s80", "s81", "s82", "s83", "s84", "s85", "s86", "s87", "s88", "s89", "s90", "s91", "s92", "s93", "s94", "s95", "s96", "s97", "s98", "s99", "vcc", "scc", "memory"

struct RedIn {
  unsigned sidx, cidx, l16, ax, am, mo, sel, to, fu, p0, p1, p2, p3, d0, d1, d2, d3, d4;
  const void* ws;
  unsigned w, go, tag, o_va, o_vb, lab, o_u, kind, o_ctl, pa, pb, f_d, f_r, ph, f_l;
};
__device__ __forceinline__ void red_role(const Params& p, int w, int T, char* smem) {
  const int grp = T & 3;
  CODE_PAD(PAD_RED);
  char* const ws = p.ws;
  Ctx cx = {ws, 0u};
  const int l = lane_id(), n = l & 15, g = l >> 4;
  comm_init(cx, grp * 9 + (T >> 2), w, smem, p.wl2 & 4);
  Bulk bk;
  bk.init(cx, grp);
  bk.pl.load(cx, grp);
  const Place& pl = bk.pl;
  char* const base = comm_base(ws) - CO;
  const unsigned dl = (unsigned)(ws - base);
  RedIn ri;
  ri.sidx = (n & 8) ? 0xEEEEu : 0x4444u;
  ri.cidx = (n & 3) * 0x5555u;
  ri.l16 = l * 16;
  ri.ax = W_PX * 32768 + (n >> 3) * 512 + g * 128 + (n & 7) * 16;
  ri.am = W_PM * 32768 + l * 16;
  const unsigned mt = (W_MEM + (T >> 4)) * 32768 + (T & 15) * 256;
  ri.mo = mt + (2 * (g & 1) + (g >> 1)) * 64 + (n >> 3) * 32 + (n & 1) * 16 + ((n >> 1) & 3) * 4;
  ri.sel = w ? (g == 1 ? (U_THIRD ? 2 : NSEG) : g >> 1) : (n & 1) ? 0x03020706u : 0x05040100u;
  ri.to = l < 4 ? CO + TW_OFF + (16384u ^ pl.dyn(l)) + 256 : 0x7F000000u;
  ri.fu = CO + pl.near(N_U + min(10 * w - 10 + min(n, 11), w == 3 ? 31 : 10 * w - 1)) + (T < 32 ? (T >> 1) & 3 : T & 3) * 8 + (g == 1 ? 64 : l & 32);
  ri.d0 = 0; ri.d1 = 0; ri.d2 = 0; ri.d3 = 0; ri.d4 = 0;
  ri.ws = base; ri.w = w; ri.go = pl.go; ri.tag = cx.tag; ri.lab = grp; ri.o_ctl = dl;
  ri.o_va = __builtin_amdgcn_readfirstlane(CO + (win_static(W_VO + 2 * (T >> 2)) ^ pl.dyn(grp)));
  ri.o_vb = __builtin_amdgcn_readfirstlane(CO + (win_static(W_VO + 2 * (T >> 2) + 1) ^ pl.dyn(grp)));
  auto dead = [&](int i) { return i < 0 ? CO + (16384u ^ pl.dyn(g)) + mt + n * 16 : (i < 4 ? ri.o_va : ri.o_vb) + (i & 3) * 1024 + l * 16; };
  ri.p0 = w ? dead(3 * w - 4) : T < 8 && l < 4 ? ri.to - 116 : 0x7F000000u;
  ri.p1 = w ? dead(3 * w - 3) : l < 4 ? 0u : 0x7F000000u; ri.p2 = dead(3 * w - 2);
  const int pz = (T >> 2) * 3 + w - 1;
  ri.p3 = pz < 24 ? CO + (16384u ^ pl.dyn(grp)) + (W_PX + (pz >> 2)) * 32768 + (pz & 3) * 1024 + l * 16 : 0x7F000000u;
  ri.o_u = __builtin_amdgcn_readfirstlane(u_tile(T, 0, 0) ^ (~bk.w13 & 8192u));
  ri.kind = __builtin_amdgcn_readfirstlane(T == 35 ? CO + (pl.near(N_K) ^ 4096u) - 32 : 0u);
  ri.pa = p.vlead << 16;
  ri.pb = p.dret | (p.late_r << 16);
  ri.ph = p.hdel | (p.hdl2 << 8) | 0x10000;
  ri.f_d = __builtin_amdgcn_readfirstlane(T < 32 ? CO + pl.near(N_RD + T) : 0x7F000000u);
  ri.f_r = dl + F_R * 128 + T * 8;
  ri.f_l = __builtin_amdgcn_readfirstlane(CO + (pl.near(N_DUR) ^ 4096u));
  asm volatile(R_ASM_TEXT : R_ASM_VOPS : R_ASM_SOPS : R_ASM_CLOB);
}

__device__ __forceinline__ int c_line(int h) { return (h >> 2) * 512 + (h & 1) * 256 + (h & 2) * 64; }
__device__ __forceinline__ void first_role(const Params& p) {
  const int l = lane_id();
  const Ctx cx = {p.ws, __builtin_amdgcn_readfirstlane(gen_load(p.ws)) + 1u};
  Place pl;
  pl.load(cx, 0);
  unsigned spins = 0;
#pragma unroll 1
  for (int s = 0; s < U_WATCH; ++s) {
    GAS u64* const f = (GAS u64*)(comm_base(p.ws) + pl.near(N_U + (l & 31))) + (l >> 5) * 2 + (s & 1) * 4;
    while (__any((int)min(flag_get(cx, f), flag_get(cx, f + 1)) <= s) && !spin_fail(spins, cx)) sleep_n(p.fsl);
    if (l == 0) flag_put(cx, (GAS u64*)(comm_base(p.ws) + (pl.near(N_DUR + s) ^ 4096u)), s + 1);
  }
}
struct ChainIn {
  unsigned sidx, l16, qw, kw, fq, fk, kxo, pst, ps2, lx, sid2, qxo, sto, plx, mk, ck0, ck2, hx, viw, fva, pk3, fvs, uo, po0, po1, po2, co0, co1, co2;
  unsigned vq0, vq1, vq2, vq3, vo0, vo1, vo2, vo3, v50, v51, v52, v53, tro0, tro1, tro2, tro3, tro4, m0o;
  const void *ws, *Wa, *Wb, *mem0;
  unsigned w, o_a, o_b, o_nx, o_exp, lab, f_p, f_m, f_dur, f_go, o_ctl, o_dbg, dret, o_qx, f_s, f_r;
};

template <bool SC>
__device__ __forceinline__ void chain_role(const Params& p, int w, int h, int grp) {
  constexpr unsigned QT = 9216, KT = 13312, SX = 16384, VI = 9216, VX = 16384;
  const int l = lane_id(), n = l & 15, g = l >> 4, q4 = (l >> 2) & 3, p4 = l & 3;
  const int ty = (n >> 1) & 1, row = 2 * (n >> 2) + (n & 1), cid = h * 2 + (SC ? 0 : 1);
  char* const base = comm_base(p.ws) - CO;
  const unsigned dl = (unsigned)(p.ws - base);
  ChainIn ci;
  ci.sidx = (n & 2) ? 0xEEEEu : 0x4444u;
  ci.l16 = l * 16;
  ci.m0o = row * (DM * 2) + g * 32 + ty * 16;
  if (SC) {
    ci.qw = QT + (32 * w + n) * 16 + g * 4;
    ci.kw = KT + (32 * w + n) * 16 + (((g & 1) << 1) | (g >> 1)) * 4;
    ci.fq = QT + (4 * g + q4) * 16 + (p4 & 1) * 8;
    ci.fk = KT + (4 * g + q4) * 16 + (p4 >> 1) * 8;
    ci.kxo = (g * 16 + 8 * (q4 & 1) + 4 * (q4 >> 1) + p4) * 16;
    const unsigned ex = CO + win_static(W_PX + (h >> 3)) + (h & 7) * 512 + (g & 1) * 128 + (n & 7) * 16 + (g >> 1) * 8 + (n >> 3);
    if (w < 2) ci.pst = ex + w * 256;
    else if (g & 1) ci.pst = CO + win_static(W_PC) + c_line(h) + (n & 7) * 16 + (((n >> 2) ^ (g >> 1)) & 1) * 8 + 2 * (w & 1) + (n >> 3);
    else ci.pst = CO + win_static(W_PM) + (h >> 1) * 256 + (VO_SWAP ? (g >> 1) * 8 + (h & 1) * 128 : (g >> 1) * 128 + (h & 1) * 8) + (n & 7) * 16 + 2 * (w & 1) + (n >> 3);
    ci.ps2 = w < 2 ? ci.pst + 2 : ex + 2 * (w & 1);
    ci.mk = (g & 1) ? 0u : 0x3F800000u;
    ci.ck0 = g == 1 && !(h & 2) ? 0x3F800000u : 0u;
    ci.ck2 = g == 1 && (h & 2) ? 0x3F800000u : 0u;
    ci.hx = SX + 2048 + ((w & 1) * 64 + l) * HAND_B;
    ci.lx = SX + l * 8 + (w == 1 ? 4 : 0);
    ci.qxo = (w >> 1) * 1024 + (g * 16 + 8 * (w & 1) + (n & 7)) * 16;
    ci.sto = 0x80000 + h * 256 + (8 * w + (n & 7)) * 8;
    ci.plx = n >= 8 ? 0x7F000000u : w * 16384 + n * 16 + ((g & 1) ? 4096 + c_line(h) + (((n >> 2) ^ (g >> 1)) & 1) * 8 : h * 128 + (g >> 1) * 8);
  } else {
    ci.viw = VI + (32 * w + n) * 16 + g * 4;
    ci.fva = VI + (4 * g + q4) * 16 + (p4 & 1) * 8;
    ci.pk3 = VI + n * 16 + g * 4; ci.fvs = w * 1024 + l * 16;
    ci.uo = w == 1 ? l * 16 : 0x7F000000u;
    auto vo_at = [&](int T0) {
      const int T = T0 + 2 * w + (g >> 1);
      return CO + win_static(W_VO + 2 * (T >> 2) + (h >> 4)) + (h & 15) * 256 + n * 16 + (T & 3);
    };
    ci.po0 = vo_at(8 * (g & 1)); ci.po1 = vo_at(16 + 8 * (g & 1));
    auto co_at = [&](int T0) { return (T0 + 2 * w + (g >> 1)) * 16384 + h * 256 + n * 16; };
    ci.co0 = co_at(8 * (g & 1)); ci.co1 = co_at(16 + 8 * (g & 1)); ci.co2 = co_at(w == 0 ? 16 : w == 1 ? 22 : 28) + (g & 1) * 8;
    if (w == 0) ci.po1 = h < 4 && l < 4 ? CO + TW_OFF + 16384 + 384 + l : 0x7F000000u;
    ci.po2 = vo_at(w == 0 ? 16 : w == 1 ? 22 : 28) + (g & 1) * 8;
  }
  ci.sid2 = (n & 8) ? 0xEEEEu : 0x4444u;
  unsigned vq[4], vo[4], v5[4];
#pragma unroll
  for (int i = 0; i < 4; ++i) {
    const int ch = n ^ (2 * ((g & 1) | (i << 1))), tp = ch >> 1;
    vq[i] = (g + 4 * i) * (HD * 2) + ch * 16 - 1024 * i;
    vo[i] = (g + 4 * i) * (DM * 2) + ch * 16 - 1024 * i;
    v5[i] = (g + 4 * i + 16 - 16 * (tp >> 2)) * (DM * 2) + (32 + (tp & 3)) * 32 + (ch & 1) * 16 - 1024 * i;
  }
  ci.vq0 = vq[0]; ci.vq1 = vq[1]; ci.vq2 = vq[2]; ci.vq3 = vq[3];
  ci.vo0 = vo[0]; ci.vo1 = vo[1]; ci.vo2 = vo[2]; ci.vo3 = vo[3];
  ci.v50 = v5[0]; ci.v51 = v5[1]; ci.v52 = v5[2]; ci.v53 = v5[3];
  {
    const int rowl = 16 * (g >> 1) + 8 * (q4 >> 1) + 4 * (g & 1) + (q4 & 1), rho = (rowl & 1) | (((rowl >> 2) & 3) << 1);
    ci.tro0 = rowl * 256 + (((4 * w + (p4 >> 1)) ^ (2 * rho)) * 16) + (p4 & 1) * 8;
    ci.tro1 = rowl * 256 + (((4 * w + 2 + (p4 >> 1)) ^ (2 * rho)) * 16) + (p4 & 1) * 8;
    const int row2 = 16 * (g & 1) + 4 * (g >> 1) + (q4 & 1), rho2 = (row2 & 1) | (((row2 >> 2) & 3) << 1);
    ci.tro2 = row2 * 256 + (((4 * w + 2 * (q4 >> 1) + (p4 >> 1)) ^ (2 * rho2)) * 16) + (p4 & 1) * 8;
    ci.tro3 = w < 2 ? (ci.tro2 ^ (w * 64)) + 7 * 16384 : ci.tro2 + 9 * 16384;
    ci.tro4 = w < 2 ? ci.tro3 + 16384 : ci.tro3 ^ 128u;
  }
  ci.ws = base; ci.Wa = SC ? p.Wq : p.Wv; ci.Wb = SC ? p.Wk : p.Wo; ci.mem0 = p.mem0;
  ci.w = w;
  ci.o_a = 16 * w * (HD * 2) + h * 256;
  ci.o_b = (h * DH + 32 * w) * (DM * 2);
  ci.o_nx = near_head(B_K, h) + (w & 1) * 4096;
  ci.o_exp = near_head(B_K, h) + (SC ? 0 : 4096);
  ci.o_qx = PB + 2 * B_O + (SC ? h * 16384 : 0); ci.f_s = __builtin_amdgcn_readfirstlane(dl + (F_S + h) * 128);
  ci.f_r = dl + F_R * 128;
  ci.lab = grp;
  ci.f_p = __builtin_amdgcn_readfirstlane(CO + near_static(N_K + h) + (SC ? 0 : 8));
  ci.f_m = SC ? dl + (F_M + cid) * 128 + (w & 1) * 8 : w == 1 ? dl + (F_M + cid) * 128 : 0x7F000000u; ci.f_dur = __builtin_amdgcn_readfirstlane(CO + near_static(N_DUR + h));
  ci.f_go = dl + (F_GO + grp) * 128; ci.o_ctl = dl;
  ci.o_dbg = dl + (unsigned)WS_END + (cid * 4 + w) * 4096;
  ci.dret = p.dret | (p.late_s << 16);
  if (SC) asm volatile(X_ASM_TEXT M_ASM_TEXT : X_ASM_VOPS : X_ASM_SOPS : X_ASM_CLOB);
  else asm volatile(W_ASM_TEXT V_ASM_TEXT : V_ASM_VOPS : V_ASM_SOPS : V_ASM_CLOB);
}

struct ProjIn {
  unsigned l16, xr0, xr1, ew, er, uo, xs0, xs1, xs2, xs3, xs4, xs5, xs6, xs7, xs8;
  unsigned vq0, vq1, vq2, vq3, vo0, vo1, vo2, vo3, v50, v51, v52, v53, tro0, tro1;
  const void *ws, *Wa, *Wb, *x;
  unsigned w, o_a, o_b, o_p, o_q, f_a, f_b, f_go, o_ctl, f_c, u8, rng, f_d;
};
template <bool VU, int QO = 0>
__device__ __forceinline__ unsigned proj_role(const Params& p, int w, int h) {
  const int l = lane_id(), n = l & 15, g = l >> 4, q4 = (l >> 2) & 3, p4 = l & 3;
  char* const base = comm_base(p.ws) - CO;
  const unsigned dl = (unsigned)(p.ws - base);
  ProjIn pi;
  pi.l16 = l * 16; pi.uo = 0;
  unsigned xs[9];
#pragma unroll
  for (int i = 0; i < 9; ++i) {
    const int ci = (9 * w + i) * 64 + l, r = ci / 72, cc = ci - r * 72;
    xs[i] = (r * 72 + (cc ^ ((r >> 1) & 7))) * 16;
  }
  pi.xs0 = xs[0]; pi.xs1 = xs[1]; pi.xs2 = xs[2]; pi.xs3 = xs[3]; pi.xs4 = xs[4]; pi.xs5 = xs[5]; pi.xs6 = xs[6]; pi.xs7 = xs[7]; pi.xs8 = xs[8];
  pi.xr0 = 16384 + n * 1152 + ((g ^ ((n >> 1) & 7)) * 16); pi.xr1 = 16384 + n * 1152 + (((4 + g) ^ ((n >> 1) & 7)) * 16);
  pi.ew = w * 2048 + n * 64 + ((g ^ ((n >> 1) & 1) ^ ((n >> 2) & 2)) * 16);
  pi.er = 8192 + (g >> 1) * 1032 + (8 * (g & 1) + q4) * 64 + ((p4 ^ (q4 >> 1) ^ (2 * (g & 1))) * 16);
  unsigned vq[4], vo[4], v5[4];
#pragma unroll
  for (int i = 0; i < 4; ++i) {
    const int ch = n ^ (2 * (g | ((i >> 1) << 2))), tp = ch >> 1;
    vq[i] = (g + 4 * i) * (HD * 2) + ch * 16 - 1024 * i;
    vo[i] = (g + 4 * i) * (DM * 2) + ch * 16 - 1024 * i;
    v5[i] = (g + 4 * i + 64 * (tp & 1)) * (DM * 2) + (32 + (tp >> 1)) * 32 + (ch & 1) * 16 - 1024 * i;
  }
  pi.vq0 = vq[0]; pi.vq1 = vq[1]; pi.vq2 = vq[2]; pi.vq3 = vq[3];
  pi.vo0 = vo[0]; pi.vo1 = vo[1]; pi.vo2 = vo[2]; pi.vo3 = vo[3];
  pi.v50 = v5[0]; pi.v51 = v5[1]; pi.v52 = v5[2]; pi.v53 = v5[3];
  const int rho = q4 | ((g & 1) << 2);
  pi.tro0 = (8 * g + q4) * 256 + (((4 * w + (p4 >> 1)) ^ (2 * rho)) * 16) + (p4 & 1) * 8;
  pi.tro1 = (8 * g + q4) * 256 + (((4 * w + 2 + (p4 >> 1)) ^ (2 * rho)) * 16) + (p4 & 1) * 8;
  pi.ws = base; pi.Wa = VU ? p.Wv : QO ? p.Wq : p.Wk; pi.Wb = VU ? p.Wo : p.Wq; pi.x = p.x;
  pi.w = w;
  pi.o_a = 16 * w * (HD * 2) + h * 256;
  pi.o_b = (h * DH + 16 * w) * (DM * 2);
  pi.o_p = PB + 2 * (VU ? B_V : QO ? B_Q : B_K) + h * 16384 + w * 2048 + (VU ? QO * 524288 : 0);
  pi.o_q = VU ? u_tile(2 * w, h >> 3, h & 7) ^ 8192u : PB + 2 * B_Q + h * 16384 + w * 2048 + (K_FIRST - 1) * 524288;
  pi.f_a = __builtin_amdgcn_readfirstlane(w || (QO && !VU) || QO == 2 ? 0x7F000000u : CO + near_static(N_K + h) + (VU ? 8 + 8 * QO : 0));
  pi.f_b = __builtin_amdgcn_readfirstlane(VU ? CO + near_static(N_U + h) + w * 8 + 32 * QO : w || QO ? 0x7F000000u : dl + (F_Q + h) * 128);
  pi.f_go = dl + (F_GO + 4 + (h & 3)) * 128; pi.o_ctl = dl;
  pi.f_c = VU ? dl + (F_M + 2 * h + 1) * 128 : QO ? 77 : 78;
  pi.f_d = __builtin_amdgcn_readfirstlane(QO ? 0x7F000000u : dl + (F_U + h) * 128 + w * 8);
  pi.rng = QO | (QO == 2 ? 4 : QO ? 2 * U_HELP + 1 : NSEG) << 8 | U_LEAD << 16;
  pi.u8 = QO && !VU ? 0x7F000000u : (VU ? u_tile(32 + w, h >> 3, h & 7) : near_head(B_K, h) + w * 2048) ^ 8192u;
  unsigned tag;
  sleep_n(VU ? (QO ? p.h0 : 0) : QO ? p.q0 : p.pj0);
  if (VU) asm volatile(U_ASM_TEXT : P_ASM_VOPS : P_ASM_SOPS : P_ASM_CLOB);
  else asm volatile(K_ASM_TEXT : P_ASM_VOPS : P_ASM_SOPS : P_ASM_CLOB);
  return tag;
}

constexpr int NS4 = 67;
__device__ __forceinline__ unsigned y_at(const Bulk& bk, int quad, unsigned o) { return bk.at(B_YT + quad * 16384 + o) ^ (quad >= Y_NEAR && quad < Y_FAR ? 8192u : 0u); }
__device__ __forceinline__ void mix_role(const Params& p, unsigned tag, int w, int ct, int s0, int ds, int s1, bool late) {
  const int l = lane_id(), n = l & 15, g = l >> 4;
  const Ctx cx = {p.ws, tag};
  Bulk bk;
  bk.init(cx, 4 + (ct & 3));
  const int tr = (w >> 1) * 16 + n, dc = ct * 96 + (w & 1) * 48 + 4 * g;
  const rsrc_t rx = mk_rsrc(p.x, (size_t)SL * DM * 2), rout = mk_rsrc(p.out, (size_t)SL * DM * 2);
  const int ro = (((w >> 1) * 16 + (l >> 2)) * DM + ct * 96 + (w & 1) * 48 + 4 * (l & 3)) * 2, pin = (4 * n + g) * 4, pout = (16 * (l & 3) + (l >> 2)) * 4;
  auto perm = [](int a, u32x2 v) { return (u32x2){(unsigned)__builtin_amdgcn_ds_bpermute(a, v[0]), (unsigned)__builtin_amdgcn_ds_bpermute(a, v[1])}; };
  u32x2 dv[3];
  unsigned yo[3];
#pragma unroll
  for (int i = 0; i < 3; ++i) { dv[i] = *(const u32x2*)(p.dsk + dc + 16 * i); yo[i] = y_at(bk, (dc + 16 * i) >> 2, tr * 8); }
  struct In { u32x4 a[4]; u32x2 b[4]; u32x2 x[3], y[3]; float g; bool ok; };
  auto ldb = [&](In& in, int sg) {
    in.g = __uint_as_float(__builtin_amdgcn_raw_buffer_load_b32(bk.rs, tr * 4, bk.at(B_G) + sg * 128, 16));
#pragma unroll
    for (int i = 0; i < 3; ++i) {
      in.y[i] = __builtin_amdgcn_raw_buffer_load_b64(bk.rs, yo[i], (sg >> 5) * 16384 + (sg & 31) * 256, 16);
      in.x[i] = __builtin_amdgcn_raw_buffer_load_b64(rx, ro + 32 * i, sg * (32 * DM * 2), 0);
    }
  };
  int seen = 0;
  const int ob0 = late ? NSEG : p.ob0, obn = p.ob1 - p.ob0, fa = F_A + (late ? 24 : 0) + ct * 4;
  auto seg = [=](int i) { return min(i < ob0 ? i : i + obn, NSEG - 1); };
  auto lda = [&](In& in, int sg) {
    in.ok = seen > sg;
    const unsigned l8 = in.ok ? l * 8 : 0x3F000000u;
#pragma unroll
    for (int kq = 0; kq < 4; ++kq) {
      const unsigned o = bk.at(B_AP + ((sg * 6 + ct) * 4 + kq) * 8192 + w * 2048);
      in.a[kq] = ld128(bk.rs, 2 * l8, o);
      in.b[kq] = __builtin_amdgcn_raw_buffer_load_b64(bk.rs, l8, o + 1024, 16);
    }
  };
  auto step = [&](In& in, In& n2, int k) {
    const int sg = seg(k);
    if (!in.ok) {
      wait_ge_n<2>(cx, fa, 1, 4, sg + 1, seen);
      lda(in, sg);
#pragma unroll
      for (int i = 0; i < 4; ++i) asm volatile("" : "+v"(in.a[i]), "+v"(in.b[i]));
    }
    ldb(n2, seg(k + 2 * ds));
    lda(n2, seg(k + 2 * ds));
    SB();
#pragma unroll
    for (int i = 0; i < 3; ++i) {
      float o[4], b[4];
      const u32x2 xv = perm(pin, in.x[i]);
#pragma unroll
      for (int j = 0; j < 4; ++j) {
        auto part = [&](int kq) { const unsigned u = i < 2 ? in.a[kq][2 * (i & 1) + (j >> 1)] : in.b[kq][j >> 1]; return (j & 1) ? hi16(u) : lo16(u); };
        const float av = (part(0) + part(1)) + (part(2) + part(3));
        const float y = (j & 1) ? hi16(in.y[i][j >> 1]) : lo16(in.y[i][j >> 1]);
        const float xf = (j & 1) ? hi16(xv[j >> 1]) : lo16(xv[j >> 1]), df = (j & 1) ? hi16(dv[i][j >> 1]) : lo16(dv[i][j >> 1]);
        o[j] = rbf(in.g * rbf(av));
        b[j] = rbf(1.0f - in.g) * rbf(y + rbf(xf * df));
      }
      u32x2 bp = {pk2(b[0], b[1]), pk2(b[2], b[3])};
      asm volatile("" : "+v"(bp));
#pragma unroll
      for (int j = 0; j < 4; ++j) o[j] += (j & 1) ? hi16(bp[j >> 1]) : lo16(bp[j >> 1]);
      __builtin_amdgcn_raw_buffer_store_b64(perm(pout, (u32x2){pk2(o[0], o[1]), pk2(o[2], o[3])}), rout, ro + 32 * i, sg * (32 * DM * 2), OUT_AUX);
    }
    return seg(k + ds) < s1;
  };
  wait_all<30>(cx, F_W, 1);
  In a, b, c;
  wait_ge_n<2>(cx, fa, 1, 4, 1, seen);
  ldb(a, seg(s0)); lda(a, seg(s0));
  ldb(b, seg(s0 + ds)); lda(b, seg(s0 + ds));
  for (int sg = s0; step(a, c, sg) && step(b, a, sg + ds) && step(c, b, sg + 2 * ds); sg += 3 * ds) {}
}

__device__ __forceinline__ void gate_rows(const Params& p, const Ctx& cx, const Bulk& bk, int blk) {
  const int l = lane_id(), n = l & 15, g = l >> 4;
  const rsrc_t rx = mk_rsrc(p.x, (size_t)SL * DM * 2), rg = mk_rsrc(p.gw, DM * 2);
  const int t = blk * 16 + g;
  u32x4 gw[4], xv[4][4];
  u32x2 gt, xt[4];
#pragma unroll
  for (int k = 0; k < 4; ++k) gw[k] = __builtin_amdgcn_raw_buffer_load_b128(rg, n * 16, k * 256, 0);
  gt = __builtin_amdgcn_raw_buffer_load_b64(rg, n * 8, 1024, 0);
#pragma unroll
  for (int r = 0; r < 4; ++r) {
#pragma unroll
    for (int k = 0; k < 4; ++k) xv[r][k] = __builtin_amdgcn_raw_buffer_load_b128(rx, t * (DM * 2) + n * 16, r * 4 * (DM * 2) + k * 256, 0);
    xt[r] = __builtin_amdgcn_raw_buffer_load_b64(rx, t * (DM * 2) + n * 8, r * 4 * (DM * 2) + 1024, 0);
  }
  SB();
#pragma unroll
  for (int r = 0; r < 4; ++r) {
    float acc = 0.f;
    auto term = [&](unsigned a, unsigned b) { const unsigned u = pk2(lo16(a) * lo16(b), hi16(a) * hi16(b)); acc += lo16(u) + hi16(u); };
#pragma unroll
    for (int k = 0; k < 4; ++k)
#pragma unroll
      for (int j = 0; j < 4; ++j) term(xv[r][k][j], gw[k][j]);
#pragma unroll
    for (int j = 0; j < 2; ++j) term(xt[r][j], gt[j]);
#pragma unroll
    for (int m = 8; m >= 1; m >>= 1) acc += __shfl_xor(acc, m);
    const float gg = rbf(1.0f / (1.0f + expf(-rbf(acc))));
    if (n == 0) __builtin_amdgcn_raw_buffer_store_b32(__float_as_uint(gg), bk.rs, (t + 4 * r) * 4, bk.at(B_G), 16);
  }
  WAIT_VM(0);
  if (l == 0) flag_put(cx, flag_ptr(p.ws, F_G) + blk, 1u);
}

__device__ __forceinline__ void att_role(const Params& p, unsigned tag, int w, int h, int sa, int sb) {
  CODE_PAD(PAD_ATT);
  char* const ws = p.ws;
  const int l = lane_id(), n = l & 15, g = l >> 4;
  const Ctx cx = {ws, tag};
  Bulk bk;
  bk.init(cx, 4 + (h & 3));
  const unsigned l16 = l * 16, vmo = (n + 16 * (((g & 1) << 1) | (g >> 1))) * 16;
  auto soft = [&](const f32x4* st, int qt, u32x4& px, u32x2& pm) {
    const int nv = qt ? 10 : 6;
    float v[10] = {(g & 1) ? st[0][2] : st[0][0], (g & 1) ? st[0][3] : st[0][1]};
#pragma unroll
    for (int j = 0; j < 4; ++j) {
      v[2 + j] = qt || 4 * g + j <= n ? st[1][j] : -__builtin_inff();
      v[6 + j] = 4 * g + j <= n ? st[2][j] : -__builtin_inff();
    }
    float mx = v[0];
#pragma unroll
    for (int j = 1; j < nv; ++j) mx = fmaxf(mx, v[j]);
    const float c = 0.08838834764831845f * 1.4426950408889634f, nx = -red_max_g(mx) * c;
    float sum = 0.f;
#pragma unroll
    for (int j = 0; j < nv; ++j) { v[j] = __builtin_amdgcn_exp2f(__builtin_fmaf(v[j], c, nx)); sum += v[j]; }
    const float inv = __builtin_amdgcn_rcpf(red_sum_g(sum));
    px = (u32x4){pk2(v[2] * inv, v[3] * inv), pk2(v[4] * inv, v[5] * inv), qt ? pk2(v[6] * inv, v[7] * inv) : 0u, qt ? pk2(v[8] * inv, v[9] * inv) : 0u};
    pm = (u32x2){pk2(v[0] * inv, v[1] * inv), 0u};
  };
  int seenp = 0, seenm = 0;
  auto wait_proj = [&](int target) {
    unsigned spins = 0;
    while (seenp < target) {
      const int v = (int)flag_get(cx, bk.proj_flag(cx, min(l, 3), h)), v2 = __builtin_amdgcn_readlane(v, 3);
      seenp = min(min(max(__builtin_amdgcn_readlane(v, 0), K_FIRST), __builtin_amdgcn_readlane(v, 1)), min(__builtin_amdgcn_readlane(v, 2), v2));
      if (seenp < target) { __builtin_amdgcn_s_sleep(4); if (spin_fail(spins, cx)) break; }
    }
    __builtin_amdgcn_fence(__ATOMIC_ACQUIRE, "wavefront");
  };
  GAS u64* const fm = flag_ptr(ws, F_M + 2 * h + ((l >> 1) & 1)) + (l & 1 & ~(l >> 1));
  auto min8 = [&](u64 f) {
    int v = (unsigned)(f >> 32) == cx.tag ? (int)(unsigned)f : 0;
#pragma unroll
    for (int m = 1; m < 4; m <<= 1) v = min(v, __shfl_xor(v, m));
    return __builtin_amdgcn_readfirstlane(v);
  };
  auto wait_m = [&](int target) {
    unsigned spins = 0;
    while (seenm < target) {
      seenm = min8(__hip_atomic_load(fm, RLX_AGENT));
      if (seenm < target) { __builtin_amdgcn_s_sleep(2); if (spin_fail(spins, cx)) break; }
    }
    __builtin_amdgcn_fence(__ATOMIC_ACQUIRE, "wavefront");
  };
  bf16x8 qn[4][2], kx[4][2], vn[8];
  auto ahead = [&](int s) {
    const unsigned so = (s * NH + h) * 8192, oq = bk.at(B_Q + so), ok = bk.at(B_K + so), ov = bk.at(B_V + so);
#pragma unroll
    for (int k = 0; k < 8; ++k) {
      qn[k >> 1][k & 1] = ldfrag(bk.rs, l16, oq + k * 1024);
      kx[k >> 1][k & 1] = ldfrag(bk.rs, l16, ok + k * 1024);
    }
#pragma unroll
    for (int et = 0; et < 8; ++et) vn[et] = ldfrag(bk.rs, l16, ov + et * 1024);
  };
  bf16x8 km[4];
  u32x2 vm[4];
  auto ldm = [&](int s) {
    const unsigned om = near_head(B_K, h) ^ (~bk.w13 & 8192u) ^ ((s ? s - 1 : 8192) << 14);
#pragma unroll
    for (int ks = 0; ks < 4; ++ks) km[ks] = ldfrag(bk.rs, l16, om + ks * 1024);
#pragma unroll
    for (int k = 0; k < 4; ++k) vm[k] = __builtin_amdgcn_raw_buffer_load_b64(bk.rs, vmo, om + 4096 + k * 1024, 16);
  };
  GAS u64* const fo = flag_ptr(ws, F_O + h) + w;
  if (l == 0) flag_put(cx, fo, sa + w);
  wait_proj(sa + w + 1);
  ahead(sa + w);
  wait_m(sa + w + 1);
  ldm(sa + w);
  bool pend = false, fq = false, gate = true;
  u64 fw = 0;
  for (int s = sa + w; s < sb; s += 4) {
    bf16x8 vx[8];
    u32x2 vc[4];
    f32x4 st[2][3] = {};
#pragma unroll
    for (int ks = 0; ks < 4; ++ks) { st[0][1] = MF(kx[ks][0], qn[ks][0], st[0][1]); st[1][1] = MF(kx[ks][0], qn[ks][1], st[1][1]); st[1][2] = MF(kx[ks][1], qn[ks][1], st[1][2]); }
#pragma unroll
    for (int ks = 0; ks < 4; ++ks) { st[0][0] = MF(km[ks], qn[ks][0], st[0][0]); st[1][0] = MF(km[ks], qn[ks][1], st[1][0]); vc[ks] = vm[ks]; vx[ks] = vn[ks]; vx[ks + 4] = vn[ks + 4]; }
    SB();
    if (fq) {
      seenm = max(seenm, min8(fw));
      __builtin_amdgcn_fence(__ATOMIC_ACQUIRE, "wavefront");
    }
    const bool more = s + 4 < sb, havep = more && seenp > s + 4, have = havep && seenm > s + 4;
    fq = have;
    if (have) {
      ldm(s + 4);
      fw = __hip_atomic_load(fm, RLX_AGENT);
    }
    if (havep) ahead(s + 4);
    SB();
    const unsigned oo = bk.at(B_O + (s * NH + h) * 8192);
#pragma unroll
    for (int qt = 0; qt < 2; ++qt) {
      u32x4 px;
      u32x2 pm;
      soft(st[qt], qt, px, pm);
      const u32x4 pxs = {px[2], px[3], px[0], px[1]};
      f32x4 o[8];
#pragma unroll
      for (int et = 0; et < 8; ++et) {
        const f32x4 z = {};
        o[et] = MF(vx[et], __builtin_bit_cast(bf16x8, (et & 1) ? pxs : px), z);
        o[et] = MF(zext8((u32x2){vc[et >> 1][et & 1], 0u}), zext8(pm), o[et]);
      }
      if (qt == 0 && pend) {
        if (havep) WAIT_VM(24); else WAIT_VM(0);
        if (l == 0) flag_put(cx, fo, s);
      }
#pragma unroll
      for (int k = 0; k < 4; ++k) st128(pack8(o[2 * k], o[2 * k + 1]), bk.rs, l16, oo + qt * 1024 + k * 2048);
    }
    SB();
    pend = true;
    if (!have) {
      pend = false;
      WAIT_VM(0);
      if (l == 0) flag_put(cx, fo, s + 4);
      if (gate && (s >= 20 || !more)) { gate_rows(p, cx, bk, 4 * h + w); gate = false; }
      if (more && !havep) { wait_proj(s + 5); ahead(s + 4); }
      if (more) { wait_m(s + 5); ldm(s + 4); }
    }
  }
}

__device__ __forceinline__ void xprep_role(const Params& p, unsigned tag, int w, int h0) {
  const int l = lane_id(), n = l & 15, g = l >> 4, qt = w & 1, par = w >> 1;
  const Ctx cx = {p.ws, tag};
  Bulk bk;
  bk.init(cx, 4 + (h0 & 3));
  const unsigned l16 = l * 16;
  const unsigned xo = X_AT + ((bk.w13 ^ 8192u) & 8192u);
  for (int i = 0; i < NDIR * 9; ++i)
    st128((u32x4){~0u, ~0u, ~0u, ~0u}, bk.rs, g * 16384 + (h0 + 8 * w) * 256 + n * 16, (bk.at(B_O + (i / 9) * 524288) ^ 8192u) + (i % 9) * 65536);
  for (unsigned spins = 0; __any(flag_get(cx, (l & 4) ? flag_ptr(p.ws, F_Q + h0 + 8 * (l & 3)) : flag_ptr(p.ws, F_Y) + 35 + h0 + 8 * (l & 3)) == 0u) && !spin_fail(spins, cx);) __builtin_amdgcn_s_sleep(8);
  __builtin_amdgcn_fence(__ATOMIC_ACQUIRE, "wavefront");
#pragma unroll 1
  for (int h = h0; h < NH; h += 8) {
  bf16x8 qq[NDIR / 2][4], kk[NDIR / 2][8];
#pragma unroll
  for (int i2 = 0; i2 < NDIR; i2 += 2) {
    const unsigned so = ((SDIR + i2 + par) * NH + h) * 8192, oq = bk.at(B_Q + so) + qt * 1024, ok = bk.at(B_K + so);
#pragma unroll
    for (int ks = 0; ks < 4; ++ks) {
      qq[i2 >> 1][ks] = ldfrag(bk.rs, l16, oq + ks * 2048);
      kk[i2 >> 1][2 * ks] = ldfrag(bk.rs, l16, ok + ks * 2048); kk[i2 >> 1][2 * ks + 1] = ldfrag(bk.rs, l16, ok + ks * 2048 + 1024);
    }
  }
  SB();
#pragma unroll
  for (int i2 = 0; i2 < NDIR; i2 += 2) {
    const int i = i2 + par;
    const unsigned oc = (bk.at(B_O + ((SDIR + i) * NH + h) * 8192) ^ 8192u) + qt * 1024;
    f32x4 a = {}, b = {};
#pragma unroll
    for (int ks = 0; ks < 4; ++ks) {
      const bf16x8 q = qq[i2 >> 1][ks];
      st128(__builtin_bit_cast(u32x4, q), bk.rs, l16, oc + ks * 2048);
      a = MF(kk[i2 >> 1][2 * ks], q, a); b = MF(kk[i2 >> 1][2 * ks + 1], q, b);
    }
    float mx = -__builtin_inff(), sum = 0.f;
#pragma unroll
    for (int j = 0; j < 4; ++j) {
      a[j] = 4 * g + j <= 16 * qt + n ? a[j] : -__builtin_inff(); b[j] = 16 + 4 * g + j <= 16 * qt + n ? b[j] : -__builtin_inff();
      mx = fmaxf(mx, fmaxf(a[j], b[j]));
    }
    const float c = 0.08838834764831845f * 1.4426950408889634f, nx = -red_max_g(mx) * c;
#pragma unroll
    for (int j = 0; j < 4; ++j) { a[j] = __builtin_amdgcn_exp2f(__builtin_fmaf(a[j], c, nx)); b[j] = __builtin_amdgcn_exp2f(__builtin_fmaf(b[j], c, nx)); sum += a[j] + b[j]; }
    sum = red_sum_g(sum);
    const unsigned eo = xo + 0x100000 + (i * 4 + 2 * qt + (n >> 3)) * 32768 + (h >> 4) * 16384 + (h & 15) * 512 + (g >> 1) * 128 + (n & 7) * 16 + (g & 1) * 8;
    __builtin_amdgcn_raw_buffer_store_b64((u32x2){pk2(a[0], a[1]), pk2(a[2], a[3])}, bk.rs, eo, 0, 16);
    __builtin_amdgcn_raw_buffer_store_b64((u32x2){pk2(b[0], b[1]), pk2(b[2], b[3])}, bk.rs, eo, 256, 16);
    if (g == 0) __builtin_amdgcn_raw_buffer_store_b64((u32x2){__float_as_uint(nx), __float_as_uint(sum)}, bk.rs, h * 256 + (16 * qt + n) * 8, xo + 0x80000 + i * 65536, 16);
    __builtin_amdgcn_raw_buffer_store_b64((u32x2){0xFFFFFFFFu, 0xFFFFFFFFu}, bk.rs, (2 * qt + (g >> 1)) * 16384 + ((g & 1) ? 4096 + c_line(h) : h * 128) + n * 8, xo + i * 65536, 16);
  }
  }
  WAIT_VM(0);
  BARRIER();
  if (l == 0) flag_put(cx, flag_ptr(p.ws, F_S + h0 + 8 * w), 1u);
}

struct OutIn {
  unsigned l16, rw, rr, fo, wv0, wv1, wv2, tro, l8, rw0, rr0;
  const void *ws, *Wo;
  unsigned w, tag, o_w, o_o, o_a, f_a, o_ctl, ns;
};
__device__ __forceinline__ void op_role(const Params& p, unsigned tag, int w, int ct, int kq, int grp, bool late) {
  const Ctx cx = {p.ws, tag};
  Bulk bk;
  bk.init(cx, grp);
  const int l = lane_id(), g = l >> 4, q4 = (l >> 2) & 3, p4 = l & 3, h0 = kq * 8 + w * 2;
  char* const base = comm_base(p.ws) - CO;
  const unsigned dl = (unsigned)(p.ws - base);
  OutIn oi;
  oi.l16 = l * 16; oi.l8 = l * 8;
  oi.rw = 65536 + w * 12288 + l * 16;
  oi.rr = 65536 + w * 3072 + l * 16;
  oi.fo = dl + (F_O + h0 + ((l >> 2) & 1)) * 128 + (l & 3) * 8;
  auto piece = [&](int i) { const int idx = i * 64 + l, row = idx / 12; return row * (DM * 2) + (idx - row * 12) * 16; };
  oi.wv0 = piece(0); oi.wv1 = piece(1); oi.wv2 = piece(2);
  oi.tro = w * 24576 + (4 * g + q4) * 192 + p4 * 8;
  oi.ws = base; oi.Wo = p.Wo; oi.w = w; oi.tag = __builtin_amdgcn_readfirstlane(tag); oi.o_ctl = dl;
  oi.ns = __builtin_amdgcn_readfirstlane(late ? p.ob0 | p.ob1 << 8 : p.ob0 ? p.ob0 << 8 | p.ob1 << 16 | SDIR << 24 : p.ob1 | SDIR << 8);
  oi.rw0 = oi.rw; oi.rr0 = oi.rr;
  oi.o_w = __builtin_amdgcn_readfirstlane((kq * 1024 + w * 256) * (DM * 2) + ct * 192);
  oi.o_o = __builtin_amdgcn_readfirstlane(bk.w13 + 2 * B_O + h0 * 16384);
  oi.o_a = __builtin_amdgcn_readfirstlane(bk.w13 + 2 * B_AP + (ct * 4 + kq) * 16384 + w * 2048);
  oi.f_a = __builtin_amdgcn_readfirstlane(w ? 0x7F000000u : dl + (F_A + ct * 4 + kq) * 128 + (late ? 24 * 128 : 0));
  asm volatile(O_ASM_TEXT : O_ASM_VOPS : O_ASM_SOPS : O_ASM_CLOB);
}

typedef __attribute__((ext_vector_type(16))) float f32x16;
constexpr int S4_PRM = 0;
constexpr int S4_KD = S4_PRM + 2560;
constexpr int S4_KH = S4_KD + 2624;
constexpr int S4_U = S4_KH + 5200;
constexpr int S4_USTR = 4352 + 64;
constexpr int S4_WSZ = S4_U + 4 * S4_USTR;
constexpr int S4_RAW = 4 * S4_WSZ;

#define S4M(i) do {} while (0)
struct S4In { u32x2 u[8]; unsigned la, ai, cr, ci, dt; };
__device__ __forceinline__ float s4_params(LAS char* my, const S4In& in) {
  const int l = lane_id();
  const u16 pl_[2] = {(u16)in.la, (u16)(in.la >> 16)}, pa_[2] = {(u16)in.ai, (u16)(in.ai >> 16)}, pr_[2] = {(u16)in.cr, (u16)(in.cr >> 16)}, pi_[2] = {(u16)in.ci, (u16)(in.ci >> 16)};
  const float dt = rbf(expf(bf2f((u16)in.dt)));
  f32x4 pr[2];
#pragma unroll
  for (int k = 0; k < 2; ++k) {
    const float a = -rbf(expf(bf2f(pl_[k]))) * dt;
    const float b = bf2f(pa_[k]) * dt;
    const float c_hi = 0.15915494f, c_lo = (float)(0.15915494309189535 - (double)0.15915494f);
    const float bh = b * c_hi;
    const float bl = __builtin_fmaf(b, c_hi, -bh) + b * c_lo;
    const float cr = bf2f(pr_[k]), ci = bf2f(pi_[k]);
    pr[k] = (f32x4){a * 1.4426950408889634f, bh, bl, log2f(hypotf(cr, ci))};
    *(LAS float*)(my + S4_PRM + 2048 + (l + 64 * k) * 4) = -atan2f(ci, cr) * c_hi;
  }
  float top = fmaxf(fmaxf(pr[0][3], pr[1][3]), -100.f);
#pragma unroll
  for (int m = 1; m < 64; m <<= 1) top = fmaxf(top, __shfl_xor(top, m));
  top = ceilf(top);
#pragma unroll
  for (int k = 0; k < 2; ++k) { pr[k][3] += 14.f - top; *(LAS f32x4*)(my + S4_PRM + (l + 64 * k) * 16) = pr[k]; }
  return exp2f(top - 27.f) * dt;
}
typedef __attribute__((ext_vector_type(2))) _Float16 f16x2;
typedef __attribute__((ext_vector_type(8))) _Float16 f16x8;
__device__ __forceinline__ void s4_modes(LAS char* my, int q0, int q1, f32x4 (&D)[3]) {
  const int l = lane_id(), i = l & 15, kk = l >> 4;
  const f32x2 pe[2] = {{(float)(8 * i), (float)(128 * (i >> 3) + (i & 7))},
                       {(float)(i < 8 ? 256 + 128 * (i >> 2) + 2 * (i & 3) : 2048), (float)(i < 8 ? 512 + 128 * (i >> 1) + 4 * (i & 1) : 128 * i)}};
  const f32x2 pp[2] = {{-pe[0][0], pe[0][1]}, pe[1]};
#pragma unroll 1
  for (int q = q0; q < q1; ++q) {
    u32x4 op[4];
    const f32x4 pc = *(LAS f32x4*)(my + S4_PRM + 2048 + (4 * q + kk) * 16);
#pragma unroll
    for (int j = 0; j < 4; ++j) {
      const f32x4 pr = *(LAS f32x4*)(my + S4_PRM + (16 * q + 4 * kk + j) * 16);
#pragma unroll
      for (int m = 0; m < 2; ++m) {
        const f32x2 ea = __builtin_elementwise_fma((f32x2){pr[0], pr[0]}, pe[m], (f32x2){m ? 14.f : pr[3], 14.f});
        const f32x2 t = pp[m] * pr[1];
        f32x2 fr = __builtin_elementwise_fma((f32x2){pr[1], pr[1]}, pp[m], -(f32x2){__builtin_rintf(t[0]), __builtin_rintf(t[1])});
        fr = __builtin_elementwise_fma((f32x2){pr[2], pr[2]}, pp[m], fr);
        if (!m) fr[0] += pc[j];
#pragma unroll
        for (int h = 0; h < 2; ++h) {
          const f32x2 v = (f32x2){__builtin_amdgcn_cosf(fr[h]), __builtin_amdgcn_sinf(fr[h])} * __builtin_amdgcn_exp2f(ea[h]);
          op[2 * m + h][j] = __builtin_bit_cast(unsigned, __builtin_convertvector(v, f16x2));
        }
      }
    }
#pragma unroll
    for (int m = 0; m < 3; ++m) D[m] = __builtin_amdgcn_mfma_f32_16x16x32_f16(__builtin_bit_cast(f16x8, op[m + 1]), __builtin_bit_cast(f16x8, op[0]), D[m], 0, 0, 0);
  }
}
__device__ __forceinline__ void s4d_task(const Ctx& cx, const Bulk& bk, unsigned yflip, int gentle, int w, int task, char* smem, const S4In& in, char* ws, int q0, f32x4 (&D)[3], float ksc) {
  const int l = lane_id(), tid = w * 64 + l;
  LAS char* const lds = (LAS char*)smem;
  LAS char* const my = lds + w * S4_WSZ;
  S4M(2);
  s4_modes(my, q0, 8, D);
  {
    const int i = l & 15, kk = l >> 4;
#pragma unroll
    for (int r = 0; r < 4; ++r) {
      const int row = 4 * kk + r;
      *(LAS float*)(my + S4_KD + (128 * (row >> 3) + (row & 7) + 8 * i) * 4) = ksc * D[0][r];
      *(LAS float*)(my + S4_KD + (row < 8 ? 256 + 64 * kk + r + 4 * i : 640 + i) * 4) = ksc * D[1][r];
      *(LAS float*)(my + S4_KD + (row < 8 ? 384 + 32 * (row >> 1) + (row & 1) + 2 * i : 384 + 16 * row + i) * 4) = ksc * D[2][r];
    }
  }
  S4M(3);
#pragma unroll
  for (int k = 0; k < 2; ++k)
#pragma unroll
    for (int j = 0; j < 8; j += 4) *(LAS u32x4*)(lds + S4_RAW + k * 8208 + tid * 32 + j * 4) = (u32x4){in.u[j][k], in.u[j + 1][k], in.u[j + 2][k], in.u[j + 3][k]};
  BARRIER();
  u32x2 r[11];
#pragma unroll
  for (int k = 0; k < 2; ++k)
#pragma unroll
    for (int j = 0; j < 12; j += 4) {
      const u32x4 v = *(LAS u32x4*)(lds + S4_RAW + k * 8208 + tid * 32 + j * 4);
#pragma unroll
      for (int i = 0; i < 4; ++i) if (j + i < 11) r[j + i][k] = v[i];
    }
#pragma unroll
  for (int ch = 0; ch < 4; ++ch) {
    unsigned pr[10];
#pragma unroll
    for (int j = 0; j < 10; ++j) pr[j] = __builtin_amdgcn_perm(r[j + 1][ch >> 1], r[j][ch >> 1], (ch & 1) ? 0x07060302u : 0x05040100u);
    if (tid == 255) { pr[7] &= 0xFFFFu; pr[8] = 0u; pr[9] = 0u; }
    LAS char* ub = lds + ch * S4_WSZ + S4_U + 64 + tid * 16;
#pragma unroll
    for (int sft = 0; sft < 4; ++sft) *(LAS u32x4*)(ub + sft * S4_USTR) = (u32x4){pr[sft], pr[sft + 2], pr[sft + 4], pr[sft + 6]};
    if (tid < 4) {
#pragma unroll
      for (int sft = 0; sft < 4; ++sft) {
        const u32x4 v = {0u, 0u, sft == 3 ? pr[0] << 16 : 0u, sft == 0 ? 0u : sft == 1 ? pr[0] << 16 : pr[sft - 2]};
        *(LAS u32x4*)(lds + ch * S4_WSZ + S4_U + sft * S4_USTR + (3 - tid) * 16) = tid == 0 ? v : (u32x4){0u, 0u, 0u, 0u};
      }
    }
  }
  {
    if (l < 5) {
      *(LAS u32x4*)(my + S4_KH + l * 16) = (u32x4){0u, 0u, 0u, 0u};
    }
#pragma unroll
    for (int k = 0; k < 4; ++k) {
      const int grp = l + 64 * k;
      float v[8];
#pragma unroll
      for (int j = 0; j < 8; j += 2) {
        const unsigned c = pk2((float)(8 * grp + j), (float)(8 * grp + j + 1)) - 0x42804280u;
        const bool low = k == 0 && grp < 16;
        v[j] = *(LAS float*)(my + S4_KD + (low ? 8 * grp + j : c & 0xFFFFu) * 4);
        v[j + 1] = *(LAS float*)(my + S4_KD + (low ? 8 * grp + j + 1 : c >> 16) * 4);
      }
      *(LAS u32x4*)(my + S4_KH + 80 + (grp >> 2) * 80 + (grp & 3) * 16) = (u32x4){pk2(v[7], v[6]), pk2(v[5], v[4]), pk2(v[3], v[2]), pk2(v[1], v[0])};
    }
  }
  __syncthreads();
  S4M(4);
  {
    const int r = l & 31, hh = l >> 5, sft = (r + 1) & 3;
    unsigned ua = w * S4_WSZ + S4_U + sft * S4_USTR + (r + 25 - sft - 8 * hh - 16) * 2;
    const unsigned kz = w * S4_WSZ + S4_KH + hh * 16;
    unsigned kb1 = kz + (33 + r) * 80;
    unsigned kb0 = kz + (1 + r) * 80;
    f32x16 acc0 = {}, acc1 = {};
    unsigned ub = ua + 72;
    asm volatile("" : "+v"(ub));
    struct Ops { bf16x8 a0, a1, b0, b1; };
    auto ld = [&](int ks) {
      Ops o;
      const unsigned uo = ua + (1 - ks) * 32;
      o.a0 = cat2(*(LAS bf16x4*)(lds + uo), *(LAS bf16x4*)(lds + uo + 8));
      o.a1 = cat2(*(LAS bf16x4*)(lds + uo + 2048), *(LAS bf16x4*)(lds + uo + 2056));
      o.b0 = *(LAS bf16x8*)(lds + kb0 + ks * 32);
      o.b1 = *(LAS bf16x8*)(lds + kb1 + ks * 32);
      return o;
    };
    auto step = [&](const Ops& o, Ops& nx, int ks) {
      const unsigned uo = ua + (1 - ks) * 32, up = ub + (1 - ks) * 32;
      acc1 = __builtin_amdgcn_mfma_f32_32x32x16_bf16(o.a0, o.b1, acc1, 0, 0, 0); SB();
      nx.a0 = cat2(*(LAS bf16x4*)(lds + uo), *(LAS bf16x4*)(lds + up)); nx.b1 = *(LAS bf16x8*)(lds + kb1 + ks * 32); SB();
      acc0 = __builtin_amdgcn_mfma_f32_32x32x16_bf16(o.a0, o.b0, acc0, 0, 0, 0); SB();
      nx.b0 = *(LAS bf16x8*)(lds + kb0 + ks * 32);
      nx.a1 = cat2(*(LAS bf16x4*)(lds + uo + 2048), *(LAS bf16x4*)(lds + up + 2048));
      if (ks) {
        ub += 64;
        ua += 64;
        kb1 -= 80;
        kb0 = max(kb0 - 80, kz);
      }
      SB();
      acc1 = __builtin_amdgcn_mfma_f32_32x32x16_bf16(o.a1, o.b0, acc1, 0, 0, 0); SB();
    };
    Ops e0 = ld(0), e1 = ld(1), o0, o1;
    ua += 64; kb1 -= 80; kb0 = max(kb0 - 80, kz);
#pragma unroll 1
    for (int D = 0; D < 32; D += 2) {
      step(e0, o0, 0);
      step(e1, o1, 1);
      step(o0, e0, 0);
      step(o1, e1, 1);
      sleep_n(gentle);
    }
    S4M(5);
#pragma unroll
    for (int j = 0; j < 4; ++j) {
      const unsigned off = (32 * r + 8 * j + 4 * hh) * 2;
      *(LAS u32x2*)(my + off) = (u32x2){pk2(acc0[4 * j], acc0[4 * j + 1]), pk2(acc0[4 * j + 2], acc0[4 * j + 3])};
      *(LAS u32x2*)(my + off + 2048) = (u32x2){pk2(acc1[4 * j], acc1[4 * j + 1]), pk2(acc1[4 * j + 2], acc1[4 * j + 3])};
    }
    __syncthreads();
#pragma unroll
    for (int j = 0; j < 2; ++j) {
      u32x2 c[4];
#pragma unroll
      for (int k = 0; k < 4; ++k) c[k] = *(LAS u32x2*)(lds + k * S4_WSZ + (j * 256 + tid) * 8);
      const unsigned o = bk.at(B_YT + task * 16384 + j * 8192) ^ yflip;
#pragma unroll
      for (int k = 0; k < 2; ++k)
        st128((u32x4){__builtin_amdgcn_perm(c[1][k], c[0][k], 0x05040100u), __builtin_amdgcn_perm(c[3][k], c[2][k], 0x05040100u),
                      __builtin_amdgcn_perm(c[1][k], c[0][k], 0x07060302u), __builtin_amdgcn_perm(c[3][k], c[2][k], 0x07060302u)}, bk.rs, tid * 32 + k * 16, o);
    }
  }
  __syncthreads();
  S4M(6);
}

__device__ __forceinline__ void warm_l2(const u16* W, unsigned off, bool rows256, int w) {
  const rsrc_t rw = mk_rsrc(W, (size_t)DM * HD * 2);
  const int l = lane_id();
  const unsigned vo = rows256 ? (l >> 4) * (HD * 2) + (l & 15) * 16 : l * 16, st = rows256 ? 4 * HD * 2 : 1024;
  u32x4 v[36];
#pragma unroll
  for (int i = 0; i < 36; ++i) v[i] = __builtin_amdgcn_raw_buffer_load_b128(rw, vo, off + (i * 4 + w) * st, 0);
#pragma unroll
  for (int i = 0; i < 36; ++i) asm volatile("" :: "v"(v[i]));
}

__device__ __forceinline__ void x_ahead(const Params& p, int w, int idx) {
  const rsrc_t rx = mk_rsrc(p.x, (size_t)SL * DM * 2);
  for (int i = 0; i < p.xpre; ++i) dma16(rx, lane_id() * 16, ((((idx - 35) >> 2) * 4 + w) + i * 32) * 1024, LDS_BYTES - 1024);
}
__device__ __forceinline__ unsigned s4_role(const Params& p, int w, int idx, char* smem) {
  CODE_PAD(PAD_S4);
  char* const ws = p.ws;
  const int l = lane_id(), tid = w * 64 + l;
  const bool near = idx >= 8 && idx < 35;
  const int r_ = idx - 8, t0 = 5 * r_ + (r_ + 2) / 3;
  const int t1 = t0 + 5 + (r_ % 3 == 0) - p.s4skip;
  const rsrc_t rx = mk_rsrc(p.x, (size_t)SL * DM * 2);
  S4In in[6];
#pragma unroll
  for (int k = 0; k < 6; ++k) {
    const int d = min(t0 + k, t1 - 1) * 4 + w, o = d * (NST / 2) + l;
    in[k].la = ((const unsigned*)p.lar)[o]; in[k].ai = ((const unsigned*)p.aim)[o]; in[k].cr = ((const unsigned*)p.cre)[o]; in[k].ci = ((const unsigned*)p.cim)[o];
    in[k].dt = __builtin_amdgcn_raw_buffer_load_b16(mk_rsrc(p.ldt, DM * 2), d * 2, 0, 0);
  }
  const unsigned gen = gen_load(ws);
  LAS char* const my = (LAS char*)smem + w * S4_WSZ;
  float ksc = s4_params(my, in[0]);
  f32x4 D[3] = {};
  const int qs = near ? p.s4q : p.s4f;
  s4_modes(my, 0, qs, D);
  const Ctx cx = {ws, __builtin_amdgcn_readfirstlane(gen) + 1u};
  Bulk bk;
  bk.init(cx, idx & 7);
  for (int m = 4 * t0; m < 4 * t1; ++m) st128((u32x4){0u, 0u, 0u, 0u}, bk.rs, tid * 16, bk.at(B_YT + m * 4096) ^ 8192u);
#pragma unroll
  for (int m = 0; m < 6; ++m)
#pragma unroll
    for (int j = 0; j < 8; ++j) in[m].u[j] = __builtin_amdgcn_raw_buffer_load_b64(rx, t0 + m < t1 ? (tid * 8 + j) * (DM * 2) : 0x7F000000u, (t0 + m) * 8, 0);
  {
    sleep_n((r_ % 3 ? r_ % 7 : 0) * p.stag);
  }
  s4_modes(my, qs, 8, D);
#pragma unroll 1
  for (int k = t0; k < t1; ++k) {
    s4d_task(cx, bk, near ? 8192u : 0u, near ? p.gentle : 0, w, k, smem, in[0], ws, k == t0 ? 8 : 0, D, ksc);
    in[0] = in[1]; in[1] = in[2]; in[2] = in[3]; in[3] = in[4]; in[4] = in[5];
    ksc = s4_params(my, in[0]);
    D[0] = D[1] = D[2] = (f32x4){0.f, 0.f, 0.f, 0.f};
  }
  WAIT_VM(0);
  BARRIER();
  if (tid == 0) flag_put(cx, flag_ptr(ws, F_Y) + idx, 1u);
  return cx.tag;
}
struct FinIn {
  unsigned sidx, cidx, l16, ae0, ae1, ae2, ae3, am, lw, lr, oo, d0;
  float g[NDIR], b[NDIR], c[NDIR];
  const void *ws, *out;
  unsigned tag, o_u, o_e, o_x, o_va, o_tw, o_ctl, pa, w, f_w, dret;
};
__device__ __forceinline__ void watch_role(const Params& p, unsigned tag) {
  const int l = lane_id();
  const Ctx cx = {p.ws, tag};
  for (unsigned spins = 0; __any(flag_get(cx, flag_ptr(p.ws, F_Y) + min(8 + l, NS4 - 1)) == 0u) && !spin_fail(spins, cx);) __builtin_amdgcn_s_sleep(30);
  wait_all<30>(cx, F_G, 64);
  wait_all<30>(cx, F_G + 4, 64);
  unsigned spins = 0;
  while (__any(flag_get(cx, flag_ptr(p.ws, F_S + (l & 31))) == 0u) && !spin_fail(spins, cx)) __builtin_amdgcn_s_sleep(30);
  publish(cx, 0, F_W, 1);
#pragma unroll 1
  for (int i = 0; i < NDIR; ++i) {
    GAS u64* const f = flag_ptr(p.ws, F_U + (l & 31)) + (l >> 5) * 2;
    while (__any((int)min(flag_get(cx, f), flag_get(cx, f + 1)) <= SDIR + i) && !spin_fail(spins, cx)) __builtin_amdgcn_s_sleep(8);
    publish(cx, 0, F_W + 1 + i, 1);
  }
}
struct FinX { unsigned x[NDIR], d; };
__device__ __forceinline__ int fin_col(int T) { const int l = lane_id(); return T * 16 + 8 * ((l >> 3) & 1) + 2 * (l & 3); }
__device__ __forceinline__ int fin_row(int w) { const int l = lane_id(); return SDIR * 32 + 8 * w + 2 * (l >> 4) + ((l >> 2) & 1); }
__device__ __forceinline__ FinX fin_x(const Params& p, int w, int T) {
  FinX f;
#pragma unroll
  for (int i = 0; i < NDIR; ++i) f.x[i] = *(const unsigned*)(p.x + (size_t)(fin_row(w) + 32 * i) * DM + fin_col(T));
  f.d = *(const unsigned*)(p.dsk + fin_col(T));
  return f;
}
__device__ __forceinline__ void fin_role(const Params& p, unsigned tag, int w, int T, const FinX& fx) {
  char* const ws = p.ws;
  const int l = lane_id(), n = l & 15, g = l >> 4;
  const Ctx cx = {ws, tag};
  Bulk bk;
  bk.init(cx, T & 7);
  bk.pl.load(cx, T & 7);
  const Place& pl = bk.pl;
  FinIn fi;
  if (w == 0) wait_all<30>(cx, F_W, 1);
  BARRIER();
  const int d = fin_col(T), r0 = fin_row(w);
  float (&gg)[NDIR] = fi.g, (&b0)[NDIR] = fi.b, (&b1)[NDIR] = fi.c;
#pragma unroll
  for (int i = 0; i < NDIR; ++i) {
    const int t = r0 + 32 * i;
    gg[i] = __uint_as_float(__builtin_amdgcn_raw_buffer_load_b32(bk.rs, t * 4, bk.at(B_G), 16));
    const unsigned yv = __builtin_amdgcn_raw_buffer_load_b32(bk.rs, y_at(bk, d >> 2, t * 8 + (d & 2) * 2), 0, 16);
    const unsigned xv = fx.x[i], dv = fx.d;
    b0[i] = rbf(rbf(1.0f - gg[i]) * rbf(lo16(yv) + rbf(lo16(xv) * lo16(dv)))); b1[i] = rbf(rbf(1.0f - gg[i]) * rbf(hi16(yv) + rbf(hi16(xv) * hi16(dv))));
  }
#pragma unroll
  for (int i = 0; i < NDIR; ++i) asm volatile("" : "+v"(b0[i]), "+v"(b1[i]));
  char* const base = comm_base(ws) - CO;
  fi.sidx = (n & 8) ? 0xEEEEu : 0x4444u;
  fi.l16 = l * 16; fi.d0 = 0;
  fi.cidx = (n & 3) * 0x5555u;
  auto ae = [&](int j) { return g <= j ? j * 32768 + (n >> 3) * 512 + g * 128 + (n & 7) * 16 : 0x7F000000u; };
  fi.ae0 = ae(0); fi.ae1 = ae(1); fi.ae2 = ae(2); fi.ae3 = ae(3);
  fi.am = w * 1024 + l * 16;
  fi.lw = (w >> 1) * 512 + (2 * (g & 1) + (g >> 1)) * 128 + (n >> 1) * 16 + (w & 1) * 8 + (n & 1) * 4;
  fi.lr = w * 2048 + ((l >> 2) & 1) * 1024 + g * 128 + (((l >> 3) & 1) * 4 + (l & 3)) * 16;
  fi.oo = (r0 * DM + d) * 2;
  fi.f_w = (unsigned)(ws - base) + (F_W + 1) * 128;
  fi.ws = base; fi.out = p.out; fi.tag = __builtin_amdgcn_readfirstlane(tag); fi.o_ctl = (unsigned)(ws - base);
  fi.o_u = __builtin_amdgcn_readfirstlane(u_tile(T, w, 0) ^ (~bk.w13 & 8192u));
  fi.o_x = X_AT + ((bk.w13 ^ 8192u) & 8192u);
  fi.o_e = __builtin_amdgcn_readfirstlane(fi.o_x + 0x100000 + (w >> 1) * 16384 + (w & 1) * 4096);
  fi.o_va = __builtin_amdgcn_readfirstlane((bk.w13 ^ 8192u) + 2 * B_O + T * 16384 + w * 2048);
  fi.o_tw = __builtin_amdgcn_readfirstlane(CO + TW_OFF + 1024 + (16384u ^ pl.dyn(T & 3)));
  fi.dret = p.dret;
  fi.pa = p.xt0 | (p.xt1 << 16) | (T == 0 || T >= 28 ? 32768 : 0); fi.w = w;
  asm volatile(F_ASM_TEXT : F_ASM_VOPS : F_ASM_SOPS : F_ASM_CLOB);
  if (T == 0 && w == 0 && lane_id() == 0) __hip_atomic_store((GAS unsigned*)flag_ptr(ws, F_GEN), tag, RLX_AGENT);
}

__device__ __forceinline__ void lines_ahead(const Params& p, int w, int h) {
  const int i = h * 4 + w, l = lane_id();
  const rsrc_t ro = mk_rsrc(p.out, (size_t)SL * DM * 2), rc = mk_rsrc(p.ws, CTL_BYTES), rb = mk_rsrc(comm_base(p.ws) - CO, BASE_BYTES);
  const unsigned z0 = __builtin_amdgcn_raw_buffer_load_b32(rc, l * 128, i * 8192, 16);
  const unsigned z1 = __builtin_amdgcn_raw_buffer_load_b32(rb, l * 128, (w & 2 ? PB + 2 * B_G : near_head(B_K, h) ^ (8192u << 14)) ^ ((w & 1) * 8192), 16);
  if (p.wl2 & 2)
    for (int k = 0; k < 18; ++k) st128((u32x4){0u, 0u, 0u, 0u}, ro, l * 16, (i * 18 + k) * 1024);
  asm volatile("" :: "v"(z0), "v"(z1));
}
__device__ __forceinline__ void code_ahead(int i) {
  const char* pc;
  unsigned back, len;
  asm volatile("s_getpc_b64 %0\n.Lcp_%=:\n\ts_mov_b32 %1, .Lcp_%=-_Z6k_mega6Params\n\ts_mov_b32 %2, k_mega_end-_Z6k_mega6Params" : "=s"(pc), "=s"(back), "=s"(len));
  const rsrc_t rc = mk_rsrc(pc - back, len);
  asm volatile("s_movk_i32 m0, 15872\n\ts_nop 0\n\tbuffer_load_dword %0, %1, 0 offen lds" :: "v"(i * 64), "s"(rc) : "memory");
}

__global__ void __launch_bounds__(256, 1) k_mega(Params p) {
  code_ahead((blockIdx.x >> 3) * 256 + threadIdx.x);
  SB();
  asm volatile("" :: "s"(p.x), "s"(p.mem0), "s"(p.Wq), "s"(p.Wk), "s"(p.Wv), "s"(p.Wo), "s"(p.lar), "s"(p.aim), "s"(p.cre), "s"(p.cim), "s"(p.ldt), "s"(p.dsk), "s"(p.gw), "s"(p.out), "s"(p.ws));
  extern __shared__ __attribute__((aligned(16))) char smem[];
  const int x = blockIdx.x & 7, j = blockIdx.x >> 3;
  const int w = __builtin_amdgcn_readfirstlane(threadIdx.x >> 6);
  if (w == 1 && blockIdx.x != 128 && ((p.tlb >> (x < 4 ? (j < 16 ? 0 : j < 25 ? 1 : 2) : (j & 24) == 16 ? 4 : 3)) & 1)) touch_pages(p.ws, j);
  if (x < 4 && (j < 16 || j > 24)) sleep_n(x ? p.v0 : p.s0);
  if (x < 4 && j < 8) { chain_role<true>(p, w, x + 4 * j, x); return; }
  else if (x < 4 && j < 16) { chain_role<false>(p, w, x + 4 * (j - 8), x); return; }
  else if (x < 4 && j < 25) red_role(p, w, x + 4 * (j - 16), smem);
  else if (x == 0 && j == 25) {
    if (w == 0) keep_role(p);
    else if (w == 1) first_role(p);
  }
  else {
    unsigned tag;
    int T = -1, opl = -1, mix = -1, ms0 = 0, mds = 1, ms1 = p.mixs;
    FinX fx;
    if (x >= 4 && j < 16) {
      const int h = (x & 3) + 4 * (j & 7);
      tag = j >= 8 ? proj_role<false>(p, w, h) : proj_role<true>(p, w, h);
      if (j >= 8 && h < 24) opl = h, mix = h >> 2, ms0 = p.ob0 + (h & 3), mds = 4, ms1 = p.ob1;
      else if (h < 30) mix = h % 6, ms0 = p.mixs + (j < 8 ? 1 + h / 6 : 0), mds = 6, ms1 = SDIR;
      if (j >= 8 && h >= 30) { T = (h - 30) * 35; fx = fin_x(p, w, T); }
    } else {
      if (x >= 4 && j < 24) {
        const int h = (x & 3) + 4 * (j & 7);
        x_ahead(p, w, 35 + h);
        if (p.wl2 & 1) warm_l2(p.Wo, h * DH * DM * 2, false, w);
        lines_ahead(p, w, h);
        WAIT_VM(0);
        BARRIER();
        if (U_THIRD) proj_role<true, 2>(p, w, h);
        tag = proj_role<false, 1>(p, w, h);
        if (w == 0 && lane_id() == 0) flag_put({p.ws, tag}, flag_ptr(p.ws, F_Y) + 35 + h, 1u);
        att_role(p, tag, w, h, 0, SDIR);
      } else {
        if (x >= 4) {
          tag = proj_role<true, 1>(p, w, (x & 3) + 4 * (j & 7));
          if (j >= 30) xprep_role(p, tag, w, (x & 3) + 4 * (j & 1));
        }
        if (x >= 4 && j < 30) opl = (j - 24) * 4 + (x & 3);
        else {
          T = x < 4 ? (j - 25) * 4 + x : (j - 23) * 4 + (x & 3);
          fx = fin_x(p, w, T);
#pragma unroll
          for (int i = 0; i < NDIR; ++i) asm volatile("" : "+v"(fx.x[i]), "+v"(fx.d));
          if (x < 4) tag = s4_role(p, w, 7 + x * 7 + (j - 25), smem);
          if (T == 35) { if (w == 0) watch_role(p, tag); T = -1; }
          if (T > 28 && p.mixs) mix = T - 29;
        }
      }
    }
    if (opl >= 0) op_role(p, tag, w, opl >> 2, opl & 3, x, j < 16);
    if (mix >= 0) mix_role(p, tag, w, mix, ms0, mds, ms1, opl >= 0);
    if (T >= 0) fin_role(p, tag, w, T, fx);
  }
}

extern "C" __device__ __attribute__((used, noinline, visibility("hidden"))) void k_mega_end() {}

extern "C" void kernel_launch(void* const* d_in, const int* in_sizes, int n_in, void* d_out, int out_size, void* d_ws, size_t ws_size,
                              hipStream_t stream) {
  Params p;
  p.x = (const u16*)d_in[0]; p.mem0 = (const u16*)d_in[1];
  p.Wq = (const u16*)d_in[2]; p.Wk = (const u16*)d_in[3]; p.Wv = (const u16*)d_in[4]; p.Wo = (const u16*)d_in[5];
  p.lar = (const u16*)d_in[8]; p.aim = (const u16*)d_in[9]; p.cre = (const u16*)d_in[10]; p.cim = (const u16*)d_in[11];
  p.ldt = (const u16*)d_in[12]; p.dsk = (const u16*)d_in[13]; p.gw = (const u16*)d_in[14];
  p.out = (u16*)d_out; p.ws = (char*)d_ws;
  (void)hipFuncSetAttribute((const void*)k_mega, hipFuncAttributeMaxDynamicSharedMemorySize, LDS_BYTES);
  {
    k_mega<<<256, 256, LDS_BYTES, stream>>>(p);
  }
}
```
